```python
import torch

def _softmax(x, axis=-1):
    return torch.softmax(x, dim=axis)

def _gelu(x):
    return torch.nn.functional.gelu(x, approximate='tanh')

def _silu(x):
    return torch.nn.functional.silu(x)

def _sigmoid(x):
    return torch.sigmoid(x)

def _layernorm(x, axis=-1, eps=1e-05):
    m = x.mean(dim=axis, keepdim=True)
    v = x.var(dim=axis, keepdim=True, unbiased=False)
    return (x - m) / torch.sqrt(v + eps)

def _scatter_add(tgt, idx, src, axis=0):
    return tgt.clone().index_add_(axis, idx, src)

def _segment_sum(src, seg, n):
    out = torch.zeros((n,) + src.shape[1:], dtype=src.dtype)
    return out.index_add_(0, seg, src)

def _pad(x, pw):
    flat = tuple((v for pair in reversed(pw) for v in pair))
    return torch.nn.functional.pad(x, flat)
kernel_name = 'adaLN_cond__mha_block_cached__moe_arch_mod__prompt'

def setup_inputs():

    _w = lambda a: (lambda t: t.to(torch.bfloat16) if t.is_floating_point() else t)(torch.as_tensor(a))

    def _A_setup():
        B, Sq, D = (1, 2048, 1024)
        H, Sk, Dh = (32, 2048, 128)
        return {'x': _w((lambda *s: torch.randn(*s, dtype=torch.bfloat16) * 0.1)(B, Sq, D)), 'k_cache': _w((lambda *s: torch.randn(*s, dtype=torch.bfloat16) * 0.8)(B, H, Sk, Dh)), 'v_cache': _w((lambda *s: torch.randn(*s, dtype=torch.bfloat16) * 0.8)(B, H, Sk, Dh)), 'wq': _w((lambda *s: torch.randn(*s, dtype=torch.bfloat16) * 0.8)(D, H * Dh) * D ** (-0.5)), 'wo': _w((lambda *s: torch.randn(*s, dtype=torch.bfloat16) * 0.8)(H * Dh, D) * (H * Dh) ** (-0.5))}
    _A_in = _A_setup()

    def _B_setup():
        B, S, D, F, C = (1, 2048, 1024, 3584, 2)
        return {'x': _w((lambda *s: torch.randn(*s, dtype=torch.bfloat16) * 0.1)(B, S, D)), 'w_r': _w((lambda *s: torch.randn(*s, dtype=torch.bfloat16) * 3.2)(D)), 'w_gate': _w((lambda *s: torch.randn(*s, dtype=torch.bfloat16) * 0.2)(D, F)), 'w_up': _w((lambda *s: torch.randn(*s, dtype=torch.bfloat16) * 0.2)(D, F)), 'w_down': _w((lambda *s: torch.randn(*s, dtype=torch.bfloat16) * 0.2)(F, D))}
    _B_in = _B_setup()
    return {'A_x': _A_in['x'], 'A_k_cache': _A_in['k_cache'], 'A_v_cache': _A_in['v_cache'], 'A_wq': _A_in['wq'], 'A_wo': _A_in['wo'], 'B_x': _B_in['x'], 'B_w_r': _B_in['w_r'], 'B_w_gate': _B_in['w_gate'], 'B_w_up': _B_in['w_up'], 'B_w_down': _B_in['w_down'], 'c': _w((lambda *s: torch.randn(*s, dtype=torch.bfloat16) * 0.2)(1, 1024) * 8.0), 'w_mod': _w((lambda *s: torch.randn(*s, dtype=torch.bfloat16) * 0.2)(1024, 6 * 1024) * 1024 ** (-0.5) * 8.0)}

def reference(A_x, A_k_cache, A_v_cache, A_wq, A_wo, B_x, B_w_r, B_w_gate, B_w_up, B_w_down, c, w_mod):

    def _A_ref(x, k_cache, v_cache, wq, wo):
        B, Sq, D = x.shape
        H, Dh = (32, 128)
        q = (lambda x, *d: x.permute(*d))((x @ wq).reshape(B, Sq, H, Dh), 0, 2, 1, 3)
        scores = torch.einsum('bhqd,bhkd->bhqk', q, k_cache) * Dh ** (-0.5)
        p = _softmax(scores, axis=-1)
        o = torch.einsum('bhqk,bhkd->bhqd', p, v_cache)
        return (lambda x, *d: x.permute(*d))(o, 0, 2, 1, 3).reshape(B, Sq, H * Dh) @ wo

    def _B_ref(x, w_r, w_gate, w_up, w_down):
        B, S, D = x.shape
        C = 2
        r = x @ w_r
        idx = torch.argsort(-r, axis=-1)[:, :C]
        idx_e = (lambda x, n, axis: torch.repeat_interleave(x, n, dim=axis))(idx.reshape(B, C, 1), D, axis=2)
        x_sel = (lambda x, i, axis: torch.take_along_dim(x, i, dim=axis))(x, idx_e, axis=1)
        g = _silu(x_sel @ w_gate)
        u = x_sel @ w_up
        y = g * u @ w_down
        r_sel = _sigmoid((lambda x, i, axis: torch.take_along_dim(x, i, dim=axis))(r, idx, axis=1))
        y = y * r_sel.reshape(B, C, 1)
        b_off = (torch.arange(B) * S).reshape(B, 1)
        flat_idx = (idx + b_off).reshape(B * C)
        zero = (x * 0.0).reshape(B * S, D)
        upd = _scatter_add(zero, flat_idx, y.reshape(B * C, D), axis=0)
        return x + upd.reshape(B, S, D)
    x = A_x
    mod = (_silu(c) @ w_mod).reshape(c.shape[0], 6, 1, -1)
    sa, ca, ga, sb, cb, gb = (mod[:, 0].clone(), mod[:, 1].clone(), mod[:, 2].clone(), mod[:, 3].clone(), mod[:, 4].clone(), mod[:, 5].clone())
    h = _layernorm(x, axis=-1) * (1 + ca) + sa
    x = x + ga * _A_ref(h, A_k_cache, A_v_cache, A_wq, A_wo)
    h = _layernorm(x, axis=-1) * (1 + cb) + sb
    x = x + gb * _B_ref(h, B_w_r, B_w_gate, B_w_up, B_w_down)
    return x
```

```cpp
#include <hip/hip_runtime.h>
#include <hip/hip_bf16.h>
#include <utility>
#include <cstdint>

#pragma clang fp contract(off)
typedef float    f32x16 __attribute__((ext_vector_type(16)));
typedef float    f32x2  __attribute__((ext_vector_type(2)));
typedef float    f32x4  __attribute__((ext_vector_type(4)));
typedef __bf16   bf16x8 __attribute__((ext_vector_type(8)));
typedef __bf16   bf16x4 __attribute__((ext_vector_type(4)));
typedef __bf16   bf16x2 __attribute__((ext_vector_type(2)));
typedef unsigned u32x4  __attribute__((ext_vector_type(4)));
typedef unsigned u32x2  __attribute__((ext_vector_type(2)));
typedef short    s16x4  __attribute__((ext_vector_type(4)));
typedef unsigned short u16;
template <int... Is> using iseq = std::integer_sequence<int, Is...>;
template <int N> using make_iseq = std::make_integer_sequence<int, N>;
#define DI __device__ __forceinline__
#define TAILB
#define PACE2
#define SELB

constexpr int SEQ = 2048, DM = 1024, NHEAD = 32, DHEAD = 128, DQ = NHEAD * DHEAD, DFF = 3584, NMOD = 6 * DM;
constexpr size_t WS_EP    = 0;
constexpr size_t WS_RST   = 32768;
constexpr size_t WS_CNT   = 65536;
constexpr size_t WS_MOD   = 131072;
constexpr size_t WS_Z     = 262144;
constexpr size_t WS_MODT  = WS_Z;
constexpr size_t WS_XFL   = WS_Z + 16384;
#define KEY_REP 8u
constexpr size_t WS_KEYS  = WS_Z + 32768;
constexpr unsigned Z_SHARE = 1280;
static_assert(KEY_REP <= 8 && Z_SHARE * 256 == 327680, "zeroed area");
constexpr size_t WS_MD    = WS_Z + 49152;
constexpr size_t WS_PFL   = WS_Z + 65536;
constexpr size_t WS_OFL   = WS_Z + 196608;
constexpr size_t WS_H     = 1048576;
constexpr size_t WS_PART  = WS_H  + (size_t)SEQ * DM * 2;
constexpr size_t WS_XIN   = WS_PART + (size_t)256 * 2 * DM * 4;
#define BOXV 38016
constexpr unsigned BOX    = BOXV;
constexpr size_t WS_END   = WS_XIN + (size_t)BOX * (SEQ / 2);

DI float bf2f(u16 b) { return __uint_as_float((unsigned)b << 16); }
DI float rbf(float x) { return (float)(__bf16)x; }
DI u16   f2bf(float x) { return __builtin_bit_cast(u16, (__bf16)x); }
DI float lo16(unsigned w) { return __uint_as_float(w << 16); }
DI float hi16(unsigned w) { return __uint_as_float(w & 0xffff0000u); }
DI unsigned cvtpk(float lo, float hi) {
  f32x2 v = {lo, hi}; bf16x2 b = __builtin_convertvector(v, bf16x2); return __builtin_bit_cast(unsigned, b);
}
template <int CTRL> DI float dpp_add(float v) { return v + __uint_as_float(__builtin_amdgcn_update_dpp(0u, __float_as_uint(v), CTRL, 0xf, 0xf, true)); }
DI float swap32_add(float a, float b) {
  auto r = __builtin_amdgcn_permlane32_swap(__float_as_uint(a), __float_as_uint(b), false, false); return __uint_as_float(r[0]) + __uint_as_float(r[1]);
}
DI float swap16_add(float a, float b) {
  auto r = __builtin_amdgcn_permlane16_swap(__float_as_uint(a), __float_as_uint(b), false, false); return __uint_as_float(r[0]) + __uint_as_float(r[1]);
}
constexpr int DPP_XOR1 = 0xB1, DPP_XOR2 = 0x4E, DPP_ROR4 = 0x124, DPP_ROR8 = 0x128;
DI float wave_sum(float v) {
  v = dpp_add<DPP_XOR1>(v); v = dpp_add<DPP_XOR2>(v); v = dpp_add<DPP_ROR4>(v); v = dpp_add<DPP_ROR8>(v);
  v = swap16_add(v, v); return swap32_add(v, v);
}

DI void st16_wt(void* p, u32x4 v) { asm volatile("global_store_dwordx4 %0, %1, off sc1\n\ts_nop 1" :: "v"(p), "v"(v) : "memory"); }

DI f32x2 unpk(unsigned w) { return f32x2{lo16(w), hi16(w)}; }
DI unsigned cvtpk2(f32x2 v) { return __builtin_bit_cast(unsigned, __builtin_convertvector(v, bf16x2)); }
DI float rbh(float v) { return __uint_as_float(cvtpk(0.f, v)); }
DI f32x2 rb2(f32x2 v) { return f32x2{__uint_as_float(cvtpk(0.f, v[0])), __uint_as_float(cvtpk(0.f, v[1]))}; }
DI unsigned ln_pair(float x0, float x1, float m, float rden, float c0, float c1, float s0, float s1) {
  const unsigned d = cvtpk2(f32x2{x0, x1} - m), l = cvtpk2(unpk(d) * rden), t = cvtpk2(unpk(l) * f32x2{c0, c1});
  return cvtpk2(unpk(t) + f32x2{s0, s1});
}
#define GAS __attribute__((address_space(1)))
#define LAS __attribute__((address_space(3)))
typedef GAS unsigned long long gu64;
typedef short v4i16_t __attribute__((ext_vector_type(4)));
DI bf16x4 lds_tr(unsigned a) { return __builtin_bit_cast(bf16x4, __builtin_amdgcn_ds_read_tr16_b64_v4i16((LAS v4i16_t*)a)); }
#define RLX_AGENT __ATOMIC_RELAXED, __HIP_MEMORY_SCOPE_AGENT
constexpr unsigned SPIN_CAP = 1u << 16;

static_assert(BOX == 38016, "box size");
#define NP_FILL 8
#define GOT_WAIT "8"
#define FA_CLOBBERS "v0", "v1", "v2", "v3", "v8", "v9", "v10", "v11", "v16", "v17", "v18", "v19", "v20", "v21", "v22", "v23", "v24", "v25", "v26", "v27", "v28", "v29", "v30", "v31", "v32", "v33", "v34", "v35", "v40", "v41", "v42", "v43", "v48", "v49", "v50", "v51", "v52", "v53", "v54", "v55", "v56", "v57", "v58", "v59", "v60", "v61", "v62", "v63", "v64", "v65", "v66", "v67", "v72", "v73", "v74", "v75", "v80", "v81", "v82", "v83", "v84", "v85", "v86", "v87", "v88", "v89", "v90", "v91", "v92", "v93", "v94", "v95", "v96", "v97", "v98", "v99", "v104", "v105", "v106", "v107", "v112", "v113", "v114", "v115", "v116", "v117", "v118", "v119", "v120", "v121", "v122", "v123", "v124", "v125", "v126", "v127", "v200", "v201", "v202", "v203", "v205", "v207", "v208", "v209", "v210", "v211", "v212", "v213", "v214", "v215", "v216", "v217", "v218", "v219", "v220", "v221", "v222", "v223", "v224", "v230", "v231", "v232", "v233", "v234", "v235", "v237", "a0", "a1", "a2", "a3", "a4", "a5", "a6", "a7", "a8", "a9", "a10", "a11", "a12", "a13", "a14", "a15", "a16", "a17", "a18", "a19", "a20", "a21", "a22", "a23", "a24", "a25", "a26", "a27", "a28", "a29", "a30", "a31", "a32", "a33", "a34", "a35", "a36", "a37", "a38", "a39", "a40", "a41", "a42", "a43", "a44", "a45", "a46", "a47", "a48", "a49", "a50", "a51", "a52", "a53", "a54", "a55", "a56", "a57", "a58", "a59", "a60", "a61", "a62", "a63", "a64", "a65", "a66", "a67", "a68", "a69", "a70", "a71", "a72", "a73", "a74", "a75", "a76", "a77", "a78", "a79", "a80", "a81", "a82", "a83", "a84", "a85", "a86", "a87", "a88", "a89", "a90", "a91", "a92", "a93", "a94", "a95", "a96", "a97", "a98", "a99", "a100", "a101", "a102", "a103", "a104", "a105", "a106", "a107", "a108", "a109", "a110", "a111", "a112", "a113", "a114", "a115", "a116", "a117", "a118", "a119", "a120", "a121", "a122", "a123", "a124", "a125", "a126", "a127", "a128", "a129", "a130", "a131", "a132", "a133", "a134", "a135", "a136", "a137", "a138", "a139", "a140", "a141", "a142", "a143", "a144", "a145", "a146", "a147", "a148", "a149", "a150", "a151", "a152", "a153", "a154", "a155", "a156", "a157", "a158", "a159", "a160", "a161", "a162", "a163", "a164", "a165", "a166", "a167", "a168", "a169", "a170", "a171", "a172", "a173", "a174", "a175", "a176", "a177", "a178", "a179", "a180", "a181", "a182", "a183", "a184", "a185", "a186", "a187", "a188", "a189", "a190", "a191", "a192", "a193", "a194", "a195", "a196", "a197", "a198", "a199", "a200", "a201", "a202", "a203", "a204", "a205", "a206", "a207", "a208", "a209", "a210", "a211", "a212", "a213", "a214", "a215", "a216", "a217", "a218", "a219", "a220", "a221", "a222", "a223", "a224", "a225", "a226", "a227", "a228", "a229", "a230", "a231", "a232", "a233", "a234", "a235", "a236", "a237", "a238", "a239", "a240", "a241", "a242", "a243", "a244", "a245", "a246", "a247", "a248", "a249", "a250", "a251", "a252", "a253", "a254", "a255", "s40", "s41", "s42", "s43", "s44", "s45", "s46", "s47", "s48", "s49", "s50", "s51", "s52", "s53", "s54", "s55", "s56", "s57", "s58", "s59", "s60", "s61", "s62", "s63", "s64", "s65", "s66", "s67", "s68", "s69", "vcc", "m0", "scc", "memory"
#define EARLY2 0
#define SHW 1
#define QROT 0
#define SKV 0
#define KVPAR 0
#define QT2 0
#define QSTAG 0
#define TLATE 0
#define NTV 3
#define BPERM 0
#define FA_OUTS [got] "=&s"(got), "=&{v[128:129]}"(xf[0]), "=&{v[130:131]}"(xf[1]), "=&{v[132:133]}"(xf[2]), "=&{v[134:135]}"(xf[3]), "=&{v[136:137]}"(xf[4]), "=&{v[138:139]}"(xf[5]), "=&{v[140:141]}"(xf[6]), "=&{v[142:143]}"(xf[7]), "=&{v[144:145]}"(xf[8]), "=&{v[146:147]}"(xf[9]), "=&{v[148:149]}"(xf[10]), "=&{v[150:151]}"(xf[11]), "=&{v[152:153]}"(xf[12]), "=&{v[154:155]}"(xf[13]), "=&{v[156:157]}"(xf[14]), "=&{v[158:159]}"(xf[15]), "=&{v[160:163]}"(gaf[0]), "=&{v[164:167]}"(gaf[1]), "=&{v[168:171]}"(gaf[2]), "=&{v[172:175]}"(gaf[3]), "=&{v[176:179]}"(gaf[4]), "=&{v[180:183]}"(gaf[5]), "=&{v[184:187]}"(gaf[6]), "=&{v[188:191]}"(gaf[7]), "=&{v[4:7]}"(aw[0]), "=&{v[12:15]}"(aw[1]), "=&{v[36:39]}"(aw[2]), "=&{v[44:47]}"(aw[3]), "=&{v[68:71]}"(aw[4]), "=&{v[76:79]}"(aw[5]), "=&{v[100:103]}"(aw[6]), "=&{v[108:111]}"(aw[7]), "=&{v[192:195]}"(alo), "=&{v[196:199]}"(ahi), "=&{v204}"(late), "=&{v206}"(arow), "+{v229}"(fl), "+{v225}"(vwb), "+{v226}"(wd0), "+{v227}"(hq0), "+{v228}"(sd0), "+{v236}"(tq0)
#define FA_BODY \
"s_branch adaln_body\n" \
".p2align 12\n" \
"adaln_pg7:\n" \
"adaln_body:\n" \
"s_cbranch_execz adaln_fr\n" \
"s_nop 0\n" \
"s_mov_b32 s55, 0x80000\n" \
"v_add_u32_e32 v219, 0x8000, v227\n" \
"v_add_u32_e32 v220, 0x10000, v227\n" \
"v_add_u32_e32 v221, 0x18000, v227\n" \
"s_mov_b32 s50, 0\n" \
"s_movk_i32 s53, 0x1000\n" \
"s_mov_b32 s52, 0\n" \
"s_nop 0\n" \
"s_mov_b32 s49, 1572864\n" \
"v_add_u32_e32 v216, 0xfffffc80, v226\n" \
"v_add_u32_e32 v217, 0xf800, v226\n" \
"v_add_u32_e32 v218, 0xf480, v226\n" \
"s_add_u32 s40, %[m0b], 0\n" \
"s_nop 0\n" \
"s_add_u32 s44, %[m0b], 65536\n" \
"s_add_u32 s41, %[m0b], 16384\n" \
"s_add_u32 s45, %[m0b], 81920\n" \
"s_add_u32 s42, %[m0b], 32768\n" \
"s_add_u32 s46, %[m0b], 98304\n" \
"s_add_u32 s43, %[m0b], 49152\n" \
"s_add_u32 s47, %[m0b], 114688\n" \
"v_xor_b32_e64 v208, 64, %[kb0]\n" \
"v_xor_b32_e32 v209, 0x80, %[kb0]\n" \
"v_xor_b32_e32 v210, 0xc0, %[kb0]\n" \
"v_xor_b32_e64 v212, 64, %[kd0]\n" \
"v_xor_b32_e32 v213, 0x80, %[kd0]\n" \
"v_xor_b32_e32 v214, 0xc0, %[kd0]\n" \
"v_xor_b32_e64 v211, 32, %[vvb]\n" \
"v_add_u32_e32 v215, 0xfffffc80, %[vd0]\n" \
"v_readlane_b32 s64, %[tlo], 17\n" \
"v_readlane_b32 s65, %[thi], 17\n" \
"s_mov_b32 s66, 12582912\n" \
"s_mov_b32 s67, 0x20000\n" \
"v_mov_b64_e32 v[0:1], 0\n" \
"v_mov_b64_e32 v[2:3], 0\n" \
"v_mov_b64_e32 v[4:5], 0\n" \
"v_mov_b64_e32 v[6:7], 0\n" \
"v_mov_b64_e32 v[8:9], 0\n" \
"v_mov_b64_e32 v[10:11], 0\n" \
"v_mov_b64_e32 v[12:13], 0\n" \
"v_mov_b64_e32 v[14:15], 0\n" \
"v_mov_b64_e32 v[16:17], 0\n" \
"v_mov_b64_e32 v[18:19], 0\n" \
"v_mov_b64_e32 v[20:21], 0\n" \
"v_mov_b64_e32 v[22:23], 0\n" \
"v_mov_b64_e32 v[24:25], 0\n" \
"v_mov_b64_e32 v[26:27], 0\n" \
"v_mov_b64_e32 v[28:29], 0\n" \
"v_mov_b64_e32 v[30:31], 0\n" \
"v_mov_b64_e32 v[32:33], 0\n" \
"v_mov_b64_e32 v[34:35], 0\n" \
"v_mov_b64_e32 v[36:37], 0\n" \
"v_mov_b64_e32 v[38:39], 0\n" \
"v_mov_b64_e32 v[40:41], 0\n" \
"v_mov_b64_e32 v[42:43], 0\n" \
"v_mov_b64_e32 v[44:45], 0\n" \
"v_mov_b64_e32 v[46:47], 0\n" \
"v_mov_b64_e32 v[48:49], 0\n" \
"v_mov_b64_e32 v[50:51], 0\n" \
"v_mov_b64_e32 v[52:53], 0\n" \
"v_mov_b64_e32 v[54:55], 0\n" \
"v_mov_b64_e32 v[56:57], 0\n" \
"v_mov_b64_e32 v[58:59], 0\n" \
"v_mov_b64_e32 v[60:61], 0\n" \
"v_mov_b64_e32 v[62:63], 0\n" \
"v_mov_b64_e32 v[64:65], 0\n" \
"v_mov_b64_e32 v[66:67], 0\n" \
"v_mov_b64_e32 v[68:69], 0\n" \
"v_mov_b64_e32 v[70:71], 0\n" \
"v_mov_b64_e32 v[72:73], 0\n" \
"v_mov_b64_e32 v[74:75], 0\n" \
"v_mov_b64_e32 v[76:77], 0\n" \
"v_mov_b64_e32 v[78:79], 0\n" \
"v_mov_b64_e32 v[80:81], 0\n" \
"v_mov_b64_e32 v[82:83], 0\n" \
"v_mov_b64_e32 v[84:85], 0\n" \
"v_mov_b64_e32 v[86:87], 0\n" \
"v_mov_b64_e32 v[88:89], 0\n" \
"v_mov_b64_e32 v[90:91], 0\n" \
"v_mov_b64_e32 v[92:93], 0\n" \
"v_mov_b64_e32 v[94:95], 0\n" \
"v_mov_b64_e32 v[96:97], 0\n" \
"v_mov_b64_e32 v[98:99], 0\n" \
"v_mov_b64_e32 v[100:101], 0\n" \
"v_mov_b64_e32 v[102:103], 0\n" \
"v_mov_b64_e32 v[104:105], 0\n" \
"v_mov_b64_e32 v[106:107], 0\n" \
"v_mov_b64_e32 v[108:109], 0\n" \
"v_mov_b64_e32 v[110:111], 0\n" \
"v_mov_b64_e32 v[112:113], 0\n" \
"v_mov_b64_e32 v[114:115], 0\n" \
"v_mov_b64_e32 v[116:117], 0\n" \
"v_mov_b64_e32 v[118:119], 0\n" \
"v_mov_b64_e32 v[120:121], 0\n" \
"v_mov_b64_e32 v[122:123], 0\n" \
"v_mov_b64_e32 v[124:125], 0\n" \
"v_mov_b64_e32 v[126:127], 0\n" \
"s_mov_b32 s51, 0x10000\n" \
"v_mbcnt_lo_u32_b32 v232, -1, 0\n" \
"v_mov_b64_e32 v[234:235], %[tag]\n" \
"v_lshlrev_b32_e32 v233, 3, v232\n" \
"v_lshlrev_b32_e32 v232, 8, v232\n" \
"s_waitcnt vmcnt(0)\n" \
"s_barrier\n" \
"s_cmp_lg_u32 %[wid], 0\n" \
"s_cbranch_scc1 Lwalk_%=\n" \
"s_mov_b32 exec_hi, 0\n" \
"global_store_dwordx2 v232, v[234:235], %[psig] sc1\n" \
"s_mov_b32 exec_hi, -1\n" \
"Lgo_%=:\n" \
"s_nop 0\n" \
"ds_read_b64_tr_b16 v[128:129], v225 offset:0\n" \
"ds_read_b64_tr_b16 v[130:131], v225 offset:256\n" \
"ds_read_b64_tr_b16 v[132:133], v225 offset:8192\n" \
"ds_read_b64_tr_b16 v[134:135], v225 offset:8448\n" \
"ds_read_b64_tr_b16 v[136:137], v225 offset:8\n" \
"ds_read_b64_tr_b16 v[138:139], v225 offset:264\n" \
"ds_read_b64_tr_b16 v[140:141], v225 offset:8200\n" \
"ds_read_b64_tr_b16 v[142:143], v225 offset:8456\n" \
"ds_read_b64_tr_b16 v[144:145], v225 offset:512\n" \
"ds_read_b64_tr_b16 v[146:147], v225 offset:768\n" \
"ds_read_b64_tr_b16 v[148:149], v225 offset:8704\n" \
"ds_read_b64_tr_b16 v[150:151], v225 offset:8960\n" \
"ds_read_b64_tr_b16 v[152:153], v225 offset:520\n" \
"ds_read_b64_tr_b16 v[154:155], v225 offset:776\n" \
"ds_read_b64_tr_b16 v[156:157], v225 offset:8712\n" \
"ds_read_b64_tr_b16 v[158:159], v225 offset:8968\n" \
"s_mov_b32 s60, 4194304\n" \
"v_and_b32_e32 v233, 56, v233\n" \
"s_lshl_b32 s61, %[wid], 6\n" \
"v_add_u32_e64 v233, s61, v233\n" \
"Lpoll_%=:\n" \
"global_load_dwordx2 v[230:231], v233, %[pcnt] sc1\n" \
"s_waitcnt vmcnt(0)\n" \
"v_cmp_ne_u64_e32 vcc, v[230:231], v[234:235]\n" \
"s_sub_u32 s51, s51, 1\n" \
"s_cbranch_scc1 Lpd_%=\n" \
"s_nop 3\n" \
"s_cbranch_vccnz Lpoll_%=\n" \
"Lpd_%=:\n" \
"buffer_load_dwordx4 a[128:131], v227, %[srdz], s50 offen offset:0 sc1\n" \
"buffer_load_dwordx4 a[132:135], v219, %[srdz], s50 offen offset:0 sc1\n" \
"buffer_load_dwordx4 a[136:139], v220, %[srdz], s50 offen offset:0 sc1\n" \
"buffer_load_dwordx4 a[140:143], v221, %[srdz], s50 offen offset:0 sc1\n" \
"buffer_load_dwordx4 a[144:147], v227, %[srdz], s50 offen offset:1024 sc1\n" \
"buffer_load_dwordx4 a[148:151], v219, %[srdz], s50 offen offset:1024 sc1\n" \
"buffer_load_dwordx4 a[152:155], v220, %[srdz], s50 offen offset:1024 sc1\n" \
"buffer_load_dwordx4 a[156:159], v221, %[srdz], s50 offen offset:1024 sc1\n" \
"buffer_load_dwordx4 a[160:163], v227, %[srdz], s50 offen offset:2048 sc1\n" \
"buffer_load_dwordx4 a[164:167], v219, %[srdz], s50 offen offset:2048 sc1\n" \
"buffer_load_dwordx4 a[168:171], v220, %[srdz], s50 offen offset:2048 sc1\n" \
"buffer_load_dwordx4 a[172:175], v221, %[srdz], s50 offen offset:2048 sc1\n" \
"buffer_load_dwordx4 a[176:179], v227, %[srdz], s50 offen offset:3072 sc1\n" \
"buffer_load_dwordx4 a[180:183], v219, %[srdz], s50 offen offset:3072 sc1\n" \
"buffer_load_dwordx4 a[184:187], v220, %[srdz], s50 offen offset:3072 sc1\n" \
"buffer_load_dwordx4 a[188:191], v221, %[srdz], s50 offen offset:3072 sc1\n" \
"s_add_u32 s50, s50, s53\n" \
"s_nop 0\n" \
"buffer_load_dwordx4 a[192:195], v227, %[srdz], s50 offen offset:0 sc1\n" \
"buffer_load_dwordx4 a[196:199], v219, %[srdz], s50 offen offset:0 sc1\n" \
"buffer_load_dwordx4 a[200:203], v220, %[srdz], s50 offen offset:0 sc1\n" \
"buffer_load_dwordx4 a[204:207], v221, %[srdz], s50 offen offset:0 sc1\n" \
"buffer_load_dwordx4 a[208:211], v227, %[srdz], s50 offen offset:1024 sc1\n" \
"buffer_load_dwordx4 a[212:215], v219, %[srdz], s50 offen offset:1024 sc1\n" \
"buffer_load_dwordx4 a[216:219], v220, %[srdz], s50 offen offset:1024 sc1\n" \
"buffer_load_dwordx4 a[220:223], v221, %[srdz], s50 offen offset:1024 sc1\n" \
"s_mov_b32 s51, 3\n" \
"s_waitcnt vmcnt(16) lgkmcnt(0)\n" \
"Lq_%=:\n" \
"v_mfma_f32_16x16x32_bf16 v[0:3], v[128:131], a[128:131], v[0:3]\n" \
"ds_read_b64_tr_b16 v[160:161], v225 offset:1024\n" \
"v_mfma_f32_16x16x32_bf16 v[0:3], v[132:135], a[144:147], v[0:3]\n" \
"ds_read_b64_tr_b16 v[162:163], v225 offset:1280\n" \
"v_mfma_f32_16x16x32_bf16 v[32:35], v[128:131], a[132:135], v[32:35]\n" \
"s_mov_b32 m0, s47\n" \
"s_nop 0\n" \
"buffer_load_dwordx4 v217, %[srdw], s49 offen offset:2048 lds\n" \
"v_mfma_f32_16x16x32_bf16 v[32:35], v[132:135], a[148:151], v[32:35]\n" \
"ds_read_b64_tr_b16 v[164:165], v225 offset:9216\n" \
"v_mfma_f32_16x16x32_bf16 v[64:67], v[128:131], a[136:139], v[64:67]\n" \
"ds_read_b64_tr_b16 v[166:167], v225 offset:9472\n" \
"v_mfma_f32_16x16x32_bf16 v[64:67], v[132:135], a[152:155], v[64:67]\n" \
"v_mfma_f32_16x16x32_bf16 v[96:99], v[128:131], a[140:143], v[96:99]\n" \
"ds_read_b64_tr_b16 v[168:169], v225 offset:1032\n" \
"v_mfma_f32_16x16x32_bf16 v[96:99], v[132:135], a[156:159], v[96:99]\n" \
"ds_read_b64_tr_b16 v[170:171], v225 offset:1288\n" \
"v_mfma_f32_16x16x32_bf16 v[4:7], v[136:139], a[128:131], v[4:7]\n" \
"buffer_load_dwordx4 v218, %[srdw], s49 offen offset:3072 lds\n" \
"v_mfma_f32_16x16x32_bf16 v[4:7], v[140:143], a[144:147], v[4:7]\n" \
"ds_read_b64_tr_b16 v[172:173], v225 offset:9224\n" \
"v_mfma_f32_16x16x32_bf16 v[36:39], v[136:139], a[132:135], v[36:39]\n" \
"ds_read_b64_tr_b16 v[174:175], v225 offset:9480\n" \
"v_mfma_f32_16x16x32_bf16 v[36:39], v[140:143], a[148:151], v[36:39]\n" \
"v_mfma_f32_16x16x32_bf16 v[68:71], v[136:139], a[136:139], v[68:71]\n" \
"ds_read_b64_tr_b16 v[176:177], v225 offset:1536\n" \
"v_mfma_f32_16x16x32_bf16 v[68:71], v[140:143], a[152:155], v[68:71]\n" \
"ds_read_b64_tr_b16 v[178:179], v225 offset:1792\n" \
"v_mfma_f32_16x16x32_bf16 v[100:103], v[136:139], a[140:143], v[100:103]\n" \
"buffer_load_dwordx4 a[224:227], v227, %[srdz], s50 offen offset:2048 sc1\n" \
"v_mfma_f32_16x16x32_bf16 v[100:103], v[140:143], a[156:159], v[100:103]\n" \
"ds_read_b64_tr_b16 v[180:181], v225 offset:9728\n" \
"v_mfma_f32_16x16x32_bf16 v[8:11], v[144:147], a[128:131], v[8:11]\n" \
"ds_read_b64_tr_b16 v[182:183], v225 offset:9984\n" \
"v_mfma_f32_16x16x32_bf16 v[8:11], v[148:151], a[144:147], v[8:11]\n" \
"v_mfma_f32_16x16x32_bf16 v[40:43], v[144:147], a[132:135], v[40:43]\n" \
"ds_read_b64_tr_b16 v[184:185], v225 offset:1544\n" \
"v_mfma_f32_16x16x32_bf16 v[40:43], v[148:151], a[148:151], v[40:43]\n" \
"ds_read_b64_tr_b16 v[186:187], v225 offset:1800\n" \
"v_mfma_f32_16x16x32_bf16 v[72:75], v[144:147], a[136:139], v[72:75]\n" \
"buffer_load_dwordx4 a[228:231], v219, %[srdz], s50 offen offset:2048 sc1\n" \
"v_mfma_f32_16x16x32_bf16 v[72:75], v[148:151], a[152:155], v[72:75]\n" \
"ds_read_b64_tr_b16 v[188:189], v225 offset:9736\n" \
"v_mfma_f32_16x16x32_bf16 v[104:107], v[144:147], a[140:143], v[104:107]\n" \
"ds_read_b64_tr_b16 v[190:191], v225 offset:9992\n" \
"v_mfma_f32_16x16x32_bf16 v[104:107], v[148:151], a[156:159], v[104:107]\n" \
"v_mfma_f32_16x16x32_bf16 v[12:15], v[152:155], a[128:131], v[12:15]\n" \
"buffer_load_dwordx4 a[232:235], v220, %[srdz], s50 offen offset:2048 sc1\n" \
"v_mfma_f32_16x16x32_bf16 v[12:15], v[156:159], a[144:147], v[12:15]\n" \
"v_mfma_f32_16x16x32_bf16 v[44:47], v[152:155], a[132:135], v[44:47]\n" \
"v_mfma_f32_16x16x32_bf16 v[44:47], v[156:159], a[148:151], v[44:47]\n" \
"v_mfma_f32_16x16x32_bf16 v[76:79], v[152:155], a[136:139], v[76:79]\n" \
"buffer_load_dwordx4 a[236:239], v221, %[srdz], s50 offen offset:2048 sc1\n" \
"v_mfma_f32_16x16x32_bf16 v[76:79], v[156:159], a[152:155], v[76:79]\n" \
"v_mfma_f32_16x16x32_bf16 v[108:111], v[152:155], a[140:143], v[108:111]\n" \
"v_mfma_f32_16x16x32_bf16 v[108:111], v[156:159], a[156:159], v[108:111]\n" \
"s_waitcnt vmcnt(29) lgkmcnt(0)\n" \
"s_barrier\n" \
"v_mfma_f32_16x16x32_bf16 v[16:19], v[160:163], a[128:131], v[16:19]\n" \
"ds_read_b64_tr_b16 v[128:129], v225 offset:16384\n" \
"v_mfma_f32_16x16x32_bf16 v[16:19], v[164:167], a[144:147], v[16:19]\n" \
"ds_read_b64_tr_b16 v[130:131], v225 offset:16640\n" \
"v_mfma_f32_16x16x32_bf16 v[48:51], v[160:163], a[132:135], v[48:51]\n" \
"s_add_u32 s49, s49, s55\n" \
"s_mov_b32 m0, s44\n" \
"s_nop 0\n" \
"buffer_load_dwordx4 v226, %[srdw], s49 offen offset:0 lds\n" \
"v_mfma_f32_16x16x32_bf16 v[48:51], v[164:167], a[148:151], v[48:51]\n" \
"ds_read_b64_tr_b16 v[132:133], v225 offset:24576\n" \
"v_mfma_f32_16x16x32_bf16 v[80:83], v[160:163], a[136:139], v[80:83]\n" \
"ds_read_b64_tr_b16 v[134:135], v225 offset:24832\n" \
"v_mfma_f32_16x16x32_bf16 v[80:83], v[164:167], a[152:155], v[80:83]\n" \
"v_mfma_f32_16x16x32_bf16 v[112:115], v[160:163], a[140:143], v[112:115]\n" \
"ds_read_b64_tr_b16 v[136:137], v225 offset:16392\n" \
"v_mfma_f32_16x16x32_bf16 v[112:115], v[164:167], a[156:159], v[112:115]\n" \
"ds_read_b64_tr_b16 v[138:139], v225 offset:16648\n" \
"v_mfma_f32_16x16x32_bf16 v[20:23], v[168:171], a[128:131], v[20:23]\n" \
"buffer_load_dwordx4 v216, %[srdw], s49 offen offset:1024 lds\n" \
"v_mfma_f32_16x16x32_bf16 v[20:23], v[172:175], a[144:147], v[20:23]\n" \
"ds_read_b64_tr_b16 v[140:141], v225 offset:24584\n" \
"v_mfma_f32_16x16x32_bf16 v[52:55], v[168:171], a[132:135], v[52:55]\n" \
"ds_read_b64_tr_b16 v[142:143], v225 offset:24840\n" \
"v_mfma_f32_16x16x32_bf16 v[52:55], v[172:175], a[148:151], v[52:55]\n" \
"v_mfma_f32_16x16x32_bf16 v[84:87], v[168:171], a[136:139], v[84:87]\n" \
"ds_read_b64_tr_b16 v[144:145], v225 offset:16896\n" \
"v_mfma_f32_16x16x32_bf16 v[84:87], v[172:175], a[152:155], v[84:87]\n" \
"ds_read_b64_tr_b16 v[146:147], v225 offset:17152\n" \
"v_mfma_f32_16x16x32_bf16 v[116:119], v[168:171], a[140:143], v[116:119]\n" \
"buffer_load_dwordx4 a[240:243], v227, %[srdz], s50 offen offset:3072 sc1\n" \
"v_mfma_f32_16x16x32_bf16 v[116:119], v[172:175], a[156:159], v[116:119]\n" \
"ds_read_b64_tr_b16 v[148:149], v225 offset:25088\n" \
"v_mfma_f32_16x16x32_bf16 v[24:27], v[176:179], a[128:131], v[24:27]\n" \
"ds_read_b64_tr_b16 v[150:151], v225 offset:25344\n" \
"v_mfma_f32_16x16x32_bf16 v[24:27], v[180:183], a[144:147], v[24:27]\n" \
"v_mfma_f32_16x16x32_bf16 v[56:59], v[176:179], a[132:135], v[56:59]\n" \
"ds_read_b64_tr_b16 v[152:153], v225 offset:16904\n" \
"v_mfma_f32_16x16x32_bf16 v[56:59], v[180:183], a[148:151], v[56:59]\n" \
"ds_read_b64_tr_b16 v[154:155], v225 offset:17160\n" \
"v_mfma_f32_16x16x32_bf16 v[88:91], v[176:179], a[136:139], v[88:91]\n" \
"buffer_load_dwordx4 a[244:247], v219, %[srdz], s50 offen offset:3072 sc1\n" \
"v_mfma_f32_16x16x32_bf16 v[88:91], v[180:183], a[152:155], v[88:91]\n" \
"ds_read_b64_tr_b16 v[156:157], v225 offset:25096\n" \
"v_mfma_f32_16x16x32_bf16 v[120:123], v[176:179], a[140:143], v[120:123]\n" \
"ds_read_b64_tr_b16 v[158:159], v225 offset:25352\n" \
"v_mfma_f32_16x16x32_bf16 v[120:123], v[180:183], a[156:159], v[120:123]\n" \
"v_mfma_f32_16x16x32_bf16 v[28:31], v[184:187], a[128:131], v[28:31]\n" \
"buffer_load_dwordx4 a[248:251], v220, %[srdz], s50 offen offset:3072 sc1\n" \
"v_mfma_f32_16x16x32_bf16 v[28:31], v[188:191], a[144:147], v[28:31]\n" \
"v_mfma_f32_16x16x32_bf16 v[60:63], v[184:187], a[132:135], v[60:63]\n" \
"v_mfma_f32_16x16x32_bf16 v[60:63], v[188:191], a[148:151], v[60:63]\n" \
"v_mfma_f32_16x16x32_bf16 v[92:95], v[184:187], a[136:139], v[92:95]\n" \
"buffer_load_dwordx4 a[252:255], v221, %[srdz], s50 offen offset:3072 sc1\n" \
"v_mfma_f32_16x16x32_bf16 v[92:95], v[188:191], a[152:155], v[92:95]\n" \
"v_mfma_f32_16x16x32_bf16 v[124:127], v[184:187], a[140:143], v[124:127]\n" \
"v_mfma_f32_16x16x32_bf16 v[124:127], v[188:191], a[156:159], v[124:127]\n" \
"s_waitcnt vmcnt(20) lgkmcnt(0)\n" \
"v_mfma_f32_16x16x32_bf16 v[0:3], v[128:131], a[160:163], v[0:3]\n" \
"ds_read_b64_tr_b16 v[160:161], v225 offset:17408\n" \
"v_mfma_f32_16x16x32_bf16 v[0:3], v[132:135], a[176:179], v[0:3]\n" \
"ds_read_b64_tr_b16 v[162:163], v225 offset:17664\n" \
"v_mfma_f32_16x16x32_bf16 v[32:35], v[128:131], a[164:167], v[32:35]\n" \
"s_mov_b32 m0, s44\n" \
"s_nop 0\n" \
"buffer_load_dwordx4 v217, %[srdw], s49 offen offset:2048 lds\n" \
"v_mfma_f32_16x16x32_bf16 v[32:35], v[132:135], a[180:183], v[32:35]\n" \
"ds_read_b64_tr_b16 v[164:165], v225 offset:25600\n" \
"v_mfma_f32_16x16x32_bf16 v[64:67], v[128:131], a[168:171], v[64:67]\n" \
"ds_read_b64_tr_b16 v[166:167], v225 offset:25856\n" \
"v_mfma_f32_16x16x32_bf16 v[64:67], v[132:135], a[184:187], v[64:67]\n" \
"v_mfma_f32_16x16x32_bf16 v[96:99], v[128:131], a[172:175], v[96:99]\n" \
"ds_read_b64_tr_b16 v[168:169], v225 offset:17416\n" \
"v_mfma_f32_16x16x32_bf16 v[96:99], v[132:135], a[188:191], v[96:99]\n" \
"ds_read_b64_tr_b16 v[170:171], v225 offset:17672\n" \
"v_mfma_f32_16x16x32_bf16 v[4:7], v[136:139], a[160:163], v[4:7]\n" \
"buffer_load_dwordx4 v218, %[srdw], s49 offen offset:3072 lds\n" \
"v_mfma_f32_16x16x32_bf16 v[4:7], v[140:143], a[176:179], v[4:7]\n" \
"ds_read_b64_tr_b16 v[172:173], v225 offset:25608\n" \
"v_mfma_f32_16x16x32_bf16 v[36:39], v[136:139], a[164:167], v[36:39]\n" \
"ds_read_b64_tr_b16 v[174:175], v225 offset:25864\n" \
"v_mfma_f32_16x16x32_bf16 v[36:39], v[140:143], a[180:183], v[36:39]\n" \
"v_mfma_f32_16x16x32_bf16 v[68:71], v[136:139], a[168:171], v[68:71]\n" \
"ds_read_b64_tr_b16 v[176:177], v225 offset:17920\n" \
"v_mfma_f32_16x16x32_bf16 v[68:71], v[140:143], a[184:187], v[68:71]\n" \
"ds_read_b64_tr_b16 v[178:179], v225 offset:18176\n" \
"v_mfma_f32_16x16x32_bf16 v[100:103], v[136:139], a[172:175], v[100:103]\n" \
"s_add_u32 s50, s50, s53\n" \
"buffer_load_dwordx4 a[128:131], v227, %[srdz], s50 offen offset:0 sc1\n" \
"v_mfma_f32_16x16x32_bf16 v[100:103], v[140:143], a[188:191], v[100:103]\n" \
"ds_read_b64_tr_b16 v[180:181], v225 offset:26112\n" \
"v_mfma_f32_16x16x32_bf16 v[8:11], v[144:147], a[160:163], v[8:11]\n" \
"ds_read_b64_tr_b16 v[182:183], v225 offset:26368\n" \
"v_mfma_f32_16x16x32_bf16 v[8:11], v[148:151], a[176:179], v[8:11]\n" \
"v_mfma_f32_16x16x32_bf16 v[40:43], v[144:147], a[164:167], v[40:43]\n" \
"ds_read_b64_tr_b16 v[184:185], v225 offset:17928\n" \
"v_mfma_f32_16x16x32_bf16 v[40:43], v[148:151], a[180:183], v[40:43]\n" \
"ds_read_b64_tr_b16 v[186:187], v225 offset:18184\n" \
"v_mfma_f32_16x16x32_bf16 v[72:75], v[144:147], a[168:171], v[72:75]\n" \
"buffer_load_dwordx4 a[132:135], v219, %[srdz], s50 offen offset:0 sc1\n" \
"v_mfma_f32_16x16x32_bf16 v[72:75], v[148:151], a[184:187], v[72:75]\n" \
"ds_read_b64_tr_b16 v[188:189], v225 offset:26120\n" \
"v_mfma_f32_16x16x32_bf16 v[104:107], v[144:147], a[172:175], v[104:107]\n" \
"ds_read_b64_tr_b16 v[190:191], v225 offset:26376\n" \
"v_mfma_f32_16x16x32_bf16 v[104:107], v[148:151], a[188:191], v[104:107]\n" \
"v_mfma_f32_16x16x32_bf16 v[12:15], v[152:155], a[160:163], v[12:15]\n" \
"buffer_load_dwordx4 a[136:139], v220, %[srdz], s50 offen offset:0 sc1\n" \
"v_mfma_f32_16x16x32_bf16 v[12:15], v[156:159], a[176:179], v[12:15]\n" \
"v_mfma_f32_16x16x32_bf16 v[44:47], v[152:155], a[164:167], v[44:47]\n" \
"v_mfma_f32_16x16x32_bf16 v[44:47], v[156:159], a[180:183], v[44:47]\n" \
"v_mfma_f32_16x16x32_bf16 v[76:79], v[152:155], a[168:171], v[76:79]\n" \
"buffer_load_dwordx4 a[140:143], v221, %[srdz], s50 offen offset:0 sc1\n" \
"v_mfma_f32_16x16x32_bf16 v[76:79], v[156:159], a[184:187], v[76:79]\n" \
"v_mfma_f32_16x16x32_bf16 v[108:111], v[152:155], a[172:175], v[108:111]\n" \
"v_mfma_f32_16x16x32_bf16 v[108:111], v[156:159], a[188:191], v[108:111]\n" \
"s_waitcnt vmcnt(29) lgkmcnt(0)\n" \
"s_barrier\n" \
"v_mfma_f32_16x16x32_bf16 v[16:19], v[160:163], a[160:163], v[16:19]\n" \
"ds_read_b64_tr_b16 v[128:129], v225 offset:32768\n" \
"v_mfma_f32_16x16x32_bf16 v[16:19], v[164:167], a[176:179], v[16:19]\n" \
"ds_read_b64_tr_b16 v[130:131], v225 offset:33024\n" \
"v_mfma_f32_16x16x32_bf16 v[48:51], v[160:163], a[164:167], v[48:51]\n" \
"s_add_u32 s49, s49, s55\n" \
"s_mov_b32 m0, s45\n" \
"s_nop 0\n" \
"buffer_load_dwordx4 v226, %[srdw], s49 offen offset:0 lds\n" \
"v_mfma_f32_16x16x32_bf16 v[48:51], v[164:167], a[180:183], v[48:51]\n" \
"ds_read_b64_tr_b16 v[132:133], v225 offset:40960\n" \
"v_mfma_f32_16x16x32_bf16 v[80:83], v[160:163], a[168:171], v[80:83]\n" \
"ds_read_b64_tr_b16 v[134:135], v225 offset:41216\n" \
"v_mfma_f32_16x16x32_bf16 v[80:83], v[164:167], a[184:187], v[80:83]\n" \
"v_mfma_f32_16x16x32_bf16 v[112:115], v[160:163], a[172:175], v[112:115]\n" \
"ds_read_b64_tr_b16 v[136:137], v225 offset:32776\n" \
"v_mfma_f32_16x16x32_bf16 v[112:115], v[164:167], a[188:191], v[112:115]\n" \
"ds_read_b64_tr_b16 v[138:139], v225 offset:33032\n" \
"v_mfma_f32_16x16x32_bf16 v[20:23], v[168:171], a[160:163], v[20:23]\n" \
"buffer_load_dwordx4 v216, %[srdw], s49 offen offset:1024 lds\n" \
"v_mfma_f32_16x16x32_bf16 v[20:23], v[172:175], a[176:179], v[20:23]\n" \
"ds_read_b64_tr_b16 v[140:141], v225 offset:40968\n" \
"v_mfma_f32_16x16x32_bf16 v[52:55], v[168:171], a[164:167], v[52:55]\n" \
"ds_read_b64_tr_b16 v[142:143], v225 offset:41224\n" \
"v_mfma_f32_16x16x32_bf16 v[52:55], v[172:175], a[180:183], v[52:55]\n" \
"v_mfma_f32_16x16x32_bf16 v[84:87], v[168:171], a[168:171], v[84:87]\n" \
"ds_read_b64_tr_b16 v[144:145], v225 offset:33280\n" \
"v_mfma_f32_16x16x32_bf16 v[84:87], v[172:175], a[184:187], v[84:87]\n" \
"ds_read_b64_tr_b16 v[146:147], v225 offset:33536\n" \
"v_mfma_f32_16x16x32_bf16 v[116:119], v[168:171], a[172:175], v[116:119]\n" \
"buffer_load_dwordx4 a[144:147], v227, %[srdz], s50 offen offset:1024 sc1\n" \
"v_mfma_f32_16x16x32_bf16 v[116:119], v[172:175], a[188:191], v[116:119]\n" \
"ds_read_b64_tr_b16 v[148:149], v225 offset:41472\n" \
"v_mfma_f32_16x16x32_bf16 v[24:27], v[176:179], a[160:163], v[24:27]\n" \
"ds_read_b64_tr_b16 v[150:151], v225 offset:41728\n" \
"v_mfma_f32_16x16x32_bf16 v[24:27], v[180:183], a[176:179], v[24:27]\n" \
"v_mfma_f32_16x16x32_bf16 v[56:59], v[176:179], a[164:167], v[56:59]\n" \
"ds_read_b64_tr_b16 v[152:153], v225 offset:33288\n" \
"v_mfma_f32_16x16x32_bf16 v[56:59], v[180:183], a[180:183], v[56:59]\n" \
"ds_read_b64_tr_b16 v[154:155], v225 offset:33544\n" \
"v_mfma_f32_16x16x32_bf16 v[88:91], v[176:179], a[168:171], v[88:91]\n" \
"buffer_load_dwordx4 a[148:151], v219, %[srdz], s50 offen offset:1024 sc1\n" \
"v_mfma_f32_16x16x32_bf16 v[88:91], v[180:183], a[184:187], v[88:91]\n" \
"ds_read_b64_tr_b16 v[156:157], v225 offset:41480\n" \
"v_mfma_f32_16x16x32_bf16 v[120:123], v[176:179], a[172:175], v[120:123]\n" \
"ds_read_b64_tr_b16 v[158:159], v225 offset:41736\n" \
"v_mfma_f32_16x16x32_bf16 v[120:123], v[180:183], a[188:191], v[120:123]\n" \
"v_mfma_f32_16x16x32_bf16 v[28:31], v[184:187], a[160:163], v[28:31]\n" \
"buffer_load_dwordx4 a[152:155], v220, %[srdz], s50 offen offset:1024 sc1\n" \
"v_mfma_f32_16x16x32_bf16 v[28:31], v[188:191], a[176:179], v[28:31]\n" \
"v_mfma_f32_16x16x32_bf16 v[60:63], v[184:187], a[164:167], v[60:63]\n" \
"v_mfma_f32_16x16x32_bf16 v[60:63], v[188:191], a[180:183], v[60:63]\n" \
"v_mfma_f32_16x16x32_bf16 v[92:95], v[184:187], a[168:171], v[92:95]\n" \
"buffer_load_dwordx4 a[156:159], v221, %[srdz], s50 offen offset:1024 sc1\n" \
"v_mfma_f32_16x16x32_bf16 v[92:95], v[188:191], a[184:187], v[92:95]\n" \
"v_mfma_f32_16x16x32_bf16 v[124:127], v[184:187], a[172:175], v[124:127]\n" \
"v_mfma_f32_16x16x32_bf16 v[124:127], v[188:191], a[188:191], v[124:127]\n" \
"s_waitcnt vmcnt(24) lgkmcnt(0)\n" \
"v_mfma_f32_16x16x32_bf16 v[0:3], v[128:131], a[192:195], v[0:3]\n" \
"ds_read_b64_tr_b16 v[160:161], v225 offset:33792\n" \
"v_mfma_f32_16x16x32_bf16 v[0:3], v[132:135], a[208:211], v[0:3]\n" \
"ds_read_b64_tr_b16 v[162:163], v225 offset:34048\n" \
"v_mfma_f32_16x16x32_bf16 v[32:35], v[128:131], a[196:199], v[32:35]\n" \
"s_mov_b32 m0, s45\n" \
"s_nop 0\n" \
"buffer_load_dwordx4 v217, %[srdw], s49 offen offset:2048 lds\n" \
"v_mfma_f32_16x16x32_bf16 v[32:35], v[132:135], a[212:215], v[32:35]\n" \
"ds_read_b64_tr_b16 v[164:165], v225 offset:41984\n" \
"v_mfma_f32_16x16x32_bf16 v[64:67], v[128:131], a[200:203], v[64:67]\n" \
"ds_read_b64_tr_b16 v[166:167], v225 offset:42240\n" \
"v_mfma_f32_16x16x32_bf16 v[64:67], v[132:135], a[216:219], v[64:67]\n" \
"v_mfma_f32_16x16x32_bf16 v[96:99], v[128:131], a[204:207], v[96:99]\n" \
"ds_read_b64_tr_b16 v[168:169], v225 offset:33800\n" \
"v_mfma_f32_16x16x32_bf16 v[96:99], v[132:135], a[220:223], v[96:99]\n" \
"ds_read_b64_tr_b16 v[170:171], v225 offset:34056\n" \
"v_mfma_f32_16x16x32_bf16 v[4:7], v[136:139], a[192:195], v[4:7]\n" \
"buffer_load_dwordx4 v218, %[srdw], s49 offen offset:3072 lds\n" \
"v_mfma_f32_16x16x32_bf16 v[4:7], v[140:143], a[208:211], v[4:7]\n" \
"ds_read_b64_tr_b16 v[172:173], v225 offset:41992\n" \
"v_mfma_f32_16x16x32_bf16 v[36:39], v[136:139], a[196:199], v[36:39]\n" \
"ds_read_b64_tr_b16 v[174:175], v225 offset:42248\n" \
"v_mfma_f32_16x16x32_bf16 v[36:39], v[140:143], a[212:215], v[36:39]\n" \
"v_mfma_f32_16x16x32_bf16 v[68:71], v[136:139], a[200:203], v[68:71]\n" \
"ds_read_b64_tr_b16 v[176:177], v225 offset:34304\n" \
"v_mfma_f32_16x16x32_bf16 v[68:71], v[140:143], a[216:219], v[68:71]\n" \
"ds_read_b64_tr_b16 v[178:179], v225 offset:34560\n" \
"v_mfma_f32_16x16x32_bf16 v[100:103], v[136:139], a[204:207], v[100:103]\n" \
"buffer_load_dwordx4 a[160:163], v227, %[srdz], s50 offen offset:2048 sc1\n" \
"v_mfma_f32_16x16x32_bf16 v[100:103], v[140:143], a[220:223], v[100:103]\n" \
"ds_read_b64_tr_b16 v[180:181], v225 offset:42496\n" \
"v_mfma_f32_16x16x32_bf16 v[8:11], v[144:147], a[192:195], v[8:11]\n" \
"ds_read_b64_tr_b16 v[182:183], v225 offset:42752\n" \
"v_mfma_f32_16x16x32_bf16 v[8:11], v[148:151], a[208:211], v[8:11]\n" \
"v_mfma_f32_16x16x32_bf16 v[40:43], v[144:147], a[196:199], v[40:43]\n" \
"ds_read_b64_tr_b16 v[184:185], v225 offset:34312\n" \
"v_mfma_f32_16x16x32_bf16 v[40:43], v[148:151], a[212:215], v[40:43]\n" \
"ds_read_b64_tr_b16 v[186:187], v225 offset:34568\n" \
"v_mfma_f32_16x16x32_bf16 v[72:75], v[144:147], a[200:203], v[72:75]\n" \
"buffer_load_dwordx4 a[164:167], v219, %[srdz], s50 offen offset:2048 sc1\n" \
"v_mfma_f32_16x16x32_bf16 v[72:75], v[148:151], a[216:219], v[72:75]\n" \
"ds_read_b64_tr_b16 v[188:189], v225 offset:42504\n" \
"v_mfma_f32_16x16x32_bf16 v[104:107], v[144:147], a[204:207], v[104:107]\n" \
"ds_read_b64_tr_b16 v[190:191], v225 offset:42760\n" \
"v_mfma_f32_16x16x32_bf16 v[104:107], v[148:151], a[220:223], v[104:107]\n" \
"v_mfma_f32_16x16x32_bf16 v[12:15], v[152:155], a[192:195], v[12:15]\n" \
"buffer_load_dwordx4 a[168:171], v220, %[srdz], s50 offen offset:2048 sc1\n" \
"v_mfma_f32_16x16x32_bf16 v[12:15], v[156:159], a[208:211], v[12:15]\n" \
"v_mfma_f32_16x16x32_bf16 v[44:47], v[152:155], a[196:199], v[44:47]\n" \
"v_mfma_f32_16x16x32_bf16 v[44:47], v[156:159], a[212:215], v[44:47]\n" \
"v_mfma_f32_16x16x32_bf16 v[76:79], v[152:155], a[200:203], v[76:79]\n" \
"buffer_load_dwordx4 a[172:175], v221, %[srdz], s50 offen offset:2048 sc1\n" \
"v_mfma_f32_16x16x32_bf16 v[76:79], v[156:159], a[216:219], v[76:79]\n" \
"v_mfma_f32_16x16x32_bf16 v[108:111], v[152:155], a[204:207], v[108:111]\n" \
"v_mfma_f32_16x16x32_bf16 v[108:111], v[156:159], a[220:223], v[108:111]\n" \
"s_waitcnt vmcnt(28) lgkmcnt(0)\n" \
"s_barrier\n" \
"v_mfma_f32_16x16x32_bf16 v[16:19], v[160:163], a[192:195], v[16:19]\n" \
"ds_read_b64_tr_b16 v[128:129], v225 offset:49152\n" \
"v_mfma_f32_16x16x32_bf16 v[16:19], v[164:167], a[208:211], v[16:19]\n" \
"ds_read_b64_tr_b16 v[130:131], v225 offset:49408\n" \
"v_mfma_f32_16x16x32_bf16 v[48:51], v[160:163], a[196:199], v[48:51]\n" \
"s_add_u32 s49, s49, s55\n" \
"s_mov_b32 m0, s46\n" \
"s_nop 0\n" \
"buffer_load_dwordx4 v226, %[srdw], s49 offen offset:0 lds\n" \
"v_mfma_f32_16x16x32_bf16 v[48:51], v[164:167], a[212:215], v[48:51]\n" \
"ds_read_b64_tr_b16 v[132:133], v225 offset:57344\n" \
"v_mfma_f32_16x16x32_bf16 v[80:83], v[160:163], a[200:203], v[80:83]\n" \
"ds_read_b64_tr_b16 v[134:135], v225 offset:57600\n" \
"v_mfma_f32_16x16x32_bf16 v[80:83], v[164:167], a[216:219], v[80:83]\n" \
"v_mfma_f32_16x16x32_bf16 v[112:115], v[160:163], a[204:207], v[112:115]\n" \
"ds_read_b64_tr_b16 v[136:137], v225 offset:49160\n" \
"v_mfma_f32_16x16x32_bf16 v[112:115], v[164:167], a[220:223], v[112:115]\n" \
"ds_read_b64_tr_b16 v[138:139], v225 offset:49416\n" \
"v_mfma_f32_16x16x32_bf16 v[20:23], v[168:171], a[192:195], v[20:23]\n" \
"buffer_load_dwordx4 v216, %[srdw], s49 offen offset:1024 lds\n" \
"v_mfma_f32_16x16x32_bf16 v[20:23], v[172:175], a[208:211], v[20:23]\n" \
"ds_read_b64_tr_b16 v[140:141], v225 offset:57352\n" \
"v_mfma_f32_16x16x32_bf16 v[52:55], v[168:171], a[196:199], v[52:55]\n" \
"ds_read_b64_tr_b16 v[142:143], v225 offset:57608\n" \
"v_mfma_f32_16x16x32_bf16 v[52:55], v[172:175], a[212:215], v[52:55]\n" \
"v_mfma_f32_16x16x32_bf16 v[84:87], v[168:171], a[200:203], v[84:87]\n" \
"ds_read_b64_tr_b16 v[144:145], v225 offset:49664\n" \
"v_mfma_f32_16x16x32_bf16 v[84:87], v[172:175], a[216:219], v[84:87]\n" \
"ds_read_b64_tr_b16 v[146:147], v225 offset:49920\n" \
"v_mfma_f32_16x16x32_bf16 v[116:119], v[168:171], a[204:207], v[116:119]\n" \
"buffer_load_dwordx4 a[176:179], v227, %[srdz], s50 offen offset:3072 sc1\n" \
"v_mfma_f32_16x16x32_bf16 v[116:119], v[172:175], a[220:223], v[116:119]\n" \
"ds_read_b64_tr_b16 v[148:149], v225 offset:57856\n" \
"v_mfma_f32_16x16x32_bf16 v[24:27], v[176:179], a[192:195], v[24:27]\n" \
"ds_read_b64_tr_b16 v[150:151], v225 offset:58112\n" \
"v_mfma_f32_16x16x32_bf16 v[24:27], v[180:183], a[208:211], v[24:27]\n" \
"v_mfma_f32_16x16x32_bf16 v[56:59], v[176:179], a[196:199], v[56:59]\n" \
"ds_read_b64_tr_b16 v[152:153], v225 offset:49672\n" \
"v_mfma_f32_16x16x32_bf16 v[56:59], v[180:183], a[212:215], v[56:59]\n" \
"ds_read_b64_tr_b16 v[154:155], v225 offset:49928\n" \
"v_mfma_f32_16x16x32_bf16 v[88:91], v[176:179], a[200:203], v[88:91]\n" \
"buffer_load_dwordx4 a[180:183], v219, %[srdz], s50 offen offset:3072 sc1\n" \
"v_mfma_f32_16x16x32_bf16 v[88:91], v[180:183], a[216:219], v[88:91]\n" \
"ds_read_b64_tr_b16 v[156:157], v225 offset:57864\n" \
"v_mfma_f32_16x16x32_bf16 v[120:123], v[176:179], a[204:207], v[120:123]\n" \
"ds_read_b64_tr_b16 v[158:159], v225 offset:58120\n" \
"v_mfma_f32_16x16x32_bf16 v[120:123], v[180:183], a[220:223], v[120:123]\n" \
"v_mfma_f32_16x16x32_bf16 v[28:31], v[184:187], a[192:195], v[28:31]\n" \
"buffer_load_dwordx4 a[184:187], v220, %[srdz], s50 offen offset:3072 sc1\n" \
"v_mfma_f32_16x16x32_bf16 v[28:31], v[188:191], a[208:211], v[28:31]\n" \
"v_mfma_f32_16x16x32_bf16 v[60:63], v[184:187], a[196:199], v[60:63]\n" \
"v_mfma_f32_16x16x32_bf16 v[60:63], v[188:191], a[212:215], v[60:63]\n" \
"v_mfma_f32_16x16x32_bf16 v[92:95], v[184:187], a[200:203], v[92:95]\n" \
"buffer_load_dwordx4 a[188:191], v221, %[srdz], s50 offen offset:3072 sc1\n" \
"v_mfma_f32_16x16x32_bf16 v[92:95], v[188:191], a[216:219], v[92:95]\n" \
"v_mfma_f32_16x16x32_bf16 v[124:127], v[184:187], a[204:207], v[124:127]\n" \
"v_mfma_f32_16x16x32_bf16 v[124:127], v[188:191], a[220:223], v[124:127]\n" \
"s_waitcnt vmcnt(24) lgkmcnt(0)\n" \
"v_mfma_f32_16x16x32_bf16 v[0:3], v[128:131], a[224:227], v[0:3]\n" \
"ds_read_b64_tr_b16 v[160:161], v225 offset:50176\n" \
"v_mfma_f32_16x16x32_bf16 v[0:3], v[132:135], a[240:243], v[0:3]\n" \
"ds_read_b64_tr_b16 v[162:163], v225 offset:50432\n" \
"v_mfma_f32_16x16x32_bf16 v[32:35], v[128:131], a[228:231], v[32:35]\n" \
"s_mov_b32 m0, s46\n" \
"s_nop 0\n" \
"buffer_load_dwordx4 v217, %[srdw], s49 offen offset:2048 lds\n" \
"v_mfma_f32_16x16x32_bf16 v[32:35], v[132:135], a[244:247], v[32:35]\n" \
"ds_read_b64_tr_b16 v[164:165], v225 offset:58368\n" \
"v_mfma_f32_16x16x32_bf16 v[64:67], v[128:131], a[232:235], v[64:67]\n" \
"ds_read_b64_tr_b16 v[166:167], v225 offset:58624\n" \
"v_mfma_f32_16x16x32_bf16 v[64:67], v[132:135], a[248:251], v[64:67]\n" \
"v_mfma_f32_16x16x32_bf16 v[96:99], v[128:131], a[236:239], v[96:99]\n" \
"ds_read_b64_tr_b16 v[168:169], v225 offset:50184\n" \
"v_mfma_f32_16x16x32_bf16 v[96:99], v[132:135], a[252:255], v[96:99]\n" \
"ds_read_b64_tr_b16 v[170:171], v225 offset:50440\n" \
"v_mfma_f32_16x16x32_bf16 v[4:7], v[136:139], a[224:227], v[4:7]\n" \
"buffer_load_dwordx4 v218, %[srdw], s49 offen offset:3072 lds\n" \
"v_mfma_f32_16x16x32_bf16 v[4:7], v[140:143], a[240:243], v[4:7]\n" \
"ds_read_b64_tr_b16 v[172:173], v225 offset:58376\n" \
"v_mfma_f32_16x16x32_bf16 v[36:39], v[136:139], a[228:231], v[36:39]\n" \
"ds_read_b64_tr_b16 v[174:175], v225 offset:58632\n" \
"v_mfma_f32_16x16x32_bf16 v[36:39], v[140:143], a[244:247], v[36:39]\n" \
"v_mfma_f32_16x16x32_bf16 v[68:71], v[136:139], a[232:235], v[68:71]\n" \
"ds_read_b64_tr_b16 v[176:177], v225 offset:50688\n" \
"v_mfma_f32_16x16x32_bf16 v[68:71], v[140:143], a[248:251], v[68:71]\n" \
"ds_read_b64_tr_b16 v[178:179], v225 offset:50944\n" \
"v_mfma_f32_16x16x32_bf16 v[100:103], v[136:139], a[236:239], v[100:103]\n" \
"s_add_u32 s50, s50, s53\n" \
"buffer_load_dwordx4 a[192:195], v227, %[srdz], s50 offen offset:0 sc1\n" \
"v_mfma_f32_16x16x32_bf16 v[100:103], v[140:143], a[252:255], v[100:103]\n" \
"ds_read_b64_tr_b16 v[180:181], v225 offset:58880\n" \
"v_mfma_f32_16x16x32_bf16 v[8:11], v[144:147], a[224:227], v[8:11]\n" \
"ds_read_b64_tr_b16 v[182:183], v225 offset:59136\n" \
"v_mfma_f32_16x16x32_bf16 v[8:11], v[148:151], a[240:243], v[8:11]\n" \
"v_mfma_f32_16x16x32_bf16 v[40:43], v[144:147], a[228:231], v[40:43]\n" \
"ds_read_b64_tr_b16 v[184:185], v225 offset:50696\n" \
"v_mfma_f32_16x16x32_bf16 v[40:43], v[148:151], a[244:247], v[40:43]\n" \
"ds_read_b64_tr_b16 v[186:187], v225 offset:50952\n" \
"v_mfma_f32_16x16x32_bf16 v[72:75], v[144:147], a[232:235], v[72:75]\n" \
"buffer_load_dwordx4 a[196:199], v219, %[srdz], s50 offen offset:0 sc1\n" \
"v_mfma_f32_16x16x32_bf16 v[72:75], v[148:151], a[248:251], v[72:75]\n" \
"ds_read_b64_tr_b16 v[188:189], v225 offset:58888\n" \
"v_mfma_f32_16x16x32_bf16 v[104:107], v[144:147], a[236:239], v[104:107]\n" \
"ds_read_b64_tr_b16 v[190:191], v225 offset:59144\n" \
"v_mfma_f32_16x16x32_bf16 v[104:107], v[148:151], a[252:255], v[104:107]\n" \
"v_mfma_f32_16x16x32_bf16 v[12:15], v[152:155], a[224:227], v[12:15]\n" \
"buffer_load_dwordx4 a[200:203], v220, %[srdz], s50 offen offset:0 sc1\n" \
"v_mfma_f32_16x16x32_bf16 v[12:15], v[156:159], a[240:243], v[12:15]\n" \
"v_mfma_f32_16x16x32_bf16 v[44:47], v[152:155], a[228:231], v[44:47]\n" \
"buffer_load_dword v237, v236, %[srdw], s60 offen\n" \
"s_add_u32 s60, s60, 0x200000\n" \
"v_mfma_f32_16x16x32_bf16 v[44:47], v[156:159], a[244:247], v[44:47]\n" \
"v_mfma_f32_16x16x32_bf16 v[76:79], v[152:155], a[232:235], v[76:79]\n" \
"buffer_load_dwordx4 a[204:207], v221, %[srdz], s50 offen offset:0 sc1\n" \
"v_mfma_f32_16x16x32_bf16 v[76:79], v[156:159], a[248:251], v[76:79]\n" \
".if . - adaln_body != 4096\n" \
".error \"size model\"\n" \
".endif\n" \
"Lk0_%=:\n" \
"s_cbranch_execz Lk1_%=\n" \
"s_nop 0\n" \
"v_mfma_f32_16x16x32_bf16 v[108:111], v[152:155], a[236:239], v[108:111]\n" \
"v_mfma_f32_16x16x32_bf16 v[108:111], v[156:159], a[252:255], v[108:111]\n" \
"s_waitcnt vmcnt(29) lgkmcnt(0)\n" \
"s_barrier\n" \
"v_mfma_f32_16x16x32_bf16 v[16:19], v[160:163], a[224:227], v[16:19]\n" \
"ds_read_b64_tr_b16 v[128:129], v225 offset:0\n" \
"v_mfma_f32_16x16x32_bf16 v[16:19], v[164:167], a[240:243], v[16:19]\n" \
"ds_read_b64_tr_b16 v[130:131], v225 offset:256\n" \
"v_mfma_f32_16x16x32_bf16 v[48:51], v[160:163], a[228:231], v[48:51]\n" \
"s_add_u32 s49, s49, s55\n" \
"s_mov_b32 m0, s47\n" \
"s_nop 0\n" \
"buffer_load_dwordx4 v226, %[srdw], s49 offen offset:0 lds\n" \
"v_mfma_f32_16x16x32_bf16 v[48:51], v[164:167], a[244:247], v[48:51]\n" \
"ds_read_b64_tr_b16 v[132:133], v225 offset:8192\n" \
"v_mfma_f32_16x16x32_bf16 v[80:83], v[160:163], a[232:235], v[80:83]\n" \
"ds_read_b64_tr_b16 v[134:135], v225 offset:8448\n" \
"v_mfma_f32_16x16x32_bf16 v[80:83], v[164:167], a[248:251], v[80:83]\n" \
"v_mfma_f32_16x16x32_bf16 v[112:115], v[160:163], a[236:239], v[112:115]\n" \
"ds_read_b64_tr_b16 v[136:137], v225 offset:8\n" \
"v_mfma_f32_16x16x32_bf16 v[112:115], v[164:167], a[252:255], v[112:115]\n" \
"ds_read_b64_tr_b16 v[138:139], v225 offset:264\n" \
"v_mfma_f32_16x16x32_bf16 v[20:23], v[168:171], a[224:227], v[20:23]\n" \
"buffer_load_dwordx4 v216, %[srdw], s49 offen offset:1024 lds\n" \
"v_mfma_f32_16x16x32_bf16 v[20:23], v[172:175], a[240:243], v[20:23]\n" \
"ds_read_b64_tr_b16 v[140:141], v225 offset:8200\n" \
"v_mfma_f32_16x16x32_bf16 v[52:55], v[168:171], a[228:231], v[52:55]\n" \
"ds_read_b64_tr_b16 v[142:143], v225 offset:8456\n" \
"v_mfma_f32_16x16x32_bf16 v[52:55], v[172:175], a[244:247], v[52:55]\n" \
"v_mfma_f32_16x16x32_bf16 v[84:87], v[168:171], a[232:235], v[84:87]\n" \
"ds_read_b64_tr_b16 v[144:145], v225 offset:512\n" \
"v_mfma_f32_16x16x32_bf16 v[84:87], v[172:175], a[248:251], v[84:87]\n" \
"ds_read_b64_tr_b16 v[146:147], v225 offset:768\n" \
"v_mfma_f32_16x16x32_bf16 v[116:119], v[168:171], a[236:239], v[116:119]\n" \
"buffer_load_dwordx4 a[208:211], v227, %[srdz], s50 offen offset:1024 sc1\n" \
"v_mfma_f32_16x16x32_bf16 v[116:119], v[172:175], a[252:255], v[116:119]\n" \
"ds_read_b64_tr_b16 v[148:149], v225 offset:8704\n" \
"v_mfma_f32_16x16x32_bf16 v[24:27], v[176:179], a[224:227], v[24:27]\n" \
"ds_read_b64_tr_b16 v[150:151], v225 offset:8960\n" \
"v_mfma_f32_16x16x32_bf16 v[24:27], v[180:183], a[240:243], v[24:27]\n" \
"v_mfma_f32_16x16x32_bf16 v[56:59], v[176:179], a[228:231], v[56:59]\n" \
"ds_read_b64_tr_b16 v[152:153], v225 offset:520\n" \
"v_mfma_f32_16x16x32_bf16 v[56:59], v[180:183], a[244:247], v[56:59]\n" \
"ds_read_b64_tr_b16 v[154:155], v225 offset:776\n" \
"v_mfma_f32_16x16x32_bf16 v[88:91], v[176:179], a[232:235], v[88:91]\n" \
"buffer_load_dwordx4 a[212:215], v219, %[srdz], s50 offen offset:1024 sc1\n" \
"v_mfma_f32_16x16x32_bf16 v[88:91], v[180:183], a[248:251], v[88:91]\n" \
"ds_read_b64_tr_b16 v[156:157], v225 offset:8712\n" \
"v_mfma_f32_16x16x32_bf16 v[120:123], v[176:179], a[236:239], v[120:123]\n" \
"ds_read_b64_tr_b16 v[158:159], v225 offset:8968\n" \
"v_mfma_f32_16x16x32_bf16 v[120:123], v[180:183], a[252:255], v[120:123]\n" \
"v_mfma_f32_16x16x32_bf16 v[28:31], v[184:187], a[224:227], v[28:31]\n" \
"buffer_load_dwordx4 a[216:219], v220, %[srdz], s50 offen offset:1024 sc1\n" \
"v_mfma_f32_16x16x32_bf16 v[28:31], v[188:191], a[240:243], v[28:31]\n" \
"v_mfma_f32_16x16x32_bf16 v[60:63], v[184:187], a[228:231], v[60:63]\n" \
"v_mfma_f32_16x16x32_bf16 v[60:63], v[188:191], a[244:247], v[60:63]\n" \
"v_mfma_f32_16x16x32_bf16 v[92:95], v[184:187], a[232:235], v[92:95]\n" \
"buffer_load_dwordx4 a[220:223], v221, %[srdz], s50 offen offset:1024 sc1\n" \
"v_mfma_f32_16x16x32_bf16 v[92:95], v[188:191], a[248:251], v[92:95]\n" \
"v_mfma_f32_16x16x32_bf16 v[124:127], v[184:187], a[236:239], v[124:127]\n" \
"v_mfma_f32_16x16x32_bf16 v[124:127], v[188:191], a[252:255], v[124:127]\n" \
"s_waitcnt vmcnt(25) lgkmcnt(0)\n" \
"s_sub_u32 s51, s51, 1\n" \
"s_cmp_lg_u32 s51, 0\n" \
"s_cbranch_scc1 Lq_%=\n" \
"s_nop 0\n" \
"v_mfma_f32_16x16x32_bf16 v[0:3], v[128:131], a[128:131], v[0:3]\n" \
"ds_read_b64_tr_b16 v[160:161], v225 offset:1024\n" \
"v_mfma_f32_16x16x32_bf16 v[0:3], v[132:135], a[144:147], v[0:3]\n" \
"ds_read_b64_tr_b16 v[162:163], v225 offset:1280\n" \
"v_mfma_f32_16x16x32_bf16 v[32:35], v[128:131], a[132:135], v[32:35]\n" \
"s_mov_b32 m0, s47\n" \
"s_nop 0\n" \
"buffer_load_dwordx4 v217, %[srdw], s49 offen offset:2048 lds\n" \
"v_mfma_f32_16x16x32_bf16 v[32:35], v[132:135], a[148:151], v[32:35]\n" \
"ds_read_b64_tr_b16 v[164:165], v225 offset:9216\n" \
"v_mfma_f32_16x16x32_bf16 v[64:67], v[128:131], a[136:139], v[64:67]\n" \
"ds_read_b64_tr_b16 v[166:167], v225 offset:9472\n" \
"v_mfma_f32_16x16x32_bf16 v[64:67], v[132:135], a[152:155], v[64:67]\n" \
"v_mfma_f32_16x16x32_bf16 v[96:99], v[128:131], a[140:143], v[96:99]\n" \
"ds_read_b64_tr_b16 v[168:169], v225 offset:1032\n" \
"v_mfma_f32_16x16x32_bf16 v[96:99], v[132:135], a[156:159], v[96:99]\n" \
"ds_read_b64_tr_b16 v[170:171], v225 offset:1288\n" \
"v_mfma_f32_16x16x32_bf16 v[4:7], v[136:139], a[128:131], v[4:7]\n" \
"buffer_load_dwordx4 v218, %[srdw], s49 offen offset:3072 lds\n" \
"v_mfma_f32_16x16x32_bf16 v[4:7], v[140:143], a[144:147], v[4:7]\n" \
"ds_read_b64_tr_b16 v[172:173], v225 offset:9224\n" \
"v_mfma_f32_16x16x32_bf16 v[36:39], v[136:139], a[132:135], v[36:39]\n" \
"ds_read_b64_tr_b16 v[174:175], v225 offset:9480\n" \
"v_mfma_f32_16x16x32_bf16 v[36:39], v[140:143], a[148:151], v[36:39]\n" \
"v_mfma_f32_16x16x32_bf16 v[68:71], v[136:139], a[136:139], v[68:71]\n" \
"ds_read_b64_tr_b16 v[176:177], v225 offset:1536\n" \
"v_mfma_f32_16x16x32_bf16 v[68:71], v[140:143], a[152:155], v[68:71]\n" \
"ds_read_b64_tr_b16 v[178:179], v225 offset:1792\n" \
"v_mfma_f32_16x16x32_bf16 v[100:103], v[136:139], a[140:143], v[100:103]\n" \
"buffer_load_dwordx4 a[224:227], v227, %[srdz], s50 offen offset:2048 sc1\n" \
"v_mfma_f32_16x16x32_bf16 v[100:103], v[140:143], a[156:159], v[100:103]\n" \
"ds_read_b64_tr_b16 v[180:181], v225 offset:9728\n" \
"v_mfma_f32_16x16x32_bf16 v[8:11], v[144:147], a[128:131], v[8:11]\n" \
"ds_read_b64_tr_b16 v[182:183], v225 offset:9984\n" \
"v_mfma_f32_16x16x32_bf16 v[8:11], v[148:151], a[144:147], v[8:11]\n" \
"v_mfma_f32_16x16x32_bf16 v[40:43], v[144:147], a[132:135], v[40:43]\n" \
"ds_read_b64_tr_b16 v[184:185], v225 offset:1544\n" \
"v_mfma_f32_16x16x32_bf16 v[40:43], v[148:151], a[148:151], v[40:43]\n" \
"ds_read_b64_tr_b16 v[186:187], v225 offset:1800\n" \
"v_mfma_f32_16x16x32_bf16 v[72:75], v[144:147], a[136:139], v[72:75]\n" \
"buffer_load_dwordx4 a[228:231], v219, %[srdz], s50 offen offset:2048 sc1\n" \
"v_mfma_f32_16x16x32_bf16 v[72:75], v[148:151], a[152:155], v[72:75]\n" \
"ds_read_b64_tr_b16 v[188:189], v225 offset:9736\n" \
"v_mfma_f32_16x16x32_bf16 v[104:107], v[144:147], a[140:143], v[104:107]\n" \
"ds_read_b64_tr_b16 v[190:191], v225 offset:9992\n" \
"v_mfma_f32_16x16x32_bf16 v[104:107], v[148:151], a[156:159], v[104:107]\n" \
"v_mfma_f32_16x16x32_bf16 v[12:15], v[152:155], a[128:131], v[12:15]\n" \
"buffer_load_dwordx4 a[232:235], v220, %[srdz], s50 offen offset:2048 sc1\n" \
"v_mfma_f32_16x16x32_bf16 v[12:15], v[156:159], a[144:147], v[12:15]\n" \
"v_mfma_f32_16x16x32_bf16 v[44:47], v[152:155], a[132:135], v[44:47]\n" \
"v_mfma_f32_16x16x32_bf16 v[44:47], v[156:159], a[148:151], v[44:47]\n" \
"v_mfma_f32_16x16x32_bf16 v[76:79], v[152:155], a[136:139], v[76:79]\n" \
"buffer_load_dwordx4 a[236:239], v221, %[srdz], s50 offen offset:2048 sc1\n" \
"v_mfma_f32_16x16x32_bf16 v[76:79], v[156:159], a[152:155], v[76:79]\n" \
"v_mfma_f32_16x16x32_bf16 v[108:111], v[152:155], a[140:143], v[108:111]\n" \
"v_mfma_f32_16x16x32_bf16 v[108:111], v[156:159], a[156:159], v[108:111]\n" \
"s_waitcnt vmcnt(29) lgkmcnt(0)\n" \
"s_barrier\n" \
"v_mfma_f32_16x16x32_bf16 v[16:19], v[160:163], a[128:131], v[16:19]\n" \
"ds_read_b64_tr_b16 v[128:129], v225 offset:16384\n" \
"v_mfma_f32_16x16x32_bf16 v[16:19], v[164:167], a[144:147], v[16:19]\n" \
"ds_read_b64_tr_b16 v[130:131], v225 offset:16640\n" \
"v_mfma_f32_16x16x32_bf16 v[48:51], v[160:163], a[132:135], v[48:51]\n" \
"s_mov_b32 s48, 16384\n" \
"s_mov_b32 m0, s44\n" \
"s_nop 0\n" \
"buffer_load_dwordx4 %[vd0], %[srdv], s48 offen offset:0 lds\n" \
"v_mfma_f32_16x16x32_bf16 v[48:51], v[164:167], a[148:151], v[48:51]\n" \
"ds_read_b64_tr_b16 v[132:133], v225 offset:24576\n" \
"v_mfma_f32_16x16x32_bf16 v[80:83], v[160:163], a[136:139], v[80:83]\n" \
"ds_read_b64_tr_b16 v[134:135], v225 offset:24832\n" \
"v_mfma_f32_16x16x32_bf16 v[80:83], v[164:167], a[152:155], v[80:83]\n" \
"v_mfma_f32_16x16x32_bf16 v[112:115], v[160:163], a[140:143], v[112:115]\n" \
"ds_read_b64_tr_b16 v[136:137], v225 offset:16392\n" \
"v_mfma_f32_16x16x32_bf16 v[112:115], v[164:167], a[156:159], v[112:115]\n" \
"ds_read_b64_tr_b16 v[138:139], v225 offset:16648\n" \
"v_mfma_f32_16x16x32_bf16 v[20:23], v[168:171], a[128:131], v[20:23]\n" \
"buffer_load_dwordx4 v215, %[srdv], s48 offen offset:1024 lds\n" \
"v_mfma_f32_16x16x32_bf16 v[20:23], v[172:175], a[144:147], v[20:23]\n" \
"ds_read_b64_tr_b16 v[140:141], v225 offset:24584\n" \
"v_mfma_f32_16x16x32_bf16 v[52:55], v[168:171], a[132:135], v[52:55]\n" \
"ds_read_b64_tr_b16 v[142:143], v225 offset:24840\n" \
"v_mfma_f32_16x16x32_bf16 v[52:55], v[172:175], a[148:151], v[52:55]\n" \
"v_mfma_f32_16x16x32_bf16 v[84:87], v[168:171], a[136:139], v[84:87]\n" \
"ds_read_b64_tr_b16 v[144:145], v225 offset:16896\n" \
"v_mfma_f32_16x16x32_bf16 v[84:87], v[172:175], a[152:155], v[84:87]\n" \
"ds_read_b64_tr_b16 v[146:147], v225 offset:17152\n" \
"v_mfma_f32_16x16x32_bf16 v[116:119], v[168:171], a[140:143], v[116:119]\n" \
"buffer_load_dwordx4 a[240:243], v227, %[srdz], s50 offen offset:3072 sc1\n" \
"v_mfma_f32_16x16x32_bf16 v[116:119], v[172:175], a[156:159], v[116:119]\n" \
"ds_read_b64_tr_b16 v[148:149], v225 offset:25088\n" \
"v_mfma_f32_16x16x32_bf16 v[24:27], v[176:179], a[128:131], v[24:27]\n" \
"ds_read_b64_tr_b16 v[150:151], v225 offset:25344\n" \
"v_mfma_f32_16x16x32_bf16 v[24:27], v[180:183], a[144:147], v[24:27]\n" \
"v_mfma_f32_16x16x32_bf16 v[56:59], v[176:179], a[132:135], v[56:59]\n" \
"ds_read_b64_tr_b16 v[152:153], v225 offset:16904\n" \
"v_mfma_f32_16x16x32_bf16 v[56:59], v[180:183], a[148:151], v[56:59]\n" \
"ds_read_b64_tr_b16 v[154:155], v225 offset:17160\n" \
"v_mfma_f32_16x16x32_bf16 v[88:91], v[176:179], a[136:139], v[88:91]\n" \
"buffer_load_dwordx4 a[244:247], v219, %[srdz], s50 offen offset:3072 sc1\n" \
"v_mfma_f32_16x16x32_bf16 v[88:91], v[180:183], a[152:155], v[88:91]\n" \
"ds_read_b64_tr_b16 v[156:157], v225 offset:25096\n" \
"v_mfma_f32_16x16x32_bf16 v[120:123], v[176:179], a[140:143], v[120:123]\n" \
"ds_read_b64_tr_b16 v[158:159], v225 offset:25352\n" \
"v_mfma_f32_16x16x32_bf16 v[120:123], v[180:183], a[156:159], v[120:123]\n" \
"v_mfma_f32_16x16x32_bf16 v[28:31], v[184:187], a[128:131], v[28:31]\n" \
"buffer_load_dwordx4 a[248:251], v220, %[srdz], s50 offen offset:3072 sc1\n" \
"v_mfma_f32_16x16x32_bf16 v[28:31], v[188:191], a[144:147], v[28:31]\n" \
"v_mfma_f32_16x16x32_bf16 v[60:63], v[184:187], a[132:135], v[60:63]\n" \
"v_mfma_f32_16x16x32_bf16 v[60:63], v[188:191], a[148:151], v[60:63]\n" \
"v_mfma_f32_16x16x32_bf16 v[92:95], v[184:187], a[136:139], v[92:95]\n" \
"buffer_load_dwordx4 a[252:255], v221, %[srdz], s50 offen offset:3072 sc1\n" \
"v_mfma_f32_16x16x32_bf16 v[92:95], v[188:191], a[152:155], v[92:95]\n" \
"v_mfma_f32_16x16x32_bf16 v[124:127], v[184:187], a[140:143], v[124:127]\n" \
"v_mfma_f32_16x16x32_bf16 v[124:127], v[188:191], a[156:159], v[124:127]\n" \
"s_waitcnt vmcnt(25) lgkmcnt(0)\n" \
"v_mfma_f32_16x16x32_bf16 v[0:3], v[128:131], a[160:163], v[0:3]\n" \
"ds_read_b64_tr_b16 v[160:161], v225 offset:17408\n" \
"v_mfma_f32_16x16x32_bf16 v[0:3], v[132:135], a[176:179], v[0:3]\n" \
"ds_read_b64_tr_b16 v[162:163], v225 offset:17664\n" \
"v_mfma_f32_16x16x32_bf16 v[32:35], v[128:131], a[164:167], v[32:35]\n" \
"s_mov_b32 s48, 16384\n" \
"s_mov_b32 m0, s44\n" \
"s_nop 0\n" \
"buffer_load_dwordx4 %[vd0], %[srdv], s48 offen offset:2048 lds\n" \
"v_mfma_f32_16x16x32_bf16 v[32:35], v[132:135], a[180:183], v[32:35]\n" \
"ds_read_b64_tr_b16 v[164:165], v225 offset:25600\n" \
"v_mfma_f32_16x16x32_bf16 v[64:67], v[128:131], a[168:171], v[64:67]\n" \
"ds_read_b64_tr_b16 v[166:167], v225 offset:25856\n" \
"v_mfma_f32_16x16x32_bf16 v[64:67], v[132:135], a[184:187], v[64:67]\n" \
"v_mfma_f32_16x16x32_bf16 v[96:99], v[128:131], a[172:175], v[96:99]\n" \
"ds_read_b64_tr_b16 v[168:169], v225 offset:17416\n" \
"v_mfma_f32_16x16x32_bf16 v[96:99], v[132:135], a[188:191], v[96:99]\n" \
"ds_read_b64_tr_b16 v[170:171], v225 offset:17672\n" \
"v_mfma_f32_16x16x32_bf16 v[4:7], v[136:139], a[160:163], v[4:7]\n" \
"buffer_load_dwordx4 v215, %[srdv], s48 offen offset:3072 lds\n" \
"v_mfma_f32_16x16x32_bf16 v[4:7], v[140:143], a[176:179], v[4:7]\n" \
"ds_read_b64_tr_b16 v[172:173], v225 offset:25608\n" \
"v_mfma_f32_16x16x32_bf16 v[36:39], v[136:139], a[164:167], v[36:39]\n" \
"ds_read_b64_tr_b16 v[174:175], v225 offset:25864\n" \
"v_mfma_f32_16x16x32_bf16 v[36:39], v[140:143], a[180:183], v[36:39]\n" \
"v_mfma_f32_16x16x32_bf16 v[68:71], v[136:139], a[168:171], v[68:71]\n" \
"ds_read_b64_tr_b16 v[176:177], v225 offset:17920\n" \
"v_mfma_f32_16x16x32_bf16 v[68:71], v[140:143], a[184:187], v[68:71]\n" \
"ds_read_b64_tr_b16 v[178:179], v225 offset:18176\n" \
"v_mfma_f32_16x16x32_bf16 v[100:103], v[136:139], a[172:175], v[100:103]\n" \
"s_mov_b32 s48, 0\n" \
"s_mov_b32 m0, s40\n" \
"s_nop 0\n" \
"buffer_load_dwordx4 %[kd0], %[srdk], s48 offen offset:0 lds\n" \
"v_mfma_f32_16x16x32_bf16 v[100:103], v[140:143], a[188:191], v[100:103]\n" \
"ds_read_b64_tr_b16 v[180:181], v225 offset:26112\n" \
"v_mfma_f32_16x16x32_bf16 v[8:11], v[144:147], a[160:163], v[8:11]\n" \
"ds_read_b64_tr_b16 v[182:183], v225 offset:26368\n" \
"v_mfma_f32_16x16x32_bf16 v[8:11], v[148:151], a[176:179], v[8:11]\n" \
"v_mfma_f32_16x16x32_bf16 v[40:43], v[144:147], a[164:167], v[40:43]\n" \
"ds_read_b64_tr_b16 v[184:185], v225 offset:17928\n" \
"v_mfma_f32_16x16x32_bf16 v[40:43], v[148:151], a[180:183], v[40:43]\n" \
"ds_read_b64_tr_b16 v[186:187], v225 offset:18184\n" \
"v_mfma_f32_16x16x32_bf16 v[72:75], v[144:147], a[168:171], v[72:75]\n" \
"buffer_load_dwordx4 v212, %[srdk], s48 offen offset:1024 lds\n" \
"v_mfma_f32_16x16x32_bf16 v[72:75], v[148:151], a[184:187], v[72:75]\n" \
"ds_read_b64_tr_b16 v[188:189], v225 offset:26120\n" \
"v_mfma_f32_16x16x32_bf16 v[104:107], v[144:147], a[172:175], v[104:107]\n" \
"ds_read_b64_tr_b16 v[190:191], v225 offset:26376\n" \
"v_mfma_f32_16x16x32_bf16 v[104:107], v[148:151], a[188:191], v[104:107]\n" \
"v_mfma_f32_16x16x32_bf16 v[12:15], v[152:155], a[160:163], v[12:15]\n" \
"buffer_load_dwordx4 v213, %[srdk], s48 offen offset:2048 lds\n" \
"v_mfma_f32_16x16x32_bf16 v[12:15], v[156:159], a[176:179], v[12:15]\n" \
"v_mfma_f32_16x16x32_bf16 v[44:47], v[152:155], a[164:167], v[44:47]\n" \
"v_mfma_f32_16x16x32_bf16 v[44:47], v[156:159], a[180:183], v[44:47]\n" \
"v_mfma_f32_16x16x32_bf16 v[76:79], v[152:155], a[168:171], v[76:79]\n" \
"buffer_load_dwordx4 v214, %[srdk], s48 offen offset:3072 lds\n" \
"v_mfma_f32_16x16x32_bf16 v[76:79], v[156:159], a[184:187], v[76:79]\n" \
"v_mfma_f32_16x16x32_bf16 v[108:111], v[152:155], a[172:175], v[108:111]\n" \
"v_mfma_f32_16x16x32_bf16 v[108:111], v[156:159], a[188:191], v[108:111]\n" \
"s_waitcnt vmcnt(29) lgkmcnt(0)\n" \
"s_barrier\n" \
"v_mfma_f32_16x16x32_bf16 v[16:19], v[160:163], a[160:163], v[16:19]\n" \
"ds_read_b64_tr_b16 v[128:129], v225 offset:32768\n" \
"v_mfma_f32_16x16x32_bf16 v[16:19], v[164:167], a[176:179], v[16:19]\n" \
"ds_read_b64_tr_b16 v[130:131], v225 offset:33024\n" \
"v_mfma_f32_16x16x32_bf16 v[48:51], v[160:163], a[164:167], v[48:51]\n" \
"s_mov_b32 s48, 32768\n" \
"s_mov_b32 m0, s45\n" \
"s_nop 0\n" \
"buffer_load_dwordx4 %[vd0], %[srdv], s48 offen offset:0 lds\n" \
"v_mfma_f32_16x16x32_bf16 v[48:51], v[164:167], a[180:183], v[48:51]\n" \
"ds_read_b64_tr_b16 v[132:133], v225 offset:40960\n" \
"v_mfma_f32_16x16x32_bf16 v[80:83], v[160:163], a[168:171], v[80:83]\n" \
"ds_read_b64_tr_b16 v[134:135], v225 offset:41216\n" \
"v_mfma_f32_16x16x32_bf16 v[80:83], v[164:167], a[184:187], v[80:83]\n" \
"v_mfma_f32_16x16x32_bf16 v[112:115], v[160:163], a[172:175], v[112:115]\n" \
"ds_read_b64_tr_b16 v[136:137], v225 offset:32776\n" \
"v_mfma_f32_16x16x32_bf16 v[112:115], v[164:167], a[188:191], v[112:115]\n" \
"ds_read_b64_tr_b16 v[138:139], v225 offset:33032\n" \
"v_mfma_f32_16x16x32_bf16 v[20:23], v[168:171], a[160:163], v[20:23]\n" \
"buffer_load_dwordx4 v215, %[srdv], s48 offen offset:1024 lds\n" \
"v_mfma_f32_16x16x32_bf16 v[20:23], v[172:175], a[176:179], v[20:23]\n" \
"ds_read_b64_tr_b16 v[140:141], v225 offset:40968\n" \
"v_mfma_f32_16x16x32_bf16 v[52:55], v[168:171], a[164:167], v[52:55]\n" \
"ds_read_b64_tr_b16 v[142:143], v225 offset:41224\n" \
"v_mfma_f32_16x16x32_bf16 v[52:55], v[172:175], a[180:183], v[52:55]\n" \
"v_mfma_f32_16x16x32_bf16 v[84:87], v[168:171], a[168:171], v[84:87]\n" \
"ds_read_b64_tr_b16 v[144:145], v225 offset:33280\n" \
"v_mfma_f32_16x16x32_bf16 v[84:87], v[172:175], a[184:187], v[84:87]\n" \
"ds_read_b64_tr_b16 v[146:147], v225 offset:33536\n" \
"v_mfma_f32_16x16x32_bf16 v[116:119], v[168:171], a[172:175], v[116:119]\n" \
"s_mov_b32 s48, 16384\n" \
"s_mov_b32 m0, s41\n" \
"s_nop 0\n" \
"buffer_load_dwordx4 %[kd0], %[srdk], s48 offen offset:0 lds\n" \
"v_mfma_f32_16x16x32_bf16 v[116:119], v[172:175], a[188:191], v[116:119]\n" \
"ds_read_b64_tr_b16 v[148:149], v225 offset:41472\n" \
"v_mfma_f32_16x16x32_bf16 v[24:27], v[176:179], a[160:163], v[24:27]\n" \
"ds_read_b64_tr_b16 v[150:151], v225 offset:41728\n" \
"v_mfma_f32_16x16x32_bf16 v[24:27], v[180:183], a[176:179], v[24:27]\n" \
"v_mfma_f32_16x16x32_bf16 v[56:59], v[176:179], a[164:167], v[56:59]\n" \
"ds_read_b64_tr_b16 v[152:153], v225 offset:33288\n" \
"v_mfma_f32_16x16x32_bf16 v[56:59], v[180:183], a[180:183], v[56:59]\n" \
"ds_read_b64_tr_b16 v[154:155], v225 offset:33544\n" \
"v_mfma_f32_16x16x32_bf16 v[88:91], v[176:179], a[168:171], v[88:91]\n" \
"buffer_load_dwordx4 v212, %[srdk], s48 offen offset:1024 lds\n" \
"v_mfma_f32_16x16x32_bf16 v[88:91], v[180:183], a[184:187], v[88:91]\n" \
"ds_read_b64_tr_b16 v[156:157], v225 offset:41480\n" \
"v_mfma_f32_16x16x32_bf16 v[120:123], v[176:179], a[172:175], v[120:123]\n" \
"ds_read_b64_tr_b16 v[158:159], v225 offset:41736\n" \
"v_mfma_f32_16x16x32_bf16 v[120:123], v[180:183], a[188:191], v[120:123]\n" \
"v_mfma_f32_16x16x32_bf16 v[28:31], v[184:187], a[160:163], v[28:31]\n" \
"buffer_load_dwordx4 v213, %[srdk], s48 offen offset:2048 lds\n" \
"v_mfma_f32_16x16x32_bf16 v[28:31], v[188:191], a[176:179], v[28:31]\n" \
"v_mfma_f32_16x16x32_bf16 v[60:63], v[184:187], a[164:167], v[60:63]\n" \
"v_mfma_f32_16x16x32_bf16 v[60:63], v[188:191], a[180:183], v[60:63]\n" \
"v_mfma_f32_16x16x32_bf16 v[92:95], v[184:187], a[168:171], v[92:95]\n" \
"buffer_load_dwordx4 v214, %[srdk], s48 offen offset:3072 lds\n" \
"v_mfma_f32_16x16x32_bf16 v[92:95], v[188:191], a[184:187], v[92:95]\n" \
"v_mfma_f32_16x16x32_bf16 v[124:127], v[184:187], a[172:175], v[124:127]\n" \
"v_mfma_f32_16x16x32_bf16 v[124:127], v[188:191], a[188:191], v[124:127]\n" \
"s_waitcnt vmcnt(24) lgkmcnt(0)\n" \
"v_mfma_f32_16x16x32_bf16 v[0:3], v[128:131], a[192:195], v[0:3]\n" \
"ds_read_b64_tr_b16 v[160:161], v225 offset:33792\n" \
"v_mfma_f32_16x16x32_bf16 v[0:3], v[132:135], a[208:211], v[0:3]\n" \
"ds_read_b64_tr_b16 v[162:163], v225 offset:34048\n" \
"v_mfma_f32_16x16x32_bf16 v[32:35], v[128:131], a[196:199], v[32:35]\n" \
"s_mov_b32 s48, 32768\n" \
"s_mov_b32 m0, s45\n" \
"s_nop 0\n" \
"buffer_load_dwordx4 %[vd0], %[srdv], s48 offen offset:2048 lds\n" \
"v_mfma_f32_16x16x32_bf16 v[32:35], v[132:135], a[212:215], v[32:35]\n" \
"ds_read_b64_tr_b16 v[164:165], v225 offset:41984\n" \
"v_mfma_f32_16x16x32_bf16 v[64:67], v[128:131], a[200:203], v[64:67]\n" \
"ds_read_b64_tr_b16 v[166:167], v225 offset:42240\n" \
"v_mfma_f32_16x16x32_bf16 v[64:67], v[132:135], a[216:219], v[64:67]\n" \
"v_mfma_f32_16x16x32_bf16 v[96:99], v[128:131], a[204:207], v[96:99]\n" \
"ds_read_b64_tr_b16 v[168:169], v225 offset:33800\n" \
"v_mfma_f32_16x16x32_bf16 v[96:99], v[132:135], a[220:223], v[96:99]\n" \
"ds_read_b64_tr_b16 v[170:171], v225 offset:34056\n" \
"v_mfma_f32_16x16x32_bf16 v[4:7], v[136:139], a[192:195], v[4:7]\n" \
"buffer_load_dwordx4 v215, %[srdv], s48 offen offset:3072 lds\n" \
"v_mfma_f32_16x16x32_bf16 v[4:7], v[140:143], a[208:211], v[4:7]\n" \
"ds_read_b64_tr_b16 v[172:173], v225 offset:41992\n" \
"v_mfma_f32_16x16x32_bf16 v[36:39], v[136:139], a[196:199], v[36:39]\n" \
"ds_read_b64_tr_b16 v[174:175], v225 offset:42248\n" \
"v_mfma_f32_16x16x32_bf16 v[36:39], v[140:143], a[212:215], v[36:39]\n" \
"v_mfma_f32_16x16x32_bf16 v[68:71], v[136:139], a[200:203], v[68:71]\n" \
"ds_read_b64_tr_b16 v[176:177], v225 offset:34304\n" \
"v_mfma_f32_16x16x32_bf16 v[68:71], v[140:143], a[216:219], v[68:71]\n" \
"ds_read_b64_tr_b16 v[178:179], v225 offset:34560\n" \
"v_mfma_f32_16x16x32_bf16 v[100:103], v[136:139], a[204:207], v[100:103]\n" \
"s_mov_b32 s48, 32768\n" \
"s_mov_b32 m0, s42\n" \
"s_nop 0\n" \
"buffer_load_dwordx4 %[kd0], %[srdk], s48 offen offset:0 lds\n" \
"v_mfma_f32_16x16x32_bf16 v[100:103], v[140:143], a[220:223], v[100:103]\n" \
"ds_read_b64_tr_b16 v[180:181], v225 offset:42496\n" \
"v_mfma_f32_16x16x32_bf16 v[8:11], v[144:147], a[192:195], v[8:11]\n" \
"ds_read_b64_tr_b16 v[182:183], v225 offset:42752\n" \
"v_mfma_f32_16x16x32_bf16 v[8:11], v[148:151], a[208:211], v[8:11]\n" \
"v_mfma_f32_16x16x32_bf16 v[40:43], v[144:147], a[196:199], v[40:43]\n" \
"ds_read_b64_tr_b16 v[184:185], v225 offset:34312\n" \
"v_mfma_f32_16x16x32_bf16 v[40:43], v[148:151], a[212:215], v[40:43]\n" \
"ds_read_b64_tr_b16 v[186:187], v225 offset:34568\n" \
"v_mfma_f32_16x16x32_bf16 v[72:75], v[144:147], a[200:203], v[72:75]\n" \
"buffer_load_dwordx4 v212, %[srdk], s48 offen offset:1024 lds\n" \
"v_mfma_f32_16x16x32_bf16 v[72:75], v[148:151], a[216:219], v[72:75]\n" \
"ds_read_b64_tr_b16 v[188:189], v225 offset:42504\n" \
"v_mfma_f32_16x16x32_bf16 v[104:107], v[144:147], a[204:207], v[104:107]\n" \
"ds_read_b64_tr_b16 v[190:191], v225 offset:42760\n" \
"v_mfma_f32_16x16x32_bf16 v[104:107], v[148:151], a[220:223], v[104:107]\n" \
"v_mfma_f32_16x16x32_bf16 v[12:15], v[152:155], a[192:195], v[12:15]\n" \
"buffer_load_dwordx4 v213, %[srdk], s48 offen offset:2048 lds\n" \
"v_mfma_f32_16x16x32_bf16 v[12:15], v[156:159], a[208:211], v[12:15]\n" \
"v_mfma_f32_16x16x32_bf16 v[44:47], v[152:155], a[196:199], v[44:47]\n" \
"v_mfma_f32_16x16x32_bf16 v[44:47], v[156:159], a[212:215], v[44:47]\n" \
"v_mfma_f32_16x16x32_bf16 v[76:79], v[152:155], a[200:203], v[76:79]\n" \
"buffer_load_dwordx4 v214, %[srdk], s48 offen offset:3072 lds\n" \
"v_mfma_f32_16x16x32_bf16 v[76:79], v[156:159], a[216:219], v[76:79]\n" \
"v_mfma_f32_16x16x32_bf16 v[108:111], v[152:155], a[204:207], v[108:111]\n" \
"v_mfma_f32_16x16x32_bf16 v[108:111], v[156:159], a[220:223], v[108:111]\n" \
"s_waitcnt vmcnt(28) lgkmcnt(0)\n" \
"s_barrier\n" \
"v_mfma_f32_16x16x32_bf16 v[16:19], v[160:163], a[192:195], v[16:19]\n" \
"ds_read_b64_tr_b16 v[128:129], v225 offset:49152\n" \
"s_mov_b32 s54, 0x4000\n" \
"v_mfma_f32_16x16x32_bf16 v[16:19], v[164:167], a[208:211], v[16:19]\n" \
"ds_read_b64_tr_b16 v[130:131], v225 offset:49408\n" \
"v_mfma_f32_16x16x32_bf16 v[48:51], v[160:163], a[196:199], v[48:51]\n" \
"s_mov_b32 s48, 49152\n" \
"s_mov_b32 m0, s46\n" \
"s_nop 0\n" \
"buffer_load_dwordx4 %[vd0], %[srdv], s48 offen offset:0 lds\n" \
"v_mbcnt_lo_u32_b32 v231, -1, 0\n" \
"v_mfma_f32_16x16x32_bf16 v[48:51], v[164:167], a[212:215], v[48:51]\n" \
"ds_read_b64_tr_b16 v[132:133], v225 offset:57344\n" \
"v_mfma_f32_16x16x32_bf16 v[80:83], v[160:163], a[200:203], v[80:83]\n" \
"ds_read_b64_tr_b16 v[134:135], v225 offset:57600\n" \
"v_mfma_f32_16x16x32_bf16 v[80:83], v[164:167], a[216:219], v[80:83]\n" \
"v_mbcnt_hi_u32_b32 v231, -1, v231\n" \
"v_mfma_f32_16x16x32_bf16 v[112:115], v[160:163], a[204:207], v[112:115]\n" \
"ds_read_b64_tr_b16 v[136:137], v225 offset:49160\n" \
"v_mfma_f32_16x16x32_bf16 v[112:115], v[164:167], a[220:223], v[112:115]\n" \
"ds_read_b64_tr_b16 v[138:139], v225 offset:49416\n" \
"v_mfma_f32_16x16x32_bf16 v[20:23], v[168:171], a[192:195], v[20:23]\n" \
"buffer_load_dwordx4 v215, %[srdv], s48 offen offset:1024 lds\n" \
"v_lshrrev_b32_e32 v232, 4, v231\n" \
"v_mfma_f32_16x16x32_bf16 v[20:23], v[172:175], a[208:211], v[20:23]\n" \
"ds_read_b64_tr_b16 v[140:141], v225 offset:57352\n" \
"v_mfma_f32_16x16x32_bf16 v[52:55], v[168:171], a[196:199], v[52:55]\n" \
"ds_read_b64_tr_b16 v[142:143], v225 offset:57608\n" \
"v_xor_b32_e64 v232, v232, v231\n" \
"v_mfma_f32_16x16x32_bf16 v[52:55], v[172:175], a[212:215], v[52:55]\n" \
"v_mfma_f32_16x16x32_bf16 v[84:87], v[168:171], a[200:203], v[84:87]\n" \
"ds_read_b64_tr_b16 v[144:145], v225 offset:49664\n" \
"v_mfma_f32_16x16x32_bf16 v[84:87], v[172:175], a[216:219], v[84:87]\n" \
"ds_read_b64_tr_b16 v[146:147], v225 offset:49920\n" \
"v_bfe_i32 v232, v232, 1, 1\n" \
"v_mfma_f32_16x16x32_bf16 v[116:119], v[168:171], a[204:207], v[116:119]\n" \
"s_mov_b32 s48, 49152\n" \
"s_mov_b32 m0, s43\n" \
"s_nop 0\n" \
"buffer_load_dwordx4 %[kd0], %[srdk], s48 offen offset:0 lds\n" \
"v_mfma_f32_16x16x32_bf16 v[116:119], v[172:175], a[220:223], v[116:119]\n" \
"ds_read_b64_tr_b16 v[148:149], v225 offset:57856\n" \
"v_mfma_f32_16x16x32_bf16 v[24:27], v[176:179], a[192:195], v[24:27]\n" \
"ds_read_b64_tr_b16 v[150:151], v225 offset:58112\n" \
"v_not_b32_e64 v232, v232\n" \
"v_mfma_f32_16x16x32_bf16 v[24:27], v[180:183], a[208:211], v[24:27]\n" \
"v_mfma_f32_16x16x32_bf16 v[56:59], v[176:179], a[196:199], v[56:59]\n" \
"ds_read_b64_tr_b16 v[152:153], v225 offset:49672\n" \
"v_mfma_f32_16x16x32_bf16 v[56:59], v[180:183], a[212:215], v[56:59]\n" \
"ds_read_b64_tr_b16 v[154:155], v225 offset:49928\n" \
"v_and_b32_e32 v230, 0x3f803f80, v232\n" \
"v_mfma_f32_16x16x32_bf16 v[88:91], v[176:179], a[200:203], v[88:91]\n" \
"buffer_load_dwordx4 v212, %[srdk], s48 offen offset:1024 lds\n" \
"v_mfma_f32_16x16x32_bf16 v[88:91], v[180:183], a[216:219], v[88:91]\n" \
"ds_read_b64_tr_b16 v[156:157], v225 offset:57864\n" \
"v_readlane_b32 s62, %[tlo], 18\n" \
"v_mfma_f32_16x16x32_bf16 v[120:123], v[176:179], a[204:207], v[120:123]\n" \
"ds_read_b64_tr_b16 v[158:159], v225 offset:58120\n" \
"v_mfma_f32_16x16x32_bf16 v[120:123], v[180:183], a[220:223], v[120:123]\n" \
"v_mfma_f32_16x16x32_bf16 v[28:31], v[184:187], a[192:195], v[28:31]\n" \
"buffer_load_dwordx4 v213, %[srdk], s48 offen offset:2048 lds\n" \
"v_readlane_b32 s61, %[tlo], 21\n" \
"v_mfma_f32_16x16x32_bf16 v[28:31], v[188:191], a[208:211], v[28:31]\n" \
"v_mfma_f32_16x16x32_bf16 v[60:63], v[184:187], a[196:199], v[60:63]\n" \
"v_mfma_f32_16x16x32_bf16 v[60:63], v[188:191], a[212:215], v[60:63]\n" \
"v_lshlrev_b32_e64 v224, 7, v231\n" \
"v_mfma_f32_16x16x32_bf16 v[92:95], v[184:187], a[200:203], v[92:95]\n" \
"buffer_load_dwordx4 v214, %[srdk], s48 offen offset:3072 lds\n" \
"v_mfma_f32_16x16x32_bf16 v[92:95], v[188:191], a[216:219], v[92:95]\n" \
"v_mfma_f32_16x16x32_bf16 v[124:127], v[184:187], a[204:207], v[124:127]\n" \
"v_mfma_f32_16x16x32_bf16 v[124:127], v[188:191], a[220:223], v[124:127]\n" \
"s_waitcnt vmcnt(24) lgkmcnt(0)\n" \
"v_mfma_f32_16x16x32_bf16 v[0:3], v[128:131], a[224:227], v[0:3]\n" \
"ds_read_b64_tr_b16 v[160:161], v225 offset:50176\n" \
"v_accvgpr_write_b32 a192, v230\n" \
"v_mfma_f32_16x16x32_bf16 v[0:3], v[132:135], a[240:243], v[0:3]\n" \
"ds_read_b64_tr_b16 v[162:163], v225 offset:50432\n" \
"v_mfma_f32_16x16x32_bf16 v[32:35], v[128:131], a[228:231], v[32:35]\n" \
"s_mov_b32 s48, 49152\n" \
"s_mov_b32 m0, s46\n" \
"s_nop 0\n" \
"buffer_load_dwordx4 %[vd0], %[srdv], s48 offen offset:2048 lds\n" \
"v_accvgpr_write_b32 a193, v230\n" \
"v_mfma_f32_16x16x32_bf16 v[32:35], v[132:135], a[244:247], v[32:35]\n" \
"ds_read_b64_tr_b16 v[164:165], v225 offset:58368\n" \
"v_mfma_f32_16x16x32_bf16 v[64:67], v[128:131], a[232:235], v[64:67]\n" \
"ds_read_b64_tr_b16 v[166:167], v225 offset:58624\n" \
"v_accvgpr_write_b32 a194, v230\n" \
"v_mfma_f32_16x16x32_bf16 v[64:67], v[132:135], a[248:251], v[64:67]\n" \
"v_mfma_f32_16x16x32_bf16 v[96:99], v[128:131], a[236:239], v[96:99]\n" \
"ds_read_b64_tr_b16 v[168:169], v225 offset:50184\n" \
"v_accvgpr_write_b32 a195, v230\n" \
"v_mfma_f32_16x16x32_bf16 v[96:99], v[132:135], a[252:255], v[96:99]\n" \
"ds_read_b64_tr_b16 v[170:171], v225 offset:50440\n" \
"v_mfma_f32_16x16x32_bf16 v[4:7], v[136:139], a[224:227], v[4:7]\n" \
"buffer_load_dwordx4 v215, %[srdv], s48 offen offset:3072 lds\n" \
"v_accvgpr_write_b32 a196, -1\n" \
"v_mfma_f32_16x16x32_bf16 v[4:7], v[140:143], a[240:243], v[4:7]\n" \
"ds_read_b64_tr_b16 v[172:173], v225 offset:58376\n" \
"v_mfma_f32_16x16x32_bf16 v[36:39], v[136:139], a[228:231], v[36:39]\n" \
"ds_read_b64_tr_b16 v[174:175], v225 offset:58632\n" \
"v_accvgpr_write_b32 a197, -1\n" \
"v_mfma_f32_16x16x32_bf16 v[36:39], v[140:143], a[244:247], v[36:39]\n" \
"v_mfma_f32_16x16x32_bf16 v[68:71], v[136:139], a[232:235], v[68:71]\n" \
"ds_read_b64_tr_b16 v[176:177], v225 offset:50688\n" \
"v_accvgpr_write_b32 a198, -1\n" \
"v_mfma_f32_16x16x32_bf16 v[68:71], v[140:143], a[248:251], v[68:71]\n" \
"ds_read_b64_tr_b16 v[178:179], v225 offset:50944\n" \
"v_mfma_f32_16x16x32_bf16 v[100:103], v[136:139], a[236:239], v[100:103]\n" \
"v_accvgpr_write_b32 a199, -1\n" \
"v_mfma_f32_16x16x32_bf16 v[100:103], v[140:143], a[252:255], v[100:103]\n" \
"ds_read_b64_tr_b16 v[180:181], v225 offset:58880\n" \
"v_mfma_f32_16x16x32_bf16 v[8:11], v[144:147], a[224:227], v[8:11]\n" \
"ds_read_b64_tr_b16 v[182:183], v225 offset:59136\n" \
"v_lshrrev_b32_e32 v231, 3, v224\n" \
"v_mfma_f32_16x16x32_bf16 v[8:11], v[148:151], a[240:243], v[8:11]\n" \
"v_mfma_f32_16x16x32_bf16 v[40:43], v[144:147], a[228:231], v[40:43]\n" \
"ds_read_b64_tr_b16 v[184:185], v225 offset:50696\n" \
"v_mov_b32_e64 v232, -1\n" \
"v_mfma_f32_16x16x32_bf16 v[40:43], v[148:151], a[244:247], v[40:43]\n" \
"ds_read_b64_tr_b16 v[186:187], v225 offset:50952\n" \
"v_mfma_f32_16x16x32_bf16 v[72:75], v[144:147], a[232:235], v[72:75]\n" \
"v_mov_b32_e64 v233, -1\n" \
"v_mfma_f32_16x16x32_bf16 v[72:75], v[148:151], a[248:251], v[72:75]\n" \
"ds_read_b64_tr_b16 v[188:189], v225 offset:58888\n" \
"v_mfma_f32_16x16x32_bf16 v[104:107], v[144:147], a[236:239], v[104:107]\n" \
"ds_read_b64_tr_b16 v[190:191], v225 offset:59144\n" \
"v_mov_b32_e64 v234, -1\n" \
"v_mfma_f32_16x16x32_bf16 v[104:107], v[148:151], a[252:255], v[104:107]\n" \
"v_mfma_f32_16x16x32_bf16 v[12:15], v[152:155], a[224:227], v[12:15]\n" \
"v_mov_b32_e64 v235, -1\n" \
"v_mfma_f32_16x16x32_bf16 v[12:15], v[156:159], a[240:243], v[12:15]\n" \
"v_mfma_f32_16x16x32_bf16 v[44:47], v[152:155], a[228:231], v[44:47]\n" \
"buffer_store_dwordx4 v[232:235], v231, %[srdz], s61 offen offset:0 sc1\n" \
"v_mfma_f32_16x16x32_bf16 v[44:47], v[156:159], a[244:247], v[44:47]\n" \
"v_mfma_f32_16x16x32_bf16 v[76:79], v[152:155], a[232:235], v[76:79]\n" \
"buffer_store_dwordx4 v[232:235], v231, %[srdz], s61 offen offset:1024 sc1\n" \
"v_mfma_f32_16x16x32_bf16 v[76:79], v[156:159], a[248:251], v[76:79]\n" \
"v_mfma_f32_16x16x32_bf16 v[108:111], v[152:155], a[236:239], v[108:111]\n" \
"v_mfma_f32_16x16x32_bf16 v[108:111], v[156:159], a[252:255], v[108:111]\n" \
"s_waitcnt vmcnt(22) lgkmcnt(0)\n" \
"s_barrier\n" \
"v_mfma_f32_16x16x32_bf16 v[16:19], v[160:163], a[224:227], v[16:19]\n" \
"ds_read_b128 a[64:67], %[kb0] offset:0\n" \
"v_mfma_f32_16x16x32_bf16 v[16:19], v[164:167], a[240:243], v[16:19]\n" \
"v_mfma_f32_16x16x32_bf16 v[20:23], v[168:171], a[224:227], v[20:23]\n" \
"v_cvt_pk_bf16_f32 v230, v0, v1\n" \
"v_cvt_pk_bf16_f32 v231, v2, v3\n" \
"v_cvt_pk_bf16_f32 v232, v4, v5\n" \
"ds_read_b128 a[68:71], v208 offset:0\n" \
"v_mfma_f32_16x16x32_bf16 v[20:23], v[172:175], a[240:243], v[20:23]\n" \
"v_cvt_pk_bf16_f32 v233, v6, v7\n" \
"v_accvgpr_write_b32 a0, v230\n" \
"v_accvgpr_write_b32 a1, v231\n" \
"v_mfma_f32_16x16x32_bf16 v[24:27], v[176:179], a[224:227], v[24:27]\n" \
"v_accvgpr_write_b32 a2, v232\n" \
"v_accvgpr_write_b32 a3, v233\n" \
"v_cvt_pk_bf16_f32 v234, v8, v9\n" \
"ds_read_b128 a[72:75], v209 offset:0\n" \
"v_mfma_f32_16x16x32_bf16 v[24:27], v[180:183], a[240:243], v[24:27]\n" \
"v_cvt_pk_bf16_f32 v235, v10, v11\n" \
"v_cvt_pk_bf16_f32 v236, v12, v13\n" \
"v_cvt_pk_bf16_f32 v237, v14, v15\n" \
"v_mfma_f32_16x16x32_bf16 v[28:31], v[184:187], a[224:227], v[28:31]\n" \
"v_accvgpr_write_b32 a4, v234\n" \
"v_accvgpr_write_b32 a5, v235\n" \
"v_accvgpr_write_b32 a6, v236\n" \
".if . - adaln_body != 8192\n" \
".error \"size model\"\n" \
".endif\n" \
"Lk1_%=:\n" \
"s_cbranch_execz Lk2_%=\n" \
"s_nop 0\n" \
"ds_read_b128 a[76:79], v210 offset:0\n" \
"v_mfma_f32_16x16x32_bf16 v[28:31], v[188:191], a[240:243], v[28:31]\n" \
"v_accvgpr_write_b32 a7, v237\n" \
"v_cvt_pk_bf16_f32 v230, v32, v33\n" \
"v_cvt_pk_bf16_f32 v231, v34, v35\n" \
"v_mfma_f32_16x16x32_bf16 v[48:51], v[160:163], a[228:231], v[48:51]\n" \
"v_cvt_pk_bf16_f32 v232, v36, v37\n" \
"v_cvt_pk_bf16_f32 v233, v38, v39\n" \
"v_accvgpr_write_b32 a16, v230\n" \
"ds_read_b128 a[92:95], v210 offset:4096\n" \
"v_mfma_f32_16x16x32_bf16 v[48:51], v[164:167], a[244:247], v[48:51]\n" \
"v_accvgpr_write_b32 a17, v231\n" \
"v_accvgpr_write_b32 a18, v232\n" \
"v_accvgpr_write_b32 a19, v233\n" \
"v_mfma_f32_16x16x32_bf16 v[52:55], v[168:171], a[228:231], v[52:55]\n" \
"v_cvt_pk_bf16_f32 v234, v40, v41\n" \
"v_cvt_pk_bf16_f32 v235, v42, v43\n" \
"v_cvt_pk_bf16_f32 v236, v44, v45\n" \
"ds_read_b128 a[88:91], v209 offset:4096\n" \
"v_mfma_f32_16x16x32_bf16 v[52:55], v[172:175], a[244:247], v[52:55]\n" \
"v_cvt_pk_bf16_f32 v237, v46, v47\n" \
"v_accvgpr_write_b32 a20, v234\n" \
"v_accvgpr_write_b32 a21, v235\n" \
"v_mfma_f32_16x16x32_bf16 v[56:59], v[176:179], a[228:231], v[56:59]\n" \
"v_accvgpr_write_b32 a22, v236\n" \
"v_accvgpr_write_b32 a23, v237\n" \
"v_cvt_pk_bf16_f32 v230, v16, v17\n" \
"ds_read_b128 a[84:87], v208 offset:4096\n" \
"v_mfma_f32_16x16x32_bf16 v[56:59], v[180:183], a[244:247], v[56:59]\n" \
"v_cvt_pk_bf16_f32 v231, v18, v19\n" \
"v_cvt_pk_bf16_f32 v232, v20, v21\n" \
"v_cvt_pk_bf16_f32 v233, v22, v23\n" \
"v_mfma_f32_16x16x32_bf16 v[60:63], v[184:187], a[228:231], v[60:63]\n" \
"v_accvgpr_write_b32 a8, v230\n" \
"v_accvgpr_write_b32 a9, v231\n" \
"v_accvgpr_write_b32 a10, v232\n" \
"ds_read_b128 a[80:83], %[kb0] offset:4096\n" \
"v_mfma_f32_16x16x32_bf16 v[60:63], v[188:191], a[244:247], v[60:63]\n" \
"v_accvgpr_write_b32 a11, v233\n" \
"v_cvt_pk_bf16_f32 v234, v24, v25\n" \
"v_cvt_pk_bf16_f32 v235, v26, v27\n" \
"v_mfma_f32_16x16x32_bf16 v[80:83], v[160:163], a[232:235], v[80:83]\n" \
"v_cvt_pk_bf16_f32 v236, v28, v29\n" \
"v_cvt_pk_bf16_f32 v237, v30, v31\n" \
"v_accvgpr_write_b32 a12, v234\n" \
"v_mfma_f32_16x16x32_bf16 v[80:83], v[164:167], a[248:251], v[80:83]\n" \
"v_accvgpr_write_b32 a13, v235\n" \
"v_accvgpr_write_b32 a14, v236\n" \
"v_accvgpr_write_b32 a15, v237\n" \
"v_mfma_f32_16x16x32_bf16 v[84:87], v[168:171], a[232:235], v[84:87]\n" \
"v_cvt_pk_bf16_f32 v230, v64, v65\n" \
"v_cvt_pk_bf16_f32 v231, v66, v67\n" \
"v_cvt_pk_bf16_f32 v232, v68, v69\n" \
"v_mfma_f32_16x16x32_bf16 v[84:87], v[172:175], a[248:251], v[84:87]\n" \
"v_cvt_pk_bf16_f32 v233, v70, v71\n" \
"v_accvgpr_write_b32 a32, v230\n" \
"v_accvgpr_write_b32 a33, v231\n" \
"v_mfma_f32_16x16x32_bf16 v[88:91], v[176:179], a[232:235], v[88:91]\n" \
"v_accvgpr_write_b32 a34, v232\n" \
"v_accvgpr_write_b32 a35, v233\n" \
"v_cvt_pk_bf16_f32 v234, v72, v73\n" \
"v_mfma_f32_16x16x32_bf16 v[88:91], v[180:183], a[248:251], v[88:91]\n" \
"v_cvt_pk_bf16_f32 v235, v74, v75\n" \
"v_cvt_pk_bf16_f32 v236, v76, v77\n" \
"v_cvt_pk_bf16_f32 v237, v78, v79\n" \
"v_mfma_f32_16x16x32_bf16 v[92:95], v[184:187], a[232:235], v[92:95]\n" \
"v_accvgpr_write_b32 a36, v234\n" \
"v_accvgpr_write_b32 a37, v235\n" \
"v_accvgpr_write_b32 a38, v236\n" \
"v_mfma_f32_16x16x32_bf16 v[92:95], v[188:191], a[248:251], v[92:95]\n" \
"v_accvgpr_write_b32 a39, v237\n" \
"v_cvt_pk_bf16_f32 v230, v48, v49\n" \
"v_cvt_pk_bf16_f32 v231, v50, v51\n" \
"v_mfma_f32_16x16x32_bf16 v[112:115], v[160:163], a[236:239], v[112:115]\n" \
"v_cvt_pk_bf16_f32 v232, v52, v53\n" \
"v_cvt_pk_bf16_f32 v233, v54, v55\n" \
"v_accvgpr_write_b32 a24, v230\n" \
"v_mfma_f32_16x16x32_bf16 v[112:115], v[164:167], a[252:255], v[112:115]\n" \
"v_accvgpr_write_b32 a25, v231\n" \
"v_accvgpr_write_b32 a26, v232\n" \
"v_accvgpr_write_b32 a27, v233\n" \
"v_mfma_f32_16x16x32_bf16 v[116:119], v[168:171], a[236:239], v[116:119]\n" \
"v_cvt_pk_bf16_f32 v234, v56, v57\n" \
"v_cvt_pk_bf16_f32 v235, v58, v59\n" \
"v_cvt_pk_bf16_f32 v236, v60, v61\n" \
"v_mfma_f32_16x16x32_bf16 v[116:119], v[172:175], a[252:255], v[116:119]\n" \
"v_cvt_pk_bf16_f32 v237, v62, v63\n" \
"v_accvgpr_write_b32 a28, v234\n" \
"v_accvgpr_write_b32 a29, v235\n" \
"v_mfma_f32_16x16x32_bf16 v[120:123], v[176:179], a[236:239], v[120:123]\n" \
"v_accvgpr_write_b32 a30, v236\n" \
"v_accvgpr_write_b32 a31, v237\n" \
"v_cvt_pk_bf16_f32 v230, v96, v97\n" \
"v_mfma_f32_16x16x32_bf16 v[120:123], v[180:183], a[252:255], v[120:123]\n" \
"v_cvt_pk_bf16_f32 v231, v98, v99\n" \
"v_cvt_pk_bf16_f32 v232, v100, v101\n" \
"v_cvt_pk_bf16_f32 v233, v102, v103\n" \
"v_mfma_f32_16x16x32_bf16 v[124:127], v[184:187], a[236:239], v[124:127]\n" \
"v_accvgpr_write_b32 a48, v230\n" \
"v_accvgpr_write_b32 a49, v231\n" \
"v_accvgpr_write_b32 a50, v232\n" \
"v_mfma_f32_16x16x32_bf16 v[124:127], v[188:191], a[252:255], v[124:127]\n" \
"v_accvgpr_write_b32 a51, v233\n" \
"s_waitcnt vmcnt(63) lgkmcnt(0)\n" \
"s_nop 0\n" \
"s_mov_b32 s48, 65536\n" \
"s_mov_b32 s60, 0\n" \
"s_waitcnt lgkmcnt(0)\n" \
"v_mfma_f32_16x16x32_bf16 v[128:131], a[64:67], a[0:3], 0\n" \
"ds_read_b128 a[96:99], %[kb0] offset:8192\n" \
"v_cvt_pk_bf16_f32 v234, v80, v81\n" \
"v_cvt_pk_bf16_f32 v235, v82, v83\n" \
"v_mfma_f32_16x16x32_bf16 v[128:131], a[68:71], a[4:7], v[128:131]\n" \
"ds_read_b128 a[100:103], v208 offset:8192\n" \
"v_cvt_pk_bf16_f32 v236, v84, v85\n" \
"v_cvt_pk_bf16_f32 v237, v86, v87\n" \
"v_mfma_f32_16x16x32_bf16 v[128:131], a[72:75], a[8:11], v[128:131]\n" \
"ds_read_b128 a[104:107], v209 offset:8192\n" \
"v_accvgpr_write_b32 a40, v234\n" \
"v_accvgpr_write_b32 a41, v235\n" \
"v_mfma_f32_16x16x32_bf16 v[128:131], a[76:79], a[12:15], v[128:131]\n" \
"ds_read_b128 a[108:111], v210 offset:8192\n" \
"v_accvgpr_write_b32 a42, v236\n" \
"v_accvgpr_write_b32 a43, v237\n" \
"v_mfma_f32_16x16x32_bf16 v[132:135], a[92:95], a[12:15], 0\n" \
"ds_read_b128 a[124:127], v210 offset:12288\n" \
"v_cvt_pk_bf16_f32 v230, v88, v89\n" \
"v_cvt_pk_bf16_f32 v231, v90, v91\n" \
"v_mfma_f32_16x16x32_bf16 v[132:135], a[88:91], a[8:11], v[132:135]\n" \
"ds_read_b128 a[120:123], v209 offset:12288\n" \
"v_cvt_pk_bf16_f32 v232, v92, v93\n" \
"v_cvt_pk_bf16_f32 v233, v94, v95\n" \
"v_mfma_f32_16x16x32_bf16 v[132:135], a[84:87], a[4:7], v[132:135]\n" \
"ds_read_b128 a[116:119], v208 offset:12288\n" \
"v_accvgpr_write_b32 a44, v230\n" \
"v_accvgpr_write_b32 a45, v231\n" \
"v_mfma_f32_16x16x32_bf16 v[132:135], a[80:83], a[0:3], v[132:135]\n" \
"ds_read_b128 a[112:115], %[kb0] offset:12288\n" \
"v_accvgpr_write_b32 a46, v232\n" \
"v_accvgpr_write_b32 a47, v233\n" \
"v_mfma_f32_16x16x32_bf16 v[136:139], a[64:67], a[16:19], 0\n" \
"ds_read_b64_tr_b16 a[128:129], %[vvb] offset:0\n" \
"v_cvt_pk_bf16_f32 v234, v104, v105\n" \
"v_cvt_pk_bf16_f32 v235, v106, v107\n" \
"v_mfma_f32_16x16x32_bf16 v[136:139], a[68:71], a[20:23], v[136:139]\n" \
"ds_read_b64_tr_b16 a[130:131], %[vvb] offset:4096\n" \
"v_cvt_pk_bf16_f32 v236, v108, v109\n" \
"v_cvt_pk_bf16_f32 v237, v110, v111\n" \
"v_mfma_f32_16x16x32_bf16 v[136:139], a[72:75], a[24:27], v[136:139]\n" \
"ds_read_b64_tr_b16 a[132:133], v211 offset:0\n" \
"v_accvgpr_write_b32 a52, v234\n" \
"v_accvgpr_write_b32 a53, v235\n" \
"v_mfma_f32_16x16x32_bf16 v[136:139], a[76:79], a[28:31], v[136:139]\n" \
"ds_read_b64_tr_b16 a[134:135], v211 offset:4096\n" \
"v_accvgpr_write_b32 a54, v236\n" \
"v_accvgpr_write_b32 a55, v237\n" \
"v_mfma_f32_16x16x32_bf16 v[140:143], a[92:95], a[28:31], 0\n" \
"ds_read_b64_tr_b16 a[136:137], %[vvb] offset:512\n" \
"v_cvt_pk_bf16_f32 v230, v112, v113\n" \
"v_cvt_pk_bf16_f32 v231, v114, v115\n" \
"v_mfma_f32_16x16x32_bf16 v[140:143], a[88:91], a[24:27], v[140:143]\n" \
"ds_read_b64_tr_b16 a[138:139], %[vvb] offset:4608\n" \
"v_cvt_pk_bf16_f32 v232, v116, v117\n" \
"v_cvt_pk_bf16_f32 v233, v118, v119\n" \
"v_mfma_f32_16x16x32_bf16 v[140:143], a[84:87], a[20:23], v[140:143]\n" \
"ds_read_b64_tr_b16 a[140:141], v211 offset:512\n" \
"v_accvgpr_write_b32 a56, v230\n" \
"v_accvgpr_write_b32 a57, v231\n" \
"v_mfma_f32_16x16x32_bf16 v[140:143], a[80:83], a[16:19], v[140:143]\n" \
"ds_read_b64_tr_b16 a[142:143], v211 offset:4608\n" \
"v_accvgpr_write_b32 a58, v232\n" \
"v_accvgpr_write_b32 a59, v233\n" \
"v_mfma_f32_16x16x32_bf16 v[144:147], a[64:67], a[32:35], 0\n" \
"ds_read_b64_tr_b16 a[144:145], %[vvb] offset:1024\n" \
"v_cvt_pk_bf16_f32 v234, v120, v121\n" \
"v_cvt_pk_bf16_f32 v235, v122, v123\n" \
"v_mfma_f32_16x16x32_bf16 v[144:147], a[68:71], a[36:39], v[144:147]\n" \
"ds_read_b64_tr_b16 a[146:147], %[vvb] offset:5120\n" \
"v_cvt_pk_bf16_f32 v236, v124, v125\n" \
"v_cvt_pk_bf16_f32 v237, v126, v127\n" \
"v_mfma_f32_16x16x32_bf16 v[144:147], a[72:75], a[40:43], v[144:147]\n" \
"ds_read_b64_tr_b16 a[148:149], v211 offset:1024\n" \
"v_accvgpr_write_b32 a60, v234\n" \
"v_accvgpr_write_b32 a61, v235\n" \
"v_mfma_f32_16x16x32_bf16 v[144:147], a[76:79], a[44:47], v[144:147]\n" \
"ds_read_b64_tr_b16 a[150:151], v211 offset:5120\n" \
"v_accvgpr_write_b32 a62, v236\n" \
"v_accvgpr_write_b32 a63, v237\n" \
"v_mfma_f32_16x16x32_bf16 v[148:151], a[92:95], a[44:47], 0\n" \
"ds_read_b64_tr_b16 a[152:153], %[vvb] offset:1536\n" \
"v_max3_f32 v192, v128, v129, v130\n" \
"v_max3_f32 v193, v131, v132, v133\n" \
"v_mfma_f32_16x16x32_bf16 v[148:151], a[88:91], a[40:43], v[148:151]\n" \
"ds_read_b64_tr_b16 a[154:155], %[vvb] offset:5632\n" \
"v_max3_f32 v192, v192, v134, v135\n" \
"v_max_f32_e64 v225, v192, v193\n" \
"v_mfma_f32_16x16x32_bf16 v[148:151], a[84:87], a[36:39], v[148:151]\n" \
"ds_read_b64_tr_b16 a[156:157], v211 offset:1536\n" \
"v_max3_f32 v192, v136, v137, v138\n" \
"v_max3_f32 v193, v139, v140, v141\n" \
"v_mfma_f32_16x16x32_bf16 v[148:151], a[80:83], a[32:35], v[148:151]\n" \
"ds_read_b64_tr_b16 a[158:159], v211 offset:5632\n" \
"v_max3_f32 v192, v192, v142, v143\n" \
"v_max_f32_e64 v226, v192, v193\n" \
"v_mfma_f32_16x16x32_bf16 v[152:155], a[64:67], a[48:51], 0\n" \
"ds_read_b128 a[200:203], v228 offset:0\n" \
"s_mov_b32 s44, %[m0t]\n" \
"s_mov_b32 s45, 0\n" \
"v_mfma_f32_16x16x32_bf16 v[152:155], a[68:71], a[52:55], v[152:155]\n" \
"ds_read_b128 a[204:207], v228 offset:64\n" \
"v_mfma_f32_16x16x32_bf16 v[152:155], a[72:75], a[56:59], v[152:155]\n" \
"ds_read_b128 a[208:211], v228 offset:128\n" \
"v_mfma_f32_16x16x32_bf16 v[152:155], a[76:79], a[60:63], v[152:155]\n" \
"ds_read_b128 a[212:215], v228 offset:192\n" \
"v_mfma_f32_16x16x32_bf16 v[156:159], a[92:95], a[60:63], 0\n" \
"ds_read_b128 a[216:219], v228 offset:256\n" \
"v_mfma_f32_16x16x32_bf16 v[156:159], a[88:91], a[56:59], v[156:159]\n" \
"ds_read_b128 a[220:223], v228 offset:320\n" \
"v_mfma_f32_16x16x32_bf16 v[156:159], a[84:87], a[52:55], v[156:159]\n" \
"ds_read_b128 a[224:227], v228 offset:384\n" \
"v_max3_f32 v192, v144, v145, v146\n" \
"v_max3_f32 v193, v147, v148, v149\n" \
"v_mfma_f32_16x16x32_bf16 v[156:159], a[80:83], a[48:51], v[156:159]\n" \
"ds_read_b128 a[228:231], v228 offset:448\n" \
"v_max3_f32 v192, v192, v150, v151\n" \
"v_max_f32_e64 v227, v192, v193\n" \
"s_nop 3\n" \
"s_nop 0\n" \
"v_max3_f32 v230, v152, v153, v154\n" \
"v_max3_f32 v231, v155, v156, v157\n" \
"v_max3_f32 v230, v230, v158, v159\n" \
"v_max_f32_e32 v228, v230, v231\n" \
"v_mov_b32_e32 v232, v225\n" \
"v_mov_b32_e32 v233, v226\n" \
"v_mov_b32_e32 v234, v227\n" \
"v_mov_b32_e32 v235, v228\n" \
"s_nop 0\n" \
"v_permlane16_swap_b32_e32 v225, v232\n" \
"v_permlane16_swap_b32_e32 v226, v233\n" \
"v_permlane16_swap_b32_e32 v227, v234\n" \
"v_permlane16_swap_b32_e32 v228, v235\n" \
"v_max_f32_e32 v225, v225, v232\n" \
"v_max_f32_e32 v226, v226, v233\n" \
"v_max_f32_e32 v227, v227, v234\n" \
"v_max_f32_e32 v228, v228, v235\n" \
"v_mov_b32_e32 v232, v225\n" \
"v_mov_b32_e32 v233, v226\n" \
"v_mov_b32_e32 v234, v227\n" \
"v_mov_b32_e32 v235, v228\n" \
"s_nop 0\n" \
"v_permlane32_swap_b32_e32 v225, v232\n" \
"v_permlane32_swap_b32_e32 v226, v233\n" \
"v_permlane32_swap_b32_e32 v227, v234\n" \
"v_permlane32_swap_b32_e32 v228, v235\n" \
"v_max_f32_e32 v225, v225, v232\n" \
"v_max_f32_e32 v226, v226, v233\n" \
"v_max_f32_e32 v227, v227, v234\n" \
"v_max_f32_e64 v228, v228, v235\n" \
"Lhavem_%=:\n" \
"s_nop 3\n" \
"v_mov_b64_e32 v[176:177], 0\n" \
"v_mov_b64_e32 v[178:179], 0\n" \
"v_mov_b64_e32 v[180:181], 0\n" \
"v_mov_b64_e32 v[182:183], 0\n" \
"v_sub_f32_e32 v160, 0, v225\n" \
"v_sub_f32_e32 v161, 0, v225\n" \
"v_sub_f32_e32 v162, 0, v225\n" \
"v_sub_f32_e32 v163, 0, v225\n" \
"v_sub_f32_e32 v164, 0, v226\n" \
"v_sub_f32_e32 v165, 0, v226\n" \
"v_sub_f32_e32 v166, 0, v226\n" \
"v_sub_f32_e32 v167, 0, v226\n" \
"v_sub_f32_e32 v168, 0, v227\n" \
"v_sub_f32_e32 v169, 0, v227\n" \
"v_sub_f32_e32 v170, 0, v227\n" \
"v_sub_f32_e32 v171, 0, v227\n" \
"v_sub_f32_e32 v172, 0, v228\n" \
"v_sub_f32_e32 v173, 0, v228\n" \
"v_sub_f32_e32 v174, 0, v228\n" \
"v_sub_f32_e32 v175, 0, v228\n" \
"v_sub_f32_e32 v128, v128, v225\n" \
"v_sub_f32_e32 v129, v129, v225\n" \
"v_sub_f32_e32 v130, v130, v225\n" \
"v_sub_f32_e32 v131, v131, v225\n" \
"v_sub_f32_e32 v132, v132, v225\n" \
"v_sub_f32_e32 v133, v133, v225\n" \
"v_sub_f32_e32 v134, v134, v225\n" \
"v_sub_f32_e32 v135, v135, v225\n" \
"v_sub_f32_e32 v136, v136, v226\n" \
"v_sub_f32_e32 v137, v137, v226\n" \
"v_sub_f32_e32 v138, v138, v226\n" \
"v_sub_f32_e32 v139, v139, v226\n" \
"v_sub_f32_e32 v140, v140, v226\n" \
"v_sub_f32_e32 v141, v141, v226\n" \
"v_sub_f32_e32 v142, v142, v226\n" \
"v_sub_f32_e32 v143, v143, v226\n" \
"v_sub_f32_e32 v144, v144, v227\n" \
"v_sub_f32_e32 v145, v145, v227\n" \
"v_sub_f32_e32 v146, v146, v227\n" \
"v_sub_f32_e32 v147, v147, v227\n" \
"v_sub_f32_e32 v148, v148, v227\n" \
"v_sub_f32_e32 v149, v149, v227\n" \
"v_sub_f32_e32 v150, v150, v227\n" \
"v_sub_f32_e32 v151, v151, v227\n" \
"v_sub_f32_e32 v152, v152, v228\n" \
"v_sub_f32_e32 v153, v153, v228\n" \
"v_sub_f32_e32 v154, v154, v228\n" \
"v_sub_f32_e32 v155, v155, v228\n" \
"v_sub_f32_e32 v156, v156, v228\n" \
"v_sub_f32_e32 v157, v157, v228\n" \
"v_sub_f32_e32 v158, v158, v228\n" \
"v_sub_f32_e32 v159, v159, v228\n" \
"v_exp_f32_e32 v128, v128\n" \
"v_exp_f32_e32 v129, v129\n" \
"v_exp_f32_e32 v130, v130\n" \
"v_exp_f32_e32 v131, v131\n" \
"v_exp_f32_e32 v132, v132\n" \
"v_exp_f32_e32 v133, v133\n" \
"v_exp_f32_e32 v134, v134\n" \
"v_exp_f32_e32 v135, v135\n" \
"v_exp_f32_e32 v136, v136\n" \
"v_exp_f32_e32 v137, v137\n" \
"v_exp_f32_e32 v138, v138\n" \
"v_exp_f32_e32 v139, v139\n" \
"v_exp_f32_e32 v140, v140\n" \
"v_exp_f32_e32 v141, v141\n" \
"v_exp_f32_e32 v142, v142\n" \
"v_exp_f32_e32 v143, v143\n" \
"v_exp_f32_e32 v144, v144\n" \
"v_exp_f32_e32 v145, v145\n" \
"v_exp_f32_e32 v146, v146\n" \
"v_exp_f32_e32 v147, v147\n" \
"v_exp_f32_e32 v148, v148\n" \
"v_exp_f32_e32 v149, v149\n" \
"v_exp_f32_e32 v150, v150\n" \
"v_exp_f32_e32 v151, v151\n" \
"v_exp_f32_e32 v152, v152\n" \
"v_exp_f32_e32 v153, v153\n" \
"v_exp_f32_e32 v154, v154\n" \
"v_exp_f32_e64 v155, v155\n" \
"v_bfe_i32 v228, v224, 7, 1\n" \
"v_and_b32_e32 v228, 0xaaaa, v228\n" \
"v_or_b32_e32 v228, 0x4444, v228\n" \
"v_cvt_pk_bf16_f32 v184, v128, v129\n" \
"v_cvt_pk_bf16_f32 v185, v130, v131\n" \
"v_cvt_pk_bf16_f32 v186, v132, v133\n" \
"v_cvt_pk_bf16_f32 v187, v134, v135\n" \
"v_cvt_pk_bf16_f32 v188, v136, v137\n" \
"v_cvt_pk_bf16_f32 v189, v138, v139\n" \
"v_cvt_pk_bf16_f32 v190, v140, v141\n" \
"v_cvt_pk_bf16_f32 v191, v142, v143\n" \
"v_cvt_pk_bf16_f32 v216, v144, v145\n" \
"v_cvt_pk_bf16_f32 v217, v146, v147\n" \
"v_cvt_pk_bf16_f32 v218, v148, v149\n" \
"v_cvt_pk_bf16_f32 v219, v150, v151\n" \
"v_lshrrev_b32_e64 v235, 3, v224\n" \
"s_waitcnt vmcnt(2) lgkmcnt(0)\n" \
"s_barrier\n" \
"v_mfma_f32_16x16x32_bf16 v[128:131], a[96:99], a[0:3], v[160:163]\n" \
"v_exp_f32_e64 v156, v156\n" \
"v_mfma_f32_16x16x32_bf16 v[128:131], a[100:103], a[4:7], v[128:131]\n" \
"s_add_u32 m0, %[m0b], 114688\n" \
"v_mfma_f32_16x16x32_bf16 v[128:131], a[104:107], a[8:11], v[128:131]\n" \
"v_exp_f32_e64 v157, v157\n" \
"v_mfma_f32_16x16x32_bf16 v[128:131], a[108:111], a[12:15], v[128:131]\n" \
"v_cvt_pk_bf16_f32 v220, v152, v153\n" \
"v_cvt_pk_bf16_f32 v221, v154, v155\n" \
"v_mfma_f32_16x16x32_bf16 v[132:135], a[124:127], a[12:15], v[160:163]\n" \
"v_exp_f32_e64 v158, v158\n" \
"v_mfma_f32_16x16x32_bf16 v[132:135], a[120:123], a[8:11], v[132:135]\n" \
"v_mfma_f32_16x16x32_bf16 v[132:135], a[116:119], a[4:7], v[132:135]\n" \
"buffer_load_dwordx4 %[vd0], %[srdv], s48 offen offset:0 lds\n" \
"v_mfma_f32_16x16x32_bf16 v[132:135], a[112:115], a[0:3], v[132:135]\n" \
"v_exp_f32_e64 v159, v159\n" \
"v_mfma_f32_16x16x32_bf16 v[0:3], a[128:131], v[184:187], 0\n" \
"ds_read_b128 a[64:67], %[kb0] offset:16384\n" \
"v_mfma_f32_16x16x32_bf16 v[32:35], a[128:131], v[188:191], 0\n" \
"v_exp_f32_e64 v128, v128\n" \
"v_mfma_f32_16x16x32_bf16 v[4:7], a[132:135], v[184:187], 0\n" \
"v_cvt_pk_bf16_f32 v222, v156, v157\n" \
"v_cvt_pk_bf16_f32 v223, v158, v159\n" \
"v_mfma_f32_16x16x32_bf16 v[36:39], a[132:135], v[188:191], 0\n" \
"v_exp_f32_e64 v129, v129\n" \
"v_mfma_f32_16x16x32_bf16 v[8:11], a[136:139], v[184:187], 0\n" \
"ds_read_b128 a[68:71], v208 offset:16384\n" \
"v_mfma_f32_16x16x32_bf16 v[40:43], a[136:139], v[188:191], 0\n" \
"v_exp_f32_e64 v130, v130\n" \
"v_mfma_f32_16x16x32_bf16 v[12:15], a[140:143], v[184:187], 0\n" \
"ds_read_b128 a[72:75], v209 offset:16384\n" \
"v_mfma_f32_16x16x32_bf16 v[44:47], a[140:143], v[188:191], 0\n" \
"v_exp_f32_e64 v131, v131\n" \
"v_smfmac_f32_16x16x64_bf16 v[176:179], a[192:195], v[184:191], v228\n" \
"v_mfma_f32_16x16x32_bf16 v[136:139], a[96:99], a[16:19], v[164:167]\n" \
"v_exp_f32_e64 v132, v132\n" \
"v_mfma_f32_16x16x32_bf16 v[136:139], a[100:103], a[20:23], v[136:139]\n" \
"ds_read_b128 a[76:79], v210 offset:16384\n" \
"v_mfma_f32_16x16x32_bf16 v[136:139], a[104:107], a[24:27], v[136:139]\n" \
"v_exp_f32_e64 v133, v133\n" \
"v_mfma_f32_16x16x32_bf16 v[136:139], a[108:111], a[28:31], v[136:139]\n" \
"v_cvt_pk_bf16_f32 v192, v128, v129\n" \
"v_cvt_pk_bf16_f32 v193, v130, v131\n" \
"v_mfma_f32_16x16x32_bf16 v[140:143], a[124:127], a[28:31], v[164:167]\n" \
"v_exp_f32_e64 v134, v134\n" \
"v_mfma_f32_16x16x32_bf16 v[140:143], a[120:123], a[24:27], v[140:143]\n" \
"ds_read_b128 a[92:95], v210 offset:20480\n" \
"v_mfma_f32_16x16x32_bf16 v[140:143], a[116:119], a[20:23], v[140:143]\n" \
"buffer_load_dwordx4 v215, %[srdv], s48 offen offset:1024 lds\n" \
"v_mfma_f32_16x16x32_bf16 v[140:143], a[112:115], a[16:19], v[140:143]\n" \
"v_exp_f32_e64 v135, v135\n" \
"v_mfma_f32_16x16x32_bf16 v[16:19], a[144:147], v[184:187], 0\n" \
"ds_read_b128 a[88:91], v209 offset:20480\n" \
"v_mfma_f32_16x16x32_bf16 v[48:51], a[144:147], v[188:191], 0\n" \
"v_exp_f32_e64 v136, v136\n" \
"v_mfma_f32_16x16x32_bf16 v[20:23], a[148:151], v[184:187], 0\n" \
"v_cvt_pk_bf16_f32 v194, v132, v133\n" \
"v_cvt_pk_bf16_f32 v195, v134, v135\n" \
"v_mfma_f32_16x16x32_bf16 v[52:55], a[148:151], v[188:191], 0\n" \
"v_exp_f32_e64 v137, v137\n" \
"v_mfma_f32_16x16x32_bf16 v[24:27], a[152:155], v[184:187], 0\n" \
"v_exp_f32_e64 v138, v138\n" \
"v_mfma_f32_16x16x32_bf16 v[56:59], a[152:155], v[188:191], 0\n" \
"ds_read_b128 a[84:87], v208 offset:20480\n" \
"v_mfma_f32_16x16x32_bf16 v[28:31], a[156:159], v[184:187], 0\n" \
"v_exp_f32_e64 v139, v139\n" \
"v_mfma_f32_16x16x32_bf16 v[60:63], a[156:159], v[188:191], 0\n" \
"v_mfma_f32_16x16x32_bf16 v[144:147], a[96:99], a[32:35], v[168:171]\n" \
"v_exp_f32_e64 v140, v140\n" \
"v_mfma_f32_16x16x32_bf16 v[144:147], a[100:103], a[36:39], v[144:147]\n" \
"ds_read_b128 a[80:83], %[kb0] offset:20480\n" \
"v_mfma_f32_16x16x32_bf16 v[144:147], a[104:107], a[40:43], v[144:147]\n" \
"v_exp_f32_e64 v141, v141\n" \
"v_mfma_f32_16x16x32_bf16 v[144:147], a[108:111], a[44:47], v[144:147]\n" \
"v_cvt_pk_bf16_f32 v196, v136, v137\n" \
"v_cvt_pk_bf16_f32 v197, v138, v139\n" \
"v_mfma_f32_16x16x32_bf16 v[148:151], a[124:127], a[44:47], v[168:171]\n" \
"v_exp_f32_e64 v142, v142\n" \
"v_mfma_f32_16x16x32_bf16 v[148:151], a[120:123], a[40:43], v[148:151]\n" \
"v_mfma_f32_16x16x32_bf16 v[148:151], a[116:119], a[36:39], v[148:151]\n" \
"buffer_load_dwordx4 %[vd0], %[srdv], s48 offen offset:2048 lds\n" \
"v_mfma_f32_16x16x32_bf16 v[148:151], a[112:115], a[32:35], v[148:151]\n" \
"v_exp_f32_e64 v143, v143\n" \
"v_mfma_f32_16x16x32_bf16 v[64:67], a[128:131], v[216:219], 0\n" \
"ds_read_b64_tr_b16 a[160:161], %[vvb] offset:8192\n" \
"v_mfma_f32_16x16x32_bf16 v[96:99], a[128:131], v[220:223], 0\n" \
"v_exp_f32_e64 v144, v144\n" \
"v_mfma_f32_16x16x32_bf16 v[68:71], a[132:135], v[216:219], 0\n" \
"v_cvt_pk_bf16_f32 v198, v140, v141\n" \
"v_cvt_pk_bf16_f32 v199, v142, v143\n" \
"v_mfma_f32_16x16x32_bf16 v[100:103], a[132:135], v[220:223], 0\n" \
"v_exp_f32_e64 v145, v145\n" \
"v_mfma_f32_16x16x32_bf16 v[72:75], a[136:139], v[216:219], 0\n" \
"ds_read_b64_tr_b16 a[162:163], %[vvb] offset:12288\n" \
"ds_read_b64_tr_b16 a[164:165], v211 offset:8192\n" \
"v_mfma_f32_16x16x32_bf16 v[104:107], a[136:139], v[220:223], 0\n" \
"v_exp_f32_e64 v146, v146\n" \
"v_mfma_f32_16x16x32_bf16 v[76:79], a[140:143], v[216:219], 0\n" \
"ds_read_b64_tr_b16 a[166:167], v211 offset:12288\n" \
"v_mfma_f32_16x16x32_bf16 v[108:111], a[140:143], v[220:223], 0\n" \
"v_exp_f32_e64 v147, v147\n" \
"v_smfmac_f32_16x16x64_bf16 v[180:183], a[192:195], v[216:223], v228\n" \
"ds_read_b64_tr_b16 a[168:169], %[vvb] offset:8704\n" \
"v_mfma_f32_16x16x32_bf16 v[152:155], a[96:99], a[48:51], v[172:175]\n" \
"v_exp_f32_e64 v148, v148\n" \
"v_mfma_f32_16x16x32_bf16 v[152:155], a[100:103], a[52:55], v[152:155]\n" \
"ds_read_b64_tr_b16 a[170:171], %[vvb] offset:12800\n" \
"v_mfma_f32_16x16x32_bf16 v[152:155], a[104:107], a[56:59], v[152:155]\n" \
"v_exp_f32_e64 v149, v149\n" \
"v_mfma_f32_16x16x32_bf16 v[152:155], a[108:111], a[60:63], v[152:155]\n" \
"v_cvt_pk_bf16_f32 v200, v144, v145\n" \
"v_cvt_pk_bf16_f32 v201, v146, v147\n" \
"v_mfma_f32_16x16x32_bf16 v[156:159], a[124:127], a[60:63], v[172:175]\n" \
"v_exp_f32_e64 v150, v150\n" \
"v_mfma_f32_16x16x32_bf16 v[156:159], a[120:123], a[56:59], v[156:159]\n" \
"ds_read_b64_tr_b16 a[172:173], v211 offset:8704\n" \
"v_mfma_f32_16x16x32_bf16 v[156:159], a[116:119], a[52:55], v[156:159]\n" \
"buffer_load_dwordx4 v215, %[srdv], s48 offen offset:3072 lds\n" \
"v_mfma_f32_16x16x32_bf16 v[156:159], a[112:115], a[48:51], v[156:159]\n" \
"v_exp_f32_e64 v151, v151\n" \
"v_mfma_f32_16x16x32_bf16 v[80:83], a[144:147], v[216:219], 0\n" \
"ds_read_b64_tr_b16 a[174:175], v211 offset:12800\n" \
"ds_read_b64_tr_b16 a[176:177], %[vvb] offset:9216\n" \
"v_mfma_f32_16x16x32_bf16 v[112:115], a[144:147], v[220:223], 0\n" \
"v_exp_f32_e64 v152, v152\n" \
"v_mfma_f32_16x16x32_bf16 v[84:87], a[148:151], v[216:219], 0\n" \
"v_cvt_pk_bf16_f32 v202, v148, v149\n" \
"v_cvt_pk_bf16_f32 v203, v150, v151\n" \
"v_mfma_f32_16x16x32_bf16 v[116:119], a[148:151], v[220:223], 0\n" \
"v_exp_f32_e64 v153, v153\n" \
"v_mfma_f32_16x16x32_bf16 v[88:91], a[152:155], v[216:219], 0\n" \
"v_exp_f32_e64 v154, v154\n" \
"v_mfma_f32_16x16x32_bf16 v[120:123], a[152:155], v[220:223], 0\n" \
"ds_read_b64_tr_b16 a[178:179], %[vvb] offset:13312\n" \
"v_mfma_f32_16x16x32_bf16 v[92:95], a[156:159], v[216:219], 0\n" \
"v_exp_f32_e64 v155, v155\n" \
"v_mov_b64_e32 v[216:217], 0\n" \
"v_mov_b64_e32 v[218:219], 0\n" \
"v_mfma_f32_16x16x32_bf16 v[124:127], a[156:159], v[220:223], 0\n" \
"ds_read_b64_tr_b16 a[180:181], v211 offset:9216\n" \
"s_waitcnt lgkmcnt(13)\n" \
"s_nop 0\n" \
"LmA_%=:\n" \
"v_mfma_f32_16x16x32_bf16 v[128:131], a[64:67], a[0:3], v[160:163]\n" \
"v_exp_f32_e64 v156, v156\n" \
"v_mfma_f32_16x16x32_bf16 v[128:131], a[68:71], a[4:7], v[128:131]\n" \
"ds_read_b64_tr_b16 a[182:183], v211 offset:13312\n" \
"s_add_u32 m0, %[m0b], 0\n" \
"v_mfma_f32_16x16x32_bf16 v[128:131], a[72:75], a[8:11], v[128:131]\n" \
"v_exp_f32_e32 v157, v157\n" \
"v_mfma_f32_16x16x32_bf16 v[128:131], a[76:79], a[12:15], v[128:131]\n" \
"v_cvt_pk_bf16_f32 v204, v152, v153\n" \
"v_cvt_pk_bf16_f32 v205, v154, v155\n" \
"v_mfma_f32_16x16x32_bf16 v[132:135], a[92:95], a[12:15], v[160:163]\n" \
"v_exp_f32_e64 v158, v158\n" \
"v_mfma_f32_16x16x32_bf16 v[132:135], a[88:91], a[8:11], v[132:135]\n" \
"ds_read_b64_tr_b16 a[184:185], %[vvb] offset:9728\n" \
"s_waitcnt lgkmcnt(5)\n" \
"v_mfma_f32_16x16x32_bf16 v[132:135], a[84:87], a[4:7], v[132:135]\n" \
"buffer_load_dwordx4 %[kd0], %[srdk], s48 offen offset:0 lds\n" \
"v_mfma_f32_16x16x32_bf16 v[132:135], a[80:83], a[0:3], v[132:135]\n" \
"v_exp_f32_e32 v159, v159\n" \
"v_mfma_f32_16x16x32_bf16 v[64:67], a[128:131], v[216:219], v[64:67]\n" \
"ds_read_b64_tr_b16 a[186:187], %[vvb] offset:13824\n" \
"ds_read_b64_tr_b16 a[188:189], v211 offset:9728\n" \
"v_mfma_f32_16x16x32_bf16 v[64:67], a[160:163], v[200:203], v[64:67]\n" \
"v_exp_f32_e64 v128, v128\n" \
"v_mfma_f32_16x16x32_bf16 v[68:71], a[132:135], v[216:219], v[68:71]\n" \
"v_cvt_pk_bf16_f32 v206, v156, v157\n" \
"v_cvt_pk_bf16_f32 v207, v158, v159\n" \
"v_mfma_f32_16x16x32_bf16 v[68:71], a[164:167], v[200:203], v[68:71]\n" \
"v_exp_f32_e64 v129, v129\n" \
"v_mfma_f32_16x16x32_bf16 v[72:75], a[136:139], v[216:219], v[72:75]\n" \
"ds_read_b64_tr_b16 a[190:191], v211 offset:13824\n" \
"ds_read_b64_tr_b16 a[128:129], %[vvb] offset:16384\n" \
"s_add_u32 s60, s60, 1\n" \
"s_nop 0\n" \
"v_readlane_b32 s58, %[tlo], s60\n" \
"v_mfma_f32_16x16x32_bf16 v[72:75], a[168:171], v[200:203], v[72:75]\n" \
"v_exp_f32_e64 v130, v130\n" \
"v_mfma_f32_16x16x32_bf16 v[76:79], a[140:143], v[216:219], v[76:79]\n" \
"ds_read_b64_tr_b16 a[130:131], %[vvb] offset:20480\n" \
"ds_read_b64_tr_b16 a[132:133], v211 offset:16384\n" \
"v_readlane_b32 s59, %[thi], s60\n" \
"v_mfma_f32_16x16x32_bf16 v[76:79], a[172:175], v[200:203], v[76:79]\n" \
"v_exp_f32_e64 v131, v131\n" \
"v_smfmac_f32_16x16x64_bf16 v[176:179], a[192:195], v[192:199], v228\n" \
"ds_read_b64_tr_b16 a[134:135], v211 offset:20480\n" \
"ds_read_b64_tr_b16 a[136:137], %[vvb] offset:16896\n" \
"v_mfma_f32_16x16x32_bf16 v[136:139], a[64:67], a[16:19], v[164:167]\n" \
"v_exp_f32_e64 v132, v132\n" \
"v_mfma_f32_16x16x32_bf16 v[136:139], a[68:71], a[20:23], v[136:139]\n" \
"ds_read_b64_tr_b16 a[138:139], %[vvb] offset:20992\n" \
"ds_read_b64_tr_b16 a[140:141], v211 offset:16896\n" \
"v_mfma_f32_16x16x32_bf16 v[136:139], a[72:75], a[24:27], v[136:139]\n" \
"v_exp_f32_e64 v133, v133\n" \
"v_mfma_f32_16x16x32_bf16 v[136:139], a[76:79], a[28:31], v[136:139]\n" \
"v_cvt_pk_bf16_f32 v184, v128, v129\n" \
"v_cvt_pk_bf16_f32 v185, v130, v131\n" \
"v_mfma_f32_16x16x32_bf16 v[140:143], a[92:95], a[28:31], v[164:167]\n" \
"v_exp_f32_e64 v134, v134\n" \
"v_mfma_f32_16x16x32_bf16 v[140:143], a[88:91], a[24:27], v[140:143]\n" \
"ds_read_b64_tr_b16 a[142:143], v211 offset:20992\n" \
"s_waitcnt lgkmcnt(8)\n" \
"v_mfma_f32_16x16x32_bf16 v[140:143], a[84:87], a[20:23], v[140:143]\n" \
"buffer_load_dwordx4 v212, %[srdk], s48 offen offset:1024 lds\n" \
"v_mfma_f32_16x16x32_bf16 v[140:143], a[80:83], a[16:19], v[140:143]\n" \
"v_exp_f32_e32 v135, v135\n" \
"v_mfma_f32_16x16x32_bf16 v[80:83], a[144:147], v[216:219], v[80:83]\n" \
"ds_read_b64_tr_b16 a[144:145], %[vvb] offset:17408\n" \
"ds_read_b64_tr_b16 a[146:147], %[vvb] offset:21504\n" \
"v_mfma_f32_16x16x32_bf16 v[80:83], a[176:179], v[200:203], v[80:83]\n" \
"v_exp_f32_e64 v136, v136\n" \
"v_mfma_f32_16x16x32_bf16 v[84:87], a[148:151], v[216:219], v[84:87]\n" \
"v_cvt_pk_bf16_f32 v186, v132, v133\n" \
"v_cvt_pk_bf16_f32 v187, v134, v135\n" \
"v_mfma_f32_16x16x32_bf16 v[84:87], a[180:183], v[200:203], v[84:87]\n" \
"v_exp_f32_e64 v137, v137\n" \
"v_mfma_f32_16x16x32_bf16 v[88:91], a[152:155], v[216:219], v[88:91]\n" \
".if . - adaln_body != 12288\n" \
".error \"size model\"\n" \
".endif\n" \
"Lk2_%=:\n" \
"s_cbranch_execz Lk3_%=\n" \
"s_nop 0\n" \
"v_exp_f32_e64 v138, v138\n" \
"v_mfma_f32_16x16x32_bf16 v[88:91], a[184:187], v[200:203], v[88:91]\n" \
"ds_read_b64_tr_b16 a[148:149], v211 offset:17408\n" \
"ds_read_b64_tr_b16 a[150:151], v211 offset:21504\n" \
"v_mfma_f32_16x16x32_bf16 v[92:95], a[156:159], v[216:219], v[92:95]\n" \
"v_exp_f32_e64 v139, v139\n" \
"v_mfma_f32_16x16x32_bf16 v[92:95], a[188:191], v[200:203], v[92:95]\n" \
"ds_read_b64_tr_b16 a[152:153], %[vvb] offset:17920\n" \
"v_mfma_f32_16x16x32_bf16 v[144:147], a[64:67], a[32:35], v[168:171]\n" \
"v_exp_f32_e64 v140, v140\n" \
"v_mfma_f32_16x16x32_bf16 v[144:147], a[68:71], a[36:39], v[144:147]\n" \
"ds_read_b64_tr_b16 a[154:155], %[vvb] offset:22016\n" \
"ds_read_b64_tr_b16 a[156:157], v211 offset:17920\n" \
"v_mfma_f32_16x16x32_bf16 v[144:147], a[72:75], a[40:43], v[144:147]\n" \
"v_exp_f32_e64 v141, v141\n" \
"v_mfma_f32_16x16x32_bf16 v[144:147], a[76:79], a[44:47], v[144:147]\n" \
"v_cvt_pk_bf16_f32 v188, v136, v137\n" \
"v_cvt_pk_bf16_f32 v189, v138, v139\n" \
"v_mfma_f32_16x16x32_bf16 v[148:151], a[92:95], a[44:47], v[168:171]\n" \
"v_exp_f32_e64 v142, v142\n" \
"v_mfma_f32_16x16x32_bf16 v[148:151], a[88:91], a[40:43], v[148:151]\n" \
"ds_read_b64_tr_b16 a[158:159], v211 offset:22016\n" \
"s_waitcnt lgkmcnt(8)\n" \
"v_mfma_f32_16x16x32_bf16 v[148:151], a[84:87], a[36:39], v[148:151]\n" \
"buffer_load_dwordx4 v213, %[srdk], s48 offen offset:2048 lds\n" \
"v_mfma_f32_16x16x32_bf16 v[148:151], a[80:83], a[32:35], v[148:151]\n" \
"v_exp_f32_e32 v143, v143\n" \
"v_mfma_f32_16x16x32_bf16 v[0:3], a[160:163], v[192:195], v[0:3]\n" \
"ds_read_b128 a[96:99], %[kb0] offset:24576\n" \
"v_mfma_f32_16x16x32_bf16 v[0:3], a[128:131], v[184:187], v[0:3]\n" \
"v_exp_f32_e64 v144, v144\n" \
"v_mfma_f32_16x16x32_bf16 v[4:7], a[164:167], v[192:195], v[4:7]\n" \
"v_cvt_pk_bf16_f32 v190, v140, v141\n" \
"v_cvt_pk_bf16_f32 v191, v142, v143\n" \
"v_mfma_f32_16x16x32_bf16 v[4:7], a[132:135], v[184:187], v[4:7]\n" \
"v_exp_f32_e64 v145, v145\n" \
"v_mfma_f32_16x16x32_bf16 v[8:11], a[168:171], v[192:195], v[8:11]\n" \
"ds_read_b128 a[100:103], v208 offset:24576\n" \
"v_mfma_f32_16x16x32_bf16 v[8:11], a[136:139], v[184:187], v[8:11]\n" \
"v_exp_f32_e64 v146, v146\n" \
"v_mfma_f32_16x16x32_bf16 v[12:15], a[172:175], v[192:195], v[12:15]\n" \
"ds_read_b128 a[104:107], v209 offset:24576\n" \
"v_mfma_f32_16x16x32_bf16 v[12:15], a[140:143], v[184:187], v[12:15]\n" \
"v_exp_f32_e64 v147, v147\n" \
"v_smfmac_f32_16x16x64_bf16 v[180:183], a[192:195], v[200:207], v228\n" \
"ds_read_b128 a[108:111], v210 offset:24576\n" \
"v_mfma_f32_16x16x32_bf16 v[152:155], a[64:67], a[48:51], v[172:175]\n" \
"v_exp_f32_e64 v148, v148\n" \
"v_mfma_f32_16x16x32_bf16 v[152:155], a[68:71], a[52:55], v[152:155]\n" \
"ds_read_b128 a[124:127], v210 offset:28672\n" \
"v_mfma_f32_16x16x32_bf16 v[152:155], a[72:75], a[56:59], v[152:155]\n" \
"v_exp_f32_e64 v149, v149\n" \
"v_mfma_f32_16x16x32_bf16 v[152:155], a[76:79], a[60:63], v[152:155]\n" \
"v_cvt_pk_bf16_f32 v216, v144, v145\n" \
"v_cvt_pk_bf16_f32 v217, v146, v147\n" \
"v_mfma_f32_16x16x32_bf16 v[156:159], a[92:95], a[60:63], v[172:175]\n" \
"v_exp_f32_e64 v150, v150\n" \
"v_mfma_f32_16x16x32_bf16 v[156:159], a[88:91], a[56:59], v[156:159]\n" \
"ds_read_b128 a[120:123], v209 offset:28672\n" \
"s_waitcnt lgkmcnt(6)\n" \
"v_mfma_f32_16x16x32_bf16 v[156:159], a[84:87], a[52:55], v[156:159]\n" \
"buffer_load_dwordx4 v214, %[srdk], s48 offen offset:3072 lds\n" \
"v_mfma_f32_16x16x32_bf16 v[156:159], a[80:83], a[48:51], v[156:159]\n" \
"v_exp_f32_e32 v151, v151\n" \
"v_mfma_f32_16x16x32_bf16 v[16:19], a[176:179], v[192:195], v[16:19]\n" \
"ds_read_b128 a[116:119], v208 offset:28672\n" \
"v_mfma_f32_16x16x32_bf16 v[16:19], a[144:147], v[184:187], v[16:19]\n" \
"v_exp_f32_e64 v152, v152\n" \
"v_mfma_f32_16x16x32_bf16 v[20:23], a[180:183], v[192:195], v[20:23]\n" \
"v_cvt_pk_bf16_f32 v218, v148, v149\n" \
"v_cvt_pk_bf16_f32 v219, v150, v151\n" \
"v_mfma_f32_16x16x32_bf16 v[20:23], a[148:151], v[184:187], v[20:23]\n" \
"v_exp_f32_e64 v153, v153\n" \
"v_mfma_f32_16x16x32_bf16 v[24:27], a[184:187], v[192:195], v[24:27]\n" \
"v_exp_f32_e64 v154, v154\n" \
"v_mfma_f32_16x16x32_bf16 v[24:27], a[152:155], v[184:187], v[24:27]\n" \
"ds_read_b128 a[112:115], %[kb0] offset:28672\n" \
"s_add_u32 s48, s48, 0x4000\n" \
"s_mov_b32 m0, s44\n" \
"s_addk_i32 s44, 0x400\n" \
"buffer_load_dwordx4 v229, s[64:67], s45 offen offset:0 nt lds\n" \
"s_add_u32 s45, s45, 393216\n" \
"v_mfma_f32_16x16x32_bf16 v[28:31], a[188:191], v[192:195], v[28:31]\n" \
"v_exp_f32_e64 v155, v155\n" \
"v_mfma_f32_16x16x32_bf16 v[28:31], a[156:159], v[184:187], v[28:31]\n" \
"s_waitcnt vmcnt(14) lgkmcnt(2)\n" \
"s_barrier\n" \
"v_mfma_f32_16x16x32_bf16 v[128:131], a[96:99], a[0:3], v[160:163]\n" \
"v_exp_f32_e64 v156, v156\n" \
"v_mfma_f32_16x16x32_bf16 v[128:131], a[100:103], a[4:7], v[128:131]\n" \
"s_add_u32 m0, %[m0b], 65536\n" \
"v_mfma_f32_16x16x32_bf16 v[128:131], a[104:107], a[8:11], v[128:131]\n" \
"v_exp_f32_e64 v157, v157\n" \
"v_mfma_f32_16x16x32_bf16 v[128:131], a[108:111], a[12:15], v[128:131]\n" \
"v_cvt_pk_bf16_f32 v220, v152, v153\n" \
"v_cvt_pk_bf16_f32 v221, v154, v155\n" \
"v_mfma_f32_16x16x32_bf16 v[132:135], a[124:127], a[12:15], v[160:163]\n" \
"v_exp_f32_e64 v158, v158\n" \
"v_mfma_f32_16x16x32_bf16 v[132:135], a[120:123], a[8:11], v[132:135]\n" \
"s_waitcnt lgkmcnt(0)\n" \
"v_mfma_f32_16x16x32_bf16 v[132:135], a[116:119], a[4:7], v[132:135]\n" \
"buffer_load_dwordx4 %[vd0], %[srdv], s48 offen offset:0 lds\n" \
"v_mfma_f32_16x16x32_bf16 v[132:135], a[112:115], a[0:3], v[132:135]\n" \
"v_exp_f32_e32 v159, v159\n" \
"v_mfma_f32_16x16x32_bf16 v[32:35], a[160:163], v[196:199], v[32:35]\n" \
"ds_read_b128 a[64:67], %[kb0] offset:32768\n" \
"v_mfma_f32_16x16x32_bf16 v[32:35], a[128:131], v[188:191], v[32:35]\n" \
"v_exp_f32_e64 v128, v128\n" \
"v_mfma_f32_16x16x32_bf16 v[36:39], a[164:167], v[196:199], v[36:39]\n" \
"v_cvt_pk_bf16_f32 v222, v156, v157\n" \
"v_cvt_pk_bf16_f32 v223, v158, v159\n" \
"v_mfma_f32_16x16x32_bf16 v[36:39], a[132:135], v[188:191], v[36:39]\n" \
"v_exp_f32_e64 v129, v129\n" \
"v_mfma_f32_16x16x32_bf16 v[40:43], a[168:171], v[196:199], v[40:43]\n" \
"ds_read_b128 a[68:71], v208 offset:32768\n" \
"v_mfma_f32_16x16x32_bf16 v[40:43], a[136:139], v[188:191], v[40:43]\n" \
"v_exp_f32_e64 v130, v130\n" \
"v_mfma_f32_16x16x32_bf16 v[44:47], a[172:175], v[196:199], v[44:47]\n" \
"ds_read_b128 a[72:75], v209 offset:32768\n" \
"v_mfma_f32_16x16x32_bf16 v[44:47], a[140:143], v[188:191], v[44:47]\n" \
"v_exp_f32_e64 v131, v131\n" \
"v_smfmac_f32_16x16x64_bf16 v[176:179], a[192:195], v[184:191], v228\n" \
"v_mfma_f32_16x16x32_bf16 v[136:139], a[96:99], a[16:19], v[164:167]\n" \
"v_exp_f32_e64 v132, v132\n" \
"v_mfma_f32_16x16x32_bf16 v[136:139], a[100:103], a[20:23], v[136:139]\n" \
"ds_read_b128 a[76:79], v210 offset:32768\n" \
"v_mfma_f32_16x16x32_bf16 v[136:139], a[104:107], a[24:27], v[136:139]\n" \
"v_exp_f32_e64 v133, v133\n" \
"v_mfma_f32_16x16x32_bf16 v[136:139], a[108:111], a[28:31], v[136:139]\n" \
"v_cvt_pk_bf16_f32 v192, v128, v129\n" \
"v_cvt_pk_bf16_f32 v193, v130, v131\n" \
"v_mfma_f32_16x16x32_bf16 v[140:143], a[124:127], a[28:31], v[164:167]\n" \
"v_exp_f32_e64 v134, v134\n" \
"v_mfma_f32_16x16x32_bf16 v[140:143], a[120:123], a[24:27], v[140:143]\n" \
"ds_read_b128 a[92:95], v210 offset:36864\n" \
"v_mfma_f32_16x16x32_bf16 v[140:143], a[116:119], a[20:23], v[140:143]\n" \
"buffer_load_dwordx4 v215, %[srdv], s48 offen offset:1024 lds\n" \
"v_mfma_f32_16x16x32_bf16 v[140:143], a[112:115], a[16:19], v[140:143]\n" \
"v_exp_f32_e64 v135, v135\n" \
"v_mfma_f32_16x16x32_bf16 v[48:51], a[176:179], v[196:199], v[48:51]\n" \
"ds_read_b128 a[88:91], v209 offset:36864\n" \
"v_mfma_f32_16x16x32_bf16 v[48:51], a[144:147], v[188:191], v[48:51]\n" \
"v_exp_f32_e64 v136, v136\n" \
"v_mfma_f32_16x16x32_bf16 v[52:55], a[180:183], v[196:199], v[52:55]\n" \
"v_cvt_pk_bf16_f32 v194, v132, v133\n" \
"v_cvt_pk_bf16_f32 v195, v134, v135\n" \
"v_mfma_f32_16x16x32_bf16 v[52:55], a[148:151], v[188:191], v[52:55]\n" \
"v_exp_f32_e64 v137, v137\n" \
"v_mfma_f32_16x16x32_bf16 v[56:59], a[184:187], v[196:199], v[56:59]\n" \
"v_exp_f32_e64 v138, v138\n" \
"v_mfma_f32_16x16x32_bf16 v[56:59], a[152:155], v[188:191], v[56:59]\n" \
"ds_read_b128 a[84:87], v208 offset:36864\n" \
"v_mfma_f32_16x16x32_bf16 v[60:63], a[188:191], v[196:199], v[60:63]\n" \
"v_exp_f32_e64 v139, v139\n" \
"v_mfma_f32_16x16x32_bf16 v[60:63], a[156:159], v[188:191], v[60:63]\n" \
"s_cmp_lg_u32 %[wid], 0\n" \
"s_cbranch_scc1 LtA3_%=\n" \
"global_load_dword v237, v224, s[58:59]\n" \
"s_add_u32 s61, s48, 49152\n" \
"s_mov_b32 exec_hi, 0\n" \
"s_nop 0\n" \
"buffer_load_dword v237, v224, %[srdp], s61 offen\n" \
"s_mov_b32 exec_hi, -1\n" \
"s_nop 0\n" \
"LtA3_%=:\n" \
"v_mfma_f32_16x16x32_bf16 v[144:147], a[96:99], a[32:35], v[168:171]\n" \
"v_exp_f32_e64 v140, v140\n" \
"v_mfma_f32_16x16x32_bf16 v[144:147], a[100:103], a[36:39], v[144:147]\n" \
"ds_read_b128 a[80:83], %[kb0] offset:36864\n" \
"v_mfma_f32_16x16x32_bf16 v[144:147], a[104:107], a[40:43], v[144:147]\n" \
"v_exp_f32_e64 v141, v141\n" \
"v_mfma_f32_16x16x32_bf16 v[144:147], a[108:111], a[44:47], v[144:147]\n" \
"v_cvt_pk_bf16_f32 v196, v136, v137\n" \
"v_cvt_pk_bf16_f32 v197, v138, v139\n" \
"v_mfma_f32_16x16x32_bf16 v[148:151], a[124:127], a[44:47], v[168:171]\n" \
"v_exp_f32_e64 v142, v142\n" \
"v_mfma_f32_16x16x32_bf16 v[148:151], a[120:123], a[40:43], v[148:151]\n" \
"v_mfma_f32_16x16x32_bf16 v[148:151], a[116:119], a[36:39], v[148:151]\n" \
"buffer_load_dwordx4 %[vd0], %[srdv], s48 offen offset:2048 lds\n" \
"v_mfma_f32_16x16x32_bf16 v[148:151], a[112:115], a[32:35], v[148:151]\n" \
"v_exp_f32_e64 v143, v143\n" \
"v_mfma_f32_16x16x32_bf16 v[96:99], a[160:163], v[204:207], v[96:99]\n" \
"ds_read_b64_tr_b16 a[160:161], %[vvb] offset:24576\n" \
"v_mfma_f32_16x16x32_bf16 v[96:99], a[128:131], v[220:223], v[96:99]\n" \
"v_exp_f32_e64 v144, v144\n" \
"v_mfma_f32_16x16x32_bf16 v[100:103], a[164:167], v[204:207], v[100:103]\n" \
"v_cvt_pk_bf16_f32 v198, v140, v141\n" \
"v_cvt_pk_bf16_f32 v199, v142, v143\n" \
"v_mfma_f32_16x16x32_bf16 v[100:103], a[132:135], v[220:223], v[100:103]\n" \
"v_exp_f32_e64 v145, v145\n" \
"v_mfma_f32_16x16x32_bf16 v[104:107], a[168:171], v[204:207], v[104:107]\n" \
"ds_read_b64_tr_b16 a[162:163], %[vvb] offset:28672\n" \
"ds_read_b64_tr_b16 a[164:165], v211 offset:24576\n" \
"v_mfma_f32_16x16x32_bf16 v[104:107], a[136:139], v[220:223], v[104:107]\n" \
"v_exp_f32_e64 v146, v146\n" \
"v_mfma_f32_16x16x32_bf16 v[108:111], a[172:175], v[204:207], v[108:111]\n" \
"ds_read_b64_tr_b16 a[166:167], v211 offset:28672\n" \
"v_mfma_f32_16x16x32_bf16 v[108:111], a[140:143], v[220:223], v[108:111]\n" \
"v_exp_f32_e64 v147, v147\n" \
"v_smfmac_f32_16x16x64_bf16 v[180:183], a[192:195], v[216:223], v228\n" \
"ds_read_b64_tr_b16 a[168:169], %[vvb] offset:25088\n" \
"v_mfma_f32_16x16x32_bf16 v[152:155], a[96:99], a[48:51], v[172:175]\n" \
"v_exp_f32_e64 v148, v148\n" \
"v_mfma_f32_16x16x32_bf16 v[152:155], a[100:103], a[52:55], v[152:155]\n" \
"ds_read_b64_tr_b16 a[170:171], %[vvb] offset:29184\n" \
"v_mfma_f32_16x16x32_bf16 v[152:155], a[104:107], a[56:59], v[152:155]\n" \
"v_exp_f32_e64 v149, v149\n" \
"v_mfma_f32_16x16x32_bf16 v[152:155], a[108:111], a[60:63], v[152:155]\n" \
"v_cvt_pk_bf16_f32 v200, v144, v145\n" \
"v_cvt_pk_bf16_f32 v201, v146, v147\n" \
"v_mfma_f32_16x16x32_bf16 v[156:159], a[124:127], a[60:63], v[172:175]\n" \
"v_exp_f32_e64 v150, v150\n" \
"v_mfma_f32_16x16x32_bf16 v[156:159], a[120:123], a[56:59], v[156:159]\n" \
"ds_read_b64_tr_b16 a[172:173], v211 offset:25088\n" \
"v_mfma_f32_16x16x32_bf16 v[156:159], a[116:119], a[52:55], v[156:159]\n" \
"buffer_load_dwordx4 v215, %[srdv], s48 offen offset:3072 lds\n" \
"v_mfma_f32_16x16x32_bf16 v[156:159], a[112:115], a[48:51], v[156:159]\n" \
"v_exp_f32_e64 v151, v151\n" \
"v_mfma_f32_16x16x32_bf16 v[112:115], a[176:179], v[204:207], v[112:115]\n" \
"ds_read_b64_tr_b16 a[174:175], v211 offset:29184\n" \
"ds_read_b64_tr_b16 a[176:177], %[vvb] offset:25600\n" \
"v_mfma_f32_16x16x32_bf16 v[112:115], a[144:147], v[220:223], v[112:115]\n" \
"v_exp_f32_e64 v152, v152\n" \
"v_mfma_f32_16x16x32_bf16 v[116:119], a[180:183], v[204:207], v[116:119]\n" \
"v_cvt_pk_bf16_f32 v202, v148, v149\n" \
"v_cvt_pk_bf16_f32 v203, v150, v151\n" \
"v_mfma_f32_16x16x32_bf16 v[116:119], a[148:151], v[220:223], v[116:119]\n" \
"v_exp_f32_e64 v153, v153\n" \
"v_mfma_f32_16x16x32_bf16 v[120:123], a[184:187], v[204:207], v[120:123]\n" \
"v_exp_f32_e64 v154, v154\n" \
"v_mfma_f32_16x16x32_bf16 v[120:123], a[152:155], v[220:223], v[120:123]\n" \
"ds_read_b64_tr_b16 a[178:179], %[vvb] offset:29696\n" \
"v_mfma_f32_16x16x32_bf16 v[124:127], a[188:191], v[204:207], v[124:127]\n" \
"v_exp_f32_e64 v155, v155\n" \
"v_mfma_f32_16x16x32_bf16 v[124:127], a[156:159], v[220:223], v[124:127]\n" \
"ds_read_b64_tr_b16 a[180:181], v211 offset:25600\n" \
"s_waitcnt lgkmcnt(13)\n" \
"v_mfma_f32_16x16x32_bf16 v[128:131], a[64:67], a[0:3], v[160:163]\n" \
"v_exp_f32_e32 v156, v156\n" \
"v_mfma_f32_16x16x32_bf16 v[128:131], a[68:71], a[4:7], v[128:131]\n" \
"ds_read_b64_tr_b16 a[182:183], v211 offset:29696\n" \
"s_add_u32 m0, %[m0b], 16384\n" \
"v_mfma_f32_16x16x32_bf16 v[128:131], a[72:75], a[8:11], v[128:131]\n" \
"v_exp_f32_e64 v157, v157\n" \
"v_mfma_f32_16x16x32_bf16 v[128:131], a[76:79], a[12:15], v[128:131]\n" \
"v_cvt_pk_bf16_f32 v204, v152, v153\n" \
"v_cvt_pk_bf16_f32 v205, v154, v155\n" \
"v_mfma_f32_16x16x32_bf16 v[132:135], a[92:95], a[12:15], v[160:163]\n" \
"v_exp_f32_e64 v158, v158\n" \
"v_mfma_f32_16x16x32_bf16 v[132:135], a[88:91], a[8:11], v[132:135]\n" \
"ds_read_b64_tr_b16 a[184:185], %[vvb] offset:26112\n" \
"s_waitcnt lgkmcnt(5)\n" \
"v_mfma_f32_16x16x32_bf16 v[132:135], a[84:87], a[4:7], v[132:135]\n" \
"buffer_load_dwordx4 %[kd0], %[srdk], s48 offen offset:0 lds\n" \
"v_mfma_f32_16x16x32_bf16 v[132:135], a[80:83], a[0:3], v[132:135]\n" \
"v_exp_f32_e32 v159, v159\n" \
"v_mfma_f32_16x16x32_bf16 v[64:67], a[128:131], v[216:219], v[64:67]\n" \
"ds_read_b64_tr_b16 a[186:187], %[vvb] offset:30208\n" \
"ds_read_b64_tr_b16 a[188:189], v211 offset:26112\n" \
"v_mfma_f32_16x16x32_bf16 v[64:67], a[160:163], v[200:203], v[64:67]\n" \
"v_exp_f32_e64 v128, v128\n" \
"v_mfma_f32_16x16x32_bf16 v[68:71], a[132:135], v[216:219], v[68:71]\n" \
"v_cvt_pk_bf16_f32 v206, v156, v157\n" \
"v_cvt_pk_bf16_f32 v207, v158, v159\n" \
"v_mfma_f32_16x16x32_bf16 v[68:71], a[164:167], v[200:203], v[68:71]\n" \
"v_exp_f32_e64 v129, v129\n" \
"v_mfma_f32_16x16x32_bf16 v[72:75], a[136:139], v[216:219], v[72:75]\n" \
"ds_read_b64_tr_b16 a[190:191], v211 offset:30208\n" \
"ds_read_b64_tr_b16 a[128:129], %[vvb] offset:32768\n" \
"v_mfma_f32_16x16x32_bf16 v[72:75], a[168:171], v[200:203], v[72:75]\n" \
"v_exp_f32_e64 v130, v130\n" \
"v_mfma_f32_16x16x32_bf16 v[76:79], a[140:143], v[216:219], v[76:79]\n" \
"ds_read_b64_tr_b16 a[130:131], %[vvb] offset:36864\n" \
"ds_read_b64_tr_b16 a[132:133], v211 offset:32768\n" \
"v_mfma_f32_16x16x32_bf16 v[76:79], a[172:175], v[200:203], v[76:79]\n" \
"v_exp_f32_e64 v131, v131\n" \
"v_smfmac_f32_16x16x64_bf16 v[176:179], a[192:195], v[192:199], v228\n" \
"ds_read_b64_tr_b16 a[134:135], v211 offset:36864\n" \
"ds_read_b64_tr_b16 a[136:137], %[vvb] offset:33280\n" \
"v_mfma_f32_16x16x32_bf16 v[136:139], a[64:67], a[16:19], v[164:167]\n" \
"v_exp_f32_e64 v132, v132\n" \
"v_mfma_f32_16x16x32_bf16 v[136:139], a[68:71], a[20:23], v[136:139]\n" \
"ds_read_b64_tr_b16 a[138:139], %[vvb] offset:37376\n" \
"ds_read_b64_tr_b16 a[140:141], v211 offset:33280\n" \
"v_mfma_f32_16x16x32_bf16 v[136:139], a[72:75], a[24:27], v[136:139]\n" \
"v_exp_f32_e64 v133, v133\n" \
"v_mfma_f32_16x16x32_bf16 v[136:139], a[76:79], a[28:31], v[136:139]\n" \
"v_cvt_pk_bf16_f32 v184, v128, v129\n" \
"v_cvt_pk_bf16_f32 v185, v130, v131\n" \
"v_mfma_f32_16x16x32_bf16 v[140:143], a[92:95], a[28:31], v[164:167]\n" \
"v_exp_f32_e64 v134, v134\n" \
"v_mfma_f32_16x16x32_bf16 v[140:143], a[88:91], a[24:27], v[140:143]\n" \
"ds_read_b64_tr_b16 a[142:143], v211 offset:37376\n" \
"s_waitcnt lgkmcnt(8)\n" \
"v_mfma_f32_16x16x32_bf16 v[140:143], a[84:87], a[20:23], v[140:143]\n" \
"buffer_load_dwordx4 v212, %[srdk], s48 offen offset:1024 lds\n" \
"v_mfma_f32_16x16x32_bf16 v[140:143], a[80:83], a[16:19], v[140:143]\n" \
"v_exp_f32_e32 v135, v135\n" \
"v_mfma_f32_16x16x32_bf16 v[80:83], a[144:147], v[216:219], v[80:83]\n" \
"ds_read_b64_tr_b16 a[144:145], %[vvb] offset:33792\n" \
"ds_read_b64_tr_b16 a[146:147], %[vvb] offset:37888\n" \
"v_mfma_f32_16x16x32_bf16 v[80:83], a[176:179], v[200:203], v[80:83]\n" \
"v_exp_f32_e64 v136, v136\n" \
"v_mfma_f32_16x16x32_bf16 v[84:87], a[148:151], v[216:219], v[84:87]\n" \
"v_cvt_pk_bf16_f32 v186, v132, v133\n" \
"v_cvt_pk_bf16_f32 v187, v134, v135\n" \
"v_mfma_f32_16x16x32_bf16 v[84:87], a[180:183], v[200:203], v[84:87]\n" \
"v_exp_f32_e64 v137, v137\n" \
"v_mfma_f32_16x16x32_bf16 v[88:91], a[152:155], v[216:219], v[88:91]\n" \
"v_exp_f32_e64 v138, v138\n" \
"v_mfma_f32_16x16x32_bf16 v[88:91], a[184:187], v[200:203], v[88:91]\n" \
"ds_read_b64_tr_b16 a[148:149], v211 offset:33792\n" \
"ds_read_b64_tr_b16 a[150:151], v211 offset:37888\n" \
"v_mfma_f32_16x16x32_bf16 v[92:95], a[156:159], v[216:219], v[92:95]\n" \
"v_exp_f32_e64 v139, v139\n" \
"v_mfma_f32_16x16x32_bf16 v[92:95], a[188:191], v[200:203], v[92:95]\n" \
"ds_read_b64_tr_b16 a[152:153], %[vvb] offset:34304\n" \
"buffer_store_dwordx4 a[196:199], v235, %[srdz], s62 offen offset:0 sc1\n" \
"v_mfma_f32_16x16x32_bf16 v[144:147], a[64:67], a[32:35], v[168:171]\n" \
"v_exp_f32_e64 v140, v140\n" \
"v_mfma_f32_16x16x32_bf16 v[144:147], a[68:71], a[36:39], v[144:147]\n" \
"ds_read_b64_tr_b16 a[154:155], %[vvb] offset:38400\n" \
"ds_read_b64_tr_b16 a[156:157], v211 offset:34304\n" \
"v_mfma_f32_16x16x32_bf16 v[144:147], a[72:75], a[40:43], v[144:147]\n" \
"v_exp_f32_e64 v141, v141\n" \
"v_mfma_f32_16x16x32_bf16 v[144:147], a[76:79], a[44:47], v[144:147]\n" \
"v_cvt_pk_bf16_f32 v188, v136, v137\n" \
"v_cvt_pk_bf16_f32 v189, v138, v139\n" \
"v_mfma_f32_16x16x32_bf16 v[148:151], a[92:95], a[44:47], v[168:171]\n" \
"v_exp_f32_e64 v142, v142\n" \
"v_mfma_f32_16x16x32_bf16 v[148:151], a[88:91], a[40:43], v[148:151]\n" \
"ds_read_b64_tr_b16 a[158:159], v211 offset:38400\n" \
"s_waitcnt lgkmcnt(8)\n" \
"v_mfma_f32_16x16x32_bf16 v[148:151], a[84:87], a[36:39], v[148:151]\n" \
"buffer_load_dwordx4 v213, %[srdk], s48 offen offset:2048 lds\n" \
"v_mfma_f32_16x16x32_bf16 v[148:151], a[80:83], a[32:35], v[148:151]\n" \
"v_exp_f32_e32 v143, v143\n" \
"v_mfma_f32_16x16x32_bf16 v[0:3], a[160:163], v[192:195], v[0:3]\n" \
"ds_read_b128 a[96:99], %[kb0] offset:40960\n" \
"v_mfma_f32_16x16x32_bf16 v[0:3], a[128:131], v[184:187], v[0:3]\n" \
"v_exp_f32_e64 v144, v144\n" \
"v_mfma_f32_16x16x32_bf16 v[4:7], a[164:167], v[192:195], v[4:7]\n" \
"v_cvt_pk_bf16_f32 v190, v140, v141\n" \
"v_cvt_pk_bf16_f32 v191, v142, v143\n" \
"v_mfma_f32_16x16x32_bf16 v[4:7], a[132:135], v[184:187], v[4:7]\n" \
"v_exp_f32_e64 v145, v145\n" \
"v_mfma_f32_16x16x32_bf16 v[8:11], a[168:171], v[192:195], v[8:11]\n" \
"ds_read_b128 a[100:103], v208 offset:40960\n" \
"v_mfma_f32_16x16x32_bf16 v[8:11], a[136:139], v[184:187], v[8:11]\n" \
"v_exp_f32_e64 v146, v146\n" \
"v_mfma_f32_16x16x32_bf16 v[12:15], a[172:175], v[192:195], v[12:15]\n" \
"ds_read_b128 a[104:107], v209 offset:40960\n" \
"v_mfma_f32_16x16x32_bf16 v[12:15], a[140:143], v[184:187], v[12:15]\n" \
"v_exp_f32_e64 v147, v147\n" \
"v_smfmac_f32_16x16x64_bf16 v[180:183], a[192:195], v[200:207], v228\n" \
"ds_read_b128 a[108:111], v210 offset:40960\n" \
"v_mfma_f32_16x16x32_bf16 v[152:155], a[64:67], a[48:51], v[172:175]\n" \
"v_exp_f32_e64 v148, v148\n" \
"v_mfma_f32_16x16x32_bf16 v[152:155], a[68:71], a[52:55], v[152:155]\n" \
"ds_read_b128 a[124:127], v210 offset:45056\n" \
"v_mfma_f32_16x16x32_bf16 v[152:155], a[72:75], a[56:59], v[152:155]\n" \
"v_exp_f32_e64 v149, v149\n" \
"v_mfma_f32_16x16x32_bf16 v[152:155], a[76:79], a[60:63], v[152:155]\n" \
"v_cvt_pk_bf16_f32 v216, v144, v145\n" \
"v_cvt_pk_bf16_f32 v217, v146, v147\n" \
"v_mfma_f32_16x16x32_bf16 v[156:159], a[92:95], a[60:63], v[172:175]\n" \
"v_exp_f32_e64 v150, v150\n" \
"v_mfma_f32_16x16x32_bf16 v[156:159], a[88:91], a[56:59], v[156:159]\n" \
"ds_read_b128 a[120:123], v209 offset:45056\n" \
"s_waitcnt lgkmcnt(6)\n" \
"v_mfma_f32_16x16x32_bf16 v[156:159], a[84:87], a[52:55], v[156:159]\n" \
"buffer_load_dwordx4 v214, %[srdk], s48 offen offset:3072 lds\n" \
"v_mfma_f32_16x16x32_bf16 v[156:159], a[80:83], a[48:51], v[156:159]\n" \
"v_exp_f32_e32 v151, v151\n" \
"v_mfma_f32_16x16x32_bf16 v[16:19], a[176:179], v[192:195], v[16:19]\n" \
"ds_read_b128 a[116:119], v208 offset:45056\n" \
"v_mfma_f32_16x16x32_bf16 v[16:19], a[144:147], v[184:187], v[16:19]\n" \
"v_exp_f32_e64 v152, v152\n" \
"v_mfma_f32_16x16x32_bf16 v[20:23], a[180:183], v[192:195], v[20:23]\n" \
"v_cvt_pk_bf16_f32 v218, v148, v149\n" \
"v_cvt_pk_bf16_f32 v219, v150, v151\n" \
"v_mfma_f32_16x16x32_bf16 v[20:23], a[148:151], v[184:187], v[20:23]\n" \
"v_exp_f32_e64 v153, v153\n" \
"v_mfma_f32_16x16x32_bf16 v[24:27], a[184:187], v[192:195], v[24:27]\n" \
"v_exp_f32_e64 v154, v154\n" \
"v_mfma_f32_16x16x32_bf16 v[24:27], a[152:155], v[184:187], v[24:27]\n" \
"ds_read_b128 a[112:115], %[kb0] offset:45056\n" \
"s_add_u32 s48, s48, 0x4000\n" \
"v_mfma_f32_16x16x32_bf16 v[28:31], a[188:191], v[192:195], v[28:31]\n" \
"v_exp_f32_e64 v155, v155\n" \
"v_mfma_f32_16x16x32_bf16 v[28:31], a[156:159], v[184:187], v[28:31]\n" \
"s_waitcnt vmcnt(14) lgkmcnt(2)\n" \
"s_barrier\n" \
"v_mfma_f32_16x16x32_bf16 v[128:131], a[96:99], a[0:3], v[160:163]\n" \
"v_exp_f32_e64 v156, v156\n" \
"v_mfma_f32_16x16x32_bf16 v[128:131], a[100:103], a[4:7], v[128:131]\n" \
"s_add_u32 m0, %[m0b], 81920\n" \
"v_mfma_f32_16x16x32_bf16 v[128:131], a[104:107], a[8:11], v[128:131]\n" \
"v_exp_f32_e64 v157, v157\n" \
"v_mfma_f32_16x16x32_bf16 v[128:131], a[108:111], a[12:15], v[128:131]\n" \
"v_cvt_pk_bf16_f32 v220, v152, v153\n" \
"v_cvt_pk_bf16_f32 v221, v154, v155\n" \
"v_mfma_f32_16x16x32_bf16 v[132:135], a[124:127], a[12:15], v[160:163]\n" \
"v_exp_f32_e64 v158, v158\n" \
"v_mfma_f32_16x16x32_bf16 v[132:135], a[120:123], a[8:11], v[132:135]\n" \
"s_waitcnt lgkmcnt(0)\n" \
"v_mfma_f32_16x16x32_bf16 v[132:135], a[116:119], a[4:7], v[132:135]\n" \
"buffer_load_dwordx4 %[vd0], %[srdv], s48 offen offset:0 lds\n" \
"v_mfma_f32_16x16x32_bf16 v[132:135], a[112:115], a[0:3], v[132:135]\n" \
"v_exp_f32_e32 v159, v159\n" \
"v_mfma_f32_16x16x32_bf16 v[32:35], a[160:163], v[196:199], v[32:35]\n" \
"ds_read_b128 a[64:67], %[kb0] offset:49152\n" \
"v_mfma_f32_16x16x32_bf16 v[32:35], a[128:131], v[188:191], v[32:35]\n" \
"v_exp_f32_e64 v128, v128\n" \
"v_mfma_f32_16x16x32_bf16 v[36:39], a[164:167], v[196:199], v[36:39]\n" \
"v_cvt_pk_bf16_f32 v222, v156, v157\n" \
"v_cvt_pk_bf16_f32 v223, v158, v159\n" \
"v_mfma_f32_16x16x32_bf16 v[36:39], a[132:135], v[188:191], v[36:39]\n" \
"v_exp_f32_e64 v129, v129\n" \
"v_mfma_f32_16x16x32_bf16 v[40:43], a[168:171], v[196:199], v[40:43]\n" \
"ds_read_b128 a[68:71], v208 offset:49152\n" \
"v_mfma_f32_16x16x32_bf16 v[40:43], a[136:139], v[188:191], v[40:43]\n" \
"v_exp_f32_e64 v130, v130\n" \
"v_mfma_f32_16x16x32_bf16 v[44:47], a[172:175], v[196:199], v[44:47]\n" \
"ds_read_b128 a[72:75], v209 offset:49152\n" \
"v_mfma_f32_16x16x32_bf16 v[44:47], a[140:143], v[188:191], v[44:47]\n" \
"v_exp_f32_e64 v131, v131\n" \
"v_smfmac_f32_16x16x64_bf16 v[176:179], a[192:195], v[184:191], v228\n" \
"v_mfma_f32_16x16x32_bf16 v[136:139], a[96:99], a[16:19], v[164:167]\n" \
"v_exp_f32_e64 v132, v132\n" \
"v_mfma_f32_16x16x32_bf16 v[136:139], a[100:103], a[20:23], v[136:139]\n" \
"ds_read_b128 a[76:79], v210 offset:49152\n" \
"v_mfma_f32_16x16x32_bf16 v[136:139], a[104:107], a[24:27], v[136:139]\n" \
"v_exp_f32_e64 v133, v133\n" \
"v_mfma_f32_16x16x32_bf16 v[136:139], a[108:111], a[28:31], v[136:139]\n" \
"v_cvt_pk_bf16_f32 v192, v128, v129\n" \
"v_cvt_pk_bf16_f32 v193, v130, v131\n" \
"v_mfma_f32_16x16x32_bf16 v[140:143], a[124:127], a[28:31], v[164:167]\n" \
"v_exp_f32_e64 v134, v134\n" \
"v_mfma_f32_16x16x32_bf16 v[140:143], a[120:123], a[24:27], v[140:143]\n" \
"ds_read_b128 a[92:95], v210 offset:53248\n" \
"v_mfma_f32_16x16x32_bf16 v[140:143], a[116:119], a[20:23], v[140:143]\n" \
"buffer_load_dwordx4 v215, %[srdv], s48 offen offset:1024 lds\n" \
"v_mfma_f32_16x16x32_bf16 v[140:143], a[112:115], a[16:19], v[140:143]\n" \
"v_exp_f32_e64 v135, v135\n" \
"v_mfma_f32_16x16x32_bf16 v[48:51], a[176:179], v[196:199], v[48:51]\n" \
"ds_read_b128 a[88:91], v209 offset:53248\n" \
"v_mfma_f32_16x16x32_bf16 v[48:51], a[144:147], v[188:191], v[48:51]\n" \
"v_exp_f32_e64 v136, v136\n" \
"v_mfma_f32_16x16x32_bf16 v[52:55], a[180:183], v[196:199], v[52:55]\n" \
"v_cvt_pk_bf16_f32 v194, v132, v133\n" \
"v_cvt_pk_bf16_f32 v195, v134, v135\n" \
"v_mfma_f32_16x16x32_bf16 v[52:55], a[148:151], v[188:191], v[52:55]\n" \
"v_exp_f32_e64 v137, v137\n" \
"v_mfma_f32_16x16x32_bf16 v[56:59], a[184:187], v[196:199], v[56:59]\n" \
"v_exp_f32_e64 v138, v138\n" \
"v_mfma_f32_16x16x32_bf16 v[56:59], a[152:155], v[188:191], v[56:59]\n" \
"ds_read_b128 a[84:87], v208 offset:53248\n" \
"v_mfma_f32_16x16x32_bf16 v[60:63], a[188:191], v[196:199], v[60:63]\n" \
"v_exp_f32_e64 v139, v139\n" \
"v_mfma_f32_16x16x32_bf16 v[60:63], a[156:159], v[188:191], v[60:63]\n" \
"s_cmp_lg_u32 %[wid], 1\n" \
"s_cbranch_scc1 LtA5_%=\n" \
"global_load_dword v237, v224, s[58:59]\n" \
"s_add_u32 s61, s48, 49152\n" \
"s_mov_b32 exec_hi, 0\n" \
"s_nop 0\n" \
"buffer_load_dword v237, v224, %[srdp], s61 offen\n" \
"s_mov_b32 exec_hi, -1\n" \
"s_nop 0\n" \
"LtA5_%=:\n" \
"v_mfma_f32_16x16x32_bf16 v[144:147], a[96:99], a[32:35], v[168:171]\n" \
"v_exp_f32_e64 v140, v140\n" \
"v_mfma_f32_16x16x32_bf16 v[144:147], a[100:103], a[36:39], v[144:147]\n" \
"ds_read_b128 a[80:83], %[kb0] offset:53248\n" \
"v_mfma_f32_16x16x32_bf16 v[144:147], a[104:107], a[40:43], v[144:147]\n" \
"v_exp_f32_e64 v141, v141\n" \
"v_mfma_f32_16x16x32_bf16 v[144:147], a[108:111], a[44:47], v[144:147]\n" \
"v_cvt_pk_bf16_f32 v196, v136, v137\n" \
"v_cvt_pk_bf16_f32 v197, v138, v139\n" \
"v_mfma_f32_16x16x32_bf16 v[148:151], a[124:127], a[44:47], v[168:171]\n" \
"v_exp_f32_e64 v142, v142\n" \
"v_mfma_f32_16x16x32_bf16 v[148:151], a[120:123], a[40:43], v[148:151]\n" \
"v_mfma_f32_16x16x32_bf16 v[148:151], a[116:119], a[36:39], v[148:151]\n" \
"buffer_load_dwordx4 %[vd0], %[srdv], s48 offen offset:2048 lds\n" \
"v_mfma_f32_16x16x32_bf16 v[148:151], a[112:115], a[32:35], v[148:151]\n" \
"v_exp_f32_e64 v143, v143\n" \
"v_mfma_f32_16x16x32_bf16 v[96:99], a[160:163], v[204:207], v[96:99]\n" \
"ds_read_b64_tr_b16 a[160:161], %[vvb] offset:40960\n" \
"v_mfma_f32_16x16x32_bf16 v[96:99], a[128:131], v[220:223], v[96:99]\n" \
"v_exp_f32_e64 v144, v144\n" \
"v_mfma_f32_16x16x32_bf16 v[100:103], a[164:167], v[204:207], v[100:103]\n" \
"v_cvt_pk_bf16_f32 v198, v140, v141\n" \
"v_cvt_pk_bf16_f32 v199, v142, v143\n" \
"v_mfma_f32_16x16x32_bf16 v[100:103], a[132:135], v[220:223], v[100:103]\n" \
"v_exp_f32_e64 v145, v145\n" \
"v_mfma_f32_16x16x32_bf16 v[104:107], a[168:171], v[204:207], v[104:107]\n" \
"ds_read_b64_tr_b16 a[162:163], %[vvb] offset:45056\n" \
"ds_read_b64_tr_b16 a[164:165], v211 offset:40960\n" \
"v_mfma_f32_16x16x32_bf16 v[104:107], a[136:139], v[220:223], v[104:107]\n" \
"v_exp_f32_e64 v146, v146\n" \
"v_mfma_f32_16x16x32_bf16 v[108:111], a[172:175], v[204:207], v[108:111]\n" \
"ds_read_b64_tr_b16 a[166:167], v211 offset:45056\n" \
"v_mfma_f32_16x16x32_bf16 v[108:111], a[140:143], v[220:223], v[108:111]\n" \
"v_exp_f32_e64 v147, v147\n" \
"v_smfmac_f32_16x16x64_bf16 v[180:183], a[192:195], v[216:223], v228\n" \
"ds_read_b64_tr_b16 a[168:169], %[vvb] offset:41472\n" \
"v_mfma_f32_16x16x32_bf16 v[152:155], a[96:99], a[48:51], v[172:175]\n" \
"v_exp_f32_e64 v148, v148\n" \
"v_mfma_f32_16x16x32_bf16 v[152:155], a[100:103], a[52:55], v[152:155]\n" \
"ds_read_b64_tr_b16 a[170:171], %[vvb] offset:45568\n" \
"v_mfma_f32_16x16x32_bf16 v[152:155], a[104:107], a[56:59], v[152:155]\n" \
"v_exp_f32_e64 v149, v149\n" \
"v_mfma_f32_16x16x32_bf16 v[152:155], a[108:111], a[60:63], v[152:155]\n" \
"v_cvt_pk_bf16_f32 v200, v144, v145\n" \
"v_cvt_pk_bf16_f32 v201, v146, v147\n" \
"v_mfma_f32_16x16x32_bf16 v[156:159], a[124:127], a[60:63], v[172:175]\n" \
"v_exp_f32_e64 v150, v150\n" \
"v_mfma_f32_16x16x32_bf16 v[156:159], a[120:123], a[56:59], v[156:159]\n" \
"ds_read_b64_tr_b16 a[172:173], v211 offset:41472\n" \
"v_mfma_f32_16x16x32_bf16 v[156:159], a[116:119], a[52:55], v[156:159]\n" \
"buffer_load_dwordx4 v215, %[srdv], s48 offen offset:3072 lds\n" \
"v_mfma_f32_16x16x32_bf16 v[156:159], a[112:115], a[48:51], v[156:159]\n" \
"v_exp_f32_e64 v151, v151\n" \
"v_mfma_f32_16x16x32_bf16 v[112:115], a[176:179], v[204:207], v[112:115]\n" \
"ds_read_b64_tr_b16 a[174:175], v211 offset:45568\n" \
"ds_read_b64_tr_b16 a[176:177], %[vvb] offset:41984\n" \
"v_mfma_f32_16x16x32_bf16 v[112:115], a[144:147], v[220:223], v[112:115]\n" \
"v_exp_f32_e64 v152, v152\n" \
"v_mfma_f32_16x16x32_bf16 v[116:119], a[180:183], v[204:207], v[116:119]\n" \
"v_cvt_pk_bf16_f32 v202, v148, v149\n" \
".if . - adaln_body != 16384\n" \
".error \"size model\"\n" \
".endif\n" \
"Lk3_%=:\n" \
"s_cbranch_execz Lk4_%=\n" \
"s_nop 0\n" \
"v_cvt_pk_bf16_f32 v203, v150, v151\n" \
"v_mfma_f32_16x16x32_bf16 v[116:119], a[148:151], v[220:223], v[116:119]\n" \
"v_exp_f32_e64 v153, v153\n" \
"v_mfma_f32_16x16x32_bf16 v[120:123], a[184:187], v[204:207], v[120:123]\n" \
"v_exp_f32_e64 v154, v154\n" \
"v_mfma_f32_16x16x32_bf16 v[120:123], a[152:155], v[220:223], v[120:123]\n" \
"ds_read_b64_tr_b16 a[178:179], %[vvb] offset:46080\n" \
"v_mfma_f32_16x16x32_bf16 v[124:127], a[188:191], v[204:207], v[124:127]\n" \
"v_exp_f32_e64 v155, v155\n" \
"v_mfma_f32_16x16x32_bf16 v[124:127], a[156:159], v[220:223], v[124:127]\n" \
"ds_read_b64_tr_b16 a[180:181], v211 offset:41984\n" \
"s_waitcnt lgkmcnt(13)\n" \
"v_mfma_f32_16x16x32_bf16 v[128:131], a[64:67], a[0:3], v[160:163]\n" \
"v_exp_f32_e32 v156, v156\n" \
"v_mfma_f32_16x16x32_bf16 v[128:131], a[68:71], a[4:7], v[128:131]\n" \
"ds_read_b64_tr_b16 a[182:183], v211 offset:46080\n" \
"s_add_u32 m0, %[m0b], 32768\n" \
"v_mfma_f32_16x16x32_bf16 v[128:131], a[72:75], a[8:11], v[128:131]\n" \
"v_exp_f32_e64 v157, v157\n" \
"v_mfma_f32_16x16x32_bf16 v[128:131], a[76:79], a[12:15], v[128:131]\n" \
"v_cvt_pk_bf16_f32 v204, v152, v153\n" \
"v_cvt_pk_bf16_f32 v205, v154, v155\n" \
"v_mfma_f32_16x16x32_bf16 v[132:135], a[92:95], a[12:15], v[160:163]\n" \
"v_exp_f32_e64 v158, v158\n" \
"v_mfma_f32_16x16x32_bf16 v[132:135], a[88:91], a[8:11], v[132:135]\n" \
"ds_read_b64_tr_b16 a[184:185], %[vvb] offset:42496\n" \
"s_waitcnt lgkmcnt(5)\n" \
"v_mfma_f32_16x16x32_bf16 v[132:135], a[84:87], a[4:7], v[132:135]\n" \
"buffer_load_dwordx4 %[kd0], %[srdk], s48 offen offset:0 lds\n" \
"v_mfma_f32_16x16x32_bf16 v[132:135], a[80:83], a[0:3], v[132:135]\n" \
"v_exp_f32_e32 v159, v159\n" \
"v_mfma_f32_16x16x32_bf16 v[64:67], a[128:131], v[216:219], v[64:67]\n" \
"ds_read_b64_tr_b16 a[186:187], %[vvb] offset:46592\n" \
"ds_read_b64_tr_b16 a[188:189], v211 offset:42496\n" \
"v_mfma_f32_16x16x32_bf16 v[64:67], a[160:163], v[200:203], v[64:67]\n" \
"v_exp_f32_e64 v128, v128\n" \
"v_mfma_f32_16x16x32_bf16 v[68:71], a[132:135], v[216:219], v[68:71]\n" \
"v_cvt_pk_bf16_f32 v206, v156, v157\n" \
"v_cvt_pk_bf16_f32 v207, v158, v159\n" \
"v_mfma_f32_16x16x32_bf16 v[68:71], a[164:167], v[200:203], v[68:71]\n" \
"v_exp_f32_e64 v129, v129\n" \
"v_mfma_f32_16x16x32_bf16 v[72:75], a[136:139], v[216:219], v[72:75]\n" \
"ds_read_b64_tr_b16 a[190:191], v211 offset:46592\n" \
"ds_read_b64_tr_b16 a[128:129], %[vvb] offset:49152\n" \
"v_mfma_f32_16x16x32_bf16 v[72:75], a[168:171], v[200:203], v[72:75]\n" \
"v_exp_f32_e64 v130, v130\n" \
"v_mfma_f32_16x16x32_bf16 v[76:79], a[140:143], v[216:219], v[76:79]\n" \
"ds_read_b64_tr_b16 a[130:131], %[vvb] offset:53248\n" \
"ds_read_b64_tr_b16 a[132:133], v211 offset:49152\n" \
"v_mfma_f32_16x16x32_bf16 v[76:79], a[172:175], v[200:203], v[76:79]\n" \
"v_exp_f32_e64 v131, v131\n" \
"v_smfmac_f32_16x16x64_bf16 v[176:179], a[192:195], v[192:199], v228\n" \
"ds_read_b64_tr_b16 a[134:135], v211 offset:53248\n" \
"ds_read_b64_tr_b16 a[136:137], %[vvb] offset:49664\n" \
"v_mfma_f32_16x16x32_bf16 v[136:139], a[64:67], a[16:19], v[164:167]\n" \
"v_exp_f32_e64 v132, v132\n" \
"v_mfma_f32_16x16x32_bf16 v[136:139], a[68:71], a[20:23], v[136:139]\n" \
"ds_read_b64_tr_b16 a[138:139], %[vvb] offset:53760\n" \
"ds_read_b64_tr_b16 a[140:141], v211 offset:49664\n" \
"v_mfma_f32_16x16x32_bf16 v[136:139], a[72:75], a[24:27], v[136:139]\n" \
"v_exp_f32_e64 v133, v133\n" \
"v_mfma_f32_16x16x32_bf16 v[136:139], a[76:79], a[28:31], v[136:139]\n" \
"v_cvt_pk_bf16_f32 v184, v128, v129\n" \
"v_cvt_pk_bf16_f32 v185, v130, v131\n" \
"v_mfma_f32_16x16x32_bf16 v[140:143], a[92:95], a[28:31], v[164:167]\n" \
"v_exp_f32_e64 v134, v134\n" \
"v_mfma_f32_16x16x32_bf16 v[140:143], a[88:91], a[24:27], v[140:143]\n" \
"ds_read_b64_tr_b16 a[142:143], v211 offset:53760\n" \
"s_waitcnt lgkmcnt(8)\n" \
"v_mfma_f32_16x16x32_bf16 v[140:143], a[84:87], a[20:23], v[140:143]\n" \
"buffer_load_dwordx4 v212, %[srdk], s48 offen offset:1024 lds\n" \
"v_mfma_f32_16x16x32_bf16 v[140:143], a[80:83], a[16:19], v[140:143]\n" \
"v_exp_f32_e32 v135, v135\n" \
"v_mfma_f32_16x16x32_bf16 v[80:83], a[144:147], v[216:219], v[80:83]\n" \
"ds_read_b64_tr_b16 a[144:145], %[vvb] offset:50176\n" \
"ds_read_b64_tr_b16 a[146:147], %[vvb] offset:54272\n" \
"v_mfma_f32_16x16x32_bf16 v[80:83], a[176:179], v[200:203], v[80:83]\n" \
"v_exp_f32_e64 v136, v136\n" \
"v_mfma_f32_16x16x32_bf16 v[84:87], a[148:151], v[216:219], v[84:87]\n" \
"v_cvt_pk_bf16_f32 v186, v132, v133\n" \
"v_cvt_pk_bf16_f32 v187, v134, v135\n" \
"v_mfma_f32_16x16x32_bf16 v[84:87], a[180:183], v[200:203], v[84:87]\n" \
"v_exp_f32_e64 v137, v137\n" \
"v_mfma_f32_16x16x32_bf16 v[88:91], a[152:155], v[216:219], v[88:91]\n" \
"v_exp_f32_e64 v138, v138\n" \
"v_mfma_f32_16x16x32_bf16 v[88:91], a[184:187], v[200:203], v[88:91]\n" \
"ds_read_b64_tr_b16 a[148:149], v211 offset:50176\n" \
"ds_read_b64_tr_b16 a[150:151], v211 offset:54272\n" \
"v_mfma_f32_16x16x32_bf16 v[92:95], a[156:159], v[216:219], v[92:95]\n" \
"v_exp_f32_e64 v139, v139\n" \
"v_mfma_f32_16x16x32_bf16 v[92:95], a[188:191], v[200:203], v[92:95]\n" \
"ds_read_b64_tr_b16 a[152:153], %[vvb] offset:50688\n" \
"global_load_dword v237, %[vct], %[pct]\n" \
"v_mfma_f32_16x16x32_bf16 v[144:147], a[64:67], a[32:35], v[168:171]\n" \
"v_exp_f32_e64 v140, v140\n" \
"v_mfma_f32_16x16x32_bf16 v[144:147], a[68:71], a[36:39], v[144:147]\n" \
"ds_read_b64_tr_b16 a[154:155], %[vvb] offset:54784\n" \
"ds_read_b64_tr_b16 a[156:157], v211 offset:50688\n" \
"v_mfma_f32_16x16x32_bf16 v[144:147], a[72:75], a[40:43], v[144:147]\n" \
"v_exp_f32_e64 v141, v141\n" \
"v_mfma_f32_16x16x32_bf16 v[144:147], a[76:79], a[44:47], v[144:147]\n" \
"v_cvt_pk_bf16_f32 v188, v136, v137\n" \
"v_cvt_pk_bf16_f32 v189, v138, v139\n" \
"v_mfma_f32_16x16x32_bf16 v[148:151], a[92:95], a[44:47], v[168:171]\n" \
"v_exp_f32_e64 v142, v142\n" \
"v_mfma_f32_16x16x32_bf16 v[148:151], a[88:91], a[40:43], v[148:151]\n" \
"ds_read_b64_tr_b16 a[158:159], v211 offset:54784\n" \
"s_waitcnt lgkmcnt(8)\n" \
"v_mfma_f32_16x16x32_bf16 v[148:151], a[84:87], a[36:39], v[148:151]\n" \
"buffer_load_dwordx4 v213, %[srdk], s48 offen offset:2048 lds\n" \
"v_mfma_f32_16x16x32_bf16 v[148:151], a[80:83], a[32:35], v[148:151]\n" \
"v_exp_f32_e32 v143, v143\n" \
"v_mfma_f32_16x16x32_bf16 v[0:3], a[160:163], v[192:195], v[0:3]\n" \
"ds_read_b128 a[96:99], %[kb0] offset:57344\n" \
"v_mfma_f32_16x16x32_bf16 v[0:3], a[128:131], v[184:187], v[0:3]\n" \
"v_exp_f32_e64 v144, v144\n" \
"v_mfma_f32_16x16x32_bf16 v[4:7], a[164:167], v[192:195], v[4:7]\n" \
"v_cvt_pk_bf16_f32 v190, v140, v141\n" \
"v_cvt_pk_bf16_f32 v191, v142, v143\n" \
"v_mfma_f32_16x16x32_bf16 v[4:7], a[132:135], v[184:187], v[4:7]\n" \
"v_exp_f32_e64 v145, v145\n" \
"v_mfma_f32_16x16x32_bf16 v[8:11], a[168:171], v[192:195], v[8:11]\n" \
"ds_read_b128 a[100:103], v208 offset:57344\n" \
"v_mfma_f32_16x16x32_bf16 v[8:11], a[136:139], v[184:187], v[8:11]\n" \
"v_exp_f32_e64 v146, v146\n" \
"v_mfma_f32_16x16x32_bf16 v[12:15], a[172:175], v[192:195], v[12:15]\n" \
"ds_read_b128 a[104:107], v209 offset:57344\n" \
"v_mfma_f32_16x16x32_bf16 v[12:15], a[140:143], v[184:187], v[12:15]\n" \
"v_exp_f32_e64 v147, v147\n" \
"v_smfmac_f32_16x16x64_bf16 v[180:183], a[192:195], v[200:207], v228\n" \
"ds_read_b128 a[108:111], v210 offset:57344\n" \
"v_mfma_f32_16x16x32_bf16 v[152:155], a[64:67], a[48:51], v[172:175]\n" \
"v_exp_f32_e64 v148, v148\n" \
"v_mfma_f32_16x16x32_bf16 v[152:155], a[68:71], a[52:55], v[152:155]\n" \
"ds_read_b128 a[124:127], v210 offset:61440\n" \
"v_mfma_f32_16x16x32_bf16 v[152:155], a[72:75], a[56:59], v[152:155]\n" \
"v_exp_f32_e64 v149, v149\n" \
"v_mfma_f32_16x16x32_bf16 v[152:155], a[76:79], a[60:63], v[152:155]\n" \
"v_cvt_pk_bf16_f32 v216, v144, v145\n" \
"v_cvt_pk_bf16_f32 v217, v146, v147\n" \
"v_mfma_f32_16x16x32_bf16 v[156:159], a[92:95], a[60:63], v[172:175]\n" \
"v_exp_f32_e64 v150, v150\n" \
"v_mfma_f32_16x16x32_bf16 v[156:159], a[88:91], a[56:59], v[156:159]\n" \
"ds_read_b128 a[120:123], v209 offset:61440\n" \
"s_waitcnt lgkmcnt(6)\n" \
"v_mfma_f32_16x16x32_bf16 v[156:159], a[84:87], a[52:55], v[156:159]\n" \
"buffer_load_dwordx4 v214, %[srdk], s48 offen offset:3072 lds\n" \
"v_mfma_f32_16x16x32_bf16 v[156:159], a[80:83], a[48:51], v[156:159]\n" \
"v_exp_f32_e32 v151, v151\n" \
"v_mfma_f32_16x16x32_bf16 v[16:19], a[176:179], v[192:195], v[16:19]\n" \
"ds_read_b128 a[116:119], v208 offset:61440\n" \
"v_mfma_f32_16x16x32_bf16 v[16:19], a[144:147], v[184:187], v[16:19]\n" \
"v_exp_f32_e64 v152, v152\n" \
"v_mfma_f32_16x16x32_bf16 v[20:23], a[180:183], v[192:195], v[20:23]\n" \
"v_cvt_pk_bf16_f32 v218, v148, v149\n" \
"v_cvt_pk_bf16_f32 v219, v150, v151\n" \
"v_mfma_f32_16x16x32_bf16 v[20:23], a[148:151], v[184:187], v[20:23]\n" \
"v_exp_f32_e64 v153, v153\n" \
"v_mfma_f32_16x16x32_bf16 v[24:27], a[184:187], v[192:195], v[24:27]\n" \
"v_exp_f32_e64 v154, v154\n" \
"v_mfma_f32_16x16x32_bf16 v[24:27], a[152:155], v[184:187], v[24:27]\n" \
"ds_read_b128 a[112:115], %[kb0] offset:61440\n" \
"s_add_u32 s48, s48, 0x4000\n" \
"s_mov_b32 m0, s44\n" \
"s_addk_i32 s44, 0x400\n" \
"buffer_load_dwordx4 v229, s[64:67], s45 offen offset:0 nt lds\n" \
"s_add_u32 s45, s45, 393216\n" \
"v_mfma_f32_16x16x32_bf16 v[28:31], a[188:191], v[192:195], v[28:31]\n" \
"v_exp_f32_e64 v155, v155\n" \
"v_mfma_f32_16x16x32_bf16 v[28:31], a[156:159], v[184:187], v[28:31]\n" \
"s_waitcnt vmcnt(15) lgkmcnt(2)\n" \
"s_barrier\n" \
"v_mfma_f32_16x16x32_bf16 v[128:131], a[96:99], a[0:3], v[160:163]\n" \
"v_exp_f32_e64 v156, v156\n" \
"v_mfma_f32_16x16x32_bf16 v[128:131], a[100:103], a[4:7], v[128:131]\n" \
"s_add_u32 m0, %[m0b], 98304\n" \
"v_mfma_f32_16x16x32_bf16 v[128:131], a[104:107], a[8:11], v[128:131]\n" \
"v_exp_f32_e64 v157, v157\n" \
"v_mfma_f32_16x16x32_bf16 v[128:131], a[108:111], a[12:15], v[128:131]\n" \
"v_cvt_pk_bf16_f32 v220, v152, v153\n" \
"v_cvt_pk_bf16_f32 v221, v154, v155\n" \
"v_mfma_f32_16x16x32_bf16 v[132:135], a[124:127], a[12:15], v[160:163]\n" \
"v_exp_f32_e64 v158, v158\n" \
"v_mfma_f32_16x16x32_bf16 v[132:135], a[120:123], a[8:11], v[132:135]\n" \
"s_waitcnt lgkmcnt(0)\n" \
"v_mfma_f32_16x16x32_bf16 v[132:135], a[116:119], a[4:7], v[132:135]\n" \
"buffer_load_dwordx4 %[vd0], %[srdv], s48 offen offset:0 lds\n" \
"v_mfma_f32_16x16x32_bf16 v[132:135], a[112:115], a[0:3], v[132:135]\n" \
"v_exp_f32_e32 v159, v159\n" \
"v_mfma_f32_16x16x32_bf16 v[32:35], a[160:163], v[196:199], v[32:35]\n" \
"ds_read_b128 a[64:67], %[kb0] offset:0\n" \
"v_mfma_f32_16x16x32_bf16 v[32:35], a[128:131], v[188:191], v[32:35]\n" \
"v_exp_f32_e64 v128, v128\n" \
"v_mfma_f32_16x16x32_bf16 v[36:39], a[164:167], v[196:199], v[36:39]\n" \
"v_cvt_pk_bf16_f32 v222, v156, v157\n" \
"v_cvt_pk_bf16_f32 v223, v158, v159\n" \
"v_mfma_f32_16x16x32_bf16 v[36:39], a[132:135], v[188:191], v[36:39]\n" \
"v_exp_f32_e64 v129, v129\n" \
"v_mfma_f32_16x16x32_bf16 v[40:43], a[168:171], v[196:199], v[40:43]\n" \
"ds_read_b128 a[68:71], v208 offset:0\n" \
"v_mfma_f32_16x16x32_bf16 v[40:43], a[136:139], v[188:191], v[40:43]\n" \
"v_exp_f32_e64 v130, v130\n" \
"v_mfma_f32_16x16x32_bf16 v[44:47], a[172:175], v[196:199], v[44:47]\n" \
"ds_read_b128 a[72:75], v209 offset:0\n" \
"v_mfma_f32_16x16x32_bf16 v[44:47], a[140:143], v[188:191], v[44:47]\n" \
"v_exp_f32_e64 v131, v131\n" \
"v_smfmac_f32_16x16x64_bf16 v[176:179], a[192:195], v[184:191], v228\n" \
"v_mfma_f32_16x16x32_bf16 v[136:139], a[96:99], a[16:19], v[164:167]\n" \
"v_exp_f32_e64 v132, v132\n" \
"v_mfma_f32_16x16x32_bf16 v[136:139], a[100:103], a[20:23], v[136:139]\n" \
"ds_read_b128 a[76:79], v210 offset:0\n" \
"v_mfma_f32_16x16x32_bf16 v[136:139], a[104:107], a[24:27], v[136:139]\n" \
"v_exp_f32_e64 v133, v133\n" \
"v_mfma_f32_16x16x32_bf16 v[136:139], a[108:111], a[28:31], v[136:139]\n" \
"v_cvt_pk_bf16_f32 v192, v128, v129\n" \
"v_cvt_pk_bf16_f32 v193, v130, v131\n" \
"v_mfma_f32_16x16x32_bf16 v[140:143], a[124:127], a[28:31], v[164:167]\n" \
"v_exp_f32_e64 v134, v134\n" \
"v_mfma_f32_16x16x32_bf16 v[140:143], a[120:123], a[24:27], v[140:143]\n" \
"ds_read_b128 a[92:95], v210 offset:4096\n" \
"v_mfma_f32_16x16x32_bf16 v[140:143], a[116:119], a[20:23], v[140:143]\n" \
"buffer_load_dwordx4 v215, %[srdv], s48 offen offset:1024 lds\n" \
"v_mfma_f32_16x16x32_bf16 v[140:143], a[112:115], a[16:19], v[140:143]\n" \
"v_exp_f32_e64 v135, v135\n" \
"v_mfma_f32_16x16x32_bf16 v[48:51], a[176:179], v[196:199], v[48:51]\n" \
"ds_read_b128 a[88:91], v209 offset:4096\n" \
"v_mfma_f32_16x16x32_bf16 v[48:51], a[144:147], v[188:191], v[48:51]\n" \
"v_exp_f32_e64 v136, v136\n" \
"v_mfma_f32_16x16x32_bf16 v[52:55], a[180:183], v[196:199], v[52:55]\n" \
"v_cvt_pk_bf16_f32 v194, v132, v133\n" \
"v_cvt_pk_bf16_f32 v195, v134, v135\n" \
"v_mfma_f32_16x16x32_bf16 v[52:55], a[148:151], v[188:191], v[52:55]\n" \
"v_exp_f32_e64 v137, v137\n" \
"v_mfma_f32_16x16x32_bf16 v[56:59], a[184:187], v[196:199], v[56:59]\n" \
"v_exp_f32_e64 v138, v138\n" \
"v_mfma_f32_16x16x32_bf16 v[56:59], a[152:155], v[188:191], v[56:59]\n" \
"ds_read_b128 a[84:87], v208 offset:4096\n" \
"v_mfma_f32_16x16x32_bf16 v[60:63], a[188:191], v[196:199], v[60:63]\n" \
"v_exp_f32_e64 v139, v139\n" \
"v_mfma_f32_16x16x32_bf16 v[60:63], a[156:159], v[188:191], v[60:63]\n" \
"s_cmp_lg_u32 %[wid], 2\n" \
"s_cbranch_scc1 LtA7_%=\n" \
"global_load_dword v237, v224, s[58:59]\n" \
"s_add_u32 s61, s48, 49152\n" \
"s_mov_b32 exec_hi, 0\n" \
"s_nop 0\n" \
"buffer_load_dword v237, v224, %[srdp], s61 offen\n" \
"s_mov_b32 exec_hi, -1\n" \
"s_nop 0\n" \
"LtA7_%=:\n" \
"v_mfma_f32_16x16x32_bf16 v[144:147], a[96:99], a[32:35], v[168:171]\n" \
"v_exp_f32_e64 v140, v140\n" \
"v_mfma_f32_16x16x32_bf16 v[144:147], a[100:103], a[36:39], v[144:147]\n" \
"ds_read_b128 a[80:83], %[kb0] offset:4096\n" \
"v_mfma_f32_16x16x32_bf16 v[144:147], a[104:107], a[40:43], v[144:147]\n" \
"v_exp_f32_e64 v141, v141\n" \
"v_mfma_f32_16x16x32_bf16 v[144:147], a[108:111], a[44:47], v[144:147]\n" \
"v_cvt_pk_bf16_f32 v196, v136, v137\n" \
"v_cvt_pk_bf16_f32 v197, v138, v139\n" \
"v_mfma_f32_16x16x32_bf16 v[148:151], a[124:127], a[44:47], v[168:171]\n" \
"v_exp_f32_e64 v142, v142\n" \
"v_mfma_f32_16x16x32_bf16 v[148:151], a[120:123], a[40:43], v[148:151]\n" \
"v_mfma_f32_16x16x32_bf16 v[148:151], a[116:119], a[36:39], v[148:151]\n" \
"buffer_load_dwordx4 %[vd0], %[srdv], s48 offen offset:2048 lds\n" \
"v_mfma_f32_16x16x32_bf16 v[148:151], a[112:115], a[32:35], v[148:151]\n" \
"v_exp_f32_e64 v143, v143\n" \
"v_mfma_f32_16x16x32_bf16 v[96:99], a[160:163], v[204:207], v[96:99]\n" \
"ds_read_b64_tr_b16 a[160:161], %[vvb] offset:57344\n" \
"v_mfma_f32_16x16x32_bf16 v[96:99], a[128:131], v[220:223], v[96:99]\n" \
"v_exp_f32_e64 v144, v144\n" \
"v_mfma_f32_16x16x32_bf16 v[100:103], a[164:167], v[204:207], v[100:103]\n" \
"v_cvt_pk_bf16_f32 v198, v140, v141\n" \
"v_cvt_pk_bf16_f32 v199, v142, v143\n" \
"v_mfma_f32_16x16x32_bf16 v[100:103], a[132:135], v[220:223], v[100:103]\n" \
"v_exp_f32_e64 v145, v145\n" \
"v_mfma_f32_16x16x32_bf16 v[104:107], a[168:171], v[204:207], v[104:107]\n" \
"ds_read_b64_tr_b16 a[162:163], %[vvb] offset:61440\n" \
"ds_read_b64_tr_b16 a[164:165], v211 offset:57344\n" \
"v_mfma_f32_16x16x32_bf16 v[104:107], a[136:139], v[220:223], v[104:107]\n" \
"v_exp_f32_e64 v146, v146\n" \
"v_mfma_f32_16x16x32_bf16 v[108:111], a[172:175], v[204:207], v[108:111]\n" \
"ds_read_b64_tr_b16 a[166:167], v211 offset:61440\n" \
"v_mfma_f32_16x16x32_bf16 v[108:111], a[140:143], v[220:223], v[108:111]\n" \
"v_exp_f32_e64 v147, v147\n" \
"v_smfmac_f32_16x16x64_bf16 v[180:183], a[192:195], v[216:223], v228\n" \
"ds_read_b64_tr_b16 a[168:169], %[vvb] offset:57856\n" \
"v_mfma_f32_16x16x32_bf16 v[152:155], a[96:99], a[48:51], v[172:175]\n" \
"v_exp_f32_e64 v148, v148\n" \
"v_mfma_f32_16x16x32_bf16 v[152:155], a[100:103], a[52:55], v[152:155]\n" \
"ds_read_b64_tr_b16 a[170:171], %[vvb] offset:61952\n" \
"v_mfma_f32_16x16x32_bf16 v[152:155], a[104:107], a[56:59], v[152:155]\n" \
"v_exp_f32_e64 v149, v149\n" \
"v_mfma_f32_16x16x32_bf16 v[152:155], a[108:111], a[60:63], v[152:155]\n" \
"v_cvt_pk_bf16_f32 v200, v144, v145\n" \
"v_cvt_pk_bf16_f32 v201, v146, v147\n" \
"v_mfma_f32_16x16x32_bf16 v[156:159], a[124:127], a[60:63], v[172:175]\n" \
"v_exp_f32_e64 v150, v150\n" \
"v_mfma_f32_16x16x32_bf16 v[156:159], a[120:123], a[56:59], v[156:159]\n" \
"ds_read_b64_tr_b16 a[172:173], v211 offset:57856\n" \
"v_mfma_f32_16x16x32_bf16 v[156:159], a[116:119], a[52:55], v[156:159]\n" \
"buffer_load_dwordx4 v215, %[srdv], s48 offen offset:3072 lds\n" \
"v_mfma_f32_16x16x32_bf16 v[156:159], a[112:115], a[48:51], v[156:159]\n" \
"v_exp_f32_e64 v151, v151\n" \
"v_mfma_f32_16x16x32_bf16 v[112:115], a[176:179], v[204:207], v[112:115]\n" \
"ds_read_b64_tr_b16 a[174:175], v211 offset:61952\n" \
"ds_read_b64_tr_b16 a[176:177], %[vvb] offset:58368\n" \
"v_mfma_f32_16x16x32_bf16 v[112:115], a[144:147], v[220:223], v[112:115]\n" \
"v_exp_f32_e64 v152, v152\n" \
"v_mfma_f32_16x16x32_bf16 v[116:119], a[180:183], v[204:207], v[116:119]\n" \
"v_cvt_pk_bf16_f32 v202, v148, v149\n" \
"v_cvt_pk_bf16_f32 v203, v150, v151\n" \
"v_mfma_f32_16x16x32_bf16 v[116:119], a[148:151], v[220:223], v[116:119]\n" \
"v_exp_f32_e64 v153, v153\n" \
"v_mfma_f32_16x16x32_bf16 v[120:123], a[184:187], v[204:207], v[120:123]\n" \
"v_exp_f32_e64 v154, v154\n" \
"v_mfma_f32_16x16x32_bf16 v[120:123], a[152:155], v[220:223], v[120:123]\n" \
"ds_read_b64_tr_b16 a[178:179], %[vvb] offset:62464\n" \
"v_mfma_f32_16x16x32_bf16 v[124:127], a[188:191], v[204:207], v[124:127]\n" \
"v_exp_f32_e64 v155, v155\n" \
"v_mfma_f32_16x16x32_bf16 v[124:127], a[156:159], v[220:223], v[124:127]\n" \
"ds_read_b64_tr_b16 a[180:181], v211 offset:58368\n" \
"s_waitcnt lgkmcnt(13)\n" \
"v_mfma_f32_16x16x32_bf16 v[128:131], a[64:67], a[0:3], v[160:163]\n" \
"v_exp_f32_e32 v156, v156\n" \
"v_mfma_f32_16x16x32_bf16 v[128:131], a[68:71], a[4:7], v[128:131]\n" \
"ds_read_b64_tr_b16 a[182:183], v211 offset:62464\n" \
"s_add_u32 m0, %[m0b], 49152\n" \
"v_mfma_f32_16x16x32_bf16 v[128:131], a[72:75], a[8:11], v[128:131]\n" \
"v_exp_f32_e64 v157, v157\n" \
"v_mfma_f32_16x16x32_bf16 v[128:131], a[76:79], a[12:15], v[128:131]\n" \
"v_cvt_pk_bf16_f32 v204, v152, v153\n" \
"v_cvt_pk_bf16_f32 v205, v154, v155\n" \
"v_mfma_f32_16x16x32_bf16 v[132:135], a[92:95], a[12:15], v[160:163]\n" \
"v_exp_f32_e64 v158, v158\n" \
"v_mfma_f32_16x16x32_bf16 v[132:135], a[88:91], a[8:11], v[132:135]\n" \
"ds_read_b64_tr_b16 a[184:185], %[vvb] offset:58880\n" \
"s_waitcnt lgkmcnt(5)\n" \
"v_mfma_f32_16x16x32_bf16 v[132:135], a[84:87], a[4:7], v[132:135]\n" \
"buffer_load_dwordx4 %[kd0], %[srdk], s48 offen offset:0 lds\n" \
"v_mfma_f32_16x16x32_bf16 v[132:135], a[80:83], a[0:3], v[132:135]\n" \
"v_exp_f32_e32 v159, v159\n" \
"v_mfma_f32_16x16x32_bf16 v[64:67], a[128:131], v[216:219], v[64:67]\n" \
"ds_read_b64_tr_b16 a[186:187], %[vvb] offset:62976\n" \
"ds_read_b64_tr_b16 a[188:189], v211 offset:58880\n" \
"v_mfma_f32_16x16x32_bf16 v[64:67], a[160:163], v[200:203], v[64:67]\n" \
"v_exp_f32_e64 v128, v128\n" \
"v_mfma_f32_16x16x32_bf16 v[68:71], a[132:135], v[216:219], v[68:71]\n" \
"v_cvt_pk_bf16_f32 v206, v156, v157\n" \
"v_cvt_pk_bf16_f32 v207, v158, v159\n" \
"v_mfma_f32_16x16x32_bf16 v[68:71], a[164:167], v[200:203], v[68:71]\n" \
"v_exp_f32_e64 v129, v129\n" \
"v_mfma_f32_16x16x32_bf16 v[72:75], a[136:139], v[216:219], v[72:75]\n" \
"ds_read_b64_tr_b16 a[190:191], v211 offset:62976\n" \
"ds_read_b64_tr_b16 a[128:129], %[vvb] offset:0\n" \
"v_mfma_f32_16x16x32_bf16 v[72:75], a[168:171], v[200:203], v[72:75]\n" \
"v_exp_f32_e64 v130, v130\n" \
"v_mfma_f32_16x16x32_bf16 v[76:79], a[140:143], v[216:219], v[76:79]\n" \
"ds_read_b64_tr_b16 a[130:131], %[vvb] offset:4096\n" \
"ds_read_b64_tr_b16 a[132:133], v211 offset:0\n" \
"v_mfma_f32_16x16x32_bf16 v[76:79], a[172:175], v[200:203], v[76:79]\n" \
"v_exp_f32_e64 v131, v131\n" \
"v_smfmac_f32_16x16x64_bf16 v[176:179], a[192:195], v[192:199], v228\n" \
"ds_read_b64_tr_b16 a[134:135], v211 offset:4096\n" \
"ds_read_b64_tr_b16 a[136:137], %[vvb] offset:512\n" \
"v_mfma_f32_16x16x32_bf16 v[136:139], a[64:67], a[16:19], v[164:167]\n" \
"v_exp_f32_e64 v132, v132\n" \
"v_mfma_f32_16x16x32_bf16 v[136:139], a[68:71], a[20:23], v[136:139]\n" \
"ds_read_b64_tr_b16 a[138:139], %[vvb] offset:4608\n" \
"ds_read_b64_tr_b16 a[140:141], v211 offset:512\n" \
"v_mfma_f32_16x16x32_bf16 v[136:139], a[72:75], a[24:27], v[136:139]\n" \
"v_exp_f32_e64 v133, v133\n" \
"v_mfma_f32_16x16x32_bf16 v[136:139], a[76:79], a[28:31], v[136:139]\n" \
"v_cvt_pk_bf16_f32 v184, v128, v129\n" \
"v_cvt_pk_bf16_f32 v185, v130, v131\n" \
"v_mfma_f32_16x16x32_bf16 v[140:143], a[92:95], a[28:31], v[164:167]\n" \
"v_exp_f32_e64 v134, v134\n" \
"v_mfma_f32_16x16x32_bf16 v[140:143], a[88:91], a[24:27], v[140:143]\n" \
"ds_read_b64_tr_b16 a[142:143], v211 offset:4608\n" \
"s_waitcnt lgkmcnt(8)\n" \
"v_mfma_f32_16x16x32_bf16 v[140:143], a[84:87], a[20:23], v[140:143]\n" \
"buffer_load_dwordx4 v212, %[srdk], s48 offen offset:1024 lds\n" \
"v_mfma_f32_16x16x32_bf16 v[140:143], a[80:83], a[16:19], v[140:143]\n" \
"v_exp_f32_e32 v135, v135\n" \
"v_mfma_f32_16x16x32_bf16 v[80:83], a[144:147], v[216:219], v[80:83]\n" \
"ds_read_b64_tr_b16 a[144:145], %[vvb] offset:1024\n" \
"ds_read_b64_tr_b16 a[146:147], %[vvb] offset:5120\n" \
"v_mfma_f32_16x16x32_bf16 v[80:83], a[176:179], v[200:203], v[80:83]\n" \
"v_exp_f32_e64 v136, v136\n" \
"v_mfma_f32_16x16x32_bf16 v[84:87], a[148:151], v[216:219], v[84:87]\n" \
"v_cvt_pk_bf16_f32 v186, v132, v133\n" \
"v_cvt_pk_bf16_f32 v187, v134, v135\n" \
"v_mfma_f32_16x16x32_bf16 v[84:87], a[180:183], v[200:203], v[84:87]\n" \
"v_exp_f32_e64 v137, v137\n" \
"v_mfma_f32_16x16x32_bf16 v[88:91], a[152:155], v[216:219], v[88:91]\n" \
"v_exp_f32_e64 v138, v138\n" \
"v_mfma_f32_16x16x32_bf16 v[88:91], a[184:187], v[200:203], v[88:91]\n" \
"ds_read_b64_tr_b16 a[148:149], v211 offset:1024\n" \
"ds_read_b64_tr_b16 a[150:151], v211 offset:5120\n" \
"v_mfma_f32_16x16x32_bf16 v[92:95], a[156:159], v[216:219], v[92:95]\n" \
"v_exp_f32_e64 v139, v139\n" \
"v_mfma_f32_16x16x32_bf16 v[92:95], a[188:191], v[200:203], v[92:95]\n" \
"ds_read_b64_tr_b16 a[152:153], %[vvb] offset:1536\n" \
"buffer_store_dwordx4 a[196:199], v235, %[srdz], s62 offen offset:1024 sc1\n" \
"s_add_u32 s62, s62, 2048\n" \
"v_mfma_f32_16x16x32_bf16 v[144:147], a[64:67], a[32:35], v[168:171]\n" \
"v_exp_f32_e64 v140, v140\n" \
"v_mfma_f32_16x16x32_bf16 v[144:147], a[68:71], a[36:39], v[144:147]\n" \
"ds_read_b64_tr_b16 a[154:155], %[vvb] offset:5632\n" \
"ds_read_b64_tr_b16 a[156:157], v211 offset:1536\n" \
"v_mfma_f32_16x16x32_bf16 v[144:147], a[72:75], a[40:43], v[144:147]\n" \
"v_exp_f32_e64 v141, v141\n" \
"v_mfma_f32_16x16x32_bf16 v[144:147], a[76:79], a[44:47], v[144:147]\n" \
"v_cvt_pk_bf16_f32 v188, v136, v137\n" \
"v_cvt_pk_bf16_f32 v189, v138, v139\n" \
"v_mfma_f32_16x16x32_bf16 v[148:151], a[92:95], a[44:47], v[168:171]\n" \
"v_exp_f32_e64 v142, v142\n" \
"v_mfma_f32_16x16x32_bf16 v[148:151], a[88:91], a[40:43], v[148:151]\n" \
"ds_read_b64_tr_b16 a[158:159], v211 offset:5632\n" \
"s_waitcnt lgkmcnt(8)\n" \
"v_mfma_f32_16x16x32_bf16 v[148:151], a[84:87], a[36:39], v[148:151]\n" \
"buffer_load_dwordx4 v213, %[srdk], s48 offen offset:2048 lds\n" \
"v_mfma_f32_16x16x32_bf16 v[148:151], a[80:83], a[32:35], v[148:151]\n" \
"v_exp_f32_e32 v143, v143\n" \
"v_mfma_f32_16x16x32_bf16 v[0:3], a[160:163], v[192:195], v[0:3]\n" \
"ds_read_b128 a[96:99], %[kb0] offset:8192\n" \
"v_mfma_f32_16x16x32_bf16 v[0:3], a[128:131], v[184:187], v[0:3]\n" \
"v_exp_f32_e64 v144, v144\n" \
"v_mfma_f32_16x16x32_bf16 v[4:7], a[164:167], v[192:195], v[4:7]\n" \
"v_cvt_pk_bf16_f32 v190, v140, v141\n" \
"v_cvt_pk_bf16_f32 v191, v142, v143\n" \
"v_mfma_f32_16x16x32_bf16 v[4:7], a[132:135], v[184:187], v[4:7]\n" \
"v_exp_f32_e64 v145, v145\n" \
"v_mfma_f32_16x16x32_bf16 v[8:11], a[168:171], v[192:195], v[8:11]\n" \
"ds_read_b128 a[100:103], v208 offset:8192\n" \
"v_mfma_f32_16x16x32_bf16 v[8:11], a[136:139], v[184:187], v[8:11]\n" \
"v_exp_f32_e64 v146, v146\n" \
"v_mfma_f32_16x16x32_bf16 v[12:15], a[172:175], v[192:195], v[12:15]\n" \
"ds_read_b128 a[104:107], v209 offset:8192\n" \
"v_mfma_f32_16x16x32_bf16 v[12:15], a[140:143], v[184:187], v[12:15]\n" \
"v_exp_f32_e64 v147, v147\n" \
"v_smfmac_f32_16x16x64_bf16 v[180:183], a[192:195], v[200:207], v228\n" \
"ds_read_b128 a[108:111], v210 offset:8192\n" \
"v_mfma_f32_16x16x32_bf16 v[152:155], a[64:67], a[48:51], v[172:175]\n" \
"v_exp_f32_e64 v148, v148\n" \
"v_mfma_f32_16x16x32_bf16 v[152:155], a[68:71], a[52:55], v[152:155]\n" \
"ds_read_b128 a[124:127], v210 offset:12288\n" \
"v_mfma_f32_16x16x32_bf16 v[152:155], a[72:75], a[56:59], v[152:155]\n" \
"v_exp_f32_e64 v149, v149\n" \
"v_mfma_f32_16x16x32_bf16 v[152:155], a[76:79], a[60:63], v[152:155]\n" \
"v_cvt_pk_bf16_f32 v216, v144, v145\n" \
"v_cvt_pk_bf16_f32 v217, v146, v147\n" \
"v_mfma_f32_16x16x32_bf16 v[156:159], a[92:95], a[60:63], v[172:175]\n" \
"v_exp_f32_e64 v150, v150\n" \
"v_mfma_f32_16x16x32_bf16 v[156:159], a[88:91], a[56:59], v[156:159]\n" \
"ds_read_b128 a[120:123], v209 offset:12288\n" \
"s_waitcnt lgkmcnt(6)\n" \
"v_mfma_f32_16x16x32_bf16 v[156:159], a[84:87], a[52:55], v[156:159]\n" \
"buffer_load_dwordx4 v214, %[srdk], s48 offen offset:3072 lds\n" \
"v_mfma_f32_16x16x32_bf16 v[156:159], a[80:83], a[48:51], v[156:159]\n" \
"v_exp_f32_e32 v151, v151\n" \
"v_mfma_f32_16x16x32_bf16 v[16:19], a[176:179], v[192:195], v[16:19]\n" \
"ds_read_b128 a[116:119], v208 offset:12288\n" \
"v_mfma_f32_16x16x32_bf16 v[16:19], a[144:147], v[184:187], v[16:19]\n" \
"v_exp_f32_e64 v152, v152\n" \
"v_mfma_f32_16x16x32_bf16 v[20:23], a[180:183], v[192:195], v[20:23]\n" \
"v_cvt_pk_bf16_f32 v218, v148, v149\n" \
"v_cvt_pk_bf16_f32 v219, v150, v151\n" \
"v_mfma_f32_16x16x32_bf16 v[20:23], a[148:151], v[184:187], v[20:23]\n" \
"v_exp_f32_e64 v153, v153\n" \
"v_mfma_f32_16x16x32_bf16 v[24:27], a[184:187], v[192:195], v[24:27]\n" \
"v_exp_f32_e64 v154, v154\n" \
"v_mfma_f32_16x16x32_bf16 v[24:27], a[152:155], v[184:187], v[24:27]\n" \
"ds_read_b128 a[112:115], %[kb0] offset:12288\n" \
"s_add_u32 s48, s48, 0x4000\n" \
"v_mfma_f32_16x16x32_bf16 v[28:31], a[188:191], v[192:195], v[28:31]\n" \
"v_exp_f32_e64 v155, v155\n" \
"v_mfma_f32_16x16x32_bf16 v[28:31], a[156:159], v[184:187], v[28:31]\n" \
"s_waitcnt vmcnt(15) lgkmcnt(2)\n" \
"s_barrier\n" \
"v_mfma_f32_16x16x32_bf16 v[128:131], a[96:99], a[0:3], v[160:163]\n" \
"v_exp_f32_e64 v156, v156\n" \
"v_mfma_f32_16x16x32_bf16 v[128:131], a[100:103], a[4:7], v[128:131]\n" \
"s_add_u32 m0, %[m0b], 114688\n" \
"v_mfma_f32_16x16x32_bf16 v[128:131], a[104:107], a[8:11], v[128:131]\n" \
"v_exp_f32_e64 v157, v157\n" \
"v_mfma_f32_16x16x32_bf16 v[128:131], a[108:111], a[12:15], v[128:131]\n" \
"v_cvt_pk_bf16_f32 v220, v152, v153\n" \
"v_cvt_pk_bf16_f32 v221, v154, v155\n" \
"v_mfma_f32_16x16x32_bf16 v[132:135], a[124:127], a[12:15], v[160:163]\n" \
"v_exp_f32_e64 v158, v158\n" \
"v_mfma_f32_16x16x32_bf16 v[132:135], a[120:123], a[8:11], v[132:135]\n" \
"s_waitcnt lgkmcnt(0)\n" \
"v_mfma_f32_16x16x32_bf16 v[132:135], a[116:119], a[4:7], v[132:135]\n" \
"buffer_load_dwordx4 %[vd0], %[srdv], s48 offen offset:0 lds\n" \
"v_mfma_f32_16x16x32_bf16 v[132:135], a[112:115], a[0:3], v[132:135]\n" \
"v_exp_f32_e32 v159, v159\n" \
"v_mfma_f32_16x16x32_bf16 v[32:35], a[160:163], v[196:199], v[32:35]\n" \
"ds_read_b128 a[64:67], %[kb0] offset:16384\n" \
"v_mfma_f32_16x16x32_bf16 v[32:35], a[128:131], v[188:191], v[32:35]\n" \
"v_exp_f32_e64 v128, v128\n" \
"v_mfma_f32_16x16x32_bf16 v[36:39], a[164:167], v[196:199], v[36:39]\n" \
"v_cvt_pk_bf16_f32 v222, v156, v157\n" \
"v_cvt_pk_bf16_f32 v223, v158, v159\n" \
"v_mfma_f32_16x16x32_bf16 v[36:39], a[132:135], v[188:191], v[36:39]\n" \
"v_exp_f32_e64 v129, v129\n" \
"v_mfma_f32_16x16x32_bf16 v[40:43], a[168:171], v[196:199], v[40:43]\n" \
"ds_read_b128 a[68:71], v208 offset:16384\n" \
"v_mfma_f32_16x16x32_bf16 v[40:43], a[136:139], v[188:191], v[40:43]\n" \
"v_exp_f32_e64 v130, v130\n" \
"v_mfma_f32_16x16x32_bf16 v[44:47], a[172:175], v[196:199], v[44:47]\n" \
"ds_read_b128 a[72:75], v209 offset:16384\n" \
"v_mfma_f32_16x16x32_bf16 v[44:47], a[140:143], v[188:191], v[44:47]\n" \
"v_exp_f32_e64 v131, v131\n" \
"v_smfmac_f32_16x16x64_bf16 v[176:179], a[192:195], v[184:191], v228\n" \
"v_mfma_f32_16x16x32_bf16 v[136:139], a[96:99], a[16:19], v[164:167]\n" \
"v_exp_f32_e64 v132, v132\n" \
"v_mfma_f32_16x16x32_bf16 v[136:139], a[100:103], a[20:23], v[136:139]\n" \
"ds_read_b128 a[76:79], v210 offset:16384\n" \
"v_mfma_f32_16x16x32_bf16 v[136:139], a[104:107], a[24:27], v[136:139]\n" \
"v_exp_f32_e64 v133, v133\n" \
"v_mfma_f32_16x16x32_bf16 v[136:139], a[108:111], a[28:31], v[136:139]\n" \
"v_cvt_pk_bf16_f32 v192, v128, v129\n" \
"v_cvt_pk_bf16_f32 v193, v130, v131\n" \
"v_mfma_f32_16x16x32_bf16 v[140:143], a[124:127], a[28:31], v[164:167]\n" \
"v_exp_f32_e64 v134, v134\n" \
"v_mfma_f32_16x16x32_bf16 v[140:143], a[120:123], a[24:27], v[140:143]\n" \
"ds_read_b128 a[92:95], v210 offset:20480\n" \
"v_mfma_f32_16x16x32_bf16 v[140:143], a[116:119], a[20:23], v[140:143]\n" \
"buffer_load_dwordx4 v215, %[srdv], s48 offen offset:1024 lds\n" \
"v_mfma_f32_16x16x32_bf16 v[140:143], a[112:115], a[16:19], v[140:143]\n" \
".if . - adaln_body != 20480\n" \
".error \"size model\"\n" \
".endif\n" \
"Lk4_%=:\n" \
"s_cbranch_execz Lk5_%=\n" \
"s_nop 0\n" \
"v_exp_f32_e64 v135, v135\n" \
"v_mfma_f32_16x16x32_bf16 v[48:51], a[176:179], v[196:199], v[48:51]\n" \
"ds_read_b128 a[88:91], v209 offset:20480\n" \
"v_mfma_f32_16x16x32_bf16 v[48:51], a[144:147], v[188:191], v[48:51]\n" \
"v_exp_f32_e64 v136, v136\n" \
"v_mfma_f32_16x16x32_bf16 v[52:55], a[180:183], v[196:199], v[52:55]\n" \
"v_cvt_pk_bf16_f32 v194, v132, v133\n" \
"v_cvt_pk_bf16_f32 v195, v134, v135\n" \
"v_mfma_f32_16x16x32_bf16 v[52:55], a[148:151], v[188:191], v[52:55]\n" \
"v_exp_f32_e64 v137, v137\n" \
"v_mfma_f32_16x16x32_bf16 v[56:59], a[184:187], v[196:199], v[56:59]\n" \
"v_exp_f32_e64 v138, v138\n" \
"v_mfma_f32_16x16x32_bf16 v[56:59], a[152:155], v[188:191], v[56:59]\n" \
"ds_read_b128 a[84:87], v208 offset:20480\n" \
"v_mfma_f32_16x16x32_bf16 v[60:63], a[188:191], v[196:199], v[60:63]\n" \
"v_exp_f32_e64 v139, v139\n" \
"v_mfma_f32_16x16x32_bf16 v[60:63], a[156:159], v[188:191], v[60:63]\n" \
"s_cmp_lg_u32 %[wid], 3\n" \
"s_cbranch_scc1 LtA1_%=\n" \
"global_load_dword v237, v224, s[58:59]\n" \
"s_add_u32 s61, s48, 49152\n" \
"s_mov_b32 exec_hi, 0\n" \
"s_nop 0\n" \
"buffer_load_dword v237, v224, %[srdp], s61 offen\n" \
"s_mov_b32 exec_hi, -1\n" \
"s_nop 0\n" \
"LtA1_%=:\n" \
"v_mfma_f32_16x16x32_bf16 v[144:147], a[96:99], a[32:35], v[168:171]\n" \
"v_exp_f32_e64 v140, v140\n" \
"v_mfma_f32_16x16x32_bf16 v[144:147], a[100:103], a[36:39], v[144:147]\n" \
"ds_read_b128 a[80:83], %[kb0] offset:20480\n" \
"v_mfma_f32_16x16x32_bf16 v[144:147], a[104:107], a[40:43], v[144:147]\n" \
"v_exp_f32_e64 v141, v141\n" \
"v_mfma_f32_16x16x32_bf16 v[144:147], a[108:111], a[44:47], v[144:147]\n" \
"v_cvt_pk_bf16_f32 v196, v136, v137\n" \
"v_cvt_pk_bf16_f32 v197, v138, v139\n" \
"v_mfma_f32_16x16x32_bf16 v[148:151], a[124:127], a[44:47], v[168:171]\n" \
"v_exp_f32_e64 v142, v142\n" \
"v_mfma_f32_16x16x32_bf16 v[148:151], a[120:123], a[40:43], v[148:151]\n" \
"v_mfma_f32_16x16x32_bf16 v[148:151], a[116:119], a[36:39], v[148:151]\n" \
"buffer_load_dwordx4 %[vd0], %[srdv], s48 offen offset:2048 lds\n" \
"v_mfma_f32_16x16x32_bf16 v[148:151], a[112:115], a[32:35], v[148:151]\n" \
"v_exp_f32_e64 v143, v143\n" \
"v_mfma_f32_16x16x32_bf16 v[96:99], a[160:163], v[204:207], v[96:99]\n" \
"ds_read_b64_tr_b16 a[160:161], %[vvb] offset:8192\n" \
"v_mfma_f32_16x16x32_bf16 v[96:99], a[128:131], v[220:223], v[96:99]\n" \
"v_exp_f32_e64 v144, v144\n" \
"v_mfma_f32_16x16x32_bf16 v[100:103], a[164:167], v[204:207], v[100:103]\n" \
"v_cvt_pk_bf16_f32 v198, v140, v141\n" \
"v_cvt_pk_bf16_f32 v199, v142, v143\n" \
"v_mfma_f32_16x16x32_bf16 v[100:103], a[132:135], v[220:223], v[100:103]\n" \
"v_exp_f32_e64 v145, v145\n" \
"v_mfma_f32_16x16x32_bf16 v[104:107], a[168:171], v[204:207], v[104:107]\n" \
"ds_read_b64_tr_b16 a[162:163], %[vvb] offset:12288\n" \
"ds_read_b64_tr_b16 a[164:165], v211 offset:8192\n" \
"v_mfma_f32_16x16x32_bf16 v[104:107], a[136:139], v[220:223], v[104:107]\n" \
"v_exp_f32_e64 v146, v146\n" \
"v_mfma_f32_16x16x32_bf16 v[108:111], a[172:175], v[204:207], v[108:111]\n" \
"ds_read_b64_tr_b16 a[166:167], v211 offset:12288\n" \
"v_mfma_f32_16x16x32_bf16 v[108:111], a[140:143], v[220:223], v[108:111]\n" \
"v_exp_f32_e64 v147, v147\n" \
"v_smfmac_f32_16x16x64_bf16 v[180:183], a[192:195], v[216:223], v228\n" \
"ds_read_b64_tr_b16 a[168:169], %[vvb] offset:8704\n" \
"v_mfma_f32_16x16x32_bf16 v[152:155], a[96:99], a[48:51], v[172:175]\n" \
"v_exp_f32_e64 v148, v148\n" \
"v_mfma_f32_16x16x32_bf16 v[152:155], a[100:103], a[52:55], v[152:155]\n" \
"ds_read_b64_tr_b16 a[170:171], %[vvb] offset:12800\n" \
"v_mfma_f32_16x16x32_bf16 v[152:155], a[104:107], a[56:59], v[152:155]\n" \
"v_exp_f32_e64 v149, v149\n" \
"v_mfma_f32_16x16x32_bf16 v[152:155], a[108:111], a[60:63], v[152:155]\n" \
"v_cvt_pk_bf16_f32 v200, v144, v145\n" \
"v_cvt_pk_bf16_f32 v201, v146, v147\n" \
"v_mfma_f32_16x16x32_bf16 v[156:159], a[124:127], a[60:63], v[172:175]\n" \
"v_exp_f32_e64 v150, v150\n" \
"v_mfma_f32_16x16x32_bf16 v[156:159], a[120:123], a[56:59], v[156:159]\n" \
"ds_read_b64_tr_b16 a[172:173], v211 offset:8704\n" \
"v_mfma_f32_16x16x32_bf16 v[156:159], a[116:119], a[52:55], v[156:159]\n" \
"buffer_load_dwordx4 v215, %[srdv], s48 offen offset:3072 lds\n" \
"v_mfma_f32_16x16x32_bf16 v[156:159], a[112:115], a[48:51], v[156:159]\n" \
"v_exp_f32_e64 v151, v151\n" \
"v_mfma_f32_16x16x32_bf16 v[112:115], a[176:179], v[204:207], v[112:115]\n" \
"ds_read_b64_tr_b16 a[174:175], v211 offset:12800\n" \
"ds_read_b64_tr_b16 a[176:177], %[vvb] offset:9216\n" \
"v_mfma_f32_16x16x32_bf16 v[112:115], a[144:147], v[220:223], v[112:115]\n" \
"v_exp_f32_e64 v152, v152\n" \
"v_mfma_f32_16x16x32_bf16 v[116:119], a[180:183], v[204:207], v[116:119]\n" \
"v_cvt_pk_bf16_f32 v202, v148, v149\n" \
"v_cvt_pk_bf16_f32 v203, v150, v151\n" \
"v_mfma_f32_16x16x32_bf16 v[116:119], a[148:151], v[220:223], v[116:119]\n" \
"v_exp_f32_e64 v153, v153\n" \
"v_mfma_f32_16x16x32_bf16 v[120:123], a[184:187], v[204:207], v[120:123]\n" \
"v_exp_f32_e64 v154, v154\n" \
"v_mfma_f32_16x16x32_bf16 v[120:123], a[152:155], v[220:223], v[120:123]\n" \
"ds_read_b64_tr_b16 a[178:179], %[vvb] offset:13312\n" \
"v_mfma_f32_16x16x32_bf16 v[124:127], a[188:191], v[204:207], v[124:127]\n" \
"v_exp_f32_e64 v155, v155\n" \
"v_mfma_f32_16x16x32_bf16 v[124:127], a[156:159], v[220:223], v[124:127]\n" \
"ds_read_b64_tr_b16 a[180:181], v211 offset:9216\n" \
"s_waitcnt lgkmcnt(13)\n" \
"s_cmp_lg_u32 s60, 4\n" \
"s_cbranch_scc1 LmA_%=\n" \
"s_nop 0\n" \
"v_readlane_b32 s64, %[tlo], 9\n" \
"v_readlane_b32 s65, %[thi], 9\n" \
"s_mov_b32 s66, %[wbytes]\n" \
"v_mov_b32_e32 v229, %[vg0]\n" \
"s_mov_b32 s44, %[m0t]\n" \
"s_mov_b32 s45, 0\n" \
"s_cmp_lg_u32 s52, 0\n" \
"s_cbranch_scc1 LmB_%=\n" \
"s_add_u32 s62, %[sin], 8192\n" \
"s_add_u32 s63, %[sin], 28672\n" \
"s_waitcnt vmcnt(13)\n" \
"s_nop 0\n" \
"ds_read_b64_tr_b16 a[232:233], %[ta0] offset:0\n" \
"ds_read_b64_tr_b16 a[234:235], %[ta0] offset:128\n" \
"ds_read_b64_tr_b16 a[236:237], %[ta0] offset:1024\n" \
"ds_read_b64_tr_b16 a[238:239], %[ta0] offset:1152\n" \
"ds_read_b64_tr_b16 a[240:241], %[ta0] offset:2048\n" \
"ds_read_b64_tr_b16 a[242:243], %[ta0] offset:2176\n" \
"ds_read_b64_tr_b16 a[244:245], %[ta0] offset:3072\n" \
"ds_read_b64_tr_b16 a[246:247], %[ta0] offset:3200\n" \
"ds_read_b64_tr_b16 a[248:249], %[ta0] offset:4096\n" \
"ds_read_b64_tr_b16 a[250:251], %[ta0] offset:4224\n" \
"ds_read_b64_tr_b16 a[252:253], %[ta0] offset:5120\n" \
"ds_read_b64_tr_b16 a[254:255], %[ta0] offset:5248\n" \
"v_add_u32_e32 v234, %[m0t], v235\n" \
"s_waitcnt lgkmcnt(8)\n" \
"v_mfma_f32_16x16x32_bf16 v[230:233], a[232:235], a[200:203], 0\n" \
"v_mfma_f32_16x16x32_bf16 v[230:233], a[236:239], a[204:207], v[230:233]\n" \
"ds_read_b64_tr_b16 a[232:233], %[ta0] offset:6144\n" \
"ds_read_b64_tr_b16 a[234:235], %[ta0] offset:6272\n" \
"ds_read_b64_tr_b16 a[236:237], %[ta0] offset:7168\n" \
"ds_read_b64_tr_b16 a[238:239], %[ta0] offset:7296\n" \
"s_waitcnt lgkmcnt(4)\n" \
"s_nop 0\n" \
"v_mfma_f32_16x16x32_bf16 v[230:233], a[240:243], a[208:211], v[230:233]\n" \
"v_mfma_f32_16x16x32_bf16 v[230:233], a[244:247], a[212:215], v[230:233]\n" \
"v_mfma_f32_16x16x32_bf16 v[230:233], a[248:251], a[216:219], v[230:233]\n" \
"v_mfma_f32_16x16x32_bf16 v[230:233], a[252:255], a[220:223], v[230:233]\n" \
"s_waitcnt lgkmcnt(0)\n" \
"s_nop 0\n" \
"v_mfma_f32_16x16x32_bf16 v[230:233], a[232:235], a[224:227], v[230:233]\n" \
"v_mfma_f32_16x16x32_bf16 v[230:233], a[236:239], a[228:231], v[230:233]\n" \
"s_nop 7\n" \
"s_nop 0\n" \
"ds_write_b128 v234, v[230:233] offset:7168\n" \
"LmB_%=:\n" \
"v_mfma_f32_16x16x32_bf16 v[128:131], a[64:67], a[0:3], v[160:163]\n" \
"v_exp_f32_e64 v156, v156\n" \
"v_mfma_f32_16x16x32_bf16 v[128:131], a[68:71], a[4:7], v[128:131]\n" \
"ds_read_b64_tr_b16 a[182:183], v211 offset:13312\n" \
"s_add_u32 m0, %[m0b], 0\n" \
"v_mfma_f32_16x16x32_bf16 v[128:131], a[72:75], a[8:11], v[128:131]\n" \
"v_exp_f32_e32 v157, v157\n" \
"v_mfma_f32_16x16x32_bf16 v[128:131], a[76:79], a[12:15], v[128:131]\n" \
"v_cvt_pk_bf16_f32 v204, v152, v153\n" \
"v_cvt_pk_bf16_f32 v205, v154, v155\n" \
"v_mfma_f32_16x16x32_bf16 v[132:135], a[92:95], a[12:15], v[160:163]\n" \
"v_exp_f32_e64 v158, v158\n" \
"v_mfma_f32_16x16x32_bf16 v[132:135], a[88:91], a[8:11], v[132:135]\n" \
"ds_read_b64_tr_b16 a[184:185], %[vvb] offset:9728\n" \
"s_waitcnt lgkmcnt(5)\n" \
"v_mfma_f32_16x16x32_bf16 v[132:135], a[84:87], a[4:7], v[132:135]\n" \
"buffer_load_dwordx4 %[kd0], %[srdk], s48 offen offset:0 lds\n" \
"v_mfma_f32_16x16x32_bf16 v[132:135], a[80:83], a[0:3], v[132:135]\n" \
"v_exp_f32_e32 v159, v159\n" \
"v_mfma_f32_16x16x32_bf16 v[64:67], a[128:131], v[216:219], v[64:67]\n" \
"ds_read_b64_tr_b16 a[186:187], %[vvb] offset:13824\n" \
"ds_read_b64_tr_b16 a[188:189], v211 offset:9728\n" \
"v_mfma_f32_16x16x32_bf16 v[64:67], a[160:163], v[200:203], v[64:67]\n" \
"v_exp_f32_e64 v128, v128\n" \
"v_mfma_f32_16x16x32_bf16 v[68:71], a[132:135], v[216:219], v[68:71]\n" \
"v_cvt_pk_bf16_f32 v206, v156, v157\n" \
"v_cvt_pk_bf16_f32 v207, v158, v159\n" \
"v_mfma_f32_16x16x32_bf16 v[68:71], a[164:167], v[200:203], v[68:71]\n" \
"v_exp_f32_e64 v129, v129\n" \
"v_mfma_f32_16x16x32_bf16 v[72:75], a[136:139], v[216:219], v[72:75]\n" \
"ds_read_b64_tr_b16 a[190:191], v211 offset:13824\n" \
"ds_read_b64_tr_b16 a[128:129], %[vvb] offset:16384\n" \
"s_add_u32 s60, s60, 1\n" \
"s_nop 0\n" \
"v_readlane_b32 s58, %[tlo], s60\n" \
"v_mfma_f32_16x16x32_bf16 v[72:75], a[168:171], v[200:203], v[72:75]\n" \
"v_exp_f32_e64 v130, v130\n" \
"v_mfma_f32_16x16x32_bf16 v[76:79], a[140:143], v[216:219], v[76:79]\n" \
"ds_read_b64_tr_b16 a[130:131], %[vvb] offset:20480\n" \
"ds_read_b64_tr_b16 a[132:133], v211 offset:16384\n" \
"v_readlane_b32 s59, %[thi], s60\n" \
"v_mfma_f32_16x16x32_bf16 v[76:79], a[172:175], v[200:203], v[76:79]\n" \
"v_exp_f32_e64 v131, v131\n" \
"v_smfmac_f32_16x16x64_bf16 v[176:179], a[192:195], v[192:199], v228\n" \
"ds_read_b64_tr_b16 a[134:135], v211 offset:20480\n" \
"ds_read_b64_tr_b16 a[136:137], %[vvb] offset:16896\n" \
"s_cmp_eq_u32 s60, 7\n" \
"s_cselect_b32 s61, 56, 1\n" \
"v_mul_u32_u24_e64 v236, s61, v224\n" \
"v_mfma_f32_16x16x32_bf16 v[136:139], a[64:67], a[16:19], v[164:167]\n" \
"v_exp_f32_e64 v132, v132\n" \
"v_mfma_f32_16x16x32_bf16 v[136:139], a[68:71], a[20:23], v[136:139]\n" \
"ds_read_b64_tr_b16 a[138:139], %[vvb] offset:20992\n" \
"ds_read_b64_tr_b16 a[140:141], v211 offset:16896\n" \
"v_mfma_f32_16x16x32_bf16 v[136:139], a[72:75], a[24:27], v[136:139]\n" \
"v_exp_f32_e64 v133, v133\n" \
"v_mfma_f32_16x16x32_bf16 v[136:139], a[76:79], a[28:31], v[136:139]\n" \
"v_cvt_pk_bf16_f32 v184, v128, v129\n" \
"v_cvt_pk_bf16_f32 v185, v130, v131\n" \
"v_mfma_f32_16x16x32_bf16 v[140:143], a[92:95], a[28:31], v[164:167]\n" \
"v_exp_f32_e64 v134, v134\n" \
"v_mfma_f32_16x16x32_bf16 v[140:143], a[88:91], a[24:27], v[140:143]\n" \
"ds_read_b64_tr_b16 a[142:143], v211 offset:20992\n" \
"s_waitcnt lgkmcnt(8)\n" \
"v_mfma_f32_16x16x32_bf16 v[140:143], a[84:87], a[20:23], v[140:143]\n" \
"buffer_load_dwordx4 v212, %[srdk], s48 offen offset:1024 lds\n" \
"v_mfma_f32_16x16x32_bf16 v[140:143], a[80:83], a[16:19], v[140:143]\n" \
"v_exp_f32_e32 v135, v135\n" \
"v_mfma_f32_16x16x32_bf16 v[80:83], a[144:147], v[216:219], v[80:83]\n" \
"ds_read_b64_tr_b16 a[144:145], %[vvb] offset:17408\n" \
"ds_read_b64_tr_b16 a[146:147], %[vvb] offset:21504\n" \
"v_mfma_f32_16x16x32_bf16 v[80:83], a[176:179], v[200:203], v[80:83]\n" \
"v_exp_f32_e64 v136, v136\n" \
"v_mfma_f32_16x16x32_bf16 v[84:87], a[148:151], v[216:219], v[84:87]\n" \
"v_cvt_pk_bf16_f32 v186, v132, v133\n" \
"v_cvt_pk_bf16_f32 v187, v134, v135\n" \
"v_mfma_f32_16x16x32_bf16 v[84:87], a[180:183], v[200:203], v[84:87]\n" \
"v_exp_f32_e64 v137, v137\n" \
"v_mfma_f32_16x16x32_bf16 v[88:91], a[152:155], v[216:219], v[88:91]\n" \
"v_exp_f32_e64 v138, v138\n" \
"v_mfma_f32_16x16x32_bf16 v[88:91], a[184:187], v[200:203], v[88:91]\n" \
"ds_read_b64_tr_b16 a[148:149], v211 offset:17408\n" \
"ds_read_b64_tr_b16 a[150:151], v211 offset:21504\n" \
"v_mfma_f32_16x16x32_bf16 v[92:95], a[156:159], v[216:219], v[92:95]\n" \
"v_exp_f32_e64 v139, v139\n" \
"v_mfma_f32_16x16x32_bf16 v[92:95], a[188:191], v[200:203], v[92:95]\n" \
"ds_read_b64_tr_b16 a[152:153], %[vvb] offset:17920\n" \
"buffer_store_dwordx4 a[196:199], v235, %[srdz], s63 offen offset:0 sc1\n" \
"v_mfma_f32_16x16x32_bf16 v[144:147], a[64:67], a[32:35], v[168:171]\n" \
"v_exp_f32_e64 v140, v140\n" \
"v_mfma_f32_16x16x32_bf16 v[144:147], a[68:71], a[36:39], v[144:147]\n" \
"ds_read_b64_tr_b16 a[154:155], %[vvb] offset:22016\n" \
"ds_read_b64_tr_b16 a[156:157], v211 offset:17920\n" \
"v_mfma_f32_16x16x32_bf16 v[144:147], a[72:75], a[40:43], v[144:147]\n" \
"v_exp_f32_e64 v141, v141\n" \
"v_mfma_f32_16x16x32_bf16 v[144:147], a[76:79], a[44:47], v[144:147]\n" \
"v_cvt_pk_bf16_f32 v188, v136, v137\n" \
"v_cvt_pk_bf16_f32 v189, v138, v139\n" \
"v_mfma_f32_16x16x32_bf16 v[148:151], a[92:95], a[44:47], v[168:171]\n" \
"v_exp_f32_e64 v142, v142\n" \
"v_mfma_f32_16x16x32_bf16 v[148:151], a[88:91], a[40:43], v[148:151]\n" \
"ds_read_b64_tr_b16 a[158:159], v211 offset:22016\n" \
"s_waitcnt lgkmcnt(8)\n" \
"v_mfma_f32_16x16x32_bf16 v[148:151], a[84:87], a[36:39], v[148:151]\n" \
"buffer_load_dwordx4 v213, %[srdk], s48 offen offset:2048 lds\n" \
"v_mfma_f32_16x16x32_bf16 v[148:151], a[80:83], a[32:35], v[148:151]\n" \
"v_exp_f32_e32 v143, v143\n" \
"v_mfma_f32_16x16x32_bf16 v[0:3], a[160:163], v[192:195], v[0:3]\n" \
"ds_read_b128 a[96:99], %[kb0] offset:24576\n" \
"v_mfma_f32_16x16x32_bf16 v[0:3], a[128:131], v[184:187], v[0:3]\n" \
"v_exp_f32_e64 v144, v144\n" \
"v_mfma_f32_16x16x32_bf16 v[4:7], a[164:167], v[192:195], v[4:7]\n" \
"v_cvt_pk_bf16_f32 v190, v140, v141\n" \
"v_cvt_pk_bf16_f32 v191, v142, v143\n" \
"v_mfma_f32_16x16x32_bf16 v[4:7], a[132:135], v[184:187], v[4:7]\n" \
"v_exp_f32_e64 v145, v145\n" \
"v_mfma_f32_16x16x32_bf16 v[8:11], a[168:171], v[192:195], v[8:11]\n" \
"ds_read_b128 a[100:103], v208 offset:24576\n" \
"v_mfma_f32_16x16x32_bf16 v[8:11], a[136:139], v[184:187], v[8:11]\n" \
"v_exp_f32_e64 v146, v146\n" \
"v_mfma_f32_16x16x32_bf16 v[12:15], a[172:175], v[192:195], v[12:15]\n" \
"ds_read_b128 a[104:107], v209 offset:24576\n" \
"v_mfma_f32_16x16x32_bf16 v[12:15], a[140:143], v[184:187], v[12:15]\n" \
"v_exp_f32_e64 v147, v147\n" \
"v_smfmac_f32_16x16x64_bf16 v[180:183], a[192:195], v[200:207], v228\n" \
"ds_read_b128 a[108:111], v210 offset:24576\n" \
"v_mfma_f32_16x16x32_bf16 v[152:155], a[64:67], a[48:51], v[172:175]\n" \
"v_exp_f32_e64 v148, v148\n" \
"v_mfma_f32_16x16x32_bf16 v[152:155], a[68:71], a[52:55], v[152:155]\n" \
"ds_read_b128 a[124:127], v210 offset:28672\n" \
"v_mfma_f32_16x16x32_bf16 v[152:155], a[72:75], a[56:59], v[152:155]\n" \
"v_exp_f32_e64 v149, v149\n" \
"v_mfma_f32_16x16x32_bf16 v[152:155], a[76:79], a[60:63], v[152:155]\n" \
"v_cvt_pk_bf16_f32 v216, v144, v145\n" \
"v_cvt_pk_bf16_f32 v217, v146, v147\n" \
"v_mfma_f32_16x16x32_bf16 v[156:159], a[92:95], a[60:63], v[172:175]\n" \
"v_exp_f32_e64 v150, v150\n" \
"v_mfma_f32_16x16x32_bf16 v[156:159], a[88:91], a[56:59], v[156:159]\n" \
"ds_read_b128 a[120:123], v209 offset:28672\n" \
"s_waitcnt lgkmcnt(6)\n" \
"v_mfma_f32_16x16x32_bf16 v[156:159], a[84:87], a[52:55], v[156:159]\n" \
"buffer_load_dwordx4 v214, %[srdk], s48 offen offset:3072 lds\n" \
"v_mfma_f32_16x16x32_bf16 v[156:159], a[80:83], a[48:51], v[156:159]\n" \
"v_exp_f32_e32 v151, v151\n" \
"v_mfma_f32_16x16x32_bf16 v[16:19], a[176:179], v[192:195], v[16:19]\n" \
"ds_read_b128 a[116:119], v208 offset:28672\n" \
"v_mfma_f32_16x16x32_bf16 v[16:19], a[144:147], v[184:187], v[16:19]\n" \
"v_exp_f32_e64 v152, v152\n" \
"v_mfma_f32_16x16x32_bf16 v[20:23], a[180:183], v[192:195], v[20:23]\n" \
"v_cvt_pk_bf16_f32 v218, v148, v149\n" \
"v_cvt_pk_bf16_f32 v219, v150, v151\n" \
"v_mfma_f32_16x16x32_bf16 v[20:23], a[148:151], v[184:187], v[20:23]\n" \
"v_exp_f32_e64 v153, v153\n" \
"v_mfma_f32_16x16x32_bf16 v[24:27], a[184:187], v[192:195], v[24:27]\n" \
"v_exp_f32_e64 v154, v154\n" \
"v_mfma_f32_16x16x32_bf16 v[24:27], a[152:155], v[184:187], v[24:27]\n" \
"ds_read_b128 a[112:115], %[kb0] offset:28672\n" \
"s_add_u32 s48, s48, 0x4000\n" \
"s_mov_b32 m0, s44\n" \
"s_addk_i32 s44, 0x400\n" \
"buffer_load_dwordx4 v229, s[64:67], s45 offen offset:0 lds\n" \
"s_add_u32 s45, s45, 229376\n" \
"v_mfma_f32_16x16x32_bf16 v[28:31], a[188:191], v[192:195], v[28:31]\n" \
"v_exp_f32_e64 v155, v155\n" \
"v_mfma_f32_16x16x32_bf16 v[28:31], a[156:159], v[184:187], v[28:31]\n" \
"s_waitcnt vmcnt(15) lgkmcnt(2)\n" \
"s_barrier\n" \
"v_mfma_f32_16x16x32_bf16 v[128:131], a[96:99], a[0:3], v[160:163]\n" \
"v_exp_f32_e64 v156, v156\n" \
"v_mfma_f32_16x16x32_bf16 v[128:131], a[100:103], a[4:7], v[128:131]\n" \
"s_add_u32 m0, %[m0b], 65536\n" \
"v_mfma_f32_16x16x32_bf16 v[128:131], a[104:107], a[8:11], v[128:131]\n" \
"v_exp_f32_e64 v157, v157\n" \
"v_mfma_f32_16x16x32_bf16 v[128:131], a[108:111], a[12:15], v[128:131]\n" \
"v_cvt_pk_bf16_f32 v220, v152, v153\n" \
"v_cvt_pk_bf16_f32 v221, v154, v155\n" \
"v_mfma_f32_16x16x32_bf16 v[132:135], a[124:127], a[12:15], v[160:163]\n" \
"v_exp_f32_e64 v158, v158\n" \
"v_mfma_f32_16x16x32_bf16 v[132:135], a[120:123], a[8:11], v[132:135]\n" \
"s_waitcnt lgkmcnt(0)\n" \
"v_mfma_f32_16x16x32_bf16 v[132:135], a[116:119], a[4:7], v[132:135]\n" \
"buffer_load_dwordx4 %[vd0], %[srdv], s48 offen offset:0 lds\n" \
"v_mfma_f32_16x16x32_bf16 v[132:135], a[112:115], a[0:3], v[132:135]\n" \
"v_exp_f32_e32 v159, v159\n" \
"v_mfma_f32_16x16x32_bf16 v[32:35], a[160:163], v[196:199], v[32:35]\n" \
"ds_read_b128 a[64:67], %[kb0] offset:32768\n" \
"v_mfma_f32_16x16x32_bf16 v[32:35], a[128:131], v[188:191], v[32:35]\n" \
"v_exp_f32_e64 v128, v128\n" \
"v_mfma_f32_16x16x32_bf16 v[36:39], a[164:167], v[196:199], v[36:39]\n" \
"v_cvt_pk_bf16_f32 v222, v156, v157\n" \
"v_cvt_pk_bf16_f32 v223, v158, v159\n" \
"v_mfma_f32_16x16x32_bf16 v[36:39], a[132:135], v[188:191], v[36:39]\n" \
"v_exp_f32_e64 v129, v129\n" \
"v_mfma_f32_16x16x32_bf16 v[40:43], a[168:171], v[196:199], v[40:43]\n" \
"ds_read_b128 a[68:71], v208 offset:32768\n" \
"v_mfma_f32_16x16x32_bf16 v[40:43], a[136:139], v[188:191], v[40:43]\n" \
"v_exp_f32_e64 v130, v130\n" \
"v_mfma_f32_16x16x32_bf16 v[44:47], a[172:175], v[196:199], v[44:47]\n" \
"ds_read_b128 a[72:75], v209 offset:32768\n" \
"v_mfma_f32_16x16x32_bf16 v[44:47], a[140:143], v[188:191], v[44:47]\n" \
"v_exp_f32_e64 v131, v131\n" \
"v_smfmac_f32_16x16x64_bf16 v[176:179], a[192:195], v[184:191], v228\n" \
"v_mfma_f32_16x16x32_bf16 v[136:139], a[96:99], a[16:19], v[164:167]\n" \
"v_exp_f32_e64 v132, v132\n" \
"v_mfma_f32_16x16x32_bf16 v[136:139], a[100:103], a[20:23], v[136:139]\n" \
"ds_read_b128 a[76:79], v210 offset:32768\n" \
"v_mfma_f32_16x16x32_bf16 v[136:139], a[104:107], a[24:27], v[136:139]\n" \
"v_exp_f32_e64 v133, v133\n" \
"v_mfma_f32_16x16x32_bf16 v[136:139], a[108:111], a[28:31], v[136:139]\n" \
"v_cvt_pk_bf16_f32 v192, v128, v129\n" \
"v_cvt_pk_bf16_f32 v193, v130, v131\n" \
"v_mfma_f32_16x16x32_bf16 v[140:143], a[124:127], a[28:31], v[164:167]\n" \
"v_exp_f32_e64 v134, v134\n" \
"v_mfma_f32_16x16x32_bf16 v[140:143], a[120:123], a[24:27], v[140:143]\n" \
"ds_read_b128 a[92:95], v210 offset:36864\n" \
"v_mfma_f32_16x16x32_bf16 v[140:143], a[116:119], a[20:23], v[140:143]\n" \
"buffer_load_dwordx4 v215, %[srdv], s48 offen offset:1024 lds\n" \
"v_mfma_f32_16x16x32_bf16 v[140:143], a[112:115], a[16:19], v[140:143]\n" \
"v_exp_f32_e64 v135, v135\n" \
"v_mfma_f32_16x16x32_bf16 v[48:51], a[176:179], v[196:199], v[48:51]\n" \
"ds_read_b128 a[88:91], v209 offset:36864\n" \
"v_mfma_f32_16x16x32_bf16 v[48:51], a[144:147], v[188:191], v[48:51]\n" \
"v_exp_f32_e64 v136, v136\n" \
"v_mfma_f32_16x16x32_bf16 v[52:55], a[180:183], v[196:199], v[52:55]\n" \
"v_cvt_pk_bf16_f32 v194, v132, v133\n" \
"v_cvt_pk_bf16_f32 v195, v134, v135\n" \
"v_mfma_f32_16x16x32_bf16 v[52:55], a[148:151], v[188:191], v[52:55]\n" \
"v_exp_f32_e64 v137, v137\n" \
"v_mfma_f32_16x16x32_bf16 v[56:59], a[184:187], v[196:199], v[56:59]\n" \
"v_exp_f32_e64 v138, v138\n" \
"v_mfma_f32_16x16x32_bf16 v[56:59], a[152:155], v[188:191], v[56:59]\n" \
"ds_read_b128 a[84:87], v208 offset:36864\n" \
"v_mfma_f32_16x16x32_bf16 v[60:63], a[188:191], v[196:199], v[60:63]\n" \
"v_exp_f32_e64 v139, v139\n" \
"v_mfma_f32_16x16x32_bf16 v[60:63], a[156:159], v[188:191], v[60:63]\n" \
"s_cmp_lg_u32 %[wid], 0\n" \
"s_cbranch_scc1 LtB3_%=\n" \
"global_load_dword v237, v236, s[58:59]\n" \
"s_add_u32 s61, s48, 49152\n" \
"s_mov_b32 exec_hi, 0\n" \
"s_nop 0\n" \
"buffer_load_dword v237, v224, %[srdp], s61 offen\n" \
"s_mov_b32 exec_hi, -1\n" \
"s_nop 0\n" \
"LtB3_%=:\n" \
"v_mfma_f32_16x16x32_bf16 v[144:147], a[96:99], a[32:35], v[168:171]\n" \
"v_exp_f32_e64 v140, v140\n" \
"v_mfma_f32_16x16x32_bf16 v[144:147], a[100:103], a[36:39], v[144:147]\n" \
"ds_read_b128 a[80:83], %[kb0] offset:36864\n" \
"v_mfma_f32_16x16x32_bf16 v[144:147], a[104:107], a[40:43], v[144:147]\n" \
"v_exp_f32_e64 v141, v141\n" \
"v_mfma_f32_16x16x32_bf16 v[144:147], a[108:111], a[44:47], v[144:147]\n" \
"v_cvt_pk_bf16_f32 v196, v136, v137\n" \
"v_cvt_pk_bf16_f32 v197, v138, v139\n" \
"v_mfma_f32_16x16x32_bf16 v[148:151], a[124:127], a[44:47], v[168:171]\n" \
"v_exp_f32_e64 v142, v142\n" \
"v_mfma_f32_16x16x32_bf16 v[148:151], a[120:123], a[40:43], v[148:151]\n" \
"v_mfma_f32_16x16x32_bf16 v[148:151], a[116:119], a[36:39], v[148:151]\n" \
"buffer_load_dwordx4 %[vd0], %[srdv], s48 offen offset:2048 lds\n" \
"v_mfma_f32_16x16x32_bf16 v[148:151], a[112:115], a[32:35], v[148:151]\n" \
"v_exp_f32_e64 v143, v143\n" \
"v_mfma_f32_16x16x32_bf16 v[96:99], a[160:163], v[204:207], v[96:99]\n" \
"ds_read_b64_tr_b16 a[160:161], %[vvb] offset:24576\n" \
"v_mfma_f32_16x16x32_bf16 v[96:99], a[128:131], v[220:223], v[96:99]\n" \
"v_exp_f32_e64 v144, v144\n" \
"v_mfma_f32_16x16x32_bf16 v[100:103], a[164:167], v[204:207], v[100:103]\n" \
"v_cvt_pk_bf16_f32 v198, v140, v141\n" \
"v_cvt_pk_bf16_f32 v199, v142, v143\n" \
"v_mfma_f32_16x16x32_bf16 v[100:103], a[132:135], v[220:223], v[100:103]\n" \
"v_exp_f32_e64 v145, v145\n" \
"v_mfma_f32_16x16x32_bf16 v[104:107], a[168:171], v[204:207], v[104:107]\n" \
"ds_read_b64_tr_b16 a[162:163], %[vvb] offset:28672\n" \
"ds_read_b64_tr_b16 a[164:165], v211 offset:24576\n" \
"v_mfma_f32_16x16x32_bf16 v[104:107], a[136:139], v[220:223], v[104:107]\n" \
"v_exp_f32_e64 v146, v146\n" \
"v_mfma_f32_16x16x32_bf16 v[108:111], a[172:175], v[204:207], v[108:111]\n" \
"ds_read_b64_tr_b16 a[166:167], v211 offset:28672\n" \
"v_mfma_f32_16x16x32_bf16 v[108:111], a[140:143], v[220:223], v[108:111]\n" \
"v_exp_f32_e64 v147, v147\n" \
"v_smfmac_f32_16x16x64_bf16 v[180:183], a[192:195], v[216:223], v228\n" \
"ds_read_b64_tr_b16 a[168:169], %[vvb] offset:25088\n" \
"buffer_store_dwordx4 a[196:199], v235, %[srdz], s62 offen offset:0 sc1\n" \
"v_mfma_f32_16x16x32_bf16 v[152:155], a[96:99], a[48:51], v[172:175]\n" \
"v_exp_f32_e64 v148, v148\n" \
"v_mfma_f32_16x16x32_bf16 v[152:155], a[100:103], a[52:55], v[152:155]\n" \
"ds_read_b64_tr_b16 a[170:171], %[vvb] offset:29184\n" \
"v_mfma_f32_16x16x32_bf16 v[152:155], a[104:107], a[56:59], v[152:155]\n" \
"v_exp_f32_e64 v149, v149\n" \
"v_mfma_f32_16x16x32_bf16 v[152:155], a[108:111], a[60:63], v[152:155]\n" \
"v_cvt_pk_bf16_f32 v200, v144, v145\n" \
"v_cvt_pk_bf16_f32 v201, v146, v147\n" \
"v_mfma_f32_16x16x32_bf16 v[156:159], a[124:127], a[60:63], v[172:175]\n" \
"v_exp_f32_e64 v150, v150\n" \
"v_mfma_f32_16x16x32_bf16 v[156:159], a[120:123], a[56:59], v[156:159]\n" \
"ds_read_b64_tr_b16 a[172:173], v211 offset:25088\n" \
"v_mfma_f32_16x16x32_bf16 v[156:159], a[116:119], a[52:55], v[156:159]\n" \
"buffer_load_dwordx4 v215, %[srdv], s48 offen offset:3072 lds\n" \
"v_mfma_f32_16x16x32_bf16 v[156:159], a[112:115], a[48:51], v[156:159]\n" \
"v_exp_f32_e64 v151, v151\n" \
"v_mfma_f32_16x16x32_bf16 v[112:115], a[176:179], v[204:207], v[112:115]\n" \
"ds_read_b64_tr_b16 a[174:175], v211 offset:29184\n" \
"ds_read_b64_tr_b16 a[176:177], %[vvb] offset:25600\n" \
"v_mfma_f32_16x16x32_bf16 v[112:115], a[144:147], v[220:223], v[112:115]\n" \
"v_exp_f32_e64 v152, v152\n" \
"v_mfma_f32_16x16x32_bf16 v[116:119], a[180:183], v[204:207], v[116:119]\n" \
"v_cvt_pk_bf16_f32 v202, v148, v149\n" \
"v_cvt_pk_bf16_f32 v203, v150, v151\n" \
"v_mfma_f32_16x16x32_bf16 v[116:119], a[148:151], v[220:223], v[116:119]\n" \
"v_exp_f32_e64 v153, v153\n" \
"v_mfma_f32_16x16x32_bf16 v[120:123], a[184:187], v[204:207], v[120:123]\n" \
"v_exp_f32_e64 v154, v154\n" \
"v_mfma_f32_16x16x32_bf16 v[120:123], a[152:155], v[220:223], v[120:123]\n" \
"ds_read_b64_tr_b16 a[178:179], %[vvb] offset:29696\n" \
"v_mfma_f32_16x16x32_bf16 v[124:127], a[188:191], v[204:207], v[124:127]\n" \
"v_exp_f32_e64 v155, v155\n" \
"v_mfma_f32_16x16x32_bf16 v[124:127], a[156:159], v[220:223], v[124:127]\n" \
"ds_read_b64_tr_b16 a[180:181], v211 offset:25600\n" \
"s_waitcnt lgkmcnt(13)\n" \
"v_mfma_f32_16x16x32_bf16 v[128:131], a[64:67], a[0:3], v[160:163]\n" \
"v_exp_f32_e32 v156, v156\n" \
"v_mfma_f32_16x16x32_bf16 v[128:131], a[68:71], a[4:7], v[128:131]\n" \
"ds_read_b64_tr_b16 a[182:183], v211 offset:29696\n" \
"s_add_u32 m0, %[m0b], 16384\n" \
"v_mfma_f32_16x16x32_bf16 v[128:131], a[72:75], a[8:11], v[128:131]\n" \
"v_exp_f32_e64 v157, v157\n" \
"v_mfma_f32_16x16x32_bf16 v[128:131], a[76:79], a[12:15], v[128:131]\n" \
"v_cvt_pk_bf16_f32 v204, v152, v153\n" \
"v_cvt_pk_bf16_f32 v205, v154, v155\n" \
"v_mfma_f32_16x16x32_bf16 v[132:135], a[92:95], a[12:15], v[160:163]\n" \
"v_exp_f32_e64 v158, v158\n" \
"v_mfma_f32_16x16x32_bf16 v[132:135], a[88:91], a[8:11], v[132:135]\n" \
"ds_read_b64_tr_b16 a[184:185], %[vvb] offset:26112\n" \
"s_waitcnt lgkmcnt(5)\n" \
"v_mfma_f32_16x16x32_bf16 v[132:135], a[84:87], a[4:7], v[132:135]\n" \
"buffer_load_dwordx4 %[kd0], %[srdk], s48 offen offset:0 lds\n" \
"v_mfma_f32_16x16x32_bf16 v[132:135], a[80:83], a[0:3], v[132:135]\n" \
"v_exp_f32_e32 v159, v159\n" \
"v_mfma_f32_16x16x32_bf16 v[64:67], a[128:131], v[216:219], v[64:67]\n" \
"ds_read_b64_tr_b16 a[186:187], %[vvb] offset:30208\n" \
"ds_read_b64_tr_b16 a[188:189], v211 offset:26112\n" \
"v_mfma_f32_16x16x32_bf16 v[64:67], a[160:163], v[200:203], v[64:67]\n" \
"v_exp_f32_e64 v128, v128\n" \
"v_mfma_f32_16x16x32_bf16 v[68:71], a[132:135], v[216:219], v[68:71]\n" \
"v_cvt_pk_bf16_f32 v206, v156, v157\n" \
"v_cvt_pk_bf16_f32 v207, v158, v159\n" \
"v_mfma_f32_16x16x32_bf16 v[68:71], a[164:167], v[200:203], v[68:71]\n" \
"v_exp_f32_e64 v129, v129\n" \
"v_mfma_f32_16x16x32_bf16 v[72:75], a[136:139], v[216:219], v[72:75]\n" \
"ds_read_b64_tr_b16 a[190:191], v211 offset:30208\n" \
"ds_read_b64_tr_b16 a[128:129], %[vvb] offset:32768\n" \
"v_mfma_f32_16x16x32_bf16 v[72:75], a[168:171], v[200:203], v[72:75]\n" \
"v_exp_f32_e64 v130, v130\n" \
"v_mfma_f32_16x16x32_bf16 v[76:79], a[140:143], v[216:219], v[76:79]\n" \
"ds_read_b64_tr_b16 a[130:131], %[vvb] offset:36864\n" \
"ds_read_b64_tr_b16 a[132:133], v211 offset:32768\n" \
"v_mfma_f32_16x16x32_bf16 v[76:79], a[172:175], v[200:203], v[76:79]\n" \
"v_exp_f32_e64 v131, v131\n" \
"v_smfmac_f32_16x16x64_bf16 v[176:179], a[192:195], v[192:199], v228\n" \
"ds_read_b64_tr_b16 a[134:135], v211 offset:36864\n" \
"ds_read_b64_tr_b16 a[136:137], %[vvb] offset:33280\n" \
"v_mfma_f32_16x16x32_bf16 v[136:139], a[64:67], a[16:19], v[164:167]\n" \
"v_exp_f32_e64 v132, v132\n" \
"v_mfma_f32_16x16x32_bf16 v[136:139], a[68:71], a[20:23], v[136:139]\n" \
"ds_read_b64_tr_b16 a[138:139], %[vvb] offset:37376\n" \
"ds_read_b64_tr_b16 a[140:141], v211 offset:33280\n" \
"v_mfma_f32_16x16x32_bf16 v[136:139], a[72:75], a[24:27], v[136:139]\n" \
"v_exp_f32_e64 v133, v133\n" \
"v_mfma_f32_16x16x32_bf16 v[136:139], a[76:79], a[28:31], v[136:139]\n" \
"v_cvt_pk_bf16_f32 v184, v128, v129\n" \
"v_cvt_pk_bf16_f32 v185, v130, v131\n" \
"v_mfma_f32_16x16x32_bf16 v[140:143], a[92:95], a[28:31], v[164:167]\n" \
"v_exp_f32_e64 v134, v134\n" \
"v_mfma_f32_16x16x32_bf16 v[140:143], a[88:91], a[24:27], v[140:143]\n" \
"ds_read_b64_tr_b16 a[142:143], v211 offset:37376\n" \
"s_waitcnt lgkmcnt(8)\n" \
"v_mfma_f32_16x16x32_bf16 v[140:143], a[84:87], a[20:23], v[140:143]\n" \
"buffer_load_dwordx4 v212, %[srdk], s48 offen offset:1024 lds\n" \
"v_mfma_f32_16x16x32_bf16 v[140:143], a[80:83], a[16:19], v[140:143]\n" \
"v_exp_f32_e32 v135, v135\n" \
"v_mfma_f32_16x16x32_bf16 v[80:83], a[144:147], v[216:219], v[80:83]\n" \
"ds_read_b64_tr_b16 a[144:145], %[vvb] offset:33792\n" \
"ds_read_b64_tr_b16 a[146:147], %[vvb] offset:37888\n" \
"v_mfma_f32_16x16x32_bf16 v[80:83], a[176:179], v[200:203], v[80:83]\n" \
"v_exp_f32_e64 v136, v136\n" \
"v_mfma_f32_16x16x32_bf16 v[84:87], a[148:151], v[216:219], v[84:87]\n" \
"v_cvt_pk_bf16_f32 v186, v132, v133\n" \
"v_cvt_pk_bf16_f32 v187, v134, v135\n" \
"v_mfma_f32_16x16x32_bf16 v[84:87], a[180:183], v[200:203], v[84:87]\n" \
"v_exp_f32_e64 v137, v137\n" \
"v_mfma_f32_16x16x32_bf16 v[88:91], a[152:155], v[216:219], v[88:91]\n" \
"v_exp_f32_e64 v138, v138\n" \
"v_mfma_f32_16x16x32_bf16 v[88:91], a[184:187], v[200:203], v[88:91]\n" \
"ds_read_b64_tr_b16 a[148:149], v211 offset:33792\n" \
"ds_read_b64_tr_b16 a[150:151], v211 offset:37888\n" \
"v_mfma_f32_16x16x32_bf16 v[92:95], a[156:159], v[216:219], v[92:95]\n" \
".if . - adaln_body != 24576\n" \
".error \"size model\"\n" \
".endif\n" \
"Lk5_%=:\n" \
"s_cbranch_execz adaln_kr\n" \
"s_nop 0\n" \
"v_exp_f32_e64 v139, v139\n" \
"v_mfma_f32_16x16x32_bf16 v[92:95], a[188:191], v[200:203], v[92:95]\n" \
"ds_read_b64_tr_b16 a[152:153], %[vvb] offset:34304\n" \
"buffer_store_dwordx4 a[196:199], v235, %[srdz], s63 offen offset:1024 sc1\n" \
"v_mfma_f32_16x16x32_bf16 v[144:147], a[64:67], a[32:35], v[168:171]\n" \
"v_exp_f32_e64 v140, v140\n" \
"v_mfma_f32_16x16x32_bf16 v[144:147], a[68:71], a[36:39], v[144:147]\n" \
"ds_read_b64_tr_b16 a[154:155], %[vvb] offset:38400\n" \
"ds_read_b64_tr_b16 a[156:157], v211 offset:34304\n" \
"v_mfma_f32_16x16x32_bf16 v[144:147], a[72:75], a[40:43], v[144:147]\n" \
"v_exp_f32_e64 v141, v141\n" \
"v_mfma_f32_16x16x32_bf16 v[144:147], a[76:79], a[44:47], v[144:147]\n" \
"v_cvt_pk_bf16_f32 v188, v136, v137\n" \
"v_cvt_pk_bf16_f32 v189, v138, v139\n" \
"v_mfma_f32_16x16x32_bf16 v[148:151], a[92:95], a[44:47], v[168:171]\n" \
"v_exp_f32_e64 v142, v142\n" \
"v_mfma_f32_16x16x32_bf16 v[148:151], a[88:91], a[40:43], v[148:151]\n" \
"ds_read_b64_tr_b16 a[158:159], v211 offset:38400\n" \
"s_waitcnt lgkmcnt(8)\n" \
"v_mfma_f32_16x16x32_bf16 v[148:151], a[84:87], a[36:39], v[148:151]\n" \
"buffer_load_dwordx4 v213, %[srdk], s48 offen offset:2048 lds\n" \
"v_mfma_f32_16x16x32_bf16 v[148:151], a[80:83], a[32:35], v[148:151]\n" \
"v_exp_f32_e32 v143, v143\n" \
"v_mfma_f32_16x16x32_bf16 v[0:3], a[160:163], v[192:195], v[0:3]\n" \
"ds_read_b128 a[96:99], %[kb0] offset:40960\n" \
"v_mfma_f32_16x16x32_bf16 v[0:3], a[128:131], v[184:187], v[0:3]\n" \
"v_exp_f32_e64 v144, v144\n" \
"v_mfma_f32_16x16x32_bf16 v[4:7], a[164:167], v[192:195], v[4:7]\n" \
"v_cvt_pk_bf16_f32 v190, v140, v141\n" \
"v_cvt_pk_bf16_f32 v191, v142, v143\n" \
"v_mfma_f32_16x16x32_bf16 v[4:7], a[132:135], v[184:187], v[4:7]\n" \
"v_exp_f32_e64 v145, v145\n" \
"v_mfma_f32_16x16x32_bf16 v[8:11], a[168:171], v[192:195], v[8:11]\n" \
"ds_read_b128 a[100:103], v208 offset:40960\n" \
"v_mfma_f32_16x16x32_bf16 v[8:11], a[136:139], v[184:187], v[8:11]\n" \
"v_exp_f32_e64 v146, v146\n" \
"v_mfma_f32_16x16x32_bf16 v[12:15], a[172:175], v[192:195], v[12:15]\n" \
"ds_read_b128 a[104:107], v209 offset:40960\n" \
"v_mfma_f32_16x16x32_bf16 v[12:15], a[140:143], v[184:187], v[12:15]\n" \
"v_exp_f32_e64 v147, v147\n" \
"v_smfmac_f32_16x16x64_bf16 v[180:183], a[192:195], v[200:207], v228\n" \
"ds_read_b128 a[108:111], v210 offset:40960\n" \
"v_mfma_f32_16x16x32_bf16 v[152:155], a[64:67], a[48:51], v[172:175]\n" \
"v_exp_f32_e64 v148, v148\n" \
"v_mfma_f32_16x16x32_bf16 v[152:155], a[68:71], a[52:55], v[152:155]\n" \
"ds_read_b128 a[124:127], v210 offset:45056\n" \
"v_mfma_f32_16x16x32_bf16 v[152:155], a[72:75], a[56:59], v[152:155]\n" \
"v_exp_f32_e64 v149, v149\n" \
"v_mfma_f32_16x16x32_bf16 v[152:155], a[76:79], a[60:63], v[152:155]\n" \
"v_cvt_pk_bf16_f32 v216, v144, v145\n" \
"v_cvt_pk_bf16_f32 v217, v146, v147\n" \
"v_mfma_f32_16x16x32_bf16 v[156:159], a[92:95], a[60:63], v[172:175]\n" \
"v_exp_f32_e64 v150, v150\n" \
"v_mfma_f32_16x16x32_bf16 v[156:159], a[88:91], a[56:59], v[156:159]\n" \
"ds_read_b128 a[120:123], v209 offset:45056\n" \
"s_waitcnt lgkmcnt(6)\n" \
"v_mfma_f32_16x16x32_bf16 v[156:159], a[84:87], a[52:55], v[156:159]\n" \
"buffer_load_dwordx4 v214, %[srdk], s48 offen offset:3072 lds\n" \
"v_mfma_f32_16x16x32_bf16 v[156:159], a[80:83], a[48:51], v[156:159]\n" \
"v_exp_f32_e32 v151, v151\n" \
"v_mfma_f32_16x16x32_bf16 v[16:19], a[176:179], v[192:195], v[16:19]\n" \
"ds_read_b128 a[116:119], v208 offset:45056\n" \
"v_mfma_f32_16x16x32_bf16 v[16:19], a[144:147], v[184:187], v[16:19]\n" \
"v_exp_f32_e64 v152, v152\n" \
"v_mfma_f32_16x16x32_bf16 v[20:23], a[180:183], v[192:195], v[20:23]\n" \
"v_cvt_pk_bf16_f32 v218, v148, v149\n" \
"v_cvt_pk_bf16_f32 v219, v150, v151\n" \
"v_mfma_f32_16x16x32_bf16 v[20:23], a[148:151], v[184:187], v[20:23]\n" \
"v_exp_f32_e64 v153, v153\n" \
"v_mfma_f32_16x16x32_bf16 v[24:27], a[184:187], v[192:195], v[24:27]\n" \
"v_exp_f32_e64 v154, v154\n" \
"v_mfma_f32_16x16x32_bf16 v[24:27], a[152:155], v[184:187], v[24:27]\n" \
"ds_read_b128 a[112:115], %[kb0] offset:45056\n" \
"s_add_u32 s48, s48, 0x4000\n" \
"v_mfma_f32_16x16x32_bf16 v[28:31], a[188:191], v[192:195], v[28:31]\n" \
"v_exp_f32_e64 v155, v155\n" \
"v_mfma_f32_16x16x32_bf16 v[28:31], a[156:159], v[184:187], v[28:31]\n" \
"s_waitcnt vmcnt(16) lgkmcnt(2)\n" \
"s_barrier\n" \
"v_mfma_f32_16x16x32_bf16 v[128:131], a[96:99], a[0:3], v[160:163]\n" \
"v_exp_f32_e64 v156, v156\n" \
"v_mfma_f32_16x16x32_bf16 v[128:131], a[100:103], a[4:7], v[128:131]\n" \
"s_add_u32 m0, %[m0b], 81920\n" \
"v_mfma_f32_16x16x32_bf16 v[128:131], a[104:107], a[8:11], v[128:131]\n" \
"v_exp_f32_e64 v157, v157\n" \
"v_mfma_f32_16x16x32_bf16 v[128:131], a[108:111], a[12:15], v[128:131]\n" \
"v_cvt_pk_bf16_f32 v220, v152, v153\n" \
"v_cvt_pk_bf16_f32 v221, v154, v155\n" \
"v_mfma_f32_16x16x32_bf16 v[132:135], a[124:127], a[12:15], v[160:163]\n" \
"v_exp_f32_e64 v158, v158\n" \
"v_mfma_f32_16x16x32_bf16 v[132:135], a[120:123], a[8:11], v[132:135]\n" \
"s_waitcnt lgkmcnt(0)\n" \
"v_mfma_f32_16x16x32_bf16 v[132:135], a[116:119], a[4:7], v[132:135]\n" \
"buffer_load_dwordx4 %[vd0], %[srdv], s48 offen offset:0 lds\n" \
"v_mfma_f32_16x16x32_bf16 v[132:135], a[112:115], a[0:3], v[132:135]\n" \
"v_exp_f32_e32 v159, v159\n" \
"v_mfma_f32_16x16x32_bf16 v[32:35], a[160:163], v[196:199], v[32:35]\n" \
"ds_read_b128 a[64:67], %[kb0] offset:49152\n" \
"v_mfma_f32_16x16x32_bf16 v[32:35], a[128:131], v[188:191], v[32:35]\n" \
"v_exp_f32_e64 v128, v128\n" \
"v_mfma_f32_16x16x32_bf16 v[36:39], a[164:167], v[196:199], v[36:39]\n" \
"v_cvt_pk_bf16_f32 v222, v156, v157\n" \
"v_cvt_pk_bf16_f32 v223, v158, v159\n" \
"v_mfma_f32_16x16x32_bf16 v[36:39], a[132:135], v[188:191], v[36:39]\n" \
"v_exp_f32_e64 v129, v129\n" \
"v_mfma_f32_16x16x32_bf16 v[40:43], a[168:171], v[196:199], v[40:43]\n" \
"ds_read_b128 a[68:71], v208 offset:49152\n" \
"v_mfma_f32_16x16x32_bf16 v[40:43], a[136:139], v[188:191], v[40:43]\n" \
"v_exp_f32_e64 v130, v130\n" \
"v_mfma_f32_16x16x32_bf16 v[44:47], a[172:175], v[196:199], v[44:47]\n" \
"ds_read_b128 a[72:75], v209 offset:49152\n" \
"v_mfma_f32_16x16x32_bf16 v[44:47], a[140:143], v[188:191], v[44:47]\n" \
"v_exp_f32_e64 v131, v131\n" \
"v_smfmac_f32_16x16x64_bf16 v[176:179], a[192:195], v[184:191], v228\n" \
"v_mfma_f32_16x16x32_bf16 v[136:139], a[96:99], a[16:19], v[164:167]\n" \
"v_exp_f32_e64 v132, v132\n" \
"v_mfma_f32_16x16x32_bf16 v[136:139], a[100:103], a[20:23], v[136:139]\n" \
"ds_read_b128 a[76:79], v210 offset:49152\n" \
"v_mfma_f32_16x16x32_bf16 v[136:139], a[104:107], a[24:27], v[136:139]\n" \
"v_exp_f32_e64 v133, v133\n" \
"v_mfma_f32_16x16x32_bf16 v[136:139], a[108:111], a[28:31], v[136:139]\n" \
"v_cvt_pk_bf16_f32 v192, v128, v129\n" \
"v_cvt_pk_bf16_f32 v193, v130, v131\n" \
"v_mfma_f32_16x16x32_bf16 v[140:143], a[124:127], a[28:31], v[164:167]\n" \
"v_exp_f32_e64 v134, v134\n" \
"v_mfma_f32_16x16x32_bf16 v[140:143], a[120:123], a[24:27], v[140:143]\n" \
"ds_read_b128 a[92:95], v210 offset:53248\n" \
"v_mfma_f32_16x16x32_bf16 v[140:143], a[116:119], a[20:23], v[140:143]\n" \
"buffer_load_dwordx4 v215, %[srdv], s48 offen offset:1024 lds\n" \
"v_mfma_f32_16x16x32_bf16 v[140:143], a[112:115], a[16:19], v[140:143]\n" \
"v_exp_f32_e64 v135, v135\n" \
"v_mfma_f32_16x16x32_bf16 v[48:51], a[176:179], v[196:199], v[48:51]\n" \
"ds_read_b128 a[88:91], v209 offset:53248\n" \
"v_mfma_f32_16x16x32_bf16 v[48:51], a[144:147], v[188:191], v[48:51]\n" \
"v_exp_f32_e64 v136, v136\n" \
"v_mfma_f32_16x16x32_bf16 v[52:55], a[180:183], v[196:199], v[52:55]\n" \
"v_cvt_pk_bf16_f32 v194, v132, v133\n" \
"v_cvt_pk_bf16_f32 v195, v134, v135\n" \
"v_mfma_f32_16x16x32_bf16 v[52:55], a[148:151], v[188:191], v[52:55]\n" \
"v_exp_f32_e64 v137, v137\n" \
"v_mfma_f32_16x16x32_bf16 v[56:59], a[184:187], v[196:199], v[56:59]\n" \
"v_exp_f32_e64 v138, v138\n" \
"v_mfma_f32_16x16x32_bf16 v[56:59], a[152:155], v[188:191], v[56:59]\n" \
"ds_read_b128 a[84:87], v208 offset:53248\n" \
"v_mfma_f32_16x16x32_bf16 v[60:63], a[188:191], v[196:199], v[60:63]\n" \
"v_exp_f32_e64 v139, v139\n" \
"v_mfma_f32_16x16x32_bf16 v[60:63], a[156:159], v[188:191], v[60:63]\n" \
"s_cmp_lg_u32 %[wid], 1\n" \
"s_cbranch_scc1 LtB5_%=\n" \
"global_load_dword v237, v236, s[58:59]\n" \
"s_add_u32 s61, s48, 49152\n" \
"s_mov_b32 exec_hi, 0\n" \
"s_nop 0\n" \
"buffer_load_dword v237, v224, %[srdp], s61 offen\n" \
"s_mov_b32 exec_hi, -1\n" \
"s_nop 0\n" \
"LtB5_%=:\n" \
"v_mfma_f32_16x16x32_bf16 v[144:147], a[96:99], a[32:35], v[168:171]\n" \
"v_exp_f32_e64 v140, v140\n" \
"v_mfma_f32_16x16x32_bf16 v[144:147], a[100:103], a[36:39], v[144:147]\n" \
"ds_read_b128 a[80:83], %[kb0] offset:53248\n" \
"v_mfma_f32_16x16x32_bf16 v[144:147], a[104:107], a[40:43], v[144:147]\n" \
"v_exp_f32_e64 v141, v141\n" \
"v_mfma_f32_16x16x32_bf16 v[144:147], a[108:111], a[44:47], v[144:147]\n" \
"v_cvt_pk_bf16_f32 v196, v136, v137\n" \
"v_cvt_pk_bf16_f32 v197, v138, v139\n" \
"v_mfma_f32_16x16x32_bf16 v[148:151], a[124:127], a[44:47], v[168:171]\n" \
"v_exp_f32_e64 v142, v142\n" \
"v_mfma_f32_16x16x32_bf16 v[148:151], a[120:123], a[40:43], v[148:151]\n" \
"v_mfma_f32_16x16x32_bf16 v[148:151], a[116:119], a[36:39], v[148:151]\n" \
"buffer_load_dwordx4 %[vd0], %[srdv], s48 offen offset:2048 lds\n" \
"v_mfma_f32_16x16x32_bf16 v[148:151], a[112:115], a[32:35], v[148:151]\n" \
"v_exp_f32_e64 v143, v143\n" \
"v_mfma_f32_16x16x32_bf16 v[96:99], a[160:163], v[204:207], v[96:99]\n" \
"ds_read_b64_tr_b16 a[160:161], %[vvb] offset:40960\n" \
"v_mfma_f32_16x16x32_bf16 v[96:99], a[128:131], v[220:223], v[96:99]\n" \
"v_exp_f32_e64 v144, v144\n" \
"v_mfma_f32_16x16x32_bf16 v[100:103], a[164:167], v[204:207], v[100:103]\n" \
"v_cvt_pk_bf16_f32 v198, v140, v141\n" \
"v_cvt_pk_bf16_f32 v199, v142, v143\n" \
"v_mfma_f32_16x16x32_bf16 v[100:103], a[132:135], v[220:223], v[100:103]\n" \
"v_exp_f32_e64 v145, v145\n" \
"v_mfma_f32_16x16x32_bf16 v[104:107], a[168:171], v[204:207], v[104:107]\n" \
"ds_read_b64_tr_b16 a[162:163], %[vvb] offset:45056\n" \
"ds_read_b64_tr_b16 a[164:165], v211 offset:40960\n" \
"v_mfma_f32_16x16x32_bf16 v[104:107], a[136:139], v[220:223], v[104:107]\n" \
"v_exp_f32_e64 v146, v146\n" \
"v_mfma_f32_16x16x32_bf16 v[108:111], a[172:175], v[204:207], v[108:111]\n" \
"ds_read_b64_tr_b16 a[166:167], v211 offset:45056\n" \
"v_mfma_f32_16x16x32_bf16 v[108:111], a[140:143], v[220:223], v[108:111]\n" \
"v_exp_f32_e64 v147, v147\n" \
"v_smfmac_f32_16x16x64_bf16 v[180:183], a[192:195], v[216:223], v228\n" \
"ds_read_b64_tr_b16 a[168:169], %[vvb] offset:41472\n" \
"v_mfma_f32_16x16x32_bf16 v[152:155], a[96:99], a[48:51], v[172:175]\n" \
"v_exp_f32_e64 v148, v148\n" \
"v_mfma_f32_16x16x32_bf16 v[152:155], a[100:103], a[52:55], v[152:155]\n" \
"ds_read_b64_tr_b16 a[170:171], %[vvb] offset:45568\n" \
"v_mfma_f32_16x16x32_bf16 v[152:155], a[104:107], a[56:59], v[152:155]\n" \
"v_exp_f32_e64 v149, v149\n" \
"v_mfma_f32_16x16x32_bf16 v[152:155], a[108:111], a[60:63], v[152:155]\n" \
"v_cvt_pk_bf16_f32 v200, v144, v145\n" \
"v_cvt_pk_bf16_f32 v201, v146, v147\n" \
"v_mfma_f32_16x16x32_bf16 v[156:159], a[124:127], a[60:63], v[172:175]\n" \
"v_exp_f32_e64 v150, v150\n" \
"v_mfma_f32_16x16x32_bf16 v[156:159], a[120:123], a[56:59], v[156:159]\n" \
"ds_read_b64_tr_b16 a[172:173], v211 offset:41472\n" \
"v_mfma_f32_16x16x32_bf16 v[156:159], a[116:119], a[52:55], v[156:159]\n" \
"buffer_load_dwordx4 v215, %[srdv], s48 offen offset:3072 lds\n" \
"v_mfma_f32_16x16x32_bf16 v[156:159], a[112:115], a[48:51], v[156:159]\n" \
"v_exp_f32_e64 v151, v151\n" \
"v_mfma_f32_16x16x32_bf16 v[112:115], a[176:179], v[204:207], v[112:115]\n" \
"ds_read_b64_tr_b16 a[174:175], v211 offset:45568\n" \
"ds_read_b64_tr_b16 a[176:177], %[vvb] offset:41984\n" \
"v_mfma_f32_16x16x32_bf16 v[112:115], a[144:147], v[220:223], v[112:115]\n" \
"v_exp_f32_e64 v152, v152\n" \
"v_mfma_f32_16x16x32_bf16 v[116:119], a[180:183], v[204:207], v[116:119]\n" \
"v_cvt_pk_bf16_f32 v202, v148, v149\n" \
"v_cvt_pk_bf16_f32 v203, v150, v151\n" \
"v_mfma_f32_16x16x32_bf16 v[116:119], a[148:151], v[220:223], v[116:119]\n" \
"v_exp_f32_e64 v153, v153\n" \
"v_mfma_f32_16x16x32_bf16 v[120:123], a[184:187], v[204:207], v[120:123]\n" \
"v_exp_f32_e64 v154, v154\n" \
"v_mfma_f32_16x16x32_bf16 v[120:123], a[152:155], v[220:223], v[120:123]\n" \
"ds_read_b64_tr_b16 a[178:179], %[vvb] offset:46080\n" \
"v_mfma_f32_16x16x32_bf16 v[124:127], a[188:191], v[204:207], v[124:127]\n" \
"v_exp_f32_e64 v155, v155\n" \
"v_mfma_f32_16x16x32_bf16 v[124:127], a[156:159], v[220:223], v[124:127]\n" \
"ds_read_b64_tr_b16 a[180:181], v211 offset:41984\n" \
"s_waitcnt lgkmcnt(13)\n" \
"v_mfma_f32_16x16x32_bf16 v[128:131], a[64:67], a[0:3], v[160:163]\n" \
"v_exp_f32_e32 v156, v156\n" \
"v_mfma_f32_16x16x32_bf16 v[128:131], a[68:71], a[4:7], v[128:131]\n" \
"ds_read_b64_tr_b16 a[182:183], v211 offset:46080\n" \
"s_add_u32 m0, %[m0b], 32768\n" \
"v_mfma_f32_16x16x32_bf16 v[128:131], a[72:75], a[8:11], v[128:131]\n" \
"v_exp_f32_e64 v157, v157\n" \
"v_mfma_f32_16x16x32_bf16 v[128:131], a[76:79], a[12:15], v[128:131]\n" \
"v_cvt_pk_bf16_f32 v204, v152, v153\n" \
"v_cvt_pk_bf16_f32 v205, v154, v155\n" \
"v_mfma_f32_16x16x32_bf16 v[132:135], a[92:95], a[12:15], v[160:163]\n" \
"v_exp_f32_e64 v158, v158\n" \
"v_mfma_f32_16x16x32_bf16 v[132:135], a[88:91], a[8:11], v[132:135]\n" \
"ds_read_b64_tr_b16 a[184:185], %[vvb] offset:42496\n" \
"s_waitcnt lgkmcnt(5)\n" \
"v_mfma_f32_16x16x32_bf16 v[132:135], a[84:87], a[4:7], v[132:135]\n" \
"buffer_load_dwordx4 %[kd0], %[srdk], s48 offen offset:0 lds\n" \
"v_mfma_f32_16x16x32_bf16 v[132:135], a[80:83], a[0:3], v[132:135]\n" \
"v_exp_f32_e32 v159, v159\n" \
"v_mfma_f32_16x16x32_bf16 v[64:67], a[128:131], v[216:219], v[64:67]\n" \
"ds_read_b64_tr_b16 a[186:187], %[vvb] offset:46592\n" \
"ds_read_b64_tr_b16 a[188:189], v211 offset:42496\n" \
"v_mfma_f32_16x16x32_bf16 v[64:67], a[160:163], v[200:203], v[64:67]\n" \
"v_exp_f32_e64 v128, v128\n" \
"v_mfma_f32_16x16x32_bf16 v[68:71], a[132:135], v[216:219], v[68:71]\n" \
"v_cvt_pk_bf16_f32 v206, v156, v157\n" \
"v_cvt_pk_bf16_f32 v207, v158, v159\n" \
"v_mfma_f32_16x16x32_bf16 v[68:71], a[164:167], v[200:203], v[68:71]\n" \
"v_exp_f32_e64 v129, v129\n" \
"v_mfma_f32_16x16x32_bf16 v[72:75], a[136:139], v[216:219], v[72:75]\n" \
"ds_read_b64_tr_b16 a[190:191], v211 offset:46592\n" \
"ds_read_b64_tr_b16 a[128:129], %[vvb] offset:49152\n" \
"v_mfma_f32_16x16x32_bf16 v[72:75], a[168:171], v[200:203], v[72:75]\n" \
"v_exp_f32_e64 v130, v130\n" \
"v_mfma_f32_16x16x32_bf16 v[76:79], a[140:143], v[216:219], v[76:79]\n" \
"ds_read_b64_tr_b16 a[130:131], %[vvb] offset:53248\n" \
"ds_read_b64_tr_b16 a[132:133], v211 offset:49152\n" \
"v_mfma_f32_16x16x32_bf16 v[76:79], a[172:175], v[200:203], v[76:79]\n" \
"v_exp_f32_e64 v131, v131\n" \
"v_smfmac_f32_16x16x64_bf16 v[176:179], a[192:195], v[192:199], v228\n" \
"ds_read_b64_tr_b16 a[134:135], v211 offset:53248\n" \
"ds_read_b64_tr_b16 a[136:137], %[vvb] offset:49664\n" \
"buffer_store_dwordx4 a[196:199], v235, %[srdz], s62 offen offset:1024 sc1\n" \
"v_mfma_f32_16x16x32_bf16 v[136:139], a[64:67], a[16:19], v[164:167]\n" \
"v_exp_f32_e64 v132, v132\n" \
"v_mfma_f32_16x16x32_bf16 v[136:139], a[68:71], a[20:23], v[136:139]\n" \
"ds_read_b64_tr_b16 a[138:139], %[vvb] offset:53760\n" \
"ds_read_b64_tr_b16 a[140:141], v211 offset:49664\n" \
"v_mfma_f32_16x16x32_bf16 v[136:139], a[72:75], a[24:27], v[136:139]\n" \
"v_exp_f32_e64 v133, v133\n" \
"v_mfma_f32_16x16x32_bf16 v[136:139], a[76:79], a[28:31], v[136:139]\n" \
"v_cvt_pk_bf16_f32 v184, v128, v129\n" \
"v_cvt_pk_bf16_f32 v185, v130, v131\n" \
"v_mfma_f32_16x16x32_bf16 v[140:143], a[92:95], a[28:31], v[164:167]\n" \
"v_exp_f32_e64 v134, v134\n" \
"v_mfma_f32_16x16x32_bf16 v[140:143], a[88:91], a[24:27], v[140:143]\n" \
"ds_read_b64_tr_b16 a[142:143], v211 offset:53760\n" \
"s_waitcnt lgkmcnt(8)\n" \
"v_mfma_f32_16x16x32_bf16 v[140:143], a[84:87], a[20:23], v[140:143]\n" \
"buffer_load_dwordx4 v212, %[srdk], s48 offen offset:1024 lds\n" \
"v_mfma_f32_16x16x32_bf16 v[140:143], a[80:83], a[16:19], v[140:143]\n" \
"v_exp_f32_e32 v135, v135\n" \
"v_mfma_f32_16x16x32_bf16 v[80:83], a[144:147], v[216:219], v[80:83]\n" \
"ds_read_b64_tr_b16 a[144:145], %[vvb] offset:50176\n" \
"ds_read_b64_tr_b16 a[146:147], %[vvb] offset:54272\n" \
"v_mfma_f32_16x16x32_bf16 v[80:83], a[176:179], v[200:203], v[80:83]\n" \
"v_exp_f32_e64 v136, v136\n" \
"v_mfma_f32_16x16x32_bf16 v[84:87], a[148:151], v[216:219], v[84:87]\n" \
"v_cvt_pk_bf16_f32 v186, v132, v133\n" \
"v_cvt_pk_bf16_f32 v187, v134, v135\n" \
"v_mfma_f32_16x16x32_bf16 v[84:87], a[180:183], v[200:203], v[84:87]\n" \
"v_exp_f32_e64 v137, v137\n" \
"v_mfma_f32_16x16x32_bf16 v[88:91], a[152:155], v[216:219], v[88:91]\n" \
"v_exp_f32_e64 v138, v138\n" \
"v_mfma_f32_16x16x32_bf16 v[88:91], a[184:187], v[200:203], v[88:91]\n" \
"ds_read_b64_tr_b16 a[148:149], v211 offset:50176\n" \
"ds_read_b64_tr_b16 a[150:151], v211 offset:54272\n" \
"v_mfma_f32_16x16x32_bf16 v[92:95], a[156:159], v[216:219], v[92:95]\n" \
"v_exp_f32_e64 v139, v139\n" \
"v_mfma_f32_16x16x32_bf16 v[92:95], a[188:191], v[200:203], v[92:95]\n" \
"ds_read_b64_tr_b16 a[152:153], %[vvb] offset:50688\n" \
"global_load_dword v237, %[vct], %[pct]\n" \
"v_mfma_f32_16x16x32_bf16 v[144:147], a[64:67], a[32:35], v[168:171]\n" \
"v_exp_f32_e64 v140, v140\n" \
"v_mfma_f32_16x16x32_bf16 v[144:147], a[68:71], a[36:39], v[144:147]\n" \
"ds_read_b64_tr_b16 a[154:155], %[vvb] offset:54784\n" \
"ds_read_b64_tr_b16 a[156:157], v211 offset:50688\n" \
"v_mfma_f32_16x16x32_bf16 v[144:147], a[72:75], a[40:43], v[144:147]\n" \
"v_exp_f32_e64 v141, v141\n" \
"v_mfma_f32_16x16x32_bf16 v[144:147], a[76:79], a[44:47], v[144:147]\n" \
"v_cvt_pk_bf16_f32 v188, v136, v137\n" \
"v_cvt_pk_bf16_f32 v189, v138, v139\n" \
"v_mfma_f32_16x16x32_bf16 v[148:151], a[92:95], a[44:47], v[168:171]\n" \
"v_exp_f32_e64 v142, v142\n" \
"v_mfma_f32_16x16x32_bf16 v[148:151], a[88:91], a[40:43], v[148:151]\n" \
"ds_read_b64_tr_b16 a[158:159], v211 offset:54784\n" \
"s_waitcnt lgkmcnt(8)\n" \
"v_mfma_f32_16x16x32_bf16 v[148:151], a[84:87], a[36:39], v[148:151]\n" \
"buffer_load_dwordx4 v213, %[srdk], s48 offen offset:2048 lds\n" \
"v_mfma_f32_16x16x32_bf16 v[148:151], a[80:83], a[32:35], v[148:151]\n" \
"v_exp_f32_e32 v143, v143\n" \
"v_mfma_f32_16x16x32_bf16 v[0:3], a[160:163], v[192:195], v[0:3]\n" \
"ds_read_b128 a[96:99], %[kb0] offset:57344\n" \
"v_mfma_f32_16x16x32_bf16 v[0:3], a[128:131], v[184:187], v[0:3]\n" \
"v_exp_f32_e64 v144, v144\n" \
"v_mfma_f32_16x16x32_bf16 v[4:7], a[164:167], v[192:195], v[4:7]\n" \
"v_cvt_pk_bf16_f32 v190, v140, v141\n" \
"v_cvt_pk_bf16_f32 v191, v142, v143\n" \
"v_mfma_f32_16x16x32_bf16 v[4:7], a[132:135], v[184:187], v[4:7]\n" \
"v_exp_f32_e64 v145, v145\n" \
"v_mfma_f32_16x16x32_bf16 v[8:11], a[168:171], v[192:195], v[8:11]\n" \
"ds_read_b128 a[100:103], v208 offset:57344\n" \
"v_mfma_f32_16x16x32_bf16 v[8:11], a[136:139], v[184:187], v[8:11]\n" \
"v_exp_f32_e64 v146, v146\n" \
"v_mfma_f32_16x16x32_bf16 v[12:15], a[172:175], v[192:195], v[12:15]\n" \
"ds_read_b128 a[104:107], v209 offset:57344\n" \
"v_mfma_f32_16x16x32_bf16 v[12:15], a[140:143], v[184:187], v[12:15]\n" \
"v_exp_f32_e64 v147, v147\n" \
"v_smfmac_f32_16x16x64_bf16 v[180:183], a[192:195], v[200:207], v228\n" \
"ds_read_b128 a[108:111], v210 offset:57344\n" \
"v_mfma_f32_16x16x32_bf16 v[152:155], a[64:67], a[48:51], v[172:175]\n" \
"v_exp_f32_e64 v148, v148\n" \
"v_mfma_f32_16x16x32_bf16 v[152:155], a[68:71], a[52:55], v[152:155]\n" \
"ds_read_b128 a[124:127], v210 offset:61440\n" \
"v_mfma_f32_16x16x32_bf16 v[152:155], a[72:75], a[56:59], v[152:155]\n" \
"v_exp_f32_e64 v149, v149\n" \
"v_mfma_f32_16x16x32_bf16 v[152:155], a[76:79], a[60:63], v[152:155]\n" \
"v_cvt_pk_bf16_f32 v216, v144, v145\n" \
"v_cvt_pk_bf16_f32 v217, v146, v147\n" \
"v_mfma_f32_16x16x32_bf16 v[156:159], a[92:95], a[60:63], v[172:175]\n" \
"v_exp_f32_e64 v150, v150\n" \
"v_mfma_f32_16x16x32_bf16 v[156:159], a[88:91], a[56:59], v[156:159]\n" \
"ds_read_b128 a[120:123], v209 offset:61440\n" \
"s_waitcnt lgkmcnt(6)\n" \
"v_mfma_f32_16x16x32_bf16 v[156:159], a[84:87], a[52:55], v[156:159]\n" \
"buffer_load_dwordx4 v214, %[srdk], s48 offen offset:3072 lds\n" \
"v_mfma_f32_16x16x32_bf16 v[156:159], a[80:83], a[48:51], v[156:159]\n" \
"v_exp_f32_e32 v151, v151\n" \
"v_mfma_f32_16x16x32_bf16 v[16:19], a[176:179], v[192:195], v[16:19]\n" \
"ds_read_b128 a[116:119], v208 offset:61440\n" \
"v_mfma_f32_16x16x32_bf16 v[16:19], a[144:147], v[184:187], v[16:19]\n" \
"v_exp_f32_e64 v152, v152\n" \
"v_mfma_f32_16x16x32_bf16 v[20:23], a[180:183], v[192:195], v[20:23]\n" \
"v_cvt_pk_bf16_f32 v218, v148, v149\n" \
"v_cvt_pk_bf16_f32 v219, v150, v151\n" \
"v_mfma_f32_16x16x32_bf16 v[20:23], a[148:151], v[184:187], v[20:23]\n" \
"v_exp_f32_e64 v153, v153\n" \
"v_mfma_f32_16x16x32_bf16 v[24:27], a[184:187], v[192:195], v[24:27]\n" \
"v_exp_f32_e64 v154, v154\n" \
"v_mfma_f32_16x16x32_bf16 v[24:27], a[152:155], v[184:187], v[24:27]\n" \
"ds_read_b128 a[112:115], %[kb0] offset:61440\n" \
"s_add_u32 s48, s48, 0x4000\n" \
"s_mov_b32 m0, s44\n" \
"s_addk_i32 s44, 0x400\n" \
"buffer_load_dwordx4 v229, s[64:67], s45 offen offset:0 lds\n" \
"s_add_u32 s45, s45, 229376\n" \
"v_mfma_f32_16x16x32_bf16 v[28:31], a[188:191], v[192:195], v[28:31]\n" \
"v_exp_f32_e64 v155, v155\n" \
"v_mfma_f32_16x16x32_bf16 v[28:31], a[156:159], v[184:187], v[28:31]\n" \
"s_waitcnt vmcnt(16) lgkmcnt(2)\n" \
"s_barrier\n" \
"v_mfma_f32_16x16x32_bf16 v[128:131], a[96:99], a[0:3], v[160:163]\n" \
"v_exp_f32_e64 v156, v156\n" \
"v_mfma_f32_16x16x32_bf16 v[128:131], a[100:103], a[4:7], v[128:131]\n" \
"s_add_u32 m0, %[m0b], 98304\n" \
"v_mfma_f32_16x16x32_bf16 v[128:131], a[104:107], a[8:11], v[128:131]\n" \
"v_exp_f32_e64 v157, v157\n" \
"v_mfma_f32_16x16x32_bf16 v[128:131], a[108:111], a[12:15], v[128:131]\n" \
"v_cvt_pk_bf16_f32 v220, v152, v153\n" \
"v_cvt_pk_bf16_f32 v221, v154, v155\n" \
"v_mfma_f32_16x16x32_bf16 v[132:135], a[124:127], a[12:15], v[160:163]\n" \
"v_exp_f32_e64 v158, v158\n" \
"v_mfma_f32_16x16x32_bf16 v[132:135], a[120:123], a[8:11], v[132:135]\n" \
"s_waitcnt lgkmcnt(0)\n" \
"v_mfma_f32_16x16x32_bf16 v[132:135], a[116:119], a[4:7], v[132:135]\n" \
"buffer_load_dwordx4 %[vd0], %[srdv], s48 offen offset:0 lds\n" \
"v_mfma_f32_16x16x32_bf16 v[132:135], a[112:115], a[0:3], v[132:135]\n" \
"v_exp_f32_e32 v159, v159\n" \
"v_mfma_f32_16x16x32_bf16 v[32:35], a[160:163], v[196:199], v[32:35]\n" \
"ds_read_b128 a[64:67], %[kb0] offset:0\n" \
"v_mfma_f32_16x16x32_bf16 v[32:35], a[128:131], v[188:191], v[32:35]\n" \
"v_exp_f32_e64 v128, v128\n" \
"v_mfma_f32_16x16x32_bf16 v[36:39], a[164:167], v[196:199], v[36:39]\n" \
"v_cvt_pk_bf16_f32 v222, v156, v157\n" \
"v_cvt_pk_bf16_f32 v223, v158, v159\n" \
"v_mfma_f32_16x16x32_bf16 v[36:39], a[132:135], v[188:191], v[36:39]\n" \
"v_exp_f32_e64 v129, v129\n" \
"v_mfma_f32_16x16x32_bf16 v[40:43], a[168:171], v[196:199], v[40:43]\n" \
"ds_read_b128 a[68:71], v208 offset:0\n" \
"v_mfma_f32_16x16x32_bf16 v[40:43], a[136:139], v[188:191], v[40:43]\n" \
"v_exp_f32_e64 v130, v130\n" \
"v_mfma_f32_16x16x32_bf16 v[44:47], a[172:175], v[196:199], v[44:47]\n" \
"ds_read_b128 a[72:75], v209 offset:0\n" \
"v_mfma_f32_16x16x32_bf16 v[44:47], a[140:143], v[188:191], v[44:47]\n" \
"v_exp_f32_e64 v131, v131\n" \
"v_smfmac_f32_16x16x64_bf16 v[176:179], a[192:195], v[184:191], v228\n" \
"buffer_store_dwordx4 a[196:199], v235, %[srdz], s63 offen offset:2048 sc1\n" \
"s_add_u32 s63, s63, 3072\n" \
"v_mfma_f32_16x16x32_bf16 v[136:139], a[96:99], a[16:19], v[164:167]\n" \
"v_exp_f32_e64 v132, v132\n" \
"v_mfma_f32_16x16x32_bf16 v[136:139], a[100:103], a[20:23], v[136:139]\n" \
"ds_read_b128 a[76:79], v210 offset:0\n" \
"v_mfma_f32_16x16x32_bf16 v[136:139], a[104:107], a[24:27], v[136:139]\n" \
"v_exp_f32_e64 v133, v133\n" \
"v_mfma_f32_16x16x32_bf16 v[136:139], a[108:111], a[28:31], v[136:139]\n" \
"v_cvt_pk_bf16_f32 v192, v128, v129\n" \
"v_cvt_pk_bf16_f32 v193, v130, v131\n" \
"v_mfma_f32_16x16x32_bf16 v[140:143], a[124:127], a[28:31], v[164:167]\n" \
"v_exp_f32_e64 v134, v134\n" \
"v_mfma_f32_16x16x32_bf16 v[140:143], a[120:123], a[24:27], v[140:143]\n" \
"ds_read_b128 a[92:95], v210 offset:4096\n" \
"v_mfma_f32_16x16x32_bf16 v[140:143], a[116:119], a[20:23], v[140:143]\n" \
"buffer_load_dwordx4 v215, %[srdv], s48 offen offset:1024 lds\n" \
"v_mfma_f32_16x16x32_bf16 v[140:143], a[112:115], a[16:19], v[140:143]\n" \
"v_exp_f32_e64 v135, v135\n" \
"v_mfma_f32_16x16x32_bf16 v[48:51], a[176:179], v[196:199], v[48:51]\n" \
"ds_read_b128 a[88:91], v209 offset:4096\n" \
"v_mfma_f32_16x16x32_bf16 v[48:51], a[144:147], v[188:191], v[48:51]\n" \
"v_exp_f32_e64 v136, v136\n" \
"v_mfma_f32_16x16x32_bf16 v[52:55], a[180:183], v[196:199], v[52:55]\n" \
"v_cvt_pk_bf16_f32 v194, v132, v133\n" \
"v_cvt_pk_bf16_f32 v195, v134, v135\n" \
"v_mfma_f32_16x16x32_bf16 v[52:55], a[148:151], v[188:191], v[52:55]\n" \
"v_exp_f32_e64 v137, v137\n" \
"v_mfma_f32_16x16x32_bf16 v[56:59], a[184:187], v[196:199], v[56:59]\n" \
"v_exp_f32_e64 v138, v138\n" \
"v_mfma_f32_16x16x32_bf16 v[56:59], a[152:155], v[188:191], v[56:59]\n" \
"ds_read_b128 a[84:87], v208 offset:4096\n" \
"v_mfma_f32_16x16x32_bf16 v[60:63], a[188:191], v[196:199], v[60:63]\n" \
"v_exp_f32_e64 v139, v139\n" \
"v_mfma_f32_16x16x32_bf16 v[60:63], a[156:159], v[188:191], v[60:63]\n" \
"s_cmp_lg_u32 %[wid], 2\n" \
"s_cbranch_scc1 LtB7_%=\n" \
"global_load_dword v237, v236, s[58:59]\n" \
"s_add_u32 s61, s48, 49152\n" \
"s_mov_b32 exec_hi, 0\n" \
"s_nop 0\n" \
"buffer_load_dword v237, v224, %[srdp], s61 offen\n" \
"s_mov_b32 exec_hi, -1\n" \
"s_nop 0\n" \
"LtB7_%=:\n" \
"v_mfma_f32_16x16x32_bf16 v[144:147], a[96:99], a[32:35], v[168:171]\n" \
"v_exp_f32_e64 v140, v140\n" \
"v_mfma_f32_16x16x32_bf16 v[144:147], a[100:103], a[36:39], v[144:147]\n" \
"ds_read_b128 a[80:83], %[kb0] offset:4096\n" \
"v_mfma_f32_16x16x32_bf16 v[144:147], a[104:107], a[40:43], v[144:147]\n" \
"v_exp_f32_e64 v141, v141\n" \
"v_mfma_f32_16x16x32_bf16 v[144:147], a[108:111], a[44:47], v[144:147]\n" \
"v_cvt_pk_bf16_f32 v196, v136, v137\n" \
"v_cvt_pk_bf16_f32 v197, v138, v139\n" \
"v_mfma_f32_16x16x32_bf16 v[148:151], a[124:127], a[44:47], v[168:171]\n" \
"v_exp_f32_e64 v142, v142\n" \
"v_mfma_f32_16x16x32_bf16 v[148:151], a[120:123], a[40:43], v[148:151]\n" \
"v_mfma_f32_16x16x32_bf16 v[148:151], a[116:119], a[36:39], v[148:151]\n" \
"buffer_load_dwordx4 %[vd0], %[srdv], s48 offen offset:2048 lds\n" \
"v_mfma_f32_16x16x32_bf16 v[148:151], a[112:115], a[32:35], v[148:151]\n" \
"v_exp_f32_e64 v143, v143\n" \
"v_mfma_f32_16x16x32_bf16 v[96:99], a[160:163], v[204:207], v[96:99]\n" \
"ds_read_b64_tr_b16 a[160:161], %[vvb] offset:57344\n" \
"v_mfma_f32_16x16x32_bf16 v[96:99], a[128:131], v[220:223], v[96:99]\n" \
"v_exp_f32_e64 v144, v144\n" \
"v_mfma_f32_16x16x32_bf16 v[100:103], a[164:167], v[204:207], v[100:103]\n" \
"v_cvt_pk_bf16_f32 v198, v140, v141\n" \
"v_cvt_pk_bf16_f32 v199, v142, v143\n" \
"v_mfma_f32_16x16x32_bf16 v[100:103], a[132:135], v[220:223], v[100:103]\n" \
"v_exp_f32_e64 v145, v145\n" \
"v_mfma_f32_16x16x32_bf16 v[104:107], a[168:171], v[204:207], v[104:107]\n" \
"ds_read_b64_tr_b16 a[162:163], %[vvb] offset:61440\n" \
"ds_read_b64_tr_b16 a[164:165], v211 offset:57344\n" \
"v_mfma_f32_16x16x32_bf16 v[104:107], a[136:139], v[220:223], v[104:107]\n" \
"v_exp_f32_e64 v146, v146\n" \
"v_mfma_f32_16x16x32_bf16 v[108:111], a[172:175], v[204:207], v[108:111]\n" \
"ds_read_b64_tr_b16 a[166:167], v211 offset:61440\n" \
"v_mfma_f32_16x16x32_bf16 v[108:111], a[140:143], v[220:223], v[108:111]\n" \
"v_exp_f32_e64 v147, v147\n" \
"v_smfmac_f32_16x16x64_bf16 v[180:183], a[192:195], v[216:223], v228\n" \
"ds_read_b64_tr_b16 a[168:169], %[vvb] offset:57856\n" \
"v_mfma_f32_16x16x32_bf16 v[152:155], a[96:99], a[48:51], v[172:175]\n" \
"v_exp_f32_e64 v148, v148\n" \
"v_mfma_f32_16x16x32_bf16 v[152:155], a[100:103], a[52:55], v[152:155]\n" \
"ds_read_b64_tr_b16 a[170:171], %[vvb] offset:61952\n" \
"v_mfma_f32_16x16x32_bf16 v[152:155], a[104:107], a[56:59], v[152:155]\n" \
"v_exp_f32_e64 v149, v149\n" \
"v_mfma_f32_16x16x32_bf16 v[152:155], a[108:111], a[60:63], v[152:155]\n" \
"v_cvt_pk_bf16_f32 v200, v144, v145\n" \
"v_cvt_pk_bf16_f32 v201, v146, v147\n" \
"v_mfma_f32_16x16x32_bf16 v[156:159], a[124:127], a[60:63], v[172:175]\n" \
"v_exp_f32_e64 v150, v150\n" \
"v_mfma_f32_16x16x32_bf16 v[156:159], a[120:123], a[56:59], v[156:159]\n" \
"ds_read_b64_tr_b16 a[172:173], v211 offset:57856\n" \
"v_mfma_f32_16x16x32_bf16 v[156:159], a[116:119], a[52:55], v[156:159]\n" \
"buffer_load_dwordx4 v215, %[srdv], s48 offen offset:3072 lds\n" \
"v_mfma_f32_16x16x32_bf16 v[156:159], a[112:115], a[48:51], v[156:159]\n" \
"v_exp_f32_e64 v151, v151\n" \
"v_mfma_f32_16x16x32_bf16 v[112:115], a[176:179], v[204:207], v[112:115]\n" \
"ds_read_b64_tr_b16 a[174:175], v211 offset:61952\n" \
"ds_read_b64_tr_b16 a[176:177], %[vvb] offset:58368\n" \
"v_mfma_f32_16x16x32_bf16 v[112:115], a[144:147], v[220:223], v[112:115]\n" \
"v_exp_f32_e64 v152, v152\n" \
"v_mfma_f32_16x16x32_bf16 v[116:119], a[180:183], v[204:207], v[116:119]\n" \
"v_cvt_pk_bf16_f32 v202, v148, v149\n" \
"v_cvt_pk_bf16_f32 v203, v150, v151\n" \
".if . - adaln_body != 28672\n" \
".error \"size model\"\n" \
".endif\n" \
"Lk6_%=:\n" \
"s_cbranch_execz Lk7_%=\n" \
"s_nop 0\n" \
"v_mfma_f32_16x16x32_bf16 v[116:119], a[148:151], v[220:223], v[116:119]\n" \
"v_exp_f32_e64 v153, v153\n" \
"v_mfma_f32_16x16x32_bf16 v[120:123], a[184:187], v[204:207], v[120:123]\n" \
"v_exp_f32_e64 v154, v154\n" \
"v_mfma_f32_16x16x32_bf16 v[120:123], a[152:155], v[220:223], v[120:123]\n" \
"ds_read_b64_tr_b16 a[178:179], %[vvb] offset:62464\n" \
"v_mfma_f32_16x16x32_bf16 v[124:127], a[188:191], v[204:207], v[124:127]\n" \
"v_exp_f32_e64 v155, v155\n" \
"v_mfma_f32_16x16x32_bf16 v[124:127], a[156:159], v[220:223], v[124:127]\n" \
"ds_read_b64_tr_b16 a[180:181], v211 offset:58368\n" \
"s_waitcnt lgkmcnt(13)\n" \
"v_mfma_f32_16x16x32_bf16 v[128:131], a[64:67], a[0:3], v[160:163]\n" \
"v_exp_f32_e32 v156, v156\n" \
"v_mfma_f32_16x16x32_bf16 v[128:131], a[68:71], a[4:7], v[128:131]\n" \
"ds_read_b64_tr_b16 a[182:183], v211 offset:62464\n" \
"s_add_u32 m0, %[m0b], 49152\n" \
"v_mfma_f32_16x16x32_bf16 v[128:131], a[72:75], a[8:11], v[128:131]\n" \
"v_exp_f32_e64 v157, v157\n" \
"v_mfma_f32_16x16x32_bf16 v[128:131], a[76:79], a[12:15], v[128:131]\n" \
"v_cvt_pk_bf16_f32 v204, v152, v153\n" \
"v_cvt_pk_bf16_f32 v205, v154, v155\n" \
"v_mfma_f32_16x16x32_bf16 v[132:135], a[92:95], a[12:15], v[160:163]\n" \
"v_exp_f32_e64 v158, v158\n" \
"v_mfma_f32_16x16x32_bf16 v[132:135], a[88:91], a[8:11], v[132:135]\n" \
"ds_read_b64_tr_b16 a[184:185], %[vvb] offset:58880\n" \
"s_waitcnt lgkmcnt(5)\n" \
"v_mfma_f32_16x16x32_bf16 v[132:135], a[84:87], a[4:7], v[132:135]\n" \
"buffer_load_dwordx4 %[kd0], %[srdk], s48 offen offset:0 lds\n" \
"v_mfma_f32_16x16x32_bf16 v[132:135], a[80:83], a[0:3], v[132:135]\n" \
"v_exp_f32_e32 v159, v159\n" \
"v_mfma_f32_16x16x32_bf16 v[64:67], a[128:131], v[216:219], v[64:67]\n" \
"ds_read_b64_tr_b16 a[186:187], %[vvb] offset:62976\n" \
"ds_read_b64_tr_b16 a[188:189], v211 offset:58880\n" \
"v_mfma_f32_16x16x32_bf16 v[64:67], a[160:163], v[200:203], v[64:67]\n" \
"v_exp_f32_e64 v128, v128\n" \
"v_mfma_f32_16x16x32_bf16 v[68:71], a[132:135], v[216:219], v[68:71]\n" \
"v_cvt_pk_bf16_f32 v206, v156, v157\n" \
"v_cvt_pk_bf16_f32 v207, v158, v159\n" \
"v_mfma_f32_16x16x32_bf16 v[68:71], a[164:167], v[200:203], v[68:71]\n" \
"v_exp_f32_e64 v129, v129\n" \
"v_mfma_f32_16x16x32_bf16 v[72:75], a[136:139], v[216:219], v[72:75]\n" \
"ds_read_b64_tr_b16 a[190:191], v211 offset:62976\n" \
"ds_read_b64_tr_b16 a[128:129], %[vvb] offset:0\n" \
"v_mfma_f32_16x16x32_bf16 v[72:75], a[168:171], v[200:203], v[72:75]\n" \
"v_exp_f32_e64 v130, v130\n" \
"v_mfma_f32_16x16x32_bf16 v[76:79], a[140:143], v[216:219], v[76:79]\n" \
"ds_read_b64_tr_b16 a[130:131], %[vvb] offset:4096\n" \
"ds_read_b64_tr_b16 a[132:133], v211 offset:0\n" \
"v_mfma_f32_16x16x32_bf16 v[76:79], a[172:175], v[200:203], v[76:79]\n" \
"v_exp_f32_e64 v131, v131\n" \
"v_smfmac_f32_16x16x64_bf16 v[176:179], a[192:195], v[192:199], v228\n" \
"ds_read_b64_tr_b16 a[134:135], v211 offset:4096\n" \
"ds_read_b64_tr_b16 a[136:137], %[vvb] offset:512\n" \
"v_mfma_f32_16x16x32_bf16 v[136:139], a[64:67], a[16:19], v[164:167]\n" \
"v_exp_f32_e64 v132, v132\n" \
"v_mfma_f32_16x16x32_bf16 v[136:139], a[68:71], a[20:23], v[136:139]\n" \
"ds_read_b64_tr_b16 a[138:139], %[vvb] offset:4608\n" \
"ds_read_b64_tr_b16 a[140:141], v211 offset:512\n" \
"v_mfma_f32_16x16x32_bf16 v[136:139], a[72:75], a[24:27], v[136:139]\n" \
"v_exp_f32_e64 v133, v133\n" \
"v_mfma_f32_16x16x32_bf16 v[136:139], a[76:79], a[28:31], v[136:139]\n" \
"v_cvt_pk_bf16_f32 v184, v128, v129\n" \
"v_cvt_pk_bf16_f32 v185, v130, v131\n" \
"v_mfma_f32_16x16x32_bf16 v[140:143], a[92:95], a[28:31], v[164:167]\n" \
"v_exp_f32_e64 v134, v134\n" \
"v_mfma_f32_16x16x32_bf16 v[140:143], a[88:91], a[24:27], v[140:143]\n" \
"ds_read_b64_tr_b16 a[142:143], v211 offset:4608\n" \
"s_waitcnt lgkmcnt(8)\n" \
"v_mfma_f32_16x16x32_bf16 v[140:143], a[84:87], a[20:23], v[140:143]\n" \
"buffer_load_dwordx4 v212, %[srdk], s48 offen offset:1024 lds\n" \
"v_mfma_f32_16x16x32_bf16 v[140:143], a[80:83], a[16:19], v[140:143]\n" \
"v_exp_f32_e32 v135, v135\n" \
"v_mfma_f32_16x16x32_bf16 v[80:83], a[144:147], v[216:219], v[80:83]\n" \
"ds_read_b64_tr_b16 a[144:145], %[vvb] offset:1024\n" \
"ds_read_b64_tr_b16 a[146:147], %[vvb] offset:5120\n" \
"v_mfma_f32_16x16x32_bf16 v[80:83], a[176:179], v[200:203], v[80:83]\n" \
"v_exp_f32_e64 v136, v136\n" \
"v_mfma_f32_16x16x32_bf16 v[84:87], a[148:151], v[216:219], v[84:87]\n" \
"v_cvt_pk_bf16_f32 v186, v132, v133\n" \
"v_cvt_pk_bf16_f32 v187, v134, v135\n" \
"v_mfma_f32_16x16x32_bf16 v[84:87], a[180:183], v[200:203], v[84:87]\n" \
"v_exp_f32_e64 v137, v137\n" \
"v_mfma_f32_16x16x32_bf16 v[88:91], a[152:155], v[216:219], v[88:91]\n" \
"v_exp_f32_e64 v138, v138\n" \
"v_mfma_f32_16x16x32_bf16 v[88:91], a[184:187], v[200:203], v[88:91]\n" \
"ds_read_b64_tr_b16 a[148:149], v211 offset:1024\n" \
"ds_read_b64_tr_b16 a[150:151], v211 offset:5120\n" \
"v_mfma_f32_16x16x32_bf16 v[92:95], a[156:159], v[216:219], v[92:95]\n" \
"v_exp_f32_e64 v139, v139\n" \
"v_mfma_f32_16x16x32_bf16 v[92:95], a[188:191], v[200:203], v[92:95]\n" \
"ds_read_b64_tr_b16 a[152:153], %[vvb] offset:1536\n" \
"buffer_store_dwordx4 a[196:199], v235, %[srdz], s62 offen offset:2048 sc1\n" \
"s_add_u32 s62, s62, 3072\n" \
"v_mfma_f32_16x16x32_bf16 v[144:147], a[64:67], a[32:35], v[168:171]\n" \
"v_exp_f32_e64 v140, v140\n" \
"v_mfma_f32_16x16x32_bf16 v[144:147], a[68:71], a[36:39], v[144:147]\n" \
"ds_read_b64_tr_b16 a[154:155], %[vvb] offset:5632\n" \
"ds_read_b64_tr_b16 a[156:157], v211 offset:1536\n" \
"v_mfma_f32_16x16x32_bf16 v[144:147], a[72:75], a[40:43], v[144:147]\n" \
"v_exp_f32_e64 v141, v141\n" \
"v_mfma_f32_16x16x32_bf16 v[144:147], a[76:79], a[44:47], v[144:147]\n" \
"v_cvt_pk_bf16_f32 v188, v136, v137\n" \
"v_cvt_pk_bf16_f32 v189, v138, v139\n" \
"v_mfma_f32_16x16x32_bf16 v[148:151], a[92:95], a[44:47], v[168:171]\n" \
"v_exp_f32_e64 v142, v142\n" \
"v_mfma_f32_16x16x32_bf16 v[148:151], a[88:91], a[40:43], v[148:151]\n" \
"ds_read_b64_tr_b16 a[158:159], v211 offset:5632\n" \
"s_waitcnt lgkmcnt(8)\n" \
"v_mfma_f32_16x16x32_bf16 v[148:151], a[84:87], a[36:39], v[148:151]\n" \
"buffer_load_dwordx4 v213, %[srdk], s48 offen offset:2048 lds\n" \
"v_mfma_f32_16x16x32_bf16 v[148:151], a[80:83], a[32:35], v[148:151]\n" \
"v_exp_f32_e32 v143, v143\n" \
"v_mfma_f32_16x16x32_bf16 v[0:3], a[160:163], v[192:195], v[0:3]\n" \
"ds_read_b128 a[96:99], %[kb0] offset:8192\n" \
"v_mfma_f32_16x16x32_bf16 v[0:3], a[128:131], v[184:187], v[0:3]\n" \
"v_exp_f32_e64 v144, v144\n" \
"v_mfma_f32_16x16x32_bf16 v[4:7], a[164:167], v[192:195], v[4:7]\n" \
"v_cvt_pk_bf16_f32 v190, v140, v141\n" \
"v_cvt_pk_bf16_f32 v191, v142, v143\n" \
"v_mfma_f32_16x16x32_bf16 v[4:7], a[132:135], v[184:187], v[4:7]\n" \
"v_exp_f32_e64 v145, v145\n" \
"v_mfma_f32_16x16x32_bf16 v[8:11], a[168:171], v[192:195], v[8:11]\n" \
"ds_read_b128 a[100:103], v208 offset:8192\n" \
"v_mfma_f32_16x16x32_bf16 v[8:11], a[136:139], v[184:187], v[8:11]\n" \
"v_exp_f32_e64 v146, v146\n" \
"v_mfma_f32_16x16x32_bf16 v[12:15], a[172:175], v[192:195], v[12:15]\n" \
"ds_read_b128 a[104:107], v209 offset:8192\n" \
"v_mfma_f32_16x16x32_bf16 v[12:15], a[140:143], v[184:187], v[12:15]\n" \
"v_exp_f32_e64 v147, v147\n" \
"v_smfmac_f32_16x16x64_bf16 v[180:183], a[192:195], v[200:207], v228\n" \
"ds_read_b128 a[108:111], v210 offset:8192\n" \
"v_mfma_f32_16x16x32_bf16 v[152:155], a[64:67], a[48:51], v[172:175]\n" \
"v_exp_f32_e64 v148, v148\n" \
"v_mfma_f32_16x16x32_bf16 v[152:155], a[68:71], a[52:55], v[152:155]\n" \
"ds_read_b128 a[124:127], v210 offset:12288\n" \
"v_mfma_f32_16x16x32_bf16 v[152:155], a[72:75], a[56:59], v[152:155]\n" \
"v_exp_f32_e64 v149, v149\n" \
"v_mfma_f32_16x16x32_bf16 v[152:155], a[76:79], a[60:63], v[152:155]\n" \
"v_cvt_pk_bf16_f32 v216, v144, v145\n" \
"v_cvt_pk_bf16_f32 v217, v146, v147\n" \
"v_mfma_f32_16x16x32_bf16 v[156:159], a[92:95], a[60:63], v[172:175]\n" \
"v_exp_f32_e64 v150, v150\n" \
"v_mfma_f32_16x16x32_bf16 v[156:159], a[88:91], a[56:59], v[156:159]\n" \
"ds_read_b128 a[120:123], v209 offset:12288\n" \
"s_waitcnt lgkmcnt(6)\n" \
"v_mfma_f32_16x16x32_bf16 v[156:159], a[84:87], a[52:55], v[156:159]\n" \
"buffer_load_dwordx4 v214, %[srdk], s48 offen offset:3072 lds\n" \
"v_mfma_f32_16x16x32_bf16 v[156:159], a[80:83], a[48:51], v[156:159]\n" \
"v_exp_f32_e32 v151, v151\n" \
"v_mfma_f32_16x16x32_bf16 v[16:19], a[176:179], v[192:195], v[16:19]\n" \
"ds_read_b128 a[116:119], v208 offset:12288\n" \
"v_mfma_f32_16x16x32_bf16 v[16:19], a[144:147], v[184:187], v[16:19]\n" \
"v_exp_f32_e64 v152, v152\n" \
"v_mfma_f32_16x16x32_bf16 v[20:23], a[180:183], v[192:195], v[20:23]\n" \
"v_cvt_pk_bf16_f32 v218, v148, v149\n" \
"v_cvt_pk_bf16_f32 v219, v150, v151\n" \
"v_mfma_f32_16x16x32_bf16 v[20:23], a[148:151], v[184:187], v[20:23]\n" \
"v_exp_f32_e64 v153, v153\n" \
"v_mfma_f32_16x16x32_bf16 v[24:27], a[184:187], v[192:195], v[24:27]\n" \
"v_exp_f32_e64 v154, v154\n" \
"v_mfma_f32_16x16x32_bf16 v[24:27], a[152:155], v[184:187], v[24:27]\n" \
"ds_read_b128 a[112:115], %[kb0] offset:12288\n" \
"s_add_u32 s48, s48, 0x4000\n" \
"v_mfma_f32_16x16x32_bf16 v[28:31], a[188:191], v[192:195], v[28:31]\n" \
"v_exp_f32_e64 v155, v155\n" \
"v_mfma_f32_16x16x32_bf16 v[28:31], a[156:159], v[184:187], v[28:31]\n" \
"s_waitcnt vmcnt(17) lgkmcnt(2)\n" \
"s_barrier\n" \
"v_mfma_f32_16x16x32_bf16 v[128:131], a[96:99], a[0:3], v[160:163]\n" \
"v_exp_f32_e64 v156, v156\n" \
"v_mfma_f32_16x16x32_bf16 v[128:131], a[100:103], a[4:7], v[128:131]\n" \
"s_add_u32 m0, %[m0b], 114688\n" \
"v_mfma_f32_16x16x32_bf16 v[128:131], a[104:107], a[8:11], v[128:131]\n" \
"v_exp_f32_e64 v157, v157\n" \
"v_mfma_f32_16x16x32_bf16 v[128:131], a[108:111], a[12:15], v[128:131]\n" \
"v_cvt_pk_bf16_f32 v220, v152, v153\n" \
"v_cvt_pk_bf16_f32 v221, v154, v155\n" \
"v_mfma_f32_16x16x32_bf16 v[132:135], a[124:127], a[12:15], v[160:163]\n" \
"v_exp_f32_e64 v158, v158\n" \
"v_mfma_f32_16x16x32_bf16 v[132:135], a[120:123], a[8:11], v[132:135]\n" \
"s_waitcnt lgkmcnt(0)\n" \
"v_mfma_f32_16x16x32_bf16 v[132:135], a[116:119], a[4:7], v[132:135]\n" \
"buffer_load_dwordx4 %[vd0], %[srdv], s48 offen offset:0 lds\n" \
"v_mfma_f32_16x16x32_bf16 v[132:135], a[112:115], a[0:3], v[132:135]\n" \
"v_exp_f32_e32 v159, v159\n" \
"v_mfma_f32_16x16x32_bf16 v[32:35], a[160:163], v[196:199], v[32:35]\n" \
"ds_read_b128 a[64:67], %[kb0] offset:16384\n" \
"v_mfma_f32_16x16x32_bf16 v[32:35], a[128:131], v[188:191], v[32:35]\n" \
"v_exp_f32_e64 v128, v128\n" \
"v_mfma_f32_16x16x32_bf16 v[36:39], a[164:167], v[196:199], v[36:39]\n" \
"v_cvt_pk_bf16_f32 v222, v156, v157\n" \
"v_cvt_pk_bf16_f32 v223, v158, v159\n" \
"v_mfma_f32_16x16x32_bf16 v[36:39], a[132:135], v[188:191], v[36:39]\n" \
"v_exp_f32_e64 v129, v129\n" \
"v_mfma_f32_16x16x32_bf16 v[40:43], a[168:171], v[196:199], v[40:43]\n" \
"ds_read_b128 a[68:71], v208 offset:16384\n" \
"v_mfma_f32_16x16x32_bf16 v[40:43], a[136:139], v[188:191], v[40:43]\n" \
"v_exp_f32_e64 v130, v130\n" \
"v_mfma_f32_16x16x32_bf16 v[44:47], a[172:175], v[196:199], v[44:47]\n" \
"ds_read_b128 a[72:75], v209 offset:16384\n" \
"v_mfma_f32_16x16x32_bf16 v[44:47], a[140:143], v[188:191], v[44:47]\n" \
"v_exp_f32_e64 v131, v131\n" \
"v_smfmac_f32_16x16x64_bf16 v[176:179], a[192:195], v[184:191], v228\n" \
"v_mfma_f32_16x16x32_bf16 v[136:139], a[96:99], a[16:19], v[164:167]\n" \
"v_exp_f32_e64 v132, v132\n" \
"v_mfma_f32_16x16x32_bf16 v[136:139], a[100:103], a[20:23], v[136:139]\n" \
"ds_read_b128 a[76:79], v210 offset:16384\n" \
"v_mfma_f32_16x16x32_bf16 v[136:139], a[104:107], a[24:27], v[136:139]\n" \
"v_exp_f32_e64 v133, v133\n" \
"v_mfma_f32_16x16x32_bf16 v[136:139], a[108:111], a[28:31], v[136:139]\n" \
"v_cvt_pk_bf16_f32 v192, v128, v129\n" \
"v_cvt_pk_bf16_f32 v193, v130, v131\n" \
"v_mfma_f32_16x16x32_bf16 v[140:143], a[124:127], a[28:31], v[164:167]\n" \
"v_exp_f32_e64 v134, v134\n" \
"v_mfma_f32_16x16x32_bf16 v[140:143], a[120:123], a[24:27], v[140:143]\n" \
"ds_read_b128 a[92:95], v210 offset:20480\n" \
"v_mfma_f32_16x16x32_bf16 v[140:143], a[116:119], a[20:23], v[140:143]\n" \
"buffer_load_dwordx4 v215, %[srdv], s48 offen offset:1024 lds\n" \
"v_mfma_f32_16x16x32_bf16 v[140:143], a[112:115], a[16:19], v[140:143]\n" \
"v_exp_f32_e64 v135, v135\n" \
"v_mfma_f32_16x16x32_bf16 v[48:51], a[176:179], v[196:199], v[48:51]\n" \
"ds_read_b128 a[88:91], v209 offset:20480\n" \
"v_mfma_f32_16x16x32_bf16 v[48:51], a[144:147], v[188:191], v[48:51]\n" \
"v_exp_f32_e64 v136, v136\n" \
"v_mfma_f32_16x16x32_bf16 v[52:55], a[180:183], v[196:199], v[52:55]\n" \
"v_cvt_pk_bf16_f32 v194, v132, v133\n" \
"v_cvt_pk_bf16_f32 v195, v134, v135\n" \
"v_mfma_f32_16x16x32_bf16 v[52:55], a[148:151], v[188:191], v[52:55]\n" \
"v_exp_f32_e64 v137, v137\n" \
"v_mfma_f32_16x16x32_bf16 v[56:59], a[184:187], v[196:199], v[56:59]\n" \
"v_exp_f32_e64 v138, v138\n" \
"v_mfma_f32_16x16x32_bf16 v[56:59], a[152:155], v[188:191], v[56:59]\n" \
"ds_read_b128 a[84:87], v208 offset:20480\n" \
"v_mfma_f32_16x16x32_bf16 v[60:63], a[188:191], v[196:199], v[60:63]\n" \
"v_exp_f32_e64 v139, v139\n" \
"v_mfma_f32_16x16x32_bf16 v[60:63], a[156:159], v[188:191], v[60:63]\n" \
"s_cmp_lg_u32 %[wid], 3\n" \
"s_cbranch_scc1 LtB1_%=\n" \
"global_load_dword v237, v236, s[58:59]\n" \
"s_add_u32 s61, s48, 49152\n" \
"s_mov_b32 exec_hi, 0\n" \
"s_nop 0\n" \
"buffer_load_dword v237, v224, %[srdp], s61 offen\n" \
"s_mov_b32 exec_hi, -1\n" \
"s_nop 0\n" \
"LtB1_%=:\n" \
"v_mfma_f32_16x16x32_bf16 v[144:147], a[96:99], a[32:35], v[168:171]\n" \
"v_exp_f32_e64 v140, v140\n" \
"v_mfma_f32_16x16x32_bf16 v[144:147], a[100:103], a[36:39], v[144:147]\n" \
"ds_read_b128 a[80:83], %[kb0] offset:20480\n" \
"v_mfma_f32_16x16x32_bf16 v[144:147], a[104:107], a[40:43], v[144:147]\n" \
"v_exp_f32_e64 v141, v141\n" \
"v_mfma_f32_16x16x32_bf16 v[144:147], a[108:111], a[44:47], v[144:147]\n" \
"v_cvt_pk_bf16_f32 v196, v136, v137\n" \
"v_cvt_pk_bf16_f32 v197, v138, v139\n" \
"v_mfma_f32_16x16x32_bf16 v[148:151], a[124:127], a[44:47], v[168:171]\n" \
"v_exp_f32_e64 v142, v142\n" \
"v_mfma_f32_16x16x32_bf16 v[148:151], a[120:123], a[40:43], v[148:151]\n" \
"v_mfma_f32_16x16x32_bf16 v[148:151], a[116:119], a[36:39], v[148:151]\n" \
"buffer_load_dwordx4 %[vd0], %[srdv], s48 offen offset:2048 lds\n" \
"v_mfma_f32_16x16x32_bf16 v[148:151], a[112:115], a[32:35], v[148:151]\n" \
"v_exp_f32_e64 v143, v143\n" \
"v_mfma_f32_16x16x32_bf16 v[96:99], a[160:163], v[204:207], v[96:99]\n" \
"ds_read_b64_tr_b16 a[160:161], %[vvb] offset:8192\n" \
"v_mfma_f32_16x16x32_bf16 v[96:99], a[128:131], v[220:223], v[96:99]\n" \
"v_exp_f32_e64 v144, v144\n" \
"v_mfma_f32_16x16x32_bf16 v[100:103], a[164:167], v[204:207], v[100:103]\n" \
"v_cvt_pk_bf16_f32 v198, v140, v141\n" \
"v_cvt_pk_bf16_f32 v199, v142, v143\n" \
"v_mfma_f32_16x16x32_bf16 v[100:103], a[132:135], v[220:223], v[100:103]\n" \
"v_exp_f32_e64 v145, v145\n" \
"v_mfma_f32_16x16x32_bf16 v[104:107], a[168:171], v[204:207], v[104:107]\n" \
"ds_read_b64_tr_b16 a[162:163], %[vvb] offset:12288\n" \
"ds_read_b64_tr_b16 a[164:165], v211 offset:8192\n" \
"v_mfma_f32_16x16x32_bf16 v[104:107], a[136:139], v[220:223], v[104:107]\n" \
"v_exp_f32_e64 v146, v146\n" \
"v_mfma_f32_16x16x32_bf16 v[108:111], a[172:175], v[204:207], v[108:111]\n" \
"ds_read_b64_tr_b16 a[166:167], v211 offset:12288\n" \
"v_mfma_f32_16x16x32_bf16 v[108:111], a[140:143], v[220:223], v[108:111]\n" \
"v_exp_f32_e64 v147, v147\n" \
"v_smfmac_f32_16x16x64_bf16 v[180:183], a[192:195], v[216:223], v228\n" \
"ds_read_b64_tr_b16 a[168:169], %[vvb] offset:8704\n" \
"v_mfma_f32_16x16x32_bf16 v[152:155], a[96:99], a[48:51], v[172:175]\n" \
"v_exp_f32_e64 v148, v148\n" \
"v_mfma_f32_16x16x32_bf16 v[152:155], a[100:103], a[52:55], v[152:155]\n" \
"ds_read_b64_tr_b16 a[170:171], %[vvb] offset:12800\n" \
"v_mfma_f32_16x16x32_bf16 v[152:155], a[104:107], a[56:59], v[152:155]\n" \
"v_exp_f32_e64 v149, v149\n" \
"v_mfma_f32_16x16x32_bf16 v[152:155], a[108:111], a[60:63], v[152:155]\n" \
"v_cvt_pk_bf16_f32 v200, v144, v145\n" \
"v_cvt_pk_bf16_f32 v201, v146, v147\n" \
"v_mfma_f32_16x16x32_bf16 v[156:159], a[124:127], a[60:63], v[172:175]\n" \
"v_exp_f32_e64 v150, v150\n" \
"v_mfma_f32_16x16x32_bf16 v[156:159], a[120:123], a[56:59], v[156:159]\n" \
"ds_read_b64_tr_b16 a[172:173], v211 offset:8704\n" \
"v_mfma_f32_16x16x32_bf16 v[156:159], a[116:119], a[52:55], v[156:159]\n" \
"buffer_load_dwordx4 v215, %[srdv], s48 offen offset:3072 lds\n" \
"v_mfma_f32_16x16x32_bf16 v[156:159], a[112:115], a[48:51], v[156:159]\n" \
"v_exp_f32_e64 v151, v151\n" \
"v_mfma_f32_16x16x32_bf16 v[112:115], a[176:179], v[204:207], v[112:115]\n" \
"ds_read_b64_tr_b16 a[174:175], v211 offset:12800\n" \
"ds_read_b64_tr_b16 a[176:177], %[vvb] offset:9216\n" \
"v_mfma_f32_16x16x32_bf16 v[112:115], a[144:147], v[220:223], v[112:115]\n" \
"v_exp_f32_e64 v152, v152\n" \
"v_mfma_f32_16x16x32_bf16 v[116:119], a[180:183], v[204:207], v[116:119]\n" \
"v_cvt_pk_bf16_f32 v202, v148, v149\n" \
"v_cvt_pk_bf16_f32 v203, v150, v151\n" \
"v_mfma_f32_16x16x32_bf16 v[116:119], a[148:151], v[220:223], v[116:119]\n" \
"v_exp_f32_e64 v153, v153\n" \
"v_mfma_f32_16x16x32_bf16 v[120:123], a[184:187], v[204:207], v[120:123]\n" \
"v_exp_f32_e64 v154, v154\n" \
"v_mfma_f32_16x16x32_bf16 v[120:123], a[152:155], v[220:223], v[120:123]\n" \
"ds_read_b64_tr_b16 a[178:179], %[vvb] offset:13312\n" \
"v_mfma_f32_16x16x32_bf16 v[124:127], a[188:191], v[204:207], v[124:127]\n" \
"v_exp_f32_e64 v155, v155\n" \
"v_mfma_f32_16x16x32_bf16 v[124:127], a[156:159], v[220:223], v[124:127]\n" \
"ds_read_b64_tr_b16 a[180:181], v211 offset:9216\n" \
"s_waitcnt lgkmcnt(13)\n" \
"s_cmp_lg_u32 s60, 7\n" \
"s_cbranch_scc1 LmB_%=\n" \
"s_nop 0\n" \
"v_readlane_b32 s48, %[tlo], 11\n" \
"v_readlane_b32 s58, %[tlo], 12\n" \
"v_mfma_f32_16x16x32_bf16 v[128:131], a[64:67], a[0:3], v[160:163]\n" \
"v_exp_f32_e64 v156, v156\n" \
"v_mfma_f32_16x16x32_bf16 v[128:131], a[68:71], a[4:7], v[128:131]\n" \
"ds_read_b64_tr_b16 a[182:183], v211 offset:13312\n" \
"s_add_u32 m0, %[m0b], 0\n" \
"s_mov_b32 s49, s48\n" \
"v_mfma_f32_16x16x32_bf16 v[128:131], a[72:75], a[8:11], v[128:131]\n" \
"v_exp_f32_e64 v157, v157\n" \
"v_mfma_f32_16x16x32_bf16 v[128:131], a[76:79], a[12:15], v[128:131]\n" \
"v_cvt_pk_bf16_f32 v204, v152, v153\n" \
"v_cvt_pk_bf16_f32 v205, v154, v155\n" \
"v_mfma_f32_16x16x32_bf16 v[132:135], a[92:95], a[12:15], v[160:163]\n" \
"v_exp_f32_e64 v158, v158\n" \
"v_mfma_f32_16x16x32_bf16 v[132:135], a[88:91], a[8:11], v[132:135]\n" \
"ds_read_b64_tr_b16 a[184:185], %[vvb] offset:9728\n" \
"s_lshl_b32 s49, s49, 18\n" \
"s_or_b32 s49, s49, s58\n" \
"s_waitcnt lgkmcnt(5)\n" \
"v_mfma_f32_16x16x32_bf16 v[132:135], a[84:87], a[4:7], v[132:135]\n" \
"buffer_load_dwordx4 %[wo0], %[srdt], s49 offen offset:0 lds\n" \
"v_mfma_f32_16x16x32_bf16 v[132:135], a[80:83], a[0:3], v[132:135]\n" \
"v_exp_f32_e32 v159, v159\n" \
"v_mfma_f32_16x16x32_bf16 v[64:67], a[128:131], v[216:219], v[64:67]\n" \
"ds_read_b64_tr_b16 a[186:187], %[vvb] offset:13824\n" \
"ds_read_b64_tr_b16 a[188:189], v211 offset:9728\n" \
"v_readlane_b32 s60, %[thi], 11\n" \
"v_mfma_f32_16x16x32_bf16 v[64:67], a[160:163], v[200:203], v[64:67]\n" \
"v_exp_f32_e64 v128, v128\n" \
"v_mfma_f32_16x16x32_bf16 v[68:71], a[132:135], v[216:219], v[68:71]\n" \
"v_cvt_pk_bf16_f32 v206, v156, v157\n" \
"v_cvt_pk_bf16_f32 v207, v158, v159\n" \
"v_mfma_f32_16x16x32_bf16 v[68:71], a[164:167], v[200:203], v[68:71]\n" \
"v_exp_f32_e64 v129, v129\n" \
"v_mfma_f32_16x16x32_bf16 v[72:75], a[136:139], v[216:219], v[72:75]\n" \
"ds_read_b64_tr_b16 a[190:191], v211 offset:13824\n" \
"ds_read_b64_tr_b16 a[128:129], %[vvb] offset:16384\n" \
"v_readlane_b32 s59, %[thi], 12\n" \
"v_mfma_f32_16x16x32_bf16 v[72:75], a[168:171], v[200:203], v[72:75]\n" \
"v_exp_f32_e64 v130, v130\n" \
"v_mfma_f32_16x16x32_bf16 v[76:79], a[140:143], v[216:219], v[76:79]\n" \
"ds_read_b64_tr_b16 a[130:131], %[vvb] offset:20480\n" \
"ds_read_b64_tr_b16 a[132:133], v211 offset:16384\n" \
"v_readlane_b32 s53, %[tlo], 13\n" \
"v_mfma_f32_16x16x32_bf16 v[76:79], a[172:175], v[200:203], v[76:79]\n" \
"v_exp_f32_e64 v131, v131\n" \
"v_smfmac_f32_16x16x64_bf16 v[176:179], a[192:195], v[192:199], v228\n" \
"ds_read_b64_tr_b16 a[134:135], v211 offset:20480\n" \
"ds_read_b64_tr_b16 a[136:137], %[vvb] offset:16896\n" \
"v_readlane_b32 s50, %[thi], 13\n" \
"v_mfma_f32_16x16x32_bf16 v[136:139], a[64:67], a[16:19], v[164:167]\n" \
"v_exp_f32_e64 v132, v132\n" \
"v_mfma_f32_16x16x32_bf16 v[136:139], a[68:71], a[20:23], v[136:139]\n" \
"ds_read_b64_tr_b16 a[138:139], %[vvb] offset:20992\n" \
"ds_read_b64_tr_b16 a[140:141], v211 offset:16896\n" \
"v_readlane_b32 s46, %[tlo], 14\n" \
"v_add_u32_e32 v212, 0x3c00, %[wo0]\n" \
"v_mfma_f32_16x16x32_bf16 v[136:139], a[72:75], a[24:27], v[136:139]\n" \
"v_exp_f32_e64 v133, v133\n" \
"v_mfma_f32_16x16x32_bf16 v[136:139], a[76:79], a[28:31], v[136:139]\n" \
"v_cvt_pk_bf16_f32 v184, v128, v129\n" \
"v_cvt_pk_bf16_f32 v185, v130, v131\n" \
"v_mfma_f32_16x16x32_bf16 v[140:143], a[92:95], a[28:31], v[164:167]\n" \
"v_exp_f32_e64 v134, v134\n" \
"v_mfma_f32_16x16x32_bf16 v[140:143], a[88:91], a[24:27], v[140:143]\n" \
"ds_read_b64_tr_b16 a[142:143], v211 offset:20992\n" \
"v_readlane_b32 s47, %[thi], 14\n" \
"v_add_u32_e32 v213, 0x7800, %[wo0]\n" \
"s_waitcnt lgkmcnt(8)\n" \
"v_mfma_f32_16x16x32_bf16 v[140:143], a[84:87], a[20:23], v[140:143]\n" \
"buffer_load_dwordx4 v212, %[srdt], s49 offen offset:1024 lds\n" \
"v_mfma_f32_16x16x32_bf16 v[140:143], a[80:83], a[16:19], v[140:143]\n" \
"v_exp_f32_e32 v135, v135\n" \
"v_mfma_f32_16x16x32_bf16 v[80:83], a[144:147], v[216:219], v[80:83]\n" \
"ds_read_b64_tr_b16 a[144:145], %[vvb] offset:17408\n" \
"ds_read_b64_tr_b16 a[146:147], %[vvb] offset:21504\n" \
"ds_read_b32 v230, %[rd0]\n" \
"v_mfma_f32_16x16x32_bf16 v[80:83], a[176:179], v[200:203], v[80:83]\n" \
"v_exp_f32_e64 v136, v136\n" \
"v_mfma_f32_16x16x32_bf16 v[84:87], a[148:151], v[216:219], v[84:87]\n" \
"v_cvt_pk_bf16_f32 v186, v132, v133\n" \
"v_cvt_pk_bf16_f32 v187, v134, v135\n" \
"v_mfma_f32_16x16x32_bf16 v[84:87], a[180:183], v[200:203], v[84:87]\n" \
"v_exp_f32_e64 v137, v137\n" \
"v_mfma_f32_16x16x32_bf16 v[88:91], a[152:155], v[216:219], v[88:91]\n" \
"v_exp_f32_e64 v138, v138\n" \
"v_mfma_f32_16x16x32_bf16 v[88:91], a[184:187], v[200:203], v[88:91]\n" \
"ds_read_b64_tr_b16 a[148:149], v211 offset:17408\n" \
"v_readlane_b32 s55, %[thi], 15\n" \
"ds_read_b64_tr_b16 a[150:151], v211 offset:21504\n" \
"v_lshrrev_b32_e64 v234, 5, v224\n" \
"v_mfma_f32_16x16x32_bf16 v[92:95], a[156:159], v[216:219], v[92:95]\n" \
"v_exp_f32_e64 v139, v139\n" \
"v_mfma_f32_16x16x32_bf16 v[92:95], a[188:191], v[200:203], v[92:95]\n" \
"ds_read_b64_tr_b16 a[152:153], %[vvb] offset:17920\n" \
"v_mfma_f32_16x16x32_bf16 v[144:147], a[64:67], a[32:35], v[168:171]\n" \
"v_exp_f32_e64 v140, v140\n" \
"v_mfma_f32_16x16x32_bf16 v[144:147], a[68:71], a[36:39], v[144:147]\n" \
"ds_read_b64_tr_b16 a[154:155], %[vvb] offset:22016\n" \
"ds_read_b64_tr_b16 a[156:157], v211 offset:17920\n" \
"s_waitcnt lgkmcnt(5)\n" \
"v_add_f32_dpp v230, v230, v230 row_ror:4 row_mask:0xf bank_mask:0xf\n" \
"v_mfma_f32_16x16x32_bf16 v[144:147], a[72:75], a[40:43], v[144:147]\n" \
"v_exp_f32_e32 v141, v141\n" \
"v_mfma_f32_16x16x32_bf16 v[144:147], a[76:79], a[44:47], v[144:147]\n" \
"v_cvt_pk_bf16_f32 v188, v136, v137\n" \
"v_cvt_pk_bf16_f32 v189, v138, v139\n" \
"v_mfma_f32_16x16x32_bf16 v[148:151], a[92:95], a[44:47], v[168:171]\n" \
"v_exp_f32_e64 v142, v142\n" \
"v_mfma_f32_16x16x32_bf16 v[148:151], a[88:91], a[40:43], v[148:151]\n" \
"ds_read_b64_tr_b16 a[158:159], v211 offset:22016\n" \
"v_add_f32_dpp v230, v230, v230 row_ror:8 row_mask:0xf bank_mask:0xf\n" \
"s_waitcnt lgkmcnt(9)\n" \
"v_mfma_f32_16x16x32_bf16 v[148:151], a[84:87], a[36:39], v[148:151]\n" \
"buffer_load_dwordx4 v213, %[srdt], s49 offen offset:2048 lds\n" \
"v_mfma_f32_16x16x32_bf16 v[148:151], a[80:83], a[32:35], v[148:151]\n" \
"v_exp_f32_e32 v143, v143\n" \
"v_mfma_f32_16x16x32_bf16 v[0:3], a[160:163], v[192:195], v[0:3]\n" \
"ds_read_b128 a[96:99], %[kb0] offset:24576\n" \
"v_cvt_pk_bf16_f32 v230, v230, v230\n" \
"v_mfma_f32_16x16x32_bf16 v[0:3], a[128:131], v[184:187], v[0:3]\n" \
"v_exp_f32_e64 v144, v144\n" \
"v_mfma_f32_16x16x32_bf16 v[4:7], a[164:167], v[192:195], v[4:7]\n" \
"v_cvt_pk_bf16_f32 v190, v140, v141\n" \
"v_cvt_pk_bf16_f32 v191, v142, v143\n" \
"v_mfma_f32_16x16x32_bf16 v[4:7], a[132:135], v[184:187], v[4:7]\n" \
"v_exp_f32_e64 v145, v145\n" \
"v_mfma_f32_16x16x32_bf16 v[8:11], a[168:171], v[192:195], v[8:11]\n" \
"ds_read_b128 a[100:103], v208 offset:24576\n" \
"v_lshlrev_b32_e64 v230, 16, v230\n" \
"v_mfma_f32_16x16x32_bf16 v[8:11], a[136:139], v[184:187], v[8:11]\n" \
"v_exp_f32_e64 v146, v146\n" \
"v_mfma_f32_16x16x32_bf16 v[12:15], a[172:175], v[192:195], v[12:15]\n" \
"ds_read_b128 a[104:107], v209 offset:24576\n" \
"v_add_f32_e64 v230, s55, v230\n" \
"v_add_u32_e32 v214, 0xb400, %[wo0]\n" \
"v_mfma_f32_16x16x32_bf16 v[12:15], a[140:143], v[184:187], v[12:15]\n" \
"v_exp_f32_e64 v147, v147\n" \
"v_smfmac_f32_16x16x64_bf16 v[180:183], a[192:195], v[200:207], v228\n" \
"ds_read_b128 a[108:111], v210 offset:24576\n" \
"v_cvt_pk_bf16_f32 v230, v230, v230\n" \
"v_readlane_b32 s55, %[tlo], 15\n" \
"v_mfma_f32_16x16x32_bf16 v[152:155], a[64:67], a[48:51], v[172:175]\n" \
"v_exp_f32_e64 v148, v148\n" \
"v_mfma_f32_16x16x32_bf16 v[152:155], a[68:71], a[52:55], v[152:155]\n" \
"ds_read_b128 a[124:127], v210 offset:28672\n" \
"v_lshl_or_b32 v230, v230, 16, 1\n" \
"v_mfma_f32_16x16x32_bf16 v[152:155], a[72:75], a[56:59], v[152:155]\n" \
"v_exp_f32_e64 v149, v149\n" \
"v_mfma_f32_16x16x32_bf16 v[152:155], a[76:79], a[60:63], v[152:155]\n" \
"v_cvt_pk_bf16_f32 v216, v144, v145\n" \
"v_cvt_pk_bf16_f32 v217, v146, v147\n" \
"v_mfma_f32_16x16x32_bf16 v[156:159], a[92:95], a[60:63], v[172:175]\n" \
"v_exp_f32_e64 v150, v150\n" \
"v_mfma_f32_16x16x32_bf16 v[156:159], a[88:91], a[56:59], v[156:159]\n" \
"ds_read_b128 a[120:123], v209 offset:28672\n" \
"s_cmp_lg_u32 s52, 0\n" \
"s_cselect_b32 s55, 0x80000000, s55\n" \
"s_mov_b64 exec, 15\n" \
"buffer_store_dword v230, v234, %[srdz], s55 offen sc1\n" \
"s_mov_b64 exec, -1\n" \
"s_waitcnt lgkmcnt(6)\n" \
"v_mfma_f32_16x16x32_bf16 v[156:159], a[84:87], a[52:55], v[156:159]\n" \
"buffer_load_dwordx4 v214, %[srdt], s49 offen offset:3072 lds\n" \
"v_mfma_f32_16x16x32_bf16 v[156:159], a[80:83], a[48:51], v[156:159]\n" \
"v_exp_f32_e64 v151, v151\n" \
"v_mfma_f32_16x16x32_bf16 v[16:19], a[176:179], v[192:195], v[16:19]\n" \
"ds_read_b128 a[116:119], v208 offset:28672\n" \
"v_mfma_f32_16x16x32_bf16 v[16:19], a[144:147], v[184:187], v[16:19]\n" \
"v_exp_f32_e64 v152, v152\n" \
"v_mfma_f32_16x16x32_bf16 v[20:23], a[180:183], v[192:195], v[20:23]\n" \
"v_cvt_pk_bf16_f32 v218, v148, v149\n" \
"v_cvt_pk_bf16_f32 v219, v150, v151\n" \
"v_mfma_f32_16x16x32_bf16 v[20:23], a[148:151], v[184:187], v[20:23]\n" \
"v_exp_f32_e64 v153, v153\n" \
"v_mfma_f32_16x16x32_bf16 v[24:27], a[184:187], v[192:195], v[24:27]\n" \
"v_exp_f32_e64 v154, v154\n" \
"v_mfma_f32_16x16x32_bf16 v[24:27], a[152:155], v[184:187], v[24:27]\n" \
"ds_read_b128 a[112:115], %[kb0] offset:28672\n" \
"s_mov_b32 m0, s44\n" \
"s_addk_i32 s44, 0x400\n" \
"buffer_load_dwordx4 v229, s[64:67], s45 offen offset:0 lds\n" \
"s_add_u32 s45, s45, 229376\n" \
"v_mfma_f32_16x16x32_bf16 v[28:31], a[188:191], v[192:195], v[28:31]\n" \
"v_exp_f32_e64 v155, v155\n" \
"v_mfma_f32_16x16x32_bf16 v[28:31], a[156:159], v[184:187], v[28:31]\n" \
"s_waitcnt vmcnt(6) lgkmcnt(2)\n" \
"s_barrier\n" \
"v_mfma_f32_16x16x32_bf16 v[128:131], a[96:99], a[0:3], v[160:163]\n" \
"v_exp_f32_e64 v156, v156\n" \
"s_mov_b32 m0, s44\n" \
"s_addk_i32 s44, 0x400\n" \
"buffer_load_dwordx4 v229, s[64:67], s45 offen offset:0 lds\n" \
"s_add_u32 s45, s45, 229376\n" \
"v_readlane_b32 s64, %[tlo], 8\n" \
"v_readlane_b32 s65, %[thi], 8\n" \
"v_mfma_f32_16x16x32_bf16 v[128:131], a[100:103], a[4:7], v[128:131]\n" \
"v_mbcnt_lo_u32_b32 v231, -1, 0\n" \
"v_mfma_f32_16x16x32_bf16 v[128:131], a[104:107], a[8:11], v[128:131]\n" \
"v_exp_f32_e64 v157, v157\n" \
"v_mfma_f32_16x16x32_bf16 v[128:131], a[108:111], a[12:15], v[128:131]\n" \
"v_cvt_pk_bf16_f32 v220, v152, v153\n" \
"v_cvt_pk_bf16_f32 v221, v154, v155\n" \
"v_mfma_f32_16x16x32_bf16 v[132:135], a[124:127], a[12:15], v[160:163]\n" \
"v_exp_f32_e64 v158, v158\n" \
"v_mfma_f32_16x16x32_bf16 v[132:135], a[120:123], a[8:11], v[132:135]\n" \
"v_mbcnt_hi_u32_b32 v231, -1, v231\n" \
"s_waitcnt lgkmcnt(0)\n" \
"v_mfma_f32_16x16x32_bf16 v[132:135], a[116:119], a[4:7], v[132:135]\n" \
"s_add_u32 m0, %[m0b], 65536\n" \
"s_mov_b32 s61, 0\n" \
"buffer_load_dwordx4 %[vg0], s[64:67], s61 offen offset:0 lds\n" \
"v_mfma_f32_16x16x32_bf16 v[132:135], a[112:115], a[0:3], v[132:135]\n" \
"v_exp_f32_e64 v159, v159\n" \
".if . - adaln_body != 32768\n" \
".error \"size model\"\n" \
".endif\n" \
"Lk7_%=:\n" \
"s_cbranch_execz Lk8_%=\n" \
"s_nop 0\n" \
"v_mfma_f32_16x16x32_bf16 v[32:35], a[160:163], v[196:199], v[32:35]\n" \
"ds_read_b128 a[64:67], %[kb0] offset:32768\n" \
"v_and_b32_e64 v226, 31, v231\n" \
"v_mfma_f32_16x16x32_bf16 v[32:35], a[128:131], v[188:191], v[32:35]\n" \
"v_exp_f32_e64 v128, v128\n" \
"v_mfma_f32_16x16x32_bf16 v[36:39], a[164:167], v[196:199], v[36:39]\n" \
"v_cvt_pk_bf16_f32 v222, v156, v157\n" \
"v_cvt_pk_bf16_f32 v223, v158, v159\n" \
"v_mfma_f32_16x16x32_bf16 v[36:39], a[132:135], v[188:191], v[36:39]\n" \
"v_exp_f32_e64 v129, v129\n" \
"v_mfma_f32_16x16x32_bf16 v[40:43], a[168:171], v[196:199], v[40:43]\n" \
"ds_read_b128 a[68:71], v208 offset:32768\n" \
"v_lshlrev_b32_e64 v226, 2, v226\n" \
"v_mfma_f32_16x16x32_bf16 v[40:43], a[136:139], v[188:191], v[40:43]\n" \
"v_exp_f32_e64 v130, v130\n" \
"v_mfma_f32_16x16x32_bf16 v[44:47], a[172:175], v[196:199], v[44:47]\n" \
"ds_read_b128 a[72:75], v209 offset:32768\n" \
"v_cmp_gt_u32_e32 vcc, 1, v231\n" \
"v_mfma_f32_16x16x32_bf16 v[44:47], a[140:143], v[188:191], v[44:47]\n" \
"v_exp_f32_e32 v131, v131\n" \
"v_smfmac_f32_16x16x64_bf16 v[176:179], a[192:195], v[184:191], v228\n" \
"v_mov_b32_e32 v225, 0x80000000\n" \
"v_mfma_f32_16x16x32_bf16 v[136:139], a[96:99], a[16:19], v[164:167]\n" \
"v_exp_f32_e64 v132, v132\n" \
"v_mfma_f32_16x16x32_bf16 v[136:139], a[100:103], a[20:23], v[136:139]\n" \
"ds_read_b128 a[76:79], v210 offset:32768\n" \
"v_mfma_f32_16x16x32_bf16 v[136:139], a[104:107], a[24:27], v[136:139]\n" \
"v_exp_f32_e64 v133, v133\n" \
"v_mfma_f32_16x16x32_bf16 v[136:139], a[108:111], a[28:31], v[136:139]\n" \
"v_cvt_pk_bf16_f32 v192, v128, v129\n" \
"v_cvt_pk_bf16_f32 v193, v130, v131\n" \
"v_mfma_f32_16x16x32_bf16 v[140:143], a[124:127], a[28:31], v[164:167]\n" \
"v_exp_f32_e64 v134, v134\n" \
"v_mfma_f32_16x16x32_bf16 v[140:143], a[120:123], a[24:27], v[140:143]\n" \
"ds_read_b128 a[92:95], v210 offset:36864\n" \
"v_cndmask_b32_e32 v225, v225, v224, vcc\n" \
"v_mfma_f32_16x16x32_bf16 v[140:143], a[116:119], a[20:23], v[140:143]\n" \
"s_add_u32 m0, %[m0b], 66560\n" \
"s_mov_b32 s61, 229376\n" \
"buffer_load_dwordx4 %[vg0], s[64:67], s61 offen offset:0 lds\n" \
"v_mfma_f32_16x16x32_bf16 v[140:143], a[112:115], a[16:19], v[140:143]\n" \
"v_exp_f32_e32 v135, v135\n" \
"v_mfma_f32_16x16x32_bf16 v[48:51], a[176:179], v[196:199], v[48:51]\n" \
"ds_read_b128 a[88:91], v209 offset:36864\n" \
"v_mfma_f32_16x16x32_bf16 v[48:51], a[144:147], v[188:191], v[48:51]\n" \
"v_exp_f32_e64 v136, v136\n" \
"v_mfma_f32_16x16x32_bf16 v[52:55], a[180:183], v[196:199], v[52:55]\n" \
"v_cvt_pk_bf16_f32 v194, v132, v133\n" \
"v_cvt_pk_bf16_f32 v195, v134, v135\n" \
"v_mfma_f32_16x16x32_bf16 v[52:55], a[148:151], v[188:191], v[52:55]\n" \
"v_exp_f32_e64 v137, v137\n" \
"v_mfma_f32_16x16x32_bf16 v[56:59], a[184:187], v[196:199], v[56:59]\n" \
"v_exp_f32_e64 v138, v138\n" \
"v_mfma_f32_16x16x32_bf16 v[56:59], a[152:155], v[188:191], v[56:59]\n" \
"ds_read_b128 a[84:87], v208 offset:36864\n" \
"v_mfma_f32_16x16x32_bf16 v[60:63], a[188:191], v[196:199], v[60:63]\n" \
"v_exp_f32_e64 v139, v139\n" \
"v_mfma_f32_16x16x32_bf16 v[60:63], a[156:159], v[188:191], v[60:63]\n" \
"v_mfma_f32_16x16x32_bf16 v[144:147], a[96:99], a[32:35], v[168:171]\n" \
"v_exp_f32_e64 v140, v140\n" \
"v_mfma_f32_16x16x32_bf16 v[144:147], a[100:103], a[36:39], v[144:147]\n" \
"ds_read_b128 a[80:83], %[kb0] offset:36864\n" \
"s_sub_u32 s61, s48, 0\n" \
"s_cmp_lt_u32 s61, 4\n" \
"s_cselect_b64 vcc, -1, 0\n" \
"v_mfma_f32_16x16x32_bf16 v[144:147], a[104:107], a[40:43], v[144:147]\n" \
"v_exp_f32_e32 v141, v141\n" \
"v_mfma_f32_16x16x32_bf16 v[144:147], a[108:111], a[44:47], v[144:147]\n" \
"v_cvt_pk_bf16_f32 v196, v136, v137\n" \
"v_cvt_pk_bf16_f32 v197, v138, v139\n" \
"v_mfma_f32_16x16x32_bf16 v[148:151], a[124:127], a[44:47], v[168:171]\n" \
"v_exp_f32_e64 v142, v142\n" \
"v_mfma_f32_16x16x32_bf16 v[148:151], a[120:123], a[40:43], v[148:151]\n" \
"v_readlane_b32 s55, %[tlo], 16\n" \
"v_mfma_f32_16x16x32_bf16 v[148:151], a[116:119], a[36:39], v[148:151]\n" \
"s_add_u32 m0, %[m0b], 67584\n" \
"s_mov_b32 s61, 458752\n" \
"buffer_load_dwordx4 %[vg0], s[64:67], s61 offen offset:0 lds\n" \
"v_mfma_f32_16x16x32_bf16 v[148:151], a[112:115], a[32:35], v[148:151]\n" \
"v_exp_f32_e64 v143, v143\n" \
"v_mfma_f32_16x16x32_bf16 v[96:99], a[160:163], v[204:207], v[96:99]\n" \
"ds_read_b64_tr_b16 a[160:161], %[vvb] offset:24576\n" \
"v_cmp_gt_u32_e64 s[56:57], 32, v231\n" \
"v_mfma_f32_16x16x32_bf16 v[96:99], a[128:131], v[220:223], v[96:99]\n" \
"v_exp_f32_e64 v144, v144\n" \
"v_mfma_f32_16x16x32_bf16 v[100:103], a[164:167], v[204:207], v[100:103]\n" \
"v_cvt_pk_bf16_f32 v198, v140, v141\n" \
"v_cvt_pk_bf16_f32 v199, v142, v143\n" \
"v_mfma_f32_16x16x32_bf16 v[100:103], a[132:135], v[220:223], v[100:103]\n" \
"v_exp_f32_e64 v145, v145\n" \
"v_mfma_f32_16x16x32_bf16 v[104:107], a[168:171], v[204:207], v[104:107]\n" \
"ds_read_b64_tr_b16 a[162:163], %[vvb] offset:28672\n" \
"ds_read_b64_tr_b16 a[164:165], v211 offset:24576\n" \
"v_lshlrev_b32_e64 v232, 2, v224\n" \
"v_mfma_f32_16x16x32_bf16 v[104:107], a[136:139], v[220:223], v[104:107]\n" \
"v_exp_f32_e64 v146, v146\n" \
"v_mfma_f32_16x16x32_bf16 v[108:111], a[172:175], v[204:207], v[108:111]\n" \
"ds_read_b64_tr_b16 a[166:167], v211 offset:28672\n" \
"v_mov_b32_e32 v233, 0x80000000\n" \
"v_mfma_f32_16x16x32_bf16 v[108:111], a[140:143], v[220:223], v[108:111]\n" \
"v_exp_f32_e64 v147, v147\n" \
"v_smfmac_f32_16x16x64_bf16 v[180:183], a[192:195], v[216:223], v228\n" \
"ds_read_b64_tr_b16 a[168:169], %[vvb] offset:25088\n" \
"v_mfma_f32_16x16x32_bf16 v[152:155], a[96:99], a[48:51], v[172:175]\n" \
"v_exp_f32_e64 v148, v148\n" \
"v_mfma_f32_16x16x32_bf16 v[152:155], a[100:103], a[52:55], v[152:155]\n" \
"ds_read_b64_tr_b16 a[170:171], %[vvb] offset:29184\n" \
"v_cndmask_b32_e64 v232, v233, v232, s[56:57]\n" \
"v_mfma_f32_16x16x32_bf16 v[152:155], a[104:107], a[56:59], v[152:155]\n" \
"v_exp_f32_e64 v149, v149\n" \
"v_mfma_f32_16x16x32_bf16 v[152:155], a[108:111], a[60:63], v[152:155]\n" \
"v_cvt_pk_bf16_f32 v200, v144, v145\n" \
"v_cvt_pk_bf16_f32 v201, v146, v147\n" \
"v_mfma_f32_16x16x32_bf16 v[156:159], a[124:127], a[60:63], v[172:175]\n" \
"v_exp_f32_e64 v150, v150\n" \
"v_mfma_f32_16x16x32_bf16 v[156:159], a[120:123], a[56:59], v[156:159]\n" \
"ds_read_b64_tr_b16 a[172:173], v211 offset:25088\n" \
"v_mov_b32_e64 v230, 1\n" \
"v_mfma_f32_16x16x32_bf16 v[156:159], a[116:119], a[52:55], v[156:159]\n" \
"s_add_u32 m0, %[m0b], 68608\n" \
"s_mov_b32 s61, 688128\n" \
"buffer_load_dwordx4 %[vg0], s[64:67], s61 offen offset:0 lds\n" \
"v_mfma_f32_16x16x32_bf16 v[156:159], a[112:115], a[48:51], v[156:159]\n" \
"v_exp_f32_e64 v151, v151\n" \
"v_mfma_f32_16x16x32_bf16 v[112:115], a[176:179], v[204:207], v[112:115]\n" \
"ds_read_b64_tr_b16 a[174:175], v211 offset:29184\n" \
"ds_read_b64_tr_b16 a[176:177], %[vvb] offset:25600\n" \
"v_mfma_f32_16x16x32_bf16 v[112:115], a[144:147], v[220:223], v[112:115]\n" \
"v_exp_f32_e64 v152, v152\n" \
"v_mfma_f32_16x16x32_bf16 v[116:119], a[180:183], v[204:207], v[116:119]\n" \
"v_cvt_pk_bf16_f32 v202, v148, v149\n" \
"v_cvt_pk_bf16_f32 v203, v150, v151\n" \
"v_mfma_f32_16x16x32_bf16 v[116:119], a[148:151], v[220:223], v[116:119]\n" \
"v_exp_f32_e64 v153, v153\n" \
"v_mfma_f32_16x16x32_bf16 v[120:123], a[184:187], v[204:207], v[120:123]\n" \
"v_exp_f32_e64 v154, v154\n" \
"v_mfma_f32_16x16x32_bf16 v[120:123], a[152:155], v[220:223], v[120:123]\n" \
"ds_read_b64_tr_b16 a[178:179], %[vvb] offset:29696\n" \
"buffer_store_dword v230, v232, %[srdz], s55 offen sc1\n" \
"v_mfma_f32_16x16x32_bf16 v[124:127], a[188:191], v[204:207], v[124:127]\n" \
"v_exp_f32_e64 v155, v155\n" \
"v_mfma_f32_16x16x32_bf16 v[124:127], a[156:159], v[220:223], v[124:127]\n" \
"ds_read_b64_tr_b16 a[180:181], v211 offset:25600\n" \
"s_waitcnt lgkmcnt(13)\n" \
"v_mfma_f32_16x16x32_bf16 v[128:131], a[64:67], a[0:3], v[160:163]\n" \
"v_exp_f32_e32 v156, v156\n" \
"v_mfma_f32_16x16x32_bf16 v[128:131], a[68:71], a[4:7], v[128:131]\n" \
"ds_read_b64_tr_b16 a[182:183], v211 offset:29696\n" \
"s_add_u32 m0, %[m0b], 16384\n" \
"s_mov_b32 s49, s48\n" \
"v_mfma_f32_16x16x32_bf16 v[128:131], a[72:75], a[8:11], v[128:131]\n" \
"v_exp_f32_e32 v157, v157\n" \
"v_mfma_f32_16x16x32_bf16 v[128:131], a[76:79], a[12:15], v[128:131]\n" \
"v_cvt_pk_bf16_f32 v204, v152, v153\n" \
"v_cvt_pk_bf16_f32 v205, v154, v155\n" \
"v_mfma_f32_16x16x32_bf16 v[132:135], a[92:95], a[12:15], v[160:163]\n" \
"v_exp_f32_e64 v158, v158\n" \
"v_mfma_f32_16x16x32_bf16 v[132:135], a[88:91], a[8:11], v[132:135]\n" \
"ds_read_b64_tr_b16 a[184:185], %[vvb] offset:26112\n" \
"s_lshl_b32 s49, s49, 18\n" \
"s_or_b32 s49, s49, s59\n" \
"s_waitcnt lgkmcnt(5)\n" \
"v_mfma_f32_16x16x32_bf16 v[132:135], a[84:87], a[4:7], v[132:135]\n" \
"buffer_load_dwordx4 %[wo0], %[srdt], s49 offen offset:0 lds\n" \
"v_mfma_f32_16x16x32_bf16 v[132:135], a[80:83], a[0:3], v[132:135]\n" \
"v_exp_f32_e32 v159, v159\n" \
"v_mfma_f32_16x16x32_bf16 v[64:67], a[128:131], v[216:219], v[64:67]\n" \
"ds_read_b64_tr_b16 a[186:187], %[vvb] offset:30208\n" \
"ds_read_b64_tr_b16 a[188:189], v211 offset:26112\n" \
"v_xor_b32_e32 v232, 0x80, %[vx0]\n" \
"v_mfma_f32_16x16x32_bf16 v[64:67], a[160:163], v[200:203], v[64:67]\n" \
"v_exp_f32_e64 v128, v128\n" \
"v_mfma_f32_16x16x32_bf16 v[68:71], a[132:135], v[216:219], v[68:71]\n" \
"v_cvt_pk_bf16_f32 v206, v156, v157\n" \
"v_cvt_pk_bf16_f32 v207, v158, v159\n" \
"v_mfma_f32_16x16x32_bf16 v[68:71], a[164:167], v[200:203], v[68:71]\n" \
"v_exp_f32_e64 v129, v129\n" \
"v_mfma_f32_16x16x32_bf16 v[72:75], a[136:139], v[216:219], v[72:75]\n" \
"ds_read_b64_tr_b16 a[190:191], v211 offset:30208\n" \
"ds_read_b64_tr_b16 a[128:129], %[vvb] offset:32768\n" \
"v_mfma_f32_16x16x32_bf16 v[72:75], a[168:171], v[200:203], v[72:75]\n" \
"v_exp_f32_e64 v130, v130\n" \
"v_mfma_f32_16x16x32_bf16 v[76:79], a[140:143], v[216:219], v[76:79]\n" \
"ds_read_b64_tr_b16 a[130:131], %[vvb] offset:36864\n" \
"ds_read_b64_tr_b16 a[132:133], v211 offset:32768\n" \
"v_mfma_f32_16x16x32_bf16 v[76:79], a[172:175], v[200:203], v[76:79]\n" \
"v_exp_f32_e64 v131, v131\n" \
"v_smfmac_f32_16x16x64_bf16 v[176:179], a[192:195], v[192:199], v228\n" \
"ds_read_b64_tr_b16 a[134:135], v211 offset:36864\n" \
"ds_read_b64_tr_b16 a[136:137], %[vvb] offset:33280\n" \
"global_load_dwordx2 a[240:241], %[vx0], %[px] offset:0\n" \
"v_mfma_f32_16x16x32_bf16 v[136:139], a[64:67], a[16:19], v[164:167]\n" \
"v_exp_f32_e64 v132, v132\n" \
"v_mfma_f32_16x16x32_bf16 v[136:139], a[68:71], a[20:23], v[136:139]\n" \
"ds_read_b64_tr_b16 a[138:139], %[vvb] offset:37376\n" \
"ds_read_b64_tr_b16 a[140:141], v211 offset:33280\n" \
"v_mfma_f32_16x16x32_bf16 v[136:139], a[72:75], a[24:27], v[136:139]\n" \
"v_exp_f32_e64 v133, v133\n" \
"v_mfma_f32_16x16x32_bf16 v[136:139], a[76:79], a[28:31], v[136:139]\n" \
"v_cvt_pk_bf16_f32 v184, v128, v129\n" \
"v_cvt_pk_bf16_f32 v185, v130, v131\n" \
"v_mfma_f32_16x16x32_bf16 v[140:143], a[92:95], a[28:31], v[164:167]\n" \
"v_exp_f32_e64 v134, v134\n" \
"v_mfma_f32_16x16x32_bf16 v[140:143], a[88:91], a[24:27], v[140:143]\n" \
"ds_read_b64_tr_b16 a[142:143], v211 offset:37376\n" \
"s_waitcnt lgkmcnt(8)\n" \
"v_mfma_f32_16x16x32_bf16 v[140:143], a[84:87], a[20:23], v[140:143]\n" \
"buffer_load_dwordx4 v212, %[srdt], s49 offen offset:1024 lds\n" \
"v_mfma_f32_16x16x32_bf16 v[140:143], a[80:83], a[16:19], v[140:143]\n" \
"v_exp_f32_e32 v135, v135\n" \
"v_mfma_f32_16x16x32_bf16 v[80:83], a[144:147], v[216:219], v[80:83]\n" \
"ds_read_b64_tr_b16 a[144:145], %[vvb] offset:33792\n" \
"ds_read_b64_tr_b16 a[146:147], %[vvb] offset:37888\n" \
"v_mfma_f32_16x16x32_bf16 v[80:83], a[176:179], v[200:203], v[80:83]\n" \
"v_exp_f32_e64 v136, v136\n" \
"v_mfma_f32_16x16x32_bf16 v[84:87], a[148:151], v[216:219], v[84:87]\n" \
"v_cvt_pk_bf16_f32 v186, v132, v133\n" \
"v_cvt_pk_bf16_f32 v187, v134, v135\n" \
"v_mfma_f32_16x16x32_bf16 v[84:87], a[180:183], v[200:203], v[84:87]\n" \
"v_exp_f32_e64 v137, v137\n" \
"v_mfma_f32_16x16x32_bf16 v[88:91], a[152:155], v[216:219], v[88:91]\n" \
"v_exp_f32_e64 v138, v138\n" \
"v_mfma_f32_16x16x32_bf16 v[88:91], a[184:187], v[200:203], v[88:91]\n" \
"ds_read_b64_tr_b16 a[148:149], v211 offset:33792\n" \
"ds_read_b64_tr_b16 a[150:151], v211 offset:37888\n" \
"v_mfma_f32_16x16x32_bf16 v[92:95], a[156:159], v[216:219], v[92:95]\n" \
"v_exp_f32_e64 v139, v139\n" \
"v_mfma_f32_16x16x32_bf16 v[92:95], a[188:191], v[200:203], v[92:95]\n" \
"ds_read_b64_tr_b16 a[152:153], %[vvb] offset:34304\n" \
"global_load_dwordx2 a[242:243], %[vx0], %[px] offset:1024\n" \
"v_mfma_f32_16x16x32_bf16 v[144:147], a[64:67], a[32:35], v[168:171]\n" \
"v_exp_f32_e64 v140, v140\n" \
"v_mfma_f32_16x16x32_bf16 v[144:147], a[68:71], a[36:39], v[144:147]\n" \
"ds_read_b64_tr_b16 a[154:155], %[vvb] offset:38400\n" \
"ds_read_b64_tr_b16 a[156:157], v211 offset:34304\n" \
"v_mfma_f32_16x16x32_bf16 v[144:147], a[72:75], a[40:43], v[144:147]\n" \
"v_exp_f32_e64 v141, v141\n" \
"v_mfma_f32_16x16x32_bf16 v[144:147], a[76:79], a[44:47], v[144:147]\n" \
"v_cvt_pk_bf16_f32 v188, v136, v137\n" \
"v_cvt_pk_bf16_f32 v189, v138, v139\n" \
"v_mfma_f32_16x16x32_bf16 v[148:151], a[92:95], a[44:47], v[168:171]\n" \
"v_exp_f32_e64 v142, v142\n" \
"v_mfma_f32_16x16x32_bf16 v[148:151], a[88:91], a[40:43], v[148:151]\n" \
"ds_read_b64_tr_b16 a[158:159], v211 offset:38400\n" \
"s_waitcnt lgkmcnt(8)\n" \
"v_mfma_f32_16x16x32_bf16 v[148:151], a[84:87], a[36:39], v[148:151]\n" \
"buffer_load_dwordx4 v213, %[srdt], s49 offen offset:2048 lds\n" \
"v_mfma_f32_16x16x32_bf16 v[148:151], a[80:83], a[32:35], v[148:151]\n" \
"v_exp_f32_e32 v143, v143\n" \
"v_mfma_f32_16x16x32_bf16 v[0:3], a[160:163], v[192:195], v[0:3]\n" \
"ds_read_b128 a[96:99], %[kb0] offset:40960\n" \
"v_mfma_f32_16x16x32_bf16 v[0:3], a[128:131], v[184:187], v[0:3]\n" \
"v_exp_f32_e64 v144, v144\n" \
"v_mfma_f32_16x16x32_bf16 v[4:7], a[164:167], v[192:195], v[4:7]\n" \
"v_cvt_pk_bf16_f32 v190, v140, v141\n" \
"v_cvt_pk_bf16_f32 v191, v142, v143\n" \
"v_mfma_f32_16x16x32_bf16 v[4:7], a[132:135], v[184:187], v[4:7]\n" \
"v_exp_f32_e64 v145, v145\n" \
"v_mfma_f32_16x16x32_bf16 v[8:11], a[168:171], v[192:195], v[8:11]\n" \
"ds_read_b128 a[100:103], v208 offset:40960\n" \
"v_mfma_f32_16x16x32_bf16 v[8:11], a[136:139], v[184:187], v[8:11]\n" \
"v_exp_f32_e64 v146, v146\n" \
"v_mfma_f32_16x16x32_bf16 v[12:15], a[172:175], v[192:195], v[12:15]\n" \
"ds_read_b128 a[104:107], v209 offset:40960\n" \
"v_mfma_f32_16x16x32_bf16 v[12:15], a[140:143], v[184:187], v[12:15]\n" \
"v_exp_f32_e64 v147, v147\n" \
"v_smfmac_f32_16x16x64_bf16 v[180:183], a[192:195], v[200:207], v228\n" \
"ds_read_b128 a[108:111], v210 offset:40960\n" \
"global_load_dwordx2 a[248:249], v232, %[px] offset:0\n" \
"v_mfma_f32_16x16x32_bf16 v[152:155], a[64:67], a[48:51], v[172:175]\n" \
"v_exp_f32_e64 v148, v148\n" \
"v_mfma_f32_16x16x32_bf16 v[152:155], a[68:71], a[52:55], v[152:155]\n" \
"ds_read_b128 a[124:127], v210 offset:45056\n" \
"v_mfma_f32_16x16x32_bf16 v[152:155], a[72:75], a[56:59], v[152:155]\n" \
"v_exp_f32_e64 v149, v149\n" \
"v_mfma_f32_16x16x32_bf16 v[152:155], a[76:79], a[60:63], v[152:155]\n" \
"v_cvt_pk_bf16_f32 v216, v144, v145\n" \
"v_cvt_pk_bf16_f32 v217, v146, v147\n" \
"v_mfma_f32_16x16x32_bf16 v[156:159], a[92:95], a[60:63], v[172:175]\n" \
"v_exp_f32_e64 v150, v150\n" \
"v_mfma_f32_16x16x32_bf16 v[156:159], a[88:91], a[56:59], v[156:159]\n" \
"ds_read_b128 a[120:123], v209 offset:45056\n" \
"s_waitcnt lgkmcnt(6)\n" \
"v_mfma_f32_16x16x32_bf16 v[156:159], a[84:87], a[52:55], v[156:159]\n" \
"buffer_load_dwordx4 v214, %[srdt], s49 offen offset:3072 lds\n" \
"v_mfma_f32_16x16x32_bf16 v[156:159], a[80:83], a[48:51], v[156:159]\n" \
"v_exp_f32_e32 v151, v151\n" \
"v_mfma_f32_16x16x32_bf16 v[16:19], a[176:179], v[192:195], v[16:19]\n" \
"ds_read_b128 a[116:119], v208 offset:45056\n" \
"v_mfma_f32_16x16x32_bf16 v[16:19], a[144:147], v[184:187], v[16:19]\n" \
"v_exp_f32_e64 v152, v152\n" \
"v_mfma_f32_16x16x32_bf16 v[20:23], a[180:183], v[192:195], v[20:23]\n" \
"v_cvt_pk_bf16_f32 v218, v148, v149\n" \
"v_cvt_pk_bf16_f32 v219, v150, v151\n" \
"v_mfma_f32_16x16x32_bf16 v[20:23], a[148:151], v[184:187], v[20:23]\n" \
"v_exp_f32_e64 v153, v153\n" \
"v_mfma_f32_16x16x32_bf16 v[24:27], a[184:187], v[192:195], v[24:27]\n" \
"v_exp_f32_e64 v154, v154\n" \
"v_mfma_f32_16x16x32_bf16 v[24:27], a[152:155], v[184:187], v[24:27]\n" \
"ds_read_b128 a[112:115], %[kb0] offset:45056\n" \
"global_load_dwordx2 a[250:251], v232, %[px] offset:1024\n" \
"v_mfma_f32_16x16x32_bf16 v[28:31], a[188:191], v[192:195], v[28:31]\n" \
"v_exp_f32_e64 v155, v155\n" \
"v_mfma_f32_16x16x32_bf16 v[28:31], a[156:159], v[184:187], v[28:31]\n" \
"s_waitcnt vmcnt(63) lgkmcnt(2)\n" \
"s_barrier\n" \
"v_mfma_f32_16x16x32_bf16 v[128:131], a[96:99], a[0:3], v[160:163]\n" \
"v_exp_f32_e64 v156, v156\n" \
"v_mfma_f32_16x16x32_bf16 v[128:131], a[100:103], a[4:7], v[128:131]\n" \
"v_lshrrev_b32_e64 v227, 5, v231\n" \
"v_mfma_f32_16x16x32_bf16 v[128:131], a[104:107], a[8:11], v[128:131]\n" \
"v_exp_f32_e64 v157, v157\n" \
"v_mfma_f32_16x16x32_bf16 v[128:131], a[108:111], a[12:15], v[128:131]\n" \
"v_cvt_pk_bf16_f32 v220, v152, v153\n" \
"v_cvt_pk_bf16_f32 v221, v154, v155\n" \
"v_mfma_f32_16x16x32_bf16 v[132:135], a[124:127], a[12:15], v[160:163]\n" \
"v_exp_f32_e64 v158, v158\n" \
"v_mfma_f32_16x16x32_bf16 v[132:135], a[120:123], a[8:11], v[132:135]\n" \
"v_lshlrev_b32_e32 v227, 10, v227\n" \
"s_waitcnt lgkmcnt(0)\n" \
"v_mfma_f32_16x16x32_bf16 v[132:135], a[116:119], a[4:7], v[132:135]\n" \
"s_add_u32 m0, %[m0b], 81920\n" \
"s_mov_b32 s61, 917504\n" \
"buffer_load_dwordx4 %[vg0], s[64:67], s61 offen offset:0 lds\n" \
"v_mfma_f32_16x16x32_bf16 v[132:135], a[112:115], a[0:3], v[132:135]\n" \
"v_exp_f32_e64 v159, v159\n" \
"v_mfma_f32_16x16x32_bf16 v[32:35], a[160:163], v[196:199], v[32:35]\n" \
"ds_read_b128 a[64:67], %[kb0] offset:49152\n" \
"v_add_u32_e32 v227, 0x10000, v227\n" \
"v_mfma_f32_16x16x32_bf16 v[32:35], a[128:131], v[188:191], v[32:35]\n" \
"v_exp_f32_e64 v128, v128\n" \
"v_mfma_f32_16x16x32_bf16 v[36:39], a[164:167], v[196:199], v[36:39]\n" \
"v_cvt_pk_bf16_f32 v222, v156, v157\n" \
"v_cvt_pk_bf16_f32 v223, v158, v159\n" \
"v_mfma_f32_16x16x32_bf16 v[36:39], a[132:135], v[188:191], v[36:39]\n" \
"v_exp_f32_e64 v129, v129\n" \
"v_mfma_f32_16x16x32_bf16 v[40:43], a[168:171], v[196:199], v[40:43]\n" \
"ds_read_b128 a[68:71], v208 offset:49152\n" \
"v_sub_u32_e64 v215, %[vvb], v227\n" \
"v_mfma_f32_16x16x32_bf16 v[40:43], a[136:139], v[188:191], v[40:43]\n" \
"v_exp_f32_e64 v130, v130\n" \
"v_mfma_f32_16x16x32_bf16 v[44:47], a[172:175], v[196:199], v[44:47]\n" \
"ds_read_b128 a[72:75], v209 offset:49152\n" \
"v_xor_b32_e64 v227, 32, v215\n" \
"v_mfma_f32_16x16x32_bf16 v[44:47], a[140:143], v[188:191], v[44:47]\n" \
"v_exp_f32_e64 v131, v131\n" \
"v_smfmac_f32_16x16x64_bf16 v[176:179], a[192:195], v[184:191], v228\n" \
"global_load_dwordx2 a[244:245], %[vx0], %[px] offset:64\n" \
"v_mfma_f32_16x16x32_bf16 v[136:139], a[96:99], a[16:19], v[164:167]\n" \
"v_exp_f32_e64 v132, v132\n" \
"v_mfma_f32_16x16x32_bf16 v[136:139], a[100:103], a[20:23], v[136:139]\n" \
"ds_read_b128 a[76:79], v210 offset:49152\n" \
"v_mfma_f32_16x16x32_bf16 v[136:139], a[104:107], a[24:27], v[136:139]\n" \
"v_exp_f32_e64 v133, v133\n" \
"v_mfma_f32_16x16x32_bf16 v[136:139], a[108:111], a[28:31], v[136:139]\n" \
"v_cvt_pk_bf16_f32 v192, v128, v129\n" \
"v_cvt_pk_bf16_f32 v193, v130, v131\n" \
"v_mfma_f32_16x16x32_bf16 v[140:143], a[124:127], a[28:31], v[164:167]\n" \
"v_exp_f32_e64 v134, v134\n" \
"v_mfma_f32_16x16x32_bf16 v[140:143], a[120:123], a[24:27], v[140:143]\n" \
"ds_read_b128 a[92:95], v210 offset:53248\n" \
"v_mfma_f32_16x16x32_bf16 v[140:143], a[116:119], a[20:23], v[140:143]\n" \
"s_add_u32 m0, %[m0b], 82944\n" \
"s_mov_b32 s61, 1146880\n" \
"buffer_load_dwordx4 %[vg0], s[64:67], s61 offen offset:0 lds\n" \
"v_mfma_f32_16x16x32_bf16 v[140:143], a[112:115], a[16:19], v[140:143]\n" \
"v_exp_f32_e64 v135, v135\n" \
"v_mfma_f32_16x16x32_bf16 v[48:51], a[176:179], v[196:199], v[48:51]\n" \
"ds_read_b128 a[88:91], v209 offset:53248\n" \
"v_mfma_f32_16x16x32_bf16 v[48:51], a[144:147], v[188:191], v[48:51]\n" \
"v_exp_f32_e64 v136, v136\n" \
"v_mfma_f32_16x16x32_bf16 v[52:55], a[180:183], v[196:199], v[52:55]\n" \
"v_cvt_pk_bf16_f32 v194, v132, v133\n" \
"v_cvt_pk_bf16_f32 v195, v134, v135\n" \
"v_mfma_f32_16x16x32_bf16 v[52:55], a[148:151], v[188:191], v[52:55]\n" \
"v_exp_f32_e64 v137, v137\n" \
"v_mfma_f32_16x16x32_bf16 v[56:59], a[184:187], v[196:199], v[56:59]\n" \
"v_exp_f32_e64 v138, v138\n" \
"v_mfma_f32_16x16x32_bf16 v[56:59], a[152:155], v[188:191], v[56:59]\n" \
"ds_read_b128 a[84:87], v208 offset:53248\n" \
"v_mfma_f32_16x16x32_bf16 v[60:63], a[188:191], v[196:199], v[60:63]\n" \
"v_exp_f32_e64 v139, v139\n" \
"v_mfma_f32_16x16x32_bf16 v[60:63], a[156:159], v[188:191], v[60:63]\n" \
"global_load_dwordx2 a[246:247], %[vx0], %[px] offset:1088\n" \
"v_mfma_f32_16x16x32_bf16 v[144:147], a[96:99], a[32:35], v[168:171]\n" \
"v_exp_f32_e64 v140, v140\n" \
"v_mfma_f32_16x16x32_bf16 v[144:147], a[100:103], a[36:39], v[144:147]\n" \
"ds_read_b128 a[80:83], %[kb0] offset:53248\n" \
"v_mfma_f32_16x16x32_bf16 v[144:147], a[104:107], a[40:43], v[144:147]\n" \
"v_exp_f32_e64 v141, v141\n" \
"v_mfma_f32_16x16x32_bf16 v[144:147], a[108:111], a[44:47], v[144:147]\n" \
"v_cvt_pk_bf16_f32 v196, v136, v137\n" \
"v_cvt_pk_bf16_f32 v197, v138, v139\n" \
"v_mfma_f32_16x16x32_bf16 v[148:151], a[124:127], a[44:47], v[168:171]\n" \
"v_exp_f32_e64 v142, v142\n" \
"v_mfma_f32_16x16x32_bf16 v[148:151], a[120:123], a[40:43], v[148:151]\n" \
"v_mfma_f32_16x16x32_bf16 v[148:151], a[116:119], a[36:39], v[148:151]\n" \
"s_add_u32 m0, %[m0b], 83968\n" \
"s_mov_b32 s61, 1376256\n" \
"buffer_load_dwordx4 %[vg0], s[64:67], s61 offen offset:0 lds\n" \
"v_mfma_f32_16x16x32_bf16 v[148:151], a[112:115], a[32:35], v[148:151]\n" \
"v_exp_f32_e64 v143, v143\n" \
"v_mfma_f32_16x16x32_bf16 v[96:99], a[160:163], v[204:207], v[96:99]\n" \
"ds_read_b64_tr_b16 a[160:161], %[vvb] offset:40960\n" \
"v_mfma_f32_16x16x32_bf16 v[96:99], a[128:131], v[220:223], v[96:99]\n" \
"v_exp_f32_e64 v144, v144\n" \
"v_mfma_f32_16x16x32_bf16 v[100:103], a[164:167], v[204:207], v[100:103]\n" \
"v_cvt_pk_bf16_f32 v198, v140, v141\n" \
"v_cvt_pk_bf16_f32 v199, v142, v143\n" \
"v_mfma_f32_16x16x32_bf16 v[100:103], a[132:135], v[220:223], v[100:103]\n" \
"v_exp_f32_e64 v145, v145\n" \
"v_mfma_f32_16x16x32_bf16 v[104:107], a[168:171], v[204:207], v[104:107]\n" \
"ds_read_b64_tr_b16 a[162:163], %[vvb] offset:45056\n" \
"ds_read_b64_tr_b16 a[164:165], v211 offset:40960\n" \
"v_mfma_f32_16x16x32_bf16 v[104:107], a[136:139], v[220:223], v[104:107]\n" \
"v_exp_f32_e64 v146, v146\n" \
"v_mfma_f32_16x16x32_bf16 v[108:111], a[172:175], v[204:207], v[108:111]\n" \
"ds_read_b64_tr_b16 a[166:167], v211 offset:45056\n" \
"v_mfma_f32_16x16x32_bf16 v[108:111], a[140:143], v[220:223], v[108:111]\n" \
"v_exp_f32_e64 v147, v147\n" \
"v_smfmac_f32_16x16x64_bf16 v[180:183], a[192:195], v[216:223], v228\n" \
"ds_read_b64_tr_b16 a[168:169], %[vvb] offset:41472\n" \
"global_load_dwordx2 a[252:253], v232, %[px] offset:64\n" \
"v_mfma_f32_16x16x32_bf16 v[152:155], a[96:99], a[48:51], v[172:175]\n" \
"v_exp_f32_e64 v148, v148\n" \
"v_mfma_f32_16x16x32_bf16 v[152:155], a[100:103], a[52:55], v[152:155]\n" \
"ds_read_b64_tr_b16 a[170:171], %[vvb] offset:45568\n" \
"v_mfma_f32_16x16x32_bf16 v[152:155], a[104:107], a[56:59], v[152:155]\n" \
"v_exp_f32_e64 v149, v149\n" \
"v_mfma_f32_16x16x32_bf16 v[152:155], a[108:111], a[60:63], v[152:155]\n" \
"v_cvt_pk_bf16_f32 v200, v144, v145\n" \
"v_cvt_pk_bf16_f32 v201, v146, v147\n" \
"v_mfma_f32_16x16x32_bf16 v[156:159], a[124:127], a[60:63], v[172:175]\n" \
"v_exp_f32_e64 v150, v150\n" \
"v_mfma_f32_16x16x32_bf16 v[156:159], a[120:123], a[56:59], v[156:159]\n" \
"ds_read_b64_tr_b16 a[172:173], v211 offset:41472\n" \
"v_mfma_f32_16x16x32_bf16 v[156:159], a[116:119], a[52:55], v[156:159]\n" \
"s_add_u32 m0, %[m0b], 84992\n" \
"s_mov_b32 s61, 1605632\n" \
"buffer_load_dwordx4 %[vg0], s[64:67], s61 offen offset:0 lds\n" \
"v_mfma_f32_16x16x32_bf16 v[156:159], a[112:115], a[48:51], v[156:159]\n" \
"v_exp_f32_e64 v151, v151\n" \
"v_mfma_f32_16x16x32_bf16 v[112:115], a[176:179], v[204:207], v[112:115]\n" \
"ds_read_b64_tr_b16 a[174:175], v211 offset:45568\n" \
"ds_read_b64_tr_b16 a[176:177], %[vvb] offset:41984\n" \
"v_mfma_f32_16x16x32_bf16 v[112:115], a[144:147], v[220:223], v[112:115]\n" \
"v_exp_f32_e64 v152, v152\n" \
"v_mfma_f32_16x16x32_bf16 v[116:119], a[180:183], v[204:207], v[116:119]\n" \
"v_cvt_pk_bf16_f32 v202, v148, v149\n" \
"v_cvt_pk_bf16_f32 v203, v150, v151\n" \
"v_mfma_f32_16x16x32_bf16 v[116:119], a[148:151], v[220:223], v[116:119]\n" \
"v_exp_f32_e64 v153, v153\n" \
"v_mfma_f32_16x16x32_bf16 v[120:123], a[184:187], v[204:207], v[120:123]\n" \
"v_exp_f32_e64 v154, v154\n" \
"v_mfma_f32_16x16x32_bf16 v[120:123], a[152:155], v[220:223], v[120:123]\n" \
"ds_read_b64_tr_b16 a[178:179], %[vvb] offset:46080\n" \
"global_load_dwordx2 a[254:255], v232, %[px] offset:1088\n" \
"v_mfma_f32_16x16x32_bf16 v[124:127], a[188:191], v[204:207], v[124:127]\n" \
"v_exp_f32_e64 v155, v155\n" \
"v_mfma_f32_16x16x32_bf16 v[124:127], a[156:159], v[220:223], v[124:127]\n" \
"ds_read_b64_tr_b16 a[180:181], v211 offset:41984\n" \
"s_waitcnt lgkmcnt(13)\n" \
"v_mfma_f32_16x16x32_bf16 v[128:131], a[64:67], a[0:3], v[160:163]\n" \
"v_exp_f32_e32 v156, v156\n" \
"v_mfma_f32_16x16x32_bf16 v[128:131], a[68:71], a[4:7], v[128:131]\n" \
"ds_read_b64_tr_b16 a[182:183], v211 offset:46080\n" \
"s_add_u32 m0, %[m0b], 32768\n" \
"s_cmp_gt_u32 s48, 0\n" \
"v_mfma_f32_16x16x32_bf16 v[128:131], a[72:75], a[8:11], v[128:131]\n" \
"v_exp_f32_e32 v157, v157\n" \
"v_mfma_f32_16x16x32_bf16 v[128:131], a[76:79], a[12:15], v[128:131]\n" \
"v_cvt_pk_bf16_f32 v204, v152, v153\n" \
"v_cvt_pk_bf16_f32 v205, v154, v155\n" \
"v_mfma_f32_16x16x32_bf16 v[132:135], a[92:95], a[12:15], v[160:163]\n" \
"v_exp_f32_e64 v158, v158\n" \
"v_mfma_f32_16x16x32_bf16 v[132:135], a[88:91], a[8:11], v[132:135]\n" \
"ds_read_b64_tr_b16 a[184:185], %[vvb] offset:42496\n" \
"s_cselect_b32 s49, 0, 1\n" \
"s_lshl_b32 s49, s49, 18\n" \
"s_or_b32 s49, s49, s58\n" \
"s_waitcnt lgkmcnt(5)\n" \
"v_mfma_f32_16x16x32_bf16 v[132:135], a[84:87], a[4:7], v[132:135]\n" \
"buffer_load_dwordx4 %[wo0], %[srdt], s49 offen offset:0 lds\n" \
"v_mfma_f32_16x16x32_bf16 v[132:135], a[80:83], a[0:3], v[132:135]\n" \
"v_exp_f32_e64 v159, v159\n" \
"v_mfma_f32_16x16x32_bf16 v[64:67], a[128:131], v[216:219], v[64:67]\n" \
"ds_read_b64_tr_b16 a[186:187], %[vvb] offset:46592\n" \
"ds_read_b64_tr_b16 a[188:189], v211 offset:42496\n" \
"v_mfma_f32_16x16x32_bf16 v[64:67], a[160:163], v[200:203], v[64:67]\n" \
"v_exp_f32_e64 v128, v128\n" \
"v_mfma_f32_16x16x32_bf16 v[68:71], a[132:135], v[216:219], v[68:71]\n" \
"v_cvt_pk_bf16_f32 v206, v156, v157\n" \
"v_cvt_pk_bf16_f32 v207, v158, v159\n" \
"v_mfma_f32_16x16x32_bf16 v[68:71], a[164:167], v[200:203], v[68:71]\n" \
"v_exp_f32_e64 v129, v129\n" \
"v_mfma_f32_16x16x32_bf16 v[72:75], a[136:139], v[216:219], v[72:75]\n" \
"ds_read_b64_tr_b16 a[190:191], v211 offset:46592\n" \
"ds_read_b64_tr_b16 a[128:129], %[vvb] offset:49152\n" \
"v_mfma_f32_16x16x32_bf16 v[72:75], a[168:171], v[200:203], v[72:75]\n" \
"v_exp_f32_e64 v130, v130\n" \
"v_mfma_f32_16x16x32_bf16 v[76:79], a[140:143], v[216:219], v[76:79]\n" \
"ds_read_b64_tr_b16 a[130:131], %[vvb] offset:53248\n" \
"ds_read_b64_tr_b16 a[132:133], v211 offset:49152\n" \
"v_mfma_f32_16x16x32_bf16 v[76:79], a[172:175], v[200:203], v[76:79]\n" \
"v_exp_f32_e64 v131, v131\n" \
"v_smfmac_f32_16x16x64_bf16 v[176:179], a[192:195], v[192:199], v228\n" \
"ds_read_b64_tr_b16 a[134:135], v211 offset:53248\n" \
"ds_read_b64_tr_b16 a[136:137], %[vvb] offset:49664\n" \
"v_mfma_f32_16x16x32_bf16 v[136:139], a[64:67], a[16:19], v[164:167]\n" \
"v_exp_f32_e64 v132, v132\n" \
"v_mfma_f32_16x16x32_bf16 v[136:139], a[68:71], a[20:23], v[136:139]\n" \
"ds_read_b64_tr_b16 a[138:139], %[vvb] offset:53760\n" \
"ds_read_b64_tr_b16 a[140:141], v211 offset:49664\n" \
"v_mfma_f32_16x16x32_bf16 v[136:139], a[72:75], a[24:27], v[136:139]\n" \
"v_exp_f32_e64 v133, v133\n" \
"v_mfma_f32_16x16x32_bf16 v[136:139], a[76:79], a[28:31], v[136:139]\n" \
"v_cvt_pk_bf16_f32 v184, v128, v129\n" \
"v_cvt_pk_bf16_f32 v185, v130, v131\n" \
"v_mfma_f32_16x16x32_bf16 v[140:143], a[92:95], a[28:31], v[164:167]\n" \
"v_exp_f32_e64 v134, v134\n" \
"v_mfma_f32_16x16x32_bf16 v[140:143], a[88:91], a[24:27], v[140:143]\n" \
"ds_read_b64_tr_b16 a[142:143], v211 offset:53760\n" \
"s_waitcnt lgkmcnt(8)\n" \
"v_mfma_f32_16x16x32_bf16 v[140:143], a[84:87], a[20:23], v[140:143]\n" \
"buffer_load_dwordx4 v212, %[srdt], s49 offen offset:1024 lds\n" \
"v_mfma_f32_16x16x32_bf16 v[140:143], a[80:83], a[16:19], v[140:143]\n" \
"v_exp_f32_e32 v135, v135\n" \
"v_mfma_f32_16x16x32_bf16 v[80:83], a[144:147], v[216:219], v[80:83]\n" \
"ds_read_b64_tr_b16 a[144:145], %[vvb] offset:50176\n" \
".if . - adaln_body != 36864\n" \
".error \"size model\"\n" \
".endif\n" \
"Lk8_%=:\n" \
"s_cbranch_execz Lk9_%=\n" \
"s_nop 0\n" \
"ds_read_b64_tr_b16 a[146:147], %[vvb] offset:54272\n" \
"v_mfma_f32_16x16x32_bf16 v[80:83], a[176:179], v[200:203], v[80:83]\n" \
"v_exp_f32_e64 v136, v136\n" \
"v_mfma_f32_16x16x32_bf16 v[84:87], a[148:151], v[216:219], v[84:87]\n" \
"v_cvt_pk_bf16_f32 v186, v132, v133\n" \
"v_cvt_pk_bf16_f32 v187, v134, v135\n" \
"v_mfma_f32_16x16x32_bf16 v[84:87], a[180:183], v[200:203], v[84:87]\n" \
"v_exp_f32_e64 v137, v137\n" \
"v_mfma_f32_16x16x32_bf16 v[88:91], a[152:155], v[216:219], v[88:91]\n" \
"v_exp_f32_e64 v138, v138\n" \
"v_mfma_f32_16x16x32_bf16 v[88:91], a[184:187], v[200:203], v[88:91]\n" \
"ds_read_b64_tr_b16 a[148:149], v211 offset:50176\n" \
"ds_read_b64_tr_b16 a[150:151], v211 offset:54272\n" \
"v_readlane_b32 s64, %[tlo], 10\n" \
"v_mfma_f32_16x16x32_bf16 v[92:95], a[156:159], v[216:219], v[92:95]\n" \
"v_exp_f32_e64 v139, v139\n" \
"v_mfma_f32_16x16x32_bf16 v[92:95], a[188:191], v[200:203], v[92:95]\n" \
"ds_read_b64_tr_b16 a[152:153], %[vvb] offset:50688\n" \
"v_readlane_b32 s65, %[thi], 10\n" \
"v_mfma_f32_16x16x32_bf16 v[144:147], a[64:67], a[32:35], v[168:171]\n" \
"v_exp_f32_e64 v140, v140\n" \
"v_mfma_f32_16x16x32_bf16 v[144:147], a[68:71], a[36:39], v[144:147]\n" \
"ds_read_b64_tr_b16 a[154:155], %[vvb] offset:54784\n" \
"ds_read_b64_tr_b16 a[156:157], v211 offset:50688\n" \
"v_mfma_f32_16x16x32_bf16 v[144:147], a[72:75], a[40:43], v[144:147]\n" \
"v_exp_f32_e64 v141, v141\n" \
"v_mfma_f32_16x16x32_bf16 v[144:147], a[76:79], a[44:47], v[144:147]\n" \
"v_cvt_pk_bf16_f32 v188, v136, v137\n" \
"v_cvt_pk_bf16_f32 v189, v138, v139\n" \
"v_mfma_f32_16x16x32_bf16 v[148:151], a[92:95], a[44:47], v[168:171]\n" \
"v_exp_f32_e64 v142, v142\n" \
"v_mfma_f32_16x16x32_bf16 v[148:151], a[88:91], a[40:43], v[148:151]\n" \
"ds_read_b64_tr_b16 a[158:159], v211 offset:54784\n" \
"s_waitcnt lgkmcnt(8)\n" \
"v_mfma_f32_16x16x32_bf16 v[148:151], a[84:87], a[36:39], v[148:151]\n" \
"buffer_load_dwordx4 v213, %[srdt], s49 offen offset:2048 lds\n" \
"v_mfma_f32_16x16x32_bf16 v[148:151], a[80:83], a[32:35], v[148:151]\n" \
"v_exp_f32_e32 v143, v143\n" \
"v_mfma_f32_16x16x32_bf16 v[0:3], a[160:163], v[192:195], v[0:3]\n" \
"ds_read_b128 a[96:99], %[kb0] offset:57344\n" \
"v_mfma_f32_16x16x32_bf16 v[0:3], a[128:131], v[184:187], v[0:3]\n" \
"v_exp_f32_e64 v144, v144\n" \
"v_mfma_f32_16x16x32_bf16 v[4:7], a[164:167], v[192:195], v[4:7]\n" \
"v_cvt_pk_bf16_f32 v190, v140, v141\n" \
"v_cvt_pk_bf16_f32 v191, v142, v143\n" \
"v_mfma_f32_16x16x32_bf16 v[4:7], a[132:135], v[184:187], v[4:7]\n" \
"v_exp_f32_e64 v145, v145\n" \
"v_mfma_f32_16x16x32_bf16 v[8:11], a[168:171], v[192:195], v[8:11]\n" \
"ds_read_b128 a[100:103], v208 offset:57344\n" \
"v_mfma_f32_16x16x32_bf16 v[8:11], a[136:139], v[184:187], v[8:11]\n" \
"v_exp_f32_e64 v146, v146\n" \
"v_mfma_f32_16x16x32_bf16 v[12:15], a[172:175], v[192:195], v[12:15]\n" \
"ds_read_b128 a[104:107], v209 offset:57344\n" \
"v_mfma_f32_16x16x32_bf16 v[12:15], a[140:143], v[184:187], v[12:15]\n" \
"v_exp_f32_e64 v147, v147\n" \
"v_smfmac_f32_16x16x64_bf16 v[180:183], a[192:195], v[200:207], v228\n" \
"ds_read_b128 a[108:111], v210 offset:57344\n" \
"buffer_load_dword v229, v226, %[srdz], s50 offen sc1\n" \
"v_mfma_f32_16x16x32_bf16 v[152:155], a[64:67], a[48:51], v[172:175]\n" \
"v_exp_f32_e64 v148, v148\n" \
"v_mfma_f32_16x16x32_bf16 v[152:155], a[68:71], a[52:55], v[152:155]\n" \
"ds_read_b128 a[124:127], v210 offset:61440\n" \
"v_mfma_f32_16x16x32_bf16 v[152:155], a[72:75], a[56:59], v[152:155]\n" \
"v_exp_f32_e64 v149, v149\n" \
"v_mfma_f32_16x16x32_bf16 v[152:155], a[76:79], a[60:63], v[152:155]\n" \
"v_cvt_pk_bf16_f32 v216, v144, v145\n" \
"v_cvt_pk_bf16_f32 v217, v146, v147\n" \
"v_mfma_f32_16x16x32_bf16 v[156:159], a[92:95], a[60:63], v[172:175]\n" \
"v_exp_f32_e64 v150, v150\n" \
"v_mfma_f32_16x16x32_bf16 v[156:159], a[88:91], a[56:59], v[156:159]\n" \
"ds_read_b128 a[120:123], v209 offset:61440\n" \
"s_waitcnt lgkmcnt(6)\n" \
"v_mfma_f32_16x16x32_bf16 v[156:159], a[84:87], a[52:55], v[156:159]\n" \
"buffer_load_dwordx4 v214, %[srdt], s49 offen offset:3072 lds\n" \
"v_mfma_f32_16x16x32_bf16 v[156:159], a[80:83], a[48:51], v[156:159]\n" \
"v_exp_f32_e32 v151, v151\n" \
"v_mfma_f32_16x16x32_bf16 v[16:19], a[176:179], v[192:195], v[16:19]\n" \
"ds_read_b128 a[116:119], v208 offset:61440\n" \
"v_mfma_f32_16x16x32_bf16 v[16:19], a[144:147], v[184:187], v[16:19]\n" \
"v_exp_f32_e64 v152, v152\n" \
"v_mfma_f32_16x16x32_bf16 v[20:23], a[180:183], v[192:195], v[20:23]\n" \
"v_cvt_pk_bf16_f32 v218, v148, v149\n" \
"v_cvt_pk_bf16_f32 v219, v150, v151\n" \
"v_mfma_f32_16x16x32_bf16 v[20:23], a[148:151], v[184:187], v[20:23]\n" \
"v_exp_f32_e64 v153, v153\n" \
"v_mfma_f32_16x16x32_bf16 v[24:27], a[184:187], v[192:195], v[24:27]\n" \
"v_exp_f32_e64 v154, v154\n" \
"v_mfma_f32_16x16x32_bf16 v[24:27], a[152:155], v[184:187], v[24:27]\n" \
"ds_read_b128 a[112:115], %[kb0] offset:61440\n" \
"v_mfma_f32_16x16x32_bf16 v[28:31], a[188:191], v[192:195], v[28:31]\n" \
"v_exp_f32_e64 v155, v155\n" \
"v_mfma_f32_16x16x32_bf16 v[28:31], a[156:159], v[184:187], v[28:31]\n" \
"s_waitcnt vmcnt(28) lgkmcnt(2)\n" \
"s_barrier\n" \
"v_mfma_f32_16x16x32_bf16 v[128:131], a[96:99], a[0:3], v[160:163]\n" \
"v_exp_f32_e64 v156, v156\n" \
"v_mfma_f32_16x16x32_bf16 v[128:131], a[100:103], a[4:7], v[128:131]\n" \
"s_add_u32 m0, %[m0b], 98304\n" \
"s_cmp_gt_u32 s48, 0\n" \
"v_mfma_f32_16x16x32_bf16 v[128:131], a[104:107], a[8:11], v[128:131]\n" \
"v_exp_f32_e32 v157, v157\n" \
"v_mfma_f32_16x16x32_bf16 v[128:131], a[108:111], a[12:15], v[128:131]\n" \
"v_cvt_pk_bf16_f32 v220, v152, v153\n" \
"v_cvt_pk_bf16_f32 v221, v154, v155\n" \
"v_mfma_f32_16x16x32_bf16 v[132:135], a[124:127], a[12:15], v[160:163]\n" \
"v_exp_f32_e64 v158, v158\n" \
"v_mfma_f32_16x16x32_bf16 v[132:135], a[120:123], a[8:11], v[132:135]\n" \
"s_cselect_b32 s49, 0, 1\n" \
"s_lshl_b32 s49, s49, 18\n" \
"s_or_b32 s49, s49, s59\n" \
"s_waitcnt lgkmcnt(0)\n" \
"v_mfma_f32_16x16x32_bf16 v[132:135], a[116:119], a[4:7], v[132:135]\n" \
"buffer_load_dwordx4 %[wo0], %[srdt], s49 offen offset:0 lds\n" \
"v_mfma_f32_16x16x32_bf16 v[132:135], a[112:115], a[0:3], v[132:135]\n" \
"v_exp_f32_e64 v159, v159\n" \
"v_mfma_f32_16x16x32_bf16 v[32:35], a[160:163], v[196:199], v[32:35]\n" \
"ds_read_b64_tr_b16 a[196:197], v215 offset:0\n" \
"v_mfma_f32_16x16x32_bf16 v[32:35], a[128:131], v[188:191], v[32:35]\n" \
"v_exp_f32_e64 v128, v128\n" \
"v_mfma_f32_16x16x32_bf16 v[36:39], a[164:167], v[196:199], v[36:39]\n" \
"v_cvt_pk_bf16_f32 v222, v156, v157\n" \
"v_cvt_pk_bf16_f32 v223, v158, v159\n" \
"v_mfma_f32_16x16x32_bf16 v[36:39], a[132:135], v[188:191], v[36:39]\n" \
"v_exp_f32_e64 v129, v129\n" \
"v_mfma_f32_16x16x32_bf16 v[40:43], a[168:171], v[196:199], v[40:43]\n" \
"ds_read_b64_tr_b16 a[198:199], v215 offset:2048\n" \
"v_mfma_f32_16x16x32_bf16 v[40:43], a[136:139], v[188:191], v[40:43]\n" \
"v_exp_f32_e64 v130, v130\n" \
"v_mfma_f32_16x16x32_bf16 v[44:47], a[172:175], v[196:199], v[44:47]\n" \
"ds_read_b64_tr_b16 a[200:201], v215 offset:4096\n" \
"v_mfma_f32_16x16x32_bf16 v[44:47], a[140:143], v[188:191], v[44:47]\n" \
"v_exp_f32_e64 v131, v131\n" \
"v_smfmac_f32_16x16x64_bf16 v[176:179], a[192:195], v[184:191], v228\n" \
"v_mfma_f32_16x16x32_bf16 v[136:139], a[96:99], a[16:19], v[164:167]\n" \
"v_exp_f32_e64 v132, v132\n" \
"v_mfma_f32_16x16x32_bf16 v[136:139], a[100:103], a[20:23], v[136:139]\n" \
"ds_read_b64_tr_b16 a[202:203], v215 offset:6144\n" \
"v_mfma_f32_16x16x32_bf16 v[136:139], a[104:107], a[24:27], v[136:139]\n" \
"v_exp_f32_e64 v133, v133\n" \
"v_mfma_f32_16x16x32_bf16 v[136:139], a[108:111], a[28:31], v[136:139]\n" \
"v_cvt_pk_bf16_f32 v192, v128, v129\n" \
"v_cvt_pk_bf16_f32 v193, v130, v131\n" \
"v_mfma_f32_16x16x32_bf16 v[140:143], a[124:127], a[28:31], v[164:167]\n" \
"v_exp_f32_e64 v134, v134\n" \
"v_mfma_f32_16x16x32_bf16 v[140:143], a[120:123], a[24:27], v[140:143]\n" \
"ds_read_b64_tr_b16 a[204:205], v215 offset:8192\n" \
"v_mfma_f32_16x16x32_bf16 v[140:143], a[116:119], a[20:23], v[140:143]\n" \
"buffer_load_dwordx4 v212, %[srdt], s49 offen offset:1024 lds\n" \
"v_mfma_f32_16x16x32_bf16 v[140:143], a[112:115], a[16:19], v[140:143]\n" \
"v_exp_f32_e64 v135, v135\n" \
"v_mfma_f32_16x16x32_bf16 v[48:51], a[176:179], v[196:199], v[48:51]\n" \
"ds_read_b64_tr_b16 a[206:207], v215 offset:10240\n" \
"v_add_u32_e32 v208, 0x1000, %[oq0]\n" \
"v_mfma_f32_16x16x32_bf16 v[48:51], a[144:147], v[188:191], v[48:51]\n" \
"v_exp_f32_e64 v136, v136\n" \
"v_mfma_f32_16x16x32_bf16 v[52:55], a[180:183], v[196:199], v[52:55]\n" \
"v_cvt_pk_bf16_f32 v194, v132, v133\n" \
"v_cvt_pk_bf16_f32 v195, v134, v135\n" \
"v_mfma_f32_16x16x32_bf16 v[52:55], a[148:151], v[188:191], v[52:55]\n" \
"v_exp_f32_e64 v137, v137\n" \
"v_mfma_f32_16x16x32_bf16 v[56:59], a[184:187], v[196:199], v[56:59]\n" \
"v_exp_f32_e64 v138, v138\n" \
"v_mfma_f32_16x16x32_bf16 v[56:59], a[152:155], v[188:191], v[56:59]\n" \
"ds_read_b64_tr_b16 a[208:209], v215 offset:12288\n" \
"v_add_u32_e32 v209, 0x2000, %[oq0]\n" \
"v_mfma_f32_16x16x32_bf16 v[60:63], a[188:191], v[196:199], v[60:63]\n" \
"v_exp_f32_e64 v139, v139\n" \
"v_mfma_f32_16x16x32_bf16 v[60:63], a[156:159], v[188:191], v[60:63]\n" \
"v_add_u32_e32 v210, 0x3000, %[oq0]\n" \
"v_mfma_f32_16x16x32_bf16 v[144:147], a[96:99], a[32:35], v[168:171]\n" \
"v_exp_f32_e64 v140, v140\n" \
"v_mfma_f32_16x16x32_bf16 v[144:147], a[100:103], a[36:39], v[144:147]\n" \
"ds_read_b64_tr_b16 a[210:211], v215 offset:14336\n" \
"v_mfma_f32_16x16x32_bf16 v[144:147], a[104:107], a[40:43], v[144:147]\n" \
"v_exp_f32_e64 v141, v141\n" \
"v_mfma_f32_16x16x32_bf16 v[144:147], a[108:111], a[44:47], v[144:147]\n" \
"v_cvt_pk_bf16_f32 v196, v136, v137\n" \
"v_cvt_pk_bf16_f32 v197, v138, v139\n" \
"v_mfma_f32_16x16x32_bf16 v[148:151], a[124:127], a[44:47], v[168:171]\n" \
"v_exp_f32_e64 v142, v142\n" \
"v_mfma_f32_16x16x32_bf16 v[148:151], a[120:123], a[40:43], v[148:151]\n" \
"v_mfma_f32_16x16x32_bf16 v[148:151], a[116:119], a[36:39], v[148:151]\n" \
"buffer_load_dwordx4 v213, %[srdt], s49 offen offset:2048 lds\n" \
"v_mfma_f32_16x16x32_bf16 v[148:151], a[112:115], a[32:35], v[148:151]\n" \
"v_exp_f32_e64 v143, v143\n" \
"v_mfma_f32_16x16x32_bf16 v[96:99], a[160:163], v[204:207], v[96:99]\n" \
"ds_read_b64_tr_b16 a[160:161], %[vvb] offset:57344\n" \
"v_mfma_f32_16x16x32_bf16 v[96:99], a[128:131], v[220:223], v[96:99]\n" \
"v_exp_f32_e64 v144, v144\n" \
"v_mfma_f32_16x16x32_bf16 v[100:103], a[164:167], v[204:207], v[100:103]\n" \
"v_cvt_pk_bf16_f32 v198, v140, v141\n" \
"v_cvt_pk_bf16_f32 v199, v142, v143\n" \
"v_mfma_f32_16x16x32_bf16 v[100:103], a[132:135], v[220:223], v[100:103]\n" \
"v_exp_f32_e64 v145, v145\n" \
"v_mfma_f32_16x16x32_bf16 v[104:107], a[168:171], v[204:207], v[104:107]\n" \
"ds_read_b64_tr_b16 a[162:163], %[vvb] offset:61440\n" \
"ds_read_b64_tr_b16 a[164:165], v211 offset:57344\n" \
"v_mfma_f32_16x16x32_bf16 v[104:107], a[136:139], v[220:223], v[104:107]\n" \
"v_exp_f32_e64 v146, v146\n" \
"v_mfma_f32_16x16x32_bf16 v[108:111], a[172:175], v[204:207], v[108:111]\n" \
"ds_read_b64_tr_b16 a[166:167], v211 offset:61440\n" \
"v_mfma_f32_16x16x32_bf16 v[108:111], a[140:143], v[220:223], v[108:111]\n" \
"v_exp_f32_e64 v147, v147\n" \
"v_smfmac_f32_16x16x64_bf16 v[180:183], a[192:195], v[216:223], v228\n" \
"ds_read_b64_tr_b16 a[168:169], %[vvb] offset:57856\n" \
"v_mfma_f32_16x16x32_bf16 v[152:155], a[96:99], a[48:51], v[172:175]\n" \
"v_exp_f32_e64 v148, v148\n" \
"v_mfma_f32_16x16x32_bf16 v[152:155], a[100:103], a[52:55], v[152:155]\n" \
"ds_read_b64_tr_b16 a[170:171], %[vvb] offset:61952\n" \
"v_mfma_f32_16x16x32_bf16 v[152:155], a[104:107], a[56:59], v[152:155]\n" \
"v_exp_f32_e64 v149, v149\n" \
"v_mfma_f32_16x16x32_bf16 v[152:155], a[108:111], a[60:63], v[152:155]\n" \
"v_cvt_pk_bf16_f32 v200, v144, v145\n" \
"v_cvt_pk_bf16_f32 v201, v146, v147\n" \
"v_mfma_f32_16x16x32_bf16 v[156:159], a[124:127], a[60:63], v[172:175]\n" \
"v_exp_f32_e64 v150, v150\n" \
"v_mfma_f32_16x16x32_bf16 v[156:159], a[120:123], a[56:59], v[156:159]\n" \
"ds_read_b64_tr_b16 a[172:173], v211 offset:57856\n" \
"v_mfma_f32_16x16x32_bf16 v[156:159], a[116:119], a[52:55], v[156:159]\n" \
"buffer_load_dwordx4 v214, %[srdt], s49 offen offset:3072 lds\n" \
"v_mfma_f32_16x16x32_bf16 v[156:159], a[112:115], a[48:51], v[156:159]\n" \
"v_exp_f32_e64 v151, v151\n" \
"v_mfma_f32_16x16x32_bf16 v[112:115], a[176:179], v[204:207], v[112:115]\n" \
"ds_read_b64_tr_b16 a[174:175], v211 offset:61952\n" \
"ds_read_b64_tr_b16 a[176:177], %[vvb] offset:58368\n" \
"v_mfma_f32_16x16x32_bf16 v[112:115], a[144:147], v[220:223], v[112:115]\n" \
"v_exp_f32_e64 v152, v152\n" \
"v_mfma_f32_16x16x32_bf16 v[116:119], a[180:183], v[204:207], v[116:119]\n" \
"v_cvt_pk_bf16_f32 v202, v148, v149\n" \
"v_cvt_pk_bf16_f32 v203, v150, v151\n" \
"v_mfma_f32_16x16x32_bf16 v[116:119], a[148:151], v[220:223], v[116:119]\n" \
"v_exp_f32_e64 v153, v153\n" \
"v_mfma_f32_16x16x32_bf16 v[120:123], a[184:187], v[204:207], v[120:123]\n" \
"v_exp_f32_e64 v154, v154\n" \
"v_mfma_f32_16x16x32_bf16 v[120:123], a[152:155], v[220:223], v[120:123]\n" \
"ds_read_b64_tr_b16 a[178:179], %[vvb] offset:62464\n" \
"s_waitcnt vmcnt(5)\n" \
"v_cmp_eq_u32_e64 s[56:57], 0, v229\n" \
"s_cmp_lg_u64 s[56:57], 0\n" \
"s_cbranch_scc1 Lspk_%=\n" \
"Lbkk_%=:\n" \
"v_mfma_f32_16x16x32_bf16 v[124:127], a[188:191], v[204:207], v[124:127]\n" \
"v_exp_f32_e32 v155, v155\n" \
"v_mfma_f32_16x16x32_bf16 v[124:127], a[156:159], v[220:223], v[124:127]\n" \
"ds_read_b64_tr_b16 a[180:181], v211 offset:58368\n" \
"ds_read_b64_tr_b16 a[182:183], v211 offset:62464\n" \
"ds_read_b64_tr_b16 a[184:185], %[vvb] offset:58880\n" \
"ds_read_b64_tr_b16 a[186:187], %[vvb] offset:62976\n" \
"ds_read_b64_tr_b16 a[188:189], v211 offset:58880\n" \
"ds_read_b64_tr_b16 a[190:191], v211 offset:62976\n" \
"s_waitcnt lgkmcnt(8)\n" \
"s_nop 0\n" \
"v_smfmac_f32_16x16x64_bf16 v[176:179], a[192:195], v[192:199], v228\n" \
"v_exp_f32_e64 v156, v156\n" \
"ds_read_b64_tr_b16 a[212:213], v227 offset:0\n" \
"v_mfma_f32_16x16x32_bf16 v[0:3], a[160:163], v[192:195], v[0:3]\n" \
"v_exp_f32_e64 v157, v157\n" \
"ds_read_b64_tr_b16 a[214:215], v227 offset:2048\n" \
"v_mfma_f32_16x16x32_bf16 v[4:7], a[164:167], v[192:195], v[4:7]\n" \
"v_cvt_pk_bf16_f32 v204, v152, v153\n" \
"ds_read_b64_tr_b16 a[216:217], v227 offset:4096\n" \
"v_mfma_f32_16x16x32_bf16 v[8:11], a[168:171], v[192:195], v[8:11]\n" \
"v_cvt_pk_bf16_f32 v205, v154, v155\n" \
"ds_read_b64_tr_b16 a[218:219], v227 offset:6144\n" \
"v_mfma_f32_16x16x32_bf16 v[12:15], a[172:175], v[192:195], v[12:15]\n" \
"v_exp_f32_e64 v158, v158\n" \
"ds_read_b64_tr_b16 a[220:221], v227 offset:8192\n" \
"s_waitcnt lgkmcnt(5)\n" \
"s_nop 0\n" \
"v_mfma_f32_16x16x32_bf16 v[16:19], a[176:179], v[192:195], v[16:19]\n" \
"v_exp_f32_e64 v159, v159\n" \
"ds_read_b64_tr_b16 a[222:223], v227 offset:10240\n" \
"v_mfma_f32_16x16x32_bf16 v[20:23], a[180:183], v[192:195], v[20:23]\n" \
"v_cvt_pk_bf16_f32 v206, v156, v157\n" \
"ds_read_b64_tr_b16 a[224:225], v227 offset:12288\n" \
"v_mfma_f32_16x16x32_bf16 v[24:27], a[184:187], v[192:195], v[24:27]\n" \
"v_cvt_pk_bf16_f32 v207, v158, v159\n" \
"ds_read_b64_tr_b16 a[226:227], v227 offset:14336\n" \
"v_mfma_f32_16x16x32_bf16 v[28:31], a[188:191], v[192:195], v[28:31]\n" \
"v_add_f32_e64 v176, v176, v177\n" \
"v_smfmac_f32_16x16x64_bf16 v[180:183], a[192:195], v[200:207], v228\n" \
"v_add_f32_e32 v178, v178, v179\n" \
"v_add_f32_e32 v231, v176, v178\n" \
"v_rcp_f32_e32 v176, v176\n" \
"v_rcp_f32_e32 v178, v178\n" \
"v_mfma_f32_16x16x32_bf16 v[32:35], a[160:163], v[196:199], v[32:35]\n" \
"v_pk_mul_f32 v[0:1], v[0:1], v[176:177] op_sel_hi:[1,0]\n" \
"v_pk_mul_f32 v[2:3], v[2:3], v[176:177] op_sel_hi:[1,0]\n" \
"v_pk_mul_f32 v[4:5], v[4:5], v[176:177] op_sel_hi:[1,0]\n" \
"v_pk_mul_f32 v[6:7], v[6:7], v[176:177] op_sel_hi:[1,0]\n" \
"v_cvt_pk_bf16_f32 v0, v0, v1\n" \
"v_cvt_pk_bf16_f32 v1, v2, v3\n" \
"v_cvt_pk_bf16_f32 v2, v4, v5\n" \
"v_mfma_f32_16x16x32_bf16 v[36:39], a[164:167], v[196:199], v[36:39]\n" \
"v_cvt_pk_bf16_f32 v3, v6, v7\n" \
"s_mov_b32 s55, 0x4e800000\n" \
"v_mov_b32_e32 v236, %[flag]\n" \
"v_add_f32_e32 v180, v180, v181\n" \
"v_add_f32_e32 v182, v182, v183\n" \
"v_add_f32_e32 v231, v231, v180\n" \
"v_add_f32_e64 v231, v231, v182\n" \
"v_mfma_f32_16x16x32_bf16 v[40:43], a[168:171], v[196:199], v[40:43]\n" \
"v_rcp_f32_e64 v180, v180\n" \
"v_cmp_nle_f32_e64 s[56:57], v231, s55\n" \
"v_rcp_f32_e32 v182, v182\n" \
"s_nop 3\n" \
"s_cmp_lg_u64 s[56:57], 0\n" \
"s_cselect_b32 s55, 1, 0\n" \
"v_mov_b32_e64 v230, s55\n" \
"v_mfma_f32_16x16x32_bf16 v[44:47], a[172:175], v[196:199], v[44:47]\n" \
"s_mov_b32 s69, s55\n" \
"s_andn2_b32 s55, s55, s52\n" \
"s_cselect_b64 vcc, 0, vcc\n" \
"s_nop 0\n" \
"ds_write_b32 v236, v230\n" \
"v_and_b32_e32 v236, -16, v236\n" \
"s_cbranch_vccz Lg37_%=\n" \
"buffer_store_dwordx4 v[0:3], %[oq0], %[srdz], s60 offen offset:0 sc1\n" \
"Lg37_%=:\n" \
"v_pk_mul_f32 v[8:9], v[8:9], v[176:177] op_sel_hi:[1,0]\n" \
"v_mfma_f32_16x16x32_bf16 v[48:51], a[176:179], v[196:199], v[48:51]\n" \
"v_pk_mul_f32 v[10:11], v[10:11], v[176:177] op_sel_hi:[1,0]\n" \
"v_pk_mul_f32 v[12:13], v[12:13], v[176:177] op_sel_hi:[1,0]\n" \
"v_pk_mul_f32 v[14:15], v[14:15], v[176:177] op_sel_hi:[1,0]\n" \
"v_cvt_pk_bf16_f32 v8, v8, v9\n" \
"v_cvt_pk_bf16_f32 v9, v10, v11\n" \
"v_cvt_pk_bf16_f32 v10, v12, v13\n" \
"v_cvt_pk_bf16_f32 v11, v14, v15\n" \
"v_mfma_f32_16x16x32_bf16 v[52:55], a[180:183], v[196:199], v[52:55]\n" \
"s_cbranch_vccz Lg38_%=\n" \
"s_nop 0\n" \
"buffer_store_dwordx4 v[8:11], %[oq0], %[srdz], s60 offen offset:1024 sc1\n" \
"Lg38_%=:\n" \
"v_pk_mul_f32 v[16:17], v[16:17], v[176:177] op_sel_hi:[1,0]\n" \
"v_pk_mul_f32 v[18:19], v[18:19], v[176:177] op_sel_hi:[1,0]\n" \
"v_pk_mul_f32 v[20:21], v[20:21], v[176:177] op_sel_hi:[1,0]\n" \
"v_pk_mul_f32 v[22:23], v[22:23], v[176:177] op_sel_hi:[1,0]\n" \
"v_cvt_pk_bf16_f32 v16, v16, v17\n" \
"v_cvt_pk_bf16_f32 v17, v18, v19\n" \
"v_mfma_f32_16x16x32_bf16 v[56:59], a[184:187], v[196:199], v[56:59]\n" \
"v_cvt_pk_bf16_f32 v18, v20, v21\n" \
"v_cvt_pk_bf16_f32 v19, v22, v23\n" \
"s_cbranch_vccz Lg39_%=\n" \
"s_nop 0\n" \
"buffer_store_dwordx4 v[16:19], %[oq0], %[srdz], s60 offen offset:2048 sc1\n" \
"Lg39_%=:\n" \
"v_pk_mul_f32 v[24:25], v[24:25], v[176:177] op_sel_hi:[1,0]\n" \
"v_pk_mul_f32 v[26:27], v[26:27], v[176:177] op_sel_hi:[1,0]\n" \
"v_pk_mul_f32 v[28:29], v[28:29], v[176:177] op_sel_hi:[1,0]\n" \
"v_pk_mul_f32 v[30:31], v[30:31], v[176:177] op_sel_hi:[1,0]\n" \
"v_mfma_f32_16x16x32_bf16 v[60:63], a[188:191], v[196:199], v[60:63]\n" \
"v_cvt_pk_bf16_f32 v24, v24, v25\n" \
"v_cvt_pk_bf16_f32 v25, v26, v27\n" \
"v_cvt_pk_bf16_f32 v26, v28, v29\n" \
"v_cvt_pk_bf16_f32 v27, v30, v31\n" \
"s_cbranch_vccz Lg40_%=\n" \
"s_nop 0\n" \
"buffer_store_dwordx4 v[24:27], %[oq0], %[srdz], s60 offen offset:3072 sc1\n" \
"Lg40_%=:\n" \
"v_mfma_f32_16x16x32_bf16 v[64:67], a[128:131], v[216:219], v[64:67]\n" \
"v_pk_mul_f32 v[32:33], v[32:33], v[178:179] op_sel_hi:[1,0]\n" \
"v_pk_mul_f32 v[34:35], v[34:35], v[178:179] op_sel_hi:[1,0]\n" \
"v_pk_mul_f32 v[36:37], v[36:37], v[178:179] op_sel_hi:[1,0]\n" \
"v_mfma_f32_16x16x32_bf16 v[64:67], a[160:163], v[200:203], v[64:67]\n" \
"v_pk_mul_f32 v[38:39], v[38:39], v[178:179] op_sel_hi:[1,0]\n" \
"v_cvt_pk_bf16_f32 v32, v32, v33\n" \
"v_cvt_pk_bf16_f32 v33, v34, v35\n" \
"v_mfma_f32_16x16x32_bf16 v[68:71], a[132:135], v[216:219], v[68:71]\n" \
"v_cvt_pk_bf16_f32 v34, v36, v37\n" \
"v_cvt_pk_bf16_f32 v35, v38, v39\n" \
"s_cbranch_vccz Lg41_%=\n" \
"s_nop 0\n" \
"buffer_store_dwordx4 v[32:35], v208, %[srdz], s60 offen offset:0 sc1\n" \
"Lg41_%=:\n" \
"v_mfma_f32_16x16x32_bf16 v[68:71], a[164:167], v[200:203], v[68:71]\n" \
"v_pk_mul_f32 v[40:41], v[40:41], v[178:179] op_sel_hi:[1,0]\n" \
"v_pk_mul_f32 v[42:43], v[42:43], v[178:179] op_sel_hi:[1,0]\n" \
"v_pk_mul_f32 v[44:45], v[44:45], v[178:179] op_sel_hi:[1,0]\n" \
"v_mfma_f32_16x16x32_bf16 v[72:75], a[136:139], v[216:219], v[72:75]\n" \
"v_pk_mul_f32 v[46:47], v[46:47], v[178:179] op_sel_hi:[1,0]\n" \
"v_cvt_pk_bf16_f32 v40, v40, v41\n" \
"v_cvt_pk_bf16_f32 v41, v42, v43\n" \
"v_mfma_f32_16x16x32_bf16 v[72:75], a[168:171], v[200:203], v[72:75]\n" \
"v_cvt_pk_bf16_f32 v42, v44, v45\n" \
"v_cvt_pk_bf16_f32 v43, v46, v47\n" \
"s_cbranch_vccz Lg42_%=\n" \
"s_nop 0\n" \
"buffer_store_dwordx4 v[40:43], v208, %[srdz], s60 offen offset:1024 sc1\n" \
"Lg42_%=:\n" \
"v_mfma_f32_16x16x32_bf16 v[76:79], a[140:143], v[216:219], v[76:79]\n" \
"v_pk_mul_f32 v[48:49], v[48:49], v[178:179] op_sel_hi:[1,0]\n" \
"v_pk_mul_f32 v[50:51], v[50:51], v[178:179] op_sel_hi:[1,0]\n" \
"v_pk_mul_f32 v[52:53], v[52:53], v[178:179] op_sel_hi:[1,0]\n" \
"v_mfma_f32_16x16x32_bf16 v[76:79], a[172:175], v[200:203], v[76:79]\n" \
"v_pk_mul_f32 v[54:55], v[54:55], v[178:179] op_sel_hi:[1,0]\n" \
"v_cvt_pk_bf16_f32 v48, v48, v49\n" \
"v_cvt_pk_bf16_f32 v49, v50, v51\n" \
"v_mfma_f32_16x16x32_bf16 v[80:83], a[144:147], v[216:219], v[80:83]\n" \
"v_cvt_pk_bf16_f32 v50, v52, v53\n" \
"v_cvt_pk_bf16_f32 v51, v54, v55\n" \
"s_cbranch_vccz Lg43_%=\n" \
"s_nop 0\n" \
"buffer_store_dwordx4 v[48:51], v208, %[srdz], s60 offen offset:2048 sc1\n" \
"Lg43_%=:\n" \
"v_mfma_f32_16x16x32_bf16 v[80:83], a[176:179], v[200:203], v[80:83]\n" \
"v_pk_mul_f32 v[56:57], v[56:57], v[178:179] op_sel_hi:[1,0]\n" \
"v_pk_mul_f32 v[58:59], v[58:59], v[178:179] op_sel_hi:[1,0]\n" \
"v_pk_mul_f32 v[60:61], v[60:61], v[178:179] op_sel_hi:[1,0]\n" \
"v_mfma_f32_16x16x32_bf16 v[84:87], a[148:151], v[216:219], v[84:87]\n" \
"v_pk_mul_f32 v[62:63], v[62:63], v[178:179] op_sel_hi:[1,0]\n" \
"v_cvt_pk_bf16_f32 v56, v56, v57\n" \
"v_cvt_pk_bf16_f32 v57, v58, v59\n" \
"v_mfma_f32_16x16x32_bf16 v[84:87], a[180:183], v[200:203], v[84:87]\n" \
"v_cvt_pk_bf16_f32 v58, v60, v61\n" \
"v_cvt_pk_bf16_f32 v59, v62, v63\n" \
"s_cbranch_vccz Lg44_%=\n" \
"s_nop 0\n" \
"buffer_store_dwordx4 v[56:59], v208, %[srdz], s60 offen offset:3072 sc1\n" \
"Lg44_%=:\n" \
"v_mfma_f32_16x16x32_bf16 v[88:91], a[152:155], v[216:219], v[88:91]\n" \
"v_mfma_f32_16x16x32_bf16 v[88:91], a[184:187], v[200:203], v[88:91]\n" \
"v_mfma_f32_16x16x32_bf16 v[92:95], a[156:159], v[216:219], v[92:95]\n" \
"v_mfma_f32_16x16x32_bf16 v[92:95], a[188:191], v[200:203], v[92:95]\n" \
"s_waitcnt vmcnt(12) lgkmcnt(0)\n" \
"s_bitcmp1_b32 0xf, s48\n" \
"s_cbranch_scc1 Lcw1_%=\n" \
"s_waitcnt vmcnt(4)\n" \
"Lcw1_%=:\n" \
"s_barrier\n" \
"s_nop 0\n" \
"ds_read_b128 v[232:235], v236\n" \
"v_mfma_f32_16x16x32_bf16 v[96:99], a[160:163], v[204:207], v[96:99]\n" \
"v_pk_mul_f32 v[64:65], v[64:65], v[180:181] op_sel_hi:[1,0]\n" \
"v_pk_mul_f32 v[66:67], v[66:67], v[180:181] op_sel_hi:[1,0]\n" \
"v_pk_mul_f32 v[68:69], v[68:69], v[180:181] op_sel_hi:[1,0]\n" \
"v_pk_mul_f32 v[70:71], v[70:71], v[180:181] op_sel_hi:[1,0]\n" \
"v_cvt_pk_bf16_f32 v64, v64, v65\n" \
"v_mfma_f32_16x16x32_bf16 v[100:103], a[164:167], v[204:207], v[100:103]\n" \
"v_cvt_pk_bf16_f32 v65, v66, v67\n" \
"v_cvt_pk_bf16_f32 v66, v68, v69\n" \
"v_cvt_pk_bf16_f32 v67, v70, v71\n" \
"s_cbranch_vccz Lg45_%=\n" \
"s_nop 0\n" \
"buffer_store_dwordx4 v[64:67], v209, %[srdz], s60 offen offset:0 sc1\n" \
"Lg45_%=:\n" \
"v_pk_mul_f32 v[72:73], v[72:73], v[180:181] op_sel_hi:[1,0]\n" \
"v_mfma_f32_16x16x32_bf16 v[104:107], a[168:171], v[204:207], v[104:107]\n" \
"v_pk_mul_f32 v[74:75], v[74:75], v[180:181] op_sel_hi:[1,0]\n" \
"v_pk_mul_f32 v[76:77], v[76:77], v[180:181] op_sel_hi:[1,0]\n" \
"v_pk_mul_f32 v[78:79], v[78:79], v[180:181] op_sel_hi:[1,0]\n" \
"v_cvt_pk_bf16_f32 v72, v72, v73\n" \
"v_cvt_pk_bf16_f32 v73, v74, v75\n" \
"v_mfma_f32_16x16x32_bf16 v[108:111], a[172:175], v[204:207], v[108:111]\n" \
"v_cvt_pk_bf16_f32 v74, v76, v77\n" \
"v_cvt_pk_bf16_f32 v75, v78, v79\n" \
"s_cbranch_vccz Lg46_%=\n" \
"s_nop 0\n" \
"buffer_store_dwordx4 v[72:75], v209, %[srdz], s60 offen offset:1024 sc1\n" \
"Lg46_%=:\n" \
"v_pk_mul_f32 v[80:81], v[80:81], v[180:181] op_sel_hi:[1,0]\n" \
"v_pk_mul_f32 v[82:83], v[82:83], v[180:181] op_sel_hi:[1,0]\n" \
"v_mfma_f32_16x16x32_bf16 v[112:115], a[176:179], v[204:207], v[112:115]\n" \
"v_pk_mul_f32 v[84:85], v[84:85], v[180:181] op_sel_hi:[1,0]\n" \
"v_pk_mul_f32 v[86:87], v[86:87], v[180:181] op_sel_hi:[1,0]\n" \
"v_cvt_pk_bf16_f32 v80, v80, v81\n" \
"v_cvt_pk_bf16_f32 v81, v82, v83\n" \
"v_cvt_pk_bf16_f32 v82, v84, v85\n" \
"v_mfma_f32_16x16x32_bf16 v[116:119], a[180:183], v[204:207], v[116:119]\n" \
"v_cvt_pk_bf16_f32 v83, v86, v87\n" \
"s_cbranch_vccz Lg47_%=\n" \
"s_nop 0\n" \
"buffer_store_dwordx4 v[80:83], v209, %[srdz], s60 offen offset:2048 sc1\n" \
"Lg47_%=:\n" \
"v_pk_mul_f32 v[88:89], v[88:89], v[180:181] op_sel_hi:[1,0]\n" \
"v_pk_mul_f32 v[90:91], v[90:91], v[180:181] op_sel_hi:[1,0]\n" \
"v_pk_mul_f32 v[92:93], v[92:93], v[180:181] op_sel_hi:[1,0]\n" \
"v_mfma_f32_16x16x32_bf16 v[120:123], a[184:187], v[204:207], v[120:123]\n" \
"v_pk_mul_f32 v[94:95], v[94:95], v[180:181] op_sel_hi:[1,0]\n" \
"v_cvt_pk_bf16_f32 v88, v88, v89\n" \
"v_cvt_pk_bf16_f32 v89, v90, v91\n" \
"v_cvt_pk_bf16_f32 v90, v92, v93\n" \
"v_cvt_pk_bf16_f32 v91, v94, v95\n" \
"v_mfma_f32_16x16x32_bf16 v[124:127], a[188:191], v[204:207], v[124:127]\n" \
"s_cbranch_vccz Lg48_%=\n" \
"s_nop 0\n" \
"buffer_store_dwordx4 v[88:91], v209, %[srdz], s60 offen offset:3072 sc1\n" \
"Lg48_%=:\n" \
"s_waitcnt lgkmcnt(0)\n" \
"s_nop 0\n" \
"v_or3_b32 v232, v232, v233, v234\n" \
"v_or_b32_e32 v232, v232, v235\n" \
"s_nop 0\n" \
"v_readfirstlane_b32 s55, v232\n" \
"s_nop 3\n" \
"s_andn2_b32 s55, s55, s52\n" \
"s_cbranch_scc1 Lslow_%=\n" \
"v_mfma_f32_16x16x32_bf16 v[192:195], a[196:199], v[0:3], 0\n" \
"v_mfma_f32_16x16x32_bf16 v[192:195], a[200:203], v[8:11], v[192:195]\n" \
"v_pk_mul_f32 v[96:97], v[96:97], v[182:183] op_sel_hi:[1,0]\n" \
"v_pk_mul_f32 v[98:99], v[98:99], v[182:183] op_sel_hi:[1,0]\n" \
"v_mfma_f32_16x16x32_bf16 v[192:195], a[204:207], v[16:19], v[192:195]\n" \
"v_pk_mul_f32 v[100:101], v[100:101], v[182:183] op_sel_hi:[1,0]\n" \
"v_pk_mul_f32 v[102:103], v[102:103], v[182:183] op_sel_hi:[1,0]\n" \
"s_add_u32 m0, %[m0b], 114688\n" \
"s_cmp_gt_u32 s48, 1\n" \
"s_cselect_b32 s49, 1, 2\n" \
"s_lshl_b32 s49, s49, 18\n" \
"s_or_b32 s49, s49, s58\n" \
"buffer_load_dwordx4 %[wo0], %[srdt], s49 offen offset:0 lds\n" \
"v_mfma_f32_16x16x32_bf16 v[192:195], a[208:211], v[24:27], v[192:195]\n" \
"v_cvt_pk_bf16_f32 v96, v96, v97\n" \
"v_cvt_pk_bf16_f32 v97, v98, v99\n" \
"v_mfma_f32_16x16x32_bf16 v[144:147], a[212:215], v[0:3], 0\n" \
"v_cvt_pk_bf16_f32 v98, v100, v101\n" \
"v_mfma_f32_16x16x32_bf16 v[144:147], a[216:219], v[8:11], v[144:147]\n" \
"v_cvt_pk_bf16_f32 v99, v102, v103\n" \
"s_cbranch_vccz Lg1_%=\n" \
"s_nop 0\n" \
"buffer_store_dwordx4 v[96:99], v210, %[srdz], s60 offen offset:0 sc1\n" \
"Lg1_%=:\n" \
"v_mfma_f32_16x16x32_bf16 v[144:147], a[220:223], v[16:19], v[144:147]\n" \
"ds_read_b64_tr_b16 a[228:229], v215 offset:512\n" \
"v_pk_mul_f32 v[104:105], v[104:105], v[182:183] op_sel_hi:[1,0]\n" \
"v_pk_mul_f32 v[106:107], v[106:107], v[182:183] op_sel_hi:[1,0]\n" \
"v_mfma_f32_16x16x32_bf16 v[144:147], a[224:227], v[24:27], v[144:147]\n" \
"ds_read_b64_tr_b16 a[230:231], v215 offset:2560\n" \
"v_pk_mul_f32 v[108:109], v[108:109], v[182:183] op_sel_hi:[1,0]\n" \
"v_mfma_f32_16x16x32_bf16 v[196:199], a[196:199], v[32:35], 0\n" \
"ds_read_b64_tr_b16 a[232:233], v215 offset:4608\n" \
"v_pk_mul_f32 v[110:111], v[110:111], v[182:183] op_sel_hi:[1,0]\n" \
"v_cvt_pk_bf16_f32 v104, v104, v105\n" \
"v_mfma_f32_16x16x32_bf16 v[196:199], a[200:203], v[40:43], v[196:199]\n" \
"ds_read_b64_tr_b16 a[234:235], v215 offset:6656\n" \
"v_cvt_pk_bf16_f32 v105, v106, v107\n" \
"v_cvt_pk_bf16_f32 v106, v108, v109\n" \
"v_mfma_f32_16x16x32_bf16 v[196:199], a[204:207], v[48:51], v[196:199]\n" \
"v_cvt_pk_bf16_f32 v107, v110, v111\n" \
"s_cbranch_vccz Lg2_%=\n" \
"s_nop 0\n" \
"buffer_store_dwordx4 v[104:107], v210, %[srdz], s60 offen offset:1024 sc1\n" \
"Lg2_%=:\n" \
"v_mfma_f32_16x16x32_bf16 v[196:199], a[208:211], v[56:59], v[196:199]\n" \
"v_pk_mul_f32 v[112:113], v[112:113], v[182:183] op_sel_hi:[1,0]\n" \
"v_mfma_f32_16x16x32_bf16 v[148:151], a[212:215], v[32:35], 0\n" \
"v_pk_mul_f32 v[114:115], v[114:115], v[182:183] op_sel_hi:[1,0]\n" \
"v_pk_mul_f32 v[116:117], v[116:117], v[182:183] op_sel_hi:[1,0]\n" \
"buffer_load_dwordx4 v212, %[srdt], s49 offen offset:1024 lds\n" \
"v_mfma_f32_16x16x32_bf16 v[148:151], a[216:219], v[40:43], v[148:151]\n" \
"v_pk_mul_f32 v[118:119], v[118:119], v[182:183] op_sel_hi:[1,0]\n" \
"v_cvt_pk_bf16_f32 v112, v112, v113\n" \
"v_mfma_f32_16x16x32_bf16 v[148:151], a[220:223], v[48:51], v[148:151]\n" \
"ds_read_b64_tr_b16 a[236:237], v215 offset:8704\n" \
"v_cvt_pk_bf16_f32 v113, v114, v115\n" \
"v_mfma_f32_16x16x32_bf16 v[148:151], a[224:227], v[56:59], v[148:151]\n" \
"v_cvt_pk_bf16_f32 v114, v116, v117\n" \
"v_cvt_pk_bf16_f32 v115, v118, v119\n" \
"v_mfma_f32_16x16x32_bf16 v[200:203], a[196:199], v[64:67], 0\n" \
"ds_read_b64_tr_b16 a[238:239], v215 offset:10752\n" \
"s_cbranch_vccz Lg3_%=\n" \
"s_nop 0\n" \
"buffer_store_dwordx4 v[112:115], v210, %[srdz], s60 offen offset:2048 sc1\n" \
"Lg3_%=:\n" \
"v_pk_mul_f32 v[120:121], v[120:121], v[182:183] op_sel_hi:[1,0]\n" \
"v_mfma_f32_16x16x32_bf16 v[200:203], a[200:203], v[72:75], v[200:203]\n" \
"v_pk_mul_f32 v[122:123], v[122:123], v[182:183] op_sel_hi:[1,0]\n" \
"v_pk_mul_f32 v[124:125], v[124:125], v[182:183] op_sel_hi:[1,0]\n" \
"v_mfma_f32_16x16x32_bf16 v[200:203], a[204:207], v[80:83], v[200:203]\n" \
"v_pk_mul_f32 v[126:127], v[126:127], v[182:183] op_sel_hi:[1,0]\n" \
"v_mfma_f32_16x16x32_bf16 v[200:203], a[208:211], v[88:91], v[200:203]\n" \
"v_cvt_pk_bf16_f32 v120, v120, v121\n" \
".if . - adaln_body != 40960\n" \
".error \"size model\"\n" \
".endif\n" \
"Lk9_%=:\n" \
"s_cbranch_execz Lk10_%=\n" \
"s_nop 0\n" \
"v_cvt_pk_bf16_f32 v121, v122, v123\n" \
"v_mfma_f32_16x16x32_bf16 v[152:155], a[212:215], v[64:67], 0\n" \
"v_cvt_pk_bf16_f32 v122, v124, v125\n" \
"v_cvt_pk_bf16_f32 v123, v126, v127\n" \
"v_mfma_f32_16x16x32_bf16 v[152:155], a[216:219], v[72:75], v[152:155]\n" \
"s_cbranch_vccz Lg4_%=\n" \
"s_nop 0\n" \
"buffer_store_dwordx4 v[120:123], v210, %[srdz], s60 offen offset:3072 sc1\n" \
"Lg4_%=:\n" \
"v_mfma_f32_16x16x32_bf16 v[152:155], a[220:223], v[80:83], v[152:155]\n" \
"v_add_u32_e32 v220, 0x10000, v215\n" \
"v_mfma_f32_16x16x32_bf16 v[152:155], a[224:227], v[88:91], v[152:155]\n" \
"v_mov_b32_e64 v230, 1\n" \
"v_readlane_b32 s62, %[tlo], 19\n" \
"v_readlane_b32 s63, %[thi], 19\n" \
"v_and_b32_e32 v233, 0x380, v224\n" \
"v_lshlrev_b32_e64 v233, 2, v233\n" \
"buffer_load_dwordx4 v213, %[srdt], s49 offen offset:2048 lds\n" \
"v_mfma_f32_16x16x32_bf16 v[204:207], a[196:199], v[96:99], 0\n" \
"v_add_u32_e32 v221, 0x10000, v227\n" \
"v_mfma_f32_16x16x32_bf16 v[204:207], a[200:203], v[104:107], v[204:207]\n" \
"ds_read_b64_tr_b16 a[196:197], v215 offset:12800\n" \
"v_mfma_f32_16x16x32_bf16 v[204:207], a[204:207], v[112:115], v[204:207]\n" \
"ds_read_b64_tr_b16 a[198:199], v215 offset:14848\n" \
"v_mfma_f32_16x16x32_bf16 v[204:207], a[208:211], v[120:123], v[204:207]\n" \
"ds_read_b64_tr_b16 a[200:201], v227 offset:512\n" \
"v_mfma_f32_16x16x32_bf16 v[156:159], a[212:215], v[96:99], 0\n" \
"ds_read_b64_tr_b16 a[202:203], v227 offset:2560\n" \
"v_mfma_f32_16x16x32_bf16 v[156:159], a[216:219], v[104:107], v[156:159]\n" \
"ds_read_b64_tr_b16 a[204:205], v227 offset:4608\n" \
"v_mfma_f32_16x16x32_bf16 v[156:159], a[220:223], v[112:115], v[156:159]\n" \
"ds_read_b64_tr_b16 a[206:207], v227 offset:6656\n" \
"v_mfma_f32_16x16x32_bf16 v[156:159], a[224:227], v[120:123], v[156:159]\n" \
"s_waitcnt vmcnt(63) lgkmcnt(4)\n" \
"s_nop 0\n" \
"v_mfma_f32_16x16x32_bf16 v[160:163], a[228:231], v[0:3], 0\n" \
"ds_read_b64_tr_b16 a[208:209], v227 offset:8704\n" \
"v_mfma_f32_16x16x32_bf16 v[160:163], a[232:235], v[8:11], v[160:163]\n" \
"ds_read_b64_tr_b16 a[210:211], v227 offset:10752\n" \
"buffer_load_dwordx4 v214, %[srdt], s49 offen offset:3072 lds\n" \
"v_mfma_f32_16x16x32_bf16 v[164:167], a[228:231], v[32:35], 0\n" \
"ds_read_b64_tr_b16 a[212:213], v227 offset:12800\n" \
"v_mfma_f32_16x16x32_bf16 v[164:167], a[232:235], v[40:43], v[164:167]\n" \
"ds_read_b64_tr_b16 a[214:215], v227 offset:14848\n" \
"v_mfma_f32_16x16x32_bf16 v[168:171], a[228:231], v[64:67], 0\n" \
"v_mfma_f32_16x16x32_bf16 v[168:171], a[232:235], v[72:75], v[168:171]\n" \
"v_mfma_f32_16x16x32_bf16 v[172:175], a[228:231], v[96:99], 0\n" \
"ds_read_b64_tr_b16 a[216:217], v215 offset:16384\n" \
"v_mfma_f32_16x16x32_bf16 v[172:175], a[232:235], v[104:107], v[172:175]\n" \
"ds_read_b64_tr_b16 a[218:219], v215 offset:18432\n" \
"v_mfma_f32_16x16x32_bf16 v[160:163], a[236:239], v[16:19], v[160:163]\n" \
"ds_read_b64_tr_b16 a[220:221], v215 offset:20480\n" \
"v_mfma_f32_16x16x32_bf16 v[160:163], a[196:199], v[24:27], v[160:163]\n" \
"ds_read_b64_tr_b16 a[222:223], v215 offset:22528\n" \
"v_mfma_f32_16x16x32_bf16 v[164:167], a[236:239], v[48:51], v[164:167]\n" \
"v_mfma_f32_16x16x32_bf16 v[164:167], a[196:199], v[56:59], v[164:167]\n" \
"v_mfma_f32_16x16x32_bf16 v[168:171], a[236:239], v[80:83], v[168:171]\n" \
"s_waitcnt vmcnt(63) lgkmcnt(0)\n" \
"s_barrier\n" \
"v_mfma_f32_16x16x32_bf16 v[168:171], a[196:199], v[88:91], v[168:171]\n" \
"v_mfma_f32_16x16x32_bf16 v[172:175], a[236:239], v[112:115], v[172:175]\n" \
"ds_read_b64_tr_b16 a[224:225], v215 offset:24576\n" \
"v_mfma_f32_16x16x32_bf16 v[172:175], a[196:199], v[120:123], v[172:175]\n" \
"s_waitcnt vmcnt(63) lgkmcnt(5)\n" \
"s_nop 0\n" \
"v_mfma_f32_16x16x32_bf16 v[176:179], a[200:203], v[0:3], 0\n" \
"ds_read_b64_tr_b16 a[226:227], v215 offset:26624\n" \
"v_mfma_f32_16x16x32_bf16 v[176:179], a[204:207], v[8:11], v[176:179]\n" \
"ds_read_b64_tr_b16 a[228:229], v215 offset:28672\n" \
"s_add_u32 m0, %[m0b], 49152\n" \
"s_cmp_gt_u32 s48, 1\n" \
"s_cselect_b32 s49, 1, 2\n" \
"s_lshl_b32 s49, s49, 18\n" \
"s_or_b32 s49, s49, s59\n" \
"buffer_load_dwordx4 %[wo0], %[srdt], s49 offen offset:0 lds\n" \
"v_mfma_f32_16x16x32_bf16 v[180:183], a[200:203], v[32:35], 0\n" \
"ds_read_b64_tr_b16 a[230:231], v215 offset:30720\n" \
"v_mfma_f32_16x16x32_bf16 v[180:183], a[204:207], v[40:43], v[180:183]\n" \
"v_mfma_f32_16x16x32_bf16 v[184:187], a[200:203], v[64:67], 0\n" \
"v_mfma_f32_16x16x32_bf16 v[184:187], a[204:207], v[72:75], v[184:187]\n" \
"buffer_load_dwordx4 v212, %[srdt], s49 offen offset:1024 lds\n" \
"v_mfma_f32_16x16x32_bf16 v[188:191], a[200:203], v[96:99], 0\n" \
"ds_read_b64_tr_b16 a[232:233], v227 offset:16384\n" \
"v_mfma_f32_16x16x32_bf16 v[188:191], a[204:207], v[104:107], v[188:191]\n" \
"ds_read_b64_tr_b16 a[234:235], v227 offset:18432\n" \
"v_mfma_f32_16x16x32_bf16 v[176:179], a[208:211], v[16:19], v[176:179]\n" \
"ds_read_b64_tr_b16 a[236:237], v227 offset:20480\n" \
"v_mfma_f32_16x16x32_bf16 v[176:179], a[212:215], v[24:27], v[176:179]\n" \
"ds_read_b64_tr_b16 a[238:239], v227 offset:22528\n" \
"v_mfma_f32_16x16x32_bf16 v[180:183], a[208:211], v[48:51], v[180:183]\n" \
"v_mfma_f32_16x16x32_bf16 v[180:183], a[212:215], v[56:59], v[180:183]\n" \
"v_mfma_f32_16x16x32_bf16 v[184:187], a[208:211], v[80:83], v[184:187]\n" \
"v_mfma_f32_16x16x32_bf16 v[184:187], a[212:215], v[88:91], v[184:187]\n" \
"buffer_load_dwordx4 v213, %[srdt], s49 offen offset:2048 lds\n" \
"v_mfma_f32_16x16x32_bf16 v[188:191], a[208:211], v[112:115], v[188:191]\n" \
"ds_read_b64_tr_b16 a[196:197], v227 offset:24576\n" \
"v_mfma_f32_16x16x32_bf16 v[188:191], a[212:215], v[120:123], v[188:191]\n" \
"s_waitcnt vmcnt(63) lgkmcnt(5)\n" \
"s_nop 0\n" \
"v_mfma_f32_16x16x32_bf16 v[4:7], a[216:219], v[0:3], 0\n" \
"ds_read_b64_tr_b16 a[198:199], v227 offset:26624\n" \
"v_mfma_f32_16x16x32_bf16 v[4:7], a[220:223], v[8:11], v[4:7]\n" \
"ds_read_b64_tr_b16 a[200:201], v227 offset:28672\n" \
"v_mfma_f32_16x16x32_bf16 v[36:39], a[216:219], v[32:35], 0\n" \
"ds_read_b64_tr_b16 a[202:203], v227 offset:30720\n" \
"v_mfma_f32_16x16x32_bf16 v[36:39], a[220:223], v[40:43], v[36:39]\n" \
"v_mfma_f32_16x16x32_bf16 v[68:71], a[216:219], v[64:67], 0\n" \
"buffer_load_dwordx4 v214, %[srdt], s49 offen offset:3072 lds\n" \
"v_mfma_f32_16x16x32_bf16 v[68:71], a[220:223], v[72:75], v[68:71]\n" \
"v_mfma_f32_16x16x32_bf16 v[100:103], a[216:219], v[96:99], 0\n" \
"ds_read_b64_tr_b16 a[204:205], v215 offset:16896\n" \
"v_mfma_f32_16x16x32_bf16 v[100:103], a[220:223], v[104:107], v[100:103]\n" \
"ds_read_b64_tr_b16 a[206:207], v215 offset:18944\n" \
"v_mfma_f32_16x16x32_bf16 v[4:7], a[224:227], v[16:19], v[4:7]\n" \
"ds_read_b64_tr_b16 a[208:209], v215 offset:20992\n" \
"v_mfma_f32_16x16x32_bf16 v[4:7], a[228:231], v[24:27], v[4:7]\n" \
"ds_read_b64_tr_b16 a[210:211], v215 offset:23040\n" \
"v_mfma_f32_16x16x32_bf16 v[36:39], a[224:227], v[48:51], v[36:39]\n" \
"v_mfma_f32_16x16x32_bf16 v[36:39], a[228:231], v[56:59], v[36:39]\n" \
"v_mfma_f32_16x16x32_bf16 v[68:71], a[224:227], v[80:83], v[68:71]\n" \
"v_mfma_f32_16x16x32_bf16 v[68:71], a[228:231], v[88:91], v[68:71]\n" \
"v_mfma_f32_16x16x32_bf16 v[100:103], a[224:227], v[112:115], v[100:103]\n" \
"ds_read_b64_tr_b16 a[212:213], v215 offset:25088\n" \
"v_mfma_f32_16x16x32_bf16 v[100:103], a[228:231], v[120:123], v[100:103]\n" \
"s_waitcnt vmcnt(63) lgkmcnt(5)\n" \
"s_nop 0\n" \
"v_mfma_f32_16x16x32_bf16 v[12:15], a[232:235], v[0:3], 0\n" \
"ds_read_b64_tr_b16 a[214:215], v215 offset:27136\n" \
"s_cmp_gt_u32 s48, 0\n" \
"s_cselect_b32 s50, 0, 1\n" \
"s_mul_i32 s50, s50, 152064\n" \
"buffer_load_dwordx4 a[64:67], %[oq0], %[srdz], s50 offen offset:0 sc1\n" \
"v_mfma_f32_16x16x32_bf16 v[12:15], a[236:239], v[8:11], v[12:15]\n" \
"ds_read_b64_tr_b16 a[216:217], v215 offset:29184\n" \
"v_mfma_f32_16x16x32_bf16 v[44:47], a[232:235], v[32:35], 0\n" \
"ds_read_b64_tr_b16 a[218:219], v215 offset:31232\n" \
"buffer_load_dwordx4 a[68:71], %[oq0], %[srdz], s50 offen offset:1024 sc1\n" \
"v_mfma_f32_16x16x32_bf16 v[44:47], a[236:239], v[40:43], v[44:47]\n" \
"v_mfma_f32_16x16x32_bf16 v[76:79], a[232:235], v[64:67], 0\n" \
"buffer_load_dwordx4 a[80:83], v208, %[srdz], s50 offen offset:0 sc1\n" \
"v_mfma_f32_16x16x32_bf16 v[76:79], a[236:239], v[72:75], v[76:79]\n" \
"v_mfma_f32_16x16x32_bf16 v[108:111], a[232:235], v[96:99], 0\n" \
"ds_read_b64_tr_b16 a[220:221], v227 offset:16896\n" \
"buffer_load_dwordx4 a[84:87], v208, %[srdz], s50 offen offset:1024 sc1\n" \
"v_mfma_f32_16x16x32_bf16 v[108:111], a[236:239], v[104:107], v[108:111]\n" \
"ds_read_b64_tr_b16 a[222:223], v227 offset:18944\n" \
"v_mfma_f32_16x16x32_bf16 v[12:15], a[196:199], v[16:19], v[12:15]\n" \
"ds_read_b64_tr_b16 a[224:225], v227 offset:25088\n" \
"buffer_load_dwordx4 a[96:99], v209, %[srdz], s50 offen offset:0 sc1\n" \
"v_mfma_f32_16x16x32_bf16 v[12:15], a[200:203], v[24:27], v[12:15]\n" \
"ds_read_b64_tr_b16 a[226:227], v227 offset:27136\n" \
"v_mfma_f32_16x16x32_bf16 v[44:47], a[196:199], v[48:51], v[44:47]\n" \
"ds_read_b64_tr_b16 a[228:229], v227 offset:20992\n" \
"buffer_load_dwordx4 a[100:103], v209, %[srdz], s50 offen offset:1024 sc1\n" \
"v_mfma_f32_16x16x32_bf16 v[44:47], a[200:203], v[56:59], v[44:47]\n" \
"ds_read_b64_tr_b16 a[230:231], v227 offset:23040\n" \
"v_mfma_f32_16x16x32_bf16 v[76:79], a[196:199], v[80:83], v[76:79]\n" \
"ds_read_b64_tr_b16 a[232:233], v227 offset:29184\n" \
"buffer_load_dwordx4 a[112:115], v210, %[srdz], s50 offen offset:0 sc1\n" \
"v_mfma_f32_16x16x32_bf16 v[76:79], a[200:203], v[88:91], v[76:79]\n" \
"ds_read_b64_tr_b16 a[234:235], v227 offset:31232\n" \
"v_mfma_f32_16x16x32_bf16 v[108:111], a[196:199], v[112:115], v[108:111]\n" \
"ds_read_b64_tr_b16 a[236:237], v215 offset:32768\n" \
"buffer_load_dwordx4 a[116:119], v210, %[srdz], s50 offen offset:1024 sc1\n" \
"v_mfma_f32_16x16x32_bf16 v[108:111], a[200:203], v[120:123], v[108:111]\n" \
"s_waitcnt vmcnt(63) lgkmcnt(13)\n" \
"s_nop 0\n" \
"v_mfma_f32_16x16x32_bf16 v[20:23], a[204:207], v[0:3], 0\n" \
"ds_read_b64_tr_b16 a[238:239], v215 offset:34816\n" \
"buffer_load_dwordx4 a[72:75], %[oq0], %[srdz], s50 offen offset:2048 sc1\n" \
"v_mfma_f32_16x16x32_bf16 v[20:23], a[208:211], v[8:11], v[20:23]\n" \
"ds_read_b64_tr_b16 a[196:197], v215 offset:36864\n" \
"v_mfma_f32_16x16x32_bf16 v[52:55], a[204:207], v[32:35], 0\n" \
"ds_read_b64_tr_b16 a[198:199], v215 offset:38912\n" \
"buffer_load_dwordx4 a[76:79], %[oq0], %[srdz], s50 offen offset:3072 sc1\n" \
"v_mfma_f32_16x16x32_bf16 v[52:55], a[208:211], v[40:43], v[52:55]\n" \
"v_mfma_f32_16x16x32_bf16 v[84:87], a[204:207], v[64:67], 0\n" \
"s_waitcnt vmcnt(63) lgkmcnt(4)\n" \
"s_barrier\n" \
"v_mfma_f32_16x16x32_bf16 v[84:87], a[208:211], v[72:75], v[84:87]\n" \
"buffer_load_dwordx4 a[88:91], v208, %[srdz], s50 offen offset:2048 sc1\n" \
"v_mfma_f32_16x16x32_bf16 v[116:119], a[204:207], v[96:99], 0\n" \
"ds_read_b64_tr_b16 a[200:201], v215 offset:40960\n" \
"v_mfma_f32_16x16x32_bf16 v[116:119], a[208:211], v[104:107], v[116:119]\n" \
"ds_read_b64_tr_b16 a[202:203], v215 offset:43008\n" \
"buffer_load_dwordx4 a[92:95], v208, %[srdz], s50 offen offset:3072 sc1\n" \
"v_mfma_f32_16x16x32_bf16 v[20:23], a[212:215], v[16:19], v[20:23]\n" \
"ds_read_b64_tr_b16 a[204:205], v215 offset:45056\n" \
"v_mfma_f32_16x16x32_bf16 v[20:23], a[216:219], v[24:27], v[20:23]\n" \
"ds_read_b64_tr_b16 a[206:207], v215 offset:47104\n" \
"buffer_load_dwordx4 a[104:107], v209, %[srdz], s50 offen offset:2048 sc1\n" \
"v_mfma_f32_16x16x32_bf16 v[52:55], a[212:215], v[48:51], v[52:55]\n" \
"v_mfma_f32_16x16x32_bf16 v[52:55], a[216:219], v[56:59], v[52:55]\n" \
"buffer_load_dwordx4 a[108:111], v209, %[srdz], s50 offen offset:3072 sc1\n" \
"v_mfma_f32_16x16x32_bf16 v[84:87], a[212:215], v[80:83], v[84:87]\n" \
"v_mfma_f32_16x16x32_bf16 v[84:87], a[216:219], v[88:91], v[84:87]\n" \
"buffer_load_dwordx4 a[120:123], v210, %[srdz], s50 offen offset:2048 sc1\n" \
"v_mfma_f32_16x16x32_bf16 v[116:119], a[212:215], v[112:115], v[116:119]\n" \
"v_mfma_f32_16x16x32_bf16 v[116:119], a[216:219], v[120:123], v[116:119]\n" \
"s_waitcnt vmcnt(63) lgkmcnt(10)\n" \
"s_nop 0\n" \
"v_mfma_f32_16x16x32_bf16 v[28:31], a[220:223], v[0:3], 0\n" \
"buffer_load_dwordx4 a[124:127], v210, %[srdz], s50 offen offset:3072 sc1\n" \
"v_mfma_f32_16x16x32_bf16 v[28:31], a[228:231], v[8:11], v[28:31]\n" \
"s_sub_u32 s61, s48, 4\n" \
"s_cmp_lt_u32 s61, 2\n" \
"s_cselect_b64 vcc, -1, 0\n" \
"s_nop 0\n" \
"v_mfma_f32_16x16x32_bf16 v[60:63], a[220:223], v[32:35], 0\n" \
"s_cbranch_vccz Lg5_%=\n" \
"s_nop 0\n" \
"buffer_store_dwordx4 v[0:3], %[oq0], %[srdz], s60 offen offset:0 sc1\n" \
"Lg5_%=:\n" \
"v_mfma_f32_16x16x32_bf16 v[60:63], a[228:231], v[40:43], v[60:63]\n" \
"v_mfma_f32_16x16x32_bf16 v[92:95], a[220:223], v[64:67], 0\n" \
"s_cmp_gt_u32 s48, 1\n" \
"s_cselect_b32 s50, 1, 2\n" \
"s_mul_i32 s50, s50, 152064\n" \
"buffer_load_dwordx4 a[0:3], %[oq0], %[srdz], s50 offen offset:0 sc1\n" \
"v_mfma_f32_16x16x32_bf16 v[92:95], a[228:231], v[72:75], v[92:95]\n" \
"v_mfma_f32_16x16x32_bf16 v[124:127], a[220:223], v[96:99], 0\n" \
"ds_read_b64_tr_b16 a[208:209], v227 offset:32768\n" \
"v_mfma_f32_16x16x32_bf16 v[124:127], a[228:231], v[104:107], v[124:127]\n" \
"ds_read_b64_tr_b16 a[210:211], v227 offset:34816\n" \
"s_waitcnt vmcnt(15) lgkmcnt(6)\n" \
"s_nop 0\n" \
"v_mfma_f32_16x16x32_bf16 v[128:131], a[236:239], a[64:67], v[192:195]\n" \
"ds_read_b64_tr_b16 a[212:213], v227 offset:36864\n" \
"v_mfma_f32_16x16x32_bf16 v[128:131], a[196:199], a[68:71], v[128:131]\n" \
"ds_read_b64_tr_b16 a[214:215], v227 offset:38912\n" \
"s_waitcnt vmcnt(13) lgkmcnt(15)\n" \
"s_nop 0\n" \
"v_mfma_f32_16x16x32_bf16 v[132:135], a[236:239], a[80:83], v[196:199]\n" \
"s_cbranch_vccz Lg6_%=\n" \
"s_nop 0\n" \
"buffer_store_dwordx4 v[8:11], %[oq0], %[srdz], s60 offen offset:1024 sc1\n" \
"Lg6_%=:\n" \
"v_mfma_f32_16x16x32_bf16 v[132:135], a[196:199], a[84:87], v[132:135]\n" \
"s_waitcnt vmcnt(13) lgkmcnt(15)\n" \
"s_bitcmp1_b32 0x30, s48\n" \
"s_cbranch_scc1 Lcw2_%=\n" \
"s_waitcnt vmcnt(11)\n" \
"Lcw2_%=:\n" \
"v_mfma_f32_16x16x32_bf16 v[136:139], a[236:239], a[96:99], v[200:203]\n" \
"buffer_load_dwordx4 a[4:7], %[oq0], %[srdz], s50 offen offset:1024 sc1\n" \
"v_mfma_f32_16x16x32_bf16 v[136:139], a[196:199], a[100:103], v[136:139]\n" \
"s_waitcnt vmcnt(12) lgkmcnt(15)\n" \
"s_bitcmp1_b32 0x30, s48\n" \
"s_cbranch_scc1 Lcw3_%=\n" \
"s_waitcnt vmcnt(10)\n" \
"Lcw3_%=:\n" \
"v_mfma_f32_16x16x32_bf16 v[140:143], a[236:239], a[112:115], v[204:207]\n" \
"ds_read_b64_tr_b16 a[216:217], v227 offset:40960\n" \
"v_mfma_f32_16x16x32_bf16 v[140:143], a[196:199], a[116:119], v[140:143]\n" \
"s_waitcnt vmcnt(10) lgkmcnt(5)\n" \
"s_bitcmp1_b32 0x30, s48\n" \
"s_cbranch_scc1 Lcw4_%=\n" \
"s_waitcnt vmcnt(8)\n" \
"Lcw4_%=:\n" \
"v_mfma_f32_16x16x32_bf16 v[128:131], a[200:203], a[72:75], v[128:131]\n" \
"ds_read_b64_tr_b16 a[218:219], v227 offset:43008\n" \
"buffer_load_dwordx4 a[8:11], %[oq0], %[srdz], s50 offen offset:2048 sc1\n" \
"v_mfma_f32_16x16x32_bf16 v[128:131], a[204:207], a[76:79], v[128:131]\n" \
"ds_read_b64_tr_b16 a[220:221], v227 offset:45056\n" \
"s_waitcnt vmcnt(9) lgkmcnt(15)\n" \
"s_bitcmp1_b32 0x30, s48\n" \
"s_cbranch_scc1 Lcw5_%=\n" \
"s_waitcnt vmcnt(7)\n" \
"Lcw5_%=:\n" \
"v_mfma_f32_16x16x32_bf16 v[132:135], a[200:203], a[88:91], v[132:135]\n" \
"ds_read_b64_tr_b16 a[222:223], v227 offset:47104\n" \
"v_mfma_f32_16x16x32_bf16 v[132:135], a[204:207], a[92:95], v[132:135]\n" \
"s_cbranch_vccz Lg7_%=\n" \
"s_nop 0\n" \
"buffer_store_dwordx4 v[16:19], %[oq0], %[srdz], s60 offen offset:2048 sc1\n" \
"Lg7_%=:\n" \
"s_waitcnt vmcnt(6) lgkmcnt(14)\n" \
"s_bitcmp1_b32 0x30, s48\n" \
"s_cbranch_scc1 Lcw6_%=\n" \
"s_waitcnt vmcnt(3)\n" \
"Lcw6_%=:\n" \
"v_mfma_f32_16x16x32_bf16 v[136:139], a[200:203], a[104:107], v[136:139]\n" \
"v_mfma_f32_16x16x32_bf16 v[136:139], a[204:207], a[108:111], v[136:139]\n" \
"buffer_load_dwordx4 a[12:15], %[oq0], %[srdz], s50 offen offset:3072 sc1\n" \
"v_mfma_f32_16x16x32_bf16 v[140:143], a[200:203], a[120:123], v[140:143]\n" \
"ds_read_b64_tr_b16 a[228:229], v215 offset:33280\n" \
"v_mfma_f32_16x16x32_bf16 v[140:143], a[204:207], a[124:127], v[140:143]\n" \
"ds_read_b64_tr_b16 a[230:231], v215 offset:35328\n" \
"v_mfma_f32_16x16x32_bf16 v[28:31], a[224:227], v[16:19], v[28:31]\n" \
"buffer_load_dwordx4 a[16:19], v208, %[srdz], s50 offen offset:0 sc1\n" \
"v_mfma_f32_16x16x32_bf16 v[28:31], a[232:235], v[24:27], v[28:31]\n" \
"v_mfma_f32_16x16x32_bf16 v[60:63], a[224:227], v[48:51], v[60:63]\n" \
"v_mfma_f32_16x16x32_bf16 v[60:63], a[232:235], v[56:59], v[60:63]\n" \
"s_cbranch_vccz Lg8_%=\n" \
"s_nop 0\n" \
"buffer_store_dwordx4 v[24:27], %[oq0], %[srdz], s60 offen offset:3072 sc1\n" \
"Lg8_%=:\n" \
"v_mfma_f32_16x16x32_bf16 v[92:95], a[224:227], v[80:83], v[92:95]\n" \
"v_mfma_f32_16x16x32_bf16 v[92:95], a[232:235], v[88:91], v[92:95]\n" \
"v_cmp_u_f32_e64 s[44:45], v128, v132\n" \
"buffer_load_dwordx4 a[20:23], v208, %[srdz], s50 offen offset:1024 sc1\n" \
"v_mfma_f32_16x16x32_bf16 v[124:127], a[224:227], v[112:115], v[124:127]\n" \
"v_mfma_f32_16x16x32_bf16 v[124:127], a[232:235], v[120:123], v[124:127]\n" \
"v_cmp_u_f32_e64 s[56:57], v136, v140\n" \
"s_or_b64 s[56:57], s[56:57], s[44:45]\n" \
"s_cbranch_scc1 Lsp1_%=\n" \
"Lbk1_%=:\n" \
"s_waitcnt vmcnt(63) lgkmcnt(2)\n" \
"s_nop 0\n" \
"v_mfma_f32_16x16x32_bf16 v[144:147], a[208:211], a[64:67], v[144:147]\n" \
"ds_read_b64_tr_b16 a[224:225], v215 offset:37376\n" \
"v_mfma_f32_16x16x32_bf16 v[144:147], a[212:215], a[68:71], v[144:147]\n" \
"ds_read_b64_tr_b16 a[226:227], v215 offset:39424\n" \
"buffer_load_dwordx4 a[24:27], v208, %[srdz], s50 offen offset:2048 sc1\n" \
"v_mfma_f32_16x16x32_bf16 v[148:151], a[208:211], a[80:83], v[148:151]\n" \
"ds_read_b64_tr_b16 a[196:197], v215 offset:41472\n" \
"v_mfma_f32_16x16x32_bf16 v[148:151], a[212:215], a[84:87], v[148:151]\n" \
"ds_read_b64_tr_b16 a[198:199], v215 offset:43520\n" \
"v_mfma_f32_16x16x32_bf16 v[152:155], a[208:211], a[96:99], v[152:155]\n" \
"ds_read_b64_tr_b16 a[200:201], v215 offset:45568\n" \
"s_cbranch_vccz Lg9_%=\n" \
"s_nop 0\n" \
"buffer_store_dwordx4 v[32:35], v208, %[srdz], s60 offen offset:0 sc1\n" \
"Lg9_%=:\n" \
"v_mfma_f32_16x16x32_bf16 v[152:155], a[212:215], a[100:103], v[152:155]\n" \
"ds_read_b64_tr_b16 a[202:203], v215 offset:47616\n" \
"v_mfma_f32_16x16x32_bf16 v[156:159], a[208:211], a[112:115], v[156:159]\n" \
"ds_read_b64_tr_b16 a[204:205], v227 offset:33280\n" \
"buffer_load_dwordx4 a[28:31], v208, %[srdz], s50 offen offset:3072 sc1\n" \
"v_mfma_f32_16x16x32_bf16 v[156:159], a[212:215], a[116:119], v[156:159]\n" \
"ds_read_b64_tr_b16 a[206:207], v227 offset:35328\n" \
"v_mfma_f32_16x16x32_bf16 v[144:147], a[216:219], a[72:75], v[144:147]\n" \
"ds_read_b64_tr_b16 a[232:233], v227 offset:37376\n" \
"v_mfma_f32_16x16x32_bf16 v[144:147], a[220:223], a[76:79], v[144:147]\n" \
"ds_read_b64_tr_b16 a[234:235], v227 offset:39424\n" \
"v_mfma_f32_16x16x32_bf16 v[148:151], a[216:219], a[88:91], v[148:151]\n" \
"buffer_load_dwordx4 a[32:35], v209, %[srdz], s50 offen offset:0 sc1\n" \
"v_mfma_f32_16x16x32_bf16 v[148:151], a[220:223], a[92:95], v[148:151]\n" \
"v_mfma_f32_16x16x32_bf16 v[152:155], a[216:219], a[104:107], v[152:155]\n" \
"s_cbranch_vccz Lg10_%=\n" \
"s_nop 0\n" \
"buffer_store_dwordx4 v[40:43], v208, %[srdz], s60 offen offset:1024 sc1\n" \
"Lg10_%=:\n" \
"v_mfma_f32_16x16x32_bf16 v[152:155], a[220:223], a[108:111], v[152:155]\n" \
"v_mfma_f32_16x16x32_bf16 v[156:159], a[216:219], a[120:123], v[156:159]\n" \
"ds_read_b64_tr_b16 a[236:237], v227 offset:41472\n" \
"buffer_load_dwordx4 a[36:39], v209, %[srdz], s50 offen offset:1024 sc1\n" \
"v_mfma_f32_16x16x32_bf16 v[156:159], a[220:223], a[124:127], v[156:159]\n" \
"s_waitcnt vmcnt(63) lgkmcnt(5)\n" \
"s_nop 0\n" \
"v_mfma_f32_16x16x32_bf16 v[160:163], a[228:231], a[64:67], v[160:163]\n" \
"ds_read_b64_tr_b16 a[238:239], v227 offset:43520\n" \
"v_mfma_f32_16x16x32_bf16 v[160:163], a[224:227], a[68:71], v[160:163]\n" \
"ds_read_b64_tr_b16 a[208:209], v227 offset:45568\n" \
"v_mfma_f32_16x16x32_bf16 v[164:167], a[228:231], a[80:83], v[164:167]\n" \
"ds_read_b64_tr_b16 a[210:211], v227 offset:47616\n" \
"buffer_load_dwordx4 a[40:43], v209, %[srdz], s50 offen offset:2048 sc1\n" \
"v_mfma_f32_16x16x32_bf16 v[164:167], a[224:227], a[84:87], v[164:167]\n" \
"v_mfma_f32_16x16x32_bf16 v[168:171], a[228:231], a[96:99], v[168:171]\n" \
"v_mfma_f32_16x16x32_bf16 v[168:171], a[224:227], a[100:103], v[168:171]\n" \
"s_cbranch_vccz Lg11_%=\n" \
"s_nop 0\n" \
"buffer_store_dwordx4 v[48:51], v208, %[srdz], s60 offen offset:2048 sc1\n" \
"Lg11_%=:\n" \
"v_mfma_f32_16x16x32_bf16 v[172:175], a[228:231], a[112:115], v[172:175]\n" \
"v_mfma_f32_16x16x32_bf16 v[172:175], a[224:227], a[116:119], v[172:175]\n" \
"buffer_load_dwordx4 a[44:47], v209, %[srdz], s50 offen offset:3072 sc1\n" \
"v_mfma_f32_16x16x32_bf16 v[160:163], a[196:199], a[72:75], v[160:163]\n" \
"v_mfma_f32_16x16x32_bf16 v[160:163], a[200:203], a[76:79], v[160:163]\n" \
"buffer_load_dwordx4 a[48:51], v210, %[srdz], s50 offen offset:0 sc1\n" \
"v_mfma_f32_16x16x32_bf16 v[164:167], a[196:199], a[88:91], v[164:167]\n" \
"v_mfma_f32_16x16x32_bf16 v[164:167], a[200:203], a[92:95], v[164:167]\n" \
"buffer_load_dwordx4 a[52:55], v210, %[srdz], s50 offen offset:1024 sc1\n" \
"v_mfma_f32_16x16x32_bf16 v[168:171], a[196:199], a[104:107], v[168:171]\n" \
"s_waitcnt vmcnt(54) lgkmcnt(0)\n" \
"s_bitcmp1_b32 0xf, s48\n" \
"s_cbranch_scc1 Lcw7_%=\n" \
"s_waitcnt vmcnt(45)\n" \
"s_bitcmp1_b32 0x30, s48\n" \
"s_cbranch_scc1 Lcw7_%=\n" \
"s_waitcnt vmcnt(38)\n" \
"Lcw7_%=:\n" \
"s_barrier\n" \
"v_mfma_f32_16x16x32_bf16 v[168:171], a[200:203], a[108:111], v[168:171]\n" \
"ds_read_b64_tr_b16 a[212:213], v220 offset:32768\n" \
"s_cbranch_vccz Lg12_%=\n" \
"s_nop 0\n" \
"buffer_store_dwordx4 v[56:59], v208, %[srdz], s60 offen offset:3072 sc1\n" \
"Lg12_%=:\n" \
"v_mfma_f32_16x16x32_bf16 v[172:175], a[196:199], a[120:123], v[172:175]\n" \
"ds_read_b64_tr_b16 a[214:215], v220 offset:34816\n" \
"v_mfma_f32_16x16x32_bf16 v[172:175], a[200:203], a[124:127], v[172:175]\n" \
"s_waitcnt vmcnt(63) lgkmcnt(2)\n" \
"s_nop 0\n" \
"v_mfma_f32_16x16x32_bf16 v[176:179], a[204:207], a[64:67], v[176:179]\n" \
"ds_read_b64_tr_b16 a[216:217], v220 offset:36864\n" \
"buffer_load_dwordx4 a[56:59], v210, %[srdz], s50 offen offset:2048 sc1\n" \
"v_mfma_f32_16x16x32_bf16 v[176:179], a[232:235], a[68:71], v[176:179]\n" \
"ds_read_b64_tr_b16 a[218:219], v220 offset:38912\n" \
"v_mfma_f32_16x16x32_bf16 v[180:183], a[204:207], a[80:83], v[180:183]\n" \
"ds_read_b64_tr_b16 a[220:221], v220 offset:40960\n" \
"v_mfma_f32_16x16x32_bf16 v[180:183], a[232:235], a[84:87], v[180:183]\n" \
"ds_read_b64_tr_b16 a[222:223], v220 offset:43008\n" \
"v_mfma_f32_16x16x32_bf16 v[184:187], a[204:207], a[96:99], v[184:187]\n" \
"ds_read_b64_tr_b16 a[228:229], v220 offset:45056\n" \
"buffer_load_dwordx4 a[60:63], v210, %[srdz], s50 offen offset:3072 sc1\n" \
"v_mfma_f32_16x16x32_bf16 v[184:187], a[232:235], a[100:103], v[184:187]\n" \
"ds_read_b64_tr_b16 a[230:231], v220 offset:47104\n" \
"v_mfma_f32_16x16x32_bf16 v[188:191], a[204:207], a[112:115], v[188:191]\n" \
"ds_read_b64_tr_b16 a[224:225], v221 offset:32768\n" \
"s_cbranch_vccz Lg13_%=\n" \
"s_nop 0\n" \
"buffer_store_dwordx4 v[64:67], v209, %[srdz], s60 offen offset:0 sc1\n" \
"Lg13_%=:\n" \
"v_mfma_f32_16x16x32_bf16 v[188:191], a[232:235], a[116:119], v[188:191]\n" \
"ds_read_b64_tr_b16 a[226:227], v221 offset:34816\n" \
"v_mfma_f32_16x16x32_bf16 v[176:179], a[236:239], a[72:75], v[176:179]\n" \
"ds_read_b64_tr_b16 a[196:197], v221 offset:36864\n" \
"v_mfma_f32_16x16x32_bf16 v[176:179], a[208:211], a[76:79], v[176:179]\n" \
"ds_read_b64_tr_b16 a[198:199], v221 offset:38912\n" \
"v_mfma_f32_16x16x32_bf16 v[180:183], a[236:239], a[88:91], v[180:183]\n" \
"s_cmp_gt_u32 s48, 2\n" \
"s_cselect_b32 s50, 2, 3\n" \
"s_mul_i32 s50, s50, 152064\n" \
"buffer_load_dwordx4 a[128:131], %[oq0], %[srdz], s50 offen offset:0 sc1\n" \
"v_mfma_f32_16x16x32_bf16 v[180:183], a[208:211], a[92:95], v[180:183]\n" \
"v_mfma_f32_16x16x32_bf16 v[184:187], a[236:239], a[104:107], v[184:187]\n" \
"v_mfma_f32_16x16x32_bf16 v[184:187], a[208:211], a[108:111], v[184:187]\n" \
"v_mfma_f32_16x16x32_bf16 v[188:191], a[236:239], a[120:123], v[188:191]\n" \
"ds_read_b64_tr_b16 a[200:201], v221 offset:40960\n" \
"s_cbranch_vccz Lg14_%=\n" \
"s_nop 0\n" \
"buffer_store_dwordx4 v[72:75], v209, %[srdz], s60 offen offset:1024 sc1\n" \
"Lg14_%=:\n" \
"v_mfma_f32_16x16x32_bf16 v[188:191], a[208:211], a[124:127], v[188:191]\n" \
"s_waitcnt vmcnt(63) lgkmcnt(5)\n" \
"s_nop 0\n" \
"v_mfma_f32_16x16x32_bf16 v[4:7], a[212:215], a[64:67], v[4:7]\n" \
"ds_read_b64_tr_b16 a[202:203], v221 offset:43008\n" \
"v_mfma_f32_16x16x32_bf16 v[4:7], a[216:219], a[68:71], v[4:7]\n" \
"ds_read_b64_tr_b16 a[204:205], v221 offset:45056\n" \
"s_add_u32 m0, %[m0b], 0\n" \
"s_cmp_gt_u32 s48, 2\n" \
"s_cselect_b32 s49, 2, 3\n" \
"s_lshl_b32 s49, s49, 18\n" \
"s_or_b32 s49, s49, s58\n" \
"s_nop 0\n" \
"buffer_load_dwordx4 %[wo0], %[srdt], s49 offen offset:0 lds\n" \
"v_mfma_f32_16x16x32_bf16 v[36:39], a[212:215], a[80:83], v[36:39]\n" \
"ds_read_b64_tr_b16 a[206:207], v221 offset:47104\n" \
"v_mfma_f32_16x16x32_bf16 v[36:39], a[216:219], a[84:87], v[36:39]\n" \
"buffer_load_dwordx4 a[132:135], %[oq0], %[srdz], s50 offen offset:1024 sc1\n" \
"v_mfma_f32_16x16x32_bf16 v[68:71], a[212:215], a[96:99], v[68:71]\n" \
"v_mfma_f32_16x16x32_bf16 v[68:71], a[216:219], a[100:103], v[68:71]\n" \
"v_mfma_f32_16x16x32_bf16 v[100:103], a[212:215], a[112:115], v[100:103]\n" \
"ds_read_b64_tr_b16 a[232:233], v220 offset:33280\n" \
"v_mfma_f32_16x16x32_bf16 v[100:103], a[216:219], a[116:119], v[100:103]\n" \
"ds_read_b64_tr_b16 a[234:235], v220 offset:35328\n" \
"s_cbranch_vccz Lg15_%=\n" \
"s_nop 0\n" \
"buffer_store_dwordx4 v[80:83], v209, %[srdz], s60 offen offset:2048 sc1\n" \
"Lg15_%=:\n" \
"v_mfma_f32_16x16x32_bf16 v[4:7], a[220:223], a[72:75], v[4:7]\n" \
"ds_read_b64_tr_b16 a[236:237], v220 offset:37376\n" \
"v_mfma_f32_16x16x32_bf16 v[4:7], a[228:231], a[76:79], v[4:7]\n" \
"ds_read_b64_tr_b16 a[238:239], v220 offset:39424\n" \
"buffer_load_dwordx4 a[136:139], %[oq0], %[srdz], s50 offen offset:2048 sc1\n" \
"v_mfma_f32_16x16x32_bf16 v[36:39], a[220:223], a[88:91], v[36:39]\n" \
"v_mfma_f32_16x16x32_bf16 v[36:39], a[228:231], a[92:95], v[36:39]\n" \
"v_mfma_f32_16x16x32_bf16 v[68:71], a[220:223], a[104:107], v[68:71]\n" \
"v_mfma_f32_16x16x32_bf16 v[68:71], a[228:231], a[108:111], v[68:71]\n" \
"buffer_load_dwordx4 a[140:143], %[oq0], %[srdz], s50 offen offset:3072 sc1\n" \
"v_mfma_f32_16x16x32_bf16 v[100:103], a[220:223], a[120:123], v[100:103]\n" \
"ds_read_b64_tr_b16 a[208:209], v220 offset:41472\n" \
"v_mfma_f32_16x16x32_bf16 v[100:103], a[228:231], a[124:127], v[100:103]\n" \
"s_waitcnt vmcnt(63) lgkmcnt(5)\n" \
"s_nop 0\n" \
"v_mfma_f32_16x16x32_bf16 v[12:15], a[224:227], a[64:67], v[12:15]\n" \
"ds_read_b64_tr_b16 a[210:211], v220 offset:43520\n" \
"s_cbranch_vccz Lg16_%=\n" \
"s_nop 0\n" \
"buffer_store_dwordx4 v[88:91], v209, %[srdz], s60 offen offset:3072 sc1\n" \
"Lg16_%=:\n" \
"v_mfma_f32_16x16x32_bf16 v[12:15], a[196:199], a[68:71], v[12:15]\n" \
"ds_read_b64_tr_b16 a[212:213], v220 offset:45568\n" \
"v_mfma_f32_16x16x32_bf16 v[44:47], a[224:227], a[80:83], v[44:47]\n" \
"ds_read_b64_tr_b16 a[214:215], v220 offset:47616\n" \
"buffer_load_dwordx4 v212, %[srdt], s49 offen offset:1024 lds\n" \
"v_mfma_f32_16x16x32_bf16 v[44:47], a[196:199], a[84:87], v[44:47]\n" \
"v_mfma_f32_16x16x32_bf16 v[76:79], a[224:227], a[96:99], v[76:79]\n" \
"v_mfma_f32_16x16x32_bf16 v[76:79], a[196:199], a[100:103], v[76:79]\n" \
"buffer_load_dwordx4 a[144:147], v208, %[srdz], s50 offen offset:0 sc1\n" \
"v_mfma_f32_16x16x32_bf16 v[108:111], a[224:227], a[112:115], v[108:111]\n" \
"ds_read_b64_tr_b16 a[216:217], v221 offset:33280\n" \
"v_mfma_f32_16x16x32_bf16 v[108:111], a[196:199], a[116:119], v[108:111]\n" \
"ds_read_b64_tr_b16 a[218:219], v221 offset:35328\n" \
"v_mfma_f32_16x16x32_bf16 v[12:15], a[200:203], a[72:75], v[12:15]\n" \
"ds_read_b64_tr_b16 a[220:221], v221 offset:41472\n" \
"s_cbranch_vccz Lg17_%=\n" \
"s_nop 0\n" \
"buffer_store_dwordx4 v[96:99], v210, %[srdz], s60 offen offset:0 sc1\n" \
"Lg17_%=:\n" \
"v_mfma_f32_16x16x32_bf16 v[12:15], a[204:207], a[76:79], v[12:15]\n" \
"ds_read_b64_tr_b16 a[222:223], v221 offset:43520\n" \
"v_mfma_f32_16x16x32_bf16 v[44:47], a[200:203], a[88:91], v[44:47]\n" \
"ds_read_b64_tr_b16 a[228:229], v221 offset:37376\n" \
"v_mfma_f32_16x16x32_bf16 v[44:47], a[204:207], a[92:95], v[44:47]\n" \
"ds_read_b64_tr_b16 a[230:231], v221 offset:39424\n" \
"v_mfma_f32_16x16x32_bf16 v[76:79], a[200:203], a[104:107], v[76:79]\n" \
"ds_read_b64_tr_b16 a[224:225], v221 offset:45568\n" \
"v_mfma_f32_16x16x32_bf16 v[76:79], a[204:207], a[108:111], v[76:79]\n" \
"ds_read_b64_tr_b16 a[226:227], v221 offset:47616\n" \
"v_mfma_f32_16x16x32_bf16 v[108:111], a[200:203], a[120:123], v[108:111]\n" \
"buffer_load_dwordx4 a[148:151], v208, %[srdz], s50 offen offset:1024 sc1\n" \
"v_mfma_f32_16x16x32_bf16 v[108:111], a[204:207], a[124:127], v[108:111]\n" \
"s_waitcnt vmcnt(63) lgkmcnt(12)\n" \
"s_nop 0\n" \
"v_mfma_f32_16x16x32_bf16 v[20:23], a[232:235], a[64:67], v[20:23]\n" \
"s_cbranch_vccz Lg18_%=\n" \
"s_nop 0\n" \
"buffer_store_dwordx4 v[104:107], v210, %[srdz], s60 offen offset:1024 sc1\n" \
"Lg18_%=:\n" \
"v_mfma_f32_16x16x32_bf16 v[20:23], a[236:239], a[68:71], v[20:23]\n" \
"v_mfma_f32_16x16x32_bf16 v[52:55], a[232:235], a[80:83], v[52:55]\n" \
"buffer_load_dwordx4 v213, %[srdt], s49 offen offset:2048 lds\n" \
"v_mfma_f32_16x16x32_bf16 v[52:55], a[236:239], a[84:87], v[52:55]\n" \
"v_mfma_f32_16x16x32_bf16 v[84:87], a[232:235], a[96:99], v[84:87]\n" \
"s_waitcnt vmcnt(59) lgkmcnt(0)\n" \
"s_bitcmp1_b32 0x30, s48\n" \
"s_cbranch_scc1 Lcw8_%=\n" \
"s_waitcnt vmcnt(45)\n" \
"Lcw8_%=:\n" \
"s_barrier\n" \
"s_nop 0\n" \
"v_mfma_f32_16x16x32_bf16 v[84:87], a[236:239], a[100:103], v[84:87]\n" \
"ds_read_b64_tr_b16 a[196:197], v220 offset:49152\n" \
"v_mfma_f32_16x16x32_bf16 v[116:119], a[232:235], a[112:115], v[116:119]\n" \
"ds_read_b64_tr_b16 a[198:199], v220 offset:51200\n" \
"buffer_load_dwordx4 a[152:155], v208, %[srdz], s50 offen offset:2048 sc1\n" \
"v_mfma_f32_16x16x32_bf16 v[116:119], a[236:239], a[116:119], v[116:119]\n" \
"ds_read_b64_tr_b16 a[200:201], v220 offset:53248\n" \
"v_mfma_f32_16x16x32_bf16 v[20:23], a[208:211], a[72:75], v[20:23]\n" \
"ds_read_b64_tr_b16 a[202:203], v220 offset:55296\n" \
"v_mfma_f32_16x16x32_bf16 v[20:23], a[212:215], a[76:79], v[20:23]\n" \
"ds_read_b64_tr_b16 a[204:205], v220 offset:57344\n" \
"s_cbranch_vccz Lg19_%=\n" \
"s_nop 0\n" \
"buffer_store_dwordx4 v[112:115], v210, %[srdz], s60 offen offset:2048 sc1\n" \
"Lg19_%=:\n" \
"v_mfma_f32_16x16x32_bf16 v[52:55], a[208:211], a[88:91], v[52:55]\n" \
"ds_read_b64_tr_b16 a[206:207], v220 offset:59392\n" \
"v_mfma_f32_16x16x32_bf16 v[52:55], a[212:215], a[92:95], v[52:55]\n" \
"ds_read_b64_tr_b16 a[232:233], v220 offset:61440\n" \
"v_mfma_f32_16x16x32_bf16 v[84:87], a[208:211], a[104:107], v[84:87]\n" \
"ds_read_b64_tr_b16 a[234:235], v220 offset:63488\n" \
"v_mfma_f32_16x16x32_bf16 v[84:87], a[212:215], a[108:111], v[84:87]\n" \
"buffer_load_dwordx4 a[156:159], v208, %[srdz], s50 offen offset:3072 sc1\n" \
"v_mfma_f32_16x16x32_bf16 v[116:119], a[208:211], a[120:123], v[116:119]\n" \
"v_mfma_f32_16x16x32_bf16 v[116:119], a[212:215], a[124:127], v[116:119]\n" \
"s_waitcnt vmcnt(21) lgkmcnt(4)\n" \
"s_bitcmp1_b32 0x30, s48\n" \
"s_cbranch_scc1 Lcw9_%=\n" \
"s_waitcnt vmcnt(13)\n" \
"Lcw9_%=:\n" \
"v_mfma_f32_16x16x32_bf16 v[28:31], a[216:219], a[64:67], v[28:31]\n" \
"v_mfma_f32_16x16x32_bf16 v[28:31], a[228:231], a[68:71], v[28:31]\n" \
"s_cbranch_vccz Lg20_%=\n" \
"s_nop 0\n" \
"buffer_store_dwordx4 v[120:123], v210, %[srdz], s60 offen offset:3072 sc1\n" \
"Lg20_%=:\n" \
"v_mfma_f32_16x16x32_bf16 v[60:63], a[216:219], a[80:83], v[60:63]\n" \
"v_mfma_f32_16x16x32_bf16 v[60:63], a[228:231], a[84:87], v[60:63]\n" \
"buffer_load_dwordx4 v214, %[srdt], s49 offen offset:3072 lds\n" \
"v_mfma_f32_16x16x32_bf16 v[92:95], a[216:219], a[96:99], v[92:95]\n" \
"v_mfma_f32_16x16x32_bf16 v[92:95], a[228:231], a[100:103], v[92:95]\n" \
"buffer_load_dwordx4 a[160:163], v209, %[srdz], s50 offen offset:0 sc1\n" \
"v_mfma_f32_16x16x32_bf16 v[124:127], a[216:219], a[112:115], v[124:127]\n" \
"ds_read_b64_tr_b16 a[236:237], v221 offset:49152\n" \
"v_mfma_f32_16x16x32_bf16 v[124:127], a[228:231], a[116:119], v[124:127]\n" \
"ds_read_b64_tr_b16 a[238:239], v221 offset:51200\n" \
"v_mfma_f32_16x16x32_bf16 v[192:195], a[196:199], a[0:3], v[128:131]\n" \
"ds_read_b64_tr_b16 a[208:209], v221 offset:53248\n" \
"v_mfma_f32_16x16x32_bf16 v[192:195], a[200:203], a[4:7], v[192:195]\n" \
"ds_read_b64_tr_b16 a[210:211], v221 offset:55296\n" \
"v_mfma_f32_16x16x32_bf16 v[196:199], a[196:199], a[16:19], v[132:135]\n" \
"buffer_load_dwordx4 a[164:167], v209, %[srdz], s50 offen offset:1024 sc1\n" \
"v_mfma_f32_16x16x32_bf16 v[196:199], a[200:203], a[20:23], v[196:199]\n" \
"v_mfma_f32_16x16x32_bf16 v[200:203], a[196:199], a[32:35], v[136:139]\n" \
"v_mfma_f32_16x16x32_bf16 v[200:203], a[200:203], a[36:39], v[200:203]\n" \
"v_mfma_f32_16x16x32_bf16 v[204:207], a[196:199], a[48:51], v[140:143]\n" \
"ds_read_b64_tr_b16 a[212:213], v221 offset:57344\n" \
"s_sub_u32 s61, s48, 6\n" \
"s_cmp_lt_u32 s61, 2\n" \
"s_cselect_b64 vcc, -1, 0\n" \
"s_nop 0\n" \
"v_mfma_f32_16x16x32_bf16 v[204:207], a[200:203], a[52:55], v[204:207]\n" \
"s_waitcnt vmcnt(22) lgkmcnt(5)\n" \
"s_bitcmp1_b32 0x30, s48\n" \
"s_cbranch_scc1 Lcw10_%=\n" \
"s_waitcnt vmcnt(14)\n" \
"Lcw10_%=:\n" \
"v_mfma_f32_16x16x32_bf16 v[192:195], a[204:207], a[8:11], v[192:195]\n" \
"ds_read_b64_tr_b16 a[214:215], v221 offset:59392\n" \
"s_cbranch_vccz Lg21_%=\n" \
"s_nop 0\n" \
"buffer_store_dwordx4 v[0:3], %[oq0], %[srdz], s60 offen offset:0 sc1\n" \
"Lg21_%=:\n" \
"v_mfma_f32_16x16x32_bf16 v[192:195], a[232:235], a[12:15], v[192:195]\n" \
".if . - adaln_body != 45064\n" \
".error \"size model\"\n" \
".endif\n" \
"Lk10_%=:\n" \
"s_cbranch_execz Lk11_%=\n" \
"s_nop 0\n" \
"ds_read_b64_tr_b16 a[216:217], v221 offset:61440\n" \
"v_mfma_f32_16x16x32_bf16 v[196:199], a[204:207], a[24:27], v[196:199]\n" \
"ds_read_b64_tr_b16 a[218:219], v221 offset:63488\n" \
"v_mfma_f32_16x16x32_bf16 v[196:199], a[232:235], a[28:31], v[196:199]\n" \
"buffer_load_dwordx4 a[168:171], v209, %[srdz], s50 offen offset:2048 sc1\n" \
"v_mfma_f32_16x16x32_bf16 v[200:203], a[204:207], a[40:43], v[200:203]\n" \
"v_mfma_f32_16x16x32_bf16 v[200:203], a[232:235], a[44:47], v[200:203]\n" \
"v_mfma_f32_16x16x32_bf16 v[204:207], a[204:207], a[56:59], v[204:207]\n" \
"ds_read_b64_tr_b16 a[228:229], v220 offset:49664\n" \
"v_mfma_f32_16x16x32_bf16 v[204:207], a[232:235], a[60:63], v[204:207]\n" \
"ds_read_b64_tr_b16 a[230:231], v220 offset:51712\n" \
"v_mfma_f32_16x16x32_bf16 v[28:31], a[220:223], a[72:75], v[28:31]\n" \
"s_cbranch_vccz Lg22_%=\n" \
"s_nop 0\n" \
"buffer_store_dwordx4 v[8:11], %[oq0], %[srdz], s60 offen offset:1024 sc1\n" \
"Lg22_%=:\n" \
"v_mfma_f32_16x16x32_bf16 v[28:31], a[224:227], a[76:79], v[28:31]\n" \
"v_mfma_f32_16x16x32_bf16 v[60:63], a[220:223], a[88:91], v[60:63]\n" \
"buffer_load_dwordx4 a[172:175], v209, %[srdz], s50 offen offset:3072 sc1\n" \
"v_mfma_f32_16x16x32_bf16 v[60:63], a[224:227], a[92:95], v[60:63]\n" \
"v_mfma_f32_16x16x32_bf16 v[92:95], a[220:223], a[104:107], v[92:95]\n" \
"v_mfma_f32_16x16x32_bf16 v[92:95], a[224:227], a[108:111], v[92:95]\n" \
"v_cmp_u_f32_e64 s[44:45], v192, v196\n" \
"v_mfma_f32_16x16x32_bf16 v[124:127], a[220:223], a[120:123], v[124:127]\n" \
"s_add_u32 m0, %[m0b], 16384\n" \
"s_cmp_gt_u32 s48, 2\n" \
"s_cselect_b32 s49, 2, 3\n" \
"s_lshl_b32 s49, s49, 18\n" \
"s_or_b32 s49, s49, s59\n" \
"buffer_load_dwordx4 %[wo0], %[srdt], s49 offen offset:0 lds\n" \
"v_mfma_f32_16x16x32_bf16 v[124:127], a[224:227], a[124:127], v[124:127]\n" \
"v_cmp_u_f32_e64 s[56:57], v200, v204\n" \
"s_or_b64 s[56:57], s[56:57], s[44:45]\n" \
"s_cbranch_scc1 Lsp2_%=\n" \
"Lbk2_%=:\n" \
"s_waitcnt vmcnt(63) lgkmcnt(2)\n" \
"s_nop 0\n" \
"v_mfma_f32_16x16x32_bf16 v[144:147], a[236:239], a[0:3], v[144:147]\n" \
"ds_read_b64_tr_b16 a[220:221], v220 offset:53760\n" \
"v_mfma_f32_16x16x32_bf16 v[144:147], a[208:211], a[4:7], v[144:147]\n" \
"ds_read_b64_tr_b16 a[222:223], v220 offset:55808\n" \
"s_cbranch_vccz Lg23_%=\n" \
"s_nop 0\n" \
"buffer_store_dwordx4 v[16:19], %[oq0], %[srdz], s60 offen offset:2048 sc1\n" \
"Lg23_%=:\n" \
"v_mfma_f32_16x16x32_bf16 v[148:151], a[236:239], a[16:19], v[148:151]\n" \
"ds_read_b64_tr_b16 a[196:197], v220 offset:57856\n" \
"v_mfma_f32_16x16x32_bf16 v[148:151], a[208:211], a[20:23], v[148:151]\n" \
"ds_read_b64_tr_b16 a[198:199], v220 offset:59904\n" \
"buffer_load_dwordx4 a[176:179], v210, %[srdz], s50 offen offset:0 sc1\n" \
"v_mfma_f32_16x16x32_bf16 v[152:155], a[236:239], a[32:35], v[152:155]\n" \
"ds_read_b64_tr_b16 a[200:201], v220 offset:61952\n" \
"v_mfma_f32_16x16x32_bf16 v[152:155], a[208:211], a[36:39], v[152:155]\n" \
"ds_read_b64_tr_b16 a[202:203], v220 offset:64000\n" \
"v_mfma_f32_16x16x32_bf16 v[156:159], a[236:239], a[48:51], v[156:159]\n" \
"ds_read_b64_tr_b16 a[204:205], v221 offset:49664\n" \
"v_mfma_f32_16x16x32_bf16 v[156:159], a[208:211], a[52:55], v[156:159]\n" \
"ds_read_b64_tr_b16 a[206:207], v221 offset:51712\n" \
"v_mfma_f32_16x16x32_bf16 v[144:147], a[212:215], a[8:11], v[144:147]\n" \
"ds_read_b64_tr_b16 a[224:225], v221 offset:53760\n" \
"v_mfma_f32_16x16x32_bf16 v[144:147], a[216:219], a[12:15], v[144:147]\n" \
"ds_read_b64_tr_b16 a[226:227], v221 offset:55808\n" \
"v_mfma_f32_16x16x32_bf16 v[148:151], a[212:215], a[24:27], v[148:151]\n" \
"s_cbranch_vccz Lg24_%=\n" \
"s_nop 0\n" \
"buffer_store_dwordx4 v[24:27], %[oq0], %[srdz], s60 offen offset:3072 sc1\n" \
"Lg24_%=:\n" \
"v_mfma_f32_16x16x32_bf16 v[148:151], a[216:219], a[28:31], v[148:151]\n" \
"v_mfma_f32_16x16x32_bf16 v[152:155], a[212:215], a[40:43], v[152:155]\n" \
"buffer_load_dwordx4 a[180:183], v210, %[srdz], s50 offen offset:1024 sc1\n" \
"v_mfma_f32_16x16x32_bf16 v[152:155], a[216:219], a[44:47], v[152:155]\n" \
"v_mfma_f32_16x16x32_bf16 v[156:159], a[212:215], a[56:59], v[156:159]\n" \
"ds_read_b64_tr_b16 a[232:233], v221 offset:57856\n" \
"v_mfma_f32_16x16x32_bf16 v[156:159], a[216:219], a[60:63], v[156:159]\n" \
"s_waitcnt vmcnt(63) lgkmcnt(5)\n" \
"s_nop 0\n" \
"v_mfma_f32_16x16x32_bf16 v[160:163], a[228:231], a[0:3], v[160:163]\n" \
"ds_read_b64_tr_b16 a[234:235], v221 offset:59904\n" \
"buffer_load_dwordx4 v212, %[srdt], s49 offen offset:1024 lds\n" \
"v_mfma_f32_16x16x32_bf16 v[160:163], a[220:223], a[4:7], v[160:163]\n" \
"ds_read_b64_tr_b16 a[236:237], v221 offset:61952\n" \
"v_mfma_f32_16x16x32_bf16 v[164:167], a[228:231], a[16:19], v[164:167]\n" \
"ds_read_b64_tr_b16 a[238:239], v221 offset:64000\n" \
"v_mfma_f32_16x16x32_bf16 v[164:167], a[220:223], a[20:23], v[164:167]\n" \
"s_cbranch_vccz Lg25_%=\n" \
"s_nop 0\n" \
"buffer_store_dwordx4 v[32:35], v208, %[srdz], s60 offen offset:0 sc1\n" \
"Lg25_%=:\n" \
"v_mfma_f32_16x16x32_bf16 v[168:171], a[228:231], a[32:35], v[168:171]\n" \
"v_mfma_f32_16x16x32_bf16 v[168:171], a[220:223], a[36:39], v[168:171]\n" \
"buffer_load_dwordx4 a[184:187], v210, %[srdz], s50 offen offset:2048 sc1\n" \
"v_mfma_f32_16x16x32_bf16 v[172:175], a[228:231], a[48:51], v[172:175]\n" \
"v_mfma_f32_16x16x32_bf16 v[172:175], a[220:223], a[52:55], v[172:175]\n" \
"buffer_load_dwordx4 a[188:191], v210, %[srdz], s50 offen offset:3072 sc1\n" \
"v_mfma_f32_16x16x32_bf16 v[160:163], a[196:199], a[8:11], v[160:163]\n" \
"v_mfma_f32_16x16x32_bf16 v[160:163], a[200:203], a[12:15], v[160:163]\n" \
"v_mfma_f32_16x16x32_bf16 v[164:167], a[196:199], a[24:27], v[164:167]\n" \
"v_mfma_f32_16x16x32_bf16 v[164:167], a[200:203], a[28:31], v[164:167]\n" \
"v_mfma_f32_16x16x32_bf16 v[168:171], a[196:199], a[40:43], v[168:171]\n" \
"s_waitcnt vmcnt(63) lgkmcnt(0)\n" \
"s_bitcmp1_b32 0x30, s48\n" \
"s_cbranch_scc1 Lcw11_%=\n" \
"s_waitcnt vmcnt(59)\n" \
"s_bitcmp1_b32 0xc0, s48\n" \
"s_cbranch_scc1 Lcw11_%=\n" \
"s_waitcnt vmcnt(54)\n" \
"Lcw11_%=:\n" \
"s_barrier\n" \
"s_nop 0\n" \
"v_mfma_f32_16x16x32_bf16 v[168:171], a[200:203], a[44:47], v[168:171]\n" \
"ds_read_b64_tr_b16 a[208:209], v215 offset:49152\n" \
"s_cbranch_vccz Lg26_%=\n" \
"s_nop 0\n" \
"buffer_store_dwordx4 v[40:43], v208, %[srdz], s60 offen offset:1024 sc1\n" \
"Lg26_%=:\n" \
"v_mfma_f32_16x16x32_bf16 v[172:175], a[196:199], a[56:59], v[172:175]\n" \
"ds_read_b64_tr_b16 a[210:211], v215 offset:51200\n" \
"v_mfma_f32_16x16x32_bf16 v[172:175], a[200:203], a[60:63], v[172:175]\n" \
"s_waitcnt vmcnt(63) lgkmcnt(2)\n" \
"s_nop 0\n" \
"v_mfma_f32_16x16x32_bf16 v[176:179], a[204:207], a[0:3], v[176:179]\n" \
"ds_read_b64_tr_b16 a[212:213], v215 offset:53248\n" \
"global_load_dword v237, %[vct], %[pct]\n" \
"v_mfma_f32_16x16x32_bf16 v[176:179], a[224:227], a[4:7], v[176:179]\n" \
"ds_read_b64_tr_b16 a[214:215], v215 offset:55296\n" \
"v_mfma_f32_16x16x32_bf16 v[180:183], a[204:207], a[16:19], v[180:183]\n" \
"ds_read_b64_tr_b16 a[216:217], v215 offset:57344\n" \
"buffer_load_dwordx4 v213, %[srdt], s49 offen offset:2048 lds\n" \
"v_mfma_f32_16x16x32_bf16 v[180:183], a[224:227], a[20:23], v[180:183]\n" \
"ds_read_b64_tr_b16 a[218:219], v215 offset:59392\n" \
"v_mfma_f32_16x16x32_bf16 v[184:187], a[204:207], a[32:35], v[184:187]\n" \
"ds_read_b64_tr_b16 a[228:229], v215 offset:61440\n" \
"v_mfma_f32_16x16x32_bf16 v[184:187], a[224:227], a[36:39], v[184:187]\n" \
"ds_read_b64_tr_b16 a[230:231], v215 offset:63488\n" \
"s_cbranch_vccz Lg27_%=\n" \
"s_nop 0\n" \
"buffer_store_dwordx4 v[48:51], v208, %[srdz], s60 offen offset:2048 sc1\n" \
"Lg27_%=:\n" \
"v_mfma_f32_16x16x32_bf16 v[188:191], a[204:207], a[48:51], v[188:191]\n" \
"ds_read_b64_tr_b16 a[220:221], v227 offset:49152\n" \
"v_mfma_f32_16x16x32_bf16 v[188:191], a[224:227], a[52:55], v[188:191]\n" \
"ds_read_b64_tr_b16 a[222:223], v227 offset:51200\n" \
"v_mfma_f32_16x16x32_bf16 v[176:179], a[232:235], a[8:11], v[176:179]\n" \
"ds_read_b64_tr_b16 a[196:197], v227 offset:53248\n" \
"v_mfma_f32_16x16x32_bf16 v[176:179], a[236:239], a[12:15], v[176:179]\n" \
"ds_read_b64_tr_b16 a[198:199], v227 offset:55296\n" \
"s_cmp_gt_u32 s48, 3\n" \
"s_cselect_b32 s50, 3, 4\n" \
"s_mul_i32 s50, s50, 152064\n" \
"buffer_load_dwordx4 a[64:67], %[oq0], %[srdz], s50 offen offset:0 sc1\n" \
"v_mfma_f32_16x16x32_bf16 v[180:183], a[232:235], a[24:27], v[180:183]\n" \
"v_mfma_f32_16x16x32_bf16 v[180:183], a[236:239], a[28:31], v[180:183]\n" \
"buffer_load_dwordx4 v214, %[srdt], s49 offen offset:3072 lds\n" \
"v_mfma_f32_16x16x32_bf16 v[184:187], a[232:235], a[40:43], v[184:187]\n" \
"v_mfma_f32_16x16x32_bf16 v[184:187], a[236:239], a[44:47], v[184:187]\n" \
"v_mfma_f32_16x16x32_bf16 v[188:191], a[232:235], a[56:59], v[188:191]\n" \
"ds_read_b64_tr_b16 a[200:201], v227 offset:57344\n" \
"s_cbranch_vccz Lg28_%=\n" \
"s_nop 0\n" \
"buffer_store_dwordx4 v[56:59], v208, %[srdz], s60 offen offset:3072 sc1\n" \
"Lg28_%=:\n" \
"v_mfma_f32_16x16x32_bf16 v[188:191], a[236:239], a[60:63], v[188:191]\n" \
"s_waitcnt vmcnt(63) lgkmcnt(5)\n" \
"s_nop 0\n" \
"v_mfma_f32_16x16x32_bf16 v[4:7], a[208:211], a[0:3], v[4:7]\n" \
"ds_read_b64_tr_b16 a[202:203], v227 offset:59392\n" \
"buffer_load_dwordx4 a[68:71], %[oq0], %[srdz], s50 offen offset:1024 sc1\n" \
"v_mfma_f32_16x16x32_bf16 v[4:7], a[212:215], a[4:7], v[4:7]\n" \
"ds_read_b64_tr_b16 a[204:205], v227 offset:61440\n" \
"v_mfma_f32_16x16x32_bf16 v[36:39], a[208:211], a[16:19], v[36:39]\n" \
"ds_read_b64_tr_b16 a[206:207], v227 offset:63488\n" \
"v_mfma_f32_16x16x32_bf16 v[36:39], a[212:215], a[20:23], v[36:39]\n" \
"v_mfma_f32_16x16x32_bf16 v[68:71], a[208:211], a[32:35], v[68:71]\n" \
"buffer_load_dwordx4 a[72:75], %[oq0], %[srdz], s50 offen offset:2048 sc1\n" \
"v_mfma_f32_16x16x32_bf16 v[68:71], a[212:215], a[36:39], v[68:71]\n" \
"v_mfma_f32_16x16x32_bf16 v[100:103], a[208:211], a[48:51], v[100:103]\n" \
"ds_read_b64_tr_b16 a[224:225], v215 offset:49664\n" \
"v_mfma_f32_16x16x32_bf16 v[100:103], a[212:215], a[52:55], v[100:103]\n" \
"ds_read_b64_tr_b16 a[226:227], v215 offset:51712\n" \
"s_cbranch_vccz Lg29_%=\n" \
"s_nop 0\n" \
"buffer_store_dwordx4 v[64:67], v209, %[srdz], s60 offen offset:0 sc1\n" \
"Lg29_%=:\n" \
"v_mfma_f32_16x16x32_bf16 v[4:7], a[216:219], a[8:11], v[4:7]\n" \
"ds_read_b64_tr_b16 a[232:233], v215 offset:53760\n" \
"v_mfma_f32_16x16x32_bf16 v[4:7], a[228:231], a[12:15], v[4:7]\n" \
"ds_read_b64_tr_b16 a[234:235], v215 offset:55808\n" \
"v_mfma_f32_16x16x32_bf16 v[36:39], a[216:219], a[24:27], v[36:39]\n" \
"v_mfma_f32_16x16x32_bf16 v[36:39], a[228:231], a[28:31], v[36:39]\n" \
"buffer_load_dwordx4 a[76:79], %[oq0], %[srdz], s50 offen offset:3072 sc1\n" \
"v_mfma_f32_16x16x32_bf16 v[68:71], a[216:219], a[40:43], v[68:71]\n" \
"v_mfma_f32_16x16x32_bf16 v[68:71], a[228:231], a[44:47], v[68:71]\n" \
"v_mfma_f32_16x16x32_bf16 v[100:103], a[216:219], a[56:59], v[100:103]\n" \
"ds_read_b64_tr_b16 a[236:237], v215 offset:57856\n" \
"v_mfma_f32_16x16x32_bf16 v[100:103], a[228:231], a[60:63], v[100:103]\n" \
"s_waitcnt vmcnt(63) lgkmcnt(5)\n" \
"s_nop 0\n" \
"v_mfma_f32_16x16x32_bf16 v[12:15], a[220:223], a[0:3], v[12:15]\n" \
"ds_read_b64_tr_b16 a[238:239], v215 offset:59904\n" \
"s_cbranch_vccz Lg30_%=\n" \
"s_nop 0\n" \
"buffer_store_dwordx4 v[72:75], v209, %[srdz], s60 offen offset:1024 sc1\n" \
"Lg30_%=:\n" \
"v_mfma_f32_16x16x32_bf16 v[12:15], a[196:199], a[4:7], v[12:15]\n" \
"ds_read_b64_tr_b16 a[208:209], v215 offset:61952\n" \
"v_mfma_f32_16x16x32_bf16 v[44:47], a[220:223], a[16:19], v[44:47]\n" \
"ds_read_b64_tr_b16 a[210:211], v215 offset:64000\n" \
"v_mfma_f32_16x16x32_bf16 v[44:47], a[196:199], a[20:23], v[44:47]\n" \
"buffer_load_dwordx4 a[80:83], v208, %[srdz], s50 offen offset:0 sc1\n" \
"v_mfma_f32_16x16x32_bf16 v[76:79], a[220:223], a[32:35], v[76:79]\n" \
"v_mfma_f32_16x16x32_bf16 v[76:79], a[196:199], a[36:39], v[76:79]\n" \
"s_add_u32 m0, %[m0b], 32768\n" \
"s_cmp_gt_u32 s48, 3\n" \
"s_cselect_b32 s49, 3, 4\n" \
"s_lshl_b32 s49, s49, 18\n" \
"s_or_b32 s49, s49, s58\n" \
"buffer_load_dwordx4 %[wo0], %[srdt], s49 offen offset:0 lds\n" \
"v_mfma_f32_16x16x32_bf16 v[108:111], a[220:223], a[48:51], v[108:111]\n" \
"ds_read_b64_tr_b16 a[212:213], v227 offset:49664\n" \
"v_mfma_f32_16x16x32_bf16 v[108:111], a[196:199], a[52:55], v[108:111]\n" \
"ds_read_b64_tr_b16 a[214:215], v227 offset:51712\n" \
"v_mfma_f32_16x16x32_bf16 v[12:15], a[200:203], a[8:11], v[12:15]\n" \
"ds_read_b64_tr_b16 a[216:217], v227 offset:57856\n" \
"v_mfma_f32_16x16x32_bf16 v[12:15], a[204:207], a[12:15], v[12:15]\n" \
"ds_read_b64_tr_b16 a[218:219], v227 offset:59904\n" \
"s_cbranch_vccz Lg31_%=\n" \
"s_nop 0\n" \
"buffer_store_dwordx4 v[80:83], v209, %[srdz], s60 offen offset:2048 sc1\n" \
"Lg31_%=:\n" \
"v_mfma_f32_16x16x32_bf16 v[44:47], a[200:203], a[24:27], v[44:47]\n" \
"ds_read_b64_tr_b16 a[228:229], v227 offset:53760\n" \
"v_mfma_f32_16x16x32_bf16 v[44:47], a[204:207], a[28:31], v[44:47]\n" \
"ds_read_b64_tr_b16 a[230:231], v227 offset:55808\n" \
"v_mfma_f32_16x16x32_bf16 v[76:79], a[200:203], a[40:43], v[76:79]\n" \
"ds_read_b64_tr_b16 a[220:221], v227 offset:61952\n" \
"buffer_load_dwordx4 a[84:87], v208, %[srdz], s50 offen offset:1024 sc1\n" \
"v_mfma_f32_16x16x32_bf16 v[76:79], a[204:207], a[44:47], v[76:79]\n" \
"ds_read_b64_tr_b16 a[222:223], v227 offset:64000\n" \
"v_mfma_f32_16x16x32_bf16 v[108:111], a[200:203], a[56:59], v[108:111]\n" \
"v_mfma_f32_16x16x32_bf16 v[108:111], a[204:207], a[60:63], v[108:111]\n" \
"s_waitcnt vmcnt(63) lgkmcnt(12)\n" \
"s_nop 0\n" \
"v_mfma_f32_16x16x32_bf16 v[20:23], a[224:227], a[0:3], v[20:23]\n" \
"v_mfma_f32_16x16x32_bf16 v[20:23], a[232:235], a[4:7], v[20:23]\n" \
"v_mfma_f32_16x16x32_bf16 v[52:55], a[224:227], a[16:19], v[52:55]\n" \
"s_cbranch_vccz Lg32_%=\n" \
"s_nop 0\n" \
"buffer_store_dwordx4 v[88:91], v209, %[srdz], s60 offen offset:3072 sc1\n" \
"Lg32_%=:\n" \
"v_mfma_f32_16x16x32_bf16 v[52:55], a[232:235], a[20:23], v[52:55]\n" \
"v_mfma_f32_16x16x32_bf16 v[84:87], a[224:227], a[32:35], v[84:87]\n" \
"s_waitcnt vmcnt(32) lgkmcnt(0)\n" \
"s_nop 0\n" \
"s_bitcmp1_b32 0xc0, s48\n" \
"s_cbranch_scc1 Lcw12_%=\n" \
"s_waitcnt vmcnt(20)\n" \
"Lcw12_%=:\n" \
"s_barrier\n" \
"s_nop 0\n" \
"v_mfma_f32_16x16x32_bf16 v[84:87], a[232:235], a[36:39], v[84:87]\n" \
"ds_read_b64_tr_b16 a[196:197], v215 offset:0\n" \
"buffer_load_dwordx4 a[88:91], v208, %[srdz], s50 offen offset:2048 sc1\n" \
"v_mfma_f32_16x16x32_bf16 v[116:119], a[224:227], a[48:51], v[116:119]\n" \
"ds_read_b64_tr_b16 a[198:199], v215 offset:2048\n" \
"v_mfma_f32_16x16x32_bf16 v[116:119], a[232:235], a[52:55], v[116:119]\n" \
"ds_read_b64_tr_b16 a[200:201], v215 offset:4096\n" \
"buffer_load_dwordx4 v212, %[srdt], s49 offen offset:1024 lds\n" \
"v_mfma_f32_16x16x32_bf16 v[20:23], a[236:239], a[8:11], v[20:23]\n" \
"ds_read_b64_tr_b16 a[202:203], v215 offset:6144\n" \
"v_mfma_f32_16x16x32_bf16 v[20:23], a[208:211], a[12:15], v[20:23]\n" \
"ds_read_b64_tr_b16 a[204:205], v215 offset:8192\n" \
"v_mfma_f32_16x16x32_bf16 v[52:55], a[236:239], a[24:27], v[52:55]\n" \
"ds_read_b64_tr_b16 a[206:207], v215 offset:10240\n" \
"v_mfma_f32_16x16x32_bf16 v[52:55], a[208:211], a[28:31], v[52:55]\n" \
"ds_read_b64_tr_b16 a[224:225], v215 offset:12288\n" \
"s_cbranch_vccz Lg33_%=\n" \
"s_nop 0\n" \
"buffer_store_dwordx4 v[96:99], v210, %[srdz], s60 offen offset:0 sc1\n" \
"Lg33_%=:\n" \
"v_mfma_f32_16x16x32_bf16 v[84:87], a[236:239], a[40:43], v[84:87]\n" \
"ds_read_b64_tr_b16 a[226:227], v215 offset:14336\n" \
"v_mfma_f32_16x16x32_bf16 v[84:87], a[208:211], a[44:47], v[84:87]\n" \
"buffer_load_dwordx4 a[92:95], v208, %[srdz], s50 offen offset:3072 sc1\n" \
"v_mfma_f32_16x16x32_bf16 v[116:119], a[236:239], a[56:59], v[116:119]\n" \
"v_mfma_f32_16x16x32_bf16 v[116:119], a[208:211], a[60:63], v[116:119]\n" \
"s_waitcnt vmcnt(25) lgkmcnt(4)\n" \
"s_nop 0\n" \
"s_bitcmp1_b32 0xc0, s48\n" \
"s_cbranch_scc1 Lcw13_%=\n" \
"s_waitcnt vmcnt(16)\n" \
"Lcw13_%=:\n" \
"v_mfma_f32_16x16x32_bf16 v[28:31], a[212:215], a[0:3], v[28:31]\n" \
"buffer_load_dwordx4 a[96:99], v209, %[srdz], s50 offen offset:0 sc1\n" \
"v_mfma_f32_16x16x32_bf16 v[28:31], a[228:231], a[4:7], v[28:31]\n" \
"v_mfma_f32_16x16x32_bf16 v[60:63], a[212:215], a[16:19], v[60:63]\n" \
"buffer_load_dwordx4 v213, %[srdt], s49 offen offset:2048 lds\n" \
"v_mfma_f32_16x16x32_bf16 v[60:63], a[228:231], a[20:23], v[60:63]\n" \
"v_mfma_f32_16x16x32_bf16 v[92:95], a[212:215], a[32:35], v[92:95]\n" \
"s_cbranch_vccz Lg34_%=\n" \
"s_nop 0\n" \
"buffer_store_dwordx4 v[104:107], v210, %[srdz], s60 offen offset:1024 sc1\n" \
"Lg34_%=:\n" \
"v_mfma_f32_16x16x32_bf16 v[92:95], a[228:231], a[36:39], v[92:95]\n" \
"v_mfma_f32_16x16x32_bf16 v[124:127], a[212:215], a[48:51], v[124:127]\n" \
"ds_read_b64_tr_b16 a[232:233], v227 offset:0\n" \
"v_mfma_f32_16x16x32_bf16 v[124:127], a[228:231], a[52:55], v[124:127]\n" \
"ds_read_b64_tr_b16 a[234:235], v227 offset:2048\n" \
"v_mfma_f32_16x16x32_bf16 v[128:131], a[196:199], a[128:131], v[192:195]\n" \
"ds_read_b64_tr_b16 a[236:237], v227 offset:4096\n" \
"v_mfma_f32_16x16x32_bf16 v[128:131], a[200:203], a[132:135], v[128:131]\n" \
"ds_read_b64_tr_b16 a[238:239], v227 offset:6144\n" \
"buffer_load_dwordx4 a[100:103], v209, %[srdz], s50 offen offset:1024 sc1\n" \
"v_mfma_f32_16x16x32_bf16 v[132:135], a[196:199], a[144:147], v[196:199]\n" \
"v_mfma_f32_16x16x32_bf16 v[132:135], a[200:203], a[148:151], v[132:135]\n" \
"v_mfma_f32_16x16x32_bf16 v[136:139], a[196:199], a[160:163], v[200:203]\n" \
"v_mfma_f32_16x16x32_bf16 v[136:139], a[200:203], a[164:167], v[136:139]\n" \
"s_cbranch_vccz Lg35_%=\n" \
"s_nop 0\n" \
"buffer_store_dwordx4 v[112:115], v210, %[srdz], s60 offen offset:2048 sc1\n" \
"Lg35_%=:\n" \
"v_mfma_f32_16x16x32_bf16 v[140:143], a[196:199], a[176:179], v[204:207]\n" \
"ds_read_b64_tr_b16 a[208:209], v227 offset:8192\n" \
"v_mfma_f32_16x16x32_bf16 v[140:143], a[200:203], a[180:183], v[140:143]\n" \
"s_waitcnt vmcnt(26) lgkmcnt(5)\n" \
"s_nop 0\n" \
"s_bitcmp1_b32 0xc0, s48\n" \
"s_cbranch_scc1 Lcw14_%=\n" \
"s_waitcnt vmcnt(16)\n" \
"Lcw14_%=:\n" \
"v_mfma_f32_16x16x32_bf16 v[128:131], a[204:207], a[136:139], v[128:131]\n" \
"ds_read_b64_tr_b16 a[210:211], v227 offset:10240\n" \
"buffer_load_dwordx4 a[104:107], v209, %[srdz], s50 offen offset:2048 sc1\n" \
"v_mfma_f32_16x16x32_bf16 v[128:131], a[224:227], a[140:143], v[128:131]\n" \
"ds_read_b64_tr_b16 a[212:213], v227 offset:12288\n" \
"v_mfma_f32_16x16x32_bf16 v[132:135], a[204:207], a[152:155], v[132:135]\n" \
"ds_read_b64_tr_b16 a[214:215], v227 offset:14336\n" \
"v_mfma_f32_16x16x32_bf16 v[132:135], a[224:227], a[156:159], v[132:135]\n" \
"buffer_load_dwordx4 v214, %[srdt], s49 offen offset:3072 lds\n" \
"v_mfma_f32_16x16x32_bf16 v[136:139], a[204:207], a[168:171], v[136:139]\n" \
"v_mfma_f32_16x16x32_bf16 v[136:139], a[224:227], a[172:175], v[136:139]\n" \
"v_mfma_f32_16x16x32_bf16 v[140:143], a[204:207], a[184:187], v[140:143]\n" \
"ds_read_b64_tr_b16 a[228:229], v215 offset:512\n" \
"s_cbranch_vccz Lg36_%=\n" \
"s_nop 0\n" \
"buffer_store_dwordx4 v[120:123], v210, %[srdz], s60 offen offset:3072 sc1\n" \
"Lg36_%=:\n" \
"v_mfma_f32_16x16x32_bf16 v[140:143], a[224:227], a[188:191], v[140:143]\n" \
"ds_read_b64_tr_b16 a[230:231], v215 offset:2560\n" \
"v_mfma_f32_16x16x32_bf16 v[28:31], a[216:219], a[8:11], v[28:31]\n" \
"buffer_load_dwordx4 a[108:111], v209, %[srdz], s50 offen offset:3072 sc1\n" \
"v_mfma_f32_16x16x32_bf16 v[28:31], a[220:223], a[12:15], v[28:31]\n" \
"v_mfma_f32_16x16x32_bf16 v[60:63], a[216:219], a[24:27], v[60:63]\n" \
"v_mfma_f32_16x16x32_bf16 v[60:63], a[220:223], a[28:31], v[60:63]\n" \
"v_mfma_f32_16x16x32_bf16 v[92:95], a[216:219], a[40:43], v[92:95]\n" \
"buffer_load_dwordx4 a[112:115], v210, %[srdz], s50 offen offset:0 sc1\n" \
"v_mfma_f32_16x16x32_bf16 v[92:95], a[220:223], a[44:47], v[92:95]\n" \
"v_cmp_u_f32_e64 s[44:45], v128, v132\n" \
"v_mfma_f32_16x16x32_bf16 v[124:127], a[216:219], a[56:59], v[124:127]\n" \
"v_mfma_f32_16x16x32_bf16 v[124:127], a[220:223], a[60:63], v[124:127]\n" \
"v_cmp_u_f32_e64 s[56:57], v136, v140\n" \
"s_or_b64 s[56:57], s[56:57], s[44:45]\n" \
"s_cbranch_scc1 Lsp3_%=\n" \
"Lbk3_%=:\n" \
"s_waitcnt vmcnt(63) lgkmcnt(2)\n" \
"s_nop 0\n" \
"v_mfma_f32_16x16x32_bf16 v[144:147], a[232:235], a[128:131], v[144:147]\n" \
"ds_read_b64_tr_b16 a[216:217], v215 offset:4608\n" \
"v_mfma_f32_16x16x32_bf16 v[144:147], a[236:239], a[132:135], v[144:147]\n" \
"ds_read_b64_tr_b16 a[218:219], v215 offset:6656\n" \
"v_mfma_f32_16x16x32_bf16 v[148:151], a[232:235], a[144:147], v[148:151]\n" \
"ds_read_b64_tr_b16 a[196:197], v215 offset:8704\n" \
"v_mfma_f32_16x16x32_bf16 v[148:151], a[236:239], a[148:151], v[148:151]\n" \
"ds_read_b64_tr_b16 a[198:199], v215 offset:10752\n" \
"v_mfma_f32_16x16x32_bf16 v[152:155], a[232:235], a[160:163], v[152:155]\n" \
"ds_read_b64_tr_b16 a[200:201], v215 offset:12800\n" \
"v_mfma_f32_16x16x32_bf16 v[152:155], a[236:239], a[164:167], v[152:155]\n" \
"ds_read_b64_tr_b16 a[202:203], v215 offset:14848\n" \
"v_mfma_f32_16x16x32_bf16 v[156:159], a[232:235], a[176:179], v[156:159]\n" \
"ds_read_b64_tr_b16 a[204:205], v227 offset:512\n" \
"buffer_load_dwordx4 a[116:119], v210, %[srdz], s50 offen offset:1024 sc1\n" \
"v_mfma_f32_16x16x32_bf16 v[156:159], a[236:239], a[180:183], v[156:159]\n" \
"ds_read_b64_tr_b16 a[206:207], v227 offset:2560\n" \
"v_mfma_f32_16x16x32_bf16 v[144:147], a[208:211], a[136:139], v[144:147]\n" \
"ds_read_b64_tr_b16 a[220:221], v227 offset:4608\n" \
"v_mfma_f32_16x16x32_bf16 v[144:147], a[212:215], a[140:143], v[144:147]\n" \
"ds_read_b64_tr_b16 a[222:223], v227 offset:6656\n" \
"v_mfma_f32_16x16x32_bf16 v[148:151], a[208:211], a[152:155], v[148:151]\n" \
"s_add_u32 m0, %[m0b], 98304\n" \
"s_cmp_gt_u32 s48, 4\n" \
"s_cselect_b32 s49, 4, 5\n" \
"s_lshl_b32 s49, s49, 18\n" \
"s_or_b32 s49, s49, s58\n" \
"buffer_load_dwordx4 %[wo0], %[srdt], s49 offen offset:0 lds\n" \
"v_mfma_f32_16x16x32_bf16 v[148:151], a[212:215], a[156:159], v[148:151]\n" \
"v_mfma_f32_16x16x32_bf16 v[152:155], a[208:211], a[168:171], v[152:155]\n" \
"buffer_load_dwordx4 a[120:123], v210, %[srdz], s50 offen offset:2048 sc1\n" \
"v_mfma_f32_16x16x32_bf16 v[152:155], a[212:215], a[172:175], v[152:155]\n" \
"v_mfma_f32_16x16x32_bf16 v[156:159], a[208:211], a[184:187], v[156:159]\n" \
"ds_read_b64_tr_b16 a[224:225], v227 offset:8704\n" \
"v_mfma_f32_16x16x32_bf16 v[156:159], a[212:215], a[188:191], v[156:159]\n" \
"s_waitcnt vmcnt(63) lgkmcnt(5)\n" \
"s_nop 0\n" \
"v_mfma_f32_16x16x32_bf16 v[160:163], a[228:231], a[128:131], v[160:163]\n" \
"ds_read_b64_tr_b16 a[226:227], v227 offset:10752\n" \
"v_mfma_f32_16x16x32_bf16 v[160:163], a[216:219], a[132:135], v[160:163]\n" \
"ds_read_b64_tr_b16 a[232:233], v227 offset:12800\n" \
"v_mfma_f32_16x16x32_bf16 v[164:167], a[228:231], a[144:147], v[164:167]\n" \
"ds_read_b64_tr_b16 a[234:235], v227 offset:14848\n" \
"v_mfma_f32_16x16x32_bf16 v[164:167], a[216:219], a[148:151], v[164:167]\n" \
"buffer_load_dwordx4 a[124:127], v210, %[srdz], s50 offen offset:3072 sc1\n" \
"v_mfma_f32_16x16x32_bf16 v[168:171], a[228:231], a[160:163], v[168:171]\n" \
"v_mfma_f32_16x16x32_bf16 v[168:171], a[216:219], a[164:167], v[168:171]\n" \
"v_mfma_f32_16x16x32_bf16 v[172:175], a[228:231], a[176:179], v[172:175]\n" \
"v_mfma_f32_16x16x32_bf16 v[172:175], a[216:219], a[180:183], v[172:175]\n" \
"v_mfma_f32_16x16x32_bf16 v[160:163], a[196:199], a[136:139], v[160:163]\n" \
"v_mfma_f32_16x16x32_bf16 v[160:163], a[200:203], a[140:143], v[160:163]\n" \
"buffer_load_dwordx4 v212, %[srdt], s49 offen offset:1024 lds\n" \
"v_mfma_f32_16x16x32_bf16 v[164:167], a[196:199], a[152:155], v[164:167]\n" \
"v_mfma_f32_16x16x32_bf16 v[164:167], a[200:203], a[156:159], v[164:167]\n" \
"v_mfma_f32_16x16x32_bf16 v[168:171], a[196:199], a[168:171], v[168:171]\n" \
"s_waitcnt vmcnt(30) lgkmcnt(0)\n" \
"s_nop 0\n" \
"s_bitcmp1_b32 0xc0, s48\n" \
"s_cbranch_scc1 Lcw15_%=\n" \
"s_waitcnt vmcnt(21)\n" \
"Lcw15_%=:\n" \
"s_barrier\n" \
"s_nop 0\n" \
"v_mfma_f32_16x16x32_bf16 v[168:171], a[200:203], a[172:175], v[168:171]\n" \
"ds_read_b64_tr_b16 a[236:237], v215 offset:16384\n" \
"v_mfma_f32_16x16x32_bf16 v[172:175], a[196:199], a[184:187], v[172:175]\n" \
"ds_read_b64_tr_b16 a[238:239], v215 offset:18432\n" \
"v_mfma_f32_16x16x32_bf16 v[172:175], a[200:203], a[188:191], v[172:175]\n" \
"s_waitcnt vmcnt(63) lgkmcnt(2)\n" \
"s_nop 0\n" \
"v_mfma_f32_16x16x32_bf16 v[176:179], a[204:207], a[128:131], v[176:179]\n" \
"ds_read_b64_tr_b16 a[208:209], v215 offset:20480\n" \
"s_cmp_gt_u32 s48, 4\n" \
"s_cselect_b32 s50, 4, 5\n" \
"s_mul_i32 s50, s50, 152064\n" \
"buffer_load_dwordx4 v[0:3], %[oq0], %[srdz], s50 offen offset:0 sc1\n" \
"v_mfma_f32_16x16x32_bf16 v[176:179], a[220:223], a[132:135], v[176:179]\n" \
"ds_read_b64_tr_b16 a[210:211], v215 offset:22528\n" \
"v_mfma_f32_16x16x32_bf16 v[180:183], a[204:207], a[144:147], v[180:183]\n" \
"ds_read_b64_tr_b16 a[212:213], v215 offset:24576\n" \
"v_mfma_f32_16x16x32_bf16 v[180:183], a[220:223], a[148:151], v[180:183]\n" \
"ds_read_b64_tr_b16 a[214:215], v215 offset:26624\n" \
"v_mfma_f32_16x16x32_bf16 v[184:187], a[204:207], a[160:163], v[184:187]\n" \
"ds_read_b64_tr_b16 a[228:229], v215 offset:28672\n" \
"buffer_load_dwordx4 v[8:11], %[oq0], %[srdz], s50 offen offset:1024 sc1\n" \
"v_mfma_f32_16x16x32_bf16 v[184:187], a[220:223], a[164:167], v[184:187]\n" \
"ds_read_b64_tr_b16 a[230:231], v215 offset:30720\n" \
"v_mfma_f32_16x16x32_bf16 v[188:191], a[204:207], a[176:179], v[188:191]\n" \
"ds_read_b64_tr_b16 a[216:217], v227 offset:16384\n" \
"v_mfma_f32_16x16x32_bf16 v[188:191], a[220:223], a[180:183], v[188:191]\n" \
"ds_read_b64_tr_b16 a[218:219], v227 offset:18432\n" \
"v_mfma_f32_16x16x32_bf16 v[176:179], a[224:227], a[136:139], v[176:179]\n" \
"ds_read_b64_tr_b16 a[196:197], v227 offset:20480\n" \
"buffer_load_dwordx4 v[16:19], %[oq0], %[srdz], s50 offen offset:2048 sc1\n" \
"v_mfma_f32_16x16x32_bf16 v[176:179], a[232:235], a[140:143], v[176:179]\n" \
"ds_read_b64_tr_b16 a[198:199], v227 offset:22528\n" \
"v_mfma_f32_16x16x32_bf16 v[180:183], a[224:227], a[152:155], v[180:183]\n" \
"buffer_load_dwordx4 v213, %[srdt], s49 offen offset:2048 lds\n" \
"v_mfma_f32_16x16x32_bf16 v[180:183], a[232:235], a[156:159], v[180:183]\n" \
"v_mfma_f32_16x16x32_bf16 v[184:187], a[224:227], a[168:171], v[184:187]\n" \
"buffer_load_dwordx4 v[24:27], %[oq0], %[srdz], s50 offen offset:3072 sc1\n" \
"v_mfma_f32_16x16x32_bf16 v[184:187], a[232:235], a[172:175], v[184:187]\n" \
"v_mfma_f32_16x16x32_bf16 v[188:191], a[224:227], a[184:187], v[188:191]\n" \
"ds_read_b64_tr_b16 a[200:201], v227 offset:24576\n" \
"v_mfma_f32_16x16x32_bf16 v[188:191], a[232:235], a[188:191], v[188:191]\n" \
"s_waitcnt vmcnt(63) lgkmcnt(5)\n" \
"s_nop 0\n" \
"v_mfma_f32_16x16x32_bf16 v[4:7], a[236:239], a[128:131], v[4:7]\n" \
"ds_read_b64_tr_b16 a[202:203], v227 offset:26624\n" \
"buffer_load_dwordx4 v[32:35], v208, %[srdz], s50 offen offset:0 sc1\n" \
"v_mfma_f32_16x16x32_bf16 v[4:7], a[208:211], a[132:135], v[4:7]\n" \
"ds_read_b64_tr_b16 a[204:205], v227 offset:28672\n" \
"v_mfma_f32_16x16x32_bf16 v[36:39], a[236:239], a[144:147], v[36:39]\n" \
"ds_read_b64_tr_b16 a[206:207], v227 offset:30720\n" \
"v_mfma_f32_16x16x32_bf16 v[36:39], a[208:211], a[148:151], v[36:39]\n" \
"buffer_load_dwordx4 v[40:43], v208, %[srdz], s50 offen offset:1024 sc1\n" \
"v_mfma_f32_16x16x32_bf16 v[68:71], a[236:239], a[160:163], v[68:71]\n" \
"v_mfma_f32_16x16x32_bf16 v[68:71], a[208:211], a[164:167], v[68:71]\n" \
"buffer_load_dwordx4 v[48:51], v208, %[srdz], s50 offen offset:2048 sc1\n" \
"v_mfma_f32_16x16x32_bf16 v[100:103], a[236:239], a[176:179], v[100:103]\n" \
"ds_read_b64_tr_b16 a[220:221], v215 offset:16896\n" \
"v_mfma_f32_16x16x32_bf16 v[100:103], a[208:211], a[180:183], v[100:103]\n" \
"ds_read_b64_tr_b16 a[222:223], v215 offset:18944\n" \
"v_mfma_f32_16x16x32_bf16 v[4:7], a[212:215], a[136:139], v[4:7]\n" \
"ds_read_b64_tr_b16 a[224:225], v215 offset:20992\n" \
"v_mfma_f32_16x16x32_bf16 v[4:7], a[228:231], a[140:143], v[4:7]\n" \
"ds_read_b64_tr_b16 a[226:227], v215 offset:23040\n" \
"v_mfma_f32_16x16x32_bf16 v[36:39], a[212:215], a[152:155], v[36:39]\n" \
"buffer_load_dwordx4 v214, %[srdt], s49 offen offset:3072 lds\n" \
"v_mfma_f32_16x16x32_bf16 v[36:39], a[228:231], a[156:159], v[36:39]\n" \
"v_mfma_f32_16x16x32_bf16 v[68:71], a[212:215], a[168:171], v[68:71]\n" \
"buffer_load_dwordx4 v[56:59], v208, %[srdz], s50 offen offset:3072 sc1\n" \
"v_mfma_f32_16x16x32_bf16 v[68:71], a[228:231], a[172:175], v[68:71]\n" \
"v_mfma_f32_16x16x32_bf16 v[100:103], a[212:215], a[184:187], v[100:103]\n" \
"ds_read_b64_tr_b16 a[232:233], v215 offset:25088\n" \
"v_mfma_f32_16x16x32_bf16 v[100:103], a[228:231], a[188:191], v[100:103]\n" \
"s_waitcnt vmcnt(63) lgkmcnt(5)\n" \
"s_nop 0\n" \
"v_mfma_f32_16x16x32_bf16 v[12:15], a[216:219], a[128:131], v[12:15]\n" \
"ds_read_b64_tr_b16 a[234:235], v215 offset:27136\n" \
"buffer_load_dwordx4 v[64:67], v209, %[srdz], s50 offen offset:0 sc1\n" \
"v_mfma_f32_16x16x32_bf16 v[12:15], a[196:199], a[132:135], v[12:15]\n" \
"ds_read_b64_tr_b16 a[236:237], v215 offset:29184\n" \
"v_mfma_f32_16x16x32_bf16 v[44:47], a[216:219], a[144:147], v[44:47]\n" \
"ds_read_b64_tr_b16 a[238:239], v215 offset:31232\n" \
"v_mfma_f32_16x16x32_bf16 v[44:47], a[196:199], a[148:151], v[44:47]\n" \
"buffer_load_dwordx4 v[72:75], v209, %[srdz], s50 offen offset:1024 sc1\n" \
"v_mfma_f32_16x16x32_bf16 v[76:79], a[216:219], a[160:163], v[76:79]\n" \
"v_mfma_f32_16x16x32_bf16 v[76:79], a[196:199], a[164:167], v[76:79]\n" \
"buffer_load_dwordx4 v[80:83], v209, %[srdz], s50 offen offset:2048 sc1\n" \
"v_mfma_f32_16x16x32_bf16 v[108:111], a[216:219], a[176:179], v[108:111]\n" \
"ds_read_b64_tr_b16 a[208:209], v227 offset:16896\n" \
"v_mfma_f32_16x16x32_bf16 v[108:111], a[196:199], a[180:183], v[108:111]\n" \
"ds_read_b64_tr_b16 a[210:211], v227 offset:18944\n" \
"v_mfma_f32_16x16x32_bf16 v[12:15], a[200:203], a[136:139], v[12:15]\n" \
"ds_read_b64_tr_b16 a[212:213], v227 offset:25088\n" \
"v_mfma_f32_16x16x32_bf16 v[12:15], a[204:207], a[140:143], v[12:15]\n" \
"ds_read_b64_tr_b16 a[214:215], v227 offset:27136\n" \
"v_mfma_f32_16x16x32_bf16 v[44:47], a[200:203], a[152:155], v[44:47]\n" \
"ds_read_b64_tr_b16 a[228:229], v227 offset:20992\n" \
"v_mfma_f32_16x16x32_bf16 v[44:47], a[204:207], a[156:159], v[44:47]\n" \
"ds_read_b64_tr_b16 a[230:231], v227 offset:23040\n" \
"buffer_load_dwordx4 v[88:91], v209, %[srdz], s50 offen offset:3072 sc1\n" \
"v_mfma_f32_16x16x32_bf16 v[76:79], a[200:203], a[168:171], v[76:79]\n" \
"ds_read_b64_tr_b16 a[216:217], v227 offset:29184\n" \
"v_mfma_f32_16x16x32_bf16 v[76:79], a[204:207], a[172:175], v[76:79]\n" \
"ds_read_b64_tr_b16 a[218:219], v227 offset:31232\n" \
"v_mfma_f32_16x16x32_bf16 v[108:111], a[200:203], a[184:187], v[108:111]\n" \
"buffer_load_dwordx4 v[96:99], v210, %[srdz], s50 offen offset:0 sc1\n" \
"v_mfma_f32_16x16x32_bf16 v[108:111], a[204:207], a[188:191], v[108:111]\n" \
"s_waitcnt vmcnt(63) lgkmcnt(12)\n" \
"s_nop 0\n" \
"v_mfma_f32_16x16x32_bf16 v[20:23], a[220:223], a[128:131], v[20:23]\n" \
"s_add_u32 m0, %[m0b], 114688\n" \
"s_cmp_gt_u32 s48, 5\n" \
"s_cselect_b32 s49, 5, 6\n" \
"s_lshl_b32 s49, s49, 18\n" \
"s_or_b32 s49, s49, s58\n" \
"buffer_load_dwordx4 %[wo0], %[srdt], s49 offen offset:0 lds\n" \
"v_mfma_f32_16x16x32_bf16 v[20:23], a[224:227], a[132:135], v[20:23]\n" \
"v_mfma_f32_16x16x32_bf16 v[52:55], a[220:223], a[144:147], v[52:55]\n" \
"buffer_load_dwordx4 v[104:107], v210, %[srdz], s50 offen offset:1024 sc1\n" \
"v_mfma_f32_16x16x32_bf16 v[52:55], a[224:227], a[148:151], v[52:55]\n" \
"v_mfma_f32_16x16x32_bf16 v[84:87], a[220:223], a[160:163], v[84:87]\n" \
"s_waitcnt vmcnt(24) lgkmcnt(0)\n" \
"s_barrier\n" \
"v_mfma_f32_16x16x32_bf16 v[84:87], a[224:227], a[164:167], v[84:87]\n" \
"ds_read_b64_tr_b16 a[196:197], v215 offset:32768\n" \
"v_mfma_f32_16x16x32_bf16 v[116:119], a[220:223], a[176:179], v[116:119]\n" \
"ds_read_b64_tr_b16 a[198:199], v215 offset:34816\n" \
"v_mfma_f32_16x16x32_bf16 v[116:119], a[224:227], a[180:183], v[116:119]\n" \
"ds_read_b64_tr_b16 a[200:201], v215 offset:36864\n" \
"buffer_load_dwordx4 v[112:115], v210, %[srdz], s50 offen offset:2048 sc1\n" \
"v_mfma_f32_16x16x32_bf16 v[20:23], a[232:235], a[136:139], v[20:23]\n" \
"ds_read_b64_tr_b16 a[202:203], v215 offset:38912\n" \
"v_mfma_f32_16x16x32_bf16 v[20:23], a[236:239], a[140:143], v[20:23]\n" \
"ds_read_b64_tr_b16 a[204:205], v215 offset:40960\n" \
"v_mfma_f32_16x16x32_bf16 v[52:55], a[232:235], a[152:155], v[52:55]\n" \
"ds_read_b64_tr_b16 a[206:207], v215 offset:43008\n" \
"v_mfma_f32_16x16x32_bf16 v[52:55], a[236:239], a[156:159], v[52:55]\n" \
"ds_read_b64_tr_b16 a[220:221], v215 offset:45056\n" \
"buffer_load_dwordx4 v[120:123], v210, %[srdz], s50 offen offset:3072 sc1\n" \
"v_mfma_f32_16x16x32_bf16 v[84:87], a[232:235], a[168:171], v[84:87]\n" \
"ds_read_b64_tr_b16 a[222:223], v215 offset:47104\n" \
"v_readlane_b32 s54, %[thi], 16\n" \
"v_mfma_f32_16x16x32_bf16 v[84:87], a[236:239], a[172:175], v[84:87]\n" \
"v_mfma_f32_16x16x32_bf16 v[116:119], a[232:235], a[184:187], v[116:119]\n" \
"v_mfma_f32_16x16x32_bf16 v[116:119], a[236:239], a[188:191], v[116:119]\n" \
"s_waitcnt vmcnt(23) lgkmcnt(4)\n" \
"s_nop 0\n" \
"v_mfma_f32_16x16x32_bf16 v[28:31], a[208:211], a[128:131], v[28:31]\n" \
"buffer_load_dwordx4 v212, %[srdt], s49 offen offset:1024 lds\n" \
"v_mfma_f32_16x16x32_bf16 v[28:31], a[228:231], a[132:135], v[28:31]\n" \
"v_mfma_f32_16x16x32_bf16 v[60:63], a[208:211], a[144:147], v[60:63]\n" \
"s_cmp_gt_u32 s48, 5\n" \
"s_cselect_b32 s50, 5, 6\n" \
"s_mul_i32 s50, s50, 152064\n" \
"buffer_load_dwordx4 a[0:3], %[oq0], %[srdz], s50 offen offset:0 sc1\n" \
"v_mfma_f32_16x16x32_bf16 v[60:63], a[228:231], a[148:151], v[60:63]\n" \
"v_mfma_f32_16x16x32_bf16 v[92:95], a[208:211], a[160:163], v[92:95]\n" \
"buffer_load_dword v232, v226, %[srdz], s54 offen sc1\n" \
"v_mfma_f32_16x16x32_bf16 v[92:95], a[228:231], a[164:167], v[92:95]\n" \
"v_mfma_f32_16x16x32_bf16 v[124:127], a[208:211], a[176:179], v[124:127]\n" \
".if . - adaln_body != 49152\n" \
".error \"size model\"\n" \
".endif\n" \
"Lk11_%=:\n" \
"s_cbranch_execz adaln_kr\n" \
"s_nop 0\n" \
"ds_read_b64_tr_b16 a[224:225], v227 offset:32768\n" \
"buffer_load_dwordx4 a[4:7], %[oq0], %[srdz], s50 offen offset:1024 sc1\n" \
"v_mfma_f32_16x16x32_bf16 v[124:127], a[228:231], a[180:183], v[124:127]\n" \
"ds_read_b64_tr_b16 a[226:227], v227 offset:34816\n" \
"v_mfma_f32_16x16x32_bf16 v[192:195], a[196:199], a[64:67], v[128:131]\n" \
"ds_read_b64_tr_b16 a[232:233], v227 offset:36864\n" \
"v_mfma_f32_16x16x32_bf16 v[192:195], a[200:203], a[68:71], v[192:195]\n" \
"ds_read_b64_tr_b16 a[234:235], v227 offset:38912\n" \
"v_mfma_f32_16x16x32_bf16 v[196:199], a[196:199], a[80:83], v[132:135]\n" \
"buffer_load_dwordx4 a[8:11], %[oq0], %[srdz], s50 offen offset:2048 sc1\n" \
"v_mfma_f32_16x16x32_bf16 v[196:199], a[200:203], a[84:87], v[196:199]\n" \
"v_mfma_f32_16x16x32_bf16 v[200:203], a[196:199], a[96:99], v[136:139]\n" \
"buffer_load_dwordx4 a[12:15], %[oq0], %[srdz], s50 offen offset:3072 sc1\n" \
"v_mfma_f32_16x16x32_bf16 v[200:203], a[200:203], a[100:103], v[200:203]\n" \
"v_mfma_f32_16x16x32_bf16 v[204:207], a[196:199], a[112:115], v[140:143]\n" \
"ds_read_b64_tr_b16 a[236:237], v227 offset:40960\n" \
"v_mfma_f32_16x16x32_bf16 v[204:207], a[200:203], a[116:119], v[204:207]\n" \
"s_waitcnt vmcnt(26) lgkmcnt(5)\n" \
"s_nop 0\n" \
"v_mfma_f32_16x16x32_bf16 v[192:195], a[204:207], a[72:75], v[192:195]\n" \
"ds_read_b64_tr_b16 a[238:239], v227 offset:43008\n" \
"v_mfma_f32_16x16x32_bf16 v[192:195], a[220:223], a[76:79], v[192:195]\n" \
"ds_read_b64_tr_b16 a[208:209], v227 offset:45056\n" \
"buffer_load_dwordx4 v213, %[srdt], s49 offen offset:2048 lds\n" \
"v_mfma_f32_16x16x32_bf16 v[196:199], a[204:207], a[88:91], v[196:199]\n" \
"ds_read_b64_tr_b16 a[210:211], v227 offset:47104\n" \
"v_mfma_f32_16x16x32_bf16 v[196:199], a[220:223], a[92:95], v[196:199]\n" \
"buffer_load_dwordx4 a[16:19], v208, %[srdz], s50 offen offset:0 sc1\n" \
"v_mfma_f32_16x16x32_bf16 v[200:203], a[204:207], a[104:107], v[200:203]\n" \
"v_mfma_f32_16x16x32_bf16 v[200:203], a[220:223], a[108:111], v[200:203]\n" \
"buffer_load_dwordx4 a[20:23], v208, %[srdz], s50 offen offset:1024 sc1\n" \
"v_mfma_f32_16x16x32_bf16 v[204:207], a[204:207], a[120:123], v[204:207]\n" \
"ds_read_b64_tr_b16 a[228:229], v215 offset:33280\n" \
"v_mfma_f32_16x16x32_bf16 v[204:207], a[220:223], a[124:127], v[204:207]\n" \
"ds_read_b64_tr_b16 a[230:231], v215 offset:35328\n" \
"v_mfma_f32_16x16x32_bf16 v[28:31], a[212:215], a[136:139], v[28:31]\n" \
"buffer_load_dwordx4 a[24:27], v208, %[srdz], s50 offen offset:2048 sc1\n" \
"v_mfma_f32_16x16x32_bf16 v[28:31], a[216:219], a[140:143], v[28:31]\n" \
"v_mfma_f32_16x16x32_bf16 v[60:63], a[212:215], a[152:155], v[60:63]\n" \
"buffer_load_dwordx4 a[28:31], v208, %[srdz], s50 offen offset:3072 sc1\n" \
"v_mfma_f32_16x16x32_bf16 v[60:63], a[216:219], a[156:159], v[60:63]\n" \
"v_mfma_f32_16x16x32_bf16 v[92:95], a[212:215], a[168:171], v[92:95]\n" \
"v_mfma_f32_16x16x32_bf16 v[92:95], a[216:219], a[172:175], v[92:95]\n" \
"v_cmp_u_f32_e64 s[44:45], v192, v196\n" \
"buffer_load_dwordx4 v214, %[srdt], s49 offen offset:3072 lds\n" \
"v_mfma_f32_16x16x32_bf16 v[124:127], a[212:215], a[184:187], v[124:127]\n" \
"v_mfma_f32_16x16x32_bf16 v[124:127], a[216:219], a[188:191], v[124:127]\n" \
"v_cmp_u_f32_e64 s[56:57], v200, v204\n" \
"s_or_b64 s[56:57], s[56:57], s[44:45]\n" \
"s_cbranch_scc1 Lsp4_%=\n" \
"Lbk4_%=:\n" \
"s_waitcnt vmcnt(63) lgkmcnt(2)\n" \
"s_nop 0\n" \
"v_mfma_f32_16x16x32_bf16 v[144:147], a[224:227], a[64:67], v[144:147]\n" \
"ds_read_b64_tr_b16 a[212:213], v215 offset:37376\n" \
"v_mfma_f32_16x16x32_bf16 v[144:147], a[232:235], a[68:71], v[144:147]\n" \
"ds_read_b64_tr_b16 a[214:215], v215 offset:39424\n" \
"buffer_load_dwordx4 a[32:35], v209, %[srdz], s50 offen offset:0 sc1\n" \
"v_mfma_f32_16x16x32_bf16 v[148:151], a[224:227], a[80:83], v[148:151]\n" \
"ds_read_b64_tr_b16 a[196:197], v215 offset:41472\n" \
"v_mfma_f32_16x16x32_bf16 v[148:151], a[232:235], a[84:87], v[148:151]\n" \
"ds_read_b64_tr_b16 a[198:199], v215 offset:43520\n" \
"v_mfma_f32_16x16x32_bf16 v[152:155], a[224:227], a[96:99], v[152:155]\n" \
"ds_read_b64_tr_b16 a[200:201], v215 offset:45568\n" \
"v_mfma_f32_16x16x32_bf16 v[152:155], a[232:235], a[100:103], v[152:155]\n" \
"ds_read_b64_tr_b16 a[202:203], v215 offset:47616\n" \
"buffer_load_dwordx4 a[36:39], v209, %[srdz], s50 offen offset:1024 sc1\n" \
"v_mfma_f32_16x16x32_bf16 v[156:159], a[224:227], a[112:115], v[156:159]\n" \
"ds_read_b64_tr_b16 a[204:205], v227 offset:33280\n" \
"v_mfma_f32_16x16x32_bf16 v[156:159], a[232:235], a[116:119], v[156:159]\n" \
"ds_read_b64_tr_b16 a[206:207], v227 offset:35328\n" \
"v_mfma_f32_16x16x32_bf16 v[144:147], a[236:239], a[72:75], v[144:147]\n" \
"ds_read_b64_tr_b16 a[216:217], v227 offset:41472\n" \
"v_mfma_f32_16x16x32_bf16 v[144:147], a[208:211], a[76:79], v[144:147]\n" \
"ds_read_b64_tr_b16 a[218:219], v227 offset:43520\n" \
"buffer_load_dwordx4 a[40:43], v209, %[srdz], s50 offen offset:2048 sc1\n" \
"v_mfma_f32_16x16x32_bf16 v[148:151], a[236:239], a[88:91], v[148:151]\n" \
"ds_read_b64_tr_b16 a[220:221], v227 offset:37376\n" \
"v_mfma_f32_16x16x32_bf16 v[148:151], a[208:211], a[92:95], v[148:151]\n" \
"ds_read_b64_tr_b16 a[222:223], v227 offset:39424\n" \
"v_mfma_f32_16x16x32_bf16 v[152:155], a[236:239], a[104:107], v[152:155]\n" \
"ds_read_b64_tr_b16 a[224:225], v227 offset:45568\n" \
"v_mfma_f32_16x16x32_bf16 v[152:155], a[208:211], a[108:111], v[152:155]\n" \
"ds_read_b64_tr_b16 a[226:227], v227 offset:47616\n" \
"buffer_load_dwordx4 a[44:47], v209, %[srdz], s50 offen offset:3072 sc1\n" \
"v_mfma_f32_16x16x32_bf16 v[156:159], a[236:239], a[120:123], v[156:159]\n" \
"v_mfma_f32_16x16x32_bf16 v[156:159], a[208:211], a[124:127], v[156:159]\n" \
"s_waitcnt vmcnt(63) lgkmcnt(12)\n" \
"s_nop 0\n" \
"v_mfma_f32_16x16x32_bf16 v[160:163], a[228:231], a[64:67], v[160:163]\n" \
"buffer_load_dwordx4 a[48:51], v210, %[srdz], s50 offen offset:0 sc1\n" \
"v_mfma_f32_16x16x32_bf16 v[160:163], a[212:215], a[68:71], v[160:163]\n" \
"v_mfma_f32_16x16x32_bf16 v[164:167], a[228:231], a[80:83], v[164:167]\n" \
"buffer_load_dwordx4 a[52:55], v210, %[srdz], s50 offen offset:1024 sc1\n" \
"v_mfma_f32_16x16x32_bf16 v[164:167], a[212:215], a[84:87], v[164:167]\n" \
"v_mfma_f32_16x16x32_bf16 v[168:171], a[228:231], a[96:99], v[168:171]\n" \
"s_waitcnt vmcnt(28) lgkmcnt(0)\n" \
"s_barrier\n" \
"v_mfma_f32_16x16x32_bf16 v[168:171], a[212:215], a[100:103], v[168:171]\n" \
"ds_read_b64_tr_b16 a[232:233], v220 offset:32768\n" \
"v_mfma_f32_16x16x32_bf16 v[172:175], a[228:231], a[112:115], v[172:175]\n" \
"ds_read_b64_tr_b16 a[234:235], v220 offset:34816\n" \
"v_mfma_f32_16x16x32_bf16 v[172:175], a[212:215], a[116:119], v[172:175]\n" \
"ds_read_b64_tr_b16 a[236:237], v220 offset:36864\n" \
"v_mfma_f32_16x16x32_bf16 v[160:163], a[196:199], a[72:75], v[160:163]\n" \
"ds_read_b64_tr_b16 a[238:239], v220 offset:38912\n" \
"v_mfma_f32_16x16x32_bf16 v[160:163], a[200:203], a[76:79], v[160:163]\n" \
"ds_read_b64_tr_b16 a[208:209], v220 offset:40960\n" \
"s_add_u32 m0, %[m0b], 49152\n" \
"s_cmp_gt_u32 s48, 6\n" \
"s_cselect_b32 s49, 6, 7\n" \
"s_lshl_b32 s49, s49, 18\n" \
"s_or_b32 s49, s49, s58\n" \
"buffer_load_dwordx4 %[wo0], %[srdt], s49 offen offset:0 lds\n" \
"v_mfma_f32_16x16x32_bf16 v[164:167], a[196:199], a[88:91], v[164:167]\n" \
"ds_read_b64_tr_b16 a[210:211], v220 offset:43008\n" \
"v_mfma_f32_16x16x32_bf16 v[164:167], a[200:203], a[92:95], v[164:167]\n" \
"ds_read_b64_tr_b16 a[228:229], v220 offset:45056\n" \
"buffer_load_dwordx4 a[56:59], v210, %[srdz], s50 offen offset:2048 sc1\n" \
"v_mfma_f32_16x16x32_bf16 v[168:171], a[196:199], a[104:107], v[168:171]\n" \
"ds_read_b64_tr_b16 a[230:231], v220 offset:47104\n" \
"v_mfma_f32_16x16x32_bf16 v[168:171], a[200:203], a[108:111], v[168:171]\n" \
"buffer_load_dwordx4 a[60:63], v210, %[srdz], s50 offen offset:3072 sc1\n" \
"v_mfma_f32_16x16x32_bf16 v[172:175], a[196:199], a[120:123], v[172:175]\n" \
"v_mfma_f32_16x16x32_bf16 v[172:175], a[200:203], a[124:127], v[172:175]\n" \
"s_waitcnt vmcnt(23) lgkmcnt(4)\n" \
"s_nop 0\n" \
"v_mfma_f32_16x16x32_bf16 v[176:179], a[204:207], a[64:67], v[176:179]\n" \
"v_mfma_f32_16x16x32_bf16 v[176:179], a[220:223], a[68:71], v[176:179]\n" \
"s_cmp_gt_u32 s48, 6\n" \
"s_cselect_b32 s50, 6, 7\n" \
"s_mul_i32 s50, s50, 152064\n" \
"buffer_load_dwordx4 a[128:131], %[oq0], %[srdz], s50 offen offset:0 sc1\n" \
"v_mfma_f32_16x16x32_bf16 v[180:183], a[204:207], a[80:83], v[180:183]\n" \
"v_mfma_f32_16x16x32_bf16 v[180:183], a[220:223], a[84:87], v[180:183]\n" \
"v_mfma_f32_16x16x32_bf16 v[184:187], a[204:207], a[96:99], v[184:187]\n" \
"v_mfma_f32_16x16x32_bf16 v[184:187], a[220:223], a[100:103], v[184:187]\n" \
"buffer_load_dwordx4 v212, %[srdt], s49 offen offset:1024 lds\n" \
"v_mfma_f32_16x16x32_bf16 v[188:191], a[204:207], a[112:115], v[188:191]\n" \
"ds_read_b64_tr_b16 a[212:213], v221 offset:32768\n" \
"v_mfma_f32_16x16x32_bf16 v[188:191], a[220:223], a[116:119], v[188:191]\n" \
"ds_read_b64_tr_b16 a[214:215], v221 offset:34816\n" \
"v_mfma_f32_16x16x32_bf16 v[128:131], a[232:235], v[0:3], v[192:195]\n" \
"ds_read_b64_tr_b16 a[196:197], v221 offset:36864\n" \
"buffer_load_dwordx4 a[132:135], %[oq0], %[srdz], s50 offen offset:1024 sc1\n" \
"v_mfma_f32_16x16x32_bf16 v[128:131], a[236:239], v[8:11], v[128:131]\n" \
"ds_read_b64_tr_b16 a[198:199], v221 offset:38912\n" \
"v_mfma_f32_16x16x32_bf16 v[132:135], a[232:235], v[32:35], v[196:199]\n" \
"buffer_load_dwordx4 a[136:139], %[oq0], %[srdz], s50 offen offset:2048 sc1\n" \
"v_mfma_f32_16x16x32_bf16 v[132:135], a[236:239], v[40:43], v[132:135]\n" \
"v_mfma_f32_16x16x32_bf16 v[136:139], a[232:235], v[64:67], v[200:203]\n" \
"v_mfma_f32_16x16x32_bf16 v[136:139], a[236:239], v[72:75], v[136:139]\n" \
"buffer_load_dwordx4 a[140:143], %[oq0], %[srdz], s50 offen offset:3072 sc1\n" \
"v_mfma_f32_16x16x32_bf16 v[140:143], a[232:235], v[96:99], v[204:207]\n" \
"ds_read_b64_tr_b16 a[200:201], v221 offset:40960\n" \
"v_mfma_f32_16x16x32_bf16 v[140:143], a[236:239], v[104:107], v[140:143]\n" \
"s_waitcnt vmcnt(26) lgkmcnt(5)\n" \
"s_nop 0\n" \
"v_mfma_f32_16x16x32_bf16 v[128:131], a[208:211], v[16:19], v[128:131]\n" \
"ds_read_b64_tr_b16 a[202:203], v221 offset:43008\n" \
"v_mfma_f32_16x16x32_bf16 v[128:131], a[228:231], v[24:27], v[128:131]\n" \
"ds_read_b64_tr_b16 a[204:205], v221 offset:45056\n" \
"v_mfma_f32_16x16x32_bf16 v[132:135], a[208:211], v[48:51], v[132:135]\n" \
"ds_read_b64_tr_b16 a[206:207], v221 offset:47104\n" \
"v_mfma_f32_16x16x32_bf16 v[132:135], a[228:231], v[56:59], v[132:135]\n" \
"buffer_load_dwordx4 a[144:147], v208, %[srdz], s50 offen offset:0 sc1\n" \
"v_mfma_f32_16x16x32_bf16 v[136:139], a[208:211], v[80:83], v[136:139]\n" \
"v_mfma_f32_16x16x32_bf16 v[136:139], a[228:231], v[88:91], v[136:139]\n" \
"buffer_load_dwordx4 v213, %[srdt], s49 offen offset:2048 lds\n" \
"v_mfma_f32_16x16x32_bf16 v[140:143], a[208:211], v[112:115], v[140:143]\n" \
"ds_read_b64_tr_b16 a[220:221], v220 offset:33280\n" \
"v_mfma_f32_16x16x32_bf16 v[140:143], a[228:231], v[120:123], v[140:143]\n" \
"ds_read_b64_tr_b16 a[222:223], v220 offset:35328\n" \
"v_mfma_f32_16x16x32_bf16 v[176:179], a[216:219], a[72:75], v[176:179]\n" \
"buffer_load_dwordx4 a[148:151], v208, %[srdz], s50 offen offset:1024 sc1\n" \
"v_mfma_f32_16x16x32_bf16 v[176:179], a[224:227], a[76:79], v[176:179]\n" \
"v_mfma_f32_16x16x32_bf16 v[180:183], a[216:219], a[88:91], v[180:183]\n" \
"buffer_load_dwordx4 a[152:155], v208, %[srdz], s50 offen offset:2048 sc1\n" \
"v_mfma_f32_16x16x32_bf16 v[180:183], a[224:227], a[92:95], v[180:183]\n" \
"v_mfma_f32_16x16x32_bf16 v[184:187], a[216:219], a[104:107], v[184:187]\n" \
"v_mfma_f32_16x16x32_bf16 v[184:187], a[224:227], a[108:111], v[184:187]\n" \
"v_cmp_u_f32_e64 s[44:45], v128, v132\n" \
"buffer_load_dwordx4 a[156:159], v208, %[srdz], s50 offen offset:3072 sc1\n" \
"v_mfma_f32_16x16x32_bf16 v[188:191], a[216:219], a[120:123], v[188:191]\n" \
"v_mfma_f32_16x16x32_bf16 v[188:191], a[224:227], a[124:127], v[188:191]\n" \
"v_cmp_u_f32_e64 s[56:57], v136, v140\n" \
"s_or_b64 s[56:57], s[56:57], s[44:45]\n" \
"s_cbranch_scc1 Lsp5_%=\n" \
"Lbk5_%=:\n" \
"s_waitcnt vmcnt(63) lgkmcnt(2)\n" \
"s_nop 0\n" \
"v_mfma_f32_16x16x32_bf16 v[144:147], a[212:215], v[0:3], v[144:147]\n" \
"ds_read_b64_tr_b16 a[216:217], v220 offset:37376\n" \
"v_mfma_f32_16x16x32_bf16 v[144:147], a[196:199], v[8:11], v[144:147]\n" \
"ds_read_b64_tr_b16 a[218:219], v220 offset:39424\n" \
"v_mfma_f32_16x16x32_bf16 v[148:151], a[212:215], v[32:35], v[148:151]\n" \
"ds_read_b64_tr_b16 a[208:209], v220 offset:41472\n" \
"v_mfma_f32_16x16x32_bf16 v[148:151], a[196:199], v[40:43], v[148:151]\n" \
"ds_read_b64_tr_b16 a[210:211], v220 offset:43520\n" \
"v_mfma_f32_16x16x32_bf16 v[152:155], a[212:215], v[64:67], v[152:155]\n" \
"ds_read_b64_tr_b16 a[224:225], v220 offset:45568\n" \
"buffer_load_dwordx4 a[160:163], v209, %[srdz], s50 offen offset:0 sc1\n" \
"v_mfma_f32_16x16x32_bf16 v[152:155], a[196:199], v[72:75], v[152:155]\n" \
"ds_read_b64_tr_b16 a[226:227], v220 offset:47616\n" \
"v_mfma_f32_16x16x32_bf16 v[156:159], a[212:215], v[96:99], v[156:159]\n" \
"ds_read_b64_tr_b16 a[228:229], v221 offset:33280\n" \
"buffer_load_dwordx4 v214, %[srdt], s49 offen offset:3072 lds\n" \
"v_mfma_f32_16x16x32_bf16 v[156:159], a[196:199], v[104:107], v[156:159]\n" \
"ds_read_b64_tr_b16 a[230:231], v221 offset:35328\n" \
"v_mfma_f32_16x16x32_bf16 v[144:147], a[200:203], v[16:19], v[144:147]\n" \
"ds_read_b64_tr_b16 a[232:233], v221 offset:41472\n" \
"buffer_load_dwordx4 a[164:167], v209, %[srdz], s50 offen offset:1024 sc1\n" \
"v_mfma_f32_16x16x32_bf16 v[144:147], a[204:207], v[24:27], v[144:147]\n" \
"ds_read_b64_tr_b16 a[234:235], v221 offset:43520\n" \
"v_mfma_f32_16x16x32_bf16 v[148:151], a[200:203], v[48:51], v[148:151]\n" \
"ds_read_b64_tr_b16 a[236:237], v221 offset:37376\n" \
"v_mfma_f32_16x16x32_bf16 v[148:151], a[204:207], v[56:59], v[148:151]\n" \
"ds_read_b64_tr_b16 a[238:239], v221 offset:39424\n" \
"v_mfma_f32_16x16x32_bf16 v[152:155], a[200:203], v[80:83], v[152:155]\n" \
"ds_read_b64_tr_b16 a[212:213], v221 offset:45568\n" \
"buffer_load_dwordx4 a[168:171], v209, %[srdz], s50 offen offset:2048 sc1\n" \
"v_mfma_f32_16x16x32_bf16 v[152:155], a[204:207], v[88:91], v[152:155]\n" \
"ds_read_b64_tr_b16 a[214:215], v221 offset:47616\n" \
"v_mfma_f32_16x16x32_bf16 v[156:159], a[200:203], v[112:115], v[156:159]\n" \
"buffer_load_dwordx4 a[172:175], v209, %[srdz], s50 offen offset:3072 sc1\n" \
"v_mfma_f32_16x16x32_bf16 v[156:159], a[204:207], v[120:123], v[156:159]\n" \
"s_waitcnt vmcnt(63) lgkmcnt(12)\n" \
"s_nop 0\n" \
"v_mfma_f32_16x16x32_bf16 v[160:163], a[220:223], v[0:3], v[160:163]\n" \
"v_mfma_f32_16x16x32_bf16 v[160:163], a[216:219], v[8:11], v[160:163]\n" \
"buffer_load_dwordx4 a[176:179], v210, %[srdz], s50 offen offset:0 sc1\n" \
"v_mfma_f32_16x16x32_bf16 v[164:167], a[220:223], v[32:35], v[164:167]\n" \
"v_mfma_f32_16x16x32_bf16 v[164:167], a[216:219], v[40:43], v[164:167]\n" \
"v_mfma_f32_16x16x32_bf16 v[168:171], a[220:223], v[64:67], v[168:171]\n" \
"s_waitcnt vmcnt(25) lgkmcnt(0)\n" \
"s_barrier\n" \
"v_mfma_f32_16x16x32_bf16 v[168:171], a[216:219], v[72:75], v[168:171]\n" \
"ds_read_b64_tr_b16 a[196:197], v220 offset:49152\n" \
"v_mfma_f32_16x16x32_bf16 v[172:175], a[220:223], v[96:99], v[172:175]\n" \
"ds_read_b64_tr_b16 a[198:199], v220 offset:51200\n" \
"v_mfma_f32_16x16x32_bf16 v[172:175], a[216:219], v[104:107], v[172:175]\n" \
"ds_read_b64_tr_b16 a[200:201], v220 offset:53248\n" \
"v_mfma_f32_16x16x32_bf16 v[160:163], a[208:211], v[16:19], v[160:163]\n" \
"ds_read_b64_tr_b16 a[202:203], v220 offset:55296\n" \
"buffer_load_dwordx4 a[180:183], v210, %[srdz], s50 offen offset:1024 sc1\n" \
"v_mfma_f32_16x16x32_bf16 v[160:163], a[224:227], v[24:27], v[160:163]\n" \
"ds_read_b64_tr_b16 a[204:205], v220 offset:57344\n" \
"v_mfma_f32_16x16x32_bf16 v[164:167], a[208:211], v[48:51], v[164:167]\n" \
"ds_read_b64_tr_b16 a[206:207], v220 offset:59392\n" \
"v_mfma_f32_16x16x32_bf16 v[164:167], a[224:227], v[56:59], v[164:167]\n" \
"ds_read_b64_tr_b16 a[220:221], v220 offset:61440\n" \
"v_mfma_f32_16x16x32_bf16 v[168:171], a[208:211], v[80:83], v[168:171]\n" \
"ds_read_b64_tr_b16 a[222:223], v220 offset:63488\n" \
"buffer_load_dwordx4 a[184:187], v210, %[srdz], s50 offen offset:2048 sc1\n" \
"v_mfma_f32_16x16x32_bf16 v[168:171], a[224:227], v[88:91], v[168:171]\n" \
"v_mfma_f32_16x16x32_bf16 v[172:175], a[208:211], v[112:115], v[172:175]\n" \
"buffer_load_dwordx4 a[188:191], v210, %[srdz], s50 offen offset:3072 sc1\n" \
"v_mfma_f32_16x16x32_bf16 v[172:175], a[224:227], v[120:123], v[172:175]\n" \
"s_waitcnt vmcnt(63) lgkmcnt(4)\n" \
"s_nop 0\n" \
"v_mfma_f32_16x16x32_bf16 v[176:179], a[228:231], v[0:3], v[176:179]\n" \
"v_mfma_f32_16x16x32_bf16 v[176:179], a[236:239], v[8:11], v[176:179]\n" \
"v_mfma_f32_16x16x32_bf16 v[180:183], a[228:231], v[32:35], v[180:183]\n" \
"v_mfma_f32_16x16x32_bf16 v[180:183], a[236:239], v[40:43], v[180:183]\n" \
"v_mfma_f32_16x16x32_bf16 v[184:187], a[228:231], v[64:67], v[184:187]\n" \
"v_mfma_f32_16x16x32_bf16 v[184:187], a[236:239], v[72:75], v[184:187]\n" \
"v_mfma_f32_16x16x32_bf16 v[188:191], a[228:231], v[96:99], v[188:191]\n" \
"ds_read_b64_tr_b16 a[216:217], v221 offset:49152\n" \
"s_waitcnt vmcnt(22)\n" \
"v_cmp_eq_u32_e32 vcc, 0, v232\n" \
"s_cbranch_vccnz Lspp_%=\n" \
"Lbkp_%=:\n" \
"s_nop 0\n" \
"v_mfma_f32_16x16x32_bf16 v[188:191], a[236:239], v[104:107], v[188:191]\n" \
"ds_read_b64_tr_b16 a[218:219], v221 offset:51200\n" \
"v_mfma_f32_16x16x32_bf16 v[192:195], a[196:199], a[0:3], v[128:131]\n" \
"ds_read_b64_tr_b16 a[208:209], v221 offset:53248\n" \
"v_mfma_f32_16x16x32_bf16 v[192:195], a[200:203], a[4:7], v[192:195]\n" \
"ds_read_b64_tr_b16 a[210:211], v221 offset:55296\n" \
"s_add_u32 m0, %[m0b], 0\n" \
"s_cmp_gt_u32 s48, 3\n" \
"s_cselect_b32 s49, 3, 4\n" \
"s_lshl_b32 s49, s49, 18\n" \
"s_or_b32 s49, s49, s59\n" \
"s_nop 0\n" \
"buffer_load_dwordx4 %[wo0], %[srdt], s49 offen offset:0 lds\n" \
"v_mfma_f32_16x16x32_bf16 v[196:199], a[196:199], a[16:19], v[132:135]\n" \
"v_mfma_f32_16x16x32_bf16 v[196:199], a[200:203], a[20:23], v[196:199]\n" \
"v_mfma_f32_16x16x32_bf16 v[200:203], a[196:199], a[32:35], v[136:139]\n" \
"v_mfma_f32_16x16x32_bf16 v[200:203], a[200:203], a[36:39], v[200:203]\n" \
"v_mfma_f32_16x16x32_bf16 v[204:207], a[196:199], a[48:51], v[140:143]\n" \
"ds_read_b64_tr_b16 a[224:225], v221 offset:57344\n" \
"v_mfma_f32_16x16x32_bf16 v[204:207], a[200:203], a[52:55], v[204:207]\n" \
"s_waitcnt vmcnt(20) lgkmcnt(5)\n" \
"s_nop 0\n" \
"v_mfma_f32_16x16x32_bf16 v[192:195], a[204:207], a[8:11], v[192:195]\n" \
"ds_read_b64_tr_b16 a[226:227], v221 offset:59392\n" \
"v_mfma_f32_16x16x32_bf16 v[192:195], a[220:223], a[12:15], v[192:195]\n" \
"ds_read_b64_tr_b16 a[228:229], v221 offset:61440\n" \
"v_mfma_f32_16x16x32_bf16 v[196:199], a[204:207], a[24:27], v[196:199]\n" \
"ds_read_b64_tr_b16 a[230:231], v221 offset:63488\n" \
"v_mfma_f32_16x16x32_bf16 v[196:199], a[220:223], a[28:31], v[196:199]\n" \
"buffer_load_dwordx4 v212, %[srdt], s49 offen offset:1024 lds\n" \
"v_mfma_f32_16x16x32_bf16 v[200:203], a[204:207], a[40:43], v[200:203]\n" \
"v_mfma_f32_16x16x32_bf16 v[200:203], a[220:223], a[44:47], v[200:203]\n" \
"v_mfma_f32_16x16x32_bf16 v[204:207], a[204:207], a[56:59], v[204:207]\n" \
"ds_read_b64_tr_b16 a[236:237], v220 offset:49664\n" \
"v_mfma_f32_16x16x32_bf16 v[204:207], a[220:223], a[60:63], v[204:207]\n" \
"ds_read_b64_tr_b16 a[238:239], v220 offset:51712\n" \
"v_mfma_f32_16x16x32_bf16 v[176:179], a[232:235], v[16:19], v[176:179]\n" \
"v_mfma_f32_16x16x32_bf16 v[176:179], a[212:215], v[24:27], v[176:179]\n" \
"v_mfma_f32_16x16x32_bf16 v[180:183], a[232:235], v[48:51], v[180:183]\n" \
"buffer_load_dwordx4 v213, %[srdt], s49 offen offset:2048 lds\n" \
"v_mfma_f32_16x16x32_bf16 v[180:183], a[212:215], v[56:59], v[180:183]\n" \
"v_mfma_f32_16x16x32_bf16 v[184:187], a[232:235], v[80:83], v[184:187]\n" \
"s_add_u32 m0, %[m0b], 98304\n" \
"s_mov_b32 s61, 0\n" \
"s_nop 0\n" \
"buffer_load_dwordx4 %[vdn], s[64:67], s61 offen offset:0 lds\n" \
"v_mfma_f32_16x16x32_bf16 v[184:187], a[212:215], v[88:91], v[184:187]\n" \
"v_cmp_u_f32_e64 s[44:45], v192, v196\n" \
"v_mfma_f32_16x16x32_bf16 v[188:191], a[232:235], v[112:115], v[188:191]\n" \
"v_mfma_f32_16x16x32_bf16 v[188:191], a[212:215], v[120:123], v[188:191]\n" \
"v_cmp_u_f32_e64 s[56:57], v200, v204\n" \
"s_or_b64 s[56:57], s[56:57], s[44:45]\n" \
"s_cbranch_scc1 Lsp6_%=\n" \
"Lbk6_%=:\n" \
"s_waitcnt vmcnt(63) lgkmcnt(2)\n" \
"s_nop 0\n" \
"v_mfma_f32_16x16x32_bf16 v[144:147], a[216:219], a[0:3], v[144:147]\n" \
"ds_read_b64_tr_b16 a[232:233], v220 offset:53760\n" \
"v_mfma_f32_16x16x32_bf16 v[144:147], a[208:211], a[4:7], v[144:147]\n" \
"ds_read_b64_tr_b16 a[234:235], v220 offset:55808\n" \
"v_mfma_f32_16x16x32_bf16 v[148:151], a[216:219], a[16:19], v[148:151]\n" \
"ds_read_b64_tr_b16 a[196:197], v220 offset:57856\n" \
"v_mfma_f32_16x16x32_bf16 v[148:151], a[208:211], a[20:23], v[148:151]\n" \
"ds_read_b64_tr_b16 a[198:199], v220 offset:59904\n" \
"v_mfma_f32_16x16x32_bf16 v[152:155], a[216:219], a[32:35], v[152:155]\n" \
"ds_read_b64_tr_b16 a[200:201], v220 offset:61952\n" \
"v_mfma_f32_16x16x32_bf16 v[152:155], a[208:211], a[36:39], v[152:155]\n" \
"ds_read_b64_tr_b16 a[202:203], v220 offset:64000\n" \
"v_mfma_f32_16x16x32_bf16 v[156:159], a[216:219], a[48:51], v[156:159]\n" \
"ds_read_b64_tr_b16 a[204:205], v221 offset:49664\n" \
"v_mfma_f32_16x16x32_bf16 v[156:159], a[208:211], a[52:55], v[156:159]\n" \
"ds_read_b64_tr_b16 a[206:207], v221 offset:51712\n" \
"s_add_u32 m0, %[m0b], 0\n" \
"s_nop 0\n" \
"buffer_load_dwordx4 v214, %[srdt], s49 offen offset:3072 lds\n" \
"v_mfma_f32_16x16x32_bf16 v[144:147], a[224:227], a[8:11], v[144:147]\n" \
"ds_read_b64_tr_b16 a[212:213], v221 offset:57856\n" \
"v_mfma_f32_16x16x32_bf16 v[144:147], a[228:231], a[12:15], v[144:147]\n" \
"ds_read_b64_tr_b16 a[214:215], v221 offset:59904\n" \
"v_mfma_f32_16x16x32_bf16 v[148:151], a[224:227], a[24:27], v[148:151]\n" \
"ds_read_b64_tr_b16 a[220:221], v221 offset:53760\n" \
"v_mfma_f32_16x16x32_bf16 v[148:151], a[228:231], a[28:31], v[148:151]\n" \
"ds_read_b64_tr_b16 a[222:223], v221 offset:55808\n" \
"v_mfma_f32_16x16x32_bf16 v[152:155], a[224:227], a[40:43], v[152:155]\n" \
"ds_read_b64_tr_b16 a[216:217], v221 offset:61952\n" \
"v_mfma_f32_16x16x32_bf16 v[152:155], a[228:231], a[44:47], v[152:155]\n" \
"ds_read_b64_tr_b16 a[218:219], v221 offset:64000\n" \
"v_mfma_f32_16x16x32_bf16 v[156:159], a[224:227], a[56:59], v[156:159]\n" \
"s_add_u32 m0, %[m0b], 99328\n" \
"s_mov_b32 s61, 4096\n" \
"buffer_load_dwordx4 %[vdn], s[64:67], s61 offen offset:0 lds\n" \
"v_mfma_f32_16x16x32_bf16 v[156:159], a[228:231], a[60:63], v[156:159]\n" \
"s_waitcnt vmcnt(63) lgkmcnt(12)\n" \
"s_nop 0\n" \
"v_mfma_f32_16x16x32_bf16 v[160:163], a[236:239], a[0:3], v[160:163]\n" \
"v_mfma_f32_16x16x32_bf16 v[160:163], a[232:235], a[4:7], v[160:163]\n" \
"v_mfma_f32_16x16x32_bf16 v[164:167], a[236:239], a[16:19], v[164:167]\n" \
"v_mfma_f32_16x16x32_bf16 v[164:167], a[232:235], a[20:23], v[164:167]\n" \
"s_add_u32 m0, %[m0b], 16384\n" \
"s_cmp_gt_u32 s48, 4\n" \
"s_cselect_b32 s49, 4, 5\n" \
"s_lshl_b32 s49, s49, 18\n" \
"s_or_b32 s49, s49, s59\n" \
"buffer_load_dwordx4 %[wo0], %[srdt], s49 offen offset:0 lds\n" \
"v_mfma_f32_16x16x32_bf16 v[168:171], a[236:239], a[32:35], v[168:171]\n" \
"s_waitcnt vmcnt(14) lgkmcnt(0)\n" \
"s_barrier\n" \
"v_mfma_f32_16x16x32_bf16 v[168:171], a[232:235], a[36:39], v[168:171]\n" \
"ds_read_b64_tr_b16 a[208:209], v215 offset:49152\n" \
"v_mfma_f32_16x16x32_bf16 v[172:175], a[236:239], a[48:51], v[172:175]\n" \
"ds_read_b64_tr_b16 a[210:211], v215 offset:51200\n" \
"v_mfma_f32_16x16x32_bf16 v[172:175], a[232:235], a[52:55], v[172:175]\n" \
"ds_read_b64_tr_b16 a[224:225], v215 offset:53248\n" \
"v_mfma_f32_16x16x32_bf16 v[160:163], a[196:199], a[8:11], v[160:163]\n" \
"ds_read_b64_tr_b16 a[226:227], v215 offset:55296\n" \
"v_mfma_f32_16x16x32_bf16 v[160:163], a[200:203], a[12:15], v[160:163]\n" \
"ds_read_b64_tr_b16 a[228:229], v215 offset:57344\n" \
"v_mfma_f32_16x16x32_bf16 v[164:167], a[196:199], a[24:27], v[164:167]\n" \
"ds_read_b64_tr_b16 a[230:231], v215 offset:59392\n" \
"v_mfma_f32_16x16x32_bf16 v[164:167], a[200:203], a[28:31], v[164:167]\n" \
"ds_read_b64_tr_b16 a[236:237], v215 offset:61440\n" \
"v_mfma_f32_16x16x32_bf16 v[168:171], a[196:199], a[40:43], v[168:171]\n" \
"ds_read_b64_tr_b16 a[238:239], v215 offset:63488\n" \
"v_mfma_f32_16x16x32_bf16 v[168:171], a[200:203], a[44:47], v[168:171]\n" \
"buffer_load_dwordx4 v212, %[srdt], s49 offen offset:1024 lds\n" \
"v_mfma_f32_16x16x32_bf16 v[172:175], a[196:199], a[56:59], v[172:175]\n" \
"v_mfma_f32_16x16x32_bf16 v[172:175], a[200:203], a[60:63], v[172:175]\n" \
"s_waitcnt vmcnt(10) lgkmcnt(4)\n" \
"s_nop 0\n" \
"v_mfma_f32_16x16x32_bf16 v[176:179], a[204:207], a[0:3], v[176:179]\n" \
"s_add_u32 m0, %[m0b], 100352\n" \
"s_mov_b32 s61, 8192\n" \
"buffer_load_dwordx4 %[vdn], s[64:67], s61 offen offset:0 lds\n" \
"v_mfma_f32_16x16x32_bf16 v[176:179], a[220:223], a[4:7], v[176:179]\n" \
"v_mfma_f32_16x16x32_bf16 v[180:183], a[204:207], a[16:19], v[180:183]\n" \
"v_mfma_f32_16x16x32_bf16 v[180:183], a[220:223], a[20:23], v[180:183]\n" \
"v_mfma_f32_16x16x32_bf16 v[184:187], a[204:207], a[32:35], v[184:187]\n" \
"v_mfma_f32_16x16x32_bf16 v[184:187], a[220:223], a[36:39], v[184:187]\n" \
"v_mfma_f32_16x16x32_bf16 v[188:191], a[204:207], a[48:51], v[188:191]\n" \
"ds_read_b64_tr_b16 a[232:233], v227 offset:49152\n" \
"v_mfma_f32_16x16x32_bf16 v[188:191], a[220:223], a[52:55], v[188:191]\n" \
"ds_read_b64_tr_b16 a[234:235], v227 offset:51200\n" \
"v_mfma_f32_16x16x32_bf16 v[128:131], a[208:211], a[128:131], v[192:195]\n" \
"ds_read_b64_tr_b16 a[196:197], v227 offset:53248\n" \
"v_mfma_f32_16x16x32_bf16 v[128:131], a[224:227], a[132:135], v[128:131]\n" \
"ds_read_b64_tr_b16 a[198:199], v227 offset:55296\n" \
"v_mfma_f32_16x16x32_bf16 v[132:135], a[208:211], a[144:147], v[196:199]\n" \
"s_add_u32 m0, %[m0b], 16384\n" \
"s_nop 0\n" \
"s_nop 0\n" \
"buffer_load_dwordx4 v213, %[srdt], s49 offen offset:2048 lds\n" \
"v_mfma_f32_16x16x32_bf16 v[132:135], a[224:227], a[148:151], v[132:135]\n" \
"v_mfma_f32_16x16x32_bf16 v[136:139], a[208:211], a[160:163], v[200:203]\n" \
"v_mfma_f32_16x16x32_bf16 v[136:139], a[224:227], a[164:167], v[136:139]\n" \
"v_mfma_f32_16x16x32_bf16 v[140:143], a[208:211], a[176:179], v[204:207]\n" \
"ds_read_b64_tr_b16 a[200:201], v227 offset:57344\n" \
"v_mfma_f32_16x16x32_bf16 v[140:143], a[224:227], a[180:183], v[140:143]\n" \
"s_waitcnt vmcnt(10) lgkmcnt(5)\n" \
"s_nop 0\n" \
"v_mfma_f32_16x16x32_bf16 v[128:131], a[228:231], a[136:139], v[128:131]\n" \
"ds_read_b64_tr_b16 a[202:203], v227 offset:59392\n" \
"v_mfma_f32_16x16x32_bf16 v[128:131], a[236:239], a[140:143], v[128:131]\n" \
"ds_read_b64_tr_b16 a[204:205], v227 offset:61440\n" \
"s_add_u32 m0, %[m0b], 101376\n" \
"s_mov_b32 s61, 12288\n" \
"buffer_load_dwordx4 %[vdn], s[64:67], s61 offen offset:0 lds\n" \
"v_mfma_f32_16x16x32_bf16 v[132:135], a[228:231], a[152:155], v[132:135]\n" \
"ds_read_b64_tr_b16 a[206:207], v227 offset:63488\n" \
"v_mfma_f32_16x16x32_bf16 v[132:135], a[236:239], a[156:159], v[132:135]\n" \
"s_add_u32 m0, %[m0b], 16384\n" \
"s_nop 0\n" \
"s_nop 0\n" \
"buffer_load_dwordx4 v214, %[srdt], s49 offen offset:3072 lds\n" \
"v_mfma_f32_16x16x32_bf16 v[136:139], a[228:231], a[168:171], v[136:139]\n" \
"v_mfma_f32_16x16x32_bf16 v[136:139], a[236:239], a[172:175], v[136:139]\n" \
"v_mfma_f32_16x16x32_bf16 v[140:143], a[228:231], a[184:187], v[140:143]\n" \
"ds_read_b64_tr_b16 a[220:221], v215 offset:49664\n" \
"v_mfma_f32_16x16x32_bf16 v[140:143], a[236:239], a[188:191], v[140:143]\n" \
"ds_read_b64_tr_b16 a[222:223], v215 offset:51712\n" \
"v_mfma_f32_16x16x32_bf16 v[176:179], a[212:215], a[8:11], v[176:179]\n" \
"v_mfma_f32_16x16x32_bf16 v[176:179], a[216:219], a[12:15], v[176:179]\n" \
"v_mfma_f32_16x16x32_bf16 v[180:183], a[212:215], a[24:27], v[180:183]\n" \
"v_mfma_f32_16x16x32_bf16 v[180:183], a[216:219], a[28:31], v[180:183]\n" \
"v_mfma_f32_16x16x32_bf16 v[184:187], a[212:215], a[40:43], v[184:187]\n" \
"v_mfma_f32_16x16x32_bf16 v[184:187], a[216:219], a[44:47], v[184:187]\n" \
"v_cmp_u_f32_e64 s[44:45], v128, v132\n" \
"v_mfma_f32_16x16x32_bf16 v[188:191], a[212:215], a[56:59], v[188:191]\n" \
"v_mfma_f32_16x16x32_bf16 v[188:191], a[216:219], a[60:63], v[188:191]\n" \
"v_cmp_u_f32_e64 s[56:57], v136, v140\n" \
"s_or_b64 s[56:57], s[56:57], s[44:45]\n" \
"s_cbranch_scc1 Lsp7_%=\n" \
"Lbk7_%=:\n" \
"s_waitcnt vmcnt(63) lgkmcnt(2)\n" \
"s_nop 0\n" \
"v_mfma_f32_16x16x32_bf16 v[144:147], a[232:235], a[128:131], v[144:147]\n" \
"ds_read_b64_tr_b16 a[212:213], v215 offset:53760\n" \
"v_mfma_f32_16x16x32_bf16 v[144:147], a[196:199], a[132:135], v[144:147]\n" \
"ds_read_b64_tr_b16 a[214:215], v215 offset:55808\n" \
"v_mfma_f32_16x16x32_bf16 v[148:151], a[232:235], a[144:147], v[148:151]\n" \
"ds_read_b64_tr_b16 a[208:209], v215 offset:57856\n" \
"v_mfma_f32_16x16x32_bf16 v[148:151], a[196:199], a[148:151], v[148:151]\n" \
"ds_read_b64_tr_b16 a[210:211], v215 offset:59904\n" \
"s_lshl_b32 s61, %[wid], 10\n" \
"v_lshrrev_b32_e32 v231, 3, v224\n" \
"v_add_u32_e64 v231, s61, v231\n" \
"s_add_u32 m0, %[m0b], 32768\n" \
"s_cmp_gt_u32 s48, 5\n" \
"s_cselect_b32 s49, 5, 6\n" \
"s_lshl_b32 s49, s49, 18\n" \
"s_or_b32 s49, s49, s59\n" \
"buffer_load_dwordx4 %[wo0], %[srdt], s49 offen offset:0 lds\n" \
"v_mfma_f32_16x16x32_bf16 v[152:155], a[232:235], a[160:163], v[152:155]\n" \
"ds_read_b64_tr_b16 a[216:217], v215 offset:61952\n" \
"v_readlane_b32 s44, %[tlo], 20\n" \
"v_readlane_b32 s45, %[thi], 20\n" \
"v_lshrrev_b32_e64 v236, 1, v231\n" \
"v_mfma_f32_16x16x32_bf16 v[152:155], a[196:199], a[164:167], v[152:155]\n" \
"ds_read_b64_tr_b16 a[218:219], v215 offset:64000\n" \
"v_mfma_f32_16x16x32_bf16 v[156:159], a[232:235], a[176:179], v[156:159]\n" \
"ds_read_b64_tr_b16 a[224:225], v227 offset:49664\n" \
"v_mfma_f32_16x16x32_bf16 v[156:159], a[196:199], a[180:183], v[156:159]\n" \
"ds_read_b64_tr_b16 a[226:227], v227 offset:51712\n" \
"v_mfma_f32_16x16x32_bf16 v[144:147], a[200:203], a[136:139], v[144:147]\n" \
"ds_read_b64_tr_b16 a[228:229], v227 offset:53760\n" \
"v_mfma_f32_16x16x32_bf16 v[144:147], a[204:207], a[140:143], v[144:147]\n" \
"ds_read_b64_tr_b16 a[230:231], v227 offset:55808\n" \
"v_mfma_f32_16x16x32_bf16 v[148:151], a[200:203], a[152:155], v[148:151]\n" \
"s_mov_b32 s61, 262144\n" \
"buffer_load_dwordx4 v[192:195], v231, %[srdz], s61 offen offset:0 sc1\n" \
"v_mfma_f32_16x16x32_bf16 v[148:151], a[204:207], a[156:159], v[148:151]\n" \
"v_mfma_f32_16x16x32_bf16 v[152:155], a[200:203], a[168:171], v[152:155]\n" \
"buffer_load_dwordx4 v212, %[srdt], s49 offen offset:1024 lds\n" \
"v_mfma_f32_16x16x32_bf16 v[152:155], a[204:207], a[172:175], v[152:155]\n" \
"v_mfma_f32_16x16x32_bf16 v[156:159], a[200:203], a[184:187], v[156:159]\n" \
"ds_read_b64_tr_b16 a[236:237], v227 offset:57856\n" \
"v_mfma_f32_16x16x32_bf16 v[156:159], a[204:207], a[188:191], v[156:159]\n" \
"s_waitcnt vmcnt(63) lgkmcnt(5)\n" \
"s_nop 0\n" \
"v_mfma_f32_16x16x32_bf16 v[160:163], a[220:223], a[128:131], v[160:163]\n" \
"ds_read_b64_tr_b16 a[238:239], v227 offset:59904\n" \
"v_mfma_f32_16x16x32_bf16 v[160:163], a[212:215], a[132:135], v[160:163]\n" \
"ds_read_b64_tr_b16 a[232:233], v227 offset:61952\n" \
"v_mfma_f32_16x16x32_bf16 v[164:167], a[220:223], a[144:147], v[164:167]\n" \
"ds_read_b64_tr_b16 a[234:235], v227 offset:64000\n" \
"s_mov_b32 s61, 266240\n" \
"buffer_load_dwordx4 v[196:199], v231, %[srdz], s61 offen offset:0 sc1\n" \
"v_mfma_f32_16x16x32_bf16 v[164:167], a[212:215], a[148:151], v[164:167]\n" \
"v_mfma_f32_16x16x32_bf16 v[168:171], a[220:223], a[160:163], v[168:171]\n" \
"v_cvt_pk_bf16_f32 v128, v128, v129\n" \
"buffer_load_dwordx4 v213, %[srdt], s49 offen offset:2048 lds\n" \
"v_mfma_f32_16x16x32_bf16 v[168:171], a[212:215], a[164:167], v[168:171]\n" \
"v_cvt_pk_bf16_f32 v129, v130, v131\n" \
"v_mfma_f32_16x16x32_bf16 v[172:175], a[220:223], a[176:179], v[172:175]\n" \
"v_cvt_pk_bf16_f32 v130, v144, v145\n" \
"v_mfma_f32_16x16x32_bf16 v[172:175], a[212:215], a[180:183], v[172:175]\n" \
"v_cvt_pk_bf16_f32 v131, v146, v147\n" \
"v_mfma_f32_16x16x32_bf16 v[160:163], a[208:211], a[136:139], v[160:163]\n" \
"s_add_u32 s55, s46, 0\n" \
"s_nop 0\n" \
"buffer_store_dwordx4 v[128:131], %[vxs], %[srdz], s55 offen offset:0 sc1\n" \
"v_mfma_f32_16x16x32_bf16 v[160:163], a[216:219], a[140:143], v[160:163]\n" \
"v_mfma_f32_16x16x32_bf16 v[164:167], a[208:211], a[152:155], v[164:167]\n" \
"v_cvt_pk_bf16_f32 v132, v132, v133\n" \
"s_mov_b32 s61, 270336\n" \
"buffer_load_dwordx4 v[200:203], v231, %[srdz], s61 offen offset:0 sc1\n" \
"v_mfma_f32_16x16x32_bf16 v[164:167], a[216:219], a[156:159], v[164:167]\n" \
"v_cvt_pk_bf16_f32 v133, v134, v135\n" \
"v_mfma_f32_16x16x32_bf16 v[168:171], a[208:211], a[168:171], v[168:171]\n" \
"s_waitcnt vmcnt(14) lgkmcnt(0)\n" \
"s_barrier\n" \
"v_mfma_f32_16x16x32_bf16 v[168:171], a[216:219], a[172:175], v[168:171]\n" \
"ds_read_b64_tr_b16 a[196:197], v215 offset:0\n" \
"v_cvt_pk_bf16_f32 v134, v148, v149\n" \
"v_mfma_f32_16x16x32_bf16 v[172:175], a[208:211], a[184:187], v[172:175]\n" \
"ds_read_b64_tr_b16 a[198:199], v215 offset:2048\n" \
"v_cvt_pk_bf16_f32 v135, v150, v151\n" \
"v_mfma_f32_16x16x32_bf16 v[172:175], a[216:219], a[188:191], v[172:175]\n" \
"s_waitcnt vmcnt(63) lgkmcnt(2)\n" \
"s_nop 0\n" \
"v_mfma_f32_16x16x32_bf16 v[176:179], a[224:227], a[128:131], v[176:179]\n" \
"ds_read_b64_tr_b16 a[200:201], v215 offset:4096\n" \
"v_cvt_pk_bf16_f32 v136, v136, v137\n" \
"v_mfma_f32_16x16x32_bf16 v[176:179], a[228:231], a[132:135], v[176:179]\n" \
"ds_read_b64_tr_b16 a[202:203], v215 offset:6144\n" \
"v_cvt_pk_bf16_f32 v137, v138, v139\n" \
".if . - adaln_body != 53248\n" \
".error \"size model\"\n" \
".endif\n" \
"Lk12_%=:\n" \
"s_cbranch_execz Lk13_%=\n" \
"s_nop 0\n" \
"v_mfma_f32_16x16x32_bf16 v[180:183], a[224:227], a[144:147], v[180:183]\n" \
"ds_read_b64_tr_b16 a[204:205], v215 offset:8192\n" \
"s_add_u32 s55, s46, 304128\n" \
"buffer_store_dwordx4 v[132:135], %[vxs], %[srdz], s55 offen offset:0 sc1\n" \
"v_mfma_f32_16x16x32_bf16 v[180:183], a[228:231], a[148:151], v[180:183]\n" \
"ds_read_b64_tr_b16 a[206:207], v215 offset:10240\n" \
"v_cvt_pk_bf16_f32 v138, v152, v153\n" \
"v_mfma_f32_16x16x32_bf16 v[184:187], a[224:227], a[160:163], v[184:187]\n" \
"ds_read_b64_tr_b16 a[220:221], v215 offset:12288\n" \
"v_cvt_pk_bf16_f32 v139, v154, v155\n" \
"s_mov_b32 s61, 274432\n" \
"buffer_load_dwordx4 v[204:207], v231, %[srdz], s61 offen offset:0 sc1\n" \
"v_mfma_f32_16x16x32_bf16 v[184:187], a[228:231], a[164:167], v[184:187]\n" \
"ds_read_b64_tr_b16 a[222:223], v215 offset:14336\n" \
"v_mfma_f32_16x16x32_bf16 v[188:191], a[224:227], a[176:179], v[188:191]\n" \
"ds_read_b64_tr_b16 a[212:213], v227 offset:0\n" \
"v_cvt_pk_bf16_f32 v140, v140, v141\n" \
"buffer_load_dwordx4 v214, %[srdt], s49 offen offset:3072 lds\n" \
"v_mfma_f32_16x16x32_bf16 v[188:191], a[228:231], a[180:183], v[188:191]\n" \
"ds_read_b64_tr_b16 a[214:215], v227 offset:2048\n" \
"v_cvt_pk_bf16_f32 v141, v142, v143\n" \
"v_mfma_f32_16x16x32_bf16 v[176:179], a[236:239], a[136:139], v[176:179]\n" \
"ds_read_b64_tr_b16 a[208:209], v227 offset:4096\n" \
"s_add_u32 s55, s46, 608256\n" \
"buffer_store_dwordx4 v[136:139], %[vxs], %[srdz], s55 offen offset:0 sc1\n" \
"v_mfma_f32_16x16x32_bf16 v[176:179], a[232:235], a[140:143], v[176:179]\n" \
"ds_read_b64_tr_b16 a[210:211], v227 offset:6144\n" \
"v_cvt_pk_bf16_f32 v142, v156, v157\n" \
"v_mfma_f32_16x16x32_bf16 v[180:183], a[236:239], a[152:155], v[180:183]\n" \
"v_cvt_pk_bf16_f32 v143, v158, v159\n" \
"v_mfma_f32_16x16x32_bf16 v[180:183], a[232:235], a[156:159], v[180:183]\n" \
"s_add_u32 s55, s46, 912384\n" \
"buffer_store_dwordx4 v[140:143], %[vxs], %[srdz], s55 offen offset:0 sc1\n" \
"v_mfma_f32_16x16x32_bf16 v[184:187], a[236:239], a[168:171], v[184:187]\n" \
"v_mfma_f32_16x16x32_bf16 v[184:187], a[232:235], a[172:175], v[184:187]\n" \
"global_load_dwordx2 v[232:233], v236, s[44:45]\n" \
"v_mfma_f32_16x16x32_bf16 v[188:191], a[236:239], a[184:187], v[188:191]\n" \
"ds_read_b64_tr_b16 a[216:217], v227 offset:8192\n" \
"v_mfma_f32_16x16x32_bf16 v[188:191], a[232:235], a[188:191], v[188:191]\n" \
"s_waitcnt vmcnt(63) lgkmcnt(5)\n" \
"s_nop 0\n" \
"v_mfma_f32_16x16x32_bf16 v[4:7], a[196:199], a[64:67], v[4:7]\n" \
"ds_read_b64_tr_b16 a[218:219], v227 offset:10240\n" \
"v_mfma_f32_16x16x32_bf16 v[4:7], a[200:203], a[68:71], v[4:7]\n" \
"ds_read_b64_tr_b16 a[224:225], v227 offset:12288\n" \
"v_mfma_f32_16x16x32_bf16 v[36:39], a[196:199], a[80:83], v[36:39]\n" \
"ds_read_b64_tr_b16 a[226:227], v227 offset:14336\n" \
"v_mfma_f32_16x16x32_bf16 v[36:39], a[200:203], a[84:87], v[36:39]\n" \
"v_mfma_f32_16x16x32_bf16 v[68:71], a[196:199], a[96:99], v[68:71]\n" \
"v_cvt_pk_bf16_f32 v160, v160, v161\n" \
"v_mfma_f32_16x16x32_bf16 v[68:71], a[200:203], a[100:103], v[68:71]\n" \
"v_cvt_pk_bf16_f32 v161, v162, v163\n" \
"v_mfma_f32_16x16x32_bf16 v[100:103], a[196:199], a[112:115], v[100:103]\n" \
"ds_read_b64_tr_b16 a[228:229], v215 offset:512\n" \
"v_cvt_pk_bf16_f32 v162, v176, v177\n" \
"v_mfma_f32_16x16x32_bf16 v[100:103], a[200:203], a[116:119], v[100:103]\n" \
"ds_read_b64_tr_b16 a[230:231], v215 offset:2560\n" \
"v_cvt_pk_bf16_f32 v163, v178, v179\n" \
"v_mfma_f32_16x16x32_bf16 v[4:7], a[204:207], a[72:75], v[4:7]\n" \
"ds_read_b64_tr_b16 a[236:237], v215 offset:4608\n" \
"v_mfma_f32_16x16x32_bf16 v[4:7], a[220:223], a[76:79], v[4:7]\n" \
"ds_read_b64_tr_b16 a[238:239], v215 offset:6656\n" \
"v_mfma_f32_16x16x32_bf16 v[36:39], a[204:207], a[88:91], v[36:39]\n" \
"s_add_u32 s55, s46, 4096\n" \
"buffer_store_dwordx4 v[160:163], %[vxs], %[srdz], s55 offen offset:0 sc1\n" \
"v_mfma_f32_16x16x32_bf16 v[36:39], a[220:223], a[92:95], v[36:39]\n" \
"v_cvt_pk_bf16_f32 v164, v164, v165\n" \
"v_cvt_pk_bf16_f32 v165, v166, v167\n" \
"v_mfma_f32_16x16x32_bf16 v[68:71], a[204:207], a[104:107], v[68:71]\n" \
"v_cvt_pk_bf16_f32 v166, v180, v181\n" \
"s_add_u32 m0, %[m0b], 49152\n" \
"s_cmp_gt_u32 s48, 6\n" \
"s_cselect_b32 s49, 6, 7\n" \
"s_lshl_b32 s49, s49, 18\n" \
"s_or_b32 s49, s49, s59\n" \
"buffer_load_dwordx4 %[wo0], %[srdt], s49 offen offset:0 lds\n" \
"v_mfma_f32_16x16x32_bf16 v[68:71], a[220:223], a[108:111], v[68:71]\n" \
"v_cvt_pk_bf16_f32 v167, v182, v183\n" \
"v_mfma_f32_16x16x32_bf16 v[100:103], a[204:207], a[120:123], v[100:103]\n" \
"ds_read_b64_tr_b16 a[232:233], v215 offset:8704\n" \
"v_mfma_f32_16x16x32_bf16 v[100:103], a[220:223], a[124:127], v[100:103]\n" \
"s_waitcnt vmcnt(63) lgkmcnt(5)\n" \
"s_nop 0\n" \
"v_mfma_f32_16x16x32_bf16 v[12:15], a[212:215], a[64:67], v[12:15]\n" \
"ds_read_b64_tr_b16 a[234:235], v215 offset:10752\n" \
"v_cvt_pk_bf16_f32 v168, v168, v169\n" \
"v_mfma_f32_16x16x32_bf16 v[12:15], a[208:211], a[68:71], v[12:15]\n" \
"ds_read_b64_tr_b16 a[196:197], v215 offset:12800\n" \
"s_add_u32 s55, s46, 308224\n" \
"buffer_store_dwordx4 v[164:167], %[vxs], %[srdz], s55 offen offset:0 sc1\n" \
"v_mfma_f32_16x16x32_bf16 v[44:47], a[212:215], a[80:83], v[44:47]\n" \
"ds_read_b64_tr_b16 a[198:199], v215 offset:14848\n" \
"v_cvt_pk_bf16_f32 v169, v170, v171\n" \
"v_mfma_f32_16x16x32_bf16 v[44:47], a[208:211], a[84:87], v[44:47]\n" \
"v_cvt_pk_bf16_f32 v170, v184, v185\n" \
"v_cvt_pk_bf16_f32 v171, v186, v187\n" \
"v_mfma_f32_16x16x32_bf16 v[76:79], a[212:215], a[96:99], v[76:79]\n" \
"s_add_u32 s55, s46, 612352\n" \
"buffer_store_dwordx4 v[168:171], %[vxs], %[srdz], s55 offen offset:0 sc1\n" \
"v_mfma_f32_16x16x32_bf16 v[76:79], a[208:211], a[100:103], v[76:79]\n" \
"v_mfma_f32_16x16x32_bf16 v[108:111], a[212:215], a[112:115], v[108:111]\n" \
"ds_read_b64_tr_b16 a[200:201], v227 offset:512\n" \
"v_cvt_pk_bf16_f32 v172, v172, v173\n" \
"buffer_load_dwordx4 v212, %[srdt], s49 offen offset:1024 lds\n" \
"v_mfma_f32_16x16x32_bf16 v[108:111], a[208:211], a[116:119], v[108:111]\n" \
"ds_read_b64_tr_b16 a[202:203], v227 offset:2560\n" \
"v_cvt_pk_bf16_f32 v173, v174, v175\n" \
"v_mfma_f32_16x16x32_bf16 v[12:15], a[216:219], a[72:75], v[12:15]\n" \
"ds_read_b64_tr_b16 a[204:205], v227 offset:4608\n" \
"v_cvt_pk_bf16_f32 v174, v188, v189\n" \
"v_mfma_f32_16x16x32_bf16 v[12:15], a[224:227], a[76:79], v[12:15]\n" \
"ds_read_b64_tr_b16 a[206:207], v227 offset:6656\n" \
"v_cvt_pk_bf16_f32 v175, v190, v191\n" \
"v_mfma_f32_16x16x32_bf16 v[44:47], a[216:219], a[88:91], v[44:47]\n" \
"s_add_u32 s55, s46, 916480\n" \
"buffer_store_dwordx4 v[172:175], %[vxs], %[srdz], s55 offen offset:0 sc1\n" \
"v_mfma_f32_16x16x32_bf16 v[44:47], a[224:227], a[92:95], v[44:47]\n" \
"v_mfma_f32_16x16x32_bf16 v[76:79], a[216:219], a[104:107], v[76:79]\n" \
"buffer_load_dwordx4 v213, %[srdt], s49 offen offset:2048 lds\n" \
"v_mfma_f32_16x16x32_bf16 v[76:79], a[224:227], a[108:111], v[76:79]\n" \
"v_mfma_f32_16x16x32_bf16 v[108:111], a[216:219], a[120:123], v[108:111]\n" \
"ds_read_b64_tr_b16 a[220:221], v227 offset:8704\n" \
"v_accvgpr_read_b32 v129, a240\n" \
"v_mfma_f32_16x16x32_bf16 v[108:111], a[224:227], a[124:127], v[108:111]\n" \
"s_waitcnt vmcnt(63) lgkmcnt(5)\n" \
"s_nop 0\n" \
"v_mfma_f32_16x16x32_bf16 v[20:23], a[228:231], a[64:67], v[20:23]\n" \
"ds_read_b64_tr_b16 a[222:223], v227 offset:10752\n" \
"v_lshlrev_b32_e64 v128, 16, v129\n" \
"v_mfma_f32_16x16x32_bf16 v[20:23], a[236:239], a[68:71], v[20:23]\n" \
"ds_read_b64_tr_b16 a[212:213], v227 offset:12800\n" \
"v_and_b32_e32 v129, 0xffff0000, v129\n" \
"v_mfma_f32_16x16x32_bf16 v[52:55], a[228:231], a[80:83], v[52:55]\n" \
"ds_read_b64_tr_b16 a[214:215], v227 offset:14848\n" \
"v_accvgpr_read_b32 v131, a241\n" \
"v_mfma_f32_16x16x32_bf16 v[52:55], a[236:239], a[84:87], v[52:55]\n" \
"v_lshlrev_b32_e64 v130, 16, v131\n" \
"v_and_b32_e32 v131, 0xffff0000, v131\n" \
"global_load_dword v237, %[vct], %[pct]\n" \
"v_mfma_f32_16x16x32_bf16 v[84:87], a[228:231], a[96:99], v[84:87]\n" \
"v_accvgpr_read_b32 v133, a242\n" \
"v_mfma_f32_16x16x32_bf16 v[84:87], a[236:239], a[100:103], v[84:87]\n" \
"v_lshlrev_b32_e64 v132, 16, v133\n" \
"s_add_u32 m0, %[m0b], 114688\n" \
"s_mov_b32 s61, 16384\n" \
"buffer_load_dwordx4 %[vdn], s[64:67], s61 offen offset:0 lds\n" \
"v_mfma_f32_16x16x32_bf16 v[116:119], a[228:231], a[112:115], v[116:119]\n" \
"v_and_b32_e32 v133, 0xffff0000, v133\n" \
"v_mfma_f32_16x16x32_bf16 v[116:119], a[236:239], a[116:119], v[116:119]\n" \
"v_accvgpr_read_b32 v135, a243\n" \
"s_add_u32 m0, %[m0b], 49152\n" \
"s_nop 0\n" \
"s_nop 0\n" \
"buffer_load_dwordx4 v214, %[srdt], s49 offen offset:3072 lds\n" \
"v_mfma_f32_16x16x32_bf16 v[20:23], a[232:235], a[72:75], v[20:23]\n" \
"v_lshlrev_b32_e64 v134, 16, v135\n" \
"v_mfma_f32_16x16x32_bf16 v[20:23], a[196:199], a[76:79], v[20:23]\n" \
"v_and_b32_e32 v135, 0xffff0000, v135\n" \
"v_mfma_f32_16x16x32_bf16 v[52:55], a[232:235], a[88:91], v[52:55]\n" \
"v_accvgpr_read_b32 v137, a244\n" \
"v_mfma_f32_16x16x32_bf16 v[52:55], a[196:199], a[92:95], v[52:55]\n" \
"v_lshlrev_b32_e64 v136, 16, v137\n" \
"v_mfma_f32_16x16x32_bf16 v[84:87], a[232:235], a[104:107], v[84:87]\n" \
"s_waitcnt vmcnt(23) lgkmcnt(0)\n" \
"s_barrier\n" \
"v_mfma_f32_16x16x32_bf16 v[84:87], a[196:199], a[108:111], v[84:87]\n" \
"ds_read_b64_tr_b16 a[208:209], v215 offset:16384\n" \
"v_and_b32_e32 v137, 0xffff0000, v137\n" \
"v_mfma_f32_16x16x32_bf16 v[116:119], a[232:235], a[120:123], v[116:119]\n" \
"ds_read_b64_tr_b16 a[210:211], v215 offset:18432\n" \
"v_accvgpr_read_b32 v139, a245\n" \
"v_mfma_f32_16x16x32_bf16 v[116:119], a[196:199], a[124:127], v[116:119]\n" \
"s_waitcnt vmcnt(63) lgkmcnt(2)\n" \
"s_nop 0\n" \
"v_mfma_f32_16x16x32_bf16 v[28:31], a[200:203], a[64:67], v[28:31]\n" \
"ds_read_b64_tr_b16 a[216:217], v215 offset:20480\n" \
"v_lshlrev_b32_e64 v138, 16, v139\n" \
"v_mfma_f32_16x16x32_bf16 v[28:31], a[204:207], a[68:71], v[28:31]\n" \
"ds_read_b64_tr_b16 a[218:219], v215 offset:22528\n" \
"v_and_b32_e32 v139, 0xffff0000, v139\n" \
"v_mfma_f32_16x16x32_bf16 v[60:63], a[200:203], a[80:83], v[60:63]\n" \
"ds_read_b64_tr_b16 a[224:225], v215 offset:24576\n" \
"v_accvgpr_read_b32 v141, a246\n" \
"v_mfma_f32_16x16x32_bf16 v[60:63], a[204:207], a[84:87], v[60:63]\n" \
"ds_read_b64_tr_b16 a[226:227], v215 offset:26624\n" \
"v_lshlrev_b32_e64 v140, 16, v141\n" \
"v_mfma_f32_16x16x32_bf16 v[92:95], a[200:203], a[96:99], v[92:95]\n" \
"ds_read_b64_tr_b16 a[228:229], v215 offset:28672\n" \
"v_and_b32_e32 v141, 0xffff0000, v141\n" \
"s_add_u32 m0, %[m0b], 115712\n" \
"s_mov_b32 s61, 20480\n" \
"buffer_load_dwordx4 %[vdn], s[64:67], s61 offen offset:0 lds\n" \
"v_mfma_f32_16x16x32_bf16 v[92:95], a[204:207], a[100:103], v[92:95]\n" \
"ds_read_b64_tr_b16 a[230:231], v215 offset:30720\n" \
"v_accvgpr_read_b32 v143, a247\n" \
"v_mfma_f32_16x16x32_bf16 v[124:127], a[200:203], a[112:115], v[124:127]\n" \
"ds_read_b64_tr_b16 a[236:237], v227 offset:16384\n" \
"v_lshlrev_b32_e64 v142, 16, v143\n" \
"v_mfma_f32_16x16x32_bf16 v[124:127], a[204:207], a[116:119], v[124:127]\n" \
"ds_read_b64_tr_b16 a[238:239], v227 offset:18432\n" \
"v_and_b32_e32 v143, 0xffff0000, v143\n" \
"v_mfma_f32_16x16x32_bf16 v[28:31], a[220:223], a[72:75], v[28:31]\n" \
"ds_read_b64_tr_b16 a[232:233], v227 offset:20480\n" \
"v_accvgpr_read_b32 v145, a248\n" \
"v_mfma_f32_16x16x32_bf16 v[28:31], a[212:215], a[76:79], v[28:31]\n" \
"ds_read_b64_tr_b16 a[234:235], v227 offset:22528\n" \
"v_lshlrev_b32_e64 v144, 16, v145\n" \
"v_mfma_f32_16x16x32_bf16 v[60:63], a[220:223], a[88:91], v[60:63]\n" \
"v_and_b32_e32 v145, 0xffff0000, v145\n" \
"v_accvgpr_read_b32 v147, a249\n" \
"v_mfma_f32_16x16x32_bf16 v[60:63], a[212:215], a[92:95], v[60:63]\n" \
"v_lshlrev_b32_e64 v146, 16, v147\n" \
"v_and_b32_e32 v147, 0xffff0000, v147\n" \
"v_mfma_f32_16x16x32_bf16 v[92:95], a[220:223], a[104:107], v[92:95]\n" \
"v_accvgpr_read_b32 v149, a250\n" \
"v_mfma_f32_16x16x32_bf16 v[92:95], a[212:215], a[108:111], v[92:95]\n" \
"v_lshlrev_b32_e64 v148, 16, v149\n" \
"v_mfma_f32_16x16x32_bf16 v[124:127], a[220:223], a[120:123], v[124:127]\n" \
"ds_read_b64_tr_b16 a[196:197], v227 offset:24576\n" \
"v_and_b32_e32 v149, 0xffff0000, v149\n" \
"v_mfma_f32_16x16x32_bf16 v[124:127], a[212:215], a[124:127], v[124:127]\n" \
"s_waitcnt vmcnt(63) lgkmcnt(5)\n" \
"s_nop 0\n" \
"v_mfma_f32_16x16x32_bf16 v[4:7], a[208:211], v[0:3], v[4:7]\n" \
"ds_read_b64_tr_b16 a[198:199], v227 offset:26624\n" \
"v_accvgpr_read_b32 v151, a251\n" \
"v_mfma_f32_16x16x32_bf16 v[4:7], a[216:219], v[8:11], v[4:7]\n" \
"ds_read_b64_tr_b16 a[200:201], v227 offset:28672\n" \
"v_lshlrev_b32_e64 v150, 16, v151\n" \
"v_mfma_f32_16x16x32_bf16 v[36:39], a[208:211], v[32:35], v[36:39]\n" \
"ds_read_b64_tr_b16 a[202:203], v227 offset:30720\n" \
"v_and_b32_e32 v151, 0xffff0000, v151\n" \
"v_mfma_f32_16x16x32_bf16 v[36:39], a[216:219], v[40:43], v[36:39]\n" \
"v_accvgpr_read_b32 v153, a252\n" \
"v_lshlrev_b32_e64 v152, 16, v153\n" \
"s_add_u32 m0, %[m0b], 116736\n" \
"s_mov_b32 s61, 24576\n" \
"buffer_load_dwordx4 %[vdn], s[64:67], s61 offen offset:0 lds\n" \
"v_mfma_f32_16x16x32_bf16 v[68:71], a[208:211], v[64:67], v[68:71]\n" \
"v_and_b32_e32 v153, 0xffff0000, v153\n" \
"v_mfma_f32_16x16x32_bf16 v[68:71], a[216:219], v[72:75], v[68:71]\n" \
"v_accvgpr_read_b32 v155, a253\n" \
"v_mfma_f32_16x16x32_bf16 v[100:103], a[208:211], v[96:99], v[100:103]\n" \
"v_lshlrev_b32_e64 v154, 16, v155\n" \
"v_mfma_f32_16x16x32_bf16 v[100:103], a[216:219], v[104:107], v[100:103]\n" \
"v_and_b32_e32 v155, 0xffff0000, v155\n" \
"v_mfma_f32_16x16x32_bf16 v[4:7], a[224:227], v[16:19], v[4:7]\n" \
"v_accvgpr_read_b32 v157, a254\n" \
"v_mfma_f32_16x16x32_bf16 v[4:7], a[228:231], v[24:27], v[4:7]\n" \
"v_lshlrev_b32_e64 v156, 16, v157\n" \
"v_mfma_f32_16x16x32_bf16 v[36:39], a[224:227], v[48:51], v[36:39]\n" \
"v_and_b32_e32 v157, 0xffff0000, v157\n" \
"v_mfma_f32_16x16x32_bf16 v[36:39], a[228:231], v[56:59], v[36:39]\n" \
"v_accvgpr_read_b32 v159, a255\n" \
"v_mfma_f32_16x16x32_bf16 v[68:71], a[224:227], v[80:83], v[68:71]\n" \
"s_waitcnt vmcnt(15) lgkmcnt(15)\n" \
"s_barrier\n" \
"v_mfma_f32_16x16x32_bf16 v[68:71], a[228:231], v[88:91], v[68:71]\n" \
"ds_read_b64_tr_b16 a[204:205], v215 offset:32768\n" \
"v_lshlrev_b32_e64 v158, 16, v159\n" \
"v_mfma_f32_16x16x32_bf16 v[100:103], a[224:227], v[112:115], v[100:103]\n" \
"ds_read_b64_tr_b16 a[206:207], v215 offset:34816\n" \
"v_and_b32_e32 v159, 0xffff0000, v159\n" \
"v_mfma_f32_16x16x32_bf16 v[100:103], a[228:231], v[120:123], v[100:103]\n" \
"s_waitcnt vmcnt(63) lgkmcnt(2)\n" \
"s_nop 0\n" \
"v_mfma_f32_16x16x32_bf16 v[12:15], a[236:239], v[0:3], v[12:15]\n" \
"ds_read_b64_tr_b16 a[220:221], v215 offset:36864\n" \
"v_and_b32_e32 v208, 0x7ff, %[vx0]\n" \
"v_mfma_f32_16x16x32_bf16 v[12:15], a[232:235], v[8:11], v[12:15]\n" \
"ds_read_b64_tr_b16 a[222:223], v215 offset:38912\n" \
"v_lshlrev_b32_e64 v208, 1, v208\n" \
"v_mfma_f32_16x16x32_bf16 v[44:47], a[236:239], v[32:35], v[44:47]\n" \
"ds_read_b64_tr_b16 a[212:213], v215 offset:40960\n" \
"v_xor_b32_e32 v209, 0x100, v208\n" \
"v_mfma_f32_16x16x32_bf16 v[44:47], a[232:235], v[40:43], v[44:47]\n" \
"ds_read_b64_tr_b16 a[214:215], v215 offset:43008\n" \
"v_lshrrev_b32_e64 v210, 3, v224\n" \
"v_mfma_f32_16x16x32_bf16 v[76:79], a[236:239], v[64:67], v[76:79]\n" \
"ds_read_b64_tr_b16 a[208:209], v215 offset:45056\n" \
"v_and_b32_e64 v210, 48, v210\n" \
"v_mfma_f32_16x16x32_bf16 v[76:79], a[232:235], v[72:75], v[76:79]\n" \
"ds_read_b64_tr_b16 a[210:211], v215 offset:47104\n" \
"s_lshl_b32 s61, %[wid], 12\n" \
"s_sub_u32 s61, %[m0b], s61\n" \
"v_add_u32_e32 v208, s61, v208\n" \
"v_add_u32_e32 v209, s61, v209\n" \
"v_add_u32_e64 v210, s61, v210\n" \
"v_mfma_f32_16x16x32_bf16 v[108:111], a[236:239], v[96:99], v[108:111]\n" \
"ds_read_b64_tr_b16 a[216:217], v227 offset:32768\n" \
"v_mfma_f32_16x16x32_bf16 v[108:111], a[232:235], v[104:107], v[108:111]\n" \
"ds_read_b64_tr_b16 a[218:219], v227 offset:34816\n" \
"s_waitcnt vmcnt(12)\n" \
"s_nop 0\n" \
"v_and_or_b32 v236, v192, v193, 0\n" \
"s_add_u32 m0, %[m0b], 117760\n" \
"s_mov_b32 s61, 28672\n" \
"buffer_load_dwordx4 %[vdn], s[64:67], s61 offen offset:0 lds\n" \
"v_mfma_f32_16x16x32_bf16 v[12:15], a[196:199], v[16:19], v[12:15]\n" \
"ds_read_b64_tr_b16 a[224:225], v227 offset:36864\n" \
"v_bitop3_b32 v236, v236, v194, v195 bitop3:0x80\n" \
"v_mfma_f32_16x16x32_bf16 v[12:15], a[200:203], v[24:27], v[12:15]\n" \
"ds_read_b64_tr_b16 a[226:227], v227 offset:38912\n" \
"v_bitop3_b32 v236, v236, v196, v197 bitop3:0x80\n" \
"v_mfma_f32_16x16x32_bf16 v[44:47], a[196:199], v[48:51], v[44:47]\n" \
"v_bitop3_b32 v236, v236, v198, v199 bitop3:0x80\n" \
"v_mfma_f32_16x16x32_bf16 v[44:47], a[200:203], v[56:59], v[44:47]\n" \
"v_bitop3_b32 v236, v236, v200, v201 bitop3:0x80\n" \
"v_mfma_f32_16x16x32_bf16 v[76:79], a[196:199], v[80:83], v[76:79]\n" \
"v_bitop3_b32 v236, v236, v202, v203 bitop3:0x80\n" \
"v_mfma_f32_16x16x32_bf16 v[76:79], a[200:203], v[88:91], v[76:79]\n" \
"v_bitop3_b32 v236, v236, v204, v205 bitop3:0x80\n" \
"v_mfma_f32_16x16x32_bf16 v[108:111], a[196:199], v[112:115], v[108:111]\n" \
"ds_read_b64_tr_b16 a[228:229], v227 offset:40960\n" \
"v_bitop3_b32 v236, v236, v206, v207 bitop3:0x80\n" \
"v_mfma_f32_16x16x32_bf16 v[108:111], a[200:203], v[120:123], v[108:111]\n" \
"s_waitcnt vmcnt(63) lgkmcnt(5)\n" \
"s_nop 0\n" \
"v_mfma_f32_16x16x32_bf16 v[4:7], a[204:207], a[0:3], v[4:7]\n" \
"ds_read_b64_tr_b16 a[230:231], v227 offset:43008\n" \
"s_mov_b32 s54, 0x07060302\n" \
"v_and_b32_e64 v236, 1, v236\n" \
"v_mfma_f32_16x16x32_bf16 v[4:7], a[220:223], a[4:7], v[4:7]\n" \
"ds_read_b64_tr_b16 a[236:237], v227 offset:45056\n" \
"v_lshrrev_b32_e64 v218, 1, v231\n" \
"v_mfma_f32_16x16x32_bf16 v[36:39], a[204:207], a[16:19], v[36:39]\n" \
"ds_read_b64_tr_b16 a[238:239], v227 offset:47104\n" \
"v_cmp_eq_u32_e64 s[56:57], 0, v236\n" \
"v_mfma_f32_16x16x32_bf16 v[36:39], a[220:223], a[20:23], v[36:39]\n" \
"s_waitcnt vmcnt(7)\n" \
"v_mov_b32_e32 v230, 1\n" \
"buffer_store_dword v230, v225, %[srdz], s62 offen offset:64 sc1\n" \
"v_mfma_f32_16x16x32_bf16 v[68:71], a[204:207], a[32:35], v[68:71]\n" \
"s_lshl_b32 s61, %[wid], 12\n" \
"s_sub_u32 s61, %[m0b], s61\n" \
"v_add_u32_e32 v231, s61, v231\n" \
"v_add_u32_e32 v218, s61, v218\n" \
"v_mov_b32_e64 v219, s61\n" \
"ds_write_b128 v231, v[192:195] offset:0\n" \
"v_mfma_f32_16x16x32_bf16 v[68:71], a[220:223], a[36:39], v[68:71]\n" \
"v_lshlrev_b32_e64 v234, 16, v233\n" \
"v_and_b32_e32 v235, 0xffff0000, v233\n" \
"v_mfma_f32_16x16x32_bf16 v[100:103], a[204:207], a[48:51], v[100:103]\n" \
"v_and_b32_e32 v233, 0xffff0000, v232\n" \
"v_mfma_f32_16x16x32_bf16 v[100:103], a[220:223], a[52:55], v[100:103]\n" \
"v_lshlrev_b32_e64 v232, 16, v232\n" \
"v_mfma_f32_16x16x32_bf16 v[4:7], a[212:215], a[8:11], v[4:7]\n" \
"v_mul_f32_e64 v234, v202, v234\n" \
"v_mfma_f32_16x16x32_bf16 v[4:7], a[208:211], a[12:15], v[4:7]\n" \
"v_mul_f32_e64 v235, v203, v235\n" \
"v_mfma_f32_16x16x32_bf16 v[36:39], a[212:215], a[24:27], v[36:39]\n" \
"v_mul_f32_e64 v233, v201, v233\n" \
"v_mfma_f32_16x16x32_bf16 v[36:39], a[208:211], a[28:31], v[36:39]\n" \
"v_mul_f32_e64 v232, v200, v232\n" \
"v_mfma_f32_16x16x32_bf16 v[68:71], a[212:215], a[40:43], v[68:71]\n" \
"s_waitcnt vmcnt(4) lgkmcnt(15)\n" \
"s_barrier\n" \
"v_mfma_f32_16x16x32_bf16 v[68:71], a[208:211], a[44:47], v[68:71]\n" \
"ds_read_b64_tr_b16 a[232:233], v215 offset:49152\n" \
"v_add_f32_e64 v223, v234, v235\n" \
"v_mfma_f32_16x16x32_bf16 v[100:103], a[212:215], a[56:59], v[100:103]\n" \
"ds_read_b64_tr_b16 a[234:235], v215 offset:51200\n" \
"v_add_f32_e64 v222, v232, v233\n" \
"v_mfma_f32_16x16x32_bf16 v[100:103], a[208:211], a[60:63], v[100:103]\n" \
"s_waitcnt vmcnt(63) lgkmcnt(3)\n" \
"s_nop 0\n" \
"v_mfma_f32_16x16x32_bf16 v[12:15], a[216:219], a[0:3], v[12:15]\n" \
"ds_read_b64_tr_b16 a[196:197], v215 offset:53248\n" \
"ds_write_b128 v231, v[232:235] offset:4096\n" \
"v_mfma_f32_16x16x32_bf16 v[12:15], a[224:227], a[4:7], v[12:15]\n" \
"ds_read_b64_tr_b16 a[198:199], v215 offset:55296\n" \
"v_add_f32_e64 v222, v222, v223\n" \
"v_mfma_f32_16x16x32_bf16 v[44:47], a[216:219], a[16:19], v[44:47]\n" \
"ds_read_b64_tr_b16 a[200:201], v215 offset:57344\n" \
"v_perm_b32 v196, v197, v196, s54\n" \
"v_mfma_f32_16x16x32_bf16 v[44:47], a[224:227], a[20:23], v[44:47]\n" \
"ds_read_b64_tr_b16 a[202:203], v215 offset:59392\n" \
"v_add_f32_dpp v222, v222, v222 quad_perm:[1,0,3,2] row_mask:0xf bank_mask:0xf\n" \
"v_mfma_f32_16x16x32_bf16 v[76:79], a[216:219], a[32:35], v[76:79]\n" \
"ds_read_b64_tr_b16 a[204:205], v215 offset:61440\n" \
"v_perm_b32 v197, v199, v198, s54\n" \
"v_mfma_f32_16x16x32_bf16 v[76:79], a[224:227], a[36:39], v[76:79]\n" \
"ds_read_b64_tr_b16 a[206:207], v215 offset:63488\n" \
"v_add_f32_dpp v222, v222, v222 quad_perm:[2,3,0,1] row_mask:0xf bank_mask:0xf\n" \
"v_mfma_f32_16x16x32_bf16 v[108:111], a[216:219], a[48:51], v[108:111]\n" \
"ds_read_b64_tr_b16 a[220:221], v227 offset:49152\n" \
"ds_write_b64 v218, v[196:197] offset:8192\n" \
"v_mfma_f32_16x16x32_bf16 v[108:111], a[224:227], a[52:55], v[108:111]\n" \
"ds_read_b64_tr_b16 a[222:223], v227 offset:51200\n" \
"v_add_f32_dpp v222, v222, v222 row_ror:4 row_mask:0xf bank_mask:0xf\n" \
"v_mfma_f32_16x16x32_bf16 v[12:15], a[228:231], a[8:11], v[12:15]\n" \
"ds_read_b64_tr_b16 a[212:213], v227 offset:53248\n" \
"v_perm_b32 v200, v201, v200, s54\n" \
"v_mfma_f32_16x16x32_bf16 v[12:15], a[236:239], a[12:15], v[12:15]\n" \
"ds_read_b64_tr_b16 a[214:215], v227 offset:55296\n" \
"v_add_f32_dpp v222, v222, v222 row_ror:8 row_mask:0xf bank_mask:0xf\n" \
"v_mfma_f32_16x16x32_bf16 v[44:47], a[228:231], a[24:27], v[44:47]\n" \
"v_perm_b32 v201, v203, v202, s54\n" \
"v_mov_b32_e64 v223, v222\n" \
"v_mfma_f32_16x16x32_bf16 v[44:47], a[236:239], a[28:31], v[44:47]\n" \
"ds_write_b64 v218, v[200:201] offset:10240\n" \
"s_nop 0\n" \
"v_permlane16_swap_b32_e32 v222, v223\n" \
"v_mfma_f32_16x16x32_bf16 v[76:79], a[228:231], a[40:43], v[76:79]\n" \
"v_perm_b32 v204, v205, v204, s54\n" \
"v_mfma_f32_16x16x32_bf16 v[76:79], a[236:239], a[44:47], v[76:79]\n" \
"v_add_f32_e64 v222, v222, v223\n" \
"v_mfma_f32_16x16x32_bf16 v[108:111], a[228:231], a[56:59], v[108:111]\n" \
"ds_read_b64_tr_b16 a[208:209], v227 offset:57344\n" \
"v_perm_b32 v205, v207, v206, s54\n" \
"v_mfma_f32_16x16x32_bf16 v[108:111], a[236:239], a[60:63], v[108:111]\n" \
"s_waitcnt vmcnt(63) lgkmcnt(7)\n" \
"s_nop 0\n" \
"v_mfma_f32_16x16x32_bf16 v[4:7], a[232:235], a[128:131], v[4:7]\n" \
"ds_read_b64_tr_b16 a[210:211], v227 offset:59392\n" \
"v_mov_b32_e64 v223, v222\n" \
"v_mfma_f32_16x16x32_bf16 v[4:7], a[196:199], a[132:135], v[4:7]\n" \
"ds_read_b64_tr_b16 a[216:217], v227 offset:61440\n" \
"ds_write_b64 v218, v[204:205] offset:14336\n" \
"v_mfma_f32_16x16x32_bf16 v[36:39], a[232:235], a[144:147], v[36:39]\n" \
"ds_read_b64_tr_b16 a[218:219], v227 offset:63488\n" \
"s_nop 0\n" \
"v_permlane32_swap_b32_e32 v222, v223\n" \
"v_mfma_f32_16x16x32_bf16 v[36:39], a[196:199], a[148:151], v[36:39]\n" \
"v_lshl_add_u32 v219, %[wid], 4, v219\n" \
"v_add_f32_e64 v222, v222, v223\n" \
"v_mfma_f32_16x16x32_bf16 v[68:71], a[232:235], a[160:163], v[68:71]\n" \
"s_cmp_lg_u64 s[56:57], 0\n" \
"s_cselect_b32 s61, 1, 0\n" \
"v_mov_b32_e64 v192, s61\n" \
"v_mfma_f32_16x16x32_bf16 v[68:71], a[196:199], a[164:167], v[68:71]\n" \
"v_mov_b32_e64 v193, v192\n" \
"v_mfma_f32_16x16x32_bf16 v[100:103], a[232:235], a[176:179], v[100:103]\n" \
"ds_read_b64_tr_b16 a[224:225], v215 offset:16896\n" \
"ds_write_b32 v219, v222 offset:12424\n" \
"v_mfma_f32_16x16x32_bf16 v[100:103], a[196:199], a[180:183], v[100:103]\n" \
"ds_read_b64_tr_b16 a[226:227], v215 offset:18944\n" \
"ds_write_b64 v219, v[192:193] offset:12416\n" \
"v_mfma_f32_16x16x32_bf16 v[4:7], a[200:203], a[136:139], v[4:7]\n" \
"ds_read_b64_tr_b16 a[228:229], v215 offset:20992\n" \
"v_mfma_f32_16x16x32_bf16 v[4:7], a[204:207], a[140:143], v[4:7]\n" \
"ds_read_b64_tr_b16 a[230:231], v215 offset:23040\n" \
"v_mfma_f32_16x16x32_bf16 v[36:39], a[200:203], a[152:155], v[36:39]\n" \
"v_mfma_f32_16x16x32_bf16 v[36:39], a[204:207], a[156:159], v[36:39]\n" \
"v_mfma_f32_16x16x32_bf16 v[68:71], a[200:203], a[168:171], v[68:71]\n" \
"v_mfma_f32_16x16x32_bf16 v[68:71], a[204:207], a[172:175], v[68:71]\n" \
"v_mfma_f32_16x16x32_bf16 v[100:103], a[200:203], a[184:187], v[100:103]\n" \
"ds_read_b64_tr_b16 a[236:237], v215 offset:25088\n" \
"v_mfma_f32_16x16x32_bf16 v[100:103], a[204:207], a[188:191], v[100:103]\n" \
"s_waitcnt vmcnt(63) lgkmcnt(3)\n" \
"s_barrier\n" \
"v_mfma_f32_16x16x32_bf16 v[12:15], a[220:223], a[128:131], v[12:15]\n" \
"ds_read_b64_tr_b16 a[238:239], v215 offset:27136\n" \
"ds_read_b128 v[204:207], v210 offset:12416\n" \
"v_mfma_f32_16x16x32_bf16 v[12:15], a[212:215], a[132:135], v[12:15]\n" \
"ds_read_b64_tr_b16 a[232:233], v215 offset:29184\n" \
"v_lshrrev_b32_e64 v200, 3, v224\n" \
"v_mfma_f32_16x16x32_bf16 v[44:47], a[220:223], a[144:147], v[44:47]\n" \
"ds_read_b64_tr_b16 a[234:235], v215 offset:31232\n" \
"ds_read_b128 v[160:163], v208 offset:0\n" \
"v_mfma_f32_16x16x32_bf16 v[44:47], a[212:215], a[148:151], v[44:47]\n" \
"v_bfe_u32 v201, v224, 8, 3\n" \
"v_and_b32_e64 v200, 16, v200\n" \
"v_mfma_f32_16x16x32_bf16 v[76:79], a[220:223], a[160:163], v[76:79]\n" \
"ds_read_b128 v[164:167], v208 offset:2048\n" \
"v_lshlrev_b32_e64 v201, v201, 1\n" \
"v_mfma_f32_16x16x32_bf16 v[76:79], a[212:215], a[164:167], v[76:79]\n" \
"s_movk_i32 s61, 0x3f80\n" \
"s_nop 0\n" \
"v_lshlrev_b32_e64 v200, v200, s61\n" \
"v_readlane_b32 s68, %[thi], 18\n" \
"v_lshlrev_b32_e64 v226, 7, v226\n" \
"v_mfma_f32_16x16x32_bf16 v[108:111], a[220:223], a[176:179], v[108:111]\n" \
"ds_read_b64_tr_b16 a[196:197], v227 offset:16896\n" \
"ds_read_b128 v[168:171], v208 offset:128\n" \
"v_mfma_f32_16x16x32_bf16 v[108:111], a[212:215], a[180:183], v[108:111]\n" \
"ds_read_b64_tr_b16 a[198:199], v227 offset:18944\n" \
"v_bfe_i32 v192, v201, 0, 1\n" \
"v_mfma_f32_16x16x32_bf16 v[12:15], a[208:211], a[136:139], v[12:15]\n" \
"ds_read_b64_tr_b16 a[200:201], v227 offset:20992\n" \
"ds_read_b128 v[172:175], v208 offset:2176\n" \
"v_mfma_f32_16x16x32_bf16 v[12:15], a[216:219], a[140:143], v[12:15]\n" \
"ds_read_b64_tr_b16 a[202:203], v227 offset:23040\n" \
"v_and_b32_e64 v192, v200, v192\n" \
"v_mfma_f32_16x16x32_bf16 v[44:47], a[208:211], a[152:155], v[44:47]\n" \
"ds_read_b128 v[176:179], v209 offset:0\n" \
"v_bfe_i32 v193, v201, 1, 1\n" \
"v_mfma_f32_16x16x32_bf16 v[44:47], a[216:219], a[156:159], v[44:47]\n" \
"v_and_b32_e64 v193, v200, v193\n" \
"v_bfe_i32 v194, v201, 2, 1\n" \
"v_mfma_f32_16x16x32_bf16 v[76:79], a[208:211], a[168:171], v[76:79]\n" \
"ds_read_b128 v[180:183], v209 offset:2048\n" \
"v_and_b32_e64 v194, v200, v194\n" \
"v_mfma_f32_16x16x32_bf16 v[76:79], a[216:219], a[172:175], v[76:79]\n" \
"v_bfe_i32 v195, v201, 3, 1\n" \
"v_and_b32_e64 v195, v200, v195\n" \
"buffer_load_dword v216, v226, %[srdz], s68 offen offset:64 sc1\n" \
"v_mfma_f32_16x16x32_bf16 v[108:111], a[208:211], a[184:187], v[108:111]\n" \
"ds_read_b64_tr_b16 a[204:205], v227 offset:25088\n" \
"ds_read_b128 v[184:187], v209 offset:128\n" \
"v_lshrrev_b32_e64 v217, 3, v224\n" \
"s_add_u32 s51, %[sin], 4096\n" \
"v_mfma_f32_16x16x32_bf16 v[108:111], a[216:219], a[188:191], v[108:111]\n" \
"s_waitcnt vmcnt(63) lgkmcnt(12)\n" \
"s_nop 0\n" \
"v_mfma_f32_16x16x32_bf16 v[20:23], a[224:227], v[0:3], v[20:23]\n" \
"ds_read_b64_tr_b16 a[206:207], v227 offset:27136\n" \
"ds_read_b128 v[188:191], v209 offset:2176\n" \
"v_mfma_f32_16x16x32_bf16 v[20:23], a[228:231], v[8:11], v[20:23]\n" \
"ds_read_b64_tr_b16 a[220:221], v227 offset:29184\n" \
"v_bfe_i32 v196, v201, 4, 1\n" \
"v_mfma_f32_16x16x32_bf16 v[52:55], a[224:227], v[32:35], v[52:55]\n" \
"ds_read_b64_tr_b16 a[222:223], v227 offset:31232\n" \
"v_and_b32_e64 v196, v200, v196\n" \
"v_mfma_f32_16x16x32_bf16 v[52:55], a[228:231], v[40:43], v[52:55]\n" \
"v_bfe_i32 v197, v201, 5, 1\n" \
"v_and_b32_e64 v197, v200, v197\n" \
"v_mfma_f32_16x16x32_bf16 v[84:87], a[224:227], v[64:67], v[84:87]\n" \
"v_cvt_pk_bf16_f32 v4, v4, v5\n" \
"v_bfe_i32 v198, v201, 6, 1\n" \
"v_mfma_f32_16x16x32_bf16 v[84:87], a[228:231], v[72:75], v[84:87]\n" \
"v_cvt_pk_bf16_f32 v5, v6, v7\n" \
"v_and_b32_e64 v198, v200, v198\n" \
"v_mfma_f32_16x16x32_bf16 v[116:119], a[224:227], v[96:99], v[116:119]\n" \
"ds_read_b64_tr_b16 a[212:213], v215 offset:33280\n" \
"v_cvt_pk_bf16_f32 v6, v12, v13\n" \
"v_mfma_f32_16x16x32_bf16 v[116:119], a[228:231], v[104:107], v[116:119]\n" \
"ds_read_b64_tr_b16 a[214:215], v215 offset:35328\n" \
"v_cvt_pk_bf16_f32 v7, v14, v15\n" \
"v_mfma_f32_16x16x32_bf16 v[20:23], a[236:239], v[16:19], v[20:23]\n" \
"ds_read_b64_tr_b16 a[208:209], v215 offset:37376\n" \
"v_bfe_i32 v199, v201, 7, 1\n" \
"v_mfma_f32_16x16x32_bf16 v[20:23], a[232:235], v[24:27], v[20:23]\n" \
"ds_read_b64_tr_b16 a[210:211], v215 offset:39424\n" \
"v_and_b32_e64 v199, v200, v199\n" \
"v_mfma_f32_16x16x32_bf16 v[52:55], a[236:239], v[48:51], v[52:55]\n" \
"s_add_u32 s55, s46, 8192\n" \
"buffer_store_dwordx4 v[4:7], %[vxs], %[srdz], s55 offen offset:0 sc1\n" \
".if . - adaln_body != 57344\n" \
".error \"size model\"\n" \
".endif\n" \
"Lk13_%=:\n" \
"s_cbranch_execz adaln_pg0\n" \
"s_nop 0\n" \
"v_mfma_f32_16x16x32_bf16 v[52:55], a[232:235], v[56:59], v[52:55]\n" \
"v_cvt_pk_bf16_f32 v36, v36, v37\n" \
"v_cvt_pk_bf16_f32 v37, v38, v39\n" \
"v_mfma_f32_16x16x32_bf16 v[84:87], a[236:239], v[80:83], v[84:87]\n" \
"v_cvt_pk_bf16_f32 v38, v44, v45\n" \
"s_waitcnt lgkmcnt(14)\n" \
"s_nop 0\n" \
"v_add_f32_dpp v206, v206, v206 quad_perm:[1,0,3,2] row_mask:0xf bank_mask:0xf\n" \
"v_mfma_f32_16x16x32_bf16 v[84:87], a[232:235], v[88:91], v[84:87]\n" \
"v_cvt_pk_bf16_f32 v39, v46, v47\n" \
"v_mfma_f32_16x16x32_bf16 v[116:119], a[236:239], v[112:115], v[116:119]\n" \
"ds_read_b64_tr_b16 a[216:217], v215 offset:41472\n" \
"v_add_f32_dpp v206, v206, v206 quad_perm:[2,3,0,1] row_mask:0xf bank_mask:0xf\n" \
"v_mfma_f32_16x16x32_bf16 v[116:119], a[232:235], v[120:123], v[116:119]\n" \
"s_waitcnt vmcnt(63) lgkmcnt(5)\n" \
"s_nop 0\n" \
"v_mfma_f32_16x16x32_bf16 v[28:31], a[196:199], v[0:3], v[28:31]\n" \
"ds_read_b64_tr_b16 a[218:219], v215 offset:43520\n" \
"v_cvt_pk_bf16_f32 v68, v68, v69\n" \
"v_mfma_f32_16x16x32_bf16 v[28:31], a[200:203], v[8:11], v[28:31]\n" \
"ds_read_b64_tr_b16 a[224:225], v215 offset:45568\n" \
"s_add_u32 s55, s46, 312320\n" \
"buffer_store_dwordx4 v[36:39], %[vxs], %[srdz], s55 offen offset:0 sc1\n" \
"v_mfma_f32_16x16x32_bf16 v[60:63], a[196:199], v[32:35], v[60:63]\n" \
"ds_read_b64_tr_b16 a[226:227], v215 offset:47616\n" \
"v_cvt_pk_bf16_f32 v69, v70, v71\n" \
"v_mfma_f32_16x16x32_bf16 v[60:63], a[200:203], v[40:43], v[60:63]\n" \
"v_cvt_pk_bf16_f32 v70, v76, v77\n" \
"v_cvt_pk_bf16_f32 v71, v78, v79\n" \
"v_mfma_f32_16x16x32_bf16 v[92:95], a[196:199], v[64:67], v[92:95]\n" \
"s_add_u32 s55, s46, 616448\n" \
"buffer_store_dwordx4 v[68:71], %[vxs], %[srdz], s55 offen offset:0 sc1\n" \
"v_mfma_f32_16x16x32_bf16 v[92:95], a[200:203], v[72:75], v[92:95]\n" \
"v_mfma_f32_16x16x32_bf16 v[124:127], a[196:199], v[96:99], v[124:127]\n" \
"ds_read_b64_tr_b16 a[228:229], v227 offset:33280\n" \
"v_cvt_pk_bf16_f32 v100, v100, v101\n" \
"v_mfma_f32_16x16x32_bf16 v[124:127], a[200:203], v[104:107], v[124:127]\n" \
"ds_read_b64_tr_b16 a[230:231], v227 offset:35328\n" \
"v_cvt_pk_bf16_f32 v101, v102, v103\n" \
"v_mfma_f32_16x16x32_bf16 v[28:31], a[204:207], v[16:19], v[28:31]\n" \
"ds_read_b64_tr_b16 a[236:237], v227 offset:37376\n" \
"v_cvt_pk_bf16_f32 v102, v108, v109\n" \
"v_mfma_f32_16x16x32_bf16 v[28:31], a[220:223], v[24:27], v[28:31]\n" \
"ds_read_b64_tr_b16 a[238:239], v227 offset:39424\n" \
"v_cvt_pk_bf16_f32 v103, v110, v111\n" \
"v_mfma_f32_16x16x32_bf16 v[60:63], a[204:207], v[48:51], v[60:63]\n" \
"s_add_u32 s55, s46, 920576\n" \
"buffer_store_dwordx4 v[100:103], %[vxs], %[srdz], s55 offen offset:0 sc1\n" \
"v_mfma_f32_16x16x32_bf16 v[60:63], a[220:223], v[56:59], v[60:63]\n" \
"v_mfma_f32_16x16x32_bf16 v[92:95], a[204:207], v[80:83], v[92:95]\n" \
"v_mov_b32_e64 v230, 1\n" \
"buffer_store_dword v230, v225, %[srdz], s62 offen offset:68 sc1\n" \
"v_mfma_f32_16x16x32_bf16 v[92:95], a[220:223], v[88:91], v[92:95]\n" \
"ds_read_b128 v[4:7], v208 offset:4096\n" \
"v_mfma_f32_16x16x32_bf16 v[124:127], a[204:207], v[112:115], v[124:127]\n" \
"ds_read_b64_tr_b16 a[232:233], v227 offset:41472\n" \
"v_mfma_f32_16x16x32_bf16 v[124:127], a[220:223], v[120:123], v[124:127]\n" \
"s_waitcnt vmcnt(63) lgkmcnt(6)\n" \
"s_nop 0\n" \
"v_mfma_f32_16x16x32_bf16 v[20:23], a[212:215], a[0:3], v[20:23]\n" \
"ds_read_b64_tr_b16 a[234:235], v227 offset:43520\n" \
"ds_read_b128 v[12:15], v208 offset:6144\n" \
"v_mfma_f32_16x16x32_bf16 v[20:23], a[208:211], a[4:7], v[20:23]\n" \
"ds_read_b64_tr_b16 a[196:197], v227 offset:45568\n" \
"v_mfma_f32_16x16x32_bf16 v[52:55], a[212:215], a[16:19], v[52:55]\n" \
"ds_read_b64_tr_b16 a[198:199], v227 offset:47616\n" \
"v_mfma_f32_16x16x32_bf16 v[52:55], a[208:211], a[20:23], v[52:55]\n" \
"ds_read_b128 v[36:39], v208 offset:4224\n" \
"v_mfma_f32_16x16x32_bf16 v[84:87], a[212:215], a[32:35], v[84:87]\n" \
"v_mfma_f32_16x16x32_bf16 v[84:87], a[208:211], a[36:39], v[84:87]\n" \
"v_mfma_f32_16x16x32_bf16 v[116:119], a[212:215], a[48:51], v[116:119]\n" \
"ds_read_b64_tr_b16 a[200:201], v215 offset:49664\n" \
"ds_read_b128 v[44:47], v208 offset:6272\n" \
"v_mfma_f32_16x16x32_bf16 v[116:119], a[208:211], a[52:55], v[116:119]\n" \
"ds_read_b64_tr_b16 a[202:203], v215 offset:51712\n" \
"v_mfma_f32_16x16x32_bf16 v[20:23], a[216:219], a[8:11], v[20:23]\n" \
"ds_read_b64_tr_b16 a[204:205], v215 offset:53760\n" \
"v_mfma_f32_16x16x32_bf16 v[20:23], a[224:227], a[12:15], v[20:23]\n" \
"ds_read_b64_tr_b16 a[206:207], v215 offset:55808\n" \
"ds_read_b128 v[68:71], v209 offset:4096\n" \
"v_mfma_f32_16x16x32_bf16 v[52:55], a[216:219], a[24:27], v[52:55]\n" \
"ds_read_b64_tr_b16 a[220:221], v215 offset:57856\n" \
"v_mfma_f32_16x16x32_bf16 v[52:55], a[224:227], a[28:31], v[52:55]\n" \
"ds_read_b64_tr_b16 a[222:223], v215 offset:59904\n" \
"v_mfma_f32_16x16x32_bf16 v[84:87], a[216:219], a[40:43], v[84:87]\n" \
"ds_read_b64_tr_b16 a[212:213], v215 offset:61952\n" \
"ds_read_b128 v[76:79], v209 offset:6144\n" \
"v_mfma_f32_16x16x32_bf16 v[84:87], a[224:227], a[44:47], v[84:87]\n" \
"ds_read_b64_tr_b16 a[214:215], v215 offset:64000\n" \
"v_mfma_f32_16x16x32_bf16 v[116:119], a[216:219], a[56:59], v[116:119]\n" \
"ds_read_b64_tr_b16 a[208:209], v227 offset:49664\n" \
"v_mfma_f32_16x16x32_bf16 v[116:119], a[224:227], a[60:63], v[116:119]\n" \
"s_waitcnt vmcnt(63) lgkmcnt(13)\n" \
"s_nop 0\n" \
"v_mfma_f32_16x16x32_bf16 v[28:31], a[228:231], a[0:3], v[28:31]\n" \
"ds_read_b64_tr_b16 a[210:211], v227 offset:51712\n" \
"ds_read_b128 v[100:103], v209 offset:4224\n" \
"v_mfma_f32_16x16x32_bf16 v[28:31], a[236:239], a[4:7], v[28:31]\n" \
"ds_read_b64_tr_b16 a[216:217], v227 offset:53760\n" \
"v_mfma_f32_16x16x32_bf16 v[60:63], a[228:231], a[16:19], v[60:63]\n" \
"ds_read_b64_tr_b16 a[218:219], v227 offset:55808\n" \
"ds_read_b128 v[108:111], v209 offset:6272\n" \
"v_mfma_f32_16x16x32_bf16 v[60:63], a[236:239], a[20:23], v[60:63]\n" \
"ds_read_b64_tr_b16 a[224:225], v227 offset:57856\n" \
"v_mfma_f32_16x16x32_bf16 v[92:95], a[228:231], a[32:35], v[92:95]\n" \
"ds_read_b64_tr_b16 a[226:227], v227 offset:59904\n" \
"v_mfma_f32_16x16x32_bf16 v[92:95], a[236:239], a[36:39], v[92:95]\n" \
"v_mfma_f32_16x16x32_bf16 v[124:127], a[228:231], a[48:51], v[124:127]\n" \
"v_mfma_f32_16x16x32_bf16 v[124:127], a[236:239], a[52:55], v[124:127]\n" \
"ds_read_b64_tr_b16 a[228:229], v227 offset:61952\n" \
"v_mfma_f32_16x16x32_bf16 v[28:31], a[232:235], a[8:11], v[28:31]\n" \
"ds_read_b64_tr_b16 a[230:231], v227 offset:64000\n" \
"s_waitcnt vmcnt(5)\n" \
"v_cmp_eq_u32_e32 vcc, 0, v216\n" \
"s_nop 0\n" \
"s_cmp_eq_u64 vcc, 0\n" \
"s_cselect_b32 %[got], 1, 0\n" \
"s_nop 0\n" \
"s_cselect_b32 s54, %[sin], 0x80000000\n" \
"s_cselect_b32 s51, s51, 0x80000000\n" \
"v_mfma_f32_16x16x32_bf16 v[28:31], a[196:199], a[12:15], v[28:31]\n" \
"v_mfma_f32_16x16x32_bf16 v[60:63], a[232:235], a[24:27], v[60:63]\n" \
"buffer_load_dwordx4 a[64:67], v217, %[srdz], s54 offen offset:0 sc1\n" \
"v_mfma_f32_16x16x32_bf16 v[60:63], a[196:199], a[28:31], v[60:63]\n" \
"v_mfma_f32_16x16x32_bf16 v[92:95], a[232:235], a[40:43], v[92:95]\n" \
"buffer_load_dwordx4 a[68:71], v217, %[srdz], s54 offen offset:1024 sc1\n" \
"v_mfma_f32_16x16x32_bf16 v[92:95], a[196:199], a[44:47], v[92:95]\n" \
"v_mfma_f32_16x16x32_bf16 v[124:127], a[232:235], a[56:59], v[124:127]\n" \
"buffer_load_dwordx4 a[72:75], v217, %[srdz], s54 offen offset:2048 sc1\n" \
"v_mfma_f32_16x16x32_bf16 v[124:127], a[196:199], a[60:63], v[124:127]\n" \
"s_waitcnt vmcnt(63) lgkmcnt(0)\n" \
"s_nop 0\n" \
"v_mfma_f32_16x16x32_bf16 v[20:23], a[200:203], a[128:131], v[20:23]\n" \
"buffer_load_dwordx4 a[76:79], v217, %[srdz], s54 offen offset:3072 sc1\n" \
"v_mfma_f32_16x16x32_bf16 v[20:23], a[204:207], a[132:135], v[20:23]\n" \
"v_mfma_f32_16x16x32_bf16 v[20:23], a[220:223], a[136:139], v[20:23]\n" \
"buffer_load_dwordx4 a[80:83], v217, %[srdz], s51 offen offset:0 sc1\n" \
"v_mfma_f32_16x16x32_bf16 v[20:23], a[212:215], a[140:143], v[20:23]\n" \
"v_mfma_f32_16x16x32_bf16 v[28:31], a[208:211], a[128:131], v[28:31]\n" \
"buffer_load_dwordx4 a[84:87], v217, %[srdz], s51 offen offset:1024 sc1\n" \
"v_mfma_f32_16x16x32_bf16 v[28:31], a[216:219], a[132:135], v[28:31]\n" \
"v_mfma_f32_16x16x32_bf16 v[28:31], a[224:227], a[136:139], v[28:31]\n" \
"buffer_load_dwordx4 a[88:91], v217, %[srdz], s51 offen offset:2048 sc1\n" \
"v_mfma_f32_16x16x32_bf16 v[28:31], a[228:231], a[140:143], v[28:31]\n" \
"v_mfma_f32_16x16x32_bf16 v[52:55], a[200:203], a[144:147], v[52:55]\n" \
"buffer_load_dwordx4 a[92:95], v217, %[srdz], s51 offen offset:3072 sc1\n" \
"v_mfma_f32_16x16x32_bf16 v[52:55], a[204:207], a[148:151], v[52:55]\n" \
"v_mfma_f32_16x16x32_bf16 v[52:55], a[220:223], a[152:155], v[52:55]\n" \
"v_mfma_f32_16x16x32_bf16 v[52:55], a[212:215], a[156:159], v[52:55]\n" \
"v_cvt_pk_bf16_f32 v20, v20, v21\n" \
"v_cvt_pk_bf16_f32 v21, v22, v23\n" \
"v_mfma_f32_16x16x32_bf16 v[60:63], a[208:211], a[144:147], v[60:63]\n" \
"buffer_load_dword v229, v226, %[srdz], s68 offen offset:68 sc1\n" \
"v_mfma_f32_16x16x32_bf16 v[60:63], a[216:219], a[148:151], v[60:63]\n" \
"v_cvt_pk_bf16_f32 v22, v28, v29\n" \
"v_cvt_pk_bf16_f32 v23, v30, v31\n" \
"v_mfma_f32_16x16x32_bf16 v[60:63], a[224:227], a[152:155], v[60:63]\n" \
"s_add_u32 s55, s46, 12288\n" \
"buffer_store_dwordx4 v[20:23], %[vxs], %[srdz], s55 offen offset:0 sc1\n" \
"v_mfma_f32_16x16x32_bf16 v[60:63], a[228:231], a[156:159], v[60:63]\n" \
"v_mfma_f32_16x16x32_bf16 v[84:87], a[200:203], a[160:163], v[84:87]\n" \
"v_mfma_f32_16x16x32_bf16 v[84:87], a[204:207], a[164:167], v[84:87]\n" \
"v_mfma_f32_16x16x32_bf16 v[84:87], a[220:223], a[168:171], v[84:87]\n" \
"v_mfma_f32_16x16x32_bf16 v[84:87], a[212:215], a[172:175], v[84:87]\n" \
"v_cvt_pk_bf16_f32 v52, v52, v53\n" \
"v_cvt_pk_bf16_f32 v53, v54, v55\n" \
"v_mfma_f32_16x16x32_bf16 v[92:95], a[208:211], a[160:163], v[92:95]\n" \
"v_mfma_f32_16x16x32_bf16 v[92:95], a[216:219], a[164:167], v[92:95]\n" \
"v_cvt_pk_bf16_f32 v54, v60, v61\n" \
"v_cvt_pk_bf16_f32 v55, v62, v63\n" \
"v_mfma_f32_16x16x32_bf16 v[92:95], a[224:227], a[168:171], v[92:95]\n" \
"s_add_u32 s55, s46, 316416\n" \
"buffer_store_dwordx4 v[52:55], %[vxs], %[srdz], s55 offen offset:0 sc1\n" \
"v_mfma_f32_16x16x32_bf16 v[92:95], a[228:231], a[172:175], v[92:95]\n" \
"v_mfma_f32_16x16x32_bf16 v[116:119], a[200:203], a[176:179], v[116:119]\n" \
"v_mfma_f32_16x16x32_bf16 v[116:119], a[204:207], a[180:183], v[116:119]\n" \
"v_mfma_f32_16x16x32_bf16 v[116:119], a[220:223], a[184:187], v[116:119]\n" \
"v_mfma_f32_16x16x32_bf16 v[116:119], a[212:215], a[188:191], v[116:119]\n" \
"v_cvt_pk_bf16_f32 v84, v84, v85\n" \
"v_cvt_pk_bf16_f32 v85, v86, v87\n" \
"v_mfma_f32_16x16x32_bf16 v[124:127], a[208:211], a[176:179], v[124:127]\n" \
"v_mfma_f32_16x16x32_bf16 v[124:127], a[216:219], a[180:183], v[124:127]\n" \
"v_cvt_pk_bf16_f32 v86, v92, v93\n" \
"v_cvt_pk_bf16_f32 v87, v94, v95\n" \
"v_mfma_f32_16x16x32_bf16 v[124:127], a[224:227], a[184:187], v[124:127]\n" \
"s_add_u32 s55, s46, 620544\n" \
"buffer_store_dwordx4 v[84:87], %[vxs], %[srdz], s55 offen offset:0 sc1\n" \
"v_mfma_f32_16x16x32_bf16 v[124:127], a[228:231], a[188:191], v[124:127]\n" \
"v_cvt_pk_bf16_f32 v116, v116, v117\n" \
"v_cvt_pk_bf16_f32 v117, v118, v119\n" \
"s_waitcnt lgkmcnt(0)\n" \
"s_nop 4\n" \
"v_cvt_pk_bf16_f32 v118, v124, v125\n" \
"v_cvt_pk_bf16_f32 v119, v126, v127\n" \
"s_add_u32 s55, s46, 924672\n" \
"buffer_store_dwordx4 v[116:119], %[vxs], %[srdz], s55 offen offset:0 sc1\n" \
"s_branch Lend_%=\n" \
"s_nop 0\n" \
"Lsp1_%=:\n" \
"s_movk_i32 s51, 0x1000\n" \
"s_cmp_gt_u32 s48, 0\n" \
"s_cselect_b32 s61, 0, 1\n" \
"s_nop 0\n" \
"s_mul_i32 s61, s61, 152064\n" \
"Lsq1_%=:\n" \
"buffer_load_dwordx4 a[64:67], %[oq0], %[srdz], s61 offen offset:0 sc1\n" \
"buffer_load_dwordx4 a[68:71], %[oq0], %[srdz], s61 offen offset:1024 sc1\n" \
"buffer_load_dwordx4 a[72:75], %[oq0], %[srdz], s61 offen offset:2048 sc1\n" \
"buffer_load_dwordx4 a[76:79], %[oq0], %[srdz], s61 offen offset:3072 sc1\n" \
"buffer_load_dwordx4 a[80:83], v208, %[srdz], s61 offen offset:0 sc1\n" \
"buffer_load_dwordx4 a[84:87], v208, %[srdz], s61 offen offset:1024 sc1\n" \
"buffer_load_dwordx4 a[88:91], v208, %[srdz], s61 offen offset:2048 sc1\n" \
"buffer_load_dwordx4 a[92:95], v208, %[srdz], s61 offen offset:3072 sc1\n" \
"buffer_load_dwordx4 a[96:99], v209, %[srdz], s61 offen offset:0 sc1\n" \
"buffer_load_dwordx4 a[100:103], v209, %[srdz], s61 offen offset:1024 sc1\n" \
"buffer_load_dwordx4 a[104:107], v209, %[srdz], s61 offen offset:2048 sc1\n" \
"buffer_load_dwordx4 a[108:111], v209, %[srdz], s61 offen offset:3072 sc1\n" \
"buffer_load_dwordx4 a[112:115], v210, %[srdz], s61 offen offset:0 sc1\n" \
"buffer_load_dwordx4 a[116:119], v210, %[srdz], s61 offen offset:1024 sc1\n" \
"buffer_load_dwordx4 a[120:123], v210, %[srdz], s61 offen offset:2048 sc1\n" \
"buffer_load_dwordx4 a[124:127], v210, %[srdz], s61 offen offset:3072 sc1\n" \
"s_waitcnt vmcnt(0)\n" \
"s_nop 0\n" \
"v_mfma_f32_16x16x32_bf16 v[128:131], a[236:239], a[64:67], v[192:195]\n" \
"v_mfma_f32_16x16x32_bf16 v[128:131], a[196:199], a[68:71], v[128:131]\n" \
"v_mfma_f32_16x16x32_bf16 v[128:131], a[200:203], a[72:75], v[128:131]\n" \
"v_mfma_f32_16x16x32_bf16 v[128:131], a[204:207], a[76:79], v[128:131]\n" \
"v_mfma_f32_16x16x32_bf16 v[132:135], a[236:239], a[80:83], v[196:199]\n" \
"v_mfma_f32_16x16x32_bf16 v[132:135], a[196:199], a[84:87], v[132:135]\n" \
"v_mfma_f32_16x16x32_bf16 v[132:135], a[200:203], a[88:91], v[132:135]\n" \
"v_mfma_f32_16x16x32_bf16 v[132:135], a[204:207], a[92:95], v[132:135]\n" \
"v_mfma_f32_16x16x32_bf16 v[136:139], a[236:239], a[96:99], v[200:203]\n" \
"v_mfma_f32_16x16x32_bf16 v[136:139], a[196:199], a[100:103], v[136:139]\n" \
"v_mfma_f32_16x16x32_bf16 v[136:139], a[200:203], a[104:107], v[136:139]\n" \
"v_mfma_f32_16x16x32_bf16 v[136:139], a[204:207], a[108:111], v[136:139]\n" \
"v_mfma_f32_16x16x32_bf16 v[140:143], a[236:239], a[112:115], v[204:207]\n" \
"v_mfma_f32_16x16x32_bf16 v[140:143], a[196:199], a[116:119], v[140:143]\n" \
"v_mfma_f32_16x16x32_bf16 v[140:143], a[200:203], a[120:123], v[140:143]\n" \
"v_mfma_f32_16x16x32_bf16 v[140:143], a[204:207], a[124:127], v[140:143]\n" \
"s_nop 7\n" \
"s_nop 0\n" \
"v_cmp_u_f32_e64 s[44:45], v128, v132\n" \
"v_cmp_u_f32_e64 s[56:57], v136, v140\n" \
"s_or_b64 s[56:57], s[56:57], s[44:45]\n" \
"s_cbranch_scc0 Lbk1_%=\n" \
"s_sub_u32 s51, s51, 1\n" \
"s_cbranch_scc0 Lsq1_%=\n" \
"s_branch Lbk1_%=\n" \
"Lsp2_%=:\n" \
"s_movk_i32 s51, 0x1000\n" \
"s_cmp_gt_u32 s48, 1\n" \
"s_cselect_b32 s61, 1, 2\n" \
"s_mul_i32 s61, s61, 152064\n" \
"Lsq2_%=:\n" \
"buffer_load_dwordx4 a[0:3], %[oq0], %[srdz], s61 offen offset:0 sc1\n" \
"buffer_load_dwordx4 a[4:7], %[oq0], %[srdz], s61 offen offset:1024 sc1\n" \
"buffer_load_dwordx4 a[8:11], %[oq0], %[srdz], s61 offen offset:2048 sc1\n" \
"buffer_load_dwordx4 a[12:15], %[oq0], %[srdz], s61 offen offset:3072 sc1\n" \
"buffer_load_dwordx4 a[16:19], v208, %[srdz], s61 offen offset:0 sc1\n" \
"buffer_load_dwordx4 a[20:23], v208, %[srdz], s61 offen offset:1024 sc1\n" \
"buffer_load_dwordx4 a[24:27], v208, %[srdz], s61 offen offset:2048 sc1\n" \
"buffer_load_dwordx4 a[28:31], v208, %[srdz], s61 offen offset:3072 sc1\n" \
"buffer_load_dwordx4 a[32:35], v209, %[srdz], s61 offen offset:0 sc1\n" \
"buffer_load_dwordx4 a[36:39], v209, %[srdz], s61 offen offset:1024 sc1\n" \
"buffer_load_dwordx4 a[40:43], v209, %[srdz], s61 offen offset:2048 sc1\n" \
"buffer_load_dwordx4 a[44:47], v209, %[srdz], s61 offen offset:3072 sc1\n" \
"buffer_load_dwordx4 a[48:51], v210, %[srdz], s61 offen offset:0 sc1\n" \
"buffer_load_dwordx4 a[52:55], v210, %[srdz], s61 offen offset:1024 sc1\n" \
"buffer_load_dwordx4 a[56:59], v210, %[srdz], s61 offen offset:2048 sc1\n" \
"buffer_load_dwordx4 a[60:63], v210, %[srdz], s61 offen offset:3072 sc1\n" \
"s_waitcnt vmcnt(0)\n" \
"s_nop 0\n" \
"v_mfma_f32_16x16x32_bf16 v[192:195], a[196:199], a[0:3], v[128:131]\n" \
"v_mfma_f32_16x16x32_bf16 v[192:195], a[200:203], a[4:7], v[192:195]\n" \
"v_mfma_f32_16x16x32_bf16 v[192:195], a[204:207], a[8:11], v[192:195]\n" \
"v_mfma_f32_16x16x32_bf16 v[192:195], a[232:235], a[12:15], v[192:195]\n" \
"v_mfma_f32_16x16x32_bf16 v[196:199], a[196:199], a[16:19], v[132:135]\n" \
"v_mfma_f32_16x16x32_bf16 v[196:199], a[200:203], a[20:23], v[196:199]\n" \
"v_mfma_f32_16x16x32_bf16 v[196:199], a[204:207], a[24:27], v[196:199]\n" \
"v_mfma_f32_16x16x32_bf16 v[196:199], a[232:235], a[28:31], v[196:199]\n" \
"v_mfma_f32_16x16x32_bf16 v[200:203], a[196:199], a[32:35], v[136:139]\n" \
"v_mfma_f32_16x16x32_bf16 v[200:203], a[200:203], a[36:39], v[200:203]\n" \
"v_mfma_f32_16x16x32_bf16 v[200:203], a[204:207], a[40:43], v[200:203]\n" \
"v_mfma_f32_16x16x32_bf16 v[200:203], a[232:235], a[44:47], v[200:203]\n" \
"v_mfma_f32_16x16x32_bf16 v[204:207], a[196:199], a[48:51], v[140:143]\n" \
"v_mfma_f32_16x16x32_bf16 v[204:207], a[200:203], a[52:55], v[204:207]\n" \
"v_mfma_f32_16x16x32_bf16 v[204:207], a[204:207], a[56:59], v[204:207]\n" \
"v_mfma_f32_16x16x32_bf16 v[204:207], a[232:235], a[60:63], v[204:207]\n" \
"s_nop 7\n" \
"s_nop 0\n" \
"v_cmp_u_f32_e64 s[44:45], v192, v196\n" \
"v_cmp_u_f32_e64 s[56:57], v200, v204\n" \
"s_or_b64 s[56:57], s[56:57], s[44:45]\n" \
"s_cbranch_scc0 Lbk2_%=\n" \
"s_sub_u32 s51, s51, 1\n" \
"s_cbranch_scc0 Lsq2_%=\n" \
"s_branch Lbk2_%=\n" \
"Lsp3_%=:\n" \
"s_movk_i32 s51, 0x1000\n" \
"s_cmp_gt_u32 s48, 2\n" \
"s_cselect_b32 s61, 2, 3\n" \
"s_mul_i32 s61, s61, 152064\n" \
"Lsq3_%=:\n" \
"buffer_load_dwordx4 a[128:131], %[oq0], %[srdz], s61 offen offset:0 sc1\n" \
"buffer_load_dwordx4 a[132:135], %[oq0], %[srdz], s61 offen offset:1024 sc1\n" \
"buffer_load_dwordx4 a[136:139], %[oq0], %[srdz], s61 offen offset:2048 sc1\n" \
"buffer_load_dwordx4 a[140:143], %[oq0], %[srdz], s61 offen offset:3072 sc1\n" \
"buffer_load_dwordx4 a[144:147], v208, %[srdz], s61 offen offset:0 sc1\n" \
"buffer_load_dwordx4 a[148:151], v208, %[srdz], s61 offen offset:1024 sc1\n" \
"buffer_load_dwordx4 a[152:155], v208, %[srdz], s61 offen offset:2048 sc1\n" \
"buffer_load_dwordx4 a[156:159], v208, %[srdz], s61 offen offset:3072 sc1\n" \
"buffer_load_dwordx4 a[160:163], v209, %[srdz], s61 offen offset:0 sc1\n" \
"buffer_load_dwordx4 a[164:167], v209, %[srdz], s61 offen offset:1024 sc1\n" \
"buffer_load_dwordx4 a[168:171], v209, %[srdz], s61 offen offset:2048 sc1\n" \
"buffer_load_dwordx4 a[172:175], v209, %[srdz], s61 offen offset:3072 sc1\n" \
"buffer_load_dwordx4 a[176:179], v210, %[srdz], s61 offen offset:0 sc1\n" \
"buffer_load_dwordx4 a[180:183], v210, %[srdz], s61 offen offset:1024 sc1\n" \
"buffer_load_dwordx4 a[184:187], v210, %[srdz], s61 offen offset:2048 sc1\n" \
"buffer_load_dwordx4 a[188:191], v210, %[srdz], s61 offen offset:3072 sc1\n" \
"s_waitcnt vmcnt(0)\n" \
"s_nop 0\n" \
"v_mfma_f32_16x16x32_bf16 v[128:131], a[196:199], a[128:131], v[192:195]\n" \
"v_mfma_f32_16x16x32_bf16 v[128:131], a[200:203], a[132:135], v[128:131]\n" \
"v_mfma_f32_16x16x32_bf16 v[128:131], a[204:207], a[136:139], v[128:131]\n" \
"v_mfma_f32_16x16x32_bf16 v[128:131], a[224:227], a[140:143], v[128:131]\n" \
"v_mfma_f32_16x16x32_bf16 v[132:135], a[196:199], a[144:147], v[196:199]\n" \
"v_mfma_f32_16x16x32_bf16 v[132:135], a[200:203], a[148:151], v[132:135]\n" \
"v_mfma_f32_16x16x32_bf16 v[132:135], a[204:207], a[152:155], v[132:135]\n" \
"v_mfma_f32_16x16x32_bf16 v[132:135], a[224:227], a[156:159], v[132:135]\n" \
"v_mfma_f32_16x16x32_bf16 v[136:139], a[196:199], a[160:163], v[200:203]\n" \
"v_mfma_f32_16x16x32_bf16 v[136:139], a[200:203], a[164:167], v[136:139]\n" \
"v_mfma_f32_16x16x32_bf16 v[136:139], a[204:207], a[168:171], v[136:139]\n" \
"v_mfma_f32_16x16x32_bf16 v[136:139], a[224:227], a[172:175], v[136:139]\n" \
"v_mfma_f32_16x16x32_bf16 v[140:143], a[196:199], a[176:179], v[204:207]\n" \
"v_mfma_f32_16x16x32_bf16 v[140:143], a[200:203], a[180:183], v[140:143]\n" \
"v_mfma_f32_16x16x32_bf16 v[140:143], a[204:207], a[184:187], v[140:143]\n" \
"v_mfma_f32_16x16x32_bf16 v[140:143], a[224:227], a[188:191], v[140:143]\n" \
"s_nop 7\n" \
"s_nop 0\n" \
"v_cmp_u_f32_e64 s[44:45], v128, v132\n" \
"v_cmp_u_f32_e64 s[56:57], v136, v140\n" \
"s_or_b64 s[56:57], s[56:57], s[44:45]\n" \
"s_cbranch_scc0 Lbk3_%=\n" \
"s_sub_u32 s51, s51, 1\n" \
"s_cbranch_scc0 Lsq3_%=\n" \
"s_branch Lbk3_%=\n" \
"Lsp4_%=:\n" \
"s_movk_i32 s51, 0x1000\n" \
"s_cmp_gt_u32 s48, 3\n" \
"s_cselect_b32 s61, 3, 4\n" \
"s_mul_i32 s61, s61, 152064\n" \
"Lsq4_%=:\n" \
"buffer_load_dwordx4 a[64:67], %[oq0], %[srdz], s61 offen offset:0 sc1\n" \
"buffer_load_dwordx4 a[68:71], %[oq0], %[srdz], s61 offen offset:1024 sc1\n" \
"buffer_load_dwordx4 a[72:75], %[oq0], %[srdz], s61 offen offset:2048 sc1\n" \
"buffer_load_dwordx4 a[76:79], %[oq0], %[srdz], s61 offen offset:3072 sc1\n" \
"buffer_load_dwordx4 a[80:83], v208, %[srdz], s61 offen offset:0 sc1\n" \
"buffer_load_dwordx4 a[84:87], v208, %[srdz], s61 offen offset:1024 sc1\n" \
"buffer_load_dwordx4 a[88:91], v208, %[srdz], s61 offen offset:2048 sc1\n" \
"buffer_load_dwordx4 a[92:95], v208, %[srdz], s61 offen offset:3072 sc1\n" \
"buffer_load_dwordx4 a[96:99], v209, %[srdz], s61 offen offset:0 sc1\n" \
"buffer_load_dwordx4 a[100:103], v209, %[srdz], s61 offen offset:1024 sc1\n" \
"buffer_load_dwordx4 a[104:107], v209, %[srdz], s61 offen offset:2048 sc1\n" \
"buffer_load_dwordx4 a[108:111], v209, %[srdz], s61 offen offset:3072 sc1\n" \
"buffer_load_dwordx4 a[112:115], v210, %[srdz], s61 offen offset:0 sc1\n" \
"buffer_load_dwordx4 a[116:119], v210, %[srdz], s61 offen offset:1024 sc1\n" \
"buffer_load_dwordx4 a[120:123], v210, %[srdz], s61 offen offset:2048 sc1\n" \
"buffer_load_dwordx4 a[124:127], v210, %[srdz], s61 offen offset:3072 sc1\n" \
"s_waitcnt vmcnt(0)\n" \
"s_nop 0\n" \
"v_mfma_f32_16x16x32_bf16 v[192:195], a[196:199], a[64:67], v[128:131]\n" \
"v_mfma_f32_16x16x32_bf16 v[192:195], a[200:203], a[68:71], v[192:195]\n" \
"v_mfma_f32_16x16x32_bf16 v[192:195], a[204:207], a[72:75], v[192:195]\n" \
"v_mfma_f32_16x16x32_bf16 v[192:195], a[220:223], a[76:79], v[192:195]\n" \
"v_mfma_f32_16x16x32_bf16 v[196:199], a[196:199], a[80:83], v[132:135]\n" \
"v_mfma_f32_16x16x32_bf16 v[196:199], a[200:203], a[84:87], v[196:199]\n" \
"v_mfma_f32_16x16x32_bf16 v[196:199], a[204:207], a[88:91], v[196:199]\n" \
"v_mfma_f32_16x16x32_bf16 v[196:199], a[220:223], a[92:95], v[196:199]\n" \
"v_mfma_f32_16x16x32_bf16 v[200:203], a[196:199], a[96:99], v[136:139]\n" \
"v_mfma_f32_16x16x32_bf16 v[200:203], a[200:203], a[100:103], v[200:203]\n" \
"v_mfma_f32_16x16x32_bf16 v[200:203], a[204:207], a[104:107], v[200:203]\n" \
"v_mfma_f32_16x16x32_bf16 v[200:203], a[220:223], a[108:111], v[200:203]\n" \
"v_mfma_f32_16x16x32_bf16 v[204:207], a[196:199], a[112:115], v[140:143]\n" \
"v_mfma_f32_16x16x32_bf16 v[204:207], a[200:203], a[116:119], v[204:207]\n" \
"v_mfma_f32_16x16x32_bf16 v[204:207], a[204:207], a[120:123], v[204:207]\n" \
"v_mfma_f32_16x16x32_bf16 v[204:207], a[220:223], a[124:127], v[204:207]\n" \
"s_nop 7\n" \
"s_nop 0\n" \
"v_cmp_u_f32_e64 s[44:45], v192, v196\n" \
"v_cmp_u_f32_e64 s[56:57], v200, v204\n" \
"s_or_b64 s[56:57], s[56:57], s[44:45]\n" \
"s_cbranch_scc0 Lbk4_%=\n" \
"s_sub_u32 s51, s51, 1\n" \
"s_cbranch_scc0 Lsq4_%=\n" \
"s_branch Lbk4_%=\n" \
"Lsp5_%=:\n" \
"s_movk_i32 s51, 0x1000\n" \
"s_cmp_gt_u32 s48, 4\n" \
"s_cselect_b32 s61, 4, 5\n" \
"s_mul_i32 s61, s61, 152064\n" \
"Lsq5_%=:\n" \
"buffer_load_dwordx4 v[0:3], %[oq0], %[srdz], s61 offen offset:0 sc1\n" \
"buffer_load_dwordx4 v[8:11], %[oq0], %[srdz], s61 offen offset:1024 sc1\n" \
"buffer_load_dwordx4 v[16:19], %[oq0], %[srdz], s61 offen offset:2048 sc1\n" \
"buffer_load_dwordx4 v[24:27], %[oq0], %[srdz], s61 offen offset:3072 sc1\n" \
"buffer_load_dwordx4 v[32:35], v208, %[srdz], s61 offen offset:0 sc1\n" \
"buffer_load_dwordx4 v[40:43], v208, %[srdz], s61 offen offset:1024 sc1\n" \
"buffer_load_dwordx4 v[48:51], v208, %[srdz], s61 offen offset:2048 sc1\n" \
"buffer_load_dwordx4 v[56:59], v208, %[srdz], s61 offen offset:3072 sc1\n" \
"buffer_load_dwordx4 v[64:67], v209, %[srdz], s61 offen offset:0 sc1\n" \
"buffer_load_dwordx4 v[72:75], v209, %[srdz], s61 offen offset:1024 sc1\n" \
"buffer_load_dwordx4 v[80:83], v209, %[srdz], s61 offen offset:2048 sc1\n" \
"buffer_load_dwordx4 v[88:91], v209, %[srdz], s61 offen offset:3072 sc1\n" \
"buffer_load_dwordx4 v[96:99], v210, %[srdz], s61 offen offset:0 sc1\n" \
"buffer_load_dwordx4 v[104:107], v210, %[srdz], s61 offen offset:1024 sc1\n" \
"buffer_load_dwordx4 v[112:115], v210, %[srdz], s61 offen offset:2048 sc1\n" \
"buffer_load_dwordx4 v[120:123], v210, %[srdz], s61 offen offset:3072 sc1\n" \
"s_waitcnt vmcnt(0)\n" \
"s_nop 0\n" \
"v_mfma_f32_16x16x32_bf16 v[128:131], a[232:235], v[0:3], v[192:195]\n" \
"v_mfma_f32_16x16x32_bf16 v[128:131], a[236:239], v[8:11], v[128:131]\n" \
"v_mfma_f32_16x16x32_bf16 v[128:131], a[208:211], v[16:19], v[128:131]\n" \
"v_mfma_f32_16x16x32_bf16 v[128:131], a[228:231], v[24:27], v[128:131]\n" \
"v_mfma_f32_16x16x32_bf16 v[132:135], a[232:235], v[32:35], v[196:199]\n" \
"v_mfma_f32_16x16x32_bf16 v[132:135], a[236:239], v[40:43], v[132:135]\n" \
"v_mfma_f32_16x16x32_bf16 v[132:135], a[208:211], v[48:51], v[132:135]\n" \
"v_mfma_f32_16x16x32_bf16 v[132:135], a[228:231], v[56:59], v[132:135]\n" \
"v_mfma_f32_16x16x32_bf16 v[136:139], a[232:235], v[64:67], v[200:203]\n" \
"v_mfma_f32_16x16x32_bf16 v[136:139], a[236:239], v[72:75], v[136:139]\n" \
"v_mfma_f32_16x16x32_bf16 v[136:139], a[208:211], v[80:83], v[136:139]\n" \
"v_mfma_f32_16x16x32_bf16 v[136:139], a[228:231], v[88:91], v[136:139]\n" \
"v_mfma_f32_16x16x32_bf16 v[140:143], a[232:235], v[96:99], v[204:207]\n" \
"v_mfma_f32_16x16x32_bf16 v[140:143], a[236:239], v[104:107], v[140:143]\n" \
"v_mfma_f32_16x16x32_bf16 v[140:143], a[208:211], v[112:115], v[140:143]\n" \
"v_mfma_f32_16x16x32_bf16 v[140:143], a[228:231], v[120:123], v[140:143]\n" \
"s_nop 7\n" \
"s_nop 0\n" \
"v_cmp_u_f32_e64 s[44:45], v128, v132\n" \
"v_cmp_u_f32_e64 s[56:57], v136, v140\n" \
"s_or_b64 s[56:57], s[56:57], s[44:45]\n" \
"s_cbranch_scc0 Lbk5_%=\n" \
"s_sub_u32 s51, s51, 1\n" \
"s_cbranch_scc0 Lsq5_%=\n" \
"s_branch Lbk5_%=\n" \
"Lsp6_%=:\n" \
"s_movk_i32 s51, 0x1000\n" \
"s_cmp_gt_u32 s48, 5\n" \
"s_cselect_b32 s61, 5, 6\n" \
"s_mul_i32 s61, s61, 152064\n" \
"Lsq6_%=:\n" \
"buffer_load_dwordx4 a[0:3], %[oq0], %[srdz], s61 offen offset:0 sc1\n" \
"buffer_load_dwordx4 a[4:7], %[oq0], %[srdz], s61 offen offset:1024 sc1\n" \
"buffer_load_dwordx4 a[8:11], %[oq0], %[srdz], s61 offen offset:2048 sc1\n" \
"buffer_load_dwordx4 a[12:15], %[oq0], %[srdz], s61 offen offset:3072 sc1\n" \
"buffer_load_dwordx4 a[16:19], v208, %[srdz], s61 offen offset:0 sc1\n" \
"buffer_load_dwordx4 a[20:23], v208, %[srdz], s61 offen offset:1024 sc1\n" \
"buffer_load_dwordx4 a[24:27], v208, %[srdz], s61 offen offset:2048 sc1\n" \
"buffer_load_dwordx4 a[28:31], v208, %[srdz], s61 offen offset:3072 sc1\n" \
"buffer_load_dwordx4 a[32:35], v209, %[srdz], s61 offen offset:0 sc1\n" \
"buffer_load_dwordx4 a[36:39], v209, %[srdz], s61 offen offset:1024 sc1\n" \
"buffer_load_dwordx4 a[40:43], v209, %[srdz], s61 offen offset:2048 sc1\n" \
"buffer_load_dwordx4 a[44:47], v209, %[srdz], s61 offen offset:3072 sc1\n" \
"buffer_load_dwordx4 a[48:51], v210, %[srdz], s61 offen offset:0 sc1\n" \
"buffer_load_dwordx4 a[52:55], v210, %[srdz], s61 offen offset:1024 sc1\n" \
"buffer_load_dwordx4 a[56:59], v210, %[srdz], s61 offen offset:2048 sc1\n" \
"buffer_load_dwordx4 a[60:63], v210, %[srdz], s61 offen offset:3072 sc1\n" \
"s_waitcnt vmcnt(0)\n" \
"s_nop 0\n" \
"v_mfma_f32_16x16x32_bf16 v[192:195], a[196:199], a[0:3], v[128:131]\n" \
"v_mfma_f32_16x16x32_bf16 v[192:195], a[200:203], a[4:7], v[192:195]\n" \
"v_mfma_f32_16x16x32_bf16 v[192:195], a[204:207], a[8:11], v[192:195]\n" \
"v_mfma_f32_16x16x32_bf16 v[192:195], a[220:223], a[12:15], v[192:195]\n" \
"v_mfma_f32_16x16x32_bf16 v[196:199], a[196:199], a[16:19], v[132:135]\n" \
"v_mfma_f32_16x16x32_bf16 v[196:199], a[200:203], a[20:23], v[196:199]\n" \
"v_mfma_f32_16x16x32_bf16 v[196:199], a[204:207], a[24:27], v[196:199]\n" \
"v_mfma_f32_16x16x32_bf16 v[196:199], a[220:223], a[28:31], v[196:199]\n" \
"v_mfma_f32_16x16x32_bf16 v[200:203], a[196:199], a[32:35], v[136:139]\n" \
"v_mfma_f32_16x16x32_bf16 v[200:203], a[200:203], a[36:39], v[200:203]\n" \
"v_mfma_f32_16x16x32_bf16 v[200:203], a[204:207], a[40:43], v[200:203]\n" \
"v_mfma_f32_16x16x32_bf16 v[200:203], a[220:223], a[44:47], v[200:203]\n" \
"v_mfma_f32_16x16x32_bf16 v[204:207], a[196:199], a[48:51], v[140:143]\n" \
"v_mfma_f32_16x16x32_bf16 v[204:207], a[200:203], a[52:55], v[204:207]\n" \
"v_mfma_f32_16x16x32_bf16 v[204:207], a[204:207], a[56:59], v[204:207]\n" \
"v_mfma_f32_16x16x32_bf16 v[204:207], a[220:223], a[60:63], v[204:207]\n" \
"s_nop 7\n" \
"s_nop 0\n" \
"v_cmp_u_f32_e64 s[44:45], v192, v196\n" \
"v_cmp_u_f32_e64 s[56:57], v200, v204\n" \
"s_or_b64 s[56:57], s[56:57], s[44:45]\n" \
"s_cbranch_scc0 Lbk6_%=\n" \
"s_sub_u32 s51, s51, 1\n" \
"s_cbranch_scc0 Lsq6_%=\n" \
"s_branch Lbk6_%=\n" \
"Lsp7_%=:\n" \
"s_movk_i32 s51, 0x1000\n" \
"s_cmp_gt_u32 s48, 6\n" \
"s_cselect_b32 s61, 6, 7\n" \
"s_mul_i32 s61, s61, 152064\n" \
"Lsq7_%=:\n" \
"buffer_load_dwordx4 a[128:131], %[oq0], %[srdz], s61 offen offset:0 sc1\n" \
"buffer_load_dwordx4 a[132:135], %[oq0], %[srdz], s61 offen offset:1024 sc1\n" \
"buffer_load_dwordx4 a[136:139], %[oq0], %[srdz], s61 offen offset:2048 sc1\n" \
"buffer_load_dwordx4 a[140:143], %[oq0], %[srdz], s61 offen offset:3072 sc1\n" \
"buffer_load_dwordx4 a[144:147], v208, %[srdz], s61 offen offset:0 sc1\n" \
"buffer_load_dwordx4 a[148:151], v208, %[srdz], s61 offen offset:1024 sc1\n" \
"buffer_load_dwordx4 a[152:155], v208, %[srdz], s61 offen offset:2048 sc1\n" \
"buffer_load_dwordx4 a[156:159], v208, %[srdz], s61 offen offset:3072 sc1\n" \
"buffer_load_dwordx4 a[160:163], v209, %[srdz], s61 offen offset:0 sc1\n" \
"buffer_load_dwordx4 a[164:167], v209, %[srdz], s61 offen offset:1024 sc1\n" \
"buffer_load_dwordx4 a[168:171], v209, %[srdz], s61 offen offset:2048 sc1\n" \
"buffer_load_dwordx4 a[172:175], v209, %[srdz], s61 offen offset:3072 sc1\n" \
"buffer_load_dwordx4 a[176:179], v210, %[srdz], s61 offen offset:0 sc1\n" \
"buffer_load_dwordx4 a[180:183], v210, %[srdz], s61 offen offset:1024 sc1\n" \
"buffer_load_dwordx4 a[184:187], v210, %[srdz], s61 offen offset:2048 sc1\n" \
"buffer_load_dwordx4 a[188:191], v210, %[srdz], s61 offen offset:3072 sc1\n" \
"s_waitcnt vmcnt(0)\n" \
"s_nop 0\n" \
"v_mfma_f32_16x16x32_bf16 v[128:131], a[208:211], a[128:131], v[192:195]\n" \
"v_mfma_f32_16x16x32_bf16 v[128:131], a[224:227], a[132:135], v[128:131]\n" \
"v_mfma_f32_16x16x32_bf16 v[128:131], a[228:231], a[136:139], v[128:131]\n" \
"v_mfma_f32_16x16x32_bf16 v[128:131], a[236:239], a[140:143], v[128:131]\n" \
"v_mfma_f32_16x16x32_bf16 v[132:135], a[208:211], a[144:147], v[196:199]\n" \
"v_mfma_f32_16x16x32_bf16 v[132:135], a[224:227], a[148:151], v[132:135]\n" \
"v_mfma_f32_16x16x32_bf16 v[132:135], a[228:231], a[152:155], v[132:135]\n" \
"v_mfma_f32_16x16x32_bf16 v[132:135], a[236:239], a[156:159], v[132:135]\n" \
"v_mfma_f32_16x16x32_bf16 v[136:139], a[208:211], a[160:163], v[200:203]\n" \
"v_mfma_f32_16x16x32_bf16 v[136:139], a[224:227], a[164:167], v[136:139]\n" \
"v_mfma_f32_16x16x32_bf16 v[136:139], a[228:231], a[168:171], v[136:139]\n" \
"v_mfma_f32_16x16x32_bf16 v[136:139], a[236:239], a[172:175], v[136:139]\n" \
"v_mfma_f32_16x16x32_bf16 v[140:143], a[208:211], a[176:179], v[204:207]\n" \
"v_mfma_f32_16x16x32_bf16 v[140:143], a[224:227], a[180:183], v[140:143]\n" \
"v_mfma_f32_16x16x32_bf16 v[140:143], a[228:231], a[184:187], v[140:143]\n" \
"v_mfma_f32_16x16x32_bf16 v[140:143], a[236:239], a[188:191], v[140:143]\n" \
"s_nop 7\n" \
"s_nop 0\n" \
"v_cmp_u_f32_e64 s[44:45], v128, v132\n" \
"v_cmp_u_f32_e64 s[56:57], v136, v140\n" \
"s_or_b64 s[56:57], s[56:57], s[44:45]\n" \
"s_cbranch_scc0 Lbk7_%=\n" \
"s_sub_u32 s51, s51, 1\n" \
"s_cbranch_scc0 Lsq7_%=\n" \
"s_branch Lbk7_%=\n" \
"Lspk_%=:\n" \
"s_nop 0\n" \
"s_mov_b32 s51, 0x10000\n" \
"Lsqk_%=:\n" \
"buffer_load_dword v229, v226, %[srdz], s50 offen sc1\n" \
"s_waitcnt vmcnt(0)\n" \
"s_nop 0\n" \
"v_cmp_eq_u32_e64 s[56:57], 0, v229\n" \
"s_cmp_lg_u64 s[56:57], 0\n" \
"s_cbranch_scc0 Lbkk_%=\n" \
"s_sub_u32 s51, s51, 1\n" \
"s_cbranch_scc0 Lsqk_%=\n" \
"s_branch Lbkk_%=\n" \
"Lspp_%=:\n" \
"s_nop 0\n" \
"s_mov_b32 s51, 0x10000\n" \
"Lsqp_%=:\n" \
"buffer_load_dword v232, v226, %[srdz], s54 offen sc1\n" \
"s_waitcnt vmcnt(0)\n" \
"v_cmp_eq_u32_e32 vcc, 0, v232\n" \
"s_cbranch_vccz Lbkp_%=\n" \
"s_sub_u32 s51, s51, 1\n" \
"s_cbranch_scc0 Lsqp_%=\n" \
"s_branch Lbkp_%=\n" \
"Lwalk_%=:\n" \
"s_mov_b64 exec, 0\n" \
"s_cmp_eq_u32 %[wid], 1\n" \
"s_cbranch_scc1 Lk0_%=\n" \
"s_cmp_eq_u32 %[wid], 2\n" \
"s_cbranch_scc1 Lk6_%=\n" \
"s_branch Lk12_%=\n" \
"adaln_kr:\n" \
"s_mov_b64 exec, -1\n" \
"s_branch Lgo_%=\n" \
"Lslow_%=:\n" \
"s_mov_b32 s52, 1\n" \
"s_mov_b32 s48, 0\n" \
"s_mov_b32 s51, 32\n" \
"s_nop 0\n" \
"s_mov_b32 s62, 0x80000000\n" \
"s_mov_b32 s63, 0x80000000\n" \
"s_waitcnt vmcnt(0)\n" \
"s_nop 0\n" \
"v_readlane_b32 s64, %[tlo], 9\n" \
"v_readlane_b32 s65, %[thi], 9\n" \
"s_mov_b32 s66, %[wbytes]\n" \
"v_mov_b32_e32 v229, %[vg0]\n" \
"s_mov_b32 s44, %[m0t]\n" \
"s_mov_b32 s45, 0\n" \
"v_xor_b32_e64 v208, 64, %[kb0]\n" \
"v_xor_b32_e32 v209, 0x80, %[kb0]\n" \
"v_xor_b32_e32 v210, 0xc0, %[kb0]\n" \
"v_xor_b32_e64 v212, 64, %[kd0]\n" \
"v_xor_b32_e32 v213, 0x80, %[kd0]\n" \
"v_xor_b32_e32 v214, 0xc0, %[kd0]\n" \
"v_xor_b32_e64 v211, 32, %[vvb]\n" \
"v_add_u32_e32 v215, 0xfffffc80, %[vd0]\n" \
"s_mov_b32 s66, 0\n" \
"s_add_u32 s40, %[m0b], 0\n" \
"s_add_u32 s41, %[m0b], 16384\n" \
"s_add_u32 s42, %[m0b], 32768\n" \
"s_add_u32 s43, %[m0b], 49152\n" \
"v_mov_b32_e32 v225, 0xff800000\n" \
"v_mov_b32_e32 v226, 0xff800000\n" \
"v_mov_b32_e32 v227, 0xff800000\n" \
"v_mov_b32_e32 v228, 0xff800000\n" \
"s_waitcnt vmcnt(0) lgkmcnt(0)\n" \
"s_barrier\n" \
"Lsl_%=:\n" \
"s_mov_b32 m0, s40\n" \
"s_nop 0\n" \
"buffer_load_dwordx4 %[kd0], %[srdk], s48 offen offset:0 lds\n" \
"buffer_load_dwordx4 v212, %[srdk], s48 offen offset:1024 lds\n" \
"buffer_load_dwordx4 v213, %[srdk], s48 offen offset:2048 lds\n" \
"buffer_load_dwordx4 v214, %[srdk], s48 offen offset:3072 lds\n" \
"s_waitcnt vmcnt(0)\n" \
"s_barrier\n" \
"ds_read_b128 a[64:67], %[kb0] offset:0\n" \
"ds_read_b128 a[68:71], v208 offset:0\n" \
"ds_read_b128 a[72:75], v209 offset:0\n" \
"ds_read_b128 a[76:79], v210 offset:0\n" \
"ds_read_b128 a[92:95], v210 offset:4096\n" \
"ds_read_b128 a[88:91], v209 offset:4096\n" \
"ds_read_b128 a[84:87], v208 offset:4096\n" \
"ds_read_b128 a[80:83], %[kb0] offset:4096\n" \
"ds_read_b128 a[96:99], %[kb0] offset:8192\n" \
"ds_read_b128 a[100:103], v208 offset:8192\n" \
"ds_read_b128 a[104:107], v209 offset:8192\n" \
"ds_read_b128 a[108:111], v210 offset:8192\n" \
"ds_read_b128 a[124:127], v210 offset:12288\n" \
"ds_read_b128 a[120:123], v209 offset:12288\n" \
"ds_read_b128 a[116:119], v208 offset:12288\n" \
"ds_read_b128 a[112:115], %[kb0] offset:12288\n" \
"s_waitcnt lgkmcnt(0)\n" \
"s_barrier\n" \
"v_mfma_f32_16x16x32_bf16 v[128:131], a[64:67], a[0:3], 0\n" \
"v_mfma_f32_16x16x32_bf16 v[128:131], a[68:71], a[4:7], v[128:131]\n" \
"v_mfma_f32_16x16x32_bf16 v[128:131], a[72:75], a[8:11], v[128:131]\n" \
"v_mfma_f32_16x16x32_bf16 v[128:131], a[76:79], a[12:15], v[128:131]\n" \
"v_mfma_f32_16x16x32_bf16 v[132:135], a[92:95], a[12:15], 0\n" \
"v_mfma_f32_16x16x32_bf16 v[132:135], a[88:91], a[8:11], v[132:135]\n" \
"v_mfma_f32_16x16x32_bf16 v[132:135], a[84:87], a[4:7], v[132:135]\n" \
"v_mfma_f32_16x16x32_bf16 v[132:135], a[80:83], a[0:3], v[132:135]\n" \
"v_mfma_f32_16x16x32_bf16 v[136:139], a[64:67], a[16:19], 0\n" \
"v_mfma_f32_16x16x32_bf16 v[136:139], a[68:71], a[20:23], v[136:139]\n" \
"v_mfma_f32_16x16x32_bf16 v[136:139], a[72:75], a[24:27], v[136:139]\n" \
"v_mfma_f32_16x16x32_bf16 v[136:139], a[76:79], a[28:31], v[136:139]\n" \
"v_mfma_f32_16x16x32_bf16 v[140:143], a[92:95], a[28:31], 0\n" \
"v_mfma_f32_16x16x32_bf16 v[140:143], a[88:91], a[24:27], v[140:143]\n" \
"v_mfma_f32_16x16x32_bf16 v[140:143], a[84:87], a[20:23], v[140:143]\n" \
"v_mfma_f32_16x16x32_bf16 v[140:143], a[80:83], a[16:19], v[140:143]\n" \
"v_mfma_f32_16x16x32_bf16 v[144:147], a[64:67], a[32:35], 0\n" \
"v_mfma_f32_16x16x32_bf16 v[144:147], a[68:71], a[36:39], v[144:147]\n" \
"v_mfma_f32_16x16x32_bf16 v[144:147], a[72:75], a[40:43], v[144:147]\n" \
"v_mfma_f32_16x16x32_bf16 v[144:147], a[76:79], a[44:47], v[144:147]\n" \
"v_mfma_f32_16x16x32_bf16 v[148:151], a[92:95], a[44:47], 0\n" \
"v_mfma_f32_16x16x32_bf16 v[148:151], a[88:91], a[40:43], v[148:151]\n" \
"v_mfma_f32_16x16x32_bf16 v[148:151], a[84:87], a[36:39], v[148:151]\n" \
"v_mfma_f32_16x16x32_bf16 v[148:151], a[80:83], a[32:35], v[148:151]\n" \
"v_mfma_f32_16x16x32_bf16 v[152:155], a[64:67], a[48:51], 0\n" \
"v_mfma_f32_16x16x32_bf16 v[152:155], a[68:71], a[52:55], v[152:155]\n" \
"v_mfma_f32_16x16x32_bf16 v[152:155], a[72:75], a[56:59], v[152:155]\n" \
"v_mfma_f32_16x16x32_bf16 v[152:155], a[76:79], a[60:63], v[152:155]\n" \
"v_mfma_f32_16x16x32_bf16 v[156:159], a[92:95], a[60:63], 0\n" \
"v_mfma_f32_16x16x32_bf16 v[156:159], a[88:91], a[56:59], v[156:159]\n" \
"v_mfma_f32_16x16x32_bf16 v[156:159], a[84:87], a[52:55], v[156:159]\n" \
"v_mfma_f32_16x16x32_bf16 v[156:159], a[80:83], a[48:51], v[156:159]\n" \
"s_nop 7\n" \
"s_nop 0\n" \
"v_max3_f32 v230, v128, v129, v130\n" \
"v_max3_f32 v231, v131, v132, v133\n" \
"v_max3_f32 v230, v230, v134, v135\n" \
"v_max3_f32 v225, v230, v231, v225\n" \
"v_max3_f32 v230, v136, v137, v138\n" \
"v_max3_f32 v231, v139, v140, v141\n" \
"v_max3_f32 v230, v230, v142, v143\n" \
"v_max3_f32 v226, v230, v231, v226\n" \
"v_max3_f32 v230, v144, v145, v146\n" \
"v_max3_f32 v231, v147, v148, v149\n" \
"v_max3_f32 v230, v230, v150, v151\n" \
"v_max3_f32 v227, v230, v231, v227\n" \
"v_max3_f32 v230, v152, v153, v154\n" \
"v_max3_f32 v231, v155, v156, v157\n" \
"v_max3_f32 v230, v230, v158, v159\n" \
"v_max3_f32 v228, v230, v231, v228\n" \
"s_nop 3\n" \
"s_nop 0\n" \
"v_mfma_f32_16x16x32_bf16 v[128:131], a[96:99], a[0:3], 0\n" \
"v_mfma_f32_16x16x32_bf16 v[128:131], a[100:103], a[4:7], v[128:131]\n" \
"v_mfma_f32_16x16x32_bf16 v[128:131], a[104:107], a[8:11], v[128:131]\n" \
"v_mfma_f32_16x16x32_bf16 v[128:131], a[108:111], a[12:15], v[128:131]\n" \
"v_mfma_f32_16x16x32_bf16 v[132:135], a[124:127], a[12:15], 0\n" \
"v_mfma_f32_16x16x32_bf16 v[132:135], a[120:123], a[8:11], v[132:135]\n" \
"v_mfma_f32_16x16x32_bf16 v[132:135], a[116:119], a[4:7], v[132:135]\n" \
"v_mfma_f32_16x16x32_bf16 v[132:135], a[112:115], a[0:3], v[132:135]\n" \
"v_mfma_f32_16x16x32_bf16 v[136:139], a[96:99], a[16:19], 0\n" \
"v_mfma_f32_16x16x32_bf16 v[136:139], a[100:103], a[20:23], v[136:139]\n" \
"v_mfma_f32_16x16x32_bf16 v[136:139], a[104:107], a[24:27], v[136:139]\n" \
"v_mfma_f32_16x16x32_bf16 v[136:139], a[108:111], a[28:31], v[136:139]\n" \
"v_mfma_f32_16x16x32_bf16 v[140:143], a[124:127], a[28:31], 0\n" \
"v_mfma_f32_16x16x32_bf16 v[140:143], a[120:123], a[24:27], v[140:143]\n" \
"v_mfma_f32_16x16x32_bf16 v[140:143], a[116:119], a[20:23], v[140:143]\n" \
"v_mfma_f32_16x16x32_bf16 v[140:143], a[112:115], a[16:19], v[140:143]\n" \
"v_mfma_f32_16x16x32_bf16 v[144:147], a[96:99], a[32:35], 0\n" \
"v_mfma_f32_16x16x32_bf16 v[144:147], a[100:103], a[36:39], v[144:147]\n" \
"v_mfma_f32_16x16x32_bf16 v[144:147], a[104:107], a[40:43], v[144:147]\n" \
"v_mfma_f32_16x16x32_bf16 v[144:147], a[108:111], a[44:47], v[144:147]\n" \
"v_mfma_f32_16x16x32_bf16 v[148:151], a[124:127], a[44:47], 0\n" \
"v_mfma_f32_16x16x32_bf16 v[148:151], a[120:123], a[40:43], v[148:151]\n" \
"v_mfma_f32_16x16x32_bf16 v[148:151], a[116:119], a[36:39], v[148:151]\n" \
"v_mfma_f32_16x16x32_bf16 v[148:151], a[112:115], a[32:35], v[148:151]\n" \
"v_mfma_f32_16x16x32_bf16 v[152:155], a[96:99], a[48:51], 0\n" \
"v_mfma_f32_16x16x32_bf16 v[152:155], a[100:103], a[52:55], v[152:155]\n" \
"v_mfma_f32_16x16x32_bf16 v[152:155], a[104:107], a[56:59], v[152:155]\n" \
"v_mfma_f32_16x16x32_bf16 v[152:155], a[108:111], a[60:63], v[152:155]\n" \
"v_mfma_f32_16x16x32_bf16 v[156:159], a[124:127], a[60:63], 0\n" \
"v_mfma_f32_16x16x32_bf16 v[156:159], a[120:123], a[56:59], v[156:159]\n" \
"v_mfma_f32_16x16x32_bf16 v[156:159], a[116:119], a[52:55], v[156:159]\n" \
"v_mfma_f32_16x16x32_bf16 v[156:159], a[112:115], a[48:51], v[156:159]\n" \
"s_nop 7\n" \
"s_nop 0\n" \
"v_max3_f32 v230, v128, v129, v130\n" \
"v_max3_f32 v231, v131, v132, v133\n" \
"v_max3_f32 v230, v230, v134, v135\n" \
"v_max3_f32 v225, v230, v231, v225\n" \
"v_max3_f32 v230, v136, v137, v138\n" \
"v_max3_f32 v231, v139, v140, v141\n" \
"v_max3_f32 v230, v230, v142, v143\n" \
"v_max3_f32 v226, v230, v231, v226\n" \
"v_max3_f32 v230, v144, v145, v146\n" \
"v_max3_f32 v231, v147, v148, v149\n" \
"v_max3_f32 v230, v230, v150, v151\n" \
"v_max3_f32 v227, v230, v231, v227\n" \
"v_max3_f32 v230, v152, v153, v154\n" \
"v_max3_f32 v231, v155, v156, v157\n" \
"v_max3_f32 v230, v230, v158, v159\n" \
"v_max3_f32 v228, v230, v231, v228\n" \
"s_nop 3\n" \
"s_add_u32 s48, s48, s54\n" \
"s_sub_u32 s51, s51, 1\n" \
"s_cmp_lg_u32 s51, 0\n" \
"s_cbranch_scc1 Lsl_%=\n" \
"s_nop 0\n" \
"v_mov_b32_e32 v232, v225\n" \
"v_mov_b32_e32 v233, v226\n" \
"v_mov_b32_e32 v234, v227\n" \
"v_mov_b32_e32 v235, v228\n" \
"s_nop 0\n" \
"v_permlane16_swap_b32_e32 v225, v232\n" \
"v_permlane16_swap_b32_e32 v226, v233\n" \
"v_permlane16_swap_b32_e32 v227, v234\n" \
"v_permlane16_swap_b32_e32 v228, v235\n" \
"v_max_f32_e32 v225, v225, v232\n" \
"v_max_f32_e32 v226, v226, v233\n" \
"v_max_f32_e32 v227, v227, v234\n" \
"v_max_f32_e32 v228, v228, v235\n" \
"v_mov_b32_e32 v232, v225\n" \
"v_mov_b32_e32 v233, v226\n" \
"v_mov_b32_e32 v234, v227\n" \
"v_mov_b32_e32 v235, v228\n" \
"s_nop 0\n" \
"v_permlane32_swap_b32_e32 v225, v232\n" \
"v_permlane32_swap_b32_e32 v226, v233\n" \
"v_permlane32_swap_b32_e32 v227, v234\n" \
"v_permlane32_swap_b32_e32 v228, v235\n" \
"v_max_f32_e32 v225, v225, v232\n" \
"v_max_f32_e32 v226, v226, v233\n" \
"v_max_f32_e32 v227, v227, v234\n" \
"v_max_f32_e32 v228, v228, v235\n" \
"s_cmp_lg_u32 s69, 0\n" \
"s_cbranch_scc1 Lnewm_%=\n" \
"v_sub_f32_e32 v225, 0, v160\n" \
"v_sub_f32_e32 v226, 0, v164\n" \
"v_sub_f32_e64 v227, 0, v168\n" \
"v_sub_f32_e32 v228, 0, v172\n" \
"Lnewm_%=:\n" \
"s_mov_b32 s48, 0\n" \
"s_mov_b32 m0, s40\n" \
"s_nop 0\n" \
"buffer_load_dwordx4 %[kd0], %[srdk], s48 offen offset:0 lds\n" \
"buffer_load_dwordx4 v212, %[srdk], s48 offen offset:1024 lds\n" \
"buffer_load_dwordx4 v213, %[srdk], s48 offen offset:2048 lds\n" \
"buffer_load_dwordx4 v214, %[srdk], s48 offen offset:3072 lds\n" \
"s_mov_b32 s48, 16384\n" \
"s_mov_b32 m0, s41\n" \
"s_nop 0\n" \
"buffer_load_dwordx4 %[kd0], %[srdk], s48 offen offset:0 lds\n" \
"buffer_load_dwordx4 v212, %[srdk], s48 offen offset:1024 lds\n" \
"buffer_load_dwordx4 v213, %[srdk], s48 offen offset:2048 lds\n" \
"buffer_load_dwordx4 v214, %[srdk], s48 offen offset:3072 lds\n" \
"s_add_u32 m0, %[m0b], 65536\n" \
"s_nop 0\n" \
"s_nop 0\n" \
"buffer_load_dwordx4 %[vd0], %[srdv], s48 offen offset:0 lds\n" \
"buffer_load_dwordx4 v215, %[srdv], s48 offen offset:1024 lds\n" \
"buffer_load_dwordx4 %[vd0], %[srdv], s48 offen offset:2048 lds\n" \
"buffer_load_dwordx4 v215, %[srdv], s48 offen offset:3072 lds\n" \
"s_mov_b32 s48, 32768\n" \
"s_mov_b32 m0, s42\n" \
"s_nop 0\n" \
"buffer_load_dwordx4 %[kd0], %[srdk], s48 offen offset:0 lds\n" \
"buffer_load_dwordx4 v212, %[srdk], s48 offen offset:1024 lds\n" \
"buffer_load_dwordx4 v213, %[srdk], s48 offen offset:2048 lds\n" \
"buffer_load_dwordx4 v214, %[srdk], s48 offen offset:3072 lds\n" \
"s_add_u32 m0, %[m0b], 81920\n" \
"s_nop 0\n" \
"s_nop 0\n" \
"buffer_load_dwordx4 %[vd0], %[srdv], s48 offen offset:0 lds\n" \
"buffer_load_dwordx4 v215, %[srdv], s48 offen offset:1024 lds\n" \
"buffer_load_dwordx4 %[vd0], %[srdv], s48 offen offset:2048 lds\n" \
"buffer_load_dwordx4 v215, %[srdv], s48 offen offset:3072 lds\n" \
"s_mov_b32 s48, 49152\n" \
"s_mov_b32 m0, s43\n" \
"s_nop 0\n" \
"buffer_load_dwordx4 %[kd0], %[srdk], s48 offen offset:0 lds\n" \
"buffer_load_dwordx4 v212, %[srdk], s48 offen offset:1024 lds\n" \
"buffer_load_dwordx4 v213, %[srdk], s48 offen offset:2048 lds\n" \
"buffer_load_dwordx4 v214, %[srdk], s48 offen offset:3072 lds\n" \
"s_add_u32 m0, %[m0b], 98304\n" \
"s_nop 0\n" \
"s_nop 0\n" \
"buffer_load_dwordx4 %[vd0], %[srdv], s48 offen offset:0 lds\n" \
"buffer_load_dwordx4 v215, %[srdv], s48 offen offset:1024 lds\n" \
"buffer_load_dwordx4 %[vd0], %[srdv], s48 offen offset:2048 lds\n" \
"buffer_load_dwordx4 v215, %[srdv], s48 offen offset:3072 lds\n" \
"s_waitcnt vmcnt(0)\n" \
"s_barrier\n" \
"ds_read_b128 a[64:67], %[kb0] offset:0\n" \
"ds_read_b128 a[68:71], v208 offset:0\n" \
"ds_read_b128 a[72:75], v209 offset:0\n" \
"ds_read_b128 a[76:79], v210 offset:0\n" \
"ds_read_b128 a[92:95], v210 offset:4096\n" \
"ds_read_b128 a[88:91], v209 offset:4096\n" \
"ds_read_b128 a[84:87], v208 offset:4096\n" \
"ds_read_b128 a[80:83], %[kb0] offset:4096\n" \
"s_mov_b32 s48, 65536\n" \
"s_mov_b32 s60, 0\n" \
"s_waitcnt lgkmcnt(0)\n" \
"v_mfma_f32_16x16x32_bf16 v[128:131], a[64:67], a[0:3], 0\n" \
"ds_read_b128 a[96:99], %[kb0] offset:8192\n" \
"v_mfma_f32_16x16x32_bf16 v[128:131], a[68:71], a[4:7], v[128:131]\n" \
"ds_read_b128 a[100:103], v208 offset:8192\n" \
"v_mfma_f32_16x16x32_bf16 v[128:131], a[72:75], a[8:11], v[128:131]\n" \
"ds_read_b128 a[104:107], v209 offset:8192\n" \
"v_mfma_f32_16x16x32_bf16 v[128:131], a[76:79], a[12:15], v[128:131]\n" \
"ds_read_b128 a[108:111], v210 offset:8192\n" \
"v_mfma_f32_16x16x32_bf16 v[132:135], a[92:95], a[12:15], 0\n" \
"ds_read_b128 a[124:127], v210 offset:12288\n" \
"v_mfma_f32_16x16x32_bf16 v[132:135], a[88:91], a[8:11], v[132:135]\n" \
"ds_read_b128 a[120:123], v209 offset:12288\n" \
"v_mfma_f32_16x16x32_bf16 v[132:135], a[84:87], a[4:7], v[132:135]\n" \
"ds_read_b128 a[116:119], v208 offset:12288\n" \
"v_mfma_f32_16x16x32_bf16 v[132:135], a[80:83], a[0:3], v[132:135]\n" \
"ds_read_b128 a[112:115], %[kb0] offset:12288\n" \
"v_mfma_f32_16x16x32_bf16 v[136:139], a[64:67], a[16:19], 0\n" \
"ds_read_b64_tr_b16 a[128:129], %[vvb] offset:0\n" \
"v_mfma_f32_16x16x32_bf16 v[136:139], a[68:71], a[20:23], v[136:139]\n" \
"ds_read_b64_tr_b16 a[130:131], %[vvb] offset:4096\n" \
"v_mfma_f32_16x16x32_bf16 v[136:139], a[72:75], a[24:27], v[136:139]\n" \
"ds_read_b64_tr_b16 a[132:133], v211 offset:0\n" \
"v_mfma_f32_16x16x32_bf16 v[136:139], a[76:79], a[28:31], v[136:139]\n" \
"ds_read_b64_tr_b16 a[134:135], v211 offset:4096\n" \
"v_mfma_f32_16x16x32_bf16 v[140:143], a[92:95], a[28:31], 0\n" \
"ds_read_b64_tr_b16 a[136:137], %[vvb] offset:512\n" \
"v_mfma_f32_16x16x32_bf16 v[140:143], a[88:91], a[24:27], v[140:143]\n" \
"ds_read_b64_tr_b16 a[138:139], %[vvb] offset:4608\n" \
"v_mfma_f32_16x16x32_bf16 v[140:143], a[84:87], a[20:23], v[140:143]\n" \
"ds_read_b64_tr_b16 a[140:141], v211 offset:512\n" \
"v_mfma_f32_16x16x32_bf16 v[140:143], a[80:83], a[16:19], v[140:143]\n" \
"ds_read_b64_tr_b16 a[142:143], v211 offset:4608\n" \
"v_mfma_f32_16x16x32_bf16 v[144:147], a[64:67], a[32:35], 0\n" \
"ds_read_b64_tr_b16 a[144:145], %[vvb] offset:1024\n" \
"v_mfma_f32_16x16x32_bf16 v[144:147], a[68:71], a[36:39], v[144:147]\n" \
"ds_read_b64_tr_b16 a[146:147], %[vvb] offset:5120\n" \
"v_mfma_f32_16x16x32_bf16 v[144:147], a[72:75], a[40:43], v[144:147]\n" \
"ds_read_b64_tr_b16 a[148:149], v211 offset:1024\n" \
"v_mfma_f32_16x16x32_bf16 v[144:147], a[76:79], a[44:47], v[144:147]\n" \
"ds_read_b64_tr_b16 a[150:151], v211 offset:5120\n" \
"v_mfma_f32_16x16x32_bf16 v[148:151], a[92:95], a[44:47], 0\n" \
"ds_read_b64_tr_b16 a[152:153], %[vvb] offset:1536\n" \
"v_mfma_f32_16x16x32_bf16 v[148:151], a[88:91], a[40:43], v[148:151]\n" \
"ds_read_b64_tr_b16 a[154:155], %[vvb] offset:5632\n" \
"v_mfma_f32_16x16x32_bf16 v[148:151], a[84:87], a[36:39], v[148:151]\n" \
"ds_read_b64_tr_b16 a[156:157], v211 offset:1536\n" \
"v_mfma_f32_16x16x32_bf16 v[148:151], a[80:83], a[32:35], v[148:151]\n" \
"ds_read_b64_tr_b16 a[158:159], v211 offset:5632\n" \
"v_mfma_f32_16x16x32_bf16 v[152:155], a[64:67], a[48:51], 0\n" \
"v_mfma_f32_16x16x32_bf16 v[152:155], a[68:71], a[52:55], v[152:155]\n" \
"v_mfma_f32_16x16x32_bf16 v[152:155], a[72:75], a[56:59], v[152:155]\n" \
"v_mfma_f32_16x16x32_bf16 v[152:155], a[76:79], a[60:63], v[152:155]\n" \
"v_mfma_f32_16x16x32_bf16 v[156:159], a[92:95], a[60:63], 0\n" \
"v_mfma_f32_16x16x32_bf16 v[156:159], a[88:91], a[56:59], v[156:159]\n" \
"v_mfma_f32_16x16x32_bf16 v[156:159], a[84:87], a[52:55], v[156:159]\n" \
"v_mfma_f32_16x16x32_bf16 v[156:159], a[80:83], a[48:51], v[156:159]\n" \
"s_branch Lhavem_%=\n" \
"s_nop 0\n" \
".p2align 12\n" \
"adaln_pg0:\n" \
"s_cbranch_execz adaln_pg1\n" \
"Lend_%=:\n" \
""

#define SRD_WORD3 0x00020000
DI u32x4 ld16_sc1(const void* p) { u32x4 v; asm volatile("global_load_dwordx4 %0, %1, off sc1" : "=v"(v) : "v"(p) : "memory"); return v; }
DI unsigned ord_key(float r, unsigned row) {
  const unsigned b = __float_as_uint(r), o = (b & 0x80000000u) ? ~b : (b | 0x80000000u);
  return max(o & 0xfffff800u, 0x800u) | (2047u - row);
}
DI void top2_merge(unsigned& a1, unsigned& a2, unsigned b1, unsigned b2) {
  const unsigned n1 = max(a1, b1), n2 = max(min(a1, b1), max(a2, b2)); a1 = n1; a2 = n2;
}
template <int CTRL> DI void top2_dpp(unsigned& k1, unsigned& k2) {
  const unsigned p1 = __builtin_amdgcn_update_dpp(0u, k1, CTRL, 0xf, 0xf, true), p2 = __builtin_amdgcn_update_dpp(0u, k2, CTRL, 0xf, 0xf, true); top2_merge(k1, k2, p1, p2);
}
DI float row_sum(float v) {
  v = dpp_add<DPP_XOR1>(v); v = dpp_add<DPP_XOR2>(v); v = dpp_add<DPP_ROR8>(v);
  v = swap16_add(v, v); return swap32_add(v, v);
}
DI unsigned h2_pair(float x0, float x1, float m, float rden, unsigned c1w, unsigned sbw) { return ln_pair(x0, x1, m, rden, lo16(c1w), hi16(c1w), lo16(sbw), hi16(sbw)); }
#define PAGE(n, nx) asm volatile("s_branch adaln_pe" #n "\n.p2align 12\nadaln_pg" #n ":\n\ts_cbranch_execz " nx "\nadaln_pe" #n ":")
#define STR_(x) #x
#define STR(x) STR_(x)
constexpr int MOE_WGS = DFF / 16;
constexpr unsigned RETRY_CAP = 4096;
#define ASLEEP 2
#define KLOOK 8
#define YSPLIT 8
#define FSLEEP 14
#define KSLEEP 3
#define YEARLY 8
#define FREST 0
#define KL0 1u
#define KLS 2u
#define KST 2u
#define KCHK 8
#define KPAUSE 2
#define KEY_SEL b

#define WBYTES (moe ? DM * DFF * 2 : 0)
namespace fa {
constexpr unsigned LDS_BYTES = 163840;
constexpr float QSCALE = 0.12751743082f;
struct Args { const u16 *x, *wmod, *wq, *kc, *vc, *wo, *wg, *wu, *wd, *c, *wr; char* ws; u16* out;
};
__global__ __launch_bounds__(256, 1) void fattn_k(const Args p) {
  extern __shared__ __attribute__((aligned(16))) char lds[];
  const unsigned wid = __builtin_amdgcn_readfirstlane(threadIdx.x >> 6);
  const unsigned b = blockIdx.x, idx = b >> 3, kg = b & 3u, m = idx & 7u, hd = 8u * kg + m, y = (b & 4u) + (idx >> 3);
  const unsigned ldsb = (unsigned)(size_t)lds;
  const unsigned m0b = ldsb + 4096u * wid, m0t = ldsb + 131072u + 8192u * wid;
  const __amdgpu_buffer_rsrc_t srdz = __builtin_amdgcn_make_buffer_rsrc((void*)p.ws, (short)0, (int)WS_END, SRD_WORD3);
  const bool moe = b < MOE_WGS;
  const unsigned sl = 4u * ((b & 7u) * 7u + (idx >> 2)) + (idx & 3u), yp2 = (y & 1u) * 2u;
  const unsigned sin = (unsigned)WS_XIN + ((y * 32u + hd) * 4u + wid) * BOX;
  u32x2 xq[8]; unsigned fl, got, got2; unsigned long long tag; u16* pout;
  f32x2 xf[16]; f32x4 gaf[8], aw[8]; u32x4 alo, ahi; unsigned late; float arow;
  const unsigned sxf = (unsigned)WS_XFL + (y * 4u + wid) * 512u, sxl = (unsigned)WS_XFL + (y * 4u + kg) * 512u;
#define FQ(i)
#define TQ(i)
  {
    const unsigned tid = threadIdx.x, lane = tid & 63u, l16 = lane & 15u, g = lane >> 4, hi = lane >> 5, r8 = (lane >> 2) & 7u;
    unsigned kb0 = ldsb + 256u * l16 + 16u * (g ^ l16);
    unsigned vvb = ldsb + 65536u + 2048u * hi + 288u * (g & 1u) + 64u * ((lane >> 2) & 3u) + 8u * (lane & 3u);
    unsigned vwb = ldsb + 65536u + 2048u * g + 64u * ((lane >> 2) & 3u) + 16u * (lane & 3u);
    unsigned kd0 = 4096u * wid + 256u * g + 16u * (l16 ^ g);
    unsigned vd0 = 4096u * wid + 256u * r8 + 64u * hi + 16u * ((lane & 3u) ^ (2u * (r8 >> 2))) + 1024u;
    unsigned wd0 = 8192u * (16u * wid + r8) + 64u * hi + 16u * (lane & 3u) + 4096u;
    unsigned hq0 = (unsigned)WS_H + (16u * y + 4u * wid) * 32768u + 16u * lane;
    unsigned oq0 = (unsigned)WS_XIN + ((y * 32u + kg * 8u) * 4u + wid) * BOX + 20480u + 16u * lane;
    unsigned flag = ldsb + 49152u + 4u * wid, wbytes = moe ? DM * DFF * 2 : 0;
    const __amdgpu_buffer_rsrc_t srdw = __builtin_amdgcn_make_buffer_rsrc((void*)((const char*)p.wq + 256u * hd - 4096), (short)0, (int)(DM * DQ * 2 - 256u * hd + 4096u), SRD_WORD3);
    __amdgpu_buffer_rsrc_t srdk = __builtin_amdgcn_make_buffer_rsrc((void*)((const char*)p.kc + (size_t)hd * (SEQ * 256)), (short)0, SEQ * 256, SRD_WORD3);
    __amdgpu_buffer_rsrc_t srdv = __builtin_amdgcn_make_buffer_rsrc((void*)((const char*)p.vc + (size_t)hd * (SEQ * 256) - 17408), (short)0, SEQ * 256 + 17408, SRD_WORD3);
    unsigned long long pct; unsigned clen;
    asm volatile("s_getpc_b64 %0\n\ts_mov_b32 %1, adaln_text_end@rel32@lo+4" : "=s"(pct), "=s"(clen));
    const unsigned vct = min(((idx * 4u + wid) * 6u + min(lane, 5u)) * 128u, (clen - 4u) & ~3u);
      const unsigned gr = 128u * (b & 7u) + 4u * (tid >> 3), gc = 64u * (b >> 3) + 8u * (tid & 7u);
      u32x4 w[4], xs[4]; u32x2 ep, cv;
#define XV(i) ({ u32x4 v_; asm volatile("v_accvgpr_read_b32 %0, a[%4]\n\tv_accvgpr_read_b32 %1, a[%4 + 1]\n\tv_accvgpr_read_b32 %2, a[%4 + 2]\n\tv_accvgpr_read_b32 %3, a[%4 + 3]" : "=v"(v_[0]), "=v"(v_[1]), "=v"(v_[2]), "=v"(v_[3]) : "n"(200 + 4 * (i))); v_; })
      asm volatile("global_load_dwordx2 %0, %1, %2" : "=v"(cv) : "v"(gr * 2u), "s"(p.c) : "memory");
      #pragma unroll
      for (int j = 0; j < 4; ++j) asm volatile("global_load_dwordx4 %0, %1, %2 nt" : "=v"(w[j]) : "v"(((gr + j) * NMOD + gc) * 2u), "s"(p.wmod) : "memory");
      #pragma unroll
      for (int q = 0; q < 4; ++q) asm volatile("global_load_dwordx4 %0, %1, %2 offset:%3 nt" : "=v"(xs[q]) : "v"((8u * (y * 32u + hd) + 2u * wid) * 2048u + 16u * lane), "s"(p.x), "n"(1024 * q) : "memory");
      asm volatile("global_load_dwordx2 %0, %1, %2 sc1" : "=v"(ep) : "v"(0u), "s"(p.ws + WS_EP) : "memory");
      asm volatile("buffer_store_dwordx4 %0, %1, %2, 0 offen sc1\n\ts_nop 1" :: "v"(u32x4{0u, 0u, 0u, 0u}), "v"(tid < Z_SHARE / 16u ? (unsigned)WS_Z + b * Z_SHARE + tid * 16u : 0x80000000u), "s"(srdz) : "memory");
      FQ(6);
      asm volatile("global_load_dword a255, %0, %1" :: "v"(vct), "s"(pct) : "memory");
      asm volatile("s_mov_b64 exec, 0\n\ts_cmp_eq_u32 %0, 1\n\ts_cbranch_scc1 adaln_pg5\n\ts_cmp_eq_u32 %0, 2\n\ts_cbranch_scc1 adaln_pg6\n\ts_cmp_eq_u32 %0, 3\n\ts_cbranch_scc1 adaln_pg7\nadaln_fr:\n\ts_mov_b64 exec, -1" :: "s"(wid) : "scc");
      FQ(7);
      unsigned tq0 = (idx * 8u + wid * 2u + ((lane >> 3) & 1u)) * 8192u + (b & 4u) * 256u + (lane & 7u) * 128u + 4096u - 256u * m;
      const unsigned rb0 = 16u * y + 4u * (hd >> 3), ch = 16u * (hd & 7u) + (tid >> 4);
      unsigned vrw = ldsb + 135168u + wid * 256u + 32u * (tid & 7u) + 8u * g, vrr = ldsb + 135168u + 4u * lane;
      unsigned vmo = wid ? 0x80000000u : (unsigned)WS_MOD + b * 512u + lane * 8u, vst = (lane & 31u) ? 0x80000000u : (unsigned)WS_RST + (8u * (y * 32u + hd) + 2u * wid + hi) * 16u;
      unsigned vx2 = (rb0 * 16u + l16) * 2048u + ch * 16u;
      pout = p.out;
      asm volatile("s_waitcnt vmcnt(11)" : "+v"(cv), "+v"(tq0), "+v"(vrw), "+v"(vrr), "+v"(vmo), "+v"(vst), "+v"(vx2), "+s"(pout) :: "memory");
      auto silu2 = [](unsigned v) { const f32x2 f = unpk(v); return cvtpk(f[0] * __builtin_amdgcn_rcpf(1.f + __builtin_amdgcn_exp2f(f[0] * -1.44269504089f)), f[1] * __builtin_amdgcn_rcpf(1.f + __builtin_amdgcn_exp2f(f[1] * -1.44269504089f))); };
      auto dot2 = [](unsigned x, unsigned z, float c) { return __builtin_amdgcn_fdot2_f32_bf16(__builtin_bit_cast(bf16x2, x), __builtin_bit_cast(bf16x2, z), c, false); };
      unsigned s01 = silu2(cv[0]), s23 = silu2(cv[1]);
      asm volatile("s_waitcnt vmcnt(7)" : "+v"(s01), "+v"(s23), "+v"(w[0]), "+v"(w[1]), "+v"(w[2]), "+v"(w[3]) :: "memory");
      FQ(4);
      asm volatile("buffer_load_dword a255, %0, %1, 0 offen" :: "v"(tq0), "s"(srdw) : "memory");
      {
        float acc[8];
        #pragma unroll
        for (int e = 0; e < 4; ++e) {
          acc[2 * e]     = dot2(__builtin_amdgcn_perm(w[3][e], w[2][e], 0x05040100u), s23, dot2(__builtin_amdgcn_perm(w[1][e], w[0][e], 0x05040100u), s01, 0.f));
          acc[2 * e + 1] = dot2(__builtin_amdgcn_perm(w[3][e], w[2][e], 0x07060302u), s23, dot2(__builtin_amdgcn_perm(w[1][e], w[0][e], 0x07060302u), s01, 0.f));
        }
        float t[4], u[2];
        #pragma unroll
        for (int e = 0; e < 4; ++e) t[e] = swap32_add(acc[e], acc[e + 4]);
        #pragma unroll
        for (int e = 0; e < 2; ++e) u[e] = dpp_add<DPP_ROR8>(swap16_add(t[e], t[e + 2]));
        asm volatile("ds_write_b64 %5, %6\n\ts_waitcnt vmcnt(3)" : "+v"(xs[0]), "+v"(xs[1]), "+v"(xs[2]), "+v"(xs[3]), "+v"(ep) : "v"(vrw), "v"(f32x2{u[0], u[1]}) : "memory");
      }
      tag = (((unsigned long long)(unsigned)__builtin_amdgcn_readfirstlane(ep[1]) << 32) | (unsigned)__builtin_amdgcn_readfirstlane(ep[0])) + 1ull;
      const unsigned tl = (unsigned)tag & 0xffu, th = (unsigned)(tag >> 8);
      u32x4 sg;
      {
        float t4[4] = {0.f, 0.f, 0.f, 0.f};
        #pragma unroll
        for (int e = 0; e < 16; ++e) { const unsigned xw = xs[e >> 2][e & 3]; t4[2 * (e >> 3)] = dot2(xw, 0x3f803f80u, t4[2 * (e >> 3)]); t4[2 * (e >> 3) + 1] = dot2(xw, xw, t4[2 * (e >> 3) + 1]); }
        float u = swap16_add(swap32_add(t4[0], t4[2]), swap32_add(t4[1], t4[3]));
        u = dpp_add<DPP_XOR1>(u); u = dpp_add<DPP_XOR2>(u); u = dpp_add<DPP_ROR4>(u); u = dpp_add<DPP_ROR8>(u);
        const auto r16 = __builtin_amdgcn_permlane16_swap(__float_as_uint(u), __float_as_uint(u), false, false);
        const float mean = __uint_as_float(r16[0]) * (1.f / DM), m2 = mean * mean;
        float var = fmaf(__uint_as_float(r16[1]), 1.f / DM, -m2);
        if (__builtin_expect(__any(!(m2 <= var)), 0)) {
          const f32x2 mm = {__uint_as_float(__builtin_amdgcn_readlane(__float_as_uint(mean), 0)), __uint_as_float(__builtin_amdgcn_readlane(__float_as_uint(mean), 32))};
          f32x2 sq = {0.f, 0.f};
          #pragma unroll
          for (int e = 0; e < 8; ++e) {
            const f32x2 d0 = f32x2{lo16(xs[e >> 2][e & 3]), lo16(xs[2 + (e >> 2)][e & 3])} - mm, d1 = f32x2{hi16(xs[e >> 2][e & 3]), hi16(xs[2 + (e >> 2)][e & 3])} - mm;
            sq = __builtin_elementwise_fma(d1, d1, __builtin_elementwise_fma(d0, d0, sq));
          }
          const float v0 = wave_sum(sq[0]), v1 = wave_sum(sq[1]);
          var = (hi ? v1 : v0) * (1.f / DM);
        }
        sg = u32x4{cvtpk(0.f, mean) | tl, th, cvtpk(0.f, __builtin_amdgcn_sqrtf(rbh(rbh(var) + 1e-5f))) | tl, th};
      }
      {
        f32x2 ra, rc;
        asm volatile("s_waitcnt vmcnt(2) lgkmcnt(0)\n\ts_barrier\n\tds_read2st64_b32 %0, %2 offset1:1\n\tds_read2st64_b32 %1, %2 offset0:2 offset1:3\n\t"
                     "buffer_store_dwordx4 %3, %4, %5, 0 offen sc1\n\ts_waitcnt lgkmcnt(0)" : "=&v"(ra), "=&v"(rc) : "v"(vrr), "v"(sg), "v"(vst), "s"(srdz) : "memory");
        ra += rc;
        asm volatile("buffer_store_dwordx2 %0, %1, %2, 0 offen sc1" :: "v"(u32x2{((__float_as_uint(ra[0] + ra[1]) + 0x80u) & 0xffffff00u) | tl, th}), "v"(vmo), "s"(srdz) : "memory");
      }
      FQ(5);
      asm volatile("global_load_dwordx2 a[216:217], %0, %1" :: "v"(tid * 8u), "s"(p.c) : "memory");
      #pragma unroll
      for (int i = 0; i < 4; ++i) asm volatile("global_load_dwordx4 a[%2:%2 + 3], %0, %1" :: "v"(vx2 + i * 32768u), "s"(p.x), "n"(200 + 4 * i) : "memory");
      PAGE(5, "adaln_fr");
      unsigned lane2 = lane; asm volatile("" : "+v"(lane2));
#define lane lane2
    unsigned wo0 = (32u * wid + r8) * 2048u + m * 256u + 64u * hi + 16u * ((lane & 3u) ^ (2u * (r8 >> 2)));
    __amdgpu_buffer_rsrc_t srdt = __builtin_amdgcn_make_buffer_rsrc((void*)((const char*)p.wo + (size_t)kg * (DM * DM * 2)), (short)0, DM * DM * 2, SRD_WORD3);
    __amdgpu_buffer_rsrc_t srdp = __builtin_amdgcn_make_buffer_rsrc((void*)((const char*)(y < 4u ? p.kc : p.vc) + (size_t)hd * (SEQ * 256) + (y & 3u) * 4096u), (short)0, (int)(SEQ * 256 - (y & 3u) * 4096u), SRD_WORD3);
    unsigned vg0 = (256u * wid + (lane >> 1)) * (DFF * 2u) + sl * 32u + 16u * (lane & 1u);
    unsigned vdn = (16u * sl + hi) * 2048u + 512u * wid + 16u * (lane & 31u);
    const unsigned mc0 = 2u * DM + 64u * ((idx >> 2) * 8u + (b & 7u)) + 16u * (idx & 3u);
    const unsigned md0 = (256u * wid + (lane >> 1)) * (NMOD * 2u) + mc0 * 2u + 16u * (lane & 1u);
    unsigned sd0 = ldsb + 131072u + 8192u + 512u * wid + 16u * g;
    fl = md0;
    unsigned rd0 = ldsb + 131072u + 7168u + 8192u * ((lane >> 2) & 3u) + 256u * wid + 4u * (lane & 3u);
    unsigned ta0 = m0t + 256u * g + 32u * ((lane >> 2) & 3u) + 8u * (lane & 3u);
    const unsigned c8 = (b * 4u + wid) * 8192u, cd = moe ? sl * 32768u + wid * 8192u : 0u, cw = (idx * 4u + wid) * 16384u;
    const unsigned cm = moe ? ((sl & 3u) * 4u + wid) * 64u * (DFF * 2u) + (sl >> 2) * 128u : 0u;
    unsigned vxs = BOX * (l16 >> 1) + 64u * (l16 & 1u) + 16u * g;
    const unsigned sxs = (unsigned)WS_XIN + (y * 128u + wid * 32u) * BOX + (m >> 1) * 1024u + kg * 256u + (m & 1u) * 128u;
    unsigned vx0 = ((256u * y + 8u * hd + 2u * wid + ((lane >> 2) & 1u)) * DM + 256u * hi + 128u * (l16 >> 3) + 16u * (g & 1u) + 4u * (lane & 3u) + 32u * yp2) * 2u;
    unsigned tlo = 0, thi = 0;
#define tab(ln, lo, hi2) asm("v_writelane_b32 %0, %2, " #ln "\n\tv_writelane_b32 %1, %3, " #ln : "+v"(tlo), "+v"(thi) : "s"(lo), "s"(hi2))
#define tabp(ln, q, off) do { const unsigned long long a_ = (size_t)(q) + (off); tab(ln, (unsigned)a_, (unsigned)(a_ >> 32)); } while (0)
#define TORD 0
    tabp(1, p.x, ((y * 32u + hd) * 2u + (wid & 1u)) * 8192u); tabp(2, p.wu, moe ? c8 : 0u); tabp(3, p.wo, c8); tabp(4, p.wd, cd);
    tabp(5, p.wo, kg * 2097152u + cw); tabp(6, p.wo, kg * 2097152u + cw + 8192u); tabp(7, p.wg, cm);
    tabp(17, p.wmod, 0u); tab(18, NP_FILL == 8 ? sin + 20480u : (unsigned)WS_XIN + ((y * 32u + kg * 8u + (m >> 1)) * 4u + wid) * BOX + 20480u + (m & 1u) * 8192u, (unsigned)WS_OFL + y * 16384u + kg * 128u);
    tab(19, (unsigned)WS_OFL + (y * 32u + hd) * 512u + wid * 128u, (unsigned)WS_OFL + (y * 32u + kg * 8u) * 512u + wid * 128u);
    tabp(8, p.wg, 0u); tabp(9, p.wu, 0u); tabp(10, p.wd, 0u);
    tab(11, m, m * 4u * BOX); tab(12, (y & 1u) * 128u, 128u - (y & 1u) * 128u);
    tab(13, sxl, (unsigned)WS_PFL + (y * 32u + hd) * 512u + kg * 128u); tab(14, sxs, sxf);
    tab(15, (unsigned)WS_MODT + (mc0 - 2u * DM + 4u * wid) * 4u, (mc0 >> 10) == 4u ? 0x3f800000u : 0u);
    tab(16, (unsigned)WS_PFL + y * 16384u + (hd * 4u + wid) * 4u, (unsigned)WS_PFL + (y * 32u + hd) * 512u + wid * 128u);
    const char* psig = p.ws + WS_CNT + y * 8192u + hd * 8u;
    const char* pcnt = p.ws + WS_CNT + y * 8192u + hd * 256u;
    tabp(20, p.wr, 0u); tab(21, (unsigned)WS_PART + b * 8192u + wid * 2048u, 0u);
      asm volatile("" : "+v"(tlo), "+v"(thi), "+v"(wo0), "+v"(vg0), "+v"(vdn), "+v"(ta0), "+v"(vxs), "+v"(vx0), "+v"(rd0), "+v"(fl), "+v"(sd0), "+v"(kb0), "+v"(vvb), "+v"(vwb), "+v"(kd0), "+v"(vd0), "+v"(wd0), "+v"(hq0), "+v"(oq0));
      asm volatile("" : "+s"(srdk), "+s"(srdv), "+s"(srdt), "+s"(srdp), "+s"(psig), "+s"(pcnt), "+s"(flag), "+s"(wbytes));
#undef lane
      u32x2 mp[8]; u32x4 rs;
      unsigned vlm = ((wid >> 1) * 16u + 2u * (hd & 7u) + (wid & 1u)) * 4096u + lane * 8u, vlr = (unsigned)WS_RST + ((rb0 + wid) * 16u + l16) * 16u;
#define LOOK9 "buffer_load_dwordx2 %0, %[vlm], %[srdz], %[som] offen sc1\n\tbuffer_load_dwordx2 %1, %[vlm], %[srdz], %[som] offen offset:512 sc1\n\tbuffer_load_dwordx2 %2, %[vlm], %[srdz], %[som] offen offset:1024 sc1\n\t" \
              "buffer_load_dwordx2 %3, %[vlm], %[srdz], %[som] offen offset:1536 sc1\n\tbuffer_load_dwordx2 %4, %[vlm], %[srdz], %[som] offen offset:2048 sc1\n\tbuffer_load_dwordx2 %5, %[vlm], %[srdz], %[som] offen offset:2560 sc1\n\t" \
              "buffer_load_dwordx2 %6, %[vlm], %[srdz], %[som] offen offset:3072 sc1\n\tbuffer_load_dwordx2 %7, %[vlm], %[srdz], %[som] offen offset:3584 sc1\n\tbuffer_load_dwordx4 %8, %[vlr], %[srdz], 0 offen sc1\n\t"
#define LREG(c) c "{v[0:1]}"(mp[0]), c "{v[2:3]}"(mp[1]), c "{v[4:5]}"(mp[2]), c "{v[6:7]}"(mp[3]), c "{v[8:9]}"(mp[4]), c "{v[10:11]}"(mp[5]), c "{v[12:13]}"(mp[6]), c "{v[14:15]}"(mp[7]), c "{v[16:19]}"(rs)
      asm volatile(LOOK9 : LREG("=&") : [vlm] "v"(vlm), [vlr] "v"(vlr), [srdz] "s"(srdz), [som] "s"((unsigned)WS_MOD) : "memory");
      #pragma unroll
      for (int q = 0; q < 14; ++q)
        asm volatile("s_mov_b32 m0, %0\n\ts_nop 0\n\tbuffer_load_dwordx4 %1, %2, %3 offen lds" :: "s"(m0b + 65536u + 16384u * (q >> 2) + 1024u * (q & 3)), "v"(wd0), "s"(srdw),
                     "s"(0x80000u * (q >> 2) + 65536u * ((q >> 1) & 1) + 128u * (q & 1)) : "m0", "memory");
      asm volatile("s_waitcnt vmcnt(23)" ::: "memory");
      { const u32x4 cs = XV(4); *(u32x2*)(lds + 131072 + 8192 + tid * 8u) = u32x2{silu2(cs[0]), silu2(cs[1])}; }
      f32x2 xu[16];
      #pragma unroll
      for (int i = 0; i < 4; ++i) { const u32x4 o = XV(i);
        #pragma unroll
        for (int e = 0; e < 4; ++e) { xu[4 * i + e] = unpk(o[e]); asm volatile("" : "+v"(xu[4 * i + e])); } }
      unsigned vpw = ldsb + 131072u + 4u * tid, vpr = ldsb + 131072u + 1024u + 32u * l16 + 8u * wid, vpc = ldsb + 131072u + 32u * (tid >> 4), vpq = ldsb + 131072u + 1024u + 32u * l16;
      unsigned vh = (unsigned)WS_H + rb0 * 32768u + ch * 256u + 16u * l16; float one = wid >> 1 ? 1.f : 0.f;
      {
        unsigned bl, bh, cnt = SPIN_CAP;
#define XO(d, r, t) "v_bitop3_b32 %[" #d "], v" #r ", %[" #t "], %[" #d "] bitop3:0xbe\n\t"
        asm volatile(
          "s_waitcnt vmcnt(14)\n\t"
          "s_branch Lm1_%=\n"
          "Lm0_%=:\n\t"
          "s_sleep " STR(ASLEEP) "\n\t"
          LOOK9
          "s_waitcnt vmcnt(0)\n"
          "Lm1_%=:\n\t"
          "v_bitop3_b32 %[bl], v0, v2, %[tl] bitop3:0x7e\n\t"
          "v_bitop3_b32 %[bh], v1, v3, %[th] bitop3:0x7e\n\t"
          XO(bl, 4, tl) XO(bh, 5, th) XO(bl, 6, tl) XO(bh, 7, th) XO(bl, 8, tl) XO(bh, 9, th) XO(bl, 10, tl) XO(bh, 11, th) XO(bl, 12, tl) XO(bh, 13, th) XO(bl, 14, tl) XO(bh, 15, th)
          XO(bl, 16, tl) XO(bh, 17, th) XO(bl, 18, tl) XO(bh, 19, th)
          "v_and_or_b32 %[bl], %[bl], %[ff], %[bh]\n\t"
          "v_cmp_ne_u32 vcc, 0, %[bl]\n\t"
          "s_sub_u32 %[cnt], %[cnt], 1\n\t"
          "s_cbranch_scc1 Lm2_%=\n\t"
          "s_cbranch_vccnz Lm0_%=\n"
          "Lm2_%=:"
          : LREG("+"), [bl] "=&v"(bl), [bh] "=&v"(bh), [cnt] "+s"(cnt), "+v"(vpw), "+v"(vpr), "+v"(vpc), "+v"(vpq), "+v"(vh), "+v"(one)
          : [vlm] "v"(vlm), [vlr] "v"(vlr), [srdz] "s"(srdz), [som] "s"((unsigned)WS_MOD), [tl] "s"(tl), [th] "s"(th), [ff] "v"(0xffu) : "vcc", "scc", "memory");
      }
      f32x4 a0, a1, c0, c1, t0, t1;
      {
        auto val = [&](int k) { return __uint_as_float(mp[k][0] & 0xffffff00u); };
        const float v = rbh(one + rbh(((val(0) + val(1)) + (val(2) + val(3))) + ((val(4) + val(5)) + (val(6) + val(7)))));
        const float rd = __builtin_amdgcn_rcpf(hi16(rs[2]));
        asm volatile("ds_write_b32 %0, %1\n\tds_write_b64 %2, %3" :: "v"(vpw), "v"(v * QSCALE), "v"(vpr), "v"(f32x2{-hi16(rs[0]) * rd, rd}) : "memory");
      }
      PAGE(6, "adaln_fr");
      asm volatile("s_waitcnt lgkmcnt(0)\n\ts_barrier\n\tds_read_b128 %4, %7\n\tds_read_b128 %0, %6\n\tds_read_b128 %2, %6 offset:512\n\tds_read_b128 %1, %6 offset:16\n\tds_read_b128 %3, %6 offset:528\n\tds_read_b128 %5, %7 offset:16\n\t"
                   "buffer_load_dword a255, %8, %9, %10 offen\n\ts_waitcnt lgkmcnt(0)"
                   : "=&v"(a0), "=&v"(a1), "=&v"(c0), "=&v"(c1), "=&v"(t0), "=&v"(t1) : "v"(vpc), "v"(vpq), "v"(tq0), "s"(srdw), "s"(0x200000u) : "memory");
      FQ(0);
      #pragma unroll
      for (int i = 0; i < 4; ++i) {
        const float mn = i < 2 ? t0[2 * i] : t1[2 * i - 4], rd = i < 2 ? t0[2 * i + 1] : t1[2 * i - 3];
        u32x4 o;
        #pragma unroll
        for (int e = 0; e < 4; ++e) {
          const f32x2 sc = e < 2 ? f32x2{c0[2 * e], c0[2 * e + 1]} : f32x2{c1[2 * e - 4], c1[2 * e - 3]}, sh = e < 2 ? f32x2{a0[2 * e], a0[2 * e + 1]} : f32x2{a1[2 * e - 4], a1[2 * e - 3]};
          o[e] = cvtpk2(__builtin_elementwise_fma(__builtin_elementwise_fma(xu[4 * i + e], f32x2{rd, rd}, f32x2{mn, mn}), sc, sh));
        }
        asm volatile("buffer_store_dwordx4 %0, %1, %2, %3 offen sc1\n\ts_nop 1" :: "v"(o), "v"(vh), "s"(srdz), "s"(32768u * i) : "memory");
      }
      FQ(1);
    asm volatile(FA_BODY
      : FA_OUTS
      : [kb0] "v"(kb0), [vvb] "v"(vvb), [kd0] "v"(kd0), [vd0] "v"(vd0), [oq0] "v"(oq0),
        [m0b] "s"(m0b), [flag] "s"(flag), [srdz] "s"(srdz), [srdw] "s"(srdw), [srdk] "s"(srdk), [srdv] "s"(srdv), [m0t] "s"(m0t), [srdt] "s"(srdt), [srdp] "s"(srdp),
        [tag] "s"(tag), [pcnt] "s"(pcnt), [psig] "s"(psig), [wid] "s"(wid), [rd0] "v"(rd0), [wo0] "v"(wo0), [vg0] "v"(vg0), [vx0] "v"(vx0), [px] "s"(p.x), [vct] "v"(vct), [pct] "s"(pct), [sin] "s"(sin), [vdn] "v"(vdn), [ta0] "v"(ta0), [vxs] "v"(vxs),
        [tlo] "v"(tlo), [thi] "v"(thi), [wbytes] "s"(wbytes)
      : FA_CLOBBERS);
  }
#define RDELAYN 22
#define RDELAY1 2
#define RDELAY2 6
#define RCHKN 5
#define PRE1 16
#define EARLYOUT 1
  unsigned lane; asm volatile("v_mbcnt_lo_u32_b32 %0, -1, 0\n\tv_mbcnt_hi_u32_b32 %0, -1, %0" : "=v"(lane));
  const unsigned tid = wid * 64u + lane, n = lane & 15u, g = lane >> 4;
  const unsigned r0 = 256u * y + 8u * hd + 2u * wid, row = r0 + ((n >> 2) & 1u);
  const unsigned colb = 256u * (g >> 1) + 128u * (n >> 3) + 16u * (g & 1u) + 4u * (n & 3u);
  char* par = lds;
  const unsigned cq0 = colb + 32u * yp2, cq1 = colb + 32u * (2u ^ yp2);
  auto parq = [&](int o, int k) { return *(const u32x2*)(par + o + (k >> 2 ? cq1 : cq0) * 2u + 64 * ((k >> 1) & 1) + 1024 * (k & 1)); };
  auto parf = [&](int o, int k) { return *(const f32x4*)(par + o + (k >> 2 ? cq1 : cq0) * 4u + 128 * ((k >> 1) & 1) + 2048 * (k & 1)); };
  if (__builtin_expect(__any(late != 0u), 0)) {
    u32x4 mt[4]; u32x2 wr;
    asm volatile("s_barrier" ::: "memory");
    for (unsigned spins = 0; spins <= SPIN_CAP; ++spins) {
      asm volatile("s_nop 4\n\tglobal_load_dwordx2 %0, %1, %2" : "=v"(wr) : "v"(tid * 8u), "s"(p.wr) : "memory");
      #pragma unroll
      for (int k = 0; k < 4; ++k) asm volatile("buffer_load_dwordx4 %0, %1, %2, %3 offen sc1" : "=v"(mt[k]) : "v"(tid * 16u), "s"(srdz), "s"((unsigned)WS_MODT + 4096u * k) : "memory");
      asm volatile("s_waitcnt vmcnt(0)" : "+v"(mt[0]), "+v"(mt[1]), "+v"(mt[2]), "+v"(mt[3]), "+v"(wr) :: "memory");
      const u32x4 t = mt[0] & mt[1] & mt[2] & mt[3];
      if (__all((t[0] & t[1] & t[2] & t[3] & 1u) != 0u)) break;
    }
    const f32x4 a = __builtin_bit_cast(f32x4, mt[2]) * f32x4{lo16(wr[0]), hi16(wr[0]), lo16(wr[1]), hi16(wr[1])};
    *(u32x4*)(par + tid * 16u) = mt[0]; *(f32x4*)(par + 4096 + tid * 16u) = a;
    #pragma unroll
    for (int k = 1; k < 4; ++k) *(u32x2*)(par + (k == 3 ? 14336 : 6144 + 2048 * k) + tid * 8u) = u32x2{__builtin_amdgcn_perm(mt[k][1], mt[k][0], 0x07060302u), __builtin_amdgcn_perm(mt[k][3], mt[k][2], 0x07060302u)};
    *(float*)(par + 12424 + 16u * wid) = wave_sum((a[0] + a[1]) + (a[2] + a[3]));
    asm volatile("s_waitcnt lgkmcnt(0)\n\ts_barrier" ::: "memory");
    #pragma unroll
    for (int k = 0; k < 8; ++k) { gaf[k] = parf(0, k); aw[k] = parf(4096, k); }
    arow = dpp_add<DPP_XOR2>(dpp_add<DPP_XOR1>(*(const float*)(par + 12424 + 16u * (lane & 3u))));
  }
  unsigned vin = lane * 16u, vin3 = lane * 16u + 12288u, vin2 = lane * 16u + 8192u, kp = (unsigned)WS_KEYS + (KEY_SEL & (KEY_REP - 1u)) * 2048u + 32u * lane;
  TQ(0);
  f32x2 x1[16], s2, d2, q2, sbf[16], gbf[16], c1f[16];
  unsigned vkey, vkb, vkr, nrow, vpub, ta, tu, tb; char* pk;
  {
#define SUM2(R) asm volatile("s_nop 1\n\tv_mfma_f32_16x16x32_bf16 %0, %2, a[" #R ":" #R "+3], 0\n\tv_mfma_f32_16x16x32_bf16 %1, %2, a[" #R "+8:" #R "+11], 0\n\t" \
                             "v_mfma_f32_16x16x32_bf16 %0, %3, a[" #R "+4:" #R "+7], %0\n\tv_mfma_f32_16x16x32_bf16 %1, %3, a[" #R "+12:" #R "+15], %1\n\ts_nop 7" : "=&v"(d[0]), "=&v"(d[1]) : "v"(alo), "v"(ahi))
    auto part_ = [&](int c2, f32x2& sa, f32x2& da, f32x2& qa) __attribute__((always_inline)) {
      f32x4 d[2];
      if (c2 == 0) SUM2(64); else if (c2 == 1) SUM2(80); else if (c2 == 2) SUM2(96); else SUM2(112);
      #pragma unroll
      for (int i = 4 * c2; i < 4 * c2 + 4; ++i) {
        const int e = 2 * (i & 1);
        const f32x2 v = rb2(__builtin_elementwise_fma(f32x2{d[(i >> 1) & 1][e], d[(i >> 1) & 1][e + 1]}, f32x2{gaf[i >> 1][e], gaf[i >> 1][e + 1]}, xf[i]));
        x1[i] = v;
        sa += v; da = __builtin_elementwise_fma(v, f32x2{aw[i >> 1][e], aw[i >> 1][e + 1]}, da); qa = __builtin_elementwise_fma(v, v, qa);
      }
    };
#define RCV4(o, a, b, c, d) \
      "buffer_load_dwordx4 " #a ", %[vin" #o "], %[srdz], %[sin] offen sc1\n\t" \
      "buffer_load_dwordx4 " #b ", %[vin" #o "], %[srdz], %[sin] offen offset:1024 sc1\n\t" \
      "buffer_load_dwordx4 " #c ", %[vin" #o "], %[srdz], %[sin] offen offset:2048 sc1\n\t" \
      "buffer_load_dwordx4 " #d ", %[vin" #o "], %[srdz], %[sin] offen offset:3072 sc1\n\t"
#define RWAIT(lv, off, late) \
      "Lrq" #lv "_%=:\n\t" \
      "v_mov_b32 %[fn], 0\n\t" \
      "s_mov_b32 %[sc], " STR(RCHKN) "\n\t" \
      "buffer_load_dword %[fn], %[vfl], %[srdz], %[sfl] offen offset:" #off " sc1\n" \
      "Lrs" #lv "_%=:\n\t" \
      "ds_read_b32 %[tmp], %[vsh]\n\t" \
      "s_waitcnt lgkmcnt(0)\n\t" \
      "v_and_or_b32 %[tmp], %[tmp], " #lv ", %[fn]\n\t" \
      "v_cmp_eq_u32 vcc, 0, %[tmp]\n\t" \
      "s_cbranch_vccz Lrd" #lv "_%=\n\t" \
      "s_sub_u32 %[sc], %[sc], 1\n\t" \
      "s_cbranch_scc0 Lrs" #lv "_%=\n\t" \
      "s_sub_u32 %[cnt], %[cnt], 1\n\t" \
      "s_cselect_b32 %[cnt], 0, %[cnt]\n\t" \
      "s_waitcnt vmcnt(0)\n\t" \
      "v_cmp_eq_u32 vcc, 0, %[fn]\n\t" \
      "s_cbranch_scc1 Lrl" #lv "_%=\n\t" \
      "s_cbranch_vccnz Lrq" #lv "_%=\n" \
      "Lrd" #lv "_%=:\n\t" \
      "v_mov_b32 %[tmp], " #lv "\n\t" \
      "s_mov_b64 exec, 1\n\t" \
      "ds_or_b32 %[vsh], %[tmp]\n\t" \
      "s_mov_b64 exec, -1\n\t" \
      "s_sleep " late "\n" \
      "Lrl" #lv "_%=:\n\t"
#define RCV4C RCV4(2, a[96:99], a[100:103], a[104:107], a[108:111])
#define RCV4D RCV4(3, a[112:115], a[116:119], a[120:123], a[124:127])
#define RCV8B RCV4C RCV4D
    unsigned cnt, tmp, fn = 0, sc;
    u32x2 sbq[8], gbq[8], c1q[8];
    #pragma unroll
    for (int k = 0; k < 8; ++k) { sbq[k] = parq(8192, k); c1q[k] = parq(10240, k); gbq[k] = parq(14336, k); }
    #pragma unroll
    for (int i = 0; i < PRE1; ++i) { sbf[i] = unpk(sbq[i >> 1][i & 1]); c1f[i] = unpk(c1q[i >> 1][i & 1]); asm volatile("" : "+v"(sbf[i]), "+v"(c1f[i])); }
    unsigned vfl = (lane & 31u) * 512u; const unsigned sfl = (unsigned)WS_OFL + y * 16384u + kg * 128u;
    PAGE(1, "adaln_pg2");
#define LOOK2 "buffer_load_dword %[fl], %[vfl], %[srdz], %[sfl] offen offset:68 sc1\n\t"
    asm volatile(
      "s_mov_b32 %[go], 0\n\t"
      "s_cmp_lg_u32 %[got], 0\n\t"
      "s_waitcnt vmcnt(4)\n\t"
      "s_cbranch_scc0 Lrn_%=\n\t"
      "v_cmp_eq_u32 vcc, 0, %[fl]\n\t"
      "s_cbranch_vccz Lrg_%=\n\t"
      LOOK2
      "s_branch Lrn_%=\n"
      "Lrg_%=:\n\t"
      "s_mov_b32 %[go], 1\n\t"
      RCV4C
      "Lrn_%=:"
      : [fl] "+v"(fl), [go] "=&s"(got2), [vfl] "+v"(vfl), "+v"(gbq[0]), "+v"(gbq[1]), "+v"(gbq[2]), "+v"(gbq[3]), "+v"(gbq[4]), "+v"(gbq[5]), "+v"(gbq[6]), "+v"(gbq[7])
      : [srdz] "s"(srdz), [got] "s"(got), [sfl] "s"(sfl), [sin] "s"(sin), [vin2] "v"(vin2) : "vcc", "scc", "memory");
#define NRE
    s2 = f32x2{0.f, 0.f}; d2 = s2; q2 = s2;
    f32x2 s3 = s2, d3 = s2, q3 = s2;
    asm volatile(
      "s_mov_b32 %[cnt], 0x10000\n\t"
      "s_cmp_lg_u32 %[got], 0\n\t"
      "s_cbranch_scc1 Lrn_%=\n\t"
      RWAIT(2, 64, "0") RCV4(0, a[64:67], a[68:71], a[72:75], a[76:79]) RCV4(1, a[80:83], a[84:87], a[88:91], a[92:95])
      LOOK2
      "s_waitcnt vmcnt(0)\n\t"
      "v_cmp_eq_u32 vcc, 0, %[fl]\n\t"
      "s_cbranch_vccnz Lrn_%=\n\t"
      "s_mov_b32 %[go], 1\n\t"
      RCV4C
      "Lrn_%=:"
      : [cnt] "=&s"(cnt), [tmp] "=&v"(tmp), [fn] "+v"(fn), [sc] "=&s"(sc), [fl] "+v"(fl), [go] "+s"(got2), "+v"(s2)
      : [srdz] "s"(srdz), [got] "s"(got), [vfl] "v"(vfl), [sfl] "s"(sfl), [sin] "s"(sin), [vin0] "v"(vin), [vin1] "v"(lane * 16u + 4096u), [vin2] "v"(vin2), [vsh] "v"(ldsb + 12420u)
      : "vcc", "scc", "memory");
    part_(0, s2, d2, q2); part_(1, s2, d2, q2);
    TQ(2);
    asm volatile(
      "s_cmp_lg_u32 %[go], 0\n\t"
      "s_cbranch_scc1 Lrn_%=\n\t"
      "s_waitcnt vmcnt(0)\n\t"
      "v_cmp_eq_u32 vcc, 0, %[fl]\n\t"
      "s_cbranch_vccz Lrm_%=\n\t"
      RWAIT(4, 68, STR(RDELAY1))
      RCV4C
      "s_sleep " STR(RDELAYN) "\n\t"
      "s_branch Lro_%=\n"
      "Lrm_%=:\n\t"
      RCV4C
      "Lrn_%=:\n\t"
      "s_sleep " STR(RDELAY2) "\n"
      "Lro_%=:\n\t"
      RCV4D
      : [cnt] "+s"(cnt), [tmp] "=&v"(tmp), [fn] "+v"(fn), [sc] "=&s"(sc), [fl] "+v"(fl), "+v"(s2), "+v"(d2), "+v"(q2), "+v"(gbq[0]), "+v"(gbq[1]), "+v"(gbq[2]), "+v"(gbq[3]), "+v"(gbq[4]), "+v"(gbq[5]), "+v"(gbq[6]), "+v"(gbq[7])
      : [srdz] "s"(srdz), [go] "s"(got2), [vfl] "v"(vfl), [sfl] "s"(sfl), [sin] "s"(sin), [vin2] "v"(vin2), [vin3] "v"(vin3), [vsh] "v"(ldsb + 12420u)
      : "vcc", "scc", "memory");
    #pragma unroll
    for (int i = 0; i < 16; ++i) { gbf[i] = unpk(gbq[i >> 1][i & 1]); if (i >= PRE1) { sbf[i] = unpk(sbq[i >> 1][i & 1]); c1f[i] = unpk(c1q[i >> 1][i & 1]); } asm volatile("" : "+v"(gbf[i]), "+v"(c1f[i]), "+v"(sbf[i])); }
    vkey = lane * 2048u + b * 8u; vkb = ldsb + 12288u + 8u * wid + (lane & 4u); vkr = ldsb + 12288u + 4u * (lane & 7u); nrow = 2047u - row;
    vpub = (unsigned)WS_XIN + (y * 32u + hd) * 4u * BOX + 16384u + (g >> 1) * 512u + ((n >> 3) * 8u + (g & 1u) * 4u + (n & 3u)) * 16u;
    ta = m0b + 65536u + 8u * lane; tu = m0t + 8u * lane; tb = m0b + 98304u + 16384u * (g >> 1) + 2048u * (g & 1u) + 512u * ((lane >> 2) & 3u) + 8u * (lane & 3u);
    pk = p.ws + WS_KEYS;
    asm volatile("s_waitcnt vmcnt(4)" : "+v"(fn), "+v"(s3), "+v"(vkey), "+v"(vkb), "+v"(vkr), "+v"(nrow), "+v"(vpub), "+v"(ta), "+v"(tu), "+v"(tb), "+s"(pk) :: "memory");
    part_(2, s3, d3, q3);
    asm volatile("s_waitcnt vmcnt(0)" : "+v"(s3), "+v"(d3), "+v"(q3) :: "memory");
    TQ(3);
    part_(3, s3, d3, q3);
    if (__builtin_expect(__any(s3[0] + s3[1] != s3[0] + s3[1]), 0)) {
      unsigned spins = 0;
      do {
        asm volatile(RCV8B "s_waitcnt vmcnt(0)" :: [srdz] "s"(srdz), [sin] "s"(sin), [vin2] "v"(vin2), [vin3] "v"(vin3) : "memory"); NRE;
        s3 = f32x2{0.f, 0.f}; d3 = s3; q3 = s3;
        part_(2, s3, d3, q3); part_(3, s3, d3, q3);
      } while (__any(s3[0] + s3[1] != s3[0] + s3[1]) && ++spins < RETRY_CAP);
    }
    asm volatile("; all in");
    s2 += s3; d2 += d3; q2 += q3;
  }
  float mrow, rdrow, drow;
  {
    const float sv = s2[0] + s2[1], dv = d2[0] + d2[1], qv = q2[0] + q2[1];
    float u = swap16_add(swap32_add(sv, dv), swap32_add(qv, qv));
    u = dpp_add<DPP_XOR1>(u); u = dpp_add<DPP_XOR2>(u); u = dpp_add<DPP_ROR8>(u);
    const auto r16 = __builtin_amdgcn_permlane16_swap(__float_as_uint(u), __float_as_uint(u), false, false);
    const auto r32 = __builtin_amdgcn_permlane32_swap(r16[0], r16[0], false, false);
    const float mean = __uint_as_float(r32[0]) * (1.f / DM);
    drow = __uint_as_float(r32[1]);
    float var = __uint_as_float(r16[1]) * (1.f / DM) - mean * mean;
    if (__builtin_expect(__any(!(mean * mean <= var)), 0)) {
      f32x2 qq = {0.f, 0.f};
      #pragma unroll
      for (int e = 0; e < 16; ++e) { const f32x2 t = x1[e] - mean; qq = __builtin_elementwise_fma(t, t, qq); }
      var = row_sum(qq[0] + qq[1]) * (1.f / DM);
    }
    mrow = rbh(mean); rdrow = __builtin_amdgcn_rcpf(rbh(__builtin_amdgcn_sqrtf(rbh(rbh(var) + 1e-5f))));
  }
  unsigned* kb = (unsigned*)(lds + 12288);
  unsigned mykey, k1w;
  { const unsigned bb = __float_as_uint(fmaf(-mrow, arow, drow) * rdrow); mykey = max((bb ^ ((unsigned)((int)bb >> 31) | 0x80000000u)) & 0xfffff800u, 0x800u) | nrow; }
  asm volatile("ds_write_b32 %1, %2\n\ts_waitcnt lgkmcnt(0)\n\ts_barrier\n\tds_read_b32 %0, %3\n\ts_waitcnt lgkmcnt(0)" : "=&v"(k1w) : "v"(vkb), "v"(mykey), "v"(vkr) : "memory");
  bool cand;
  u32x4 ka, kc;
#define KSC " sc1"
  {
    unsigned k1 = k1w, k2 = 0;
    top2_dpp<DPP_XOR1>(k1, k2); top2_dpp<DPP_XOR2>(k1, k2); top2_dpp<0x141>(k1, k2);
    asm volatile("s_cmp_lg_u32 %[wid], 0\n\ts_cbranch_scc1 Lka_%=\n\ts_mov_b64 exec, %[mk]\n\tglobal_atomic_or_x2 %[va], %[kk], %[pk]" KSC "\n\ts_mov_b64 exec, -1\nLka_%=:"
                 :: [wid] "s"(wid), [mk] "n"((1u << KEY_REP) - 1u), [va] "v"(vkey), [kk] "v"(u32x2{k2, k1}), [pk] "s"(pk) : "scc", "memory");
    cand = mykey >= k2;
    vpub += mykey == k1 ? 0u : 2u * BOX;
  }
  TQ(4);
  PAGE(2, "adaln_pg3");
#define TRA(r, a, o) asm volatile("ds_read_b64_tr_b16 a[%0:%0 + 1], %1 offset:%2" :: "n"(r), "v"(a), "n"(o) : "memory")
#define FRAGS(k) do { \
      TRA(128 + 8 * (k), ta, 2048 * ((k) >> 2) + 512 * ((k) & 1) + 16384 * (((k) >> 1) & 1)); TRA(130 + 8 * (k), ta, 2048 * ((k) >> 2) + 512 * ((k) & 1) + 16384 * (((k) >> 1) & 1) + 1024); \
      TRA(132 + 8 * (k), tu, 2048 * ((k) >> 2) + 512 * ((k) & 1) + 4096 * (((k) >> 1) & 1)); TRA(134 + 8 * (k), tu, 2048 * ((k) >> 2) + 512 * ((k) & 1) + 4096 * (((k) >> 1) & 1) + 1024); \
      TRA(192 + 4 * (k), tb, 64 * (k)); TRA(194 + 4 * (k), tb, 64 * (k) + 32); } while (0)
  f32x2 h2[16];
  {
    asm volatile("" : "+v"(mrow));
    const float nm = -mrow * rdrow;
    #pragma unroll
    for (int k = 0; k < 8; ++k) {
      #pragma unroll
      for (int e = 0; e < 2; ++e) { const int i = 2 * k + e; h2[i] = __builtin_elementwise_fma(__builtin_elementwise_fma(x1[i], f32x2{rdrow, rdrow}, f32x2{nm, nm}), c1f[i], sbf[i]); }
      if (k & 1) { asm volatile("" : "+v"(h2[2 * k]), "+v"(h2[2 * k + 1])); FRAGS(k >> 1); }
    }
  }
  const unsigned cw = __builtin_amdgcn_readfirstlane((unsigned)__ballot(cand) & 0x11u);
  auto publish = [&](const f32x2* v, unsigned off) __attribute__((always_inline)) {
    #pragma unroll
    for (int k = 0; k < 4; ++k) { const int q = 4 * (k >> 1) + (k & 1);
      asm volatile("buffer_store_dwordx4 %0, %1, %2, %3 offen sc1\n\ts_nop 1" :: "v"(u32x4{cvtpk2(v[2 * q]), cvtpk2(v[2 * q + 1]), cvtpk2(v[2 * q + 4]), cvtpk2(v[2 * q + 5])}), "v"(vpub), "s"(srdz), "s"(off + (k & 1) * BOX + (((k >> 1) * 2u) ^ yp2) * 128u) : "memory"); }
  };
  if (cand) publish(h2, 0u);
  char* st = lds + 32768 + 4096u * wid;
  auto look1 = [&]() __attribute__((always_inline)) {
    asm volatile("s_cmp_lg_u32 %2, 0\n\ts_cbranch_scc1 Lkl_%=\n\tbuffer_load_dwordx4 %0, %3, %4, 0 offen sc1\n\tbuffer_load_dwordx4 %1, %3, %4, 0 offen offset:16 sc1\nLkl_%=:" : "=&v"(ka), "=&v"(kc) : "s"(wid), "v"(kp), "s"(srdz) : "scc", "memory");
  };
  if (KLOOK == 0) look1();
  #pragma unroll
  for (int k = 0; k < 8; ++k) {
    u32x2 yq;
    #pragma unroll
    for (int e = 0; e < 2; ++e) yq[e] = cvtpk2(__builtin_elementwise_fma(gbf[2 * k + e], h2[2 * k + e], x1[2 * k + e]));
    *(u32x2*)(st + (row & 1u) * 2048u + (colb + 32u * ((k >> 2) * 2u ^ yp2)) * 2u + 64 * ((k >> 1) & 1) + 1024 * (k & 1)) = yq;
    if (k & 1) FRAGS(4 + (k >> 1));
    if (k == KLOOK - 1) look1();
  }
  auto out_rows = [&](unsigned which) __attribute__((always_inline)) {
    #pragma unroll
    for (int r = 0; r < 2; ++r) {
      if ((which >> (4 * r)) & 1u) {
        const u32x4 a = *(const u32x4*)(st + 2048 * r + 16u * lane), c = *(const u32x4*)(st + 2048 * r + 1024 + 16u * lane);
        asm volatile("s_nop 4\n\tglobal_store_dwordx4 %0, %1, %3 offset:%4 sc1\n\tglobal_store_dwordx4 %0, %2, %3 offset:%5 sc1\n\ts_nop 1" :: "v"(r0 * 2048u + 16u * lane), "v"(a), "v"(c), "s"(pout), "n"(2048 * r), "n"(2048 * r + 1024) : "memory");
      }
    }
  };
  const unsigned un = tid & 127u, ca = 256u * (un >> 5) + 64u * ((un >> 4) & 1u) + 128u * ((un >> 3) & 1u) + 16u * ((un >> 2) & 1u) + 4u * (un & 3u);
  unsigned vrow = (unsigned)WS_XIN + 16384u + (moe ? (wid >> 1) * BOX + (wid & 1u) * 512u + 16u * g : (un >> 6) * BOX + (un & 63u) * 16u), vsel = ldsb + 12288u + 40u + 4u * (moe ? lane & 1u : wid >> 1);
  unsigned mk[8];
  #pragma unroll
  for (int t = 0; t < 8; ++t) { mk[t] = (unsigned)__builtin_amdgcn_sbfe((int)(1u << (n >> 1)), t, 1); asm volatile("" : "+v"(mk[t])); }
  unsigned vred = ldsb + 24576u + wid * 256u + (lane & 1u) * 64u + 16u * g, vrf = ldsb + 24576u + ((lane & 1u) * 4u + g) * 16u + ((lane >> 1) & 3u) * 4u, vpt = (unsigned)WS_PART + b * 8192u + wid * 2048u + lane * 16u, vkk = ldsb + 12288u + 32u;
  unsigned long long m2 = 0x0003000300030003ull;
  asm volatile("" : "+v"(vrow), "+v"(vsel), "+v"(vred), "+v"(vrf), "+v"(vpt), "+v"(vkk), "+s"(m2));
  const unsigned later = __builtin_amdgcn_readfirstlane(wid && EARLYOUT ? cw : 0x11u);
  out_rows(~later);
  TQ(1);
  PAGE(3, "adaln_pg8");
  if (wid == 0) {
    asm volatile("s_waitcnt vmcnt(0)\nLk0_%=:" : "+v"(ka), "+v"(kc) :: "memory");
    for (unsigned spins = 0;; ++spins) {
      const u32x4 t = __builtin_elementwise_min(ka, kc);
      if (__all(min(min(t[0], t[1]), min(t[2], t[3])) != 0u) || spins > SPIN_CAP) break;
      __builtin_amdgcn_s_sleep(KSLEEP);
      asm volatile("buffer_load_dwordx4 %0, %2, %3, 0 offen sc1\n\tbuffer_load_dwordx4 %1, %2, %3, 0 offen offset:16 sc1\n\ts_waitcnt vmcnt(0)" : "=&v"(ka), "=&v"(kc) : "v"(kp), "s"(srdz) : "memory");
    }
    unsigned k1 = ka[1], k2 = ka[0];
    unsigned l1 = kc[1], l2 = kc[0];
    top2_merge(k1, k2, ka[3], ka[2]); top2_merge(l1, l2, kc[3], kc[2]); top2_merge(k1, k2, l1, l2);
    top2_dpp<DPP_XOR1>(k1, k2); top2_dpp<DPP_XOR2>(k1, k2); top2_dpp<0x141>(k1, k2); top2_dpp<0x140>(k1, k2);
    { auto r1 = __builtin_amdgcn_permlane16_swap(k1, k1, false, false); auto r2 = __builtin_amdgcn_permlane16_swap(k2, k2, false, false); k1 = r1[0]; k2 = r2[0]; top2_merge(k1, k2, r1[1], r2[1]); }
    { auto r1 = __builtin_amdgcn_permlane32_swap(k1, k1, false, false); auto r2 = __builtin_amdgcn_permlane32_swap(k2, k2, false, false); k1 = r1[0]; k2 = r2[0]; top2_merge(k1, k2, r1[1], r2[1]); }
    k1 = ~k1 & 2047u; k2 = ~k2 & 2047u;
    *(u32x4*)(kb + 8) = u32x4{k1, k2, (k1 >> 3) * (4u * BOX), (k2 >> 3) * (4u * BOX) + ((k1 ^ k2) < 8u ? 2u * BOX : 0u)};
  }
  u32x2 kk; unsigned vo;
  asm volatile("s_waitcnt lgkmcnt(0)\n\ts_barrier\n\tds_read_b32 %0, %3\n\tds_read_b64 %1, %4\n\ts_waitcnt lgkmcnt(0)\n\tv_add_u32 %2, %2, %0" : "=&v"(vo), "=&v"(kk), "+v"(vrow) : "v"(vsel), "v"(vkk) : "memory");
  TQ(5);
  float* part = (float*)(p.ws + WS_PART);
  unsigned k1, k2, rest;
  auto routed = [&]() __attribute__((always_inline)) {
    asm volatile("" : "+v"(kk));
    k1 = __builtin_amdgcn_readfirstlane(kk[0]); k2 = __builtin_amdgcn_readfirstlane(kk[1]);
    rest = __builtin_amdgcn_readfirstlane(later & ((r0 != k1 && r0 != k2 ? 1u : 0u) | (r0 + 1u != k1 && r0 + 1u != k2 ? 16u : 0u)));
    if (__builtin_expect(rest != later, 0)) { if (row == k1 || row == k2) publish(x1, 20480u); }
  };
  if (__builtin_expect(!moe, 0)) {
    asm volatile("adaln_pg8:\n\ts_cbranch_execz adaln_pg4");
    PAGE(4, "adaln_kr");
    u32x4 ha; u32x2 w0, w1;
    asm volatile("s_nop 4\n\tglobal_load_dwordx2 %0, %2, %3\n\tglobal_load_dwordx2 %1, %2, %3 offset:64" : "=&v"(w0), "=&v"(w1) : "v"(ca * 2u), "s"(p.wr) : "memory");
    asm volatile("buffer_load_dwordx4 %0, %3, %4, 0 offen sc1\n\ts_waitcnt vmcnt(0)" : "=&v"(ha), "+v"(w0), "+v"(w1) : "v"(vrow), "s"(srdz) : "memory");
    for (unsigned spins = 0; __builtin_expect(__any(max(max(ha[0], ha[1]), max(ha[2], ha[3])) == 0xffffffffu), 0) && spins < RETRY_CAP; ++spins)
      asm volatile("buffer_load_dwordx4 %0, %1, %2, 0 offen sc1\n\ts_waitcnt vmcnt(0)" : "=&v"(ha) : "v"(vrow), "s"(srdz) : "memory");
    routed();
    if (b == 255u && tid == 0u) __hip_atomic_store((gu64*)(p.ws + WS_EP), tag, RLX_AGENT);
    char* rows = lds + 16384 + (tid >> 7) * 2048u + ca * 2u;
    *(u32x2*)rows = u32x2{ha[0], ha[1]}; *(u32x2*)(rows + 64) = u32x2{ha[2], ha[3]};
    {
      float dot = 0.f;
      #pragma unroll
      for (int e = 0; e < 4; ++e) { const unsigned w = e < 2 ? w0[e] : w1[e - 2]; dot = fmaf(hi16(ha[e]), hi16(w), fmaf(lo16(ha[e]), lo16(w), dot)); }
      dot = wave_sum(dot);
      if (lane == 0) kb[16u + wid] = __float_as_uint(dot);
    }
    asm volatile("s_waitcnt lgkmcnt(0)\n\ts_barrier" ::: "memory");
    TQ(6);
    const unsigned pp = (b - MOE_WGS) * 64u + lane, sq = pp & 1u, nn = 128u * (pp >> 8) + 16u * (2u * ((pp >> 6) & 3u) + ((pp >> 1) & 1u)) + ((pp >> 2) & 15u);
    float gbv = bf2f(*(const u16*)(par + 14336 + nn * 2u)), hv = bf2f(*(const u16*)(lds + 16384 + sq * 2048u + nn * 2u));
    float rsel = rbf(__builtin_amdgcn_rcpf(1.f + __builtin_amdgcn_exp2f(rbf(__uint_as_float(kb[16u + 2u * sq]) + __uint_as_float(kb[17u + 2u * sq])) * -1.44269504089f)));
    u16* vout = pout + (sq ? k2 : k1) * DM + nn;
    unsigned vp = (unsigned)WS_PART + (tid >> 4) * 8192u + (b - MOE_WGS) * 256u + (tid & 15u) * 16u;
    unsigned vw = ldsb + 24576u + wid * 256u + (lane & 15u) * 16u + 4u * g, vr = ldsb + 24576u + 4u * lane;
    unsigned vx = (unsigned)WS_XIN + 36864u + kb[10u + sq] + (nn >> 9) * BOX + (32u * ((nn >> 8) & 1u) + 16u * ((nn >> 6) & 1u) + 8u * ((nn >> 7) & 1u) + 4u * ((nn >> 4) & 1u) + ((nn >> 2) & 3u)) * 16u + ((nn >> 5) & 1u) * 8u + (nn & 3u) * 2u;
    asm volatile("" : "+v"(gbv), "+v"(hv), "+v"(rsel), "+v"(vout), "+v"(vp), "+v"(vw), "+v"(vr), "+v"(vx));
    out_rows(rest);
    TQ(7);
    __builtin_amdgcn_s_sleep(FSLEEP);
    float u; unsigned xw;
    for (unsigned spins = 0;; ++spins) {
      f32x4 v[14];
      asm volatile("buffer_load_ushort %0, %1, %2, 0 offen sc1" : "=v"(xw) : "v"(vx), "s"(srdz) : "memory");
      #pragma unroll
      for (int k = 0; k < 14; ++k) asm volatile("buffer_load_dwordx4 %0, %1, %2, %3 offen sc1" : "=v"(v[k]) : "v"(vp), "s"(srdz), "s"(131072u * k) : "memory");
      asm volatile("s_waitcnt vmcnt(12)" : "+v"(v[0]), "+v"(v[1]), "+v"(xw) :: "memory");
      f32x4 sum = v[0] + v[1];
#define NEXT2(k) asm volatile("s_waitcnt vmcnt(%3)" : "+v"(v[k]), "+v"(v[k + 1]), "+v"(sum) : "n"(12 - k) : "memory"); sum += v[k]; sum += v[k + 1]
      NEXT2(2); NEXT2(4); NEXT2(6); NEXT2(8); NEXT2(10); NEXT2(12);
      u = swap16_add(swap32_add(sum[0], sum[2]), swap32_add(sum[1], sum[3]));
      if (!__any(u != u || xw == 0xffffu) || spins > RETRY_CAP) break;
    }
    TQ(1);
    f32x2 ra, rc;
    asm volatile("ds_write_b32 %0, %1\n\ts_waitcnt lgkmcnt(0)\n\ts_barrier" :: "v"(vw), "v"(u) : "memory");
    if (wid == 0) {
      asm volatile("ds_read2st64_b32 %0, %2 offset1:1\n\tds_read2st64_b32 %1, %2 offset0:2 offset1:3\n\ts_waitcnt lgkmcnt(0)" : "=&v"(ra), "=&v"(rc) : "v"(vr) : "memory");
      ra += rc;
      const unsigned ov = cvtpk(fmaf(gbv, fmaf(ra[0] + ra[1], rsel, hv), lo16(xw)), 0.f);
      asm volatile("global_store_short %0, %1, off sc1" :: "v"(vout), "v"(ov) : "memory");
    }
  } else {
    f32x4 dg, du;
    bf16x8 hb[8];
    auto fetch = [&]() __attribute__((always_inline)) {
      asm volatile("s_mov_b64 exec, %8\n\t"
                   "buffer_load_dwordx4 %0, %9, %10, 0 offen sc1\n\tbuffer_load_dwordx4 %1, %9, %10, 0 offen offset:64 sc1\n\tbuffer_load_dwordx4 %2, %9, %10, 0 offen offset:128 sc1\n\tbuffer_load_dwordx4 %3, %9, %10, 0 offen offset:192 sc1\n\t"
                   "buffer_load_dwordx4 %4, %9, %10, 0 offen offset:256 sc1\n\tbuffer_load_dwordx4 %5, %9, %10, 0 offen offset:320 sc1\n\tbuffer_load_dwordx4 %6, %9, %10, 0 offen offset:384 sc1\n\tbuffer_load_dwordx4 %7, %9, %10, 0 offen offset:448 sc1\n\t"
                   "s_mov_b64 exec, -1"
                   : "=&v"(hb[0]), "=&v"(hb[1]), "=&v"(hb[2]), "=&v"(hb[3]), "=&v"(hb[4]), "=&v"(hb[5]), "=&v"(hb[6]), "=&v"(hb[7]) : "s"(m2), "v"(vrow), "s"(srdz) : "memory");
    };
#define MM2(a, h, c0, c1) "v_mfma_f32_16x16x32_bf16 %0, a[" #a ":" #a "+3], %" #h ", " c0 "\n\tv_mfma_f32_16x16x32_bf16 %1, a[" #a "+4:" #a "+7], %" #h ", " c1 "\n\t"
#define MM16 MM2(128, 2, "0", "0") MM2(136, 3, "%0", "%1") MM2(144, 4, "%0", "%1") MM2(152, 5, "%0", "%1") MM2(160, 6, "%0", "%1") MM2(168, 7, "%0", "%1") MM2(176, 8, "%0", "%1") MM2(184, 9, "%0", "%1") "s_nop 7"
#define HB "+v"(hb[0]), "+v"(hb[1]), "+v"(hb[2]), "+v"(hb[3]), "+v"(hb[4]), "+v"(hb[5]), "+v"(hb[6]), "+v"(hb[7])
    fetch();
    routed();
    asm volatile("s_cmp_lg_u32 %10, %11\n\ts_waitcnt vmcnt(4)\n\ts_cbranch_scc1 Lh_%=\n\ts_waitcnt vmcnt(0)\nLh_%=:\n\t" MM16 : "=&v"(dg), "=&v"(du), HB : "s"(rest), "s"(later) : "scc", "memory");
    TQ(6);
    for (unsigned spins = 0; __builtin_expect((__ballot(dg[0] != dg[0]) & m2) != 0ull, 0) && spins < RETRY_CAP; ++spins) {
      fetch();
      asm volatile("s_waitcnt vmcnt(0)\n\t" MM16 : "=&v"(dg), "=&v"(du), HB :: "memory");
    }
    f32x2 ra, rc, rd, re;
    asm volatile("s_mov_b64 exec, %7\n\tds_write_b128 %4, %5\n\tds_write_b128 %4, %6 offset:128\n\ts_mov_b64 exec, -1\n\ts_waitcnt lgkmcnt(0)\n\ts_barrier\n\t"
                 "ds_read2_b32 %0, %8 offset1:32\n\tds_read2_b32 %1, %8 offset0:64 offset1:96\n\tds_read2_b32 %2, %8 offset0:128 offset1:160\n\tds_read2_b32 %3, %8 offset0:192 offset1:224\n\ts_waitcnt lgkmcnt(0)"
                 : "=&v"(ra), "=&v"(rc), "=&v"(rd), "=&v"(re) : "v"(vred), "v"(dg), "v"(du), "s"(m2), "v"(vrf) : "memory");
    u32x2 actp;
    {
      const f32x2 gu = rb2((ra + rc) + (rd + re));
      const float sg = __uint_as_float(cvtpk(0.f, gu[0] * __builtin_amdgcn_rcpf(1.f + __builtin_amdgcn_exp2f(gu[0] * -1.44269504089f))));
      const unsigned a = cvtpk(0.f, sg * gu[1]);
      const unsigned o2 = __builtin_amdgcn_update_dpp(0u, a, DPP_XOR2, 0xf, 0xf, true);
      const unsigned pr = (lane & 2u) ? ((o2 >> 16) | a) : ((a >> 16) | o2);
      const unsigned o4 = __builtin_amdgcn_update_dpp(0u, pr, DPP_ROR4, 0xf, 0xf, true);
      actp = (lane & 4u) ? u32x2{o4, pr} : u32x2{pr, o4};
    }
    f32x4 yv[2];
#define ABD(t) "v"(u32x4{actp[0] & mk[t], actp[1] & mk[t], actp[0] & mk[t + 1], actp[1] & mk[t + 1]})
#define MM3(a, w, c0, c1) "v_mfma_f32_16x16x32_bf16 %0, %" #a ", a[" #w ":" #w "+3], " c0 "\n\tv_mfma_f32_16x16x32_bf16 %1, %" #a ", a[" #w "+16:" #w "+19], " c1 "\n\t"
    asm volatile("s_nop 1\n\t" MM3(2, 192, "0", "0") MM3(3, 196, "%0", "%1") MM3(4, 200, "%0", "%1") MM3(5, 204, "%0", "%1") "s_nop 6\n\t"
                 "buffer_store_dwordx4 %0, %6, %7, 0 offen sc1\n\tbuffer_store_dwordx4 %1, %6, %7, 0 offen offset:1024 sc1\n\ts_nop 1"
                 : "=&v"(yv[0]), "=&v"(yv[1]) : ABD(0), ABD(2), ABD(4), ABD(6), "v"(vpt), "s"(srdz) : "memory");
    TQ(7);
    out_rows(rest);
  }
  asm volatile("adaln_text_end:");
}
}

extern "C" void kernel_launch(void* const* d_in, const int* in_sizes, int n_in, void* d_out, int out_size, void* d_ws, size_t ws_size, hipStream_t stream) {
  (void)in_sizes; (void)n_in; (void)out_size;
  if (ws_size < WS_END) return;
  const u16* x    = (const u16*)d_in[0];
  const u16* kc   = (const u16*)d_in[1];
  const u16* vc   = (const u16*)d_in[2];
  const u16* wq   = (const u16*)d_in[3];
  const u16* wo   = (const u16*)d_in[4];
  const u16* wr   = (const u16*)d_in[6];
  const u16* wg   = (const u16*)d_in[7];
  const u16* wu   = (const u16*)d_in[8];
  const u16* wd   = (const u16*)d_in[9];
  const u16* c    = (const u16*)d_in[10];
  const u16* wmod = (const u16*)d_in[11];
  u16* out = (u16*)d_out;
  char* ws = (char*)d_ws;
  fa::Args fr;
  fr.x = x; fr.wmod = wmod; fr.wq = wq; fr.kc = kc; fr.vc = vc; fr.wo = wo; fr.wg = wg; fr.wu = wu; fr.wd = wd; fr.ws = ws; fr.out = out; fr.c = c; fr.wr = wr;
  hipFuncSetAttribute((const void*)fa::fattn_k, hipFuncAttributeMaxDynamicSharedMemorySize, (int)fa::LDS_BYTES);
  hipLaunchKernelGGL(fa::fattn_k, dim3(256), dim3(256), fa::LDS_BYTES, stream, fr);
}
```

```cpp
#include <hip/hip_runtime.h>
#include <hip/hip_bf16.h>
#include <utility>
#include <cstdint>

#pragma clang fp contract(off)
typedef float    f32x16 __attribute__((ext_vector_type(16)));
typedef float    f32x2  __attribute__((ext_vector_type(2)));
typedef float    f32x4  __attribute__((ext_vector_type(4)));
typedef __bf16   bf16x8 __attribute__((ext_vector_type(8)));
typedef __bf16   bf16x4 __attribute__((ext_vector_type(4)));
typedef __bf16   bf16x2 __attribute__((ext_vector_type(2)));
typedef unsigned u32x4  __attribute__((ext_vector_type(4)));
typedef unsigned u32x2  __attribute__((ext_vector_type(2)));
typedef short    s16x4  __attribute__((ext_vector_type(4)));
typedef unsigned short u16;
template <int... Is> using iseq = std::integer_sequence<int, Is...>;
template <int N> using make_iseq = std::make_integer_sequence<int, N>;
#define DI __device__ __forceinline__
#define TAILB
#define PACE2
#define SELB

constexpr int SEQ = 2048, DM = 1024, NHEAD = 32, DHEAD = 128, DQ = NHEAD * DHEAD, DFF = 3584, NMOD = 6 * DM;
constexpr size_t WS_EP    = 0;
constexpr size_t WS_RST   = 32768;
constexpr size_t WS_CNT   = 65536;
constexpr size_t WS_MOD   = 131072;
constexpr size_t WS_Z     = 262144;
constexpr size_t WS_MODT  = WS_Z;
#define KEY_REP 8u
#define KSTR 128u
constexpr size_t WS_KEYS  = WS_Z + 16384;
constexpr unsigned Z_SHARE = 1280;
static_assert(KEY_REP <= 8 && KSTR <= 384 && Z_SHARE * 256 == 327680, "zeroed area");
constexpr size_t WS_PFL   = WS_Z + 65536;
constexpr size_t WS_OFL   = WS_Z + 196608;
constexpr size_t WS_H     = 1048576;
constexpr size_t WS_PART  = WS_H  + (size_t)SEQ * DM * 2;
constexpr size_t WS_XIN   = WS_PART + (size_t)256 * 2 * DM * 4;
#define BOXV 38016
constexpr unsigned BOX    = BOXV;
constexpr size_t WS_END   = WS_XIN + (size_t)BOX * (SEQ / 2);

DI float bf2f(u16 b) { return __uint_as_float((unsigned)b << 16); }
DI float rbf(float x) { return (float)(__bf16)x; }
DI u16   f2bf(float x) { return __builtin_bit_cast(u16, (__bf16)x); }
DI float lo16(unsigned w) { return __uint_as_float(w << 16); }
DI float hi16(unsigned w) { return __uint_as_float(w & 0xffff0000u); }
DI unsigned cvtpk(float lo, float hi) {
  f32x2 v = {lo, hi}; bf16x2 b = __builtin_convertvector(v, bf16x2); return __builtin_bit_cast(unsigned, b);
}
template <int CTRL> DI float dpp_add(float v) { return v + __uint_as_float(__builtin_amdgcn_update_dpp(0u, __float_as_uint(v), CTRL, 0xf, 0xf, true)); }
DI float swap32_add(float a, float b) {
  auto r = __builtin_amdgcn_permlane32_swap(__float_as_uint(a), __float_as_uint(b), false, false); return __uint_as_float(r[0]) + __uint_as_float(r[1]);
}
DI float swap16_add(float a, float b) {
  auto r = __builtin_amdgcn_permlane16_swap(__float_as_uint(a), __float_as_uint(b), false, false); return __uint_as_float(r[0]) + __uint_as_float(r[1]);
}
constexpr int DPP_XOR1 = 0xB1, DPP_XOR2 = 0x4E, DPP_ROR4 = 0x124, DPP_ROR8 = 0x128;
DI float wave_sum(float v) {
  v = dpp_add<DPP_XOR1>(v); v = dpp_add<DPP_XOR2>(v); v = dpp_add<DPP_ROR4>(v); v = dpp_add<DPP_ROR8>(v);
  v = swap16_add(v, v); return swap32_add(v, v);
}

DI void st16_wt(void* p, u32x4 v) { asm volatile("global_store_dwordx4 %0, %1, off sc1\n\ts_nop 1" :: "v"(p), "v"(v) : "memory"); }

DI f32x2 unpk(unsigned w) { return f32x2{lo16(w), hi16(w)}; }
DI unsigned cvtpk2(f32x2 v) { return __builtin_bit_cast(unsigned, __builtin_convertvector(v, bf16x2)); }
DI float rbh(float v) { return __uint_as_float(cvtpk(0.f, v)); }
DI f32x2 rb2(f32x2 v) { return f32x2{__uint_as_float(cvtpk(0.f, v[0])), __uint_as_float(cvtpk(0.f, v[1]))}; }
DI unsigned ln_pair(float x0, float x1, float m, float rden, float c0, float c1, float s0, float s1) {
  const unsigned d = cvtpk2(f32x2{x0, x1} - m), l = cvtpk2(unpk(d) * rden), t = cvtpk2(unpk(l) * f32x2{c0, c1});
  return cvtpk2(unpk(t) + f32x2{s0, s1});
}
#define GAS __attribute__((address_space(1)))
#define LAS __attribute__((address_space(3)))
typedef GAS unsigned long long gu64;
typedef short v4i16_t __attribute__((ext_vector_type(4)));
DI bf16x4 lds_tr(unsigned a) { return __builtin_bit_cast(bf16x4, __builtin_amdgcn_ds_read_tr16_b64_v4i16((LAS v4i16_t*)a)); }
#define RLX_AGENT __ATOMIC_RELAXED, __HIP_MEMORY_SCOPE_AGENT
constexpr unsigned SPIN_CAP = 1u << 16;

static_assert(BOX == 38016, "box size");
#define NP_FILL 8
#define GOT_WAIT "8"
#define FA_CLOBBERS "v0", "v1", "v2", "v3", "v8", "v9", "v10", "v11", "v16", "v17", "v18", "v19", "v20", "v21", "v22", "v23", "v24", "v25", "v26", "v27", "v28", "v29", "v30", "v31", "v32", "v33", "v34", "v35", "v40", "v41", "v42", "v43", "v48", "v49", "v50", "v51", "v52", "v53", "v54", "v55", "v56", "v57", "v58", "v59", "v60", "v61", "v62", "v63", "v64", "v65", "v66", "v67", "v72", "v73", "v74", "v75", "v80", "v81", "v82", "v83", "v84", "v85", "v86", "v87", "v88", "v89", "v90", "v91", "v92", "v93", "v94", "v95", "v96", "v97", "v98", "v99", "v104", "v105", "v106", "v107", "v112", "v113", "v114", "v115", "v116", "v117", "v118", "v119", "v120", "v121", "v122", "v123", "v124", "v125", "v126", "v127", "v200", "v201", "v202", "v203", "v205", "v207", "v208", "v209", "v210", "v211", "v212", "v213", "v214", "v215", "v216", "v217", "v218", "v219", "v220", "v221", "v222", "v223", "v224", "v230", "v231", "v232", "v233", "v234", "v235", "v237", "a0", "a1", "a2", "a3", "a4", "a5", "a6", "a7", "a8", "a9", "a10", "a11", "a12", "a13", "a14", "a15", "a16", "a17", "a18", "a19", "a20", "a21", "a22", "a23", "a24", "a25", "a26", "a27", "a28", "a29", "a30", "a31", "a32", "a33", "a34", "a35", "a36", "a37", "a38", "a39", "a40", "a41", "a42", "a43", "a44", "a45", "a46", "a47", "a48", "a49", "a50", "a51", "a52", "a53", "a54", "a55", "a56", "a57", "a58", "a59", "a60", "a61", "a62", "a63", "a64", "a65", "a66", "a67", "a68", "a69", "a70", "a71", "a72", "a73", "a74", "a75", "a76", "a77", "a78", "a79", "a80", "a81", "a82", "a83", "a84", "a85", "a86", "a87", "a88", "a89", "a90", "a91", "a92", "a93", "a94", "a95", "a96", "a97", "a98", "a99", "a100", "a101", "a102", "a103", "a104", "a105", "a106", "a107", "a108", "a109", "a110", "a111", "a112", "a113", "a114", "a115", "a116", "a117", "a118", "a119", "a120", "a121", "a122", "a123", "a124", "a125", "a126", "a127", "a128", "a129", "a130", "a131", "a132", "a133", "a134", "a135", "a136", "a137", "a138", "a139", "a140", "a141", "a142", "a143", "a144", "a145", "a146", "a147", "a148", "a149", "a150", "a151", "a152", "a153", "a154", "a155", "a156", "a157", "a158", "a159", "a160", "a161", "a162", "a163", "a164", "a165", "a166", "a167", "a168", "a169", "a170", "a171", "a172", "a173", "a174", "a175", "a176", "a177", "a178", "a179", "a180", "a181", "a182", "a183", "a184", "a185", "a186", "a187", "a188", "a189", "a190", "a191", "a192", "a193", "a194", "a195", "a196", "a197", "a198", "a199", "a200", "a201", "a202", "a203", "a204", "a205", "a206", "a207", "a208", "a209", "a210", "a211", "a212", "a213", "a214", "a215", "a216", "a217", "a218", "a219", "a220", "a221", "a222", "a223", "a224", "a225", "a226", "a227", "a228", "a229", "a230", "a231", "a232", "a233", "a234", "a235", "a236", "a237", "a238", "a239", "a240", "a241", "a242", "a243", "a244", "a245", "a246", "a247", "a248", "a249", "a250", "a251", "a252", "a253", "a254", "a255", "s40", "s41", "s42", "s43", "s44", "s45", "s46", "s47", "s48", "s49", "s50", "s51", "s52", "s53", "s54", "s55", "s56", "s57", "s58", "s59", "s60", "s61", "s62", "s63", "s64", "s65", "s66", "s67", "s68", "s69", "vcc", "m0", "scc", "memory"
#define EARLY2 0
#define SHW 1
#define QROT 0
#define SKV 0
#define KVPAR 0
#define QT2 0
#define QSTAG 0
#define TLATE 0
#define NTV 5
#define BPERM 0
#define FA_OUTS [got] "=&s"(got), "=&{v[128:129]}"(xf[0]), "=&{v[130:131]}"(xf[1]), "=&{v[132:133]}"(xf[2]), "=&{v[134:135]}"(xf[3]), "=&{v[136:137]}"(xf[4]), "=&{v[138:139]}"(xf[5]), "=&{v[140:141]}"(xf[6]), "=&{v[142:143]}"(xf[7]), "=&{v[144:145]}"(xf[8]), "=&{v[146:147]}"(xf[9]), "=&{v[148:149]}"(xf[10]), "=&{v[150:151]}"(xf[11]), "=&{v[152:153]}"(xf[12]), "=&{v[154:155]}"(xf[13]), "=&{v[156:157]}"(xf[14]), "=&{v[158:159]}"(xf[15]), "=&{v[160:163]}"(gaf[0]), "=&{v[164:167]}"(gaf[1]), "=&{v[168:171]}"(gaf[2]), "=&{v[172:175]}"(gaf[3]), "=&{v[176:179]}"(gaf[4]), "=&{v[180:183]}"(gaf[5]), "=&{v[184:187]}"(gaf[6]), "=&{v[188:191]}"(gaf[7]), "=&{v[4:7]}"(aw[0]), "=&{v[12:15]}"(aw[1]), "=&{v[36:39]}"(aw[2]), "=&{v[44:47]}"(aw[3]), "=&{v[68:71]}"(aw[4]), "=&{v[76:79]}"(aw[5]), "=&{v[100:103]}"(aw[6]), "=&{v[108:111]}"(aw[7]), "=&{v[192:195]}"(alo), "=&{v[196:199]}"(ahi), "=&{v204}"(late), "=&{v206}"(arow), "+{v229}"(fl), "+{v225}"(vwb), "+{v226}"(wd0), "+{v227}"(hq0), "+{v228}"(sd0), "+{v236}"(tq0)
#define FA_BODY \
"s_branch adaln_body\n" \
".p2align 12\n" \
"adaln_pg7:\n" \
"adaln_body:\n" \
"s_cbranch_execz adaln_fr\n" \
"s_nop 0\n" \
"s_mov_b32 s55, 0x80000\n" \
"v_add_u32_e32 v219, 0x8000, v227\n" \
"v_add_u32_e32 v220, 0x10000, v227\n" \
"v_add_u32_e32 v221, 0x18000, v227\n" \
"s_mov_b32 s50, 0\n" \
"s_movk_i32 s53, 0x1000\n" \
"s_mov_b32 s52, 0\n" \
"s_nop 0\n" \
"s_mov_b32 s49, 1572864\n" \
"v_add_u32_e32 v216, 0xfffffc80, v226\n" \
"v_add_u32_e32 v217, 0xf800, v226\n" \
"v_add_u32_e32 v218, 0xf480, v226\n" \
"s_add_u32 s40, %[m0b], 0\n" \
"s_nop 0\n" \
"s_add_u32 s44, %[m0b], 65536\n" \
"s_add_u32 s41, %[m0b], 16384\n" \
"s_add_u32 s45, %[m0b], 81920\n" \
"s_add_u32 s42, %[m0b], 32768\n" \
"s_add_u32 s46, %[m0b], 98304\n" \
"s_add_u32 s43, %[m0b], 49152\n" \
"s_add_u32 s47, %[m0b], 114688\n" \
"v_xor_b32_e64 v208, 64, %[kb0]\n" \
"v_xor_b32_e32 v209, 0x80, %[kb0]\n" \
"v_xor_b32_e32 v210, 0xc0, %[kb0]\n" \
"v_xor_b32_e64 v212, 64, %[kd0]\n" \
"v_xor_b32_e32 v213, 0x80, %[kd0]\n" \
"v_xor_b32_e32 v214, 0xc0, %[kd0]\n" \
"v_xor_b32_e64 v211, 32, %[vvb]\n" \
"v_add_u32_e32 v215, 0xfffffc80, %[vd0]\n" \
"v_readlane_b32 s64, %[tlo], 17\n" \
"v_readlane_b32 s65, %[thi], 17\n" \
"s_mov_b32 s66, 12582912\n" \
"s_mov_b32 s67, 0x20000\n" \
"v_mov_b64_e32 v[0:1], 0\n" \
"v_mov_b64_e32 v[2:3], 0\n" \
"v_mov_b64_e32 v[4:5], 0\n" \
"v_mov_b64_e32 v[6:7], 0\n" \
"v_mov_b64_e32 v[8:9], 0\n" \
"v_mov_b64_e32 v[10:11], 0\n" \
"v_mov_b64_e32 v[12:13], 0\n" \
"v_mov_b64_e32 v[14:15], 0\n" \
"v_mov_b64_e32 v[16:17], 0\n" \
"v_mov_b64_e32 v[18:19], 0\n" \
"v_mov_b64_e32 v[20:21], 0\n" \
"v_mov_b64_e32 v[22:23], 0\n" \
"v_mov_b64_e32 v[24:25], 0\n" \
"v_mov_b64_e32 v[26:27], 0\n" \
"v_mov_b64_e32 v[28:29], 0\n" \
"v_mov_b64_e32 v[30:31], 0\n" \
"v_mov_b64_e32 v[32:33], 0\n" \
"v_mov_b64_e32 v[34:35], 0\n" \
"v_mov_b64_e32 v[36:37], 0\n" \
"v_mov_b64_e32 v[38:39], 0\n" \
"v_mov_b64_e32 v[40:41], 0\n" \
"v_mov_b64_e32 v[42:43], 0\n" \
"v_mov_b64_e32 v[44:45], 0\n" \
"v_mov_b64_e32 v[46:47], 0\n" \
"v_mov_b64_e32 v[48:49], 0\n" \
"v_mov_b64_e32 v[50:51], 0\n" \
"v_mov_b64_e32 v[52:53], 0\n" \
"v_mov_b64_e32 v[54:55], 0\n" \
"v_mov_b64_e32 v[56:57], 0\n" \
"v_mov_b64_e32 v[58:59], 0\n" \
"v_mov_b64_e32 v[60:61], 0\n" \
"v_mov_b64_e32 v[62:63], 0\n" \
"v_mov_b64_e32 v[64:65], 0\n" \
"v_mov_b64_e32 v[66:67], 0\n" \
"v_mov_b64_e32 v[68:69], 0\n" \
"v_mov_b64_e32 v[70:71], 0\n" \
"v_mov_b64_e32 v[72:73], 0\n" \
"v_mov_b64_e32 v[74:75], 0\n" \
"v_mov_b64_e32 v[76:77], 0\n" \
"v_mov_b64_e32 v[78:79], 0\n" \
"v_mov_b64_e32 v[80:81], 0\n" \
"v_mov_b64_e32 v[82:83], 0\n" \
"v_mov_b64_e32 v[84:85], 0\n" \
"v_mov_b64_e32 v[86:87], 0\n" \
"v_mov_b64_e32 v[88:89], 0\n" \
"v_mov_b64_e32 v[90:91], 0\n" \
"v_mov_b64_e32 v[92:93], 0\n" \
"v_mov_b64_e32 v[94:95], 0\n" \
"v_mov_b64_e32 v[96:97], 0\n" \
"v_mov_b64_e32 v[98:99], 0\n" \
"v_mov_b64_e32 v[100:101], 0\n" \
"v_mov_b64_e32 v[102:103], 0\n" \
"v_mov_b64_e32 v[104:105], 0\n" \
"v_mov_b64_e32 v[106:107], 0\n" \
"v_mov_b64_e32 v[108:109], 0\n" \
"v_mov_b64_e32 v[110:111], 0\n" \
"v_mov_b64_e32 v[112:113], 0\n" \
"v_mov_b64_e32 v[114:115], 0\n" \
"v_mov_b64_e32 v[116:117], 0\n" \
"v_mov_b64_e32 v[118:119], 0\n" \
"v_mov_b64_e32 v[120:121], 0\n" \
"v_mov_b64_e32 v[122:123], 0\n" \
"v_mov_b64_e32 v[124:125], 0\n" \
"v_mov_b64_e32 v[126:127], 0\n" \
"s_mov_b32 s51, 0x10000\n" \
"v_mbcnt_lo_u32_b32 v232, -1, 0\n" \
"v_mov_b64_e32 v[234:235], %[tag]\n" \
"v_lshlrev_b32_e32 v233, 3, v232\n" \
"v_lshlrev_b32_e32 v232, 8, v232\n" \
"s_waitcnt vmcnt(0)\n" \
"s_barrier\n" \
"s_cmp_lg_u32 %[wid], 0\n" \
"s_cbranch_scc1 Lwalk_%=\n" \
"s_mov_b32 exec_hi, 0\n" \
"global_store_dwordx2 v232, v[234:235], %[psig] sc1\n" \
"s_mov_b32 exec_hi, -1\n" \
"Lgo_%=:\n" \
"s_nop 0\n" \
"ds_read_b64_tr_b16 v[128:129], v225 offset:0\n" \
"ds_read_b64_tr_b16 v[130:131], v225 offset:256\n" \
"ds_read_b64_tr_b16 v[132:133], v225 offset:8192\n" \
"ds_read_b64_tr_b16 v[134:135], v225 offset:8448\n" \
"ds_read_b64_tr_b16 v[136:137], v225 offset:8\n" \
"ds_read_b64_tr_b16 v[138:139], v225 offset:264\n" \
"ds_read_b64_tr_b16 v[140:141], v225 offset:8200\n" \
"ds_read_b64_tr_b16 v[142:143], v225 offset:8456\n" \
"ds_read_b64_tr_b16 v[144:145], v225 offset:512\n" \
"ds_read_b64_tr_b16 v[146:147], v225 offset:768\n" \
"ds_read_b64_tr_b16 v[148:149], v225 offset:8704\n" \
"ds_read_b64_tr_b16 v[150:151], v225 offset:8960\n" \
"ds_read_b64_tr_b16 v[152:153], v225 offset:520\n" \
"ds_read_b64_tr_b16 v[154:155], v225 offset:776\n" \
"ds_read_b64_tr_b16 v[156:157], v225 offset:8712\n" \
"ds_read_b64_tr_b16 v[158:159], v225 offset:8968\n" \
"s_mov_b32 s60, 4194304\n" \
"v_and_b32_e32 v233, 56, v233\n" \
"s_lshl_b32 s61, %[wid], 6\n" \
"v_add_u32_e64 v233, s61, v233\n" \
"Lpoll_%=:\n" \
"global_load_dwordx2 v[230:231], v233, %[pcnt] sc1\n" \
"s_waitcnt vmcnt(0)\n" \
"v_cmp_ne_u64_e32 vcc, v[230:231], v[234:235]\n" \
"s_sub_u32 s51, s51, 1\n" \
"s_cbranch_scc1 Lpd_%=\n" \
"s_nop 3\n" \
"s_cbranch_vccnz Lpoll_%=\n" \
"Lpd_%=:\n" \
"buffer_load_dwordx4 a[128:131], v227, %[srdz], s50 offen offset:0 sc1\n" \
"buffer_load_dwordx4 a[132:135], v219, %[srdz], s50 offen offset:0 sc1\n" \
"buffer_load_dwordx4 a[136:139], v220, %[srdz], s50 offen offset:0 sc1\n" \
"buffer_load_dwordx4 a[140:143], v221, %[srdz], s50 offen offset:0 sc1\n" \
"buffer_load_dwordx4 a[144:147], v227, %[srdz], s50 offen offset:1024 sc1\n" \
"buffer_load_dwordx4 a[148:151], v219, %[srdz], s50 offen offset:1024 sc1\n" \
"buffer_load_dwordx4 a[152:155], v220, %[srdz], s50 offen offset:1024 sc1\n" \
"buffer_load_dwordx4 a[156:159], v221, %[srdz], s50 offen offset:1024 sc1\n" \
"buffer_load_dwordx4 a[160:163], v227, %[srdz], s50 offen offset:2048 sc1\n" \
"buffer_load_dwordx4 a[164:167], v219, %[srdz], s50 offen offset:2048 sc1\n" \
"buffer_load_dwordx4 a[168:171], v220, %[srdz], s50 offen offset:2048 sc1\n" \
"buffer_load_dwordx4 a[172:175], v221, %[srdz], s50 offen offset:2048 sc1\n" \
"buffer_load_dwordx4 a[176:179], v227, %[srdz], s50 offen offset:3072 sc1\n" \
"buffer_load_dwordx4 a[180:183], v219, %[srdz], s50 offen offset:3072 sc1\n" \
"buffer_load_dwordx4 a[184:187], v220, %[srdz], s50 offen offset:3072 sc1\n" \
"buffer_load_dwordx4 a[188:191], v221, %[srdz], s50 offen offset:3072 sc1\n" \
"s_add_u32 s50, s50, s53\n" \
"s_nop 0\n" \
"buffer_load_dwordx4 a[192:195], v227, %[srdz], s50 offen offset:0 sc1\n" \
"buffer_load_dwordx4 a[196:199], v219, %[srdz], s50 offen offset:0 sc1\n" \
"buffer_load_dwordx4 a[200:203], v220, %[srdz], s50 offen offset:0 sc1\n" \
"buffer_load_dwordx4 a[204:207], v221, %[srdz], s50 offen offset:0 sc1\n" \
"buffer_load_dwordx4 a[208:211], v227, %[srdz], s50 offen offset:1024 sc1\n" \
"buffer_load_dwordx4 a[212:215], v219, %[srdz], s50 offen offset:1024 sc1\n" \
"buffer_load_dwordx4 a[216:219], v220, %[srdz], s50 offen offset:1024 sc1\n" \
"buffer_load_dwordx4 a[220:223], v221, %[srdz], s50 offen offset:1024 sc1\n" \
"s_mov_b32 s51, 3\n" \
"s_waitcnt vmcnt(16) lgkmcnt(0)\n" \
"Lq_%=:\n" \
"v_mfma_f32_16x16x32_bf16 v[0:3], v[128:131], a[128:131], v[0:3]\n" \
"ds_read_b64_tr_b16 v[160:161], v225 offset:1024\n" \
"v_mfma_f32_16x16x32_bf16 v[0:3], v[132:135], a[144:147], v[0:3]\n" \
"ds_read_b64_tr_b16 v[162:163], v225 offset:1280\n" \
"v_mfma_f32_16x16x32_bf16 v[32:35], v[128:131], a[132:135], v[32:35]\n" \
"s_mov_b32 m0, s47\n" \
"s_nop 0\n" \
"buffer_load_dwordx4 v217, %[srdw], s49 offen offset:2048 lds\n" \
"v_mfma_f32_16x16x32_bf16 v[32:35], v[132:135], a[148:151], v[32:35]\n" \
"ds_read_b64_tr_b16 v[164:165], v225 offset:9216\n" \
"v_mfma_f32_16x16x32_bf16 v[64:67], v[128:131], a[136:139], v[64:67]\n" \
"ds_read_b64_tr_b16 v[166:167], v225 offset:9472\n" \
"v_mfma_f32_16x16x32_bf16 v[64:67], v[132:135], a[152:155], v[64:67]\n" \
"v_mfma_f32_16x16x32_bf16 v[96:99], v[128:131], a[140:143], v[96:99]\n" \
"ds_read_b64_tr_b16 v[168:169], v225 offset:1032\n" \
"v_mfma_f32_16x16x32_bf16 v[96:99], v[132:135], a[156:159], v[96:99]\n" \
"ds_read_b64_tr_b16 v[170:171], v225 offset:1288\n" \
"v_mfma_f32_16x16x32_bf16 v[4:7], v[136:139], a[128:131], v[4:7]\n" \
"buffer_load_dwordx4 v218, %[srdw], s49 offen offset:3072 lds\n" \
"v_mfma_f32_16x16x32_bf16 v[4:7], v[140:143], a[144:147], v[4:7]\n" \
"ds_read_b64_tr_b16 v[172:173], v225 offset:9224\n" \
"v_mfma_f32_16x16x32_bf16 v[36:39], v[136:139], a[132:135], v[36:39]\n" \
"ds_read_b64_tr_b16 v[174:175], v225 offset:9480\n" \
"v_mfma_f32_16x16x32_bf16 v[36:39], v[140:143], a[148:151], v[36:39]\n" \
"v_mfma_f32_16x16x32_bf16 v[68:71], v[136:139], a[136:139], v[68:71]\n" \
"ds_read_b64_tr_b16 v[176:177], v225 offset:1536\n" \
"v_mfma_f32_16x16x32_bf16 v[68:71], v[140:143], a[152:155], v[68:71]\n" \
"ds_read_b64_tr_b16 v[178:179], v225 offset:1792\n" \
"v_mfma_f32_16x16x32_bf16 v[100:103], v[136:139], a[140:143], v[100:103]\n" \
"buffer_load_dwordx4 a[224:227], v227, %[srdz], s50 offen offset:2048 sc1\n" \
"v_mfma_f32_16x16x32_bf16 v[100:103], v[140:143], a[156:159], v[100:103]\n" \
"ds_read_b64_tr_b16 v[180:181], v225 offset:9728\n" \
"v_mfma_f32_16x16x32_bf16 v[8:11], v[144:147], a[128:131], v[8:11]\n" \
"ds_read_b64_tr_b16 v[182:183], v225 offset:9984\n" \
"v_mfma_f32_16x16x32_bf16 v[8:11], v[148:151], a[144:147], v[8:11]\n" \
"v_mfma_f32_16x16x32_bf16 v[40:43], v[144:147], a[132:135], v[40:43]\n" \
"ds_read_b64_tr_b16 v[184:185], v225 offset:1544\n" \
"v_mfma_f32_16x16x32_bf16 v[40:43], v[148:151], a[148:151], v[40:43]\n" \
"ds_read_b64_tr_b16 v[186:187], v225 offset:1800\n" \
"v_mfma_f32_16x16x32_bf16 v[72:75], v[144:147], a[136:139], v[72:75]\n" \
"buffer_load_dwordx4 a[228:231], v219, %[srdz], s50 offen offset:2048 sc1\n" \
"v_mfma_f32_16x16x32_bf16 v[72:75], v[148:151], a[152:155], v[72:75]\n" \
"ds_read_b64_tr_b16 v[188:189], v225 offset:9736\n" \
"v_mfma_f32_16x16x32_bf16 v[104:107], v[144:147], a[140:143], v[104:107]\n" \
"ds_read_b64_tr_b16 v[190:191], v225 offset:9992\n" \
"v_mfma_f32_16x16x32_bf16 v[104:107], v[148:151], a[156:159], v[104:107]\n" \
"v_mfma_f32_16x16x32_bf16 v[12:15], v[152:155], a[128:131], v[12:15]\n" \
"buffer_load_dwordx4 a[232:235], v220, %[srdz], s50 offen offset:2048 sc1\n" \
"v_mfma_f32_16x16x32_bf16 v[12:15], v[156:159], a[144:147], v[12:15]\n" \
"v_mfma_f32_16x16x32_bf16 v[44:47], v[152:155], a[132:135], v[44:47]\n" \
"v_mfma_f32_16x16x32_bf16 v[44:47], v[156:159], a[148:151], v[44:47]\n" \
"v_mfma_f32_16x16x32_bf16 v[76:79], v[152:155], a[136:139], v[76:79]\n" \
"buffer_load_dwordx4 a[236:239], v221, %[srdz], s50 offen offset:2048 sc1\n" \
"v_mfma_f32_16x16x32_bf16 v[76:79], v[156:159], a[152:155], v[76:79]\n" \
"v_mfma_f32_16x16x32_bf16 v[108:111], v[152:155], a[140:143], v[108:111]\n" \
"v_mfma_f32_16x16x32_bf16 v[108:111], v[156:159], a[156:159], v[108:111]\n" \
"s_waitcnt vmcnt(29) lgkmcnt(0)\n" \
"s_barrier\n" \
"v_mfma_f32_16x16x32_bf16 v[16:19], v[160:163], a[128:131], v[16:19]\n" \
"ds_read_b64_tr_b16 v[128:129], v225 offset:16384\n" \
"v_mfma_f32_16x16x32_bf16 v[16:19], v[164:167], a[144:147], v[16:19]\n" \
"ds_read_b64_tr_b16 v[130:131], v225 offset:16640\n" \
"v_mfma_f32_16x16x32_bf16 v[48:51], v[160:163], a[132:135], v[48:51]\n" \
"s_add_u32 s49, s49, s55\n" \
"s_mov_b32 m0, s44\n" \
"s_nop 0\n" \
"buffer_load_dwordx4 v226, %[srdw], s49 offen offset:0 lds\n" \
"v_mfma_f32_16x16x32_bf16 v[48:51], v[164:167], a[148:151], v[48:51]\n" \
"ds_read_b64_tr_b16 v[132:133], v225 offset:24576\n" \
"v_mfma_f32_16x16x32_bf16 v[80:83], v[160:163], a[136:139], v[80:83]\n" \
"ds_read_b64_tr_b16 v[134:135], v225 offset:24832\n" \
"v_mfma_f32_16x16x32_bf16 v[80:83], v[164:167], a[152:155], v[80:83]\n" \
"v_mfma_f32_16x16x32_bf16 v[112:115], v[160:163], a[140:143], v[112:115]\n" \
"ds_read_b64_tr_b16 v[136:137], v225 offset:16392\n" \
"v_mfma_f32_16x16x32_bf16 v[112:115], v[164:167], a[156:159], v[112:115]\n" \
"ds_read_b64_tr_b16 v[138:139], v225 offset:16648\n" \
"v_mfma_f32_16x16x32_bf16 v[20:23], v[168:171], a[128:131], v[20:23]\n" \
"buffer_load_dwordx4 v216, %[srdw], s49 offen offset:1024 lds\n" \
"v_mfma_f32_16x16x32_bf16 v[20:23], v[172:175], a[144:147], v[20:23]\n" \
"ds_read_b64_tr_b16 v[140:141], v225 offset:24584\n" \
"v_mfma_f32_16x16x32_bf16 v[52:55], v[168:171], a[132:135], v[52:55]\n" \
"ds_read_b64_tr_b16 v[142:143], v225 offset:24840\n" \
"v_mfma_f32_16x16x32_bf16 v[52:55], v[172:175], a[148:151], v[52:55]\n" \
"v_mfma_f32_16x16x32_bf16 v[84:87], v[168:171], a[136:139], v[84:87]\n" \
"ds_read_b64_tr_b16 v[144:145], v225 offset:16896\n" \
"v_mfma_f32_16x16x32_bf16 v[84:87], v[172:175], a[152:155], v[84:87]\n" \
"ds_read_b64_tr_b16 v[146:147], v225 offset:17152\n" \
"v_mfma_f32_16x16x32_bf16 v[116:119], v[168:171], a[140:143], v[116:119]\n" \
"buffer_load_dwordx4 a[240:243], v227, %[srdz], s50 offen offset:3072 sc1\n" \
"v_mfma_f32_16x16x32_bf16 v[116:119], v[172:175], a[156:159], v[116:119]\n" \
"ds_read_b64_tr_b16 v[148:149], v225 offset:25088\n" \
"v_mfma_f32_16x16x32_bf16 v[24:27], v[176:179], a[128:131], v[24:27]\n" \
"ds_read_b64_tr_b16 v[150:151], v225 offset:25344\n" \
"v_mfma_f32_16x16x32_bf16 v[24:27], v[180:183], a[144:147], v[24:27]\n" \
"v_mfma_f32_16x16x32_bf16 v[56:59], v[176:179], a[132:135], v[56:59]\n" \
"ds_read_b64_tr_b16 v[152:153], v225 offset:16904\n" \
"v_mfma_f32_16x16x32_bf16 v[56:59], v[180:183], a[148:151], v[56:59]\n" \
"ds_read_b64_tr_b16 v[154:155], v225 offset:17160\n" \
"v_mfma_f32_16x16x32_bf16 v[88:91], v[176:179], a[136:139], v[88:91]\n" \
"buffer_load_dwordx4 a[244:247], v219, %[srdz], s50 offen offset:3072 sc1\n" \
"v_mfma_f32_16x16x32_bf16 v[88:91], v[180:183], a[152:155], v[88:91]\n" \
"ds_read_b64_tr_b16 v[156:157], v225 offset:25096\n" \
"v_mfma_f32_16x16x32_bf16 v[120:123], v[176:179], a[140:143], v[120:123]\n" \
"ds_read_b64_tr_b16 v[158:159], v225 offset:25352\n" \
"v_mfma_f32_16x16x32_bf16 v[120:123], v[180:183], a[156:159], v[120:123]\n" \
"v_mfma_f32_16x16x32_bf16 v[28:31], v[184:187], a[128:131], v[28:31]\n" \
"buffer_load_dwordx4 a[248:251], v220, %[srdz], s50 offen offset:3072 sc1\n" \
"v_mfma_f32_16x16x32_bf16 v[28:31], v[188:191], a[144:147], v[28:31]\n" \
"v_mfma_f32_16x16x32_bf16 v[60:63], v[184:187], a[132:135], v[60:63]\n" \
"v_mfma_f32_16x16x32_bf16 v[60:63], v[188:191], a[148:151], v[60:63]\n" \
"v_mfma_f32_16x16x32_bf16 v[92:95], v[184:187], a[136:139], v[92:95]\n" \
"buffer_load_dwordx4 a[252:255], v221, %[srdz], s50 offen offset:3072 sc1\n" \
"v_mfma_f32_16x16x32_bf16 v[92:95], v[188:191], a[152:155], v[92:95]\n" \
"v_mfma_f32_16x16x32_bf16 v[124:127], v[184:187], a[140:143], v[124:127]\n" \
"v_mfma_f32_16x16x32_bf16 v[124:127], v[188:191], a[156:159], v[124:127]\n" \
"s_waitcnt vmcnt(20) lgkmcnt(0)\n" \
"v_mfma_f32_16x16x32_bf16 v[0:3], v[128:131], a[160:163], v[0:3]\n" \
"ds_read_b64_tr_b16 v[160:161], v225 offset:17408\n" \
"v_mfma_f32_16x16x32_bf16 v[0:3], v[132:135], a[176:179], v[0:3]\n" \
"ds_read_b64_tr_b16 v[162:163], v225 offset:17664\n" \
"v_mfma_f32_16x16x32_bf16 v[32:35], v[128:131], a[164:167], v[32:35]\n" \
"s_mov_b32 m0, s44\n" \
"s_nop 0\n" \
"buffer_load_dwordx4 v217, %[srdw], s49 offen offset:2048 lds\n" \
"v_mfma_f32_16x16x32_bf16 v[32:35], v[132:135], a[180:183], v[32:35]\n" \
"ds_read_b64_tr_b16 v[164:165], v225 offset:25600\n" \
"v_mfma_f32_16x16x32_bf16 v[64:67], v[128:131], a[168:171], v[64:67]\n" \
"ds_read_b64_tr_b16 v[166:167], v225 offset:25856\n" \
"v_mfma_f32_16x16x32_bf16 v[64:67], v[132:135], a[184:187], v[64:67]\n" \
"v_mfma_f32_16x16x32_bf16 v[96:99], v[128:131], a[172:175], v[96:99]\n" \
"ds_read_b64_tr_b16 v[168:169], v225 offset:17416\n" \
"v_mfma_f32_16x16x32_bf16 v[96:99], v[132:135], a[188:191], v[96:99]\n" \
"ds_read_b64_tr_b16 v[170:171], v225 offset:17672\n" \
"v_mfma_f32_16x16x32_bf16 v[4:7], v[136:139], a[160:163], v[4:7]\n" \
"buffer_load_dwordx4 v218, %[srdw], s49 offen offset:3072 lds\n" \
"v_mfma_f32_16x16x32_bf16 v[4:7], v[140:143], a[176:179], v[4:7]\n" \
"ds_read_b64_tr_b16 v[172:173], v225 offset:25608\n" \
"v_mfma_f32_16x16x32_bf16 v[36:39], v[136:139], a[164:167], v[36:39]\n" \
"ds_read_b64_tr_b16 v[174:175], v225 offset:25864\n" \
"v_mfma_f32_16x16x32_bf16 v[36:39], v[140:143], a[180:183], v[36:39]\n" \
"v_mfma_f32_16x16x32_bf16 v[68:71], v[136:139], a[168:171], v[68:71]\n" \
"ds_read_b64_tr_b16 v[176:177], v225 offset:17920\n" \
"v_mfma_f32_16x16x32_bf16 v[68:71], v[140:143], a[184:187], v[68:71]\n" \
"ds_read_b64_tr_b16 v[178:179], v225 offset:18176\n" \
"v_mfma_f32_16x16x32_bf16 v[100:103], v[136:139], a[172:175], v[100:103]\n" \
"s_add_u32 s50, s50, s53\n" \
"buffer_load_dwordx4 a[128:131], v227, %[srdz], s50 offen offset:0 sc1\n" \
"v_mfma_f32_16x16x32_bf16 v[100:103], v[140:143], a[188:191], v[100:103]\n" \
"ds_read_b64_tr_b16 v[180:181], v225 offset:26112\n" \
"v_mfma_f32_16x16x32_bf16 v[8:11], v[144:147], a[160:163], v[8:11]\n" \
"ds_read_b64_tr_b16 v[182:183], v225 offset:26368\n" \
"v_mfma_f32_16x16x32_bf16 v[8:11], v[148:151], a[176:179], v[8:11]\n" \
"v_mfma_f32_16x16x32_bf16 v[40:43], v[144:147], a[164:167], v[40:43]\n" \
"ds_read_b64_tr_b16 v[184:185], v225 offset:17928\n" \
"v_mfma_f32_16x16x32_bf16 v[40:43], v[148:151], a[180:183], v[40:43]\n" \
"ds_read_b64_tr_b16 v[186:187], v225 offset:18184\n" \
"v_mfma_f32_16x16x32_bf16 v[72:75], v[144:147], a[168:171], v[72:75]\n" \
"buffer_load_dwordx4 a[132:135], v219, %[srdz], s50 offen offset:0 sc1\n" \
"v_mfma_f32_16x16x32_bf16 v[72:75], v[148:151], a[184:187], v[72:75]\n" \
"ds_read_b64_tr_b16 v[188:189], v225 offset:26120\n" \
"v_mfma_f32_16x16x32_bf16 v[104:107], v[144:147], a[172:175], v[104:107]\n" \
"ds_read_b64_tr_b16 v[190:191], v225 offset:26376\n" \
"v_mfma_f32_16x16x32_bf16 v[104:107], v[148:151], a[188:191], v[104:107]\n" \
"v_mfma_f32_16x16x32_bf16 v[12:15], v[152:155], a[160:163], v[12:15]\n" \
"buffer_load_dwordx4 a[136:139], v220, %[srdz], s50 offen offset:0 sc1\n" \
"v_mfma_f32_16x16x32_bf16 v[12:15], v[156:159], a[176:179], v[12:15]\n" \
"v_mfma_f32_16x16x32_bf16 v[44:47], v[152:155], a[164:167], v[44:47]\n" \
"v_mfma_f32_16x16x32_bf16 v[44:47], v[156:159], a[180:183], v[44:47]\n" \
"v_mfma_f32_16x16x32_bf16 v[76:79], v[152:155], a[168:171], v[76:79]\n" \
"buffer_load_dwordx4 a[140:143], v221, %[srdz], s50 offen offset:0 sc1\n" \
"v_mfma_f32_16x16x32_bf16 v[76:79], v[156:159], a[184:187], v[76:79]\n" \
"v_mfma_f32_16x16x32_bf16 v[108:111], v[152:155], a[172:175], v[108:111]\n" \
"v_mfma_f32_16x16x32_bf16 v[108:111], v[156:159], a[188:191], v[108:111]\n" \
"s_waitcnt vmcnt(29) lgkmcnt(0)\n" \
"s_barrier\n" \
"v_mfma_f32_16x16x32_bf16 v[16:19], v[160:163], a[160:163], v[16:19]\n" \
"ds_read_b64_tr_b16 v[128:129], v225 offset:32768\n" \
"v_mfma_f32_16x16x32_bf16 v[16:19], v[164:167], a[176:179], v[16:19]\n" \
"ds_read_b64_tr_b16 v[130:131], v225 offset:33024\n" \
"v_mfma_f32_16x16x32_bf16 v[48:51], v[160:163], a[164:167], v[48:51]\n" \
"s_add_u32 s49, s49, s55\n" \
"s_mov_b32 m0, s45\n" \
"s_nop 0\n" \
"buffer_load_dwordx4 v226, %[srdw], s49 offen offset:0 lds\n" \
"v_mfma_f32_16x16x32_bf16 v[48:51], v[164:167], a[180:183], v[48:51]\n" \
"ds_read_b64_tr_b16 v[132:133], v225 offset:40960\n" \
"v_mfma_f32_16x16x32_bf16 v[80:83], v[160:163], a[168:171], v[80:83]\n" \
"ds_read_b64_tr_b16 v[134:135], v225 offset:41216\n" \
"v_mfma_f32_16x16x32_bf16 v[80:83], v[164:167], a[184:187], v[80:83]\n" \
"v_mfma_f32_16x16x32_bf16 v[112:115], v[160:163], a[172:175], v[112:115]\n" \
"ds_read_b64_tr_b16 v[136:137], v225 offset:32776\n" \
"v_mfma_f32_16x16x32_bf16 v[112:115], v[164:167], a[188:191], v[112:115]\n" \
"ds_read_b64_tr_b16 v[138:139], v225 offset:33032\n" \
"v_mfma_f32_16x16x32_bf16 v[20:23], v[168:171], a[160:163], v[20:23]\n" \
"buffer_load_dwordx4 v216, %[srdw], s49 offen offset:1024 lds\n" \
"v_mfma_f32_16x16x32_bf16 v[20:23], v[172:175], a[176:179], v[20:23]\n" \
"ds_read_b64_tr_b16 v[140:141], v225 offset:40968\n" \
"v_mfma_f32_16x16x32_bf16 v[52:55], v[168:171], a[164:167], v[52:55]\n" \
"ds_read_b64_tr_b16 v[142:143], v225 offset:41224\n" \
"v_mfma_f32_16x16x32_bf16 v[52:55], v[172:175], a[180:183], v[52:55]\n" \
"v_mfma_f32_16x16x32_bf16 v[84:87], v[168:171], a[168:171], v[84:87]\n" \
"ds_read_b64_tr_b16 v[144:145], v225 offset:33280\n" \
"v_mfma_f32_16x16x32_bf16 v[84:87], v[172:175], a[184:187], v[84:87]\n" \
"ds_read_b64_tr_b16 v[146:147], v225 offset:33536\n" \
"v_mfma_f32_16x16x32_bf16 v[116:119], v[168:171], a[172:175], v[116:119]\n" \
"buffer_load_dwordx4 a[144:147], v227, %[srdz], s50 offen offset:1024 sc1\n" \
"v_mfma_f32_16x16x32_bf16 v[116:119], v[172:175], a[188:191], v[116:119]\n" \
"ds_read_b64_tr_b16 v[148:149], v225 offset:41472\n" \
"v_mfma_f32_16x16x32_bf16 v[24:27], v[176:179], a[160:163], v[24:27]\n" \
"ds_read_b64_tr_b16 v[150:151], v225 offset:41728\n" \
"v_mfma_f32_16x16x32_bf16 v[24:27], v[180:183], a[176:179], v[24:27]\n" \
"v_mfma_f32_16x16x32_bf16 v[56:59], v[176:179], a[164:167], v[56:59]\n" \
"ds_read_b64_tr_b16 v[152:153], v225 offset:33288\n" \
"v_mfma_f32_16x16x32_bf16 v[56:59], v[180:183], a[180:183], v[56:59]\n" \
"ds_read_b64_tr_b16 v[154:155], v225 offset:33544\n" \
"v_mfma_f32_16x16x32_bf16 v[88:91], v[176:179], a[168:171], v[88:91]\n" \
"buffer_load_dwordx4 a[148:151], v219, %[srdz], s50 offen offset:1024 sc1\n" \
"v_mfma_f32_16x16x32_bf16 v[88:91], v[180:183], a[184:187], v[88:91]\n" \
"ds_read_b64_tr_b16 v[156:157], v225 offset:41480\n" \
"v_mfma_f32_16x16x32_bf16 v[120:123], v[176:179], a[172:175], v[120:123]\n" \
"ds_read_b64_tr_b16 v[158:159], v225 offset:41736\n" \
"v_mfma_f32_16x16x32_bf16 v[120:123], v[180:183], a[188:191], v[120:123]\n" \
"v_mfma_f32_16x16x32_bf16 v[28:31], v[184:187], a[160:163], v[28:31]\n" \
"buffer_load_dwordx4 a[152:155], v220, %[srdz], s50 offen offset:1024 sc1\n" \
"v_mfma_f32_16x16x32_bf16 v[28:31], v[188:191], a[176:179], v[28:31]\n" \
"v_mfma_f32_16x16x32_bf16 v[60:63], v[184:187], a[164:167], v[60:63]\n" \
"v_mfma_f32_16x16x32_bf16 v[60:63], v[188:191], a[180:183], v[60:63]\n" \
"v_mfma_f32_16x16x32_bf16 v[92:95], v[184:187], a[168:171], v[92:95]\n" \
"buffer_load_dwordx4 a[156:159], v221, %[srdz], s50 offen offset:1024 sc1\n" \
"v_mfma_f32_16x16x32_bf16 v[92:95], v[188:191], a[184:187], v[92:95]\n" \
"v_mfma_f32_16x16x32_bf16 v[124:127], v[184:187], a[172:175], v[124:127]\n" \
"v_mfma_f32_16x16x32_bf16 v[124:127], v[188:191], a[188:191], v[124:127]\n" \
"s_waitcnt vmcnt(24) lgkmcnt(0)\n" \
"v_mfma_f32_16x16x32_bf16 v[0:3], v[128:131], a[192:195], v[0:3]\n" \
"ds_read_b64_tr_b16 v[160:161], v225 offset:33792\n" \
"v_mfma_f32_16x16x32_bf16 v[0:3], v[132:135], a[208:211], v[0:3]\n" \
"ds_read_b64_tr_b16 v[162:163], v225 offset:34048\n" \
"v_mfma_f32_16x16x32_bf16 v[32:35], v[128:131], a[196:199], v[32:35]\n" \
"s_mov_b32 m0, s45\n" \
"s_nop 0\n" \
"buffer_load_dwordx4 v217, %[srdw], s49 offen offset:2048 lds\n" \
"v_mfma_f32_16x16x32_bf16 v[32:35], v[132:135], a[212:215], v[32:35]\n" \
"ds_read_b64_tr_b16 v[164:165], v225 offset:41984\n" \
"v_mfma_f32_16x16x32_bf16 v[64:67], v[128:131], a[200:203], v[64:67]\n" \
"ds_read_b64_tr_b16 v[166:167], v225 offset:42240\n" \
"v_mfma_f32_16x16x32_bf16 v[64:67], v[132:135], a[216:219], v[64:67]\n" \
"v_mfma_f32_16x16x32_bf16 v[96:99], v[128:131], a[204:207], v[96:99]\n" \
"ds_read_b64_tr_b16 v[168:169], v225 offset:33800\n" \
"v_mfma_f32_16x16x32_bf16 v[96:99], v[132:135], a[220:223], v[96:99]\n" \
"ds_read_b64_tr_b16 v[170:171], v225 offset:34056\n" \
"v_mfma_f32_16x16x32_bf16 v[4:7], v[136:139], a[192:195], v[4:7]\n" \
"buffer_load_dwordx4 v218, %[srdw], s49 offen offset:3072 lds\n" \
"v_mfma_f32_16x16x32_bf16 v[4:7], v[140:143], a[208:211], v[4:7]\n" \
"ds_read_b64_tr_b16 v[172:173], v225 offset:41992\n" \
"v_mfma_f32_16x16x32_bf16 v[36:39], v[136:139], a[196:199], v[36:39]\n" \
"ds_read_b64_tr_b16 v[174:175], v225 offset:42248\n" \
"v_mfma_f32_16x16x32_bf16 v[36:39], v[140:143], a[212:215], v[36:39]\n" \
"v_mfma_f32_16x16x32_bf16 v[68:71], v[136:139], a[200:203], v[68:71]\n" \
"ds_read_b64_tr_b16 v[176:177], v225 offset:34304\n" \
"v_mfma_f32_16x16x32_bf16 v[68:71], v[140:143], a[216:219], v[68:71]\n" \
"ds_read_b64_tr_b16 v[178:179], v225 offset:34560\n" \
"v_mfma_f32_16x16x32_bf16 v[100:103], v[136:139], a[204:207], v[100:103]\n" \
"buffer_load_dwordx4 a[160:163], v227, %[srdz], s50 offen offset:2048 sc1\n" \
"v_mfma_f32_16x16x32_bf16 v[100:103], v[140:143], a[220:223], v[100:103]\n" \
"ds_read_b64_tr_b16 v[180:181], v225 offset:42496\n" \
"v_mfma_f32_16x16x32_bf16 v[8:11], v[144:147], a[192:195], v[8:11]\n" \
"ds_read_b64_tr_b16 v[182:183], v225 offset:42752\n" \
"v_mfma_f32_16x16x32_bf16 v[8:11], v[148:151], a[208:211], v[8:11]\n" \
"v_mfma_f32_16x16x32_bf16 v[40:43], v[144:147], a[196:199], v[40:43]\n" \
"ds_read_b64_tr_b16 v[184:185], v225 offset:34312\n" \
"v_mfma_f32_16x16x32_bf16 v[40:43], v[148:151], a[212:215], v[40:43]\n" \
"ds_read_b64_tr_b16 v[186:187], v225 offset:34568\n" \
"v_mfma_f32_16x16x32_bf16 v[72:75], v[144:147], a[200:203], v[72:75]\n" \
"buffer_load_dwordx4 a[164:167], v219, %[srdz], s50 offen offset:2048 sc1\n" \
"v_mfma_f32_16x16x32_bf16 v[72:75], v[148:151], a[216:219], v[72:75]\n" \
"ds_read_b64_tr_b16 v[188:189], v225 offset:42504\n" \
"v_mfma_f32_16x16x32_bf16 v[104:107], v[144:147], a[204:207], v[104:107]\n" \
"ds_read_b64_tr_b16 v[190:191], v225 offset:42760\n" \
"v_mfma_f32_16x16x32_bf16 v[104:107], v[148:151], a[220:223], v[104:107]\n" \
"v_mfma_f32_16x16x32_bf16 v[12:15], v[152:155], a[192:195], v[12:15]\n" \
"buffer_load_dwordx4 a[168:171], v220, %[srdz], s50 offen offset:2048 sc1\n" \
"v_mfma_f32_16x16x32_bf16 v[12:15], v[156:159], a[208:211], v[12:15]\n" \
"v_mfma_f32_16x16x32_bf16 v[44:47], v[152:155], a[196:199], v[44:47]\n" \
"v_mfma_f32_16x16x32_bf16 v[44:47], v[156:159], a[212:215], v[44:47]\n" \
"v_mfma_f32_16x16x32_bf16 v[76:79], v[152:155], a[200:203], v[76:79]\n" \
"buffer_load_dwordx4 a[172:175], v221, %[srdz], s50 offen offset:2048 sc1\n" \
"v_mfma_f32_16x16x32_bf16 v[76:79], v[156:159], a[216:219], v[76:79]\n" \
"v_mfma_f32_16x16x32_bf16 v[108:111], v[152:155], a[204:207], v[108:111]\n" \
"v_mfma_f32_16x16x32_bf16 v[108:111], v[156:159], a[220:223], v[108:111]\n" \
"s_waitcnt vmcnt(28) lgkmcnt(0)\n" \
"s_barrier\n" \
"v_mfma_f32_16x16x32_bf16 v[16:19], v[160:163], a[192:195], v[16:19]\n" \
"ds_read_b64_tr_b16 v[128:129], v225 offset:49152\n" \
"v_mfma_f32_16x16x32_bf16 v[16:19], v[164:167], a[208:211], v[16:19]\n" \
"ds_read_b64_tr_b16 v[130:131], v225 offset:49408\n" \
"v_mfma_f32_16x16x32_bf16 v[48:51], v[160:163], a[196:199], v[48:51]\n" \
"s_add_u32 s49, s49, s55\n" \
"s_mov_b32 m0, s46\n" \
"s_nop 0\n" \
"buffer_load_dwordx4 v226, %[srdw], s49 offen offset:0 lds\n" \
"v_mfma_f32_16x16x32_bf16 v[48:51], v[164:167], a[212:215], v[48:51]\n" \
"ds_read_b64_tr_b16 v[132:133], v225 offset:57344\n" \
"v_mfma_f32_16x16x32_bf16 v[80:83], v[160:163], a[200:203], v[80:83]\n" \
"ds_read_b64_tr_b16 v[134:135], v225 offset:57600\n" \
"v_mfma_f32_16x16x32_bf16 v[80:83], v[164:167], a[216:219], v[80:83]\n" \
"v_mfma_f32_16x16x32_bf16 v[112:115], v[160:163], a[204:207], v[112:115]\n" \
"ds_read_b64_tr_b16 v[136:137], v225 offset:49160\n" \
"v_mfma_f32_16x16x32_bf16 v[112:115], v[164:167], a[220:223], v[112:115]\n" \
"ds_read_b64_tr_b16 v[138:139], v225 offset:49416\n" \
"v_mfma_f32_16x16x32_bf16 v[20:23], v[168:171], a[192:195], v[20:23]\n" \
"buffer_load_dwordx4 v216, %[srdw], s49 offen offset:1024 lds\n" \
"v_mfma_f32_16x16x32_bf16 v[20:23], v[172:175], a[208:211], v[20:23]\n" \
"ds_read_b64_tr_b16 v[140:141], v225 offset:57352\n" \
"v_mfma_f32_16x16x32_bf16 v[52:55], v[168:171], a[196:199], v[52:55]\n" \
"ds_read_b64_tr_b16 v[142:143], v225 offset:57608\n" \
"v_mfma_f32_16x16x32_bf16 v[52:55], v[172:175], a[212:215], v[52:55]\n" \
"v_mfma_f32_16x16x32_bf16 v[84:87], v[168:171], a[200:203], v[84:87]\n" \
"ds_read_b64_tr_b16 v[144:145], v225 offset:49664\n" \
"v_mfma_f32_16x16x32_bf16 v[84:87], v[172:175], a[216:219], v[84:87]\n" \
"ds_read_b64_tr_b16 v[146:147], v225 offset:49920\n" \
"v_mfma_f32_16x16x32_bf16 v[116:119], v[168:171], a[204:207], v[116:119]\n" \
"buffer_load_dwordx4 a[176:179], v227, %[srdz], s50 offen offset:3072 sc1\n" \
"v_mfma_f32_16x16x32_bf16 v[116:119], v[172:175], a[220:223], v[116:119]\n" \
"ds_read_b64_tr_b16 v[148:149], v225 offset:57856\n" \
"v_mfma_f32_16x16x32_bf16 v[24:27], v[176:179], a[192:195], v[24:27]\n" \
"ds_read_b64_tr_b16 v[150:151], v225 offset:58112\n" \
"v_mfma_f32_16x16x32_bf16 v[24:27], v[180:183], a[208:211], v[24:27]\n" \
"v_mfma_f32_16x16x32_bf16 v[56:59], v[176:179], a[196:199], v[56:59]\n" \
"ds_read_b64_tr_b16 v[152:153], v225 offset:49672\n" \
"v_mfma_f32_16x16x32_bf16 v[56:59], v[180:183], a[212:215], v[56:59]\n" \
"ds_read_b64_tr_b16 v[154:155], v225 offset:49928\n" \
"v_mfma_f32_16x16x32_bf16 v[88:91], v[176:179], a[200:203], v[88:91]\n" \
"buffer_load_dwordx4 a[180:183], v219, %[srdz], s50 offen offset:3072 sc1\n" \
"v_mfma_f32_16x16x32_bf16 v[88:91], v[180:183], a[216:219], v[88:91]\n" \
"ds_read_b64_tr_b16 v[156:157], v225 offset:57864\n" \
"v_mfma_f32_16x16x32_bf16 v[120:123], v[176:179], a[204:207], v[120:123]\n" \
"ds_read_b64_tr_b16 v[158:159], v225 offset:58120\n" \
"v_mfma_f32_16x16x32_bf16 v[120:123], v[180:183], a[220:223], v[120:123]\n" \
"v_mfma_f32_16x16x32_bf16 v[28:31], v[184:187], a[192:195], v[28:31]\n" \
"buffer_load_dwordx4 a[184:187], v220, %[srdz], s50 offen offset:3072 sc1\n" \
"v_mfma_f32_16x16x32_bf16 v[28:31], v[188:191], a[208:211], v[28:31]\n" \
"v_mfma_f32_16x16x32_bf16 v[60:63], v[184:187], a[196:199], v[60:63]\n" \
"v_mfma_f32_16x16x32_bf16 v[60:63], v[188:191], a[212:215], v[60:63]\n" \
"v_mfma_f32_16x16x32_bf16 v[92:95], v[184:187], a[200:203], v[92:95]\n" \
"buffer_load_dwordx4 a[188:191], v221, %[srdz], s50 offen offset:3072 sc1\n" \
"v_mfma_f32_16x16x32_bf16 v[92:95], v[188:191], a[216:219], v[92:95]\n" \
"v_mfma_f32_16x16x32_bf16 v[124:127], v[184:187], a[204:207], v[124:127]\n" \
"v_mfma_f32_16x16x32_bf16 v[124:127], v[188:191], a[220:223], v[124:127]\n" \
"s_waitcnt vmcnt(24) lgkmcnt(0)\n" \
"v_mfma_f32_16x16x32_bf16 v[0:3], v[128:131], a[224:227], v[0:3]\n" \
"ds_read_b64_tr_b16 v[160:161], v225 offset:50176\n" \
"v_mfma_f32_16x16x32_bf16 v[0:3], v[132:135], a[240:243], v[0:3]\n" \
"ds_read_b64_tr_b16 v[162:163], v225 offset:50432\n" \
"v_mfma_f32_16x16x32_bf16 v[32:35], v[128:131], a[228:231], v[32:35]\n" \
"s_mov_b32 m0, s46\n" \
"s_nop 0\n" \
"buffer_load_dwordx4 v217, %[srdw], s49 offen offset:2048 lds\n" \
"v_mfma_f32_16x16x32_bf16 v[32:35], v[132:135], a[244:247], v[32:35]\n" \
"ds_read_b64_tr_b16 v[164:165], v225 offset:58368\n" \
"v_mfma_f32_16x16x32_bf16 v[64:67], v[128:131], a[232:235], v[64:67]\n" \
"ds_read_b64_tr_b16 v[166:167], v225 offset:58624\n" \
"v_mfma_f32_16x16x32_bf16 v[64:67], v[132:135], a[248:251], v[64:67]\n" \
"v_mfma_f32_16x16x32_bf16 v[96:99], v[128:131], a[236:239], v[96:99]\n" \
"ds_read_b64_tr_b16 v[168:169], v225 offset:50184\n" \
"v_mfma_f32_16x16x32_bf16 v[96:99], v[132:135], a[252:255], v[96:99]\n" \
"ds_read_b64_tr_b16 v[170:171], v225 offset:50440\n" \
"v_mfma_f32_16x16x32_bf16 v[4:7], v[136:139], a[224:227], v[4:7]\n" \
"buffer_load_dwordx4 v218, %[srdw], s49 offen offset:3072 lds\n" \
"v_mfma_f32_16x16x32_bf16 v[4:7], v[140:143], a[240:243], v[4:7]\n" \
"ds_read_b64_tr_b16 v[172:173], v225 offset:58376\n" \
"v_mfma_f32_16x16x32_bf16 v[36:39], v[136:139], a[228:231], v[36:39]\n" \
"ds_read_b64_tr_b16 v[174:175], v225 offset:58632\n" \
"v_mfma_f32_16x16x32_bf16 v[36:39], v[140:143], a[244:247], v[36:39]\n" \
"v_mfma_f32_16x16x32_bf16 v[68:71], v[136:139], a[232:235], v[68:71]\n" \
"ds_read_b64_tr_b16 v[176:177], v225 offset:50688\n" \
"v_mfma_f32_16x16x32_bf16 v[68:71], v[140:143], a[248:251], v[68:71]\n" \
"ds_read_b64_tr_b16 v[178:179], v225 offset:50944\n" \
"v_mfma_f32_16x16x32_bf16 v[100:103], v[136:139], a[236:239], v[100:103]\n" \
"s_add_u32 s50, s50, s53\n" \
"buffer_load_dwordx4 a[192:195], v227, %[srdz], s50 offen offset:0 sc1\n" \
"v_mfma_f32_16x16x32_bf16 v[100:103], v[140:143], a[252:255], v[100:103]\n" \
"ds_read_b64_tr_b16 v[180:181], v225 offset:58880\n" \
"v_mfma_f32_16x16x32_bf16 v[8:11], v[144:147], a[224:227], v[8:11]\n" \
"ds_read_b64_tr_b16 v[182:183], v225 offset:59136\n" \
"v_mfma_f32_16x16x32_bf16 v[8:11], v[148:151], a[240:243], v[8:11]\n" \
"v_mfma_f32_16x16x32_bf16 v[40:43], v[144:147], a[228:231], v[40:43]\n" \
"ds_read_b64_tr_b16 v[184:185], v225 offset:50696\n" \
"v_mfma_f32_16x16x32_bf16 v[40:43], v[148:151], a[244:247], v[40:43]\n" \
"ds_read_b64_tr_b16 v[186:187], v225 offset:50952\n" \
"v_mfma_f32_16x16x32_bf16 v[72:75], v[144:147], a[232:235], v[72:75]\n" \
"buffer_load_dwordx4 a[196:199], v219, %[srdz], s50 offen offset:0 sc1\n" \
"v_mfma_f32_16x16x32_bf16 v[72:75], v[148:151], a[248:251], v[72:75]\n" \
"ds_read_b64_tr_b16 v[188:189], v225 offset:58888\n" \
"v_mfma_f32_16x16x32_bf16 v[104:107], v[144:147], a[236:239], v[104:107]\n" \
"ds_read_b64_tr_b16 v[190:191], v225 offset:59144\n" \
"v_mfma_f32_16x16x32_bf16 v[104:107], v[148:151], a[252:255], v[104:107]\n" \
"v_mfma_f32_16x16x32_bf16 v[12:15], v[152:155], a[224:227], v[12:15]\n" \
"buffer_load_dwordx4 a[200:203], v220, %[srdz], s50 offen offset:0 sc1\n" \
"v_mfma_f32_16x16x32_bf16 v[12:15], v[156:159], a[240:243], v[12:15]\n" \
"v_mfma_f32_16x16x32_bf16 v[44:47], v[152:155], a[228:231], v[44:47]\n" \
"buffer_load_dword v237, v236, %[srdw], s60 offen\n" \
"s_add_u32 s60, s60, 0x200000\n" \
"v_mfma_f32_16x16x32_bf16 v[44:47], v[156:159], a[244:247], v[44:47]\n" \
"v_mfma_f32_16x16x32_bf16 v[76:79], v[152:155], a[232:235], v[76:79]\n" \
"buffer_load_dwordx4 a[204:207], v221, %[srdz], s50 offen offset:0 sc1\n" \
"v_mfma_f32_16x16x32_bf16 v[76:79], v[156:159], a[248:251], v[76:79]\n" \
".if . - adaln_body != 4096\n" \
".error \"size model\"\n" \
".endif\n" \
"Lk0_%=:\n" \
"s_cbranch_execz Lk1_%=\n" \
"s_nop 0\n" \
"v_mfma_f32_16x16x32_bf16 v[108:111], v[152:155], a[236:239], v[108:111]\n" \
"v_mfma_f32_16x16x32_bf16 v[108:111], v[156:159], a[252:255], v[108:111]\n" \
"s_waitcnt vmcnt(29) lgkmcnt(0)\n" \
"s_barrier\n" \
"v_mfma_f32_16x16x32_bf16 v[16:19], v[160:163], a[224:227], v[16:19]\n" \
"ds_read_b64_tr_b16 v[128:129], v225 offset:0\n" \
"v_mfma_f32_16x16x32_bf16 v[16:19], v[164:167], a[240:243], v[16:19]\n" \
"ds_read_b64_tr_b16 v[130:131], v225 offset:256\n" \
"v_mfma_f32_16x16x32_bf16 v[48:51], v[160:163], a[228:231], v[48:51]\n" \
"s_add_u32 s49, s49, s55\n" \
"s_mov_b32 m0, s47\n" \
"s_nop 0\n" \
"buffer_load_dwordx4 v226, %[srdw], s49 offen offset:0 lds\n" \
"v_mfma_f32_16x16x32_bf16 v[48:51], v[164:167], a[244:247], v[48:51]\n" \
"ds_read_b64_tr_b16 v[132:133], v225 offset:8192\n" \
"v_mfma_f32_16x16x32_bf16 v[80:83], v[160:163], a[232:235], v[80:83]\n" \
"ds_read_b64_tr_b16 v[134:135], v225 offset:8448\n" \
"v_mfma_f32_16x16x32_bf16 v[80:83], v[164:167], a[248:251], v[80:83]\n" \
"v_mfma_f32_16x16x32_bf16 v[112:115], v[160:163], a[236:239], v[112:115]\n" \
"ds_read_b64_tr_b16 v[136:137], v225 offset:8\n" \
"v_mfma_f32_16x16x32_bf16 v[112:115], v[164:167], a[252:255], v[112:115]\n" \
"ds_read_b64_tr_b16 v[138:139], v225 offset:264\n" \
"v_mfma_f32_16x16x32_bf16 v[20:23], v[168:171], a[224:227], v[20:23]\n" \
"buffer_load_dwordx4 v216, %[srdw], s49 offen offset:1024 lds\n" \
"v_mfma_f32_16x16x32_bf16 v[20:23], v[172:175], a[240:243], v[20:23]\n" \
"ds_read_b64_tr_b16 v[140:141], v225 offset:8200\n" \
"v_mfma_f32_16x16x32_bf16 v[52:55], v[168:171], a[228:231], v[52:55]\n" \
"ds_read_b64_tr_b16 v[142:143], v225 offset:8456\n" \
"v_mfma_f32_16x16x32_bf16 v[52:55], v[172:175], a[244:247], v[52:55]\n" \
"v_mfma_f32_16x16x32_bf16 v[84:87], v[168:171], a[232:235], v[84:87]\n" \
"ds_read_b64_tr_b16 v[144:145], v225 offset:512\n" \
"v_mfma_f32_16x16x32_bf16 v[84:87], v[172:175], a[248:251], v[84:87]\n" \
"ds_read_b64_tr_b16 v[146:147], v225 offset:768\n" \
"v_mfma_f32_16x16x32_bf16 v[116:119], v[168:171], a[236:239], v[116:119]\n" \
"buffer_load_dwordx4 a[208:211], v227, %[srdz], s50 offen offset:1024 sc1\n" \
"v_mfma_f32_16x16x32_bf16 v[116:119], v[172:175], a[252:255], v[116:119]\n" \
"ds_read_b64_tr_b16 v[148:149], v225 offset:8704\n" \
"v_mfma_f32_16x16x32_bf16 v[24:27], v[176:179], a[224:227], v[24:27]\n" \
"ds_read_b64_tr_b16 v[150:151], v225 offset:8960\n" \
"v_mfma_f32_16x16x32_bf16 v[24:27], v[180:183], a[240:243], v[24:27]\n" \
"v_mfma_f32_16x16x32_bf16 v[56:59], v[176:179], a[228:231], v[56:59]\n" \
"ds_read_b64_tr_b16 v[152:153], v225 offset:520\n" \
"v_mfma_f32_16x16x32_bf16 v[56:59], v[180:183], a[244:247], v[56:59]\n" \
"ds_read_b64_tr_b16 v[154:155], v225 offset:776\n" \
"v_mfma_f32_16x16x32_bf16 v[88:91], v[176:179], a[232:235], v[88:91]\n" \
"buffer_load_dwordx4 a[212:215], v219, %[srdz], s50 offen offset:1024 sc1\n" \
"v_mfma_f32_16x16x32_bf16 v[88:91], v[180:183], a[248:251], v[88:91]\n" \
"ds_read_b64_tr_b16 v[156:157], v225 offset:8712\n" \
"v_mfma_f32_16x16x32_bf16 v[120:123], v[176:179], a[236:239], v[120:123]\n" \
"ds_read_b64_tr_b16 v[158:159], v225 offset:8968\n" \
"v_mfma_f32_16x16x32_bf16 v[120:123], v[180:183], a[252:255], v[120:123]\n" \
"v_mfma_f32_16x16x32_bf16 v[28:31], v[184:187], a[224:227], v[28:31]\n" \
"buffer_load_dwordx4 a[216:219], v220, %[srdz], s50 offen offset:1024 sc1\n" \
"v_mfma_f32_16x16x32_bf16 v[28:31], v[188:191], a[240:243], v[28:31]\n" \
"v_mfma_f32_16x16x32_bf16 v[60:63], v[184:187], a[228:231], v[60:63]\n" \
"v_mfma_f32_16x16x32_bf16 v[60:63], v[188:191], a[244:247], v[60:63]\n" \
"v_mfma_f32_16x16x32_bf16 v[92:95], v[184:187], a[232:235], v[92:95]\n" \
"buffer_load_dwordx4 a[220:223], v221, %[srdz], s50 offen offset:1024 sc1\n" \
"v_mfma_f32_16x16x32_bf16 v[92:95], v[188:191], a[248:251], v[92:95]\n" \
"v_mfma_f32_16x16x32_bf16 v[124:127], v[184:187], a[236:239], v[124:127]\n" \
"v_mfma_f32_16x16x32_bf16 v[124:127], v[188:191], a[252:255], v[124:127]\n" \
"s_waitcnt vmcnt(25) lgkmcnt(0)\n" \
"s_sub_u32 s51, s51, 1\n" \
"s_cmp_lg_u32 s51, 0\n" \
"s_cbranch_scc1 Lq_%=\n" \
"s_nop 0\n" \
"v_mfma_f32_16x16x32_bf16 v[0:3], v[128:131], a[128:131], v[0:3]\n" \
"ds_read_b64_tr_b16 v[160:161], v225 offset:1024\n" \
"v_mfma_f32_16x16x32_bf16 v[0:3], v[132:135], a[144:147], v[0:3]\n" \
"ds_read_b64_tr_b16 v[162:163], v225 offset:1280\n" \
"v_mfma_f32_16x16x32_bf16 v[32:35], v[128:131], a[132:135], v[32:35]\n" \
"s_mov_b32 m0, s47\n" \
"s_nop 0\n" \
"buffer_load_dwordx4 v217, %[srdw], s49 offen offset:2048 lds\n" \
"v_mfma_f32_16x16x32_bf16 v[32:35], v[132:135], a[148:151], v[32:35]\n" \
"ds_read_b64_tr_b16 v[164:165], v225 offset:9216\n" \
"v_mfma_f32_16x16x32_bf16 v[64:67], v[128:131], a[136:139], v[64:67]\n" \
"ds_read_b64_tr_b16 v[166:167], v225 offset:9472\n" \
"v_mfma_f32_16x16x32_bf16 v[64:67], v[132:135], a[152:155], v[64:67]\n" \
"v_mfma_f32_16x16x32_bf16 v[96:99], v[128:131], a[140:143], v[96:99]\n" \
"ds_read_b64_tr_b16 v[168:169], v225 offset:1032\n" \
"v_mfma_f32_16x16x32_bf16 v[96:99], v[132:135], a[156:159], v[96:99]\n" \
"ds_read_b64_tr_b16 v[170:171], v225 offset:1288\n" \
"v_mfma_f32_16x16x32_bf16 v[4:7], v[136:139], a[128:131], v[4:7]\n" \
"buffer_load_dwordx4 v218, %[srdw], s49 offen offset:3072 lds\n" \
"v_mfma_f32_16x16x32_bf16 v[4:7], v[140:143], a[144:147], v[4:7]\n" \
"ds_read_b64_tr_b16 v[172:173], v225 offset:9224\n" \
"v_mfma_f32_16x16x32_bf16 v[36:39], v[136:139], a[132:135], v[36:39]\n" \
"ds_read_b64_tr_b16 v[174:175], v225 offset:9480\n" \
"v_mfma_f32_16x16x32_bf16 v[36:39], v[140:143], a[148:151], v[36:39]\n" \
"v_mfma_f32_16x16x32_bf16 v[68:71], v[136:139], a[136:139], v[68:71]\n" \
"ds_read_b64_tr_b16 v[176:177], v225 offset:1536\n" \
"v_mfma_f32_16x16x32_bf16 v[68:71], v[140:143], a[152:155], v[68:71]\n" \
"ds_read_b64_tr_b16 v[178:179], v225 offset:1792\n" \
"v_mfma_f32_16x16x32_bf16 v[100:103], v[136:139], a[140:143], v[100:103]\n" \
"buffer_load_dwordx4 a[224:227], v227, %[srdz], s50 offen offset:2048 sc1\n" \
"v_mfma_f32_16x16x32_bf16 v[100:103], v[140:143], a[156:159], v[100:103]\n" \
"ds_read_b64_tr_b16 v[180:181], v225 offset:9728\n" \
"v_mfma_f32_16x16x32_bf16 v[8:11], v[144:147], a[128:131], v[8:11]\n" \
"ds_read_b64_tr_b16 v[182:183], v225 offset:9984\n" \
"v_mfma_f32_16x16x32_bf16 v[8:11], v[148:151], a[144:147], v[8:11]\n" \
"v_mfma_f32_16x16x32_bf16 v[40:43], v[144:147], a[132:135], v[40:43]\n" \
"ds_read_b64_tr_b16 v[184:185], v225 offset:1544\n" \
"v_mfma_f32_16x16x32_bf16 v[40:43], v[148:151], a[148:151], v[40:43]\n" \
"ds_read_b64_tr_b16 v[186:187], v225 offset:1800\n" \
"v_mfma_f32_16x16x32_bf16 v[72:75], v[144:147], a[136:139], v[72:75]\n" \
"buffer_load_dwordx4 a[228:231], v219, %[srdz], s50 offen offset:2048 sc1\n" \
"v_mfma_f32_16x16x32_bf16 v[72:75], v[148:151], a[152:155], v[72:75]\n" \
"ds_read_b64_tr_b16 v[188:189], v225 offset:9736\n" \
"v_mfma_f32_16x16x32_bf16 v[104:107], v[144:147], a[140:143], v[104:107]\n" \
"ds_read_b64_tr_b16 v[190:191], v225 offset:9992\n" \
"v_mfma_f32_16x16x32_bf16 v[104:107], v[148:151], a[156:159], v[104:107]\n" \
"v_mfma_f32_16x16x32_bf16 v[12:15], v[152:155], a[128:131], v[12:15]\n" \
"buffer_load_dwordx4 a[232:235], v220, %[srdz], s50 offen offset:2048 sc1\n" \
"v_mfma_f32_16x16x32_bf16 v[12:15], v[156:159], a[144:147], v[12:15]\n" \
"v_mfma_f32_16x16x32_bf16 v[44:47], v[152:155], a[132:135], v[44:47]\n" \
"v_mfma_f32_16x16x32_bf16 v[44:47], v[156:159], a[148:151], v[44:47]\n" \
"v_mfma_f32_16x16x32_bf16 v[76:79], v[152:155], a[136:139], v[76:79]\n" \
"buffer_load_dwordx4 a[236:239], v221, %[srdz], s50 offen offset:2048 sc1\n" \
"v_mfma_f32_16x16x32_bf16 v[76:79], v[156:159], a[152:155], v[76:79]\n" \
"v_mfma_f32_16x16x32_bf16 v[108:111], v[152:155], a[140:143], v[108:111]\n" \
"v_mfma_f32_16x16x32_bf16 v[108:111], v[156:159], a[156:159], v[108:111]\n" \
"s_waitcnt vmcnt(29) lgkmcnt(0)\n" \
"s_barrier\n" \
"v_mfma_f32_16x16x32_bf16 v[16:19], v[160:163], a[128:131], v[16:19]\n" \
"ds_read_b64_tr_b16 v[128:129], v225 offset:16384\n" \
"v_mfma_f32_16x16x32_bf16 v[16:19], v[164:167], a[144:147], v[16:19]\n" \
"ds_read_b64_tr_b16 v[130:131], v225 offset:16640\n" \
"v_mfma_f32_16x16x32_bf16 v[48:51], v[160:163], a[132:135], v[48:51]\n" \
"s_mov_b32 s48, 16384\n" \
"s_mov_b32 m0, s44\n" \
"s_nop 0\n" \
"buffer_load_dwordx4 %[vd0], %[srdv], s48 offen offset:0 lds\n" \
"v_mfma_f32_16x16x32_bf16 v[48:51], v[164:167], a[148:151], v[48:51]\n" \
"ds_read_b64_tr_b16 v[132:133], v225 offset:24576\n" \
"v_mfma_f32_16x16x32_bf16 v[80:83], v[160:163], a[136:139], v[80:83]\n" \
"ds_read_b64_tr_b16 v[134:135], v225 offset:24832\n" \
"v_mfma_f32_16x16x32_bf16 v[80:83], v[164:167], a[152:155], v[80:83]\n" \
"v_mfma_f32_16x16x32_bf16 v[112:115], v[160:163], a[140:143], v[112:115]\n" \
"ds_read_b64_tr_b16 v[136:137], v225 offset:16392\n" \
"v_mfma_f32_16x16x32_bf16 v[112:115], v[164:167], a[156:159], v[112:115]\n" \
"ds_read_b64_tr_b16 v[138:139], v225 offset:16648\n" \
"v_mfma_f32_16x16x32_bf16 v[20:23], v[168:171], a[128:131], v[20:23]\n" \
"buffer_load_dwordx4 v215, %[srdv], s48 offen offset:1024 lds\n" \
"v_mfma_f32_16x16x32_bf16 v[20:23], v[172:175], a[144:147], v[20:23]\n" \
"ds_read_b64_tr_b16 v[140:141], v225 offset:24584\n" \
"v_mfma_f32_16x16x32_bf16 v[52:55], v[168:171], a[132:135], v[52:55]\n" \
"ds_read_b64_tr_b16 v[142:143], v225 offset:24840\n" \
"v_mfma_f32_16x16x32_bf16 v[52:55], v[172:175], a[148:151], v[52:55]\n" \
"v_mfma_f32_16x16x32_bf16 v[84:87], v[168:171], a[136:139], v[84:87]\n" \
"ds_read_b64_tr_b16 v[144:145], v225 offset:16896\n" \
"v_mfma_f32_16x16x32_bf16 v[84:87], v[172:175], a[152:155], v[84:87]\n" \
"ds_read_b64_tr_b16 v[146:147], v225 offset:17152\n" \
"v_mfma_f32_16x16x32_bf16 v[116:119], v[168:171], a[140:143], v[116:119]\n" \
"buffer_load_dwordx4 a[240:243], v227, %[srdz], s50 offen offset:3072 sc1\n" \
"v_mfma_f32_16x16x32_bf16 v[116:119], v[172:175], a[156:159], v[116:119]\n" \
"ds_read_b64_tr_b16 v[148:149], v225 offset:25088\n" \
"v_mfma_f32_16x16x32_bf16 v[24:27], v[176:179], a[128:131], v[24:27]\n" \
"ds_read_b64_tr_b16 v[150:151], v225 offset:25344\n" \
"v_mfma_f32_16x16x32_bf16 v[24:27], v[180:183], a[144:147], v[24:27]\n" \
"v_mfma_f32_16x16x32_bf16 v[56:59], v[176:179], a[132:135], v[56:59]\n" \
"ds_read_b64_tr_b16 v[152:153], v225 offset:16904\n" \
"v_mfma_f32_16x16x32_bf16 v[56:59], v[180:183], a[148:151], v[56:59]\n" \
"ds_read_b64_tr_b16 v[154:155], v225 offset:17160\n" \
"v_mfma_f32_16x16x32_bf16 v[88:91], v[176:179], a[136:139], v[88:91]\n" \
"buffer_load_dwordx4 a[244:247], v219, %[srdz], s50 offen offset:3072 sc1\n" \
"v_mfma_f32_16x16x32_bf16 v[88:91], v[180:183], a[152:155], v[88:91]\n" \
"ds_read_b64_tr_b16 v[156:157], v225 offset:25096\n" \
"v_mfma_f32_16x16x32_bf16 v[120:123], v[176:179], a[140:143], v[120:123]\n" \
"ds_read_b64_tr_b16 v[158:159], v225 offset:25352\n" \
"v_mfma_f32_16x16x32_bf16 v[120:123], v[180:183], a[156:159], v[120:123]\n" \
"v_mfma_f32_16x16x32_bf16 v[28:31], v[184:187], a[128:131], v[28:31]\n" \
"buffer_load_dwordx4 a[248:251], v220, %[srdz], s50 offen offset:3072 sc1\n" \
"v_mfma_f32_16x16x32_bf16 v[28:31], v[188:191], a[144:147], v[28:31]\n" \
"v_mfma_f32_16x16x32_bf16 v[60:63], v[184:187], a[132:135], v[60:63]\n" \
"v_mfma_f32_16x16x32_bf16 v[60:63], v[188:191], a[148:151], v[60:63]\n" \
"v_mfma_f32_16x16x32_bf16 v[92:95], v[184:187], a[136:139], v[92:95]\n" \
"buffer_load_dwordx4 a[252:255], v221, %[srdz], s50 offen offset:3072 sc1\n" \
"v_mfma_f32_16x16x32_bf16 v[92:95], v[188:191], a[152:155], v[92:95]\n" \
"v_mfma_f32_16x16x32_bf16 v[124:127], v[184:187], a[140:143], v[124:127]\n" \
"v_mfma_f32_16x16x32_bf16 v[124:127], v[188:191], a[156:159], v[124:127]\n" \
"s_waitcnt vmcnt(25) lgkmcnt(0)\n" \
"v_mfma_f32_16x16x32_bf16 v[0:3], v[128:131], a[160:163], v[0:3]\n" \
"ds_read_b64_tr_b16 v[160:161], v225 offset:17408\n" \
"v_mfma_f32_16x16x32_bf16 v[0:3], v[132:135], a[176:179], v[0:3]\n" \
"ds_read_b64_tr_b16 v[162:163], v225 offset:17664\n" \
"v_mfma_f32_16x16x32_bf16 v[32:35], v[128:131], a[164:167], v[32:35]\n" \
"s_mov_b32 s48, 16384\n" \
"s_mov_b32 m0, s44\n" \
"s_nop 0\n" \
"buffer_load_dwordx4 %[vd0], %[srdv], s48 offen offset:2048 lds\n" \
"v_mfma_f32_16x16x32_bf16 v[32:35], v[132:135], a[180:183], v[32:35]\n" \
"ds_read_b64_tr_b16 v[164:165], v225 offset:25600\n" \
"v_mfma_f32_16x16x32_bf16 v[64:67], v[128:131], a[168:171], v[64:67]\n" \
"ds_read_b64_tr_b16 v[166:167], v225 offset:25856\n" \
"v_mfma_f32_16x16x32_bf16 v[64:67], v[132:135], a[184:187], v[64:67]\n" \
"v_mfma_f32_16x16x32_bf16 v[96:99], v[128:131], a[172:175], v[96:99]\n" \
"ds_read_b64_tr_b16 v[168:169], v225 offset:17416\n" \
"v_mfma_f32_16x16x32_bf16 v[96:99], v[132:135], a[188:191], v[96:99]\n" \
"ds_read_b64_tr_b16 v[170:171], v225 offset:17672\n" \
"v_mfma_f32_16x16x32_bf16 v[4:7], v[136:139], a[160:163], v[4:7]\n" \
"buffer_load_dwordx4 v215, %[srdv], s48 offen offset:3072 lds\n" \
"v_mfma_f32_16x16x32_bf16 v[4:7], v[140:143], a[176:179], v[4:7]\n" \
"ds_read_b64_tr_b16 v[172:173], v225 offset:25608\n" \
"v_mfma_f32_16x16x32_bf16 v[36:39], v[136:139], a[164:167], v[36:39]\n" \
"ds_read_b64_tr_b16 v[174:175], v225 offset:25864\n" \
"v_mfma_f32_16x16x32_bf16 v[36:39], v[140:143], a[180:183], v[36:39]\n" \
"v_mfma_f32_16x16x32_bf16 v[68:71], v[136:139], a[168:171], v[68:71]\n" \
"ds_read_b64_tr_b16 v[176:177], v225 offset:17920\n" \
"v_mfma_f32_16x16x32_bf16 v[68:71], v[140:143], a[184:187], v[68:71]\n" \
"ds_read_b64_tr_b16 v[178:179], v225 offset:18176\n" \
"v_mfma_f32_16x16x32_bf16 v[100:103], v[136:139], a[172:175], v[100:103]\n" \
"s_mov_b32 s48, 0\n" \
"s_mov_b32 m0, s40\n" \
"s_nop 0\n" \
"buffer_load_dwordx4 %[kd0], %[srdk], s48 offen offset:0 lds\n" \
"v_mfma_f32_16x16x32_bf16 v[100:103], v[140:143], a[188:191], v[100:103]\n" \
"ds_read_b64_tr_b16 v[180:181], v225 offset:26112\n" \
"v_mfma_f32_16x16x32_bf16 v[8:11], v[144:147], a[160:163], v[8:11]\n" \
"ds_read_b64_tr_b16 v[182:183], v225 offset:26368\n" \
"v_mfma_f32_16x16x32_bf16 v[8:11], v[148:151], a[176:179], v[8:11]\n" \
"v_mfma_f32_16x16x32_bf16 v[40:43], v[144:147], a[164:167], v[40:43]\n" \
"ds_read_b64_tr_b16 v[184:185], v225 offset:17928\n" \
"v_mfma_f32_16x16x32_bf16 v[40:43], v[148:151], a[180:183], v[40:43]\n" \
"ds_read_b64_tr_b16 v[186:187], v225 offset:18184\n" \
"v_mfma_f32_16x16x32_bf16 v[72:75], v[144:147], a[168:171], v[72:75]\n" \
"buffer_load_dwordx4 v212, %[srdk], s48 offen offset:1024 lds\n" \
"v_mfma_f32_16x16x32_bf16 v[72:75], v[148:151], a[184:187], v[72:75]\n" \
"ds_read_b64_tr_b16 v[188:189], v225 offset:26120\n" \
"v_mfma_f32_16x16x32_bf16 v[104:107], v[144:147], a[172:175], v[104:107]\n" \
"ds_read_b64_tr_b16 v[190:191], v225 offset:26376\n" \
"v_mfma_f32_16x16x32_bf16 v[104:107], v[148:151], a[188:191], v[104:107]\n" \
"v_mfma_f32_16x16x32_bf16 v[12:15], v[152:155], a[160:163], v[12:15]\n" \
"buffer_load_dwordx4 v213, %[srdk], s48 offen offset:2048 lds\n" \
"v_mfma_f32_16x16x32_bf16 v[12:15], v[156:159], a[176:179], v[12:15]\n" \
"v_mfma_f32_16x16x32_bf16 v[44:47], v[152:155], a[164:167], v[44:47]\n" \
"v_mfma_f32_16x16x32_bf16 v[44:47], v[156:159], a[180:183], v[44:47]\n" \
"v_mfma_f32_16x16x32_bf16 v[76:79], v[152:155], a[168:171], v[76:79]\n" \
"buffer_load_dwordx4 v214, %[srdk], s48 offen offset:3072 lds\n" \
"v_mfma_f32_16x16x32_bf16 v[76:79], v[156:159], a[184:187], v[76:79]\n" \
"v_mfma_f32_16x16x32_bf16 v[108:111], v[152:155], a[172:175], v[108:111]\n" \
"v_mfma_f32_16x16x32_bf16 v[108:111], v[156:159], a[188:191], v[108:111]\n" \
"s_waitcnt vmcnt(29) lgkmcnt(0)\n" \
"s_barrier\n" \
"v_mfma_f32_16x16x32_bf16 v[16:19], v[160:163], a[160:163], v[16:19]\n" \
"ds_read_b64_tr_b16 v[128:129], v225 offset:32768\n" \
"v_mfma_f32_16x16x32_bf16 v[16:19], v[164:167], a[176:179], v[16:19]\n" \
"ds_read_b64_tr_b16 v[130:131], v225 offset:33024\n" \
"v_mfma_f32_16x16x32_bf16 v[48:51], v[160:163], a[164:167], v[48:51]\n" \
"s_mov_b32 s48, 32768\n" \
"s_mov_b32 m0, s45\n" \
"s_nop 0\n" \
"buffer_load_dwordx4 %[vd0], %[srdv], s48 offen offset:0 lds\n" \
"v_mfma_f32_16x16x32_bf16 v[48:51], v[164:167], a[180:183], v[48:51]\n" \
"ds_read_b64_tr_b16 v[132:133], v225 offset:40960\n" \
"v_mfma_f32_16x16x32_bf16 v[80:83], v[160:163], a[168:171], v[80:83]\n" \
"ds_read_b64_tr_b16 v[134:135], v225 offset:41216\n" \
"v_mfma_f32_16x16x32_bf16 v[80:83], v[164:167], a[184:187], v[80:83]\n" \
"v_mfma_f32_16x16x32_bf16 v[112:115], v[160:163], a[172:175], v[112:115]\n" \
"ds_read_b64_tr_b16 v[136:137], v225 offset:32776\n" \
"v_mfma_f32_16x16x32_bf16 v[112:115], v[164:167], a[188:191], v[112:115]\n" \
"ds_read_b64_tr_b16 v[138:139], v225 offset:33032\n" \
"v_mfma_f32_16x16x32_bf16 v[20:23], v[168:171], a[160:163], v[20:23]\n" \
"buffer_load_dwordx4 v215, %[srdv], s48 offen offset:1024 lds\n" \
"v_mfma_f32_16x16x32_bf16 v[20:23], v[172:175], a[176:179], v[20:23]\n" \
"ds_read_b64_tr_b16 v[140:141], v225 offset:40968\n" \
"v_mfma_f32_16x16x32_bf16 v[52:55], v[168:171], a[164:167], v[52:55]\n" \
"ds_read_b64_tr_b16 v[142:143], v225 offset:41224\n" \
"v_mfma_f32_16x16x32_bf16 v[52:55], v[172:175], a[180:183], v[52:55]\n" \
"v_mfma_f32_16x16x32_bf16 v[84:87], v[168:171], a[168:171], v[84:87]\n" \
"ds_read_b64_tr_b16 v[144:145], v225 offset:33280\n" \
"v_mfma_f32_16x16x32_bf16 v[84:87], v[172:175], a[184:187], v[84:87]\n" \
"ds_read_b64_tr_b16 v[146:147], v225 offset:33536\n" \
"v_mfma_f32_16x16x32_bf16 v[116:119], v[168:171], a[172:175], v[116:119]\n" \
"s_mov_b32 s48, 16384\n" \
"s_mov_b32 m0, s41\n" \
"s_nop 0\n" \
"buffer_load_dwordx4 %[kd0], %[srdk], s48 offen offset:0 lds\n" \
"v_mfma_f32_16x16x32_bf16 v[116:119], v[172:175], a[188:191], v[116:119]\n" \
"ds_read_b64_tr_b16 v[148:149], v225 offset:41472\n" \
"v_mfma_f32_16x16x32_bf16 v[24:27], v[176:179], a[160:163], v[24:27]\n" \
"ds_read_b64_tr_b16 v[150:151], v225 offset:41728\n" \
"v_mfma_f32_16x16x32_bf16 v[24:27], v[180:183], a[176:179], v[24:27]\n" \
"v_mfma_f32_16x16x32_bf16 v[56:59], v[176:179], a[164:167], v[56:59]\n" \
"ds_read_b64_tr_b16 v[152:153], v225 offset:33288\n" \
"v_mfma_f32_16x16x32_bf16 v[56:59], v[180:183], a[180:183], v[56:59]\n" \
"ds_read_b64_tr_b16 v[154:155], v225 offset:33544\n" \
"v_mfma_f32_16x16x32_bf16 v[88:91], v[176:179], a[168:171], v[88:91]\n" \
"buffer_load_dwordx4 v212, %[srdk], s48 offen offset:1024 lds\n" \
"v_mfma_f32_16x16x32_bf16 v[88:91], v[180:183], a[184:187], v[88:91]\n" \
"ds_read_b64_tr_b16 v[156:157], v225 offset:41480\n" \
"v_mfma_f32_16x16x32_bf16 v[120:123], v[176:179], a[172:175], v[120:123]\n" \
"ds_read_b64_tr_b16 v[158:159], v225 offset:41736\n" \
"v_mfma_f32_16x16x32_bf16 v[120:123], v[180:183], a[188:191], v[120:123]\n" \
"v_mfma_f32_16x16x32_bf16 v[28:31], v[184:187], a[160:163], v[28:31]\n" \
"buffer_load_dwordx4 v213, %[srdk], s48 offen offset:2048 lds\n" \
"v_mfma_f32_16x16x32_bf16 v[28:31], v[188:191], a[176:179], v[28:31]\n" \
"v_mfma_f32_16x16x32_bf16 v[60:63], v[184:187], a[164:167], v[60:63]\n" \
"v_mfma_f32_16x16x32_bf16 v[60:63], v[188:191], a[180:183], v[60:63]\n" \
"v_mfma_f32_16x16x32_bf16 v[92:95], v[184:187], a[168:171], v[92:95]\n" \
"buffer_load_dwordx4 v214, %[srdk], s48 offen offset:3072 lds\n" \
"v_mfma_f32_16x16x32_bf16 v[92:95], v[188:191], a[184:187], v[92:95]\n" \
"v_mfma_f32_16x16x32_bf16 v[124:127], v[184:187], a[172:175], v[124:127]\n" \
"v_mfma_f32_16x16x32_bf16 v[124:127], v[188:191], a[188:191], v[124:127]\n" \
"s_waitcnt vmcnt(24) lgkmcnt(0)\n" \
"v_mfma_f32_16x16x32_bf16 v[0:3], v[128:131], a[192:195], v[0:3]\n" \
"ds_read_b64_tr_b16 v[160:161], v225 offset:33792\n" \
"v_mfma_f32_16x16x32_bf16 v[0:3], v[132:135], a[208:211], v[0:3]\n" \
"ds_read_b64_tr_b16 v[162:163], v225 offset:34048\n" \
"v_mfma_f32_16x16x32_bf16 v[32:35], v[128:131], a[196:199], v[32:35]\n" \
"s_mov_b32 s48, 32768\n" \
"s_mov_b32 m0, s45\n" \
"s_nop 0\n" \
"buffer_load_dwordx4 %[vd0], %[srdv], s48 offen offset:2048 lds\n" \
"v_mfma_f32_16x16x32_bf16 v[32:35], v[132:135], a[212:215], v[32:35]\n" \
"ds_read_b64_tr_b16 v[164:165], v225 offset:41984\n" \
"v_mfma_f32_16x16x32_bf16 v[64:67], v[128:131], a[200:203], v[64:67]\n" \
"ds_read_b64_tr_b16 v[166:167], v225 offset:42240\n" \
"v_mfma_f32_16x16x32_bf16 v[64:67], v[132:135], a[216:219], v[64:67]\n" \
"v_mfma_f32_16x16x32_bf16 v[96:99], v[128:131], a[204:207], v[96:99]\n" \
"ds_read_b64_tr_b16 v[168:169], v225 offset:33800\n" \
"v_mfma_f32_16x16x32_bf16 v[96:99], v[132:135], a[220:223], v[96:99]\n" \
"ds_read_b64_tr_b16 v[170:171], v225 offset:34056\n" \
"v_mfma_f32_16x16x32_bf16 v[4:7], v[136:139], a[192:195], v[4:7]\n" \
"buffer_load_dwordx4 v215, %[srdv], s48 offen offset:3072 lds\n" \
"v_mfma_f32_16x16x32_bf16 v[4:7], v[140:143], a[208:211], v[4:7]\n" \
"ds_read_b64_tr_b16 v[172:173], v225 offset:41992\n" \
"v_mfma_f32_16x16x32_bf16 v[36:39], v[136:139], a[196:199], v[36:39]\n" \
"ds_read_b64_tr_b16 v[174:175], v225 offset:42248\n" \
"v_mfma_f32_16x16x32_bf16 v[36:39], v[140:143], a[212:215], v[36:39]\n" \
"v_mfma_f32_16x16x32_bf16 v[68:71], v[136:139], a[200:203], v[68:71]\n" \
"ds_read_b64_tr_b16 v[176:177], v225 offset:34304\n" \
"v_mfma_f32_16x16x32_bf16 v[68:71], v[140:143], a[216:219], v[68:71]\n" \
"ds_read_b64_tr_b16 v[178:179], v225 offset:34560\n" \
"v_mfma_f32_16x16x32_bf16 v[100:103], v[136:139], a[204:207], v[100:103]\n" \
"s_mov_b32 s48, 32768\n" \
"s_mov_b32 m0, s42\n" \
"s_nop 0\n" \
"buffer_load_dwordx4 %[kd0], %[srdk], s48 offen offset:0 lds\n" \
"v_mfma_f32_16x16x32_bf16 v[100:103], v[140:143], a[220:223], v[100:103]\n" \
"ds_read_b64_tr_b16 v[180:181], v225 offset:42496\n" \
"v_mfma_f32_16x16x32_bf16 v[8:11], v[144:147], a[192:195], v[8:11]\n" \
"ds_read_b64_tr_b16 v[182:183], v225 offset:42752\n" \
"v_mfma_f32_16x16x32_bf16 v[8:11], v[148:151], a[208:211], v[8:11]\n" \
"v_mfma_f32_16x16x32_bf16 v[40:43], v[144:147], a[196:199], v[40:43]\n" \
"ds_read_b64_tr_b16 v[184:185], v225 offset:34312\n" \
"v_mfma_f32_16x16x32_bf16 v[40:43], v[148:151], a[212:215], v[40:43]\n" \
"ds_read_b64_tr_b16 v[186:187], v225 offset:34568\n" \
"v_mfma_f32_16x16x32_bf16 v[72:75], v[144:147], a[200:203], v[72:75]\n" \
"buffer_load_dwordx4 v212, %[srdk], s48 offen offset:1024 lds\n" \
"v_mfma_f32_16x16x32_bf16 v[72:75], v[148:151], a[216:219], v[72:75]\n" \
"ds_read_b64_tr_b16 v[188:189], v225 offset:42504\n" \
"v_mfma_f32_16x16x32_bf16 v[104:107], v[144:147], a[204:207], v[104:107]\n" \
"ds_read_b64_tr_b16 v[190:191], v225 offset:42760\n" \
"v_mfma_f32_16x16x32_bf16 v[104:107], v[148:151], a[220:223], v[104:107]\n" \
"v_mfma_f32_16x16x32_bf16 v[12:15], v[152:155], a[192:195], v[12:15]\n" \
"buffer_load_dwordx4 v213, %[srdk], s48 offen offset:2048 lds\n" \
"v_mfma_f32_16x16x32_bf16 v[12:15], v[156:159], a[208:211], v[12:15]\n" \
"v_mfma_f32_16x16x32_bf16 v[44:47], v[152:155], a[196:199], v[44:47]\n" \
"v_mfma_f32_16x16x32_bf16 v[44:47], v[156:159], a[212:215], v[44:47]\n" \
"v_mfma_f32_16x16x32_bf16 v[76:79], v[152:155], a[200:203], v[76:79]\n" \
"buffer_load_dwordx4 v214, %[srdk], s48 offen offset:3072 lds\n" \
"v_mfma_f32_16x16x32_bf16 v[76:79], v[156:159], a[216:219], v[76:79]\n" \
"v_mfma_f32_16x16x32_bf16 v[108:111], v[152:155], a[204:207], v[108:111]\n" \
"v_mfma_f32_16x16x32_bf16 v[108:111], v[156:159], a[220:223], v[108:111]\n" \
"s_waitcnt vmcnt(28) lgkmcnt(0)\n" \
"s_barrier\n" \
"v_mfma_f32_16x16x32_bf16 v[16:19], v[160:163], a[192:195], v[16:19]\n" \
"ds_read_b64_tr_b16 v[128:129], v225 offset:49152\n" \
"s_mov_b32 s54, 0x4000\n" \
"v_mfma_f32_16x16x32_bf16 v[16:19], v[164:167], a[208:211], v[16:19]\n" \
"ds_read_b64_tr_b16 v[130:131], v225 offset:49408\n" \
"v_mfma_f32_16x16x32_bf16 v[48:51], v[160:163], a[196:199], v[48:51]\n" \
"s_mov_b32 s48, 49152\n" \
"s_mov_b32 m0, s46\n" \
"s_nop 0\n" \
"buffer_load_dwordx4 %[vd0], %[srdv], s48 offen offset:0 lds\n" \
"v_mbcnt_lo_u32_b32 v231, -1, 0\n" \
"v_mfma_f32_16x16x32_bf16 v[48:51], v[164:167], a[212:215], v[48:51]\n" \
"ds_read_b64_tr_b16 v[132:133], v225 offset:57344\n" \
"v_mfma_f32_16x16x32_bf16 v[80:83], v[160:163], a[200:203], v[80:83]\n" \
"ds_read_b64_tr_b16 v[134:135], v225 offset:57600\n" \
"v_mfma_f32_16x16x32_bf16 v[80:83], v[164:167], a[216:219], v[80:83]\n" \
"v_mbcnt_hi_u32_b32 v231, -1, v231\n" \
"v_mfma_f32_16x16x32_bf16 v[112:115], v[160:163], a[204:207], v[112:115]\n" \
"ds_read_b64_tr_b16 v[136:137], v225 offset:49160\n" \
"v_mfma_f32_16x16x32_bf16 v[112:115], v[164:167], a[220:223], v[112:115]\n" \
"ds_read_b64_tr_b16 v[138:139], v225 offset:49416\n" \
"v_mfma_f32_16x16x32_bf16 v[20:23], v[168:171], a[192:195], v[20:23]\n" \
"buffer_load_dwordx4 v215, %[srdv], s48 offen offset:1024 lds\n" \
"v_lshrrev_b32_e32 v232, 4, v231\n" \
"v_mfma_f32_16x16x32_bf16 v[20:23], v[172:175], a[208:211], v[20:23]\n" \
"ds_read_b64_tr_b16 v[140:141], v225 offset:57352\n" \
"v_mfma_f32_16x16x32_bf16 v[52:55], v[168:171], a[196:199], v[52:55]\n" \
"ds_read_b64_tr_b16 v[142:143], v225 offset:57608\n" \
"v_xor_b32_e64 v232, v232, v231\n" \
"v_mfma_f32_16x16x32_bf16 v[52:55], v[172:175], a[212:215], v[52:55]\n" \
"v_mfma_f32_16x16x32_bf16 v[84:87], v[168:171], a[200:203], v[84:87]\n" \
"ds_read_b64_tr_b16 v[144:145], v225 offset:49664\n" \
"v_mfma_f32_16x16x32_bf16 v[84:87], v[172:175], a[216:219], v[84:87]\n" \
"ds_read_b64_tr_b16 v[146:147], v225 offset:49920\n" \
"v_bfe_i32 v232, v232, 1, 1\n" \
"v_mfma_f32_16x16x32_bf16 v[116:119], v[168:171], a[204:207], v[116:119]\n" \
"s_mov_b32 s48, 49152\n" \
"s_mov_b32 m0, s43\n" \
"s_nop 0\n" \
"buffer_load_dwordx4 %[kd0], %[srdk], s48 offen offset:0 lds\n" \
"v_mfma_f32_16x16x32_bf16 v[116:119], v[172:175], a[220:223], v[116:119]\n" \
"ds_read_b64_tr_b16 v[148:149], v225 offset:57856\n" \
"v_mfma_f32_16x16x32_bf16 v[24:27], v[176:179], a[192:195], v[24:27]\n" \
"ds_read_b64_tr_b16 v[150:151], v225 offset:58112\n" \
"v_not_b32_e64 v232, v232\n" \
"v_mfma_f32_16x16x32_bf16 v[24:27], v[180:183], a[208:211], v[24:27]\n" \
"v_mfma_f32_16x16x32_bf16 v[56:59], v[176:179], a[196:199], v[56:59]\n" \
"ds_read_b64_tr_b16 v[152:153], v225 offset:49672\n" \
"v_mfma_f32_16x16x32_bf16 v[56:59], v[180:183], a[212:215], v[56:59]\n" \
"ds_read_b64_tr_b16 v[154:155], v225 offset:49928\n" \
"v_and_b32_e32 v230, 0x3f803f80, v232\n" \
"v_mfma_f32_16x16x32_bf16 v[88:91], v[176:179], a[200:203], v[88:91]\n" \
"buffer_load_dwordx4 v212, %[srdk], s48 offen offset:1024 lds\n" \
"v_mfma_f32_16x16x32_bf16 v[88:91], v[180:183], a[216:219], v[88:91]\n" \
"ds_read_b64_tr_b16 v[156:157], v225 offset:57864\n" \
"v_readlane_b32 s62, %[tlo], 18\n" \
"v_mfma_f32_16x16x32_bf16 v[120:123], v[176:179], a[204:207], v[120:123]\n" \
"ds_read_b64_tr_b16 v[158:159], v225 offset:58120\n" \
"v_mfma_f32_16x16x32_bf16 v[120:123], v[180:183], a[220:223], v[120:123]\n" \
"v_mfma_f32_16x16x32_bf16 v[28:31], v[184:187], a[192:195], v[28:31]\n" \
"buffer_load_dwordx4 v213, %[srdk], s48 offen offset:2048 lds\n" \
"v_readlane_b32 s61, %[tlo], 21\n" \
"v_mfma_f32_16x16x32_bf16 v[28:31], v[188:191], a[208:211], v[28:31]\n" \
"v_mfma_f32_16x16x32_bf16 v[60:63], v[184:187], a[196:199], v[60:63]\n" \
"v_mfma_f32_16x16x32_bf16 v[60:63], v[188:191], a[212:215], v[60:63]\n" \
"v_lshlrev_b32_e64 v224, 7, v231\n" \
"v_mfma_f32_16x16x32_bf16 v[92:95], v[184:187], a[200:203], v[92:95]\n" \
"buffer_load_dwordx4 v214, %[srdk], s48 offen offset:3072 lds\n" \
"v_mfma_f32_16x16x32_bf16 v[92:95], v[188:191], a[216:219], v[92:95]\n" \
"v_mfma_f32_16x16x32_bf16 v[124:127], v[184:187], a[204:207], v[124:127]\n" \
"v_mfma_f32_16x16x32_bf16 v[124:127], v[188:191], a[220:223], v[124:127]\n" \
"s_waitcnt vmcnt(24) lgkmcnt(0)\n" \
"v_mfma_f32_16x16x32_bf16 v[0:3], v[128:131], a[224:227], v[0:3]\n" \
"ds_read_b64_tr_b16 v[160:161], v225 offset:50176\n" \
"v_accvgpr_write_b32 a192, v230\n" \
"v_mfma_f32_16x16x32_bf16 v[0:3], v[132:135], a[240:243], v[0:3]\n" \
"ds_read_b64_tr_b16 v[162:163], v225 offset:50432\n" \
"v_mfma_f32_16x16x32_bf16 v[32:35], v[128:131], a[228:231], v[32:35]\n" \
"s_mov_b32 s48, 49152\n" \
"s_mov_b32 m0, s46\n" \
"s_nop 0\n" \
"buffer_load_dwordx4 %[vd0], %[srdv], s48 offen offset:2048 lds\n" \
"v_accvgpr_write_b32 a193, v230\n" \
"v_mfma_f32_16x16x32_bf16 v[32:35], v[132:135], a[244:247], v[32:35]\n" \
"ds_read_b64_tr_b16 v[164:165], v225 offset:58368\n" \
"v_mfma_f32_16x16x32_bf16 v[64:67], v[128:131], a[232:235], v[64:67]\n" \
"ds_read_b64_tr_b16 v[166:167], v225 offset:58624\n" \
"v_accvgpr_write_b32 a194, v230\n" \
"v_mfma_f32_16x16x32_bf16 v[64:67], v[132:135], a[248:251], v[64:67]\n" \
"v_mfma_f32_16x16x32_bf16 v[96:99], v[128:131], a[236:239], v[96:99]\n" \
"ds_read_b64_tr_b16 v[168:169], v225 offset:50184\n" \
"v_accvgpr_write_b32 a195, v230\n" \
"v_mfma_f32_16x16x32_bf16 v[96:99], v[132:135], a[252:255], v[96:99]\n" \
"ds_read_b64_tr_b16 v[170:171], v225 offset:50440\n" \
"v_mfma_f32_16x16x32_bf16 v[4:7], v[136:139], a[224:227], v[4:7]\n" \
"buffer_load_dwordx4 v215, %[srdv], s48 offen offset:3072 lds\n" \
"v_accvgpr_write_b32 a196, -1\n" \
"v_mfma_f32_16x16x32_bf16 v[4:7], v[140:143], a[240:243], v[4:7]\n" \
"ds_read_b64_tr_b16 v[172:173], v225 offset:58376\n" \
"v_mfma_f32_16x16x32_bf16 v[36:39], v[136:139], a[228:231], v[36:39]\n" \
"ds_read_b64_tr_b16 v[174:175], v225 offset:58632\n" \
"v_accvgpr_write_b32 a197, -1\n" \
"v_mfma_f32_16x16x32_bf16 v[36:39], v[140:143], a[244:247], v[36:39]\n" \
"v_mfma_f32_16x16x32_bf16 v[68:71], v[136:139], a[232:235], v[68:71]\n" \
"ds_read_b64_tr_b16 v[176:177], v225 offset:50688\n" \
"v_accvgpr_write_b32 a198, -1\n" \
"v_mfma_f32_16x16x32_bf16 v[68:71], v[140:143], a[248:251], v[68:71]\n" \
"ds_read_b64_tr_b16 v[178:179], v225 offset:50944\n" \
"v_mfma_f32_16x16x32_bf16 v[100:103], v[136:139], a[236:239], v[100:103]\n" \
"v_accvgpr_write_b32 a199, -1\n" \
"v_mfma_f32_16x16x32_bf16 v[100:103], v[140:143], a[252:255], v[100:103]\n" \
"ds_read_b64_tr_b16 v[180:181], v225 offset:58880\n" \
"v_mfma_f32_16x16x32_bf16 v[8:11], v[144:147], a[224:227], v[8:11]\n" \
"ds_read_b64_tr_b16 v[182:183], v225 offset:59136\n" \
"v_lshrrev_b32_e32 v231, 3, v224\n" \
"v_mfma_f32_16x16x32_bf16 v[8:11], v[148:151], a[240:243], v[8:11]\n" \
"v_mfma_f32_16x16x32_bf16 v[40:43], v[144:147], a[228:231], v[40:43]\n" \
"ds_read_b64_tr_b16 v[184:185], v225 offset:50696\n" \
"v_mov_b32_e64 v232, -1\n" \
"v_mfma_f32_16x16x32_bf16 v[40:43], v[148:151], a[244:247], v[40:43]\n" \
"ds_read_b64_tr_b16 v[186:187], v225 offset:50952\n" \
"v_mfma_f32_16x16x32_bf16 v[72:75], v[144:147], a[232:235], v[72:75]\n" \
"v_mov_b32_e64 v233, -1\n" \
"v_mfma_f32_16x16x32_bf16 v[72:75], v[148:151], a[248:251], v[72:75]\n" \
"ds_read_b64_tr_b16 v[188:189], v225 offset:58888\n" \
"v_mfma_f32_16x16x32_bf16 v[104:107], v[144:147], a[236:239], v[104:107]\n" \
"ds_read_b64_tr_b16 v[190:191], v225 offset:59144\n" \
"v_mov_b32_e64 v234, -1\n" \
"v_mfma_f32_16x16x32_bf16 v[104:107], v[148:151], a[252:255], v[104:107]\n" \
"v_mfma_f32_16x16x32_bf16 v[12:15], v[152:155], a[224:227], v[12:15]\n" \
"v_mov_b32_e64 v235, -1\n" \
"v_mfma_f32_16x16x32_bf16 v[12:15], v[156:159], a[240:243], v[12:15]\n" \
"v_mfma_f32_16x16x32_bf16 v[44:47], v[152:155], a[228:231], v[44:47]\n" \
"buffer_store_dwordx4 v[232:235], v231, %[srdz], s61 offen offset:0 sc1\n" \
"v_mfma_f32_16x16x32_bf16 v[44:47], v[156:159], a[244:247], v[44:47]\n" \
"v_mfma_f32_16x16x32_bf16 v[76:79], v[152:155], a[232:235], v[76:79]\n" \
"buffer_store_dwordx4 v[232:235], v231, %[srdz], s61 offen offset:1024 sc1\n" \
"v_mfma_f32_16x16x32_bf16 v[76:79], v[156:159], a[248:251], v[76:79]\n" \
"v_mfma_f32_16x16x32_bf16 v[108:111], v[152:155], a[236:239], v[108:111]\n" \
"v_mfma_f32_16x16x32_bf16 v[108:111], v[156:159], a[252:255], v[108:111]\n" \
"s_waitcnt vmcnt(22) lgkmcnt(0)\n" \
"s_barrier\n" \
"v_mfma_f32_16x16x32_bf16 v[16:19], v[160:163], a[224:227], v[16:19]\n" \
"ds_read_b128 a[64:67], %[kb0] offset:0\n" \
"v_mfma_f32_16x16x32_bf16 v[16:19], v[164:167], a[240:243], v[16:19]\n" \
"v_mfma_f32_16x16x32_bf16 v[20:23], v[168:171], a[224:227], v[20:23]\n" \
"v_cvt_pk_bf16_f32 v230, v0, v1\n" \
"v_cvt_pk_bf16_f32 v231, v2, v3\n" \
"v_cvt_pk_bf16_f32 v232, v4, v5\n" \
"ds_read_b128 a[68:71], v208 offset:0\n" \
"v_mfma_f32_16x16x32_bf16 v[20:23], v[172:175], a[240:243], v[20:23]\n" \
"v_cvt_pk_bf16_f32 v233, v6, v7\n" \
"v_accvgpr_write_b32 a0, v230\n" \
"v_accvgpr_write_b32 a1, v231\n" \
"v_mfma_f32_16x16x32_bf16 v[24:27], v[176:179], a[224:227], v[24:27]\n" \
"v_accvgpr_write_b32 a2, v232\n" \
"v_accvgpr_write_b32 a3, v233\n" \
"v_cvt_pk_bf16_f32 v234, v8, v9\n" \
"ds_read_b128 a[72:75], v209 offset:0\n" \
"v_mfma_f32_16x16x32_bf16 v[24:27], v[180:183], a[240:243], v[24:27]\n" \
"v_cvt_pk_bf16_f32 v235, v10, v11\n" \
"v_cvt_pk_bf16_f32 v236, v12, v13\n" \
"v_cvt_pk_bf16_f32 v237, v14, v15\n" \
"v_mfma_f32_16x16x32_bf16 v[28:31], v[184:187], a[224:227], v[28:31]\n" \
"v_accvgpr_write_b32 a4, v234\n" \
"v_accvgpr_write_b32 a5, v235\n" \
"v_accvgpr_write_b32 a6, v236\n" \
".if . - adaln_body != 8192\n" \
".error \"size model\"\n" \
".endif\n" \
"Lk1_%=:\n" \
"s_cbranch_execz Lk2_%=\n" \
"s_nop 0\n" \
"ds_read_b128 a[76:79], v210 offset:0\n" \
"v_mfma_f32_16x16x32_bf16 v[28:31], v[188:191], a[240:243], v[28:31]\n" \
"v_accvgpr_write_b32 a7, v237\n" \
"v_cvt_pk_bf16_f32 v230, v32, v33\n" \
"v_cvt_pk_bf16_f32 v231, v34, v35\n" \
"v_mfma_f32_16x16x32_bf16 v[48:51], v[160:163], a[228:231], v[48:51]\n" \
"v_cvt_pk_bf16_f32 v232, v36, v37\n" \
"v_cvt_pk_bf16_f32 v233, v38, v39\n" \
"v_accvgpr_write_b32 a16, v230\n" \
"ds_read_b128 a[92:95], v210 offset:4096\n" \
"v_mfma_f32_16x16x32_bf16 v[48:51], v[164:167], a[244:247], v[48:51]\n" \
"v_accvgpr_write_b32 a17, v231\n" \
"v_accvgpr_write_b32 a18, v232\n" \
"v_accvgpr_write_b32 a19, v233\n" \
"v_mfma_f32_16x16x32_bf16 v[52:55], v[168:171], a[228:231], v[52:55]\n" \
"v_cvt_pk_bf16_f32 v234, v40, v41\n" \
"v_cvt_pk_bf16_f32 v235, v42, v43\n" \
"v_cvt_pk_bf16_f32 v236, v44, v45\n" \
"ds_read_b128 a[88:91], v209 offset:4096\n" \
"v_mfma_f32_16x16x32_bf16 v[52:55], v[172:175], a[244:247], v[52:55]\n" \
"v_cvt_pk_bf16_f32 v237, v46, v47\n" \
"v_accvgpr_write_b32 a20, v234\n" \
"v_accvgpr_write_b32 a21, v235\n" \
"v_mfma_f32_16x16x32_bf16 v[56:59], v[176:179], a[228:231], v[56:59]\n" \
"v_accvgpr_write_b32 a22, v236\n" \
"v_accvgpr_write_b32 a23, v237\n" \
"v_cvt_pk_bf16_f32 v230, v16, v17\n" \
"ds_read_b128 a[84:87], v208 offset:4096\n" \
"v_mfma_f32_16x16x32_bf16 v[56:59], v[180:183], a[244:247], v[56:59]\n" \
"v_cvt_pk_bf16_f32 v231, v18, v19\n" \
"v_cvt_pk_bf16_f32 v232, v20, v21\n" \
"v_cvt_pk_bf16_f32 v233, v22, v23\n" \
"v_mfma_f32_16x16x32_bf16 v[60:63], v[184:187], a[228:231], v[60:63]\n" \
"v_accvgpr_write_b32 a8, v230\n" \
"v_accvgpr_write_b32 a9, v231\n" \
"v_accvgpr_write_b32 a10, v232\n" \
"ds_read_b128 a[80:83], %[kb0] offset:4096\n" \
"v_mfma_f32_16x16x32_bf16 v[60:63], v[188:191], a[244:247], v[60:63]\n" \
"v_accvgpr_write_b32 a11, v233\n" \
"v_cvt_pk_bf16_f32 v234, v24, v25\n" \
"v_cvt_pk_bf16_f32 v235, v26, v27\n" \
"v_mfma_f32_16x16x32_bf16 v[80:83], v[160:163], a[232:235], v[80:83]\n" \
"v_cvt_pk_bf16_f32 v236, v28, v29\n" \
"v_cvt_pk_bf16_f32 v237, v30, v31\n" \
"v_accvgpr_write_b32 a12, v234\n" \
"v_mfma_f32_16x16x32_bf16 v[80:83], v[164:167], a[248:251], v[80:83]\n" \
"v_accvgpr_write_b32 a13, v235\n" \
"v_accvgpr_write_b32 a14, v236\n" \
"v_accvgpr_write_b32 a15, v237\n" \
"v_mfma_f32_16x16x32_bf16 v[84:87], v[168:171], a[232:235], v[84:87]\n" \
"v_cvt_pk_bf16_f32 v230, v64, v65\n" \
"v_cvt_pk_bf16_f32 v231, v66, v67\n" \
"v_cvt_pk_bf16_f32 v232, v68, v69\n" \
"v_mfma_f32_16x16x32_bf16 v[84:87], v[172:175], a[248:251], v[84:87]\n" \
"v_cvt_pk_bf16_f32 v233, v70, v71\n" \
"v_accvgpr_write_b32 a32, v230\n" \
"v_accvgpr_write_b32 a33, v231\n" \
"v_mfma_f32_16x16x32_bf16 v[88:91], v[176:179], a[232:235], v[88:91]\n" \
"v_accvgpr_write_b32 a34, v232\n" \
"v_accvgpr_write_b32 a35, v233\n" \
"v_cvt_pk_bf16_f32 v234, v72, v73\n" \
"v_mfma_f32_16x16x32_bf16 v[88:91], v[180:183], a[248:251], v[88:91]\n" \
"v_cvt_pk_bf16_f32 v235, v74, v75\n" \
"v_cvt_pk_bf16_f32 v236, v76, v77\n" \
"v_cvt_pk_bf16_f32 v237, v78, v79\n" \
"v_mfma_f32_16x16x32_bf16 v[92:95], v[184:187], a[232:235], v[92:95]\n" \
"v_accvgpr_write_b32 a36, v234\n" \
"v_accvgpr_write_b32 a37, v235\n" \
"v_accvgpr_write_b32 a38, v236\n" \
"v_mfma_f32_16x16x32_bf16 v[92:95], v[188:191], a[248:251], v[92:95]\n" \
"v_accvgpr_write_b32 a39, v237\n" \
"v_cvt_pk_bf16_f32 v230, v48, v49\n" \
"v_cvt_pk_bf16_f32 v231, v50, v51\n" \
"v_mfma_f32_16x16x32_bf16 v[112:115], v[160:163], a[236:239], v[112:115]\n" \
"v_cvt_pk_bf16_f32 v232, v52, v53\n" \
"v_cvt_pk_bf16_f32 v233, v54, v55\n" \
"v_accvgpr_write_b32 a24, v230\n" \
"v_mfma_f32_16x16x32_bf16 v[112:115], v[164:167], a[252:255], v[112:115]\n" \
"v_accvgpr_write_b32 a25, v231\n" \
"v_accvgpr_write_b32 a26, v232\n" \
"v_accvgpr_write_b32 a27, v233\n" \
"v_mfma_f32_16x16x32_bf16 v[116:119], v[168:171], a[236:239], v[116:119]\n" \
"v_cvt_pk_bf16_f32 v234, v56, v57\n" \
"v_cvt_pk_bf16_f32 v235, v58, v59\n" \
"v_cvt_pk_bf16_f32 v236, v60, v61\n" \
"v_mfma_f32_16x16x32_bf16 v[116:119], v[172:175], a[252:255], v[116:119]\n" \
"v_cvt_pk_bf16_f32 v237, v62, v63\n" \
"v_accvgpr_write_b32 a28, v234\n" \
"v_accvgpr_write_b32 a29, v235\n" \
"v_mfma_f32_16x16x32_bf16 v[120:123], v[176:179], a[236:239], v[120:123]\n" \
"v_accvgpr_write_b32 a30, v236\n" \
"v_accvgpr_write_b32 a31, v237\n" \
"v_cvt_pk_bf16_f32 v230, v96, v97\n" \
"v_mfma_f32_16x16x32_bf16 v[120:123], v[180:183], a[252:255], v[120:123]\n" \
"v_cvt_pk_bf16_f32 v231, v98, v99\n" \
"v_cvt_pk_bf16_f32 v232, v100, v101\n" \
"v_cvt_pk_bf16_f32 v233, v102, v103\n" \
"v_mfma_f32_16x16x32_bf16 v[124:127], v[184:187], a[236:239], v[124:127]\n" \
"v_accvgpr_write_b32 a48, v230\n" \
"v_accvgpr_write_b32 a49, v231\n" \
"v_accvgpr_write_b32 a50, v232\n" \
"v_mfma_f32_16x16x32_bf16 v[124:127], v[188:191], a[252:255], v[124:127]\n" \
"v_accvgpr_write_b32 a51, v233\n" \
"s_waitcnt vmcnt(63) lgkmcnt(0)\n" \
"s_nop 0\n" \
"s_mov_b32 s48, 65536\n" \
"s_mov_b32 s60, 0\n" \
"s_waitcnt lgkmcnt(0)\n" \
"v_mfma_f32_16x16x32_bf16 v[128:131], a[64:67], a[0:3], 0\n" \
"ds_read_b128 a[96:99], %[kb0] offset:8192\n" \
"v_cvt_pk_bf16_f32 v234, v80, v81\n" \
"v_cvt_pk_bf16_f32 v235, v82, v83\n" \
"v_mfma_f32_16x16x32_bf16 v[128:131], a[68:71], a[4:7], v[128:131]\n" \
"ds_read_b128 a[100:103], v208 offset:8192\n" \
"v_cvt_pk_bf16_f32 v236, v84, v85\n" \
"v_cvt_pk_bf16_f32 v237, v86, v87\n" \
"v_mfma_f32_16x16x32_bf16 v[128:131], a[72:75], a[8:11], v[128:131]\n" \
"ds_read_b128 a[104:107], v209 offset:8192\n" \
"v_accvgpr_write_b32 a40, v234\n" \
"v_accvgpr_write_b32 a41, v235\n" \
"v_mfma_f32_16x16x32_bf16 v[128:131], a[76:79], a[12:15], v[128:131]\n" \
"ds_read_b128 a[108:111], v210 offset:8192\n" \
"v_accvgpr_write_b32 a42, v236\n" \
"v_accvgpr_write_b32 a43, v237\n" \
"v_mfma_f32_16x16x32_bf16 v[132:135], a[92:95], a[12:15], 0\n" \
"ds_read_b128 a[124:127], v210 offset:12288\n" \
"v_cvt_pk_bf16_f32 v230, v88, v89\n" \
"v_cvt_pk_bf16_f32 v231, v90, v91\n" \
"v_mfma_f32_16x16x32_bf16 v[132:135], a[88:91], a[8:11], v[132:135]\n" \
"ds_read_b128 a[120:123], v209 offset:12288\n" \
"v_cvt_pk_bf16_f32 v232, v92, v93\n" \
"v_cvt_pk_bf16_f32 v233, v94, v95\n" \
"v_mfma_f32_16x16x32_bf16 v[132:135], a[84:87], a[4:7], v[132:135]\n" \
"ds_read_b128 a[116:119], v208 offset:12288\n" \
"v_accvgpr_write_b32 a44, v230\n" \
"v_accvgpr_write_b32 a45, v231\n" \
"v_mfma_f32_16x16x32_bf16 v[132:135], a[80:83], a[0:3], v[132:135]\n" \
"ds_read_b128 a[112:115], %[kb0] offset:12288\n" \
"v_accvgpr_write_b32 a46, v232\n" \
"v_accvgpr_write_b32 a47, v233\n" \
"v_mfma_f32_16x16x32_bf16 v[136:139], a[64:67], a[16:19], 0\n" \
"ds_read_b64_tr_b16 a[128:129], %[vvb] offset:0\n" \
"v_cvt_pk_bf16_f32 v234, v104, v105\n" \
"v_cvt_pk_bf16_f32 v235, v106, v107\n" \
"v_mfma_f32_16x16x32_bf16 v[136:139], a[68:71], a[20:23], v[136:139]\n" \
"ds_read_b64_tr_b16 a[130:131], %[vvb] offset:4096\n" \
"v_cvt_pk_bf16_f32 v236, v108, v109\n" \
"v_cvt_pk_bf16_f32 v237, v110, v111\n" \
"v_mfma_f32_16x16x32_bf16 v[136:139], a[72:75], a[24:27], v[136:139]\n" \
"ds_read_b64_tr_b16 a[132:133], v211 offset:0\n" \
"v_accvgpr_write_b32 a52, v234\n" \
"v_accvgpr_write_b32 a53, v235\n" \
"v_mfma_f32_16x16x32_bf16 v[136:139], a[76:79], a[28:31], v[136:139]\n" \
"ds_read_b64_tr_b16 a[134:135], v211 offset:4096\n" \
"v_accvgpr_write_b32 a54, v236\n" \
"v_accvgpr_write_b32 a55, v237\n" \
"v_mfma_f32_16x16x32_bf16 v[140:143], a[92:95], a[28:31], 0\n" \
"ds_read_b64_tr_b16 a[136:137], %[vvb] offset:512\n" \
"v_cvt_pk_bf16_f32 v230, v112, v113\n" \
"v_cvt_pk_bf16_f32 v231, v114, v115\n" \
"v_mfma_f32_16x16x32_bf16 v[140:143], a[88:91], a[24:27], v[140:143]\n" \
"ds_read_b64_tr_b16 a[138:139], %[vvb] offset:4608\n" \
"v_cvt_pk_bf16_f32 v232, v116, v117\n" \
"v_cvt_pk_bf16_f32 v233, v118, v119\n" \
"v_mfma_f32_16x16x32_bf16 v[140:143], a[84:87], a[20:23], v[140:143]\n" \
"ds_read_b64_tr_b16 a[140:141], v211 offset:512\n" \
"v_accvgpr_write_b32 a56, v230\n" \
"v_accvgpr_write_b32 a57, v231\n" \
"v_mfma_f32_16x16x32_bf16 v[140:143], a[80:83], a[16:19], v[140:143]\n" \
"ds_read_b64_tr_b16 a[142:143], v211 offset:4608\n" \
"v_accvgpr_write_b32 a58, v232\n" \
"v_accvgpr_write_b32 a59, v233\n" \
"v_mfma_f32_16x16x32_bf16 v[144:147], a[64:67], a[32:35], 0\n" \
"ds_read_b64_tr_b16 a[144:145], %[vvb] offset:1024\n" \
"v_cvt_pk_bf16_f32 v234, v120, v121\n" \
"v_cvt_pk_bf16_f32 v235, v122, v123\n" \
"v_mfma_f32_16x16x32_bf16 v[144:147], a[68:71], a[36:39], v[144:147]\n" \
"ds_read_b64_tr_b16 a[146:147], %[vvb] offset:5120\n" \
"v_cvt_pk_bf16_f32 v236, v124, v125\n" \
"v_cvt_pk_bf16_f32 v237, v126, v127\n" \
"v_mfma_f32_16x16x32_bf16 v[144:147], a[72:75], a[40:43], v[144:147]\n" \
"ds_read_b64_tr_b16 a[148:149], v211 offset:1024\n" \
"v_accvgpr_write_b32 a60, v234\n" \
"v_accvgpr_write_b32 a61, v235\n" \
"v_mfma_f32_16x16x32_bf16 v[144:147], a[76:79], a[44:47], v[144:147]\n" \
"ds_read_b64_tr_b16 a[150:151], v211 offset:5120\n" \
"v_accvgpr_write_b32 a62, v236\n" \
"v_accvgpr_write_b32 a63, v237\n" \
"v_mfma_f32_16x16x32_bf16 v[148:151], a[92:95], a[44:47], 0\n" \
"ds_read_b64_tr_b16 a[152:153], %[vvb] offset:1536\n" \
"v_max3_f32 v192, v128, v129, v130\n" \
"v_max3_f32 v193, v131, v132, v133\n" \
"v_mfma_f32_16x16x32_bf16 v[148:151], a[88:91], a[40:43], v[148:151]\n" \
"ds_read_b64_tr_b16 a[154:155], %[vvb] offset:5632\n" \
"v_max3_f32 v192, v192, v134, v135\n" \
"v_max_f32_e64 v225, v192, v193\n" \
"v_mfma_f32_16x16x32_bf16 v[148:151], a[84:87], a[36:39], v[148:151]\n" \
"ds_read_b64_tr_b16 a[156:157], v211 offset:1536\n" \
"v_max3_f32 v192, v136, v137, v138\n" \
"v_max3_f32 v193, v139, v140, v141\n" \
"v_mfma_f32_16x16x32_bf16 v[148:151], a[80:83], a[32:35], v[148:151]\n" \
"ds_read_b64_tr_b16 a[158:159], v211 offset:5632\n" \
"v_max3_f32 v192, v192, v142, v143\n" \
"v_max_f32_e64 v226, v192, v193\n" \
"v_mfma_f32_16x16x32_bf16 v[152:155], a[64:67], a[48:51], 0\n" \
"ds_read_b128 a[200:203], v228 offset:0\n" \
"s_mov_b32 s44, %[m0t]\n" \
"s_mov_b32 s45, 0\n" \
"v_mfma_f32_16x16x32_bf16 v[152:155], a[68:71], a[52:55], v[152:155]\n" \
"ds_read_b128 a[204:207], v228 offset:64\n" \
"v_mfma_f32_16x16x32_bf16 v[152:155], a[72:75], a[56:59], v[152:155]\n" \
"ds_read_b128 a[208:211], v228 offset:128\n" \
"v_mfma_f32_16x16x32_bf16 v[152:155], a[76:79], a[60:63], v[152:155]\n" \
"ds_read_b128 a[212:215], v228 offset:192\n" \
"v_mfma_f32_16x16x32_bf16 v[156:159], a[92:95], a[60:63], 0\n" \
"ds_read_b128 a[216:219], v228 offset:256\n" \
"v_mfma_f32_16x16x32_bf16 v[156:159], a[88:91], a[56:59], v[156:159]\n" \
"ds_read_b128 a[220:223], v228 offset:320\n" \
"v_mfma_f32_16x16x32_bf16 v[156:159], a[84:87], a[52:55], v[156:159]\n" \
"ds_read_b128 a[224:227], v228 offset:384\n" \
"v_max3_f32 v192, v144, v145, v146\n" \
"v_max3_f32 v193, v147, v148, v149\n" \
"v_mfma_f32_16x16x32_bf16 v[156:159], a[80:83], a[48:51], v[156:159]\n" \
"ds_read_b128 a[228:231], v228 offset:448\n" \
"v_max3_f32 v192, v192, v150, v151\n" \
"v_max_f32_e64 v227, v192, v193\n" \
"s_nop 3\n" \
"s_nop 0\n" \
"v_max3_f32 v230, v152, v153, v154\n" \
"v_max3_f32 v231, v155, v156, v157\n" \
"v_max3_f32 v230, v230, v158, v159\n" \
"v_max_f32_e32 v228, v230, v231\n" \
"v_mov_b32_e32 v232, v225\n" \
"v_mov_b32_e32 v233, v226\n" \
"v_mov_b32_e32 v234, v227\n" \
"v_mov_b32_e32 v235, v228\n" \
"s_nop 0\n" \
"v_permlane16_swap_b32_e32 v225, v232\n" \
"v_permlane16_swap_b32_e32 v226, v233\n" \
"v_permlane16_swap_b32_e32 v227, v234\n" \
"v_permlane16_swap_b32_e32 v228, v235\n" \
"v_max_f32_e32 v225, v225, v232\n" \
"v_max_f32_e32 v226, v226, v233\n" \
"v_max_f32_e32 v227, v227, v234\n" \
"v_max_f32_e32 v228, v228, v235\n" \
"v_mov_b32_e32 v232, v225\n" \
"v_mov_b32_e32 v233, v226\n" \
"v_mov_b32_e32 v234, v227\n" \
"v_mov_b32_e32 v235, v228\n" \
"s_nop 0\n" \
"v_permlane32_swap_b32_e32 v225, v232\n" \
"v_permlane32_swap_b32_e32 v226, v233\n" \
"v_permlane32_swap_b32_e32 v227, v234\n" \
"v_permlane32_swap_b32_e32 v228, v235\n" \
"v_max_f32_e32 v225, v225, v232\n" \
"v_max_f32_e32 v226, v226, v233\n" \
"v_max_f32_e32 v227, v227, v234\n" \
"v_max_f32_e64 v228, v228, v235\n" \
"Lhavem_%=:\n" \
"s_nop 3\n" \
"v_mov_b64_e32 v[176:177], 0\n" \
"v_mov_b64_e32 v[178:179], 0\n" \
"v_mov_b64_e32 v[180:181], 0\n" \
"v_mov_b64_e32 v[182:183], 0\n" \
"v_sub_f32_e32 v160, 0, v225\n" \
"v_sub_f32_e32 v161, 0, v225\n" \
"v_sub_f32_e32 v162, 0, v225\n" \
"v_sub_f32_e32 v163, 0, v225\n" \
"v_sub_f32_e32 v164, 0, v226\n" \
"v_sub_f32_e32 v165, 0, v226\n" \
"v_sub_f32_e32 v166, 0, v226\n" \
"v_sub_f32_e32 v167, 0, v226\n" \
"v_sub_f32_e32 v168, 0, v227\n" \
"v_sub_f32_e32 v169, 0, v227\n" \
"v_sub_f32_e32 v170, 0, v227\n" \
"v_sub_f32_e32 v171, 0, v227\n" \
"v_sub_f32_e32 v172, 0, v228\n" \
"v_sub_f32_e32 v173, 0, v228\n" \
"v_sub_f32_e32 v174, 0, v228\n" \
"v_sub_f32_e32 v175, 0, v228\n" \
"v_sub_f32_e32 v128, v128, v225\n" \
"v_sub_f32_e32 v129, v129, v225\n" \
"v_sub_f32_e32 v130, v130, v225\n" \
"v_sub_f32_e32 v131, v131, v225\n" \
"v_sub_f32_e32 v132, v132, v225\n" \
"v_sub_f32_e32 v133, v133, v225\n" \
"v_sub_f32_e32 v134, v134, v225\n" \
"v_sub_f32_e32 v135, v135, v225\n" \
"v_sub_f32_e32 v136, v136, v226\n" \
"v_sub_f32_e32 v137, v137, v226\n" \
"v_sub_f32_e32 v138, v138, v226\n" \
"v_sub_f32_e32 v139, v139, v226\n" \
"v_sub_f32_e32 v140, v140, v226\n" \
"v_sub_f32_e32 v141, v141, v226\n" \
"v_sub_f32_e32 v142, v142, v226\n" \
"v_sub_f32_e32 v143, v143, v226\n" \
"v_sub_f32_e32 v144, v144, v227\n" \
"v_sub_f32_e32 v145, v145, v227\n" \
"v_sub_f32_e32 v146, v146, v227\n" \
"v_sub_f32_e32 v147, v147, v227\n" \
"v_sub_f32_e32 v148, v148, v227\n" \
"v_sub_f32_e32 v149, v149, v227\n" \
"v_sub_f32_e32 v150, v150, v227\n" \
"v_sub_f32_e32 v151, v151, v227\n" \
"v_sub_f32_e32 v152, v152, v228\n" \
"v_sub_f32_e32 v153, v153, v228\n" \
"v_sub_f32_e32 v154, v154, v228\n" \
"v_sub_f32_e32 v155, v155, v228\n" \
"v_sub_f32_e32 v156, v156, v228\n" \
"v_sub_f32_e32 v157, v157, v228\n" \
"v_sub_f32_e32 v158, v158, v228\n" \
"v_sub_f32_e32 v159, v159, v228\n" \
"v_exp_f32_e32 v128, v128\n" \
"v_exp_f32_e32 v129, v129\n" \
"v_exp_f32_e32 v130, v130\n" \
"v_exp_f32_e32 v131, v131\n" \
"v_exp_f32_e32 v132, v132\n" \
"v_exp_f32_e32 v133, v133\n" \
"v_exp_f32_e32 v134, v134\n" \
"v_exp_f32_e32 v135, v135\n" \
"v_exp_f32_e32 v136, v136\n" \
"v_exp_f32_e32 v137, v137\n" \
"v_exp_f32_e32 v138, v138\n" \
"v_exp_f32_e32 v139, v139\n" \
"v_exp_f32_e32 v140, v140\n" \
"v_exp_f32_e32 v141, v141\n" \
"v_exp_f32_e32 v142, v142\n" \
"v_exp_f32_e32 v143, v143\n" \
"v_exp_f32_e32 v144, v144\n" \
"v_exp_f32_e32 v145, v145\n" \
"v_exp_f32_e32 v146, v146\n" \
"v_exp_f32_e32 v147, v147\n" \
"v_exp_f32_e32 v148, v148\n" \
"v_exp_f32_e32 v149, v149\n" \
"v_exp_f32_e32 v150, v150\n" \
"v_exp_f32_e32 v151, v151\n" \
"v_exp_f32_e32 v152, v152\n" \
"v_exp_f32_e32 v153, v153\n" \
"v_exp_f32_e32 v154, v154\n" \
"v_exp_f32_e64 v155, v155\n" \
"v_bfe_i32 v228, v224, 7, 1\n" \
"v_and_b32_e32 v228, 0xaaaa, v228\n" \
"v_or_b32_e32 v228, 0x4444, v228\n" \
"v_cvt_pk_bf16_f32 v184, v128, v129\n" \
"v_cvt_pk_bf16_f32 v185, v130, v131\n" \
"v_cvt_pk_bf16_f32 v186, v132, v133\n" \
"v_cvt_pk_bf16_f32 v187, v134, v135\n" \
"v_cvt_pk_bf16_f32 v188, v136, v137\n" \
"v_cvt_pk_bf16_f32 v189, v138, v139\n" \
"v_cvt_pk_bf16_f32 v190, v140, v141\n" \
"v_cvt_pk_bf16_f32 v191, v142, v143\n" \
"v_cvt_pk_bf16_f32 v216, v144, v145\n" \
"v_cvt_pk_bf16_f32 v217, v146, v147\n" \
"v_cvt_pk_bf16_f32 v218, v148, v149\n" \
"v_cvt_pk_bf16_f32 v219, v150, v151\n" \
"v_lshrrev_b32_e64 v235, 3, v224\n" \
"s_waitcnt vmcnt(2) lgkmcnt(0)\n" \
"s_barrier\n" \
"v_mfma_f32_16x16x32_bf16 v[128:131], a[96:99], a[0:3], v[160:163]\n" \
"v_exp_f32_e64 v156, v156\n" \
"v_mfma_f32_16x16x32_bf16 v[128:131], a[100:103], a[4:7], v[128:131]\n" \
"s_add_u32 m0, %[m0b], 114688\n" \
"v_mfma_f32_16x16x32_bf16 v[128:131], a[104:107], a[8:11], v[128:131]\n" \
"v_exp_f32_e64 v157, v157\n" \
"v_mfma_f32_16x16x32_bf16 v[128:131], a[108:111], a[12:15], v[128:131]\n" \
"v_cvt_pk_bf16_f32 v220, v152, v153\n" \
"v_cvt_pk_bf16_f32 v221, v154, v155\n" \
"v_mfma_f32_16x16x32_bf16 v[132:135], a[124:127], a[12:15], v[160:163]\n" \
"v_exp_f32_e64 v158, v158\n" \
"v_mfma_f32_16x16x32_bf16 v[132:135], a[120:123], a[8:11], v[132:135]\n" \
"v_mfma_f32_16x16x32_bf16 v[132:135], a[116:119], a[4:7], v[132:135]\n" \
"buffer_load_dwordx4 %[vd0], %[srdv], s48 offen offset:0 lds\n" \
"v_mfma_f32_16x16x32_bf16 v[132:135], a[112:115], a[0:3], v[132:135]\n" \
"v_exp_f32_e64 v159, v159\n" \
"v_mfma_f32_16x16x32_bf16 v[0:3], a[128:131], v[184:187], 0\n" \
"ds_read_b128 a[64:67], %[kb0] offset:16384\n" \
"v_mfma_f32_16x16x32_bf16 v[32:35], a[128:131], v[188:191], 0\n" \
"v_exp_f32_e64 v128, v128\n" \
"v_mfma_f32_16x16x32_bf16 v[4:7], a[132:135], v[184:187], 0\n" \
"v_cvt_pk_bf16_f32 v222, v156, v157\n" \
"v_cvt_pk_bf16_f32 v223, v158, v159\n" \
"v_mfma_f32_16x16x32_bf16 v[36:39], a[132:135], v[188:191], 0\n" \
"v_exp_f32_e64 v129, v129\n" \
"v_mfma_f32_16x16x32_bf16 v[8:11], a[136:139], v[184:187], 0\n" \
"ds_read_b128 a[68:71], v208 offset:16384\n" \
"v_mfma_f32_16x16x32_bf16 v[40:43], a[136:139], v[188:191], 0\n" \
"v_exp_f32_e64 v130, v130\n" \
"v_mfma_f32_16x16x32_bf16 v[12:15], a[140:143], v[184:187], 0\n" \
"ds_read_b128 a[72:75], v209 offset:16384\n" \
"v_mfma_f32_16x16x32_bf16 v[44:47], a[140:143], v[188:191], 0\n" \
"v_exp_f32_e64 v131, v131\n" \
"v_smfmac_f32_16x16x64_bf16 v[176:179], a[192:195], v[184:191], v228\n" \
"v_mfma_f32_16x16x32_bf16 v[136:139], a[96:99], a[16:19], v[164:167]\n" \
"v_exp_f32_e64 v132, v132\n" \
"v_mfma_f32_16x16x32_bf16 v[136:139], a[100:103], a[20:23], v[136:139]\n" \
"ds_read_b128 a[76:79], v210 offset:16384\n" \
"v_mfma_f32_16x16x32_bf16 v[136:139], a[104:107], a[24:27], v[136:139]\n" \
"v_exp_f32_e64 v133, v133\n" \
"v_mfma_f32_16x16x32_bf16 v[136:139], a[108:111], a[28:31], v[136:139]\n" \
"v_cvt_pk_bf16_f32 v192, v128, v129\n" \
"v_cvt_pk_bf16_f32 v193, v130, v131\n" \
"v_mfma_f32_16x16x32_bf16 v[140:143], a[124:127], a[28:31], v[164:167]\n" \
"v_exp_f32_e64 v134, v134\n" \
"v_mfma_f32_16x16x32_bf16 v[140:143], a[120:123], a[24:27], v[140:143]\n" \
"ds_read_b128 a[92:95], v210 offset:20480\n" \
"v_mfma_f32_16x16x32_bf16 v[140:143], a[116:119], a[20:23], v[140:143]\n" \
"buffer_load_dwordx4 v215, %[srdv], s48 offen offset:1024 lds\n" \
"v_mfma_f32_16x16x32_bf16 v[140:143], a[112:115], a[16:19], v[140:143]\n" \
"v_exp_f32_e64 v135, v135\n" \
"v_mfma_f32_16x16x32_bf16 v[16:19], a[144:147], v[184:187], 0\n" \
"ds_read_b128 a[88:91], v209 offset:20480\n" \
"v_mfma_f32_16x16x32_bf16 v[48:51], a[144:147], v[188:191], 0\n" \
"v_exp_f32_e64 v136, v136\n" \
"v_mfma_f32_16x16x32_bf16 v[20:23], a[148:151], v[184:187], 0\n" \
"v_cvt_pk_bf16_f32 v194, v132, v133\n" \
"v_cvt_pk_bf16_f32 v195, v134, v135\n" \
"v_mfma_f32_16x16x32_bf16 v[52:55], a[148:151], v[188:191], 0\n" \
"v_exp_f32_e64 v137, v137\n" \
"v_mfma_f32_16x16x32_bf16 v[24:27], a[152:155], v[184:187], 0\n" \
"v_exp_f32_e64 v138, v138\n" \
"v_mfma_f32_16x16x32_bf16 v[56:59], a[152:155], v[188:191], 0\n" \
"ds_read_b128 a[84:87], v208 offset:20480\n" \
"v_mfma_f32_16x16x32_bf16 v[28:31], a[156:159], v[184:187], 0\n" \
"v_exp_f32_e64 v139, v139\n" \
"v_mfma_f32_16x16x32_bf16 v[60:63], a[156:159], v[188:191], 0\n" \
"v_mfma_f32_16x16x32_bf16 v[144:147], a[96:99], a[32:35], v[168:171]\n" \
"v_exp_f32_e64 v140, v140\n" \
"v_mfma_f32_16x16x32_bf16 v[144:147], a[100:103], a[36:39], v[144:147]\n" \
"ds_read_b128 a[80:83], %[kb0] offset:20480\n" \
"v_mfma_f32_16x16x32_bf16 v[144:147], a[104:107], a[40:43], v[144:147]\n" \
"v_exp_f32_e64 v141, v141\n" \
"v_mfma_f32_16x16x32_bf16 v[144:147], a[108:111], a[44:47], v[144:147]\n" \
"v_cvt_pk_bf16_f32 v196, v136, v137\n" \
"v_cvt_pk_bf16_f32 v197, v138, v139\n" \
"v_mfma_f32_16x16x32_bf16 v[148:151], a[124:127], a[44:47], v[168:171]\n" \
"v_exp_f32_e64 v142, v142\n" \
"v_mfma_f32_16x16x32_bf16 v[148:151], a[120:123], a[40:43], v[148:151]\n" \
"v_mfma_f32_16x16x32_bf16 v[148:151], a[116:119], a[36:39], v[148:151]\n" \
"buffer_load_dwordx4 %[vd0], %[srdv], s48 offen offset:2048 lds\n" \
"v_mfma_f32_16x16x32_bf16 v[148:151], a[112:115], a[32:35], v[148:151]\n" \
"v_exp_f32_e64 v143, v143\n" \
"v_mfma_f32_16x16x32_bf16 v[64:67], a[128:131], v[216:219], 0\n" \
"ds_read_b64_tr_b16 a[160:161], %[vvb] offset:8192\n" \
"v_mfma_f32_16x16x32_bf16 v[96:99], a[128:131], v[220:223], 0\n" \
"v_exp_f32_e64 v144, v144\n" \
"v_mfma_f32_16x16x32_bf16 v[68:71], a[132:135], v[216:219], 0\n" \
"v_cvt_pk_bf16_f32 v198, v140, v141\n" \
"v_cvt_pk_bf16_f32 v199, v142, v143\n" \
"v_mfma_f32_16x16x32_bf16 v[100:103], a[132:135], v[220:223], 0\n" \
"v_exp_f32_e64 v145, v145\n" \
"v_mfma_f32_16x16x32_bf16 v[72:75], a[136:139], v[216:219], 0\n" \
"ds_read_b64_tr_b16 a[162:163], %[vvb] offset:12288\n" \
"ds_read_b64_tr_b16 a[164:165], v211 offset:8192\n" \
"v_mfma_f32_16x16x32_bf16 v[104:107], a[136:139], v[220:223], 0\n" \
"v_exp_f32_e64 v146, v146\n" \
"v_mfma_f32_16x16x32_bf16 v[76:79], a[140:143], v[216:219], 0\n" \
"ds_read_b64_tr_b16 a[166:167], v211 offset:12288\n" \
"v_mfma_f32_16x16x32_bf16 v[108:111], a[140:143], v[220:223], 0\n" \
"v_exp_f32_e64 v147, v147\n" \
"v_smfmac_f32_16x16x64_bf16 v[180:183], a[192:195], v[216:223], v228\n" \
"ds_read_b64_tr_b16 a[168:169], %[vvb] offset:8704\n" \
"v_mfma_f32_16x16x32_bf16 v[152:155], a[96:99], a[48:51], v[172:175]\n" \
"v_exp_f32_e64 v148, v148\n" \
"v_mfma_f32_16x16x32_bf16 v[152:155], a[100:103], a[52:55], v[152:155]\n" \
"ds_read_b64_tr_b16 a[170:171], %[vvb] offset:12800\n" \
"v_mfma_f32_16x16x32_bf16 v[152:155], a[104:107], a[56:59], v[152:155]\n" \
"v_exp_f32_e64 v149, v149\n" \
"v_mfma_f32_16x16x32_bf16 v[152:155], a[108:111], a[60:63], v[152:155]\n" \
"v_cvt_pk_bf16_f32 v200, v144, v145\n" \
"v_cvt_pk_bf16_f32 v201, v146, v147\n" \
"v_mfma_f32_16x16x32_bf16 v[156:159], a[124:127], a[60:63], v[172:175]\n" \
"v_exp_f32_e64 v150, v150\n" \
"v_mfma_f32_16x16x32_bf16 v[156:159], a[120:123], a[56:59], v[156:159]\n" \
"ds_read_b64_tr_b16 a[172:173], v211 offset:8704\n" \
"v_mfma_f32_16x16x32_bf16 v[156:159], a[116:119], a[52:55], v[156:159]\n" \
"buffer_load_dwordx4 v215, %[srdv], s48 offen offset:3072 lds\n" \
"v_mfma_f32_16x16x32_bf16 v[156:159], a[112:115], a[48:51], v[156:159]\n" \
"v_exp_f32_e64 v151, v151\n" \
"v_mfma_f32_16x16x32_bf16 v[80:83], a[144:147], v[216:219], 0\n" \
"ds_read_b64_tr_b16 a[174:175], v211 offset:12800\n" \
"ds_read_b64_tr_b16 a[176:177], %[vvb] offset:9216\n" \
"v_mfma_f32_16x16x32_bf16 v[112:115], a[144:147], v[220:223], 0\n" \
"v_exp_f32_e64 v152, v152\n" \
"v_mfma_f32_16x16x32_bf16 v[84:87], a[148:151], v[216:219], 0\n" \
"v_cvt_pk_bf16_f32 v202, v148, v149\n" \
"v_cvt_pk_bf16_f32 v203, v150, v151\n" \
"v_mfma_f32_16x16x32_bf16 v[116:119], a[148:151], v[220:223], 0\n" \
"v_exp_f32_e64 v153, v153\n" \
"v_mfma_f32_16x16x32_bf16 v[88:91], a[152:155], v[216:219], 0\n" \
"v_exp_f32_e64 v154, v154\n" \
"v_mfma_f32_16x16x32_bf16 v[120:123], a[152:155], v[220:223], 0\n" \
"ds_read_b64_tr_b16 a[178:179], %[vvb] offset:13312\n" \
"v_mfma_f32_16x16x32_bf16 v[92:95], a[156:159], v[216:219], 0\n" \
"v_exp_f32_e64 v155, v155\n" \
"v_mov_b64_e32 v[216:217], 0\n" \
"v_mov_b64_e32 v[218:219], 0\n" \
"v_mfma_f32_16x16x32_bf16 v[124:127], a[156:159], v[220:223], 0\n" \
"ds_read_b64_tr_b16 a[180:181], v211 offset:9216\n" \
"s_waitcnt lgkmcnt(13)\n" \
"s_nop 0\n" \
"LmA_%=:\n" \
"v_mfma_f32_16x16x32_bf16 v[128:131], a[64:67], a[0:3], v[160:163]\n" \
"v_exp_f32_e64 v156, v156\n" \
"v_mfma_f32_16x16x32_bf16 v[128:131], a[68:71], a[4:7], v[128:131]\n" \
"ds_read_b64_tr_b16 a[182:183], v211 offset:13312\n" \
"s_add_u32 m0, %[m0b], 0\n" \
"v_mfma_f32_16x16x32_bf16 v[128:131], a[72:75], a[8:11], v[128:131]\n" \
"v_exp_f32_e32 v157, v157\n" \
"v_mfma_f32_16x16x32_bf16 v[128:131], a[76:79], a[12:15], v[128:131]\n" \
"v_cvt_pk_bf16_f32 v204, v152, v153\n" \
"v_cvt_pk_bf16_f32 v205, v154, v155\n" \
"v_mfma_f32_16x16x32_bf16 v[132:135], a[92:95], a[12:15], v[160:163]\n" \
"v_exp_f32_e64 v158, v158\n" \
"v_mfma_f32_16x16x32_bf16 v[132:135], a[88:91], a[8:11], v[132:135]\n" \
"ds_read_b64_tr_b16 a[184:185], %[vvb] offset:9728\n" \
"s_waitcnt lgkmcnt(5)\n" \
"v_mfma_f32_16x16x32_bf16 v[132:135], a[84:87], a[4:7], v[132:135]\n" \
"buffer_load_dwordx4 %[kd0], %[srdk], s48 offen offset:0 lds\n" \
"v_mfma_f32_16x16x32_bf16 v[132:135], a[80:83], a[0:3], v[132:135]\n" \
"v_exp_f32_e32 v159, v159\n" \
"v_mfma_f32_16x16x32_bf16 v[64:67], a[128:131], v[216:219], v[64:67]\n" \
"ds_read_b64_tr_b16 a[186:187], %[vvb] offset:13824\n" \
"ds_read_b64_tr_b16 a[188:189], v211 offset:9728\n" \
"v_mfma_f32_16x16x32_bf16 v[64:67], a[160:163], v[200:203], v[64:67]\n" \
"v_exp_f32_e64 v128, v128\n" \
"v_mfma_f32_16x16x32_bf16 v[68:71], a[132:135], v[216:219], v[68:71]\n" \
"v_cvt_pk_bf16_f32 v206, v156, v157\n" \
"v_cvt_pk_bf16_f32 v207, v158, v159\n" \
"v_mfma_f32_16x16x32_bf16 v[68:71], a[164:167], v[200:203], v[68:71]\n" \
"v_exp_f32_e64 v129, v129\n" \
"v_mfma_f32_16x16x32_bf16 v[72:75], a[136:139], v[216:219], v[72:75]\n" \
"ds_read_b64_tr_b16 a[190:191], v211 offset:13824\n" \
"ds_read_b64_tr_b16 a[128:129], %[vvb] offset:16384\n" \
"s_add_u32 s60, s60, 1\n" \
"s_nop 0\n" \
"v_readlane_b32 s58, %[tlo], s60\n" \
"v_mfma_f32_16x16x32_bf16 v[72:75], a[168:171], v[200:203], v[72:75]\n" \
"v_exp_f32_e64 v130, v130\n" \
"v_mfma_f32_16x16x32_bf16 v[76:79], a[140:143], v[216:219], v[76:79]\n" \
"ds_read_b64_tr_b16 a[130:131], %[vvb] offset:20480\n" \
"ds_read_b64_tr_b16 a[132:133], v211 offset:16384\n" \
"v_readlane_b32 s59, %[thi], s60\n" \
"v_mfma_f32_16x16x32_bf16 v[76:79], a[172:175], v[200:203], v[76:79]\n" \
"v_exp_f32_e64 v131, v131\n" \
"v_smfmac_f32_16x16x64_bf16 v[176:179], a[192:195], v[192:199], v228\n" \
"ds_read_b64_tr_b16 a[134:135], v211 offset:20480\n" \
"ds_read_b64_tr_b16 a[136:137], %[vvb] offset:16896\n" \
"v_mfma_f32_16x16x32_bf16 v[136:139], a[64:67], a[16:19], v[164:167]\n" \
"v_exp_f32_e64 v132, v132\n" \
"v_mfma_f32_16x16x32_bf16 v[136:139], a[68:71], a[20:23], v[136:139]\n" \
"ds_read_b64_tr_b16 a[138:139], %[vvb] offset:20992\n" \
"ds_read_b64_tr_b16 a[140:141], v211 offset:16896\n" \
"v_mfma_f32_16x16x32_bf16 v[136:139], a[72:75], a[24:27], v[136:139]\n" \
"v_exp_f32_e64 v133, v133\n" \
"v_mfma_f32_16x16x32_bf16 v[136:139], a[76:79], a[28:31], v[136:139]\n" \
"v_cvt_pk_bf16_f32 v184, v128, v129\n" \
"v_cvt_pk_bf16_f32 v185, v130, v131\n" \
"v_mfma_f32_16x16x32_bf16 v[140:143], a[92:95], a[28:31], v[164:167]\n" \
"v_exp_f32_e64 v134, v134\n" \
"v_mfma_f32_16x16x32_bf16 v[140:143], a[88:91], a[24:27], v[140:143]\n" \
"ds_read_b64_tr_b16 a[142:143], v211 offset:20992\n" \
"s_waitcnt lgkmcnt(8)\n" \
"v_mfma_f32_16x16x32_bf16 v[140:143], a[84:87], a[20:23], v[140:143]\n" \
"buffer_load_dwordx4 v212, %[srdk], s48 offen offset:1024 lds\n" \
"v_mfma_f32_16x16x32_bf16 v[140:143], a[80:83], a[16:19], v[140:143]\n" \
"v_exp_f32_e32 v135, v135\n" \
"v_mfma_f32_16x16x32_bf16 v[80:83], a[144:147], v[216:219], v[80:83]\n" \
"ds_read_b64_tr_b16 a[144:145], %[vvb] offset:17408\n" \
"ds_read_b64_tr_b16 a[146:147], %[vvb] offset:21504\n" \
"v_mfma_f32_16x16x32_bf16 v[80:83], a[176:179], v[200:203], v[80:83]\n" \
"v_exp_f32_e64 v136, v136\n" \
"v_mfma_f32_16x16x32_bf16 v[84:87], a[148:151], v[216:219], v[84:87]\n" \
"v_cvt_pk_bf16_f32 v186, v132, v133\n" \
"v_cvt_pk_bf16_f32 v187, v134, v135\n" \
"v_mfma_f32_16x16x32_bf16 v[84:87], a[180:183], v[200:203], v[84:87]\n" \
"v_exp_f32_e64 v137, v137\n" \
"v_mfma_f32_16x16x32_bf16 v[88:91], a[152:155], v[216:219], v[88:91]\n" \
".if . - adaln_body != 12288\n" \
".error \"size model\"\n" \
".endif\n" \
"Lk2_%=:\n" \
"s_cbranch_execz Lk3_%=\n" \
"s_nop 0\n" \
"v_exp_f32_e64 v138, v138\n" \
"v_mfma_f32_16x16x32_bf16 v[88:91], a[184:187], v[200:203], v[88:91]\n" \
"ds_read_b64_tr_b16 a[148:149], v211 offset:17408\n" \
"ds_read_b64_tr_b16 a[150:151], v211 offset:21504\n" \
"v_mfma_f32_16x16x32_bf16 v[92:95], a[156:159], v[216:219], v[92:95]\n" \
"v_exp_f32_e64 v139, v139\n" \
"v_mfma_f32_16x16x32_bf16 v[92:95], a[188:191], v[200:203], v[92:95]\n" \
"ds_read_b64_tr_b16 a[152:153], %[vvb] offset:17920\n" \
"v_mfma_f32_16x16x32_bf16 v[144:147], a[64:67], a[32:35], v[168:171]\n" \
"v_exp_f32_e64 v140, v140\n" \
"v_mfma_f32_16x16x32_bf16 v[144:147], a[68:71], a[36:39], v[144:147]\n" \
"ds_read_b64_tr_b16 a[154:155], %[vvb] offset:22016\n" \
"ds_read_b64_tr_b16 a[156:157], v211 offset:17920\n" \
"v_mfma_f32_16x16x32_bf16 v[144:147], a[72:75], a[40:43], v[144:147]\n" \
"v_exp_f32_e64 v141, v141\n" \
"v_mfma_f32_16x16x32_bf16 v[144:147], a[76:79], a[44:47], v[144:147]\n" \
"v_cvt_pk_bf16_f32 v188, v136, v137\n" \
"v_cvt_pk_bf16_f32 v189, v138, v139\n" \
"v_mfma_f32_16x16x32_bf16 v[148:151], a[92:95], a[44:47], v[168:171]\n" \
"v_exp_f32_e64 v142, v142\n" \
"v_mfma_f32_16x16x32_bf16 v[148:151], a[88:91], a[40:43], v[148:151]\n" \
"ds_read_b64_tr_b16 a[158:159], v211 offset:22016\n" \
"s_waitcnt lgkmcnt(8)\n" \
"v_mfma_f32_16x16x32_bf16 v[148:151], a[84:87], a[36:39], v[148:151]\n" \
"buffer_load_dwordx4 v213, %[srdk], s48 offen offset:2048 lds\n" \
"v_mfma_f32_16x16x32_bf16 v[148:151], a[80:83], a[32:35], v[148:151]\n" \
"v_exp_f32_e32 v143, v143\n" \
"v_mfma_f32_16x16x32_bf16 v[0:3], a[160:163], v[192:195], v[0:3]\n" \
"ds_read_b128 a[96:99], %[kb0] offset:24576\n" \
"v_mfma_f32_16x16x32_bf16 v[0:3], a[128:131], v[184:187], v[0:3]\n" \
"v_exp_f32_e64 v144, v144\n" \
"v_mfma_f32_16x16x32_bf16 v[4:7], a[164:167], v[192:195], v[4:7]\n" \
"v_cvt_pk_bf16_f32 v190, v140, v141\n" \
"v_cvt_pk_bf16_f32 v191, v142, v143\n" \
"v_mfma_f32_16x16x32_bf16 v[4:7], a[132:135], v[184:187], v[4:7]\n" \
"v_exp_f32_e64 v145, v145\n" \
"v_mfma_f32_16x16x32_bf16 v[8:11], a[168:171], v[192:195], v[8:11]\n" \
"ds_read_b128 a[100:103], v208 offset:24576\n" \
"v_mfma_f32_16x16x32_bf16 v[8:11], a[136:139], v[184:187], v[8:11]\n" \
"v_exp_f32_e64 v146, v146\n" \
"v_mfma_f32_16x16x32_bf16 v[12:15], a[172:175], v[192:195], v[12:15]\n" \
"ds_read_b128 a[104:107], v209 offset:24576\n" \
"v_mfma_f32_16x16x32_bf16 v[12:15], a[140:143], v[184:187], v[12:15]\n" \
"v_exp_f32_e64 v147, v147\n" \
"v_smfmac_f32_16x16x64_bf16 v[180:183], a[192:195], v[200:207], v228\n" \
"ds_read_b128 a[108:111], v210 offset:24576\n" \
"v_mfma_f32_16x16x32_bf16 v[152:155], a[64:67], a[48:51], v[172:175]\n" \
"v_exp_f32_e64 v148, v148\n" \
"v_mfma_f32_16x16x32_bf16 v[152:155], a[68:71], a[52:55], v[152:155]\n" \
"ds_read_b128 a[124:127], v210 offset:28672\n" \
"v_mfma_f32_16x16x32_bf16 v[152:155], a[72:75], a[56:59], v[152:155]\n" \
"v_exp_f32_e64 v149, v149\n" \
"v_mfma_f32_16x16x32_bf16 v[152:155], a[76:79], a[60:63], v[152:155]\n" \
"v_cvt_pk_bf16_f32 v216, v144, v145\n" \
"v_cvt_pk_bf16_f32 v217, v146, v147\n" \
"v_mfma_f32_16x16x32_bf16 v[156:159], a[92:95], a[60:63], v[172:175]\n" \
"v_exp_f32_e64 v150, v150\n" \
"v_mfma_f32_16x16x32_bf16 v[156:159], a[88:91], a[56:59], v[156:159]\n" \
"ds_read_b128 a[120:123], v209 offset:28672\n" \
"s_waitcnt lgkmcnt(6)\n" \
"v_mfma_f32_16x16x32_bf16 v[156:159], a[84:87], a[52:55], v[156:159]\n" \
"buffer_load_dwordx4 v214, %[srdk], s48 offen offset:3072 lds\n" \
"v_mfma_f32_16x16x32_bf16 v[156:159], a[80:83], a[48:51], v[156:159]\n" \
"v_exp_f32_e32 v151, v151\n" \
"v_mfma_f32_16x16x32_bf16 v[16:19], a[176:179], v[192:195], v[16:19]\n" \
"ds_read_b128 a[116:119], v208 offset:28672\n" \
"v_mfma_f32_16x16x32_bf16 v[16:19], a[144:147], v[184:187], v[16:19]\n" \
"v_exp_f32_e64 v152, v152\n" \
"v_mfma_f32_16x16x32_bf16 v[20:23], a[180:183], v[192:195], v[20:23]\n" \
"v_cvt_pk_bf16_f32 v218, v148, v149\n" \
"v_cvt_pk_bf16_f32 v219, v150, v151\n" \
"v_mfma_f32_16x16x32_bf16 v[20:23], a[148:151], v[184:187], v[20:23]\n" \
"v_exp_f32_e64 v153, v153\n" \
"v_mfma_f32_16x16x32_bf16 v[24:27], a[184:187], v[192:195], v[24:27]\n" \
"v_exp_f32_e64 v154, v154\n" \
"v_mfma_f32_16x16x32_bf16 v[24:27], a[152:155], v[184:187], v[24:27]\n" \
"ds_read_b128 a[112:115], %[kb0] offset:28672\n" \
"s_add_u32 s48, s48, 0x4000\n" \
"s_mov_b32 m0, s44\n" \
"s_addk_i32 s44, 0x400\n" \
"buffer_load_dwordx4 v229, s[64:67], s45 offen offset:0 nt lds\n" \
"s_add_u32 s45, s45, 393216\n" \
"v_mfma_f32_16x16x32_bf16 v[28:31], a[188:191], v[192:195], v[28:31]\n" \
"v_exp_f32_e64 v155, v155\n" \
"v_mfma_f32_16x16x32_bf16 v[28:31], a[156:159], v[184:187], v[28:31]\n" \
"s_waitcnt vmcnt(14) lgkmcnt(2)\n" \
"s_barrier\n" \
"v_mfma_f32_16x16x32_bf16 v[128:131], a[96:99], a[0:3], v[160:163]\n" \
"v_exp_f32_e64 v156, v156\n" \
"v_mfma_f32_16x16x32_bf16 v[128:131], a[100:103], a[4:7], v[128:131]\n" \
"s_add_u32 m0, %[m0b], 65536\n" \
"v_mfma_f32_16x16x32_bf16 v[128:131], a[104:107], a[8:11], v[128:131]\n" \
"v_exp_f32_e64 v157, v157\n" \
"v_mfma_f32_16x16x32_bf16 v[128:131], a[108:111], a[12:15], v[128:131]\n" \
"v_cvt_pk_bf16_f32 v220, v152, v153\n" \
"v_cvt_pk_bf16_f32 v221, v154, v155\n" \
"v_mfma_f32_16x16x32_bf16 v[132:135], a[124:127], a[12:15], v[160:163]\n" \
"v_exp_f32_e64 v158, v158\n" \
"v_mfma_f32_16x16x32_bf16 v[132:135], a[120:123], a[8:11], v[132:135]\n" \
"s_waitcnt lgkmcnt(0)\n" \
"v_mfma_f32_16x16x32_bf16 v[132:135], a[116:119], a[4:7], v[132:135]\n" \
"buffer_load_dwordx4 %[vd0], %[srdv], s48 offen offset:0 lds\n" \
"v_mfma_f32_16x16x32_bf16 v[132:135], a[112:115], a[0:3], v[132:135]\n" \
"v_exp_f32_e32 v159, v159\n" \
"v_mfma_f32_16x16x32_bf16 v[32:35], a[160:163], v[196:199], v[32:35]\n" \
"ds_read_b128 a[64:67], %[kb0] offset:32768\n" \
"v_mfma_f32_16x16x32_bf16 v[32:35], a[128:131], v[188:191], v[32:35]\n" \
"v_exp_f32_e64 v128, v128\n" \
"v_mfma_f32_16x16x32_bf16 v[36:39], a[164:167], v[196:199], v[36:39]\n" \
"v_cvt_pk_bf16_f32 v222, v156, v157\n" \
"v_cvt_pk_bf16_f32 v223, v158, v159\n" \
"v_mfma_f32_16x16x32_bf16 v[36:39], a[132:135], v[188:191], v[36:39]\n" \
"v_exp_f32_e64 v129, v129\n" \
"v_mfma_f32_16x16x32_bf16 v[40:43], a[168:171], v[196:199], v[40:43]\n" \
"ds_read_b128 a[68:71], v208 offset:32768\n" \
"v_mfma_f32_16x16x32_bf16 v[40:43], a[136:139], v[188:191], v[40:43]\n" \
"v_exp_f32_e64 v130, v130\n" \
"v_mfma_f32_16x16x32_bf16 v[44:47], a[172:175], v[196:199], v[44:47]\n" \
"ds_read_b128 a[72:75], v209 offset:32768\n" \
"v_mfma_f32_16x16x32_bf16 v[44:47], a[140:143], v[188:191], v[44:47]\n" \
"v_exp_f32_e64 v131, v131\n" \
"v_smfmac_f32_16x16x64_bf16 v[176:179], a[192:195], v[184:191], v228\n" \
"v_mfma_f32_16x16x32_bf16 v[136:139], a[96:99], a[16:19], v[164:167]\n" \
"v_exp_f32_e64 v132, v132\n" \
"v_mfma_f32_16x16x32_bf16 v[136:139], a[100:103], a[20:23], v[136:139]\n" \
"ds_read_b128 a[76:79], v210 offset:32768\n" \
"v_mfma_f32_16x16x32_bf16 v[136:139], a[104:107], a[24:27], v[136:139]\n" \
"v_exp_f32_e64 v133, v133\n" \
"v_mfma_f32_16x16x32_bf16 v[136:139], a[108:111], a[28:31], v[136:139]\n" \
"v_cvt_pk_bf16_f32 v192, v128, v129\n" \
"v_cvt_pk_bf16_f32 v193, v130, v131\n" \
"v_mfma_f32_16x16x32_bf16 v[140:143], a[124:127], a[28:31], v[164:167]\n" \
"v_exp_f32_e64 v134, v134\n" \
"v_mfma_f32_16x16x32_bf16 v[140:143], a[120:123], a[24:27], v[140:143]\n" \
"ds_read_b128 a[92:95], v210 offset:36864\n" \
"v_mfma_f32_16x16x32_bf16 v[140:143], a[116:119], a[20:23], v[140:143]\n" \
"buffer_load_dwordx4 v215, %[srdv], s48 offen offset:1024 lds\n" \
"v_mfma_f32_16x16x32_bf16 v[140:143], a[112:115], a[16:19], v[140:143]\n" \
"v_exp_f32_e64 v135, v135\n" \
"v_mfma_f32_16x16x32_bf16 v[48:51], a[176:179], v[196:199], v[48:51]\n" \
"ds_read_b128 a[88:91], v209 offset:36864\n" \
"v_mfma_f32_16x16x32_bf16 v[48:51], a[144:147], v[188:191], v[48:51]\n" \
"v_exp_f32_e64 v136, v136\n" \
"v_mfma_f32_16x16x32_bf16 v[52:55], a[180:183], v[196:199], v[52:55]\n" \
"v_cvt_pk_bf16_f32 v194, v132, v133\n" \
"v_cvt_pk_bf16_f32 v195, v134, v135\n" \
"v_mfma_f32_16x16x32_bf16 v[52:55], a[148:151], v[188:191], v[52:55]\n" \
"v_exp_f32_e64 v137, v137\n" \
"v_mfma_f32_16x16x32_bf16 v[56:59], a[184:187], v[196:199], v[56:59]\n" \
"v_exp_f32_e64 v138, v138\n" \
"v_mfma_f32_16x16x32_bf16 v[56:59], a[152:155], v[188:191], v[56:59]\n" \
"ds_read_b128 a[84:87], v208 offset:36864\n" \
"v_mfma_f32_16x16x32_bf16 v[60:63], a[188:191], v[196:199], v[60:63]\n" \
"v_exp_f32_e64 v139, v139\n" \
"v_mfma_f32_16x16x32_bf16 v[60:63], a[156:159], v[188:191], v[60:63]\n" \
"s_cmp_lg_u32 %[wid], 0\n" \
"s_cbranch_scc1 LtA3_%=\n" \
"global_load_dword v237, v224, s[58:59]\n" \
"s_add_u32 s61, s48, 49152\n" \
"s_mov_b32 exec_hi, 0\n" \
"s_nop 0\n" \
"buffer_load_dword v237, v224, %[srdp], s61 offen\n" \
"s_mov_b32 exec_hi, -1\n" \
"s_nop 0\n" \
"LtA3_%=:\n" \
"v_mfma_f32_16x16x32_bf16 v[144:147], a[96:99], a[32:35], v[168:171]\n" \
"v_exp_f32_e64 v140, v140\n" \
"v_mfma_f32_16x16x32_bf16 v[144:147], a[100:103], a[36:39], v[144:147]\n" \
"ds_read_b128 a[80:83], %[kb0] offset:36864\n" \
"v_mfma_f32_16x16x32_bf16 v[144:147], a[104:107], a[40:43], v[144:147]\n" \
"v_exp_f32_e64 v141, v141\n" \
"v_mfma_f32_16x16x32_bf16 v[144:147], a[108:111], a[44:47], v[144:147]\n" \
"v_cvt_pk_bf16_f32 v196, v136, v137\n" \
"v_cvt_pk_bf16_f32 v197, v138, v139\n" \
"v_mfma_f32_16x16x32_bf16 v[148:151], a[124:127], a[44:47], v[168:171]\n" \
"v_exp_f32_e64 v142, v142\n" \
"v_mfma_f32_16x16x32_bf16 v[148:151], a[120:123], a[40:43], v[148:151]\n" \
"v_mfma_f32_16x16x32_bf16 v[148:151], a[116:119], a[36:39], v[148:151]\n" \
"buffer_load_dwordx4 %[vd0], %[srdv], s48 offen offset:2048 lds\n" \
"v_mfma_f32_16x16x32_bf16 v[148:151], a[112:115], a[32:35], v[148:151]\n" \
"v_exp_f32_e64 v143, v143\n" \
"v_mfma_f32_16x16x32_bf16 v[96:99], a[160:163], v[204:207], v[96:99]\n" \
"ds_read_b64_tr_b16 a[160:161], %[vvb] offset:24576\n" \
"v_mfma_f32_16x16x32_bf16 v[96:99], a[128:131], v[220:223], v[96:99]\n" \
"v_exp_f32_e64 v144, v144\n" \
"v_mfma_f32_16x16x32_bf16 v[100:103], a[164:167], v[204:207], v[100:103]\n" \
"v_cvt_pk_bf16_f32 v198, v140, v141\n" \
"v_cvt_pk_bf16_f32 v199, v142, v143\n" \
"v_mfma_f32_16x16x32_bf16 v[100:103], a[132:135], v[220:223], v[100:103]\n" \
"v_exp_f32_e64 v145, v145\n" \
"v_mfma_f32_16x16x32_bf16 v[104:107], a[168:171], v[204:207], v[104:107]\n" \
"ds_read_b64_tr_b16 a[162:163], %[vvb] offset:28672\n" \
"ds_read_b64_tr_b16 a[164:165], v211 offset:24576\n" \
"v_mfma_f32_16x16x32_bf16 v[104:107], a[136:139], v[220:223], v[104:107]\n" \
"v_exp_f32_e64 v146, v146\n" \
"v_mfma_f32_16x16x32_bf16 v[108:111], a[172:175], v[204:207], v[108:111]\n" \
"ds_read_b64_tr_b16 a[166:167], v211 offset:28672\n" \
"v_mfma_f32_16x16x32_bf16 v[108:111], a[140:143], v[220:223], v[108:111]\n" \
"v_exp_f32_e64 v147, v147\n" \
"v_smfmac_f32_16x16x64_bf16 v[180:183], a[192:195], v[216:223], v228\n" \
"ds_read_b64_tr_b16 a[168:169], %[vvb] offset:25088\n" \
"v_mfma_f32_16x16x32_bf16 v[152:155], a[96:99], a[48:51], v[172:175]\n" \
"v_exp_f32_e64 v148, v148\n" \
"v_mfma_f32_16x16x32_bf16 v[152:155], a[100:103], a[52:55], v[152:155]\n" \
"ds_read_b64_tr_b16 a[170:171], %[vvb] offset:29184\n" \
"v_mfma_f32_16x16x32_bf16 v[152:155], a[104:107], a[56:59], v[152:155]\n" \
"v_exp_f32_e64 v149, v149\n" \
"v_mfma_f32_16x16x32_bf16 v[152:155], a[108:111], a[60:63], v[152:155]\n" \
"v_cvt_pk_bf16_f32 v200, v144, v145\n" \
"v_cvt_pk_bf16_f32 v201, v146, v147\n" \
"v_mfma_f32_16x16x32_bf16 v[156:159], a[124:127], a[60:63], v[172:175]\n" \
"v_exp_f32_e64 v150, v150\n" \
"v_mfma_f32_16x16x32_bf16 v[156:159], a[120:123], a[56:59], v[156:159]\n" \
"ds_read_b64_tr_b16 a[172:173], v211 offset:25088\n" \
"v_mfma_f32_16x16x32_bf16 v[156:159], a[116:119], a[52:55], v[156:159]\n" \
"buffer_load_dwordx4 v215, %[srdv], s48 offen offset:3072 lds\n" \
"v_mfma_f32_16x16x32_bf16 v[156:159], a[112:115], a[48:51], v[156:159]\n" \
"v_exp_f32_e64 v151, v151\n" \
"v_mfma_f32_16x16x32_bf16 v[112:115], a[176:179], v[204:207], v[112:115]\n" \
"ds_read_b64_tr_b16 a[174:175], v211 offset:29184\n" \
"ds_read_b64_tr_b16 a[176:177], %[vvb] offset:25600\n" \
"v_mfma_f32_16x16x32_bf16 v[112:115], a[144:147], v[220:223], v[112:115]\n" \
"v_exp_f32_e64 v152, v152\n" \
"v_mfma_f32_16x16x32_bf16 v[116:119], a[180:183], v[204:207], v[116:119]\n" \
"v_cvt_pk_bf16_f32 v202, v148, v149\n" \
"v_cvt_pk_bf16_f32 v203, v150, v151\n" \
"v_mfma_f32_16x16x32_bf16 v[116:119], a[148:151], v[220:223], v[116:119]\n" \
"v_exp_f32_e64 v153, v153\n" \
"v_mfma_f32_16x16x32_bf16 v[120:123], a[184:187], v[204:207], v[120:123]\n" \
"v_exp_f32_e64 v154, v154\n" \
"v_mfma_f32_16x16x32_bf16 v[120:123], a[152:155], v[220:223], v[120:123]\n" \
"ds_read_b64_tr_b16 a[178:179], %[vvb] offset:29696\n" \
"v_mfma_f32_16x16x32_bf16 v[124:127], a[188:191], v[204:207], v[124:127]\n" \
"v_exp_f32_e64 v155, v155\n" \
"v_mfma_f32_16x16x32_bf16 v[124:127], a[156:159], v[220:223], v[124:127]\n" \
"ds_read_b64_tr_b16 a[180:181], v211 offset:25600\n" \
"s_waitcnt lgkmcnt(13)\n" \
"v_mfma_f32_16x16x32_bf16 v[128:131], a[64:67], a[0:3], v[160:163]\n" \
"v_exp_f32_e32 v156, v156\n" \
"v_mfma_f32_16x16x32_bf16 v[128:131], a[68:71], a[4:7], v[128:131]\n" \
"ds_read_b64_tr_b16 a[182:183], v211 offset:29696\n" \
"s_add_u32 m0, %[m0b], 16384\n" \
"v_mfma_f32_16x16x32_bf16 v[128:131], a[72:75], a[8:11], v[128:131]\n" \
"v_exp_f32_e64 v157, v157\n" \
"v_mfma_f32_16x16x32_bf16 v[128:131], a[76:79], a[12:15], v[128:131]\n" \
"v_cvt_pk_bf16_f32 v204, v152, v153\n" \
"v_cvt_pk_bf16_f32 v205, v154, v155\n" \
"v_mfma_f32_16x16x32_bf16 v[132:135], a[92:95], a[12:15], v[160:163]\n" \
"v_exp_f32_e64 v158, v158\n" \
"v_mfma_f32_16x16x32_bf16 v[132:135], a[88:91], a[8:11], v[132:135]\n" \
"ds_read_b64_tr_b16 a[184:185], %[vvb] offset:26112\n" \
"s_waitcnt lgkmcnt(5)\n" \
"v_mfma_f32_16x16x32_bf16 v[132:135], a[84:87], a[4:7], v[132:135]\n" \
"buffer_load_dwordx4 %[kd0], %[srdk], s48 offen offset:0 lds\n" \
"v_mfma_f32_16x16x32_bf16 v[132:135], a[80:83], a[0:3], v[132:135]\n" \
"v_exp_f32_e32 v159, v159\n" \
"v_mfma_f32_16x16x32_bf16 v[64:67], a[128:131], v[216:219], v[64:67]\n" \
"ds_read_b64_tr_b16 a[186:187], %[vvb] offset:30208\n" \
"ds_read_b64_tr_b16 a[188:189], v211 offset:26112\n" \
"v_mfma_f32_16x16x32_bf16 v[64:67], a[160:163], v[200:203], v[64:67]\n" \
"v_exp_f32_e64 v128, v128\n" \
"v_mfma_f32_16x16x32_bf16 v[68:71], a[132:135], v[216:219], v[68:71]\n" \
"v_cvt_pk_bf16_f32 v206, v156, v157\n" \
"v_cvt_pk_bf16_f32 v207, v158, v159\n" \
"v_mfma_f32_16x16x32_bf16 v[68:71], a[164:167], v[200:203], v[68:71]\n" \
"v_exp_f32_e64 v129, v129\n" \
"v_mfma_f32_16x16x32_bf16 v[72:75], a[136:139], v[216:219], v[72:75]\n" \
"ds_read_b64_tr_b16 a[190:191], v211 offset:30208\n" \
"ds_read_b64_tr_b16 a[128:129], %[vvb] offset:32768\n" \
"v_mfma_f32_16x16x32_bf16 v[72:75], a[168:171], v[200:203], v[72:75]\n" \
"v_exp_f32_e64 v130, v130\n" \
"v_mfma_f32_16x16x32_bf16 v[76:79], a[140:143], v[216:219], v[76:79]\n" \
"ds_read_b64_tr_b16 a[130:131], %[vvb] offset:36864\n" \
"ds_read_b64_tr_b16 a[132:133], v211 offset:32768\n" \
"v_mfma_f32_16x16x32_bf16 v[76:79], a[172:175], v[200:203], v[76:79]\n" \
"v_exp_f32_e64 v131, v131\n" \
"v_smfmac_f32_16x16x64_bf16 v[176:179], a[192:195], v[192:199], v228\n" \
"ds_read_b64_tr_b16 a[134:135], v211 offset:36864\n" \
"ds_read_b64_tr_b16 a[136:137], %[vvb] offset:33280\n" \
"v_mfma_f32_16x16x32_bf16 v[136:139], a[64:67], a[16:19], v[164:167]\n" \
"v_exp_f32_e64 v132, v132\n" \
"v_mfma_f32_16x16x32_bf16 v[136:139], a[68:71], a[20:23], v[136:139]\n" \
"ds_read_b64_tr_b16 a[138:139], %[vvb] offset:37376\n" \
"ds_read_b64_tr_b16 a[140:141], v211 offset:33280\n" \
"v_mfma_f32_16x16x32_bf16 v[136:139], a[72:75], a[24:27], v[136:139]\n" \
"v_exp_f32_e64 v133, v133\n" \
"v_mfma_f32_16x16x32_bf16 v[136:139], a[76:79], a[28:31], v[136:139]\n" \
"v_cvt_pk_bf16_f32 v184, v128, v129\n" \
"v_cvt_pk_bf16_f32 v185, v130, v131\n" \
"v_mfma_f32_16x16x32_bf16 v[140:143], a[92:95], a[28:31], v[164:167]\n" \
"v_exp_f32_e64 v134, v134\n" \
"v_mfma_f32_16x16x32_bf16 v[140:143], a[88:91], a[24:27], v[140:143]\n" \
"ds_read_b64_tr_b16 a[142:143], v211 offset:37376\n" \
"s_waitcnt lgkmcnt(8)\n" \
"v_mfma_f32_16x16x32_bf16 v[140:143], a[84:87], a[20:23], v[140:143]\n" \
"buffer_load_dwordx4 v212, %[srdk], s48 offen offset:1024 lds\n" \
"v_mfma_f32_16x16x32_bf16 v[140:143], a[80:83], a[16:19], v[140:143]\n" \
"v_exp_f32_e32 v135, v135\n" \
"v_mfma_f32_16x16x32_bf16 v[80:83], a[144:147], v[216:219], v[80:83]\n" \
"ds_read_b64_tr_b16 a[144:145], %[vvb] offset:33792\n" \
"ds_read_b64_tr_b16 a[146:147], %[vvb] offset:37888\n" \
"v_mfma_f32_16x16x32_bf16 v[80:83], a[176:179], v[200:203], v[80:83]\n" \
"v_exp_f32_e64 v136, v136\n" \
"v_mfma_f32_16x16x32_bf16 v[84:87], a[148:151], v[216:219], v[84:87]\n" \
"v_cvt_pk_bf16_f32 v186, v132, v133\n" \
"v_cvt_pk_bf16_f32 v187, v134, v135\n" \
"v_mfma_f32_16x16x32_bf16 v[84:87], a[180:183], v[200:203], v[84:87]\n" \
"v_exp_f32_e64 v137, v137\n" \
"v_mfma_f32_16x16x32_bf16 v[88:91], a[152:155], v[216:219], v[88:91]\n" \
"v_exp_f32_e64 v138, v138\n" \
"v_mfma_f32_16x16x32_bf16 v[88:91], a[184:187], v[200:203], v[88:91]\n" \
"ds_read_b64_tr_b16 a[148:149], v211 offset:33792\n" \
"ds_read_b64_tr_b16 a[150:151], v211 offset:37888\n" \
"v_mfma_f32_16x16x32_bf16 v[92:95], a[156:159], v[216:219], v[92:95]\n" \
"v_exp_f32_e64 v139, v139\n" \
"v_mfma_f32_16x16x32_bf16 v[92:95], a[188:191], v[200:203], v[92:95]\n" \
"ds_read_b64_tr_b16 a[152:153], %[vvb] offset:34304\n" \
"buffer_store_dwordx4 a[196:199], v235, %[srdz], s62 offen offset:0 sc1\n" \
"v_mfma_f32_16x16x32_bf16 v[144:147], a[64:67], a[32:35], v[168:171]\n" \
"v_exp_f32_e64 v140, v140\n" \
"v_mfma_f32_16x16x32_bf16 v[144:147], a[68:71], a[36:39], v[144:147]\n" \
"ds_read_b64_tr_b16 a[154:155], %[vvb] offset:38400\n" \
"ds_read_b64_tr_b16 a[156:157], v211 offset:34304\n" \
"v_mfma_f32_16x16x32_bf16 v[144:147], a[72:75], a[40:43], v[144:147]\n" \
"v_exp_f32_e64 v141, v141\n" \
"v_mfma_f32_16x16x32_bf16 v[144:147], a[76:79], a[44:47], v[144:147]\n" \
"v_cvt_pk_bf16_f32 v188, v136, v137\n" \
"v_cvt_pk_bf16_f32 v189, v138, v139\n" \
"v_mfma_f32_16x16x32_bf16 v[148:151], a[92:95], a[44:47], v[168:171]\n" \
"v_exp_f32_e64 v142, v142\n" \
"v_mfma_f32_16x16x32_bf16 v[148:151], a[88:91], a[40:43], v[148:151]\n" \
"ds_read_b64_tr_b16 a[158:159], v211 offset:38400\n" \
"s_waitcnt lgkmcnt(8)\n" \
"v_mfma_f32_16x16x32_bf16 v[148:151], a[84:87], a[36:39], v[148:151]\n" \
"buffer_load_dwordx4 v213, %[srdk], s48 offen offset:2048 lds\n" \
"v_mfma_f32_16x16x32_bf16 v[148:151], a[80:83], a[32:35], v[148:151]\n" \
"v_exp_f32_e32 v143, v143\n" \
"v_mfma_f32_16x16x32_bf16 v[0:3], a[160:163], v[192:195], v[0:3]\n" \
"ds_read_b128 a[96:99], %[kb0] offset:40960\n" \
"v_mfma_f32_16x16x32_bf16 v[0:3], a[128:131], v[184:187], v[0:3]\n" \
"v_exp_f32_e64 v144, v144\n" \
"v_mfma_f32_16x16x32_bf16 v[4:7], a[164:167], v[192:195], v[4:7]\n" \
"v_cvt_pk_bf16_f32 v190, v140, v141\n" \
"v_cvt_pk_bf16_f32 v191, v142, v143\n" \
"v_mfma_f32_16x16x32_bf16 v[4:7], a[132:135], v[184:187], v[4:7]\n" \
"v_exp_f32_e64 v145, v145\n" \
"v_mfma_f32_16x16x32_bf16 v[8:11], a[168:171], v[192:195], v[8:11]\n" \
"ds_read_b128 a[100:103], v208 offset:40960\n" \
"v_mfma_f32_16x16x32_bf16 v[8:11], a[136:139], v[184:187], v[8:11]\n" \
"v_exp_f32_e64 v146, v146\n" \
"v_mfma_f32_16x16x32_bf16 v[12:15], a[172:175], v[192:195], v[12:15]\n" \
"ds_read_b128 a[104:107], v209 offset:40960\n" \
"v_mfma_f32_16x16x32_bf16 v[12:15], a[140:143], v[184:187], v[12:15]\n" \
"v_exp_f32_e64 v147, v147\n" \
"v_smfmac_f32_16x16x64_bf16 v[180:183], a[192:195], v[200:207], v228\n" \
"ds_read_b128 a[108:111], v210 offset:40960\n" \
"v_mfma_f32_16x16x32_bf16 v[152:155], a[64:67], a[48:51], v[172:175]\n" \
"v_exp_f32_e64 v148, v148\n" \
"v_mfma_f32_16x16x32_bf16 v[152:155], a[68:71], a[52:55], v[152:155]\n" \
"ds_read_b128 a[124:127], v210 offset:45056\n" \
"v_mfma_f32_16x16x32_bf16 v[152:155], a[72:75], a[56:59], v[152:155]\n" \
"v_exp_f32_e64 v149, v149\n" \
"v_mfma_f32_16x16x32_bf16 v[152:155], a[76:79], a[60:63], v[152:155]\n" \
"v_cvt_pk_bf16_f32 v216, v144, v145\n" \
"v_cvt_pk_bf16_f32 v217, v146, v147\n" \
"v_mfma_f32_16x16x32_bf16 v[156:159], a[92:95], a[60:63], v[172:175]\n" \
"v_exp_f32_e64 v150, v150\n" \
"v_mfma_f32_16x16x32_bf16 v[156:159], a[88:91], a[56:59], v[156:159]\n" \
"ds_read_b128 a[120:123], v209 offset:45056\n" \
"s_waitcnt lgkmcnt(6)\n" \
"v_mfma_f32_16x16x32_bf16 v[156:159], a[84:87], a[52:55], v[156:159]\n" \
"buffer_load_dwordx4 v214, %[srdk], s48 offen offset:3072 lds\n" \
"v_mfma_f32_16x16x32_bf16 v[156:159], a[80:83], a[48:51], v[156:159]\n" \
"v_exp_f32_e32 v151, v151\n" \
"v_mfma_f32_16x16x32_bf16 v[16:19], a[176:179], v[192:195], v[16:19]\n" \
"ds_read_b128 a[116:119], v208 offset:45056\n" \
"v_mfma_f32_16x16x32_bf16 v[16:19], a[144:147], v[184:187], v[16:19]\n" \
"v_exp_f32_e64 v152, v152\n" \
"v_mfma_f32_16x16x32_bf16 v[20:23], a[180:183], v[192:195], v[20:23]\n" \
"v_cvt_pk_bf16_f32 v218, v148, v149\n" \
"v_cvt_pk_bf16_f32 v219, v150, v151\n" \
"v_mfma_f32_16x16x32_bf16 v[20:23], a[148:151], v[184:187], v[20:23]\n" \
"v_exp_f32_e64 v153, v153\n" \
"v_mfma_f32_16x16x32_bf16 v[24:27], a[184:187], v[192:195], v[24:27]\n" \
"v_exp_f32_e64 v154, v154\n" \
"v_mfma_f32_16x16x32_bf16 v[24:27], a[152:155], v[184:187], v[24:27]\n" \
"ds_read_b128 a[112:115], %[kb0] offset:45056\n" \
"s_add_u32 s48, s48, 0x4000\n" \
"v_mfma_f32_16x16x32_bf16 v[28:31], a[188:191], v[192:195], v[28:31]\n" \
"v_exp_f32_e64 v155, v155\n" \
"v_mfma_f32_16x16x32_bf16 v[28:31], a[156:159], v[184:187], v[28:31]\n" \
"s_waitcnt vmcnt(14) lgkmcnt(2)\n" \
"s_barrier\n" \
"v_mfma_f32_16x16x32_bf16 v[128:131], a[96:99], a[0:3], v[160:163]\n" \
"v_exp_f32_e64 v156, v156\n" \
"v_mfma_f32_16x16x32_bf16 v[128:131], a[100:103], a[4:7], v[128:131]\n" \
"s_add_u32 m0, %[m0b], 81920\n" \
"v_mfma_f32_16x16x32_bf16 v[128:131], a[104:107], a[8:11], v[128:131]\n" \
"v_exp_f32_e64 v157, v157\n" \
"v_mfma_f32_16x16x32_bf16 v[128:131], a[108:111], a[12:15], v[128:131]\n" \
"v_cvt_pk_bf16_f32 v220, v152, v153\n" \
"v_cvt_pk_bf16_f32 v221, v154, v155\n" \
"v_mfma_f32_16x16x32_bf16 v[132:135], a[124:127], a[12:15], v[160:163]\n" \
"v_exp_f32_e64 v158, v158\n" \
"v_mfma_f32_16x16x32_bf16 v[132:135], a[120:123], a[8:11], v[132:135]\n" \
"s_waitcnt lgkmcnt(0)\n" \
"v_mfma_f32_16x16x32_bf16 v[132:135], a[116:119], a[4:7], v[132:135]\n" \
"buffer_load_dwordx4 %[vd0], %[srdv], s48 offen offset:0 lds\n" \
"v_mfma_f32_16x16x32_bf16 v[132:135], a[112:115], a[0:3], v[132:135]\n" \
"v_exp_f32_e32 v159, v159\n" \
"v_mfma_f32_16x16x32_bf16 v[32:35], a[160:163], v[196:199], v[32:35]\n" \
"ds_read_b128 a[64:67], %[kb0] offset:49152\n" \
"v_mfma_f32_16x16x32_bf16 v[32:35], a[128:131], v[188:191], v[32:35]\n" \
"v_exp_f32_e64 v128, v128\n" \
"v_mfma_f32_16x16x32_bf16 v[36:39], a[164:167], v[196:199], v[36:39]\n" \
"v_cvt_pk_bf16_f32 v222, v156, v157\n" \
"v_cvt_pk_bf16_f32 v223, v158, v159\n" \
"v_mfma_f32_16x16x32_bf16 v[36:39], a[132:135], v[188:191], v[36:39]\n" \
"v_exp_f32_e64 v129, v129\n" \
"v_mfma_f32_16x16x32_bf16 v[40:43], a[168:171], v[196:199], v[40:43]\n" \
"ds_read_b128 a[68:71], v208 offset:49152\n" \
"v_mfma_f32_16x16x32_bf16 v[40:43], a[136:139], v[188:191], v[40:43]\n" \
"v_exp_f32_e64 v130, v130\n" \
"v_mfma_f32_16x16x32_bf16 v[44:47], a[172:175], v[196:199], v[44:47]\n" \
"ds_read_b128 a[72:75], v209 offset:49152\n" \
"v_mfma_f32_16x16x32_bf16 v[44:47], a[140:143], v[188:191], v[44:47]\n" \
"v_exp_f32_e64 v131, v131\n" \
"v_smfmac_f32_16x16x64_bf16 v[176:179], a[192:195], v[184:191], v228\n" \
"v_mfma_f32_16x16x32_bf16 v[136:139], a[96:99], a[16:19], v[164:167]\n" \
"v_exp_f32_e64 v132, v132\n" \
"v_mfma_f32_16x16x32_bf16 v[136:139], a[100:103], a[20:23], v[136:139]\n" \
"ds_read_b128 a[76:79], v210 offset:49152\n" \
"v_mfma_f32_16x16x32_bf16 v[136:139], a[104:107], a[24:27], v[136:139]\n" \
"v_exp_f32_e64 v133, v133\n" \
"v_mfma_f32_16x16x32_bf16 v[136:139], a[108:111], a[28:31], v[136:139]\n" \
"v_cvt_pk_bf16_f32 v192, v128, v129\n" \
"v_cvt_pk_bf16_f32 v193, v130, v131\n" \
"v_mfma_f32_16x16x32_bf16 v[140:143], a[124:127], a[28:31], v[164:167]\n" \
"v_exp_f32_e64 v134, v134\n" \
"v_mfma_f32_16x16x32_bf16 v[140:143], a[120:123], a[24:27], v[140:143]\n" \
"ds_read_b128 a[92:95], v210 offset:53248\n" \
"v_mfma_f32_16x16x32_bf16 v[140:143], a[116:119], a[20:23], v[140:143]\n" \
"buffer_load_dwordx4 v215, %[srdv], s48 offen offset:1024 lds\n" \
"v_mfma_f32_16x16x32_bf16 v[140:143], a[112:115], a[16:19], v[140:143]\n" \
"v_exp_f32_e64 v135, v135\n" \
"v_mfma_f32_16x16x32_bf16 v[48:51], a[176:179], v[196:199], v[48:51]\n" \
"ds_read_b128 a[88:91], v209 offset:53248\n" \
"v_mfma_f32_16x16x32_bf16 v[48:51], a[144:147], v[188:191], v[48:51]\n" \
"v_exp_f32_e64 v136, v136\n" \
"v_mfma_f32_16x16x32_bf16 v[52:55], a[180:183], v[196:199], v[52:55]\n" \
"v_cvt_pk_bf16_f32 v194, v132, v133\n" \
"v_cvt_pk_bf16_f32 v195, v134, v135\n" \
"v_mfma_f32_16x16x32_bf16 v[52:55], a[148:151], v[188:191], v[52:55]\n" \
"v_exp_f32_e64 v137, v137\n" \
"v_mfma_f32_16x16x32_bf16 v[56:59], a[184:187], v[196:199], v[56:59]\n" \
"v_exp_f32_e64 v138, v138\n" \
"v_mfma_f32_16x16x32_bf16 v[56:59], a[152:155], v[188:191], v[56:59]\n" \
"ds_read_b128 a[84:87], v208 offset:53248\n" \
"v_mfma_f32_16x16x32_bf16 v[60:63], a[188:191], v[196:199], v[60:63]\n" \
"v_exp_f32_e64 v139, v139\n" \
"v_mfma_f32_16x16x32_bf16 v[60:63], a[156:159], v[188:191], v[60:63]\n" \
"s_cmp_lg_u32 %[wid], 1\n" \
"s_cbranch_scc1 LtA5_%=\n" \
"global_load_dword v237, v224, s[58:59]\n" \
"s_add_u32 s61, s48, 49152\n" \
"s_mov_b32 exec_hi, 0\n" \
"s_nop 0\n" \
"buffer_load_dword v237, v224, %[srdp], s61 offen\n" \
"s_mov_b32 exec_hi, -1\n" \
"s_nop 0\n" \
"LtA5_%=:\n" \
"v_mfma_f32_16x16x32_bf16 v[144:147], a[96:99], a[32:35], v[168:171]\n" \
"v_exp_f32_e64 v140, v140\n" \
"v_mfma_f32_16x16x32_bf16 v[144:147], a[100:103], a[36:39], v[144:147]\n" \
"ds_read_b128 a[80:83], %[kb0] offset:53248\n" \
"v_mfma_f32_16x16x32_bf16 v[144:147], a[104:107], a[40:43], v[144:147]\n" \
"v_exp_f32_e64 v141, v141\n" \
"v_mfma_f32_16x16x32_bf16 v[144:147], a[108:111], a[44:47], v[144:147]\n" \
"v_cvt_pk_bf16_f32 v196, v136, v137\n" \
"v_cvt_pk_bf16_f32 v197, v138, v139\n" \
"v_mfma_f32_16x16x32_bf16 v[148:151], a[124:127], a[44:47], v[168:171]\n" \
"v_exp_f32_e64 v142, v142\n" \
"v_mfma_f32_16x16x32_bf16 v[148:151], a[120:123], a[40:43], v[148:151]\n" \
"v_mfma_f32_16x16x32_bf16 v[148:151], a[116:119], a[36:39], v[148:151]\n" \
"buffer_load_dwordx4 %[vd0], %[srdv], s48 offen offset:2048 lds\n" \
"v_mfma_f32_16x16x32_bf16 v[148:151], a[112:115], a[32:35], v[148:151]\n" \
"v_exp_f32_e64 v143, v143\n" \
"v_mfma_f32_16x16x32_bf16 v[96:99], a[160:163], v[204:207], v[96:99]\n" \
"ds_read_b64_tr_b16 a[160:161], %[vvb] offset:40960\n" \
"v_mfma_f32_16x16x32_bf16 v[96:99], a[128:131], v[220:223], v[96:99]\n" \
"v_exp_f32_e64 v144, v144\n" \
"v_mfma_f32_16x16x32_bf16 v[100:103], a[164:167], v[204:207], v[100:103]\n" \
"v_cvt_pk_bf16_f32 v198, v140, v141\n" \
"v_cvt_pk_bf16_f32 v199, v142, v143\n" \
"v_mfma_f32_16x16x32_bf16 v[100:103], a[132:135], v[220:223], v[100:103]\n" \
"v_exp_f32_e64 v145, v145\n" \
"v_mfma_f32_16x16x32_bf16 v[104:107], a[168:171], v[204:207], v[104:107]\n" \
"ds_read_b64_tr_b16 a[162:163], %[vvb] offset:45056\n" \
"ds_read_b64_tr_b16 a[164:165], v211 offset:40960\n" \
"v_mfma_f32_16x16x32_bf16 v[104:107], a[136:139], v[220:223], v[104:107]\n" \
"v_exp_f32_e64 v146, v146\n" \
"v_mfma_f32_16x16x32_bf16 v[108:111], a[172:175], v[204:207], v[108:111]\n" \
"ds_read_b64_tr_b16 a[166:167], v211 offset:45056\n" \
"v_mfma_f32_16x16x32_bf16 v[108:111], a[140:143], v[220:223], v[108:111]\n" \
"v_exp_f32_e64 v147, v147\n" \
"v_smfmac_f32_16x16x64_bf16 v[180:183], a[192:195], v[216:223], v228\n" \
"ds_read_b64_tr_b16 a[168:169], %[vvb] offset:41472\n" \
"v_mfma_f32_16x16x32_bf16 v[152:155], a[96:99], a[48:51], v[172:175]\n" \
"v_exp_f32_e64 v148, v148\n" \
"v_mfma_f32_16x16x32_bf16 v[152:155], a[100:103], a[52:55], v[152:155]\n" \
"ds_read_b64_tr_b16 a[170:171], %[vvb] offset:45568\n" \
"v_mfma_f32_16x16x32_bf16 v[152:155], a[104:107], a[56:59], v[152:155]\n" \
"v_exp_f32_e64 v149, v149\n" \
"v_mfma_f32_16x16x32_bf16 v[152:155], a[108:111], a[60:63], v[152:155]\n" \
"v_cvt_pk_bf16_f32 v200, v144, v145\n" \
"v_cvt_pk_bf16_f32 v201, v146, v147\n" \
"v_mfma_f32_16x16x32_bf16 v[156:159], a[124:127], a[60:63], v[172:175]\n" \
"v_exp_f32_e64 v150, v150\n" \
"v_mfma_f32_16x16x32_bf16 v[156:159], a[120:123], a[56:59], v[156:159]\n" \
"ds_read_b64_tr_b16 a[172:173], v211 offset:41472\n" \
"v_mfma_f32_16x16x32_bf16 v[156:159], a[116:119], a[52:55], v[156:159]\n" \
"buffer_load_dwordx4 v215, %[srdv], s48 offen offset:3072 lds\n" \
"v_mfma_f32_16x16x32_bf16 v[156:159], a[112:115], a[48:51], v[156:159]\n" \
"v_exp_f32_e64 v151, v151\n" \
"v_mfma_f32_16x16x32_bf16 v[112:115], a[176:179], v[204:207], v[112:115]\n" \
"ds_read_b64_tr_b16 a[174:175], v211 offset:45568\n" \
"ds_read_b64_tr_b16 a[176:177], %[vvb] offset:41984\n" \
"v_mfma_f32_16x16x32_bf16 v[112:115], a[144:147], v[220:223], v[112:115]\n" \
"v_exp_f32_e64 v152, v152\n" \
"v_mfma_f32_16x16x32_bf16 v[116:119], a[180:183], v[204:207], v[116:119]\n" \
"v_cvt_pk_bf16_f32 v202, v148, v149\n" \
".if . - adaln_body != 16384\n" \
".error \"size model\"\n" \
".endif\n" \
"Lk3_%=:\n" \
"s_cbranch_execz Lk4_%=\n" \
"s_nop 0\n" \
"v_cvt_pk_bf16_f32 v203, v150, v151\n" \
"v_mfma_f32_16x16x32_bf16 v[116:119], a[148:151], v[220:223], v[116:119]\n" \
"v_exp_f32_e64 v153, v153\n" \
"v_mfma_f32_16x16x32_bf16 v[120:123], a[184:187], v[204:207], v[120:123]\n" \
"v_exp_f32_e64 v154, v154\n" \
"v_mfma_f32_16x16x32_bf16 v[120:123], a[152:155], v[220:223], v[120:123]\n" \
"ds_read_b64_tr_b16 a[178:179], %[vvb] offset:46080\n" \
"v_mfma_f32_16x16x32_bf16 v[124:127], a[188:191], v[204:207], v[124:127]\n" \
"v_exp_f32_e64 v155, v155\n" \
"v_mfma_f32_16x16x32_bf16 v[124:127], a[156:159], v[220:223], v[124:127]\n" \
"ds_read_b64_tr_b16 a[180:181], v211 offset:41984\n" \
"s_waitcnt lgkmcnt(13)\n" \
"v_mfma_f32_16x16x32_bf16 v[128:131], a[64:67], a[0:3], v[160:163]\n" \
"v_exp_f32_e32 v156, v156\n" \
"v_mfma_f32_16x16x32_bf16 v[128:131], a[68:71], a[4:7], v[128:131]\n" \
"ds_read_b64_tr_b16 a[182:183], v211 offset:46080\n" \
"s_add_u32 m0, %[m0b], 32768\n" \
"v_mfma_f32_16x16x32_bf16 v[128:131], a[72:75], a[8:11], v[128:131]\n" \
"v_exp_f32_e64 v157, v157\n" \
"v_mfma_f32_16x16x32_bf16 v[128:131], a[76:79], a[12:15], v[128:131]\n" \
"v_cvt_pk_bf16_f32 v204, v152, v153\n" \
"v_cvt_pk_bf16_f32 v205, v154, v155\n" \
"v_mfma_f32_16x16x32_bf16 v[132:135], a[92:95], a[12:15], v[160:163]\n" \
"v_exp_f32_e64 v158, v158\n" \
"v_mfma_f32_16x16x32_bf16 v[132:135], a[88:91], a[8:11], v[132:135]\n" \
"ds_read_b64_tr_b16 a[184:185], %[vvb] offset:42496\n" \
"s_waitcnt lgkmcnt(5)\n" \
"v_mfma_f32_16x16x32_bf16 v[132:135], a[84:87], a[4:7], v[132:135]\n" \
"buffer_load_dwordx4 %[kd0], %[srdk], s48 offen offset:0 lds\n" \
"v_mfma_f32_16x16x32_bf16 v[132:135], a[80:83], a[0:3], v[132:135]\n" \
"v_exp_f32_e32 v159, v159\n" \
"v_mfma_f32_16x16x32_bf16 v[64:67], a[128:131], v[216:219], v[64:67]\n" \
"ds_read_b64_tr_b16 a[186:187], %[vvb] offset:46592\n" \
"ds_read_b64_tr_b16 a[188:189], v211 offset:42496\n" \
"v_mfma_f32_16x16x32_bf16 v[64:67], a[160:163], v[200:203], v[64:67]\n" \
"v_exp_f32_e64 v128, v128\n" \
"v_mfma_f32_16x16x32_bf16 v[68:71], a[132:135], v[216:219], v[68:71]\n" \
"v_cvt_pk_bf16_f32 v206, v156, v157\n" \
"v_cvt_pk_bf16_f32 v207, v158, v159\n" \
"v_mfma_f32_16x16x32_bf16 v[68:71], a[164:167], v[200:203], v[68:71]\n" \
"v_exp_f32_e64 v129, v129\n" \
"v_mfma_f32_16x16x32_bf16 v[72:75], a[136:139], v[216:219], v[72:75]\n" \
"ds_read_b64_tr_b16 a[190:191], v211 offset:46592\n" \
"ds_read_b64_tr_b16 a[128:129], %[vvb] offset:49152\n" \
"v_mfma_f32_16x16x32_bf16 v[72:75], a[168:171], v[200:203], v[72:75]\n" \
"v_exp_f32_e64 v130, v130\n" \
"v_mfma_f32_16x16x32_bf16 v[76:79], a[140:143], v[216:219], v[76:79]\n" \
"ds_read_b64_tr_b16 a[130:131], %[vvb] offset:53248\n" \
"ds_read_b64_tr_b16 a[132:133], v211 offset:49152\n" \
"v_mfma_f32_16x16x32_bf16 v[76:79], a[172:175], v[200:203], v[76:79]\n" \
"v_exp_f32_e64 v131, v131\n" \
"v_smfmac_f32_16x16x64_bf16 v[176:179], a[192:195], v[192:199], v228\n" \
"ds_read_b64_tr_b16 a[134:135], v211 offset:53248\n" \
"ds_read_b64_tr_b16 a[136:137], %[vvb] offset:49664\n" \
"v_mfma_f32_16x16x32_bf16 v[136:139], a[64:67], a[16:19], v[164:167]\n" \
"v_exp_f32_e64 v132, v132\n" \
"v_mfma_f32_16x16x32_bf16 v[136:139], a[68:71], a[20:23], v[136:139]\n" \
"ds_read_b64_tr_b16 a[138:139], %[vvb] offset:53760\n" \
"ds_read_b64_tr_b16 a[140:141], v211 offset:49664\n" \
"v_mfma_f32_16x16x32_bf16 v[136:139], a[72:75], a[24:27], v[136:139]\n" \
"v_exp_f32_e64 v133, v133\n" \
"v_mfma_f32_16x16x32_bf16 v[136:139], a[76:79], a[28:31], v[136:139]\n" \
"v_cvt_pk_bf16_f32 v184, v128, v129\n" \
"v_cvt_pk_bf16_f32 v185, v130, v131\n" \
"v_mfma_f32_16x16x32_bf16 v[140:143], a[92:95], a[28:31], v[164:167]\n" \
"v_exp_f32_e64 v134, v134\n" \
"v_mfma_f32_16x16x32_bf16 v[140:143], a[88:91], a[24:27], v[140:143]\n" \
"ds_read_b64_tr_b16 a[142:143], v211 offset:53760\n" \
"s_waitcnt lgkmcnt(8)\n" \
"v_mfma_f32_16x16x32_bf16 v[140:143], a[84:87], a[20:23], v[140:143]\n" \
"buffer_load_dwordx4 v212, %[srdk], s48 offen offset:1024 lds\n" \
"v_mfma_f32_16x16x32_bf16 v[140:143], a[80:83], a[16:19], v[140:143]\n" \
"v_exp_f32_e32 v135, v135\n" \
"v_mfma_f32_16x16x32_bf16 v[80:83], a[144:147], v[216:219], v[80:83]\n" \
"ds_read_b64_tr_b16 a[144:145], %[vvb] offset:50176\n" \
"ds_read_b64_tr_b16 a[146:147], %[vvb] offset:54272\n" \
"v_mfma_f32_16x16x32_bf16 v[80:83], a[176:179], v[200:203], v[80:83]\n" \
"v_exp_f32_e64 v136, v136\n" \
"v_mfma_f32_16x16x32_bf16 v[84:87], a[148:151], v[216:219], v[84:87]\n" \
"v_cvt_pk_bf16_f32 v186, v132, v133\n" \
"v_cvt_pk_bf16_f32 v187, v134, v135\n" \
"v_mfma_f32_16x16x32_bf16 v[84:87], a[180:183], v[200:203], v[84:87]\n" \
"v_exp_f32_e64 v137, v137\n" \
"v_mfma_f32_16x16x32_bf16 v[88:91], a[152:155], v[216:219], v[88:91]\n" \
"v_exp_f32_e64 v138, v138\n" \
"v_mfma_f32_16x16x32_bf16 v[88:91], a[184:187], v[200:203], v[88:91]\n" \
"ds_read_b64_tr_b16 a[148:149], v211 offset:50176\n" \
"ds_read_b64_tr_b16 a[150:151], v211 offset:54272\n" \
"v_mfma_f32_16x16x32_bf16 v[92:95], a[156:159], v[216:219], v[92:95]\n" \
"v_exp_f32_e64 v139, v139\n" \
"v_mfma_f32_16x16x32_bf16 v[92:95], a[188:191], v[200:203], v[92:95]\n" \
"ds_read_b64_tr_b16 a[152:153], %[vvb] offset:50688\n" \
"global_load_dword v237, %[vct], %[pct]\n" \
"v_mfma_f32_16x16x32_bf16 v[144:147], a[64:67], a[32:35], v[168:171]\n" \
"v_exp_f32_e64 v140, v140\n" \
"v_mfma_f32_16x16x32_bf16 v[144:147], a[68:71], a[36:39], v[144:147]\n" \
"ds_read_b64_tr_b16 a[154:155], %[vvb] offset:54784\n" \
"ds_read_b64_tr_b16 a[156:157], v211 offset:50688\n" \
"v_mfma_f32_16x16x32_bf16 v[144:147], a[72:75], a[40:43], v[144:147]\n" \
"v_exp_f32_e64 v141, v141\n" \
"v_mfma_f32_16x16x32_bf16 v[144:147], a[76:79], a[44:47], v[144:147]\n" \
"v_cvt_pk_bf16_f32 v188, v136, v137\n" \
"v_cvt_pk_bf16_f32 v189, v138, v139\n" \
"v_mfma_f32_16x16x32_bf16 v[148:151], a[92:95], a[44:47], v[168:171]\n" \
"v_exp_f32_e64 v142, v142\n" \
"v_mfma_f32_16x16x32_bf16 v[148:151], a[88:91], a[40:43], v[148:151]\n" \
"ds_read_b64_tr_b16 a[158:159], v211 offset:54784\n" \
"s_waitcnt lgkmcnt(8)\n" \
"v_mfma_f32_16x16x32_bf16 v[148:151], a[84:87], a[36:39], v[148:151]\n" \
"buffer_load_dwordx4 v213, %[srdk], s48 offen offset:2048 lds\n" \
"v_mfma_f32_16x16x32_bf16 v[148:151], a[80:83], a[32:35], v[148:151]\n" \
"v_exp_f32_e32 v143, v143\n" \
"v_mfma_f32_16x16x32_bf16 v[0:3], a[160:163], v[192:195], v[0:3]\n" \
"ds_read_b128 a[96:99], %[kb0] offset:57344\n" \
"v_mfma_f32_16x16x32_bf16 v[0:3], a[128:131], v[184:187], v[0:3]\n" \
"v_exp_f32_e64 v144, v144\n" \
"v_mfma_f32_16x16x32_bf16 v[4:7], a[164:167], v[192:195], v[4:7]\n" \
"v_cvt_pk_bf16_f32 v190, v140, v141\n" \
"v_cvt_pk_bf16_f32 v191, v142, v143\n" \
"v_mfma_f32_16x16x32_bf16 v[4:7], a[132:135], v[184:187], v[4:7]\n" \
"v_exp_f32_e64 v145, v145\n" \
"v_mfma_f32_16x16x32_bf16 v[8:11], a[168:171], v[192:195], v[8:11]\n" \
"ds_read_b128 a[100:103], v208 offset:57344\n" \
"v_mfma_f32_16x16x32_bf16 v[8:11], a[136:139], v[184:187], v[8:11]\n" \
"v_exp_f32_e64 v146, v146\n" \
"v_mfma_f32_16x16x32_bf16 v[12:15], a[172:175], v[192:195], v[12:15]\n" \
"ds_read_b128 a[104:107], v209 offset:57344\n" \
"v_mfma_f32_16x16x32_bf16 v[12:15], a[140:143], v[184:187], v[12:15]\n" \
"v_exp_f32_e64 v147, v147\n" \
"v_smfmac_f32_16x16x64_bf16 v[180:183], a[192:195], v[200:207], v228\n" \
"ds_read_b128 a[108:111], v210 offset:57344\n" \
"v_mfma_f32_16x16x32_bf16 v[152:155], a[64:67], a[48:51], v[172:175]\n" \
"v_exp_f32_e64 v148, v148\n" \
"v_mfma_f32_16x16x32_bf16 v[152:155], a[68:71], a[52:55], v[152:155]\n" \
"ds_read_b128 a[124:127], v210 offset:61440\n" \
"v_mfma_f32_16x16x32_bf16 v[152:155], a[72:75], a[56:59], v[152:155]\n" \
"v_exp_f32_e64 v149, v149\n" \
"v_mfma_f32_16x16x32_bf16 v[152:155], a[76:79], a[60:63], v[152:155]\n" \
"v_cvt_pk_bf16_f32 v216, v144, v145\n" \
"v_cvt_pk_bf16_f32 v217, v146, v147\n" \
"v_mfma_f32_16x16x32_bf16 v[156:159], a[92:95], a[60:63], v[172:175]\n" \
"v_exp_f32_e64 v150, v150\n" \
"v_mfma_f32_16x16x32_bf16 v[156:159], a[88:91], a[56:59], v[156:159]\n" \
"ds_read_b128 a[120:123], v209 offset:61440\n" \
"s_waitcnt lgkmcnt(6)\n" \
"v_mfma_f32_16x16x32_bf16 v[156:159], a[84:87], a[52:55], v[156:159]\n" \
"buffer_load_dwordx4 v214, %[srdk], s48 offen offset:3072 lds\n" \
"v_mfma_f32_16x16x32_bf16 v[156:159], a[80:83], a[48:51], v[156:159]\n" \
"v_exp_f32_e32 v151, v151\n" \
"v_mfma_f32_16x16x32_bf16 v[16:19], a[176:179], v[192:195], v[16:19]\n" \
"ds_read_b128 a[116:119], v208 offset:61440\n" \
"v_mfma_f32_16x16x32_bf16 v[16:19], a[144:147], v[184:187], v[16:19]\n" \
"v_exp_f32_e64 v152, v152\n" \
"v_mfma_f32_16x16x32_bf16 v[20:23], a[180:183], v[192:195], v[20:23]\n" \
"v_cvt_pk_bf16_f32 v218, v148, v149\n" \
"v_cvt_pk_bf16_f32 v219, v150, v151\n" \
"v_mfma_f32_16x16x32_bf16 v[20:23], a[148:151], v[184:187], v[20:23]\n" \
"v_exp_f32_e64 v153, v153\n" \
"v_mfma_f32_16x16x32_bf16 v[24:27], a[184:187], v[192:195], v[24:27]\n" \
"v_exp_f32_e64 v154, v154\n" \
"v_mfma_f32_16x16x32_bf16 v[24:27], a[152:155], v[184:187], v[24:27]\n" \
"ds_read_b128 a[112:115], %[kb0] offset:61440\n" \
"s_add_u32 s48, s48, 0x4000\n" \
"s_mov_b32 m0, s44\n" \
"s_addk_i32 s44, 0x400\n" \
"buffer_load_dwordx4 v229, s[64:67], s45 offen offset:0 nt lds\n" \
"s_add_u32 s45, s45, 393216\n" \
"v_mfma_f32_16x16x32_bf16 v[28:31], a[188:191], v[192:195], v[28:31]\n" \
"v_exp_f32_e64 v155, v155\n" \
"v_mfma_f32_16x16x32_bf16 v[28:31], a[156:159], v[184:187], v[28:31]\n" \
"s_waitcnt vmcnt(15) lgkmcnt(2)\n" \
"s_barrier\n" \
"v_mfma_f32_16x16x32_bf16 v[128:131], a[96:99], a[0:3], v[160:163]\n" \
"v_exp_f32_e64 v156, v156\n" \
"v_mfma_f32_16x16x32_bf16 v[128:131], a[100:103], a[4:7], v[128:131]\n" \
"s_add_u32 m0, %[m0b], 98304\n" \
"v_mfma_f32_16x16x32_bf16 v[128:131], a[104:107], a[8:11], v[128:131]\n" \
"v_exp_f32_e64 v157, v157\n" \
"v_mfma_f32_16x16x32_bf16 v[128:131], a[108:111], a[12:15], v[128:131]\n" \
"v_cvt_pk_bf16_f32 v220, v152, v153\n" \
"v_cvt_pk_bf16_f32 v221, v154, v155\n" \
"v_mfma_f32_16x16x32_bf16 v[132:135], a[124:127], a[12:15], v[160:163]\n" \
"v_exp_f32_e64 v158, v158\n" \
"v_mfma_f32_16x16x32_bf16 v[132:135], a[120:123], a[8:11], v[132:135]\n" \
"s_waitcnt lgkmcnt(0)\n" \
"v_mfma_f32_16x16x32_bf16 v[132:135], a[116:119], a[4:7], v[132:135]\n" \
"buffer_load_dwordx4 %[vd0], %[srdv], s48 offen offset:0 lds\n" \
"v_mfma_f32_16x16x32_bf16 v[132:135], a[112:115], a[0:3], v[132:135]\n" \
"v_exp_f32_e32 v159, v159\n" \
"v_mfma_f32_16x16x32_bf16 v[32:35], a[160:163], v[196:199], v[32:35]\n" \
"ds_read_b128 a[64:67], %[kb0] offset:0\n" \
"v_mfma_f32_16x16x32_bf16 v[32:35], a[128:131], v[188:191], v[32:35]\n" \
"v_exp_f32_e64 v128, v128\n" \
"v_mfma_f32_16x16x32_bf16 v[36:39], a[164:167], v[196:199], v[36:39]\n" \
"v_cvt_pk_bf16_f32 v222, v156, v157\n" \
"v_cvt_pk_bf16_f32 v223, v158, v159\n" \
"v_mfma_f32_16x16x32_bf16 v[36:39], a[132:135], v[188:191], v[36:39]\n" \
"v_exp_f32_e64 v129, v129\n" \
"v_mfma_f32_16x16x32_bf16 v[40:43], a[168:171], v[196:199], v[40:43]\n" \
"ds_read_b128 a[68:71], v208 offset:0\n" \
"v_mfma_f32_16x16x32_bf16 v[40:43], a[136:139], v[188:191], v[40:43]\n" \
"v_exp_f32_e64 v130, v130\n" \
"v_mfma_f32_16x16x32_bf16 v[44:47], a[172:175], v[196:199], v[44:47]\n" \
"ds_read_b128 a[72:75], v209 offset:0\n" \
"v_mfma_f32_16x16x32_bf16 v[44:47], a[140:143], v[188:191], v[44:47]\n" \
"v_exp_f32_e64 v131, v131\n" \
"v_smfmac_f32_16x16x64_bf16 v[176:179], a[192:195], v[184:191], v228\n" \
"v_mfma_f32_16x16x32_bf16 v[136:139], a[96:99], a[16:19], v[164:167]\n" \
"v_exp_f32_e64 v132, v132\n" \
"v_mfma_f32_16x16x32_bf16 v[136:139], a[100:103], a[20:23], v[136:139]\n" \
"ds_read_b128 a[76:79], v210 offset:0\n" \
"v_mfma_f32_16x16x32_bf16 v[136:139], a[104:107], a[24:27], v[136:139]\n" \
"v_exp_f32_e64 v133, v133\n" \
"v_mfma_f32_16x16x32_bf16 v[136:139], a[108:111], a[28:31], v[136:139]\n" \
"v_cvt_pk_bf16_f32 v192, v128, v129\n" \
"v_cvt_pk_bf16_f32 v193, v130, v131\n" \
"v_mfma_f32_16x16x32_bf16 v[140:143], a[124:127], a[28:31], v[164:167]\n" \
"v_exp_f32_e64 v134, v134\n" \
"v_mfma_f32_16x16x32_bf16 v[140:143], a[120:123], a[24:27], v[140:143]\n" \
"ds_read_b128 a[92:95], v210 offset:4096\n" \
"v_mfma_f32_16x16x32_bf16 v[140:143], a[116:119], a[20:23], v[140:143]\n" \
"buffer_load_dwordx4 v215, %[srdv], s48 offen offset:1024 lds\n" \
"v_mfma_f32_16x16x32_bf16 v[140:143], a[112:115], a[16:19], v[140:143]\n" \
"v_exp_f32_e64 v135, v135\n" \
"v_mfma_f32_16x16x32_bf16 v[48:51], a[176:179], v[196:199], v[48:51]\n" \
"ds_read_b128 a[88:91], v209 offset:4096\n" \
"v_mfma_f32_16x16x32_bf16 v[48:51], a[144:147], v[188:191], v[48:51]\n" \
"v_exp_f32_e64 v136, v136\n" \
"v_mfma_f32_16x16x32_bf16 v[52:55], a[180:183], v[196:199], v[52:55]\n" \
"v_cvt_pk_bf16_f32 v194, v132, v133\n" \
"v_cvt_pk_bf16_f32 v195, v134, v135\n" \
"v_mfma_f32_16x16x32_bf16 v[52:55], a[148:151], v[188:191], v[52:55]\n" \
"v_exp_f32_e64 v137, v137\n" \
"v_mfma_f32_16x16x32_bf16 v[56:59], a[184:187], v[196:199], v[56:59]\n" \
"v_exp_f32_e64 v138, v138\n" \
"v_mfma_f32_16x16x32_bf16 v[56:59], a[152:155], v[188:191], v[56:59]\n" \
"ds_read_b128 a[84:87], v208 offset:4096\n" \
"v_mfma_f32_16x16x32_bf16 v[60:63], a[188:191], v[196:199], v[60:63]\n" \
"v_exp_f32_e64 v139, v139\n" \
"v_mfma_f32_16x16x32_bf16 v[60:63], a[156:159], v[188:191], v[60:63]\n" \
"s_cmp_lg_u32 %[wid], 2\n" \
"s_cbranch_scc1 LtA7_%=\n" \
"global_load_dword v237, v224, s[58:59]\n" \
"s_add_u32 s61, s48, 49152\n" \
"s_mov_b32 exec_hi, 0\n" \
"s_nop 0\n" \
"buffer_load_dword v237, v224, %[srdp], s61 offen\n" \
"s_mov_b32 exec_hi, -1\n" \
"s_nop 0\n" \
"LtA7_%=:\n" \
"v_mfma_f32_16x16x32_bf16 v[144:147], a[96:99], a[32:35], v[168:171]\n" \
"v_exp_f32_e64 v140, v140\n" \
"v_mfma_f32_16x16x32_bf16 v[144:147], a[100:103], a[36:39], v[144:147]\n" \
"ds_read_b128 a[80:83], %[kb0] offset:4096\n" \
"v_mfma_f32_16x16x32_bf16 v[144:147], a[104:107], a[40:43], v[144:147]\n" \
"v_exp_f32_e64 v141, v141\n" \
"v_mfma_f32_16x16x32_bf16 v[144:147], a[108:111], a[44:47], v[144:147]\n" \
"v_cvt_pk_bf16_f32 v196, v136, v137\n" \
"v_cvt_pk_bf16_f32 v197, v138, v139\n" \
"v_mfma_f32_16x16x32_bf16 v[148:151], a[124:127], a[44:47], v[168:171]\n" \
"v_exp_f32_e64 v142, v142\n" \
"v_mfma_f32_16x16x32_bf16 v[148:151], a[120:123], a[40:43], v[148:151]\n" \
"v_mfma_f32_16x16x32_bf16 v[148:151], a[116:119], a[36:39], v[148:151]\n" \
"buffer_load_dwordx4 %[vd0], %[srdv], s48 offen offset:2048 lds\n" \
"v_mfma_f32_16x16x32_bf16 v[148:151], a[112:115], a[32:35], v[148:151]\n" \
"v_exp_f32_e64 v143, v143\n" \
"v_mfma_f32_16x16x32_bf16 v[96:99], a[160:163], v[204:207], v[96:99]\n" \
"ds_read_b64_tr_b16 a[160:161], %[vvb] offset:57344\n" \
"v_mfma_f32_16x16x32_bf16 v[96:99], a[128:131], v[220:223], v[96:99]\n" \
"v_exp_f32_e64 v144, v144\n" \
"v_mfma_f32_16x16x32_bf16 v[100:103], a[164:167], v[204:207], v[100:103]\n" \
"v_cvt_pk_bf16_f32 v198, v140, v141\n" \
"v_cvt_pk_bf16_f32 v199, v142, v143\n" \
"v_mfma_f32_16x16x32_bf16 v[100:103], a[132:135], v[220:223], v[100:103]\n" \
"v_exp_f32_e64 v145, v145\n" \
"v_mfma_f32_16x16x32_bf16 v[104:107], a[168:171], v[204:207], v[104:107]\n" \
"ds_read_b64_tr_b16 a[162:163], %[vvb] offset:61440\n" \
"ds_read_b64_tr_b16 a[164:165], v211 offset:57344\n" \
"v_mfma_f32_16x16x32_bf16 v[104:107], a[136:139], v[220:223], v[104:107]\n" \
"v_exp_f32_e64 v146, v146\n" \
"v_mfma_f32_16x16x32_bf16 v[108:111], a[172:175], v[204:207], v[108:111]\n" \
"ds_read_b64_tr_b16 a[166:167], v211 offset:61440\n" \
"v_mfma_f32_16x16x32_bf16 v[108:111], a[140:143], v[220:223], v[108:111]\n" \
"v_exp_f32_e64 v147, v147\n" \
"v_smfmac_f32_16x16x64_bf16 v[180:183], a[192:195], v[216:223], v228\n" \
"ds_read_b64_tr_b16 a[168:169], %[vvb] offset:57856\n" \
"v_mfma_f32_16x16x32_bf16 v[152:155], a[96:99], a[48:51], v[172:175]\n" \
"v_exp_f32_e64 v148, v148\n" \
"v_mfma_f32_16x16x32_bf16 v[152:155], a[100:103], a[52:55], v[152:155]\n" \
"ds_read_b64_tr_b16 a[170:171], %[vvb] offset:61952\n" \
"v_mfma_f32_16x16x32_bf16 v[152:155], a[104:107], a[56:59], v[152:155]\n" \
"v_exp_f32_e64 v149, v149\n" \
"v_mfma_f32_16x16x32_bf16 v[152:155], a[108:111], a[60:63], v[152:155]\n" \
"v_cvt_pk_bf16_f32 v200, v144, v145\n" \
"v_cvt_pk_bf16_f32 v201, v146, v147\n" \
"v_mfma_f32_16x16x32_bf16 v[156:159], a[124:127], a[60:63], v[172:175]\n" \
"v_exp_f32_e64 v150, v150\n" \
"v_mfma_f32_16x16x32_bf16 v[156:159], a[120:123], a[56:59], v[156:159]\n" \
"ds_read_b64_tr_b16 a[172:173], v211 offset:57856\n" \
"v_mfma_f32_16x16x32_bf16 v[156:159], a[116:119], a[52:55], v[156:159]\n" \
"buffer_load_dwordx4 v215, %[srdv], s48 offen offset:3072 lds\n" \
"v_mfma_f32_16x16x32_bf16 v[156:159], a[112:115], a[48:51], v[156:159]\n" \
"v_exp_f32_e64 v151, v151\n" \
"v_mfma_f32_16x16x32_bf16 v[112:115], a[176:179], v[204:207], v[112:115]\n" \
"ds_read_b64_tr_b16 a[174:175], v211 offset:61952\n" \
"ds_read_b64_tr_b16 a[176:177], %[vvb] offset:58368\n" \
"v_mfma_f32_16x16x32_bf16 v[112:115], a[144:147], v[220:223], v[112:115]\n" \
"v_exp_f32_e64 v152, v152\n" \
"v_mfma_f32_16x16x32_bf16 v[116:119], a[180:183], v[204:207], v[116:119]\n" \
"v_cvt_pk_bf16_f32 v202, v148, v149\n" \
"v_cvt_pk_bf16_f32 v203, v150, v151\n" \
"v_mfma_f32_16x16x32_bf16 v[116:119], a[148:151], v[220:223], v[116:119]\n" \
"v_exp_f32_e64 v153, v153\n" \
"v_mfma_f32_16x16x32_bf16 v[120:123], a[184:187], v[204:207], v[120:123]\n" \
"v_exp_f32_e64 v154, v154\n" \
"v_mfma_f32_16x16x32_bf16 v[120:123], a[152:155], v[220:223], v[120:123]\n" \
"ds_read_b64_tr_b16 a[178:179], %[vvb] offset:62464\n" \
"v_mfma_f32_16x16x32_bf16 v[124:127], a[188:191], v[204:207], v[124:127]\n" \
"v_exp_f32_e64 v155, v155\n" \
"v_mfma_f32_16x16x32_bf16 v[124:127], a[156:159], v[220:223], v[124:127]\n" \
"ds_read_b64_tr_b16 a[180:181], v211 offset:58368\n" \
"s_waitcnt lgkmcnt(13)\n" \
"v_mfma_f32_16x16x32_bf16 v[128:131], a[64:67], a[0:3], v[160:163]\n" \
"v_exp_f32_e32 v156, v156\n" \
"v_mfma_f32_16x16x32_bf16 v[128:131], a[68:71], a[4:7], v[128:131]\n" \
"ds_read_b64_tr_b16 a[182:183], v211 offset:62464\n" \
"s_add_u32 m0, %[m0b], 49152\n" \
"v_mfma_f32_16x16x32_bf16 v[128:131], a[72:75], a[8:11], v[128:131]\n" \
"v_exp_f32_e64 v157, v157\n" \
"v_mfma_f32_16x16x32_bf16 v[128:131], a[76:79], a[12:15], v[128:131]\n" \
"v_cvt_pk_bf16_f32 v204, v152, v153\n" \
"v_cvt_pk_bf16_f32 v205, v154, v155\n" \
"v_mfma_f32_16x16x32_bf16 v[132:135], a[92:95], a[12:15], v[160:163]\n" \
"v_exp_f32_e64 v158, v158\n" \
"v_mfma_f32_16x16x32_bf16 v[132:135], a[88:91], a[8:11], v[132:135]\n" \
"ds_read_b64_tr_b16 a[184:185], %[vvb] offset:58880\n" \
"s_waitcnt lgkmcnt(5)\n" \
"v_mfma_f32_16x16x32_bf16 v[132:135], a[84:87], a[4:7], v[132:135]\n" \
"buffer_load_dwordx4 %[kd0], %[srdk], s48 offen offset:0 lds\n" \
"v_mfma_f32_16x16x32_bf16 v[132:135], a[80:83], a[0:3], v[132:135]\n" \
"v_exp_f32_e32 v159, v159\n" \
"v_mfma_f32_16x16x32_bf16 v[64:67], a[128:131], v[216:219], v[64:67]\n" \
"ds_read_b64_tr_b16 a[186:187], %[vvb] offset:62976\n" \
"ds_read_b64_tr_b16 a[188:189], v211 offset:58880\n" \
"v_mfma_f32_16x16x32_bf16 v[64:67], a[160:163], v[200:203], v[64:67]\n" \
"v_exp_f32_e64 v128, v128\n" \
"v_mfma_f32_16x16x32_bf16 v[68:71], a[132:135], v[216:219], v[68:71]\n" \
"v_cvt_pk_bf16_f32 v206, v156, v157\n" \
"v_cvt_pk_bf16_f32 v207, v158, v159\n" \
"v_mfma_f32_16x16x32_bf16 v[68:71], a[164:167], v[200:203], v[68:71]\n" \
"v_exp_f32_e64 v129, v129\n" \
"v_mfma_f32_16x16x32_bf16 v[72:75], a[136:139], v[216:219], v[72:75]\n" \
"ds_read_b64_tr_b16 a[190:191], v211 offset:62976\n" \
"ds_read_b64_tr_b16 a[128:129], %[vvb] offset:0\n" \
"v_mfma_f32_16x16x32_bf16 v[72:75], a[168:171], v[200:203], v[72:75]\n" \
"v_exp_f32_e64 v130, v130\n" \
"v_mfma_f32_16x16x32_bf16 v[76:79], a[140:143], v[216:219], v[76:79]\n" \
"ds_read_b64_tr_b16 a[130:131], %[vvb] offset:4096\n" \
"ds_read_b64_tr_b16 a[132:133], v211 offset:0\n" \
"v_mfma_f32_16x16x32_bf16 v[76:79], a[172:175], v[200:203], v[76:79]\n" \
"v_exp_f32_e64 v131, v131\n" \
"v_smfmac_f32_16x16x64_bf16 v[176:179], a[192:195], v[192:199], v228\n" \
"ds_read_b64_tr_b16 a[134:135], v211 offset:4096\n" \
"ds_read_b64_tr_b16 a[136:137], %[vvb] offset:512\n" \
"v_mfma_f32_16x16x32_bf16 v[136:139], a[64:67], a[16:19], v[164:167]\n" \
"v_exp_f32_e64 v132, v132\n" \
"v_mfma_f32_16x16x32_bf16 v[136:139], a[68:71], a[20:23], v[136:139]\n" \
"ds_read_b64_tr_b16 a[138:139], %[vvb] offset:4608\n" \
"ds_read_b64_tr_b16 a[140:141], v211 offset:512\n" \
"v_mfma_f32_16x16x32_bf16 v[136:139], a[72:75], a[24:27], v[136:139]\n" \
"v_exp_f32_e64 v133, v133\n" \
"v_mfma_f32_16x16x32_bf16 v[136:139], a[76:79], a[28:31], v[136:139]\n" \
"v_cvt_pk_bf16_f32 v184, v128, v129\n" \
"v_cvt_pk_bf16_f32 v185, v130, v131\n" \
"v_mfma_f32_16x16x32_bf16 v[140:143], a[92:95], a[28:31], v[164:167]\n" \
"v_exp_f32_e64 v134, v134\n" \
"v_mfma_f32_16x16x32_bf16 v[140:143], a[88:91], a[24:27], v[140:143]\n" \
"ds_read_b64_tr_b16 a[142:143], v211 offset:4608\n" \
"s_waitcnt lgkmcnt(8)\n" \
"v_mfma_f32_16x16x32_bf16 v[140:143], a[84:87], a[20:23], v[140:143]\n" \
"buffer_load_dwordx4 v212, %[srdk], s48 offen offset:1024 lds\n" \
"v_mfma_f32_16x16x32_bf16 v[140:143], a[80:83], a[16:19], v[140:143]\n" \
"v_exp_f32_e32 v135, v135\n" \
"v_mfma_f32_16x16x32_bf16 v[80:83], a[144:147], v[216:219], v[80:83]\n" \
"ds_read_b64_tr_b16 a[144:145], %[vvb] offset:1024\n" \
"ds_read_b64_tr_b16 a[146:147], %[vvb] offset:5120\n" \
"v_mfma_f32_16x16x32_bf16 v[80:83], a[176:179], v[200:203], v[80:83]\n" \
"v_exp_f32_e64 v136, v136\n" \
"v_mfma_f32_16x16x32_bf16 v[84:87], a[148:151], v[216:219], v[84:87]\n" \
"v_cvt_pk_bf16_f32 v186, v132, v133\n" \
"v_cvt_pk_bf16_f32 v187, v134, v135\n" \
"v_mfma_f32_16x16x32_bf16 v[84:87], a[180:183], v[200:203], v[84:87]\n" \
"v_exp_f32_e64 v137, v137\n" \
"v_mfma_f32_16x16x32_bf16 v[88:91], a[152:155], v[216:219], v[88:91]\n" \
"v_exp_f32_e64 v138, v138\n" \
"v_mfma_f32_16x16x32_bf16 v[88:91], a[184:187], v[200:203], v[88:91]\n" \
"ds_read_b64_tr_b16 a[148:149], v211 offset:1024\n" \
"ds_read_b64_tr_b16 a[150:151], v211 offset:5120\n" \
"v_mfma_f32_16x16x32_bf16 v[92:95], a[156:159], v[216:219], v[92:95]\n" \
"v_exp_f32_e64 v139, v139\n" \
"v_mfma_f32_16x16x32_bf16 v[92:95], a[188:191], v[200:203], v[92:95]\n" \
"ds_read_b64_tr_b16 a[152:153], %[vvb] offset:1536\n" \
"buffer_store_dwordx4 a[196:199], v235, %[srdz], s62 offen offset:1024 sc1\n" \
"s_add_u32 s62, s62, 2048\n" \
"v_mfma_f32_16x16x32_bf16 v[144:147], a[64:67], a[32:35], v[168:171]\n" \
"v_exp_f32_e64 v140, v140\n" \
"v_mfma_f32_16x16x32_bf16 v[144:147], a[68:71], a[36:39], v[144:147]\n" \
"ds_read_b64_tr_b16 a[154:155], %[vvb] offset:5632\n" \
"ds_read_b64_tr_b16 a[156:157], v211 offset:1536\n" \
"v_mfma_f32_16x16x32_bf16 v[144:147], a[72:75], a[40:43], v[144:147]\n" \
"v_exp_f32_e64 v141, v141\n" \
"v_mfma_f32_16x16x32_bf16 v[144:147], a[76:79], a[44:47], v[144:147]\n" \
"v_cvt_pk_bf16_f32 v188, v136, v137\n" \
"v_cvt_pk_bf16_f32 v189, v138, v139\n" \
"v_mfma_f32_16x16x32_bf16 v[148:151], a[92:95], a[44:47], v[168:171]\n" \
"v_exp_f32_e64 v142, v142\n" \
"v_mfma_f32_16x16x32_bf16 v[148:151], a[88:91], a[40:43], v[148:151]\n" \
"ds_read_b64_tr_b16 a[158:159], v211 offset:5632\n" \
"s_waitcnt lgkmcnt(8)\n" \
"v_mfma_f32_16x16x32_bf16 v[148:151], a[84:87], a[36:39], v[148:151]\n" \
"buffer_load_dwordx4 v213, %[srdk], s48 offen offset:2048 lds\n" \
"v_mfma_f32_16x16x32_bf16 v[148:151], a[80:83], a[32:35], v[148:151]\n" \
"v_exp_f32_e32 v143, v143\n" \
"v_mfma_f32_16x16x32_bf16 v[0:3], a[160:163], v[192:195], v[0:3]\n" \
"ds_read_b128 a[96:99], %[kb0] offset:8192\n" \
"v_mfma_f32_16x16x32_bf16 v[0:3], a[128:131], v[184:187], v[0:3]\n" \
"v_exp_f32_e64 v144, v144\n" \
"v_mfma_f32_16x16x32_bf16 v[4:7], a[164:167], v[192:195], v[4:7]\n" \
"v_cvt_pk_bf16_f32 v190, v140, v141\n" \
"v_cvt_pk_bf16_f32 v191, v142, v143\n" \
"v_mfma_f32_16x16x32_bf16 v[4:7], a[132:135], v[184:187], v[4:7]\n" \
"v_exp_f32_e64 v145, v145\n" \
"v_mfma_f32_16x16x32_bf16 v[8:11], a[168:171], v[192:195], v[8:11]\n" \
"ds_read_b128 a[100:103], v208 offset:8192\n" \
"v_mfma_f32_16x16x32_bf16 v[8:11], a[136:139], v[184:187], v[8:11]\n" \
"v_exp_f32_e64 v146, v146\n" \
"v_mfma_f32_16x16x32_bf16 v[12:15], a[172:175], v[192:195], v[12:15]\n" \
"ds_read_b128 a[104:107], v209 offset:8192\n" \
"v_mfma_f32_16x16x32_bf16 v[12:15], a[140:143], v[184:187], v[12:15]\n" \
"v_exp_f32_e64 v147, v147\n" \
"v_smfmac_f32_16x16x64_bf16 v[180:183], a[192:195], v[200:207], v228\n" \
"ds_read_b128 a[108:111], v210 offset:8192\n" \
"v_mfma_f32_16x16x32_bf16 v[152:155], a[64:67], a[48:51], v[172:175]\n" \
"v_exp_f32_e64 v148, v148\n" \
"v_mfma_f32_16x16x32_bf16 v[152:155], a[68:71], a[52:55], v[152:155]\n" \
"ds_read_b128 a[124:127], v210 offset:12288\n" \
"v_mfma_f32_16x16x32_bf16 v[152:155], a[72:75], a[56:59], v[152:155]\n" \
"v_exp_f32_e64 v149, v149\n" \
"v_mfma_f32_16x16x32_bf16 v[152:155], a[76:79], a[60:63], v[152:155]\n" \
"v_cvt_pk_bf16_f32 v216, v144, v145\n" \
"v_cvt_pk_bf16_f32 v217, v146, v147\n" \
"v_mfma_f32_16x16x32_bf16 v[156:159], a[92:95], a[60:63], v[172:175]\n" \
"v_exp_f32_e64 v150, v150\n" \
"v_mfma_f32_16x16x32_bf16 v[156:159], a[88:91], a[56:59], v[156:159]\n" \
"ds_read_b128 a[120:123], v209 offset:12288\n" \
"s_waitcnt lgkmcnt(6)\n" \
"v_mfma_f32_16x16x32_bf16 v[156:159], a[84:87], a[52:55], v[156:159]\n" \
"buffer_load_dwordx4 v214, %[srdk], s48 offen offset:3072 lds\n" \
"v_mfma_f32_16x16x32_bf16 v[156:159], a[80:83], a[48:51], v[156:159]\n" \
"v_exp_f32_e32 v151, v151\n" \
"v_mfma_f32_16x16x32_bf16 v[16:19], a[176:179], v[192:195], v[16:19]\n" \
"ds_read_b128 a[116:119], v208 offset:12288\n" \
"v_mfma_f32_16x16x32_bf16 v[16:19], a[144:147], v[184:187], v[16:19]\n" \
"v_exp_f32_e64 v152, v152\n" \
"v_mfma_f32_16x16x32_bf16 v[20:23], a[180:183], v[192:195], v[20:23]\n" \
"v_cvt_pk_bf16_f32 v218, v148, v149\n" \
"v_cvt_pk_bf16_f32 v219, v150, v151\n" \
"v_mfma_f32_16x16x32_bf16 v[20:23], a[148:151], v[184:187], v[20:23]\n" \
"v_exp_f32_e64 v153, v153\n" \
"v_mfma_f32_16x16x32_bf16 v[24:27], a[184:187], v[192:195], v[24:27]\n" \
"v_exp_f32_e64 v154, v154\n" \
"v_mfma_f32_16x16x32_bf16 v[24:27], a[152:155], v[184:187], v[24:27]\n" \
"ds_read_b128 a[112:115], %[kb0] offset:12288\n" \
"s_add_u32 s48, s48, 0x4000\n" \
"v_mfma_f32_16x16x32_bf16 v[28:31], a[188:191], v[192:195], v[28:31]\n" \
"v_exp_f32_e64 v155, v155\n" \
"v_mfma_f32_16x16x32_bf16 v[28:31], a[156:159], v[184:187], v[28:31]\n" \
"s_waitcnt vmcnt(15) lgkmcnt(2)\n" \
"s_barrier\n" \
"v_mfma_f32_16x16x32_bf16 v[128:131], a[96:99], a[0:3], v[160:163]\n" \
"v_exp_f32_e64 v156, v156\n" \
"v_mfma_f32_16x16x32_bf16 v[128:131], a[100:103], a[4:7], v[128:131]\n" \
"s_add_u32 m0, %[m0b], 114688\n" \
"v_mfma_f32_16x16x32_bf16 v[128:131], a[104:107], a[8:11], v[128:131]\n" \
"v_exp_f32_e64 v157, v157\n" \
"v_mfma_f32_16x16x32_bf16 v[128:131], a[108:111], a[12:15], v[128:131]\n" \
"v_cvt_pk_bf16_f32 v220, v152, v153\n" \
"v_cvt_pk_bf16_f32 v221, v154, v155\n" \
"v_mfma_f32_16x16x32_bf16 v[132:135], a[124:127], a[12:15], v[160:163]\n" \
"v_exp_f32_e64 v158, v158\n" \
"v_mfma_f32_16x16x32_bf16 v[132:135], a[120:123], a[8:11], v[132:135]\n" \
"s_waitcnt lgkmcnt(0)\n" \
"v_mfma_f32_16x16x32_bf16 v[132:135], a[116:119], a[4:7], v[132:135]\n" \
"buffer_load_dwordx4 %[vd0], %[srdv], s48 offen offset:0 lds\n" \
"v_mfma_f32_16x16x32_bf16 v[132:135], a[112:115], a[0:3], v[132:135]\n" \
"v_exp_f32_e32 v159, v159\n" \
"v_mfma_f32_16x16x32_bf16 v[32:35], a[160:163], v[196:199], v[32:35]\n" \
"ds_read_b128 a[64:67], %[kb0] offset:16384\n" \
"v_mfma_f32_16x16x32_bf16 v[32:35], a[128:131], v[188:191], v[32:35]\n" \
"v_exp_f32_e64 v128, v128\n" \
"v_mfma_f32_16x16x32_bf16 v[36:39], a[164:167], v[196:199], v[36:39]\n" \
"v_cvt_pk_bf16_f32 v222, v156, v157\n" \
"v_cvt_pk_bf16_f32 v223, v158, v159\n" \
"v_mfma_f32_16x16x32_bf16 v[36:39], a[132:135], v[188:191], v[36:39]\n" \
"v_exp_f32_e64 v129, v129\n" \
"v_mfma_f32_16x16x32_bf16 v[40:43], a[168:171], v[196:199], v[40:43]\n" \
"ds_read_b128 a[68:71], v208 offset:16384\n" \
"v_mfma_f32_16x16x32_bf16 v[40:43], a[136:139], v[188:191], v[40:43]\n" \
"v_exp_f32_e64 v130, v130\n" \
"v_mfma_f32_16x16x32_bf16 v[44:47], a[172:175], v[196:199], v[44:47]\n" \
"ds_read_b128 a[72:75], v209 offset:16384\n" \
"v_mfma_f32_16x16x32_bf16 v[44:47], a[140:143], v[188:191], v[44:47]\n" \
"v_exp_f32_e64 v131, v131\n" \
"v_smfmac_f32_16x16x64_bf16 v[176:179], a[192:195], v[184:191], v228\n" \
"v_mfma_f32_16x16x32_bf16 v[136:139], a[96:99], a[16:19], v[164:167]\n" \
"v_exp_f32_e64 v132, v132\n" \
"v_mfma_f32_16x16x32_bf16 v[136:139], a[100:103], a[20:23], v[136:139]\n" \
"ds_read_b128 a[76:79], v210 offset:16384\n" \
"v_mfma_f32_16x16x32_bf16 v[136:139], a[104:107], a[24:27], v[136:139]\n" \
"v_exp_f32_e64 v133, v133\n" \
"v_mfma_f32_16x16x32_bf16 v[136:139], a[108:111], a[28:31], v[136:139]\n" \
"v_cvt_pk_bf16_f32 v192, v128, v129\n" \
"v_cvt_pk_bf16_f32 v193, v130, v131\n" \
"v_mfma_f32_16x16x32_bf16 v[140:143], a[124:127], a[28:31], v[164:167]\n" \
"v_exp_f32_e64 v134, v134\n" \
"v_mfma_f32_16x16x32_bf16 v[140:143], a[120:123], a[24:27], v[140:143]\n" \
"ds_read_b128 a[92:95], v210 offset:20480\n" \
"v_mfma_f32_16x16x32_bf16 v[140:143], a[116:119], a[20:23], v[140:143]\n" \
"buffer_load_dwordx4 v215, %[srdv], s48 offen offset:1024 lds\n" \
"v_mfma_f32_16x16x32_bf16 v[140:143], a[112:115], a[16:19], v[140:143]\n" \
".if . - adaln_body != 20480\n" \
".error \"size model\"\n" \
".endif\n" \
"Lk4_%=:\n" \
"s_cbranch_execz Lk5_%=\n" \
"s_nop 0\n" \
"v_exp_f32_e64 v135, v135\n" \
"v_mfma_f32_16x16x32_bf16 v[48:51], a[176:179], v[196:199], v[48:51]\n" \
"ds_read_b128 a[88:91], v209 offset:20480\n" \
"v_mfma_f32_16x16x32_bf16 v[48:51], a[144:147], v[188:191], v[48:51]\n" \
"v_exp_f32_e64 v136, v136\n" \
"v_mfma_f32_16x16x32_bf16 v[52:55], a[180:183], v[196:199], v[52:55]\n" \
"v_cvt_pk_bf16_f32 v194, v132, v133\n" \
"v_cvt_pk_bf16_f32 v195, v134, v135\n" \
"v_mfma_f32_16x16x32_bf16 v[52:55], a[148:151], v[188:191], v[52:55]\n" \
"v_exp_f32_e64 v137, v137\n" \
"v_mfma_f32_16x16x32_bf16 v[56:59], a[184:187], v[196:199], v[56:59]\n" \
"v_exp_f32_e64 v138, v138\n" \
"v_mfma_f32_16x16x32_bf16 v[56:59], a[152:155], v[188:191], v[56:59]\n" \
"ds_read_b128 a[84:87], v208 offset:20480\n" \
"v_mfma_f32_16x16x32_bf16 v[60:63], a[188:191], v[196:199], v[60:63]\n" \
"v_exp_f32_e64 v139, v139\n" \
"v_mfma_f32_16x16x32_bf16 v[60:63], a[156:159], v[188:191], v[60:63]\n" \
"s_cmp_lg_u32 %[wid], 3\n" \
"s_cbranch_scc1 LtA1_%=\n" \
"global_load_dword v237, v224, s[58:59]\n" \
"s_add_u32 s61, s48, 49152\n" \
"s_mov_b32 exec_hi, 0\n" \
"s_nop 0\n" \
"buffer_load_dword v237, v224, %[srdp], s61 offen\n" \
"s_mov_b32 exec_hi, -1\n" \
"s_nop 0\n" \
"LtA1_%=:\n" \
"v_mfma_f32_16x16x32_bf16 v[144:147], a[96:99], a[32:35], v[168:171]\n" \
"v_exp_f32_e64 v140, v140\n" \
"v_mfma_f32_16x16x32_bf16 v[144:147], a[100:103], a[36:39], v[144:147]\n" \
"ds_read_b128 a[80:83], %[kb0] offset:20480\n" \
"v_mfma_f32_16x16x32_bf16 v[144:147], a[104:107], a[40:43], v[144:147]\n" \
"v_exp_f32_e64 v141, v141\n" \
"v_mfma_f32_16x16x32_bf16 v[144:147], a[108:111], a[44:47], v[144:147]\n" \
"v_cvt_pk_bf16_f32 v196, v136, v137\n" \
"v_cvt_pk_bf16_f32 v197, v138, v139\n" \
"v_mfma_f32_16x16x32_bf16 v[148:151], a[124:127], a[44:47], v[168:171]\n" \
"v_exp_f32_e64 v142, v142\n" \
"v_mfma_f32_16x16x32_bf16 v[148:151], a[120:123], a[40:43], v[148:151]\n" \
"v_mfma_f32_16x16x32_bf16 v[148:151], a[116:119], a[36:39], v[148:151]\n" \
"buffer_load_dwordx4 %[vd0], %[srdv], s48 offen offset:2048 lds\n" \
"v_mfma_f32_16x16x32_bf16 v[148:151], a[112:115], a[32:35], v[148:151]\n" \
"v_exp_f32_e64 v143, v143\n" \
"v_mfma_f32_16x16x32_bf16 v[96:99], a[160:163], v[204:207], v[96:99]\n" \
"ds_read_b64_tr_b16 a[160:161], %[vvb] offset:8192\n" \
"v_mfma_f32_16x16x32_bf16 v[96:99], a[128:131], v[220:223], v[96:99]\n" \
"v_exp_f32_e64 v144, v144\n" \
"v_mfma_f32_16x16x32_bf16 v[100:103], a[164:167], v[204:207], v[100:103]\n" \
"v_cvt_pk_bf16_f32 v198, v140, v141\n" \
"v_cvt_pk_bf16_f32 v199, v142, v143\n" \
"v_mfma_f32_16x16x32_bf16 v[100:103], a[132:135], v[220:223], v[100:103]\n" \
"v_exp_f32_e64 v145, v145\n" \
"v_mfma_f32_16x16x32_bf16 v[104:107], a[168:171], v[204:207], v[104:107]\n" \
"ds_read_b64_tr_b16 a[162:163], %[vvb] offset:12288\n" \
"ds_read_b64_tr_b16 a[164:165], v211 offset:8192\n" \
"v_mfma_f32_16x16x32_bf16 v[104:107], a[136:139], v[220:223], v[104:107]\n" \
"v_exp_f32_e64 v146, v146\n" \
"v_mfma_f32_16x16x32_bf16 v[108:111], a[172:175], v[204:207], v[108:111]\n" \
"ds_read_b64_tr_b16 a[166:167], v211 offset:12288\n" \
"v_mfma_f32_16x16x32_bf16 v[108:111], a[140:143], v[220:223], v[108:111]\n" \
"v_exp_f32_e64 v147, v147\n" \
"v_smfmac_f32_16x16x64_bf16 v[180:183], a[192:195], v[216:223], v228\n" \
"ds_read_b64_tr_b16 a[168:169], %[vvb] offset:8704\n" \
"v_mfma_f32_16x16x32_bf16 v[152:155], a[96:99], a[48:51], v[172:175]\n" \
"v_exp_f32_e64 v148, v148\n" \
"v_mfma_f32_16x16x32_bf16 v[152:155], a[100:103], a[52:55], v[152:155]\n" \
"ds_read_b64_tr_b16 a[170:171], %[vvb] offset:12800\n" \
"v_mfma_f32_16x16x32_bf16 v[152:155], a[104:107], a[56:59], v[152:155]\n" \
"v_exp_f32_e64 v149, v149\n" \
"v_mfma_f32_16x16x32_bf16 v[152:155], a[108:111], a[60:63], v[152:155]\n" \
"v_cvt_pk_bf16_f32 v200, v144, v145\n" \
"v_cvt_pk_bf16_f32 v201, v146, v147\n" \
"v_mfma_f32_16x16x32_bf16 v[156:159], a[124:127], a[60:63], v[172:175]\n" \
"v_exp_f32_e64 v150, v150\n" \
"v_mfma_f32_16x16x32_bf16 v[156:159], a[120:123], a[56:59], v[156:159]\n" \
"ds_read_b64_tr_b16 a[172:173], v211 offset:8704\n" \
"v_mfma_f32_16x16x32_bf16 v[156:159], a[116:119], a[52:55], v[156:159]\n" \
"buffer_load_dwordx4 v215, %[srdv], s48 offen offset:3072 lds\n" \
"v_mfma_f32_16x16x32_bf16 v[156:159], a[112:115], a[48:51], v[156:159]\n" \
"v_exp_f32_e64 v151, v151\n" \
"v_mfma_f32_16x16x32_bf16 v[112:115], a[176:179], v[204:207], v[112:115]\n" \
"ds_read_b64_tr_b16 a[174:175], v211 offset:12800\n" \
"ds_read_b64_tr_b16 a[176:177], %[vvb] offset:9216\n" \
"v_mfma_f32_16x16x32_bf16 v[112:115], a[144:147], v[220:223], v[112:115]\n" \
"v_exp_f32_e64 v152, v152\n" \
"v_mfma_f32_16x16x32_bf16 v[116:119], a[180:183], v[204:207], v[116:119]\n" \
"v_cvt_pk_bf16_f32 v202, v148, v149\n" \
"v_cvt_pk_bf16_f32 v203, v150, v151\n" \
"v_mfma_f32_16x16x32_bf16 v[116:119], a[148:151], v[220:223], v[116:119]\n" \
"v_exp_f32_e64 v153, v153\n" \
"v_mfma_f32_16x16x32_bf16 v[120:123], a[184:187], v[204:207], v[120:123]\n" \
"v_exp_f32_e64 v154, v154\n" \
"v_mfma_f32_16x16x32_bf16 v[120:123], a[152:155], v[220:223], v[120:123]\n" \
"ds_read_b64_tr_b16 a[178:179], %[vvb] offset:13312\n" \
"v_mfma_f32_16x16x32_bf16 v[124:127], a[188:191], v[204:207], v[124:127]\n" \
"v_exp_f32_e64 v155, v155\n" \
"v_mfma_f32_16x16x32_bf16 v[124:127], a[156:159], v[220:223], v[124:127]\n" \
"ds_read_b64_tr_b16 a[180:181], v211 offset:9216\n" \
"s_waitcnt lgkmcnt(13)\n" \
"s_cmp_lg_u32 s60, 4\n" \
"s_cbranch_scc1 LmA_%=\n" \
"s_nop 0\n" \
"v_readlane_b32 s64, %[tlo], 9\n" \
"v_readlane_b32 s65, %[thi], 9\n" \
"s_mov_b32 s66, %[wbytes]\n" \
"v_mov_b32_e32 v229, %[vg0]\n" \
"s_mov_b32 s44, %[m0t]\n" \
"s_mov_b32 s45, 0\n" \
"s_cmp_lg_u32 s52, 0\n" \
"s_cbranch_scc1 LmB_%=\n" \
"s_add_u32 s62, %[sin], 8192\n" \
"s_add_u32 s63, %[sin], 28672\n" \
"s_waitcnt vmcnt(13)\n" \
"s_nop 0\n" \
"ds_read_b64_tr_b16 a[232:233], %[ta0] offset:0\n" \
"ds_read_b64_tr_b16 a[234:235], %[ta0] offset:128\n" \
"ds_read_b64_tr_b16 a[236:237], %[ta0] offset:1024\n" \
"ds_read_b64_tr_b16 a[238:239], %[ta0] offset:1152\n" \
"ds_read_b64_tr_b16 a[240:241], %[ta0] offset:2048\n" \
"ds_read_b64_tr_b16 a[242:243], %[ta0] offset:2176\n" \
"ds_read_b64_tr_b16 a[244:245], %[ta0] offset:3072\n" \
"ds_read_b64_tr_b16 a[246:247], %[ta0] offset:3200\n" \
"ds_read_b64_tr_b16 a[248:249], %[ta0] offset:4096\n" \
"ds_read_b64_tr_b16 a[250:251], %[ta0] offset:4224\n" \
"ds_read_b64_tr_b16 a[252:253], %[ta0] offset:5120\n" \
"ds_read_b64_tr_b16 a[254:255], %[ta0] offset:5248\n" \
"v_add_u32_e32 v234, %[m0t], v235\n" \
"s_waitcnt lgkmcnt(8)\n" \
"v_mfma_f32_16x16x32_bf16 v[230:233], a[232:235], a[200:203], 0\n" \
"v_mfma_f32_16x16x32_bf16 v[230:233], a[236:239], a[204:207], v[230:233]\n" \
"ds_read_b64_tr_b16 a[232:233], %[ta0] offset:6144\n" \
"ds_read_b64_tr_b16 a[234:235], %[ta0] offset:6272\n" \
"ds_read_b64_tr_b16 a[236:237], %[ta0] offset:7168\n" \
"ds_read_b64_tr_b16 a[238:239], %[ta0] offset:7296\n" \
"s_waitcnt lgkmcnt(4)\n" \
"s_nop 0\n" \
"v_mfma_f32_16x16x32_bf16 v[230:233], a[240:243], a[208:211], v[230:233]\n" \
"v_mfma_f32_16x16x32_bf16 v[230:233], a[244:247], a[212:215], v[230:233]\n" \
"v_mfma_f32_16x16x32_bf16 v[230:233], a[248:251], a[216:219], v[230:233]\n" \
"v_mfma_f32_16x16x32_bf16 v[230:233], a[252:255], a[220:223], v[230:233]\n" \
"s_waitcnt lgkmcnt(0)\n" \
"s_nop 0\n" \
"v_mfma_f32_16x16x32_bf16 v[230:233], a[232:235], a[224:227], v[230:233]\n" \
"v_mfma_f32_16x16x32_bf16 v[230:233], a[236:239], a[228:231], v[230:233]\n" \
"s_nop 7\n" \
"s_nop 0\n" \
"ds_write_b128 v234, v[230:233] offset:7168\n" \
"LmB_%=:\n" \
"v_mfma_f32_16x16x32_bf16 v[128:131], a[64:67], a[0:3], v[160:163]\n" \
"v_exp_f32_e64 v156, v156\n" \
"v_mfma_f32_16x16x32_bf16 v[128:131], a[68:71], a[4:7], v[128:131]\n" \
"ds_read_b64_tr_b16 a[182:183], v211 offset:13312\n" \
"s_add_u32 m0, %[m0b], 0\n" \
"v_mfma_f32_16x16x32_bf16 v[128:131], a[72:75], a[8:11], v[128:131]\n" \
"v_exp_f32_e32 v157, v157\n" \
"v_mfma_f32_16x16x32_bf16 v[128:131], a[76:79], a[12:15], v[128:131]\n" \
"v_cvt_pk_bf16_f32 v204, v152, v153\n" \
"v_cvt_pk_bf16_f32 v205, v154, v155\n" \
"v_mfma_f32_16x16x32_bf16 v[132:135], a[92:95], a[12:15], v[160:163]\n" \
"v_exp_f32_e64 v158, v158\n" \
"v_mfma_f32_16x16x32_bf16 v[132:135], a[88:91], a[8:11], v[132:135]\n" \
"ds_read_b64_tr_b16 a[184:185], %[vvb] offset:9728\n" \
"s_waitcnt lgkmcnt(5)\n" \
"v_mfma_f32_16x16x32_bf16 v[132:135], a[84:87], a[4:7], v[132:135]\n" \
"buffer_load_dwordx4 %[kd0], %[srdk], s48 offen offset:0 lds\n" \
"v_mfma_f32_16x16x32_bf16 v[132:135], a[80:83], a[0:3], v[132:135]\n" \
"v_exp_f32_e32 v159, v159\n" \
"v_mfma_f32_16x16x32_bf16 v[64:67], a[128:131], v[216:219], v[64:67]\n" \
"ds_read_b64_tr_b16 a[186:187], %[vvb] offset:13824\n" \
"ds_read_b64_tr_b16 a[188:189], v211 offset:9728\n" \
"v_mfma_f32_16x16x32_bf16 v[64:67], a[160:163], v[200:203], v[64:67]\n" \
"v_exp_f32_e64 v128, v128\n" \
"v_mfma_f32_16x16x32_bf16 v[68:71], a[132:135], v[216:219], v[68:71]\n" \
"v_cvt_pk_bf16_f32 v206, v156, v157\n" \
"v_cvt_pk_bf16_f32 v207, v158, v159\n" \
"v_mfma_f32_16x16x32_bf16 v[68:71], a[164:167], v[200:203], v[68:71]\n" \
"v_exp_f32_e64 v129, v129\n" \
"v_mfma_f32_16x16x32_bf16 v[72:75], a[136:139], v[216:219], v[72:75]\n" \
"ds_read_b64_tr_b16 a[190:191], v211 offset:13824\n" \
"ds_read_b64_tr_b16 a[128:129], %[vvb] offset:16384\n" \
"s_add_u32 s60, s60, 1\n" \
"s_nop 0\n" \
"v_readlane_b32 s58, %[tlo], s60\n" \
"v_mfma_f32_16x16x32_bf16 v[72:75], a[168:171], v[200:203], v[72:75]\n" \
"v_exp_f32_e64 v130, v130\n" \
"v_mfma_f32_16x16x32_bf16 v[76:79], a[140:143], v[216:219], v[76:79]\n" \
"ds_read_b64_tr_b16 a[130:131], %[vvb] offset:20480\n" \
"ds_read_b64_tr_b16 a[132:133], v211 offset:16384\n" \
"v_readlane_b32 s59, %[thi], s60\n" \
"v_mfma_f32_16x16x32_bf16 v[76:79], a[172:175], v[200:203], v[76:79]\n" \
"v_exp_f32_e64 v131, v131\n" \
"v_smfmac_f32_16x16x64_bf16 v[176:179], a[192:195], v[192:199], v228\n" \
"ds_read_b64_tr_b16 a[134:135], v211 offset:20480\n" \
"ds_read_b64_tr_b16 a[136:137], %[vvb] offset:16896\n" \
"s_cmp_eq_u32 s60, 7\n" \
"s_cselect_b32 s61, 56, 1\n" \
"v_mul_u32_u24_e64 v236, s61, v224\n" \
"v_mfma_f32_16x16x32_bf16 v[136:139], a[64:67], a[16:19], v[164:167]\n" \
"v_exp_f32_e64 v132, v132\n" \
"v_mfma_f32_16x16x32_bf16 v[136:139], a[68:71], a[20:23], v[136:139]\n" \
"ds_read_b64_tr_b16 a[138:139], %[vvb] offset:20992\n" \
"ds_read_b64_tr_b16 a[140:141], v211 offset:16896\n" \
"v_mfma_f32_16x16x32_bf16 v[136:139], a[72:75], a[24:27], v[136:139]\n" \
"v_exp_f32_e64 v133, v133\n" \
"v_mfma_f32_16x16x32_bf16 v[136:139], a[76:79], a[28:31], v[136:139]\n" \
"v_cvt_pk_bf16_f32 v184, v128, v129\n" \
"v_cvt_pk_bf16_f32 v185, v130, v131\n" \
"v_mfma_f32_16x16x32_bf16 v[140:143], a[92:95], a[28:31], v[164:167]\n" \
"v_exp_f32_e64 v134, v134\n" \
"v_mfma_f32_16x16x32_bf16 v[140:143], a[88:91], a[24:27], v[140:143]\n" \
"ds_read_b64_tr_b16 a[142:143], v211 offset:20992\n" \
"s_waitcnt lgkmcnt(8)\n" \
"v_mfma_f32_16x16x32_bf16 v[140:143], a[84:87], a[20:23], v[140:143]\n" \
"buffer_load_dwordx4 v212, %[srdk], s48 offen offset:1024 lds\n" \
"v_mfma_f32_16x16x32_bf16 v[140:143], a[80:83], a[16:19], v[140:143]\n" \
"v_exp_f32_e32 v135, v135\n" \
"v_mfma_f32_16x16x32_bf16 v[80:83], a[144:147], v[216:219], v[80:83]\n" \
"ds_read_b64_tr_b16 a[144:145], %[vvb] offset:17408\n" \
"ds_read_b64_tr_b16 a[146:147], %[vvb] offset:21504\n" \
"v_mfma_f32_16x16x32_bf16 v[80:83], a[176:179], v[200:203], v[80:83]\n" \
"v_exp_f32_e64 v136, v136\n" \
"v_mfma_f32_16x16x32_bf16 v[84:87], a[148:151], v[216:219], v[84:87]\n" \
"v_cvt_pk_bf16_f32 v186, v132, v133\n" \
"v_cvt_pk_bf16_f32 v187, v134, v135\n" \
"v_mfma_f32_16x16x32_bf16 v[84:87], a[180:183], v[200:203], v[84:87]\n" \
"v_exp_f32_e64 v137, v137\n" \
"v_mfma_f32_16x16x32_bf16 v[88:91], a[152:155], v[216:219], v[88:91]\n" \
"v_exp_f32_e64 v138, v138\n" \
"v_mfma_f32_16x16x32_bf16 v[88:91], a[184:187], v[200:203], v[88:91]\n" \
"ds_read_b64_tr_b16 a[148:149], v211 offset:17408\n" \
"ds_read_b64_tr_b16 a[150:151], v211 offset:21504\n" \
"v_mfma_f32_16x16x32_bf16 v[92:95], a[156:159], v[216:219], v[92:95]\n" \
"v_exp_f32_e64 v139, v139\n" \
"v_mfma_f32_16x16x32_bf16 v[92:95], a[188:191], v[200:203], v[92:95]\n" \
"ds_read_b64_tr_b16 a[152:153], %[vvb] offset:17920\n" \
"buffer_store_dwordx4 a[196:199], v235, %[srdz], s63 offen offset:0 sc1\n" \
"v_mfma_f32_16x16x32_bf16 v[144:147], a[64:67], a[32:35], v[168:171]\n" \
"v_exp_f32_e64 v140, v140\n" \
"v_mfma_f32_16x16x32_bf16 v[144:147], a[68:71], a[36:39], v[144:147]\n" \
"ds_read_b64_tr_b16 a[154:155], %[vvb] offset:22016\n" \
"ds_read_b64_tr_b16 a[156:157], v211 offset:17920\n" \
"v_mfma_f32_16x16x32_bf16 v[144:147], a[72:75], a[40:43], v[144:147]\n" \
"v_exp_f32_e64 v141, v141\n" \
"v_mfma_f32_16x16x32_bf16 v[144:147], a[76:79], a[44:47], v[144:147]\n" \
"v_cvt_pk_bf16_f32 v188, v136, v137\n" \
"v_cvt_pk_bf16_f32 v189, v138, v139\n" \
"v_mfma_f32_16x16x32_bf16 v[148:151], a[92:95], a[44:47], v[168:171]\n" \
"v_exp_f32_e64 v142, v142\n" \
"v_mfma_f32_16x16x32_bf16 v[148:151], a[88:91], a[40:43], v[148:151]\n" \
"ds_read_b64_tr_b16 a[158:159], v211 offset:22016\n" \
"s_waitcnt lgkmcnt(8)\n" \
"v_mfma_f32_16x16x32_bf16 v[148:151], a[84:87], a[36:39], v[148:151]\n" \
"buffer_load_dwordx4 v213, %[srdk], s48 offen offset:2048 lds\n" \
"v_mfma_f32_16x16x32_bf16 v[148:151], a[80:83], a[32:35], v[148:151]\n" \
"v_exp_f32_e32 v143, v143\n" \
"v_mfma_f32_16x16x32_bf16 v[0:3], a[160:163], v[192:195], v[0:3]\n" \
"ds_read_b128 a[96:99], %[kb0] offset:24576\n" \
"v_mfma_f32_16x16x32_bf16 v[0:3], a[128:131], v[184:187], v[0:3]\n" \
"v_exp_f32_e64 v144, v144\n" \
"v_mfma_f32_16x16x32_bf16 v[4:7], a[164:167], v[192:195], v[4:7]\n" \
"v_cvt_pk_bf16_f32 v190, v140, v141\n" \
"v_cvt_pk_bf16_f32 v191, v142, v143\n" \
"v_mfma_f32_16x16x32_bf16 v[4:7], a[132:135], v[184:187], v[4:7]\n" \
"v_exp_f32_e64 v145, v145\n" \
"v_mfma_f32_16x16x32_bf16 v[8:11], a[168:171], v[192:195], v[8:11]\n" \
"ds_read_b128 a[100:103], v208 offset:24576\n" \
"v_mfma_f32_16x16x32_bf16 v[8:11], a[136:139], v[184:187], v[8:11]\n" \
"v_exp_f32_e64 v146, v146\n" \
"v_mfma_f32_16x16x32_bf16 v[12:15], a[172:175], v[192:195], v[12:15]\n" \
"ds_read_b128 a[104:107], v209 offset:24576\n" \
"v_mfma_f32_16x16x32_bf16 v[12:15], a[140:143], v[184:187], v[12:15]\n" \
"v_exp_f32_e64 v147, v147\n" \
"v_smfmac_f32_16x16x64_bf16 v[180:183], a[192:195], v[200:207], v228\n" \
"ds_read_b128 a[108:111], v210 offset:24576\n" \
"v_mfma_f32_16x16x32_bf16 v[152:155], a[64:67], a[48:51], v[172:175]\n" \
"v_exp_f32_e64 v148, v148\n" \
"v_mfma_f32_16x16x32_bf16 v[152:155], a[68:71], a[52:55], v[152:155]\n" \
"ds_read_b128 a[124:127], v210 offset:28672\n" \
"v_mfma_f32_16x16x32_bf16 v[152:155], a[72:75], a[56:59], v[152:155]\n" \
"v_exp_f32_e64 v149, v149\n" \
"v_mfma_f32_16x16x32_bf16 v[152:155], a[76:79], a[60:63], v[152:155]\n" \
"v_cvt_pk_bf16_f32 v216, v144, v145\n" \
"v_cvt_pk_bf16_f32 v217, v146, v147\n" \
"v_mfma_f32_16x16x32_bf16 v[156:159], a[92:95], a[60:63], v[172:175]\n" \
"v_exp_f32_e64 v150, v150\n" \
"v_mfma_f32_16x16x32_bf16 v[156:159], a[88:91], a[56:59], v[156:159]\n" \
"ds_read_b128 a[120:123], v209 offset:28672\n" \
"s_waitcnt lgkmcnt(6)\n" \
"v_mfma_f32_16x16x32_bf16 v[156:159], a[84:87], a[52:55], v[156:159]\n" \
"buffer_load_dwordx4 v214, %[srdk], s48 offen offset:3072 lds\n" \
"v_mfma_f32_16x16x32_bf16 v[156:159], a[80:83], a[48:51], v[156:159]\n" \
"v_exp_f32_e32 v151, v151\n" \
"v_mfma_f32_16x16x32_bf16 v[16:19], a[176:179], v[192:195], v[16:19]\n" \
"ds_read_b128 a[116:119], v208 offset:28672\n" \
"v_mfma_f32_16x16x32_bf16 v[16:19], a[144:147], v[184:187], v[16:19]\n" \
"v_exp_f32_e64 v152, v152\n" \
"v_mfma_f32_16x16x32_bf16 v[20:23], a[180:183], v[192:195], v[20:23]\n" \
"v_cvt_pk_bf16_f32 v218, v148, v149\n" \
"v_cvt_pk_bf16_f32 v219, v150, v151\n" \
"v_mfma_f32_16x16x32_bf16 v[20:23], a[148:151], v[184:187], v[20:23]\n" \
"v_exp_f32_e64 v153, v153\n" \
"v_mfma_f32_16x16x32_bf16 v[24:27], a[184:187], v[192:195], v[24:27]\n" \
"v_exp_f32_e64 v154, v154\n" \
"v_mfma_f32_16x16x32_bf16 v[24:27], a[152:155], v[184:187], v[24:27]\n" \
"ds_read_b128 a[112:115], %[kb0] offset:28672\n" \
"s_add_u32 s48, s48, 0x4000\n" \
"s_mov_b32 m0, s44\n" \
"s_addk_i32 s44, 0x400\n" \
"buffer_load_dwordx4 v229, s[64:67], s45 offen offset:0 lds\n" \
"s_add_u32 s45, s45, 229376\n" \
"v_mfma_f32_16x16x32_bf16 v[28:31], a[188:191], v[192:195], v[28:31]\n" \
"v_exp_f32_e64 v155, v155\n" \
"v_mfma_f32_16x16x32_bf16 v[28:31], a[156:159], v[184:187], v[28:31]\n" \
"s_waitcnt vmcnt(15) lgkmcnt(2)\n" \
"s_barrier\n" \
"v_mfma_f32_16x16x32_bf16 v[128:131], a[96:99], a[0:3], v[160:163]\n" \
"v_exp_f32_e64 v156, v156\n" \
"v_mfma_f32_16x16x32_bf16 v[128:131], a[100:103], a[4:7], v[128:131]\n" \
"s_add_u32 m0, %[m0b], 65536\n" \
"v_mfma_f32_16x16x32_bf16 v[128:131], a[104:107], a[8:11], v[128:131]\n" \
"v_exp_f32_e64 v157, v157\n" \
"v_mfma_f32_16x16x32_bf16 v[128:131], a[108:111], a[12:15], v[128:131]\n" \
"v_cvt_pk_bf16_f32 v220, v152, v153\n" \
"v_cvt_pk_bf16_f32 v221, v154, v155\n" \
"v_mfma_f32_16x16x32_bf16 v[132:135], a[124:127], a[12:15], v[160:163]\n" \
"v_exp_f32_e64 v158, v158\n" \
"v_mfma_f32_16x16x32_bf16 v[132:135], a[120:123], a[8:11], v[132:135]\n" \
"s_waitcnt lgkmcnt(0)\n" \
"v_mfma_f32_16x16x32_bf16 v[132:135], a[116:119], a[4:7], v[132:135]\n" \
"buffer_load_dwordx4 %[vd0], %[srdv], s48 offen offset:0 lds\n" \
"v_mfma_f32_16x16x32_bf16 v[132:135], a[112:115], a[0:3], v[132:135]\n" \
"v_exp_f32_e32 v159, v159\n" \
"v_mfma_f32_16x16x32_bf16 v[32:35], a[160:163], v[196:199], v[32:35]\n" \
"ds_read_b128 a[64:67], %[kb0] offset:32768\n" \
"v_mfma_f32_16x16x32_bf16 v[32:35], a[128:131], v[188:191], v[32:35]\n" \
"v_exp_f32_e64 v128, v128\n" \
"v_mfma_f32_16x16x32_bf16 v[36:39], a[164:167], v[196:199], v[36:39]\n" \
"v_cvt_pk_bf16_f32 v222, v156, v157\n" \
"v_cvt_pk_bf16_f32 v223, v158, v159\n" \
"v_mfma_f32_16x16x32_bf16 v[36:39], a[132:135], v[188:191], v[36:39]\n" \
"v_exp_f32_e64 v129, v129\n" \
"v_mfma_f32_16x16x32_bf16 v[40:43], a[168:171], v[196:199], v[40:43]\n" \
"ds_read_b128 a[68:71], v208 offset:32768\n" \
"v_mfma_f32_16x16x32_bf16 v[40:43], a[136:139], v[188:191], v[40:43]\n" \
"v_exp_f32_e64 v130, v130\n" \
"v_mfma_f32_16x16x32_bf16 v[44:47], a[172:175], v[196:199], v[44:47]\n" \
"ds_read_b128 a[72:75], v209 offset:32768\n" \
"v_mfma_f32_16x16x32_bf16 v[44:47], a[140:143], v[188:191], v[44:47]\n" \
"v_exp_f32_e64 v131, v131\n" \
"v_smfmac_f32_16x16x64_bf16 v[176:179], a[192:195], v[184:191], v228\n" \
"v_mfma_f32_16x16x32_bf16 v[136:139], a[96:99], a[16:19], v[164:167]\n" \
"v_exp_f32_e64 v132, v132\n" \
"v_mfma_f32_16x16x32_bf16 v[136:139], a[100:103], a[20:23], v[136:139]\n" \
"ds_read_b128 a[76:79], v210 offset:32768\n" \
"v_mfma_f32_16x16x32_bf16 v[136:139], a[104:107], a[24:27], v[136:139]\n" \
"v_exp_f32_e64 v133, v133\n" \
"v_mfma_f32_16x16x32_bf16 v[136:139], a[108:111], a[28:31], v[136:139]\n" \
"v_cvt_pk_bf16_f32 v192, v128, v129\n" \
"v_cvt_pk_bf16_f32 v193, v130, v131\n" \
"v_mfma_f32_16x16x32_bf16 v[140:143], a[124:127], a[28:31], v[164:167]\n" \
"v_exp_f32_e64 v134, v134\n" \
"v_mfma_f32_16x16x32_bf16 v[140:143], a[120:123], a[24:27], v[140:143]\n" \
"ds_read_b128 a[92:95], v210 offset:36864\n" \
"v_mfma_f32_16x16x32_bf16 v[140:143], a[116:119], a[20:23], v[140:143]\n" \
"buffer_load_dwordx4 v215, %[srdv], s48 offen offset:1024 lds\n" \
"v_mfma_f32_16x16x32_bf16 v[140:143], a[112:115], a[16:19], v[140:143]\n" \
"v_exp_f32_e64 v135, v135\n" \
"v_mfma_f32_16x16x32_bf16 v[48:51], a[176:179], v[196:199], v[48:51]\n" \
"ds_read_b128 a[88:91], v209 offset:36864\n" \
"v_mfma_f32_16x16x32_bf16 v[48:51], a[144:147], v[188:191], v[48:51]\n" \
"v_exp_f32_e64 v136, v136\n" \
"v_mfma_f32_16x16x32_bf16 v[52:55], a[180:183], v[196:199], v[52:55]\n" \
"v_cvt_pk_bf16_f32 v194, v132, v133\n" \
"v_cvt_pk_bf16_f32 v195, v134, v135\n" \
"v_mfma_f32_16x16x32_bf16 v[52:55], a[148:151], v[188:191], v[52:55]\n" \
"v_exp_f32_e64 v137, v137\n" \
"v_mfma_f32_16x16x32_bf16 v[56:59], a[184:187], v[196:199], v[56:59]\n" \
"v_exp_f32_e64 v138, v138\n" \
"v_mfma_f32_16x16x32_bf16 v[56:59], a[152:155], v[188:191], v[56:59]\n" \
"ds_read_b128 a[84:87], v208 offset:36864\n" \
"v_mfma_f32_16x16x32_bf16 v[60:63], a[188:191], v[196:199], v[60:63]\n" \
"v_exp_f32_e64 v139, v139\n" \
"v_mfma_f32_16x16x32_bf16 v[60:63], a[156:159], v[188:191], v[60:63]\n" \
"s_cmp_lg_u32 %[wid], 0\n" \
"s_cbranch_scc1 LtB3_%=\n" \
"global_load_dword v237, v236, s[58:59]\n" \
"s_add_u32 s61, s48, 49152\n" \
"s_mov_b32 exec_hi, 0\n" \
"s_nop 0\n" \
"buffer_load_dword v237, v224, %[srdp], s61 offen\n" \
"s_mov_b32 exec_hi, -1\n" \
"s_nop 0\n" \
"LtB3_%=:\n" \
"v_mfma_f32_16x16x32_bf16 v[144:147], a[96:99], a[32:35], v[168:171]\n" \
"v_exp_f32_e64 v140, v140\n" \
"v_mfma_f32_16x16x32_bf16 v[144:147], a[100:103], a[36:39], v[144:147]\n" \
"ds_read_b128 a[80:83], %[kb0] offset:36864\n" \
"v_mfma_f32_16x16x32_bf16 v[144:147], a[104:107], a[40:43], v[144:147]\n" \
"v_exp_f32_e64 v141, v141\n" \
"v_mfma_f32_16x16x32_bf16 v[144:147], a[108:111], a[44:47], v[144:147]\n" \
"v_cvt_pk_bf16_f32 v196, v136, v137\n" \
"v_cvt_pk_bf16_f32 v197, v138, v139\n" \
"v_mfma_f32_16x16x32_bf16 v[148:151], a[124:127], a[44:47], v[168:171]\n" \
"v_exp_f32_e64 v142, v142\n" \
"v_mfma_f32_16x16x32_bf16 v[148:151], a[120:123], a[40:43], v[148:151]\n" \
"v_mfma_f32_16x16x32_bf16 v[148:151], a[116:119], a[36:39], v[148:151]\n" \
"buffer_load_dwordx4 %[vd0], %[srdv], s48 offen offset:2048 lds\n" \
"v_mfma_f32_16x16x32_bf16 v[148:151], a[112:115], a[32:35], v[148:151]\n" \
"v_exp_f32_e64 v143, v143\n" \
"v_mfma_f32_16x16x32_bf16 v[96:99], a[160:163], v[204:207], v[96:99]\n" \
"ds_read_b64_tr_b16 a[160:161], %[vvb] offset:24576\n" \
"v_mfma_f32_16x16x32_bf16 v[96:99], a[128:131], v[220:223], v[96:99]\n" \
"v_exp_f32_e64 v144, v144\n" \
"v_mfma_f32_16x16x32_bf16 v[100:103], a[164:167], v[204:207], v[100:103]\n" \
"v_cvt_pk_bf16_f32 v198, v140, v141\n" \
"v_cvt_pk_bf16_f32 v199, v142, v143\n" \
"v_mfma_f32_16x16x32_bf16 v[100:103], a[132:135], v[220:223], v[100:103]\n" \
"v_exp_f32_e64 v145, v145\n" \
"v_mfma_f32_16x16x32_bf16 v[104:107], a[168:171], v[204:207], v[104:107]\n" \
"ds_read_b64_tr_b16 a[162:163], %[vvb] offset:28672\n" \
"ds_read_b64_tr_b16 a[164:165], v211 offset:24576\n" \
"v_mfma_f32_16x16x32_bf16 v[104:107], a[136:139], v[220:223], v[104:107]\n" \
"v_exp_f32_e64 v146, v146\n" \
"v_mfma_f32_16x16x32_bf16 v[108:111], a[172:175], v[204:207], v[108:111]\n" \
"ds_read_b64_tr_b16 a[166:167], v211 offset:28672\n" \
"v_mfma_f32_16x16x32_bf16 v[108:111], a[140:143], v[220:223], v[108:111]\n" \
"v_exp_f32_e64 v147, v147\n" \
"v_smfmac_f32_16x16x64_bf16 v[180:183], a[192:195], v[216:223], v228\n" \
"ds_read_b64_tr_b16 a[168:169], %[vvb] offset:25088\n" \
"buffer_store_dwordx4 a[196:199], v235, %[srdz], s62 offen offset:0 sc1\n" \
"v_mfma_f32_16x16x32_bf16 v[152:155], a[96:99], a[48:51], v[172:175]\n" \
"v_exp_f32_e64 v148, v148\n" \
"v_mfma_f32_16x16x32_bf16 v[152:155], a[100:103], a[52:55], v[152:155]\n" \
"ds_read_b64_tr_b16 a[170:171], %[vvb] offset:29184\n" \
"v_mfma_f32_16x16x32_bf16 v[152:155], a[104:107], a[56:59], v[152:155]\n" \
"v_exp_f32_e64 v149, v149\n" \
"v_mfma_f32_16x16x32_bf16 v[152:155], a[108:111], a[60:63], v[152:155]\n" \
"v_cvt_pk_bf16_f32 v200, v144, v145\n" \
"v_cvt_pk_bf16_f32 v201, v146, v147\n" \
"v_mfma_f32_16x16x32_bf16 v[156:159], a[124:127], a[60:63], v[172:175]\n" \
"v_exp_f32_e64 v150, v150\n" \
"v_mfma_f32_16x16x32_bf16 v[156:159], a[120:123], a[56:59], v[156:159]\n" \
"ds_read_b64_tr_b16 a[172:173], v211 offset:25088\n" \
"v_mfma_f32_16x16x32_bf16 v[156:159], a[116:119], a[52:55], v[156:159]\n" \
"buffer_load_dwordx4 v215, %[srdv], s48 offen offset:3072 lds\n" \
"v_mfma_f32_16x16x32_bf16 v[156:159], a[112:115], a[48:51], v[156:159]\n" \
"v_exp_f32_e64 v151, v151\n" \
"v_mfma_f32_16x16x32_bf16 v[112:115], a[176:179], v[204:207], v[112:115]\n" \
"ds_read_b64_tr_b16 a[174:175], v211 offset:29184\n" \
"ds_read_b64_tr_b16 a[176:177], %[vvb] offset:25600\n" \
"v_mfma_f32_16x16x32_bf16 v[112:115], a[144:147], v[220:223], v[112:115]\n" \
"v_exp_f32_e64 v152, v152\n" \
"v_mfma_f32_16x16x32_bf16 v[116:119], a[180:183], v[204:207], v[116:119]\n" \
"v_cvt_pk_bf16_f32 v202, v148, v149\n" \
"v_cvt_pk_bf16_f32 v203, v150, v151\n" \
"v_mfma_f32_16x16x32_bf16 v[116:119], a[148:151], v[220:223], v[116:119]\n" \
"v_exp_f32_e64 v153, v153\n" \
"v_mfma_f32_16x16x32_bf16 v[120:123], a[184:187], v[204:207], v[120:123]\n" \
"v_exp_f32_e64 v154, v154\n" \
"v_mfma_f32_16x16x32_bf16 v[120:123], a[152:155], v[220:223], v[120:123]\n" \
"ds_read_b64_tr_b16 a[178:179], %[vvb] offset:29696\n" \
"v_mfma_f32_16x16x32_bf16 v[124:127], a[188:191], v[204:207], v[124:127]\n" \
"v_exp_f32_e64 v155, v155\n" \
"v_mfma_f32_16x16x32_bf16 v[124:127], a[156:159], v[220:223], v[124:127]\n" \
"ds_read_b64_tr_b16 a[180:181], v211 offset:25600\n" \
"s_waitcnt lgkmcnt(13)\n" \
"v_mfma_f32_16x16x32_bf16 v[128:131], a[64:67], a[0:3], v[160:163]\n" \
"v_exp_f32_e32 v156, v156\n" \
"v_mfma_f32_16x16x32_bf16 v[128:131], a[68:71], a[4:7], v[128:131]\n" \
"ds_read_b64_tr_b16 a[182:183], v211 offset:29696\n" \
"s_add_u32 m0, %[m0b], 16384\n" \
"v_mfma_f32_16x16x32_bf16 v[128:131], a[72:75], a[8:11], v[128:131]\n" \
"v_exp_f32_e64 v157, v157\n" \
"v_mfma_f32_16x16x32_bf16 v[128:131], a[76:79], a[12:15], v[128:131]\n" \
"v_cvt_pk_bf16_f32 v204, v152, v153\n" \
"v_cvt_pk_bf16_f32 v205, v154, v155\n" \
"v_mfma_f32_16x16x32_bf16 v[132:135], a[92:95], a[12:15], v[160:163]\n" \
"v_exp_f32_e64 v158, v158\n" \
"v_mfma_f32_16x16x32_bf16 v[132:135], a[88:91], a[8:11], v[132:135]\n" \
"ds_read_b64_tr_b16 a[184:185], %[vvb] offset:26112\n" \
"s_waitcnt lgkmcnt(5)\n" \
"v_mfma_f32_16x16x32_bf16 v[132:135], a[84:87], a[4:7], v[132:135]\n" \
"buffer_load_dwordx4 %[kd0], %[srdk], s48 offen offset:0 lds\n" \
"v_mfma_f32_16x16x32_bf16 v[132:135], a[80:83], a[0:3], v[132:135]\n" \
"v_exp_f32_e32 v159, v159\n" \
"v_mfma_f32_16x16x32_bf16 v[64:67], a[128:131], v[216:219], v[64:67]\n" \
"ds_read_b64_tr_b16 a[186:187], %[vvb] offset:30208\n" \
"ds_read_b64_tr_b16 a[188:189], v211 offset:26112\n" \
"v_mfma_f32_16x16x32_bf16 v[64:67], a[160:163], v[200:203], v[64:67]\n" \
"v_exp_f32_e64 v128, v128\n" \
"v_mfma_f32_16x16x32_bf16 v[68:71], a[132:135], v[216:219], v[68:71]\n" \
"v_cvt_pk_bf16_f32 v206, v156, v157\n" \
"v_cvt_pk_bf16_f32 v207, v158, v159\n" \
"v_mfma_f32_16x16x32_bf16 v[68:71], a[164:167], v[200:203], v[68:71]\n" \
"v_exp_f32_e64 v129, v129\n" \
"v_mfma_f32_16x16x32_bf16 v[72:75], a[136:139], v[216:219], v[72:75]\n" \
"ds_read_b64_tr_b16 a[190:191], v211 offset:30208\n" \
"ds_read_b64_tr_b16 a[128:129], %[vvb] offset:32768\n" \
"v_mfma_f32_16x16x32_bf16 v[72:75], a[168:171], v[200:203], v[72:75]\n" \
"v_exp_f32_e64 v130, v130\n" \
"v_mfma_f32_16x16x32_bf16 v[76:79], a[140:143], v[216:219], v[76:79]\n" \
"ds_read_b64_tr_b16 a[130:131], %[vvb] offset:36864\n" \
"ds_read_b64_tr_b16 a[132:133], v211 offset:32768\n" \
"v_mfma_f32_16x16x32_bf16 v[76:79], a[172:175], v[200:203], v[76:79]\n" \
"v_exp_f32_e64 v131, v131\n" \
"v_smfmac_f32_16x16x64_bf16 v[176:179], a[192:195], v[192:199], v228\n" \
"ds_read_b64_tr_b16 a[134:135], v211 offset:36864\n" \
"ds_read_b64_tr_b16 a[136:137], %[vvb] offset:33280\n" \
"v_mfma_f32_16x16x32_bf16 v[136:139], a[64:67], a[16:19], v[164:167]\n" \
"v_exp_f32_e64 v132, v132\n" \
"v_mfma_f32_16x16x32_bf16 v[136:139], a[68:71], a[20:23], v[136:139]\n" \
"ds_read_b64_tr_b16 a[138:139], %[vvb] offset:37376\n" \
"ds_read_b64_tr_b16 a[140:141], v211 offset:33280\n" \
"v_mfma_f32_16x16x32_bf16 v[136:139], a[72:75], a[24:27], v[136:139]\n" \
"v_exp_f32_e64 v133, v133\n" \
"v_mfma_f32_16x16x32_bf16 v[136:139], a[76:79], a[28:31], v[136:139]\n" \
"v_cvt_pk_bf16_f32 v184, v128, v129\n" \
"v_cvt_pk_bf16_f32 v185, v130, v131\n" \
"v_mfma_f32_16x16x32_bf16 v[140:143], a[92:95], a[28:31], v[164:167]\n" \
"v_exp_f32_e64 v134, v134\n" \
"v_mfma_f32_16x16x32_bf16 v[140:143], a[88:91], a[24:27], v[140:143]\n" \
"ds_read_b64_tr_b16 a[142:143], v211 offset:37376\n" \
"s_waitcnt lgkmcnt(8)\n" \
"v_mfma_f32_16x16x32_bf16 v[140:143], a[84:87], a[20:23], v[140:143]\n" \
"buffer_load_dwordx4 v212, %[srdk], s48 offen offset:1024 lds\n" \
"v_mfma_f32_16x16x32_bf16 v[140:143], a[80:83], a[16:19], v[140:143]\n" \
"v_exp_f32_e32 v135, v135\n" \
"v_mfma_f32_16x16x32_bf16 v[80:83], a[144:147], v[216:219], v[80:83]\n" \
"ds_read_b64_tr_b16 a[144:145], %[vvb] offset:33792\n" \
"ds_read_b64_tr_b16 a[146:147], %[vvb] offset:37888\n" \
"v_mfma_f32_16x16x32_bf16 v[80:83], a[176:179], v[200:203], v[80:83]\n" \
"v_exp_f32_e64 v136, v136\n" \
"v_mfma_f32_16x16x32_bf16 v[84:87], a[148:151], v[216:219], v[84:87]\n" \
"v_cvt_pk_bf16_f32 v186, v132, v133\n" \
"v_cvt_pk_bf16_f32 v187, v134, v135\n" \
"v_mfma_f32_16x16x32_bf16 v[84:87], a[180:183], v[200:203], v[84:87]\n" \
"v_exp_f32_e64 v137, v137\n" \
"v_mfma_f32_16x16x32_bf16 v[88:91], a[152:155], v[216:219], v[88:91]\n" \
"v_exp_f32_e64 v138, v138\n" \
"v_mfma_f32_16x16x32_bf16 v[88:91], a[184:187], v[200:203], v[88:91]\n" \
"ds_read_b64_tr_b16 a[148:149], v211 offset:33792\n" \
"ds_read_b64_tr_b16 a[150:151], v211 offset:37888\n" \
"v_mfma_f32_16x16x32_bf16 v[92:95], a[156:159], v[216:219], v[92:95]\n" \
".if . - adaln_body != 24576\n" \
".error \"size model\"\n" \
".endif\n" \
"Lk5_%=:\n" \
"s_cbranch_execz adaln_kr\n" \
"s_nop 0\n" \
"v_exp_f32_e64 v139, v139\n" \
"v_mfma_f32_16x16x32_bf16 v[92:95], a[188:191], v[200:203], v[92:95]\n" \
"ds_read_b64_tr_b16 a[152:153], %[vvb] offset:34304\n" \
"buffer_store_dwordx4 a[196:199], v235, %[srdz], s63 offen offset:1024 sc1\n" \
"v_mfma_f32_16x16x32_bf16 v[144:147], a[64:67], a[32:35], v[168:171]\n" \
"v_exp_f32_e64 v140, v140\n" \
"v_mfma_f32_16x16x32_bf16 v[144:147], a[68:71], a[36:39], v[144:147]\n" \
"ds_read_b64_tr_b16 a[154:155], %[vvb] offset:38400\n" \
"ds_read_b64_tr_b16 a[156:157], v211 offset:34304\n" \
"v_mfma_f32_16x16x32_bf16 v[144:147], a[72:75], a[40:43], v[144:147]\n" \
"v_exp_f32_e64 v141, v141\n" \
"v_mfma_f32_16x16x32_bf16 v[144:147], a[76:79], a[44:47], v[144:147]\n" \
"v_cvt_pk_bf16_f32 v188, v136, v137\n" \
"v_cvt_pk_bf16_f32 v189, v138, v139\n" \
"v_mfma_f32_16x16x32_bf16 v[148:151], a[92:95], a[44:47], v[168:171]\n" \
"v_exp_f32_e64 v142, v142\n" \
"v_mfma_f32_16x16x32_bf16 v[148:151], a[88:91], a[40:43], v[148:151]\n" \
"ds_read_b64_tr_b16 a[158:159], v211 offset:38400\n" \
"s_waitcnt lgkmcnt(8)\n" \
"v_mfma_f32_16x16x32_bf16 v[148:151], a[84:87], a[36:39], v[148:151]\n" \
"buffer_load_dwordx4 v213, %[srdk], s48 offen offset:2048 lds\n" \
"v_mfma_f32_16x16x32_bf16 v[148:151], a[80:83], a[32:35], v[148:151]\n" \
"v_exp_f32_e32 v143, v143\n" \
"v_mfma_f32_16x16x32_bf16 v[0:3], a[160:163], v[192:195], v[0:3]\n" \
"ds_read_b128 a[96:99], %[kb0] offset:40960\n" \
"v_mfma_f32_16x16x32_bf16 v[0:3], a[128:131], v[184:187], v[0:3]\n" \
"v_exp_f32_e64 v144, v144\n" \
"v_mfma_f32_16x16x32_bf16 v[4:7], a[164:167], v[192:195], v[4:7]\n" \
"v_cvt_pk_bf16_f32 v190, v140, v141\n" \
"v_cvt_pk_bf16_f32 v191, v142, v143\n" \
"v_mfma_f32_16x16x32_bf16 v[4:7], a[132:135], v[184:187], v[4:7]\n" \
"v_exp_f32_e64 v145, v145\n" \
"v_mfma_f32_16x16x32_bf16 v[8:11], a[168:171], v[192:195], v[8:11]\n" \
"ds_read_b128 a[100:103], v208 offset:40960\n" \
"v_mfma_f32_16x16x32_bf16 v[8:11], a[136:139], v[184:187], v[8:11]\n" \
"v_exp_f32_e64 v146, v146\n" \
"v_mfma_f32_16x16x32_bf16 v[12:15], a[172:175], v[192:195], v[12:15]\n" \
"ds_read_b128 a[104:107], v209 offset:40960\n" \
"v_mfma_f32_16x16x32_bf16 v[12:15], a[140:143], v[184:187], v[12:15]\n" \
"v_exp_f32_e64 v147, v147\n" \
"v_smfmac_f32_16x16x64_bf16 v[180:183], a[192:195], v[200:207], v228\n" \
"ds_read_b128 a[108:111], v210 offset:40960\n" \
"v_mfma_f32_16x16x32_bf16 v[152:155], a[64:67], a[48:51], v[172:175]\n" \
"v_exp_f32_e64 v148, v148\n" \
"v_mfma_f32_16x16x32_bf16 v[152:155], a[68:71], a[52:55], v[152:155]\n" \
"ds_read_b128 a[124:127], v210 offset:45056\n" \
"v_mfma_f32_16x16x32_bf16 v[152:155], a[72:75], a[56:59], v[152:155]\n" \
"v_exp_f32_e64 v149, v149\n" \
"v_mfma_f32_16x16x32_bf16 v[152:155], a[76:79], a[60:63], v[152:155]\n" \
"v_cvt_pk_bf16_f32 v216, v144, v145\n" \
"v_cvt_pk_bf16_f32 v217, v146, v147\n" \
"v_mfma_f32_16x16x32_bf16 v[156:159], a[92:95], a[60:63], v[172:175]\n" \
"v_exp_f32_e64 v150, v150\n" \
"v_mfma_f32_16x16x32_bf16 v[156:159], a[88:91], a[56:59], v[156:159]\n" \
"ds_read_b128 a[120:123], v209 offset:45056\n" \
"s_waitcnt lgkmcnt(6)\n" \
"v_mfma_f32_16x16x32_bf16 v[156:159], a[84:87], a[52:55], v[156:159]\n" \
"buffer_load_dwordx4 v214, %[srdk], s48 offen offset:3072 lds\n" \
"v_mfma_f32_16x16x32_bf16 v[156:159], a[80:83], a[48:51], v[156:159]\n" \
"v_exp_f32_e32 v151, v151\n" \
"v_mfma_f32_16x16x32_bf16 v[16:19], a[176:179], v[192:195], v[16:19]\n" \
"ds_read_b128 a[116:119], v208 offset:45056\n" \
"v_mfma_f32_16x16x32_bf16 v[16:19], a[144:147], v[184:187], v[16:19]\n" \
"v_exp_f32_e64 v152, v152\n" \
"v_mfma_f32_16x16x32_bf16 v[20:23], a[180:183], v[192:195], v[20:23]\n" \
"v_cvt_pk_bf16_f32 v218, v148, v149\n" \
"v_cvt_pk_bf16_f32 v219, v150, v151\n" \
"v_mfma_f32_16x16x32_bf16 v[20:23], a[148:151], v[184:187], v[20:23]\n" \
"v_exp_f32_e64 v153, v153\n" \
"v_mfma_f32_16x16x32_bf16 v[24:27], a[184:187], v[192:195], v[24:27]\n" \
"v_exp_f32_e64 v154, v154\n" \
"v_mfma_f32_16x16x32_bf16 v[24:27], a[152:155], v[184:187], v[24:27]\n" \
"ds_read_b128 a[112:115], %[kb0] offset:45056\n" \
"s_add_u32 s48, s48, 0x4000\n" \
"v_mfma_f32_16x16x32_bf16 v[28:31], a[188:191], v[192:195], v[28:31]\n" \
"v_exp_f32_e64 v155, v155\n" \
"v_mfma_f32_16x16x32_bf16 v[28:31], a[156:159], v[184:187], v[28:31]\n" \
"s_waitcnt vmcnt(16) lgkmcnt(2)\n" \
"s_barrier\n" \
"v_mfma_f32_16x16x32_bf16 v[128:131], a[96:99], a[0:3], v[160:163]\n" \
"v_exp_f32_e64 v156, v156\n" \
"v_mfma_f32_16x16x32_bf16 v[128:131], a[100:103], a[4:7], v[128:131]\n" \
"s_add_u32 m0, %[m0b], 81920\n" \
"v_mfma_f32_16x16x32_bf16 v[128:131], a[104:107], a[8:11], v[128:131]\n" \
"v_exp_f32_e64 v157, v157\n" \
"v_mfma_f32_16x16x32_bf16 v[128:131], a[108:111], a[12:15], v[128:131]\n" \
"v_cvt_pk_bf16_f32 v220, v152, v153\n" \
"v_cvt_pk_bf16_f32 v221, v154, v155\n" \
"v_mfma_f32_16x16x32_bf16 v[132:135], a[124:127], a[12:15], v[160:163]\n" \
"v_exp_f32_e64 v158, v158\n" \
"v_mfma_f32_16x16x32_bf16 v[132:135], a[120:123], a[8:11], v[132:135]\n" \
"s_waitcnt lgkmcnt(0)\n" \
"v_mfma_f32_16x16x32_bf16 v[132:135], a[116:119], a[4:7], v[132:135]\n" \
"buffer_load_dwordx4 %[vd0], %[srdv], s48 offen offset:0 lds\n" \
"v_mfma_f32_16x16x32_bf16 v[132:135], a[112:115], a[0:3], v[132:135]\n" \
"v_exp_f32_e32 v159, v159\n" \
"v_mfma_f32_16x16x32_bf16 v[32:35], a[160:163], v[196:199], v[32:35]\n" \
"ds_read_b128 a[64:67], %[kb0] offset:49152\n" \
"v_mfma_f32_16x16x32_bf16 v[32:35], a[128:131], v[188:191], v[32:35]\n" \
"v_exp_f32_e64 v128, v128\n" \
"v_mfma_f32_16x16x32_bf16 v[36:39], a[164:167], v[196:199], v[36:39]\n" \
"v_cvt_pk_bf16_f32 v222, v156, v157\n" \
"v_cvt_pk_bf16_f32 v223, v158, v159\n" \
"v_mfma_f32_16x16x32_bf16 v[36:39], a[132:135], v[188:191], v[36:39]\n" \
"v_exp_f32_e64 v129, v129\n" \
"v_mfma_f32_16x16x32_bf16 v[40:43], a[168:171], v[196:199], v[40:43]\n" \
"ds_read_b128 a[68:71], v208 offset:49152\n" \
"v_mfma_f32_16x16x32_bf16 v[40:43], a[136:139], v[188:191], v[40:43]\n" \
"v_exp_f32_e64 v130, v130\n" \
"v_mfma_f32_16x16x32_bf16 v[44:47], a[172:175], v[196:199], v[44:47]\n" \
"ds_read_b128 a[72:75], v209 offset:49152\n" \
"v_mfma_f32_16x16x32_bf16 v[44:47], a[140:143], v[188:191], v[44:47]\n" \
"v_exp_f32_e64 v131, v131\n" \
"v_smfmac_f32_16x16x64_bf16 v[176:179], a[192:195], v[184:191], v228\n" \
"v_mfma_f32_16x16x32_bf16 v[136:139], a[96:99], a[16:19], v[164:167]\n" \
"v_exp_f32_e64 v132, v132\n" \
"v_mfma_f32_16x16x32_bf16 v[136:139], a[100:103], a[20:23], v[136:139]\n" \
"ds_read_b128 a[76:79], v210 offset:49152\n" \
"v_mfma_f32_16x16x32_bf16 v[136:139], a[104:107], a[24:27], v[136:139]\n" \
"v_exp_f32_e64 v133, v133\n" \
"v_mfma_f32_16x16x32_bf16 v[136:139], a[108:111], a[28:31], v[136:139]\n" \
"v_cvt_pk_bf16_f32 v192, v128, v129\n" \
"v_cvt_pk_bf16_f32 v193, v130, v131\n" \
"v_mfma_f32_16x16x32_bf16 v[140:143], a[124:127], a[28:31], v[164:167]\n" \
"v_exp_f32_e64 v134, v134\n" \
"v_mfma_f32_16x16x32_bf16 v[140:143], a[120:123], a[24:27], v[140:143]\n" \
"ds_read_b128 a[92:95], v210 offset:53248\n" \
"v_mfma_f32_16x16x32_bf16 v[140:143], a[116:119], a[20:23], v[140:143]\n" \
"buffer_load_dwordx4 v215, %[srdv], s48 offen offset:1024 lds\n" \
"v_mfma_f32_16x16x32_bf16 v[140:143], a[112:115], a[16:19], v[140:143]\n" \
"v_exp_f32_e64 v135, v135\n" \
"v_mfma_f32_16x16x32_bf16 v[48:51], a[176:179], v[196:199], v[48:51]\n" \
"ds_read_b128 a[88:91], v209 offset:53248\n" \
"v_mfma_f32_16x16x32_bf16 v[48:51], a[144:147], v[188:191], v[48:51]\n" \
"v_exp_f32_e64 v136, v136\n" \
"v_mfma_f32_16x16x32_bf16 v[52:55], a[180:183], v[196:199], v[52:55]\n" \
"v_cvt_pk_bf16_f32 v194, v132, v133\n" \
"v_cvt_pk_bf16_f32 v195, v134, v135\n" \
"v_mfma_f32_16x16x32_bf16 v[52:55], a[148:151], v[188:191], v[52:55]\n" \
"v_exp_f32_e64 v137, v137\n" \
"v_mfma_f32_16x16x32_bf16 v[56:59], a[184:187], v[196:199], v[56:59]\n" \
"v_exp_f32_e64 v138, v138\n" \
"v_mfma_f32_16x16x32_bf16 v[56:59], a[152:155], v[188:191], v[56:59]\n" \
"ds_read_b128 a[84:87], v208 offset:53248\n" \
"v_mfma_f32_16x16x32_bf16 v[60:63], a[188:191], v[196:199], v[60:63]\n" \
"v_exp_f32_e64 v139, v139\n" \
"v_mfma_f32_16x16x32_bf16 v[60:63], a[156:159], v[188:191], v[60:63]\n" \
"s_cmp_lg_u32 %[wid], 1\n" \
"s_cbranch_scc1 LtB5_%=\n" \
"global_load_dword v237, v236, s[58:59]\n" \
"s_add_u32 s61, s48, 49152\n" \
"s_mov_b32 exec_hi, 0\n" \
"s_nop 0\n" \
"buffer_load_dword v237, v224, %[srdp], s61 offen\n" \
"s_mov_b32 exec_hi, -1\n" \
"s_nop 0\n" \
"LtB5_%=:\n" \
"v_mfma_f32_16x16x32_bf16 v[144:147], a[96:99], a[32:35], v[168:171]\n" \
"v_exp_f32_e64 v140, v140\n" \
"v_mfma_f32_16x16x32_bf16 v[144:147], a[100:103], a[36:39], v[144:147]\n" \
"ds_read_b128 a[80:83], %[kb0] offset:53248\n" \
"v_mfma_f32_16x16x32_bf16 v[144:147], a[104:107], a[40:43], v[144:147]\n" \
"v_exp_f32_e64 v141, v141\n" \
"v_mfma_f32_16x16x32_bf16 v[144:147], a[108:111], a[44:47], v[144:147]\n" \
"v_cvt_pk_bf16_f32 v196, v136, v137\n" \
"v_cvt_pk_bf16_f32 v197, v138, v139\n" \
"v_mfma_f32_16x16x32_bf16 v[148:151], a[124:127], a[44:47], v[168:171]\n" \
"v_exp_f32_e64 v142, v142\n" \
"v_mfma_f32_16x16x32_bf16 v[148:151], a[120:123], a[40:43], v[148:151]\n" \
"v_mfma_f32_16x16x32_bf16 v[148:151], a[116:119], a[36:39], v[148:151]\n" \
"buffer_load_dwordx4 %[vd0], %[srdv], s48 offen offset:2048 lds\n" \
"v_mfma_f32_16x16x32_bf16 v[148:151], a[112:115], a[32:35], v[148:151]\n" \
"v_exp_f32_e64 v143, v143\n" \
"v_mfma_f32_16x16x32_bf16 v[96:99], a[160:163], v[204:207], v[96:99]\n" \
"ds_read_b64_tr_b16 a[160:161], %[vvb] offset:40960\n" \
"v_mfma_f32_16x16x32_bf16 v[96:99], a[128:131], v[220:223], v[96:99]\n" \
"v_exp_f32_e64 v144, v144\n" \
"v_mfma_f32_16x16x32_bf16 v[100:103], a[164:167], v[204:207], v[100:103]\n" \
"v_cvt_pk_bf16_f32 v198, v140, v141\n" \
"v_cvt_pk_bf16_f32 v199, v142, v143\n" \
"v_mfma_f32_16x16x32_bf16 v[100:103], a[132:135], v[220:223], v[100:103]\n" \
"v_exp_f32_e64 v145, v145\n" \
"v_mfma_f32_16x16x32_bf16 v[104:107], a[168:171], v[204:207], v[104:107]\n" \
"ds_read_b64_tr_b16 a[162:163], %[vvb] offset:45056\n" \
"ds_read_b64_tr_b16 a[164:165], v211 offset:40960\n" \
"v_mfma_f32_16x16x32_bf16 v[104:107], a[136:139], v[220:223], v[104:107]\n" \
"v_exp_f32_e64 v146, v146\n" \
"v_mfma_f32_16x16x32_bf16 v[108:111], a[172:175], v[204:207], v[108:111]\n" \
"ds_read_b64_tr_b16 a[166:167], v211 offset:45056\n" \
"v_mfma_f32_16x16x32_bf16 v[108:111], a[140:143], v[220:223], v[108:111]\n" \
"v_exp_f32_e64 v147, v147\n" \
"v_smfmac_f32_16x16x64_bf16 v[180:183], a[192:195], v[216:223], v228\n" \
"ds_read_b64_tr_b16 a[168:169], %[vvb] offset:41472\n" \
"v_mfma_f32_16x16x32_bf16 v[152:155], a[96:99], a[48:51], v[172:175]\n" \
"v_exp_f32_e64 v148, v148\n" \
"v_mfma_f32_16x16x32_bf16 v[152:155], a[100:103], a[52:55], v[152:155]\n" \
"ds_read_b64_tr_b16 a[170:171], %[vvb] offset:45568\n" \
"v_mfma_f32_16x16x32_bf16 v[152:155], a[104:107], a[56:59], v[152:155]\n" \
"v_exp_f32_e64 v149, v149\n" \
"v_mfma_f32_16x16x32_bf16 v[152:155], a[108:111], a[60:63], v[152:155]\n" \
"v_cvt_pk_bf16_f32 v200, v144, v145\n" \
"v_cvt_pk_bf16_f32 v201, v146, v147\n" \
"v_mfma_f32_16x16x32_bf16 v[156:159], a[124:127], a[60:63], v[172:175]\n" \
"v_exp_f32_e64 v150, v150\n" \
"v_mfma_f32_16x16x32_bf16 v[156:159], a[120:123], a[56:59], v[156:159]\n" \
"ds_read_b64_tr_b16 a[172:173], v211 offset:41472\n" \
"v_mfma_f32_16x16x32_bf16 v[156:159], a[116:119], a[52:55], v[156:159]\n" \
"buffer_load_dwordx4 v215, %[srdv], s48 offen offset:3072 lds\n" \
"v_mfma_f32_16x16x32_bf16 v[156:159], a[112:115], a[48:51], v[156:159]\n" \
"v_exp_f32_e64 v151, v151\n" \
"v_mfma_f32_16x16x32_bf16 v[112:115], a[176:179], v[204:207], v[112:115]\n" \
"ds_read_b64_tr_b16 a[174:175], v211 offset:45568\n" \
"ds_read_b64_tr_b16 a[176:177], %[vvb] offset:41984\n" \
"v_mfma_f32_16x16x32_bf16 v[112:115], a[144:147], v[220:223], v[112:115]\n" \
"v_exp_f32_e64 v152, v152\n" \
"v_mfma_f32_16x16x32_bf16 v[116:119], a[180:183], v[204:207], v[116:119]\n" \
"v_cvt_pk_bf16_f32 v202, v148, v149\n" \
"v_cvt_pk_bf16_f32 v203, v150, v151\n" \
"v_mfma_f32_16x16x32_bf16 v[116:119], a[148:151], v[220:223], v[116:119]\n" \
"v_exp_f32_e64 v153, v153\n" \
"v_mfma_f32_16x16x32_bf16 v[120:123], a[184:187], v[204:207], v[120:123]\n" \
"v_exp_f32_e64 v154, v154\n" \
"v_mfma_f32_16x16x32_bf16 v[120:123], a[152:155], v[220:223], v[120:123]\n" \
"ds_read_b64_tr_b16 a[178:179], %[vvb] offset:46080\n" \
"v_mfma_f32_16x16x32_bf16 v[124:127], a[188:191], v[204:207], v[124:127]\n" \
"v_exp_f32_e64 v155, v155\n" \
"v_mfma_f32_16x16x32_bf16 v[124:127], a[156:159], v[220:223], v[124:127]\n" \
"ds_read_b64_tr_b16 a[180:181], v211 offset:41984\n" \
"s_waitcnt lgkmcnt(13)\n" \
"v_mfma_f32_16x16x32_bf16 v[128:131], a[64:67], a[0:3], v[160:163]\n" \
"v_exp_f32_e32 v156, v156\n" \
"v_mfma_f32_16x16x32_bf16 v[128:131], a[68:71], a[4:7], v[128:131]\n" \
"ds_read_b64_tr_b16 a[182:183], v211 offset:46080\n" \
"s_add_u32 m0, %[m0b], 32768\n" \
"v_mfma_f32_16x16x32_bf16 v[128:131], a[72:75], a[8:11], v[128:131]\n" \
"v_exp_f32_e64 v157, v157\n" \
"v_mfma_f32_16x16x32_bf16 v[128:131], a[76:79], a[12:15], v[128:131]\n" \
"v_cvt_pk_bf16_f32 v204, v152, v153\n" \
"v_cvt_pk_bf16_f32 v205, v154, v155\n" \
"v_mfma_f32_16x16x32_bf16 v[132:135], a[92:95], a[12:15], v[160:163]\n" \
"v_exp_f32_e64 v158, v158\n" \
"v_mfma_f32_16x16x32_bf16 v[132:135], a[88:91], a[8:11], v[132:135]\n" \
"ds_read_b64_tr_b16 a[184:185], %[vvb] offset:42496\n" \
"s_waitcnt lgkmcnt(5)\n" \
"v_mfma_f32_16x16x32_bf16 v[132:135], a[84:87], a[4:7], v[132:135]\n" \
"buffer_load_dwordx4 %[kd0], %[srdk], s48 offen offset:0 lds\n" \
"v_mfma_f32_16x16x32_bf16 v[132:135], a[80:83], a[0:3], v[132:135]\n" \
"v_exp_f32_e32 v159, v159\n" \
"v_mfma_f32_16x16x32_bf16 v[64:67], a[128:131], v[216:219], v[64:67]\n" \
"ds_read_b64_tr_b16 a[186:187], %[vvb] offset:46592\n" \
"ds_read_b64_tr_b16 a[188:189], v211 offset:42496\n" \
"v_mfma_f32_16x16x32_bf16 v[64:67], a[160:163], v[200:203], v[64:67]\n" \
"v_exp_f32_e64 v128, v128\n" \
"v_mfma_f32_16x16x32_bf16 v[68:71], a[132:135], v[216:219], v[68:71]\n" \
"v_cvt_pk_bf16_f32 v206, v156, v157\n" \
"v_cvt_pk_bf16_f32 v207, v158, v159\n" \
"v_mfma_f32_16x16x32_bf16 v[68:71], a[164:167], v[200:203], v[68:71]\n" \
"v_exp_f32_e64 v129, v129\n" \
"v_mfma_f32_16x16x32_bf16 v[72:75], a[136:139], v[216:219], v[72:75]\n" \
"ds_read_b64_tr_b16 a[190:191], v211 offset:46592\n" \
"ds_read_b64_tr_b16 a[128:129], %[vvb] offset:49152\n" \
"v_mfma_f32_16x16x32_bf16 v[72:75], a[168:171], v[200:203], v[72:75]\n" \
"v_exp_f32_e64 v130, v130\n" \
"v_mfma_f32_16x16x32_bf16 v[76:79], a[140:143], v[216:219], v[76:79]\n" \
"ds_read_b64_tr_b16 a[130:131], %[vvb] offset:53248\n" \
"ds_read_b64_tr_b16 a[132:133], v211 offset:49152\n" \
"v_mfma_f32_16x16x32_bf16 v[76:79], a[172:175], v[200:203], v[76:79]\n" \
"v_exp_f32_e64 v131, v131\n" \
"v_smfmac_f32_16x16x64_bf16 v[176:179], a[192:195], v[192:199], v228\n" \
"ds_read_b64_tr_b16 a[134:135], v211 offset:53248\n" \
"ds_read_b64_tr_b16 a[136:137], %[vvb] offset:49664\n" \
"buffer_store_dwordx4 a[196:199], v235, %[srdz], s62 offen offset:1024 sc1\n" \
"v_mfma_f32_16x16x32_bf16 v[136:139], a[64:67], a[16:19], v[164:167]\n" \
"v_exp_f32_e64 v132, v132\n" \
"v_mfma_f32_16x16x32_bf16 v[136:139], a[68:71], a[20:23], v[136:139]\n" \
"ds_read_b64_tr_b16 a[138:139], %[vvb] offset:53760\n" \
"ds_read_b64_tr_b16 a[140:141], v211 offset:49664\n" \
"v_mfma_f32_16x16x32_bf16 v[136:139], a[72:75], a[24:27], v[136:139]\n" \
"v_exp_f32_e64 v133, v133\n" \
"v_mfma_f32_16x16x32_bf16 v[136:139], a[76:79], a[28:31], v[136:139]\n" \
"v_cvt_pk_bf16_f32 v184, v128, v129\n" \
"v_cvt_pk_bf16_f32 v185, v130, v131\n" \
"v_mfma_f32_16x16x32_bf16 v[140:143], a[92:95], a[28:31], v[164:167]\n" \
"v_exp_f32_e64 v134, v134\n" \
"v_mfma_f32_16x16x32_bf16 v[140:143], a[88:91], a[24:27], v[140:143]\n" \
"ds_read_b64_tr_b16 a[142:143], v211 offset:53760\n" \
"s_waitcnt lgkmcnt(8)\n" \
"v_mfma_f32_16x16x32_bf16 v[140:143], a[84:87], a[20:23], v[140:143]\n" \
"buffer_load_dwordx4 v212, %[srdk], s48 offen offset:1024 lds\n" \
"v_mfma_f32_16x16x32_bf16 v[140:143], a[80:83], a[16:19], v[140:143]\n" \
"v_exp_f32_e32 v135, v135\n" \
"v_mfma_f32_16x16x32_bf16 v[80:83], a[144:147], v[216:219], v[80:83]\n" \
"ds_read_b64_tr_b16 a[144:145], %[vvb] offset:50176\n" \
"ds_read_b64_tr_b16 a[146:147], %[vvb] offset:54272\n" \
"v_mfma_f32_16x16x32_bf16 v[80:83], a[176:179], v[200:203], v[80:83]\n" \
"v_exp_f32_e64 v136, v136\n" \
"v_mfma_f32_16x16x32_bf16 v[84:87], a[148:151], v[216:219], v[84:87]\n" \
"v_cvt_pk_bf16_f32 v186, v132, v133\n" \
"v_cvt_pk_bf16_f32 v187, v134, v135\n" \
"v_mfma_f32_16x16x32_bf16 v[84:87], a[180:183], v[200:203], v[84:87]\n" \
"v_exp_f32_e64 v137, v137\n" \
"v_mfma_f32_16x16x32_bf16 v[88:91], a[152:155], v[216:219], v[88:91]\n" \
"v_exp_f32_e64 v138, v138\n" \
"v_mfma_f32_16x16x32_bf16 v[88:91], a[184:187], v[200:203], v[88:91]\n" \
"ds_read_b64_tr_b16 a[148:149], v211 offset:50176\n" \
"ds_read_b64_tr_b16 a[150:151], v211 offset:54272\n" \
"v_mfma_f32_16x16x32_bf16 v[92:95], a[156:159], v[216:219], v[92:95]\n" \
"v_exp_f32_e64 v139, v139\n" \
"v_mfma_f32_16x16x32_bf16 v[92:95], a[188:191], v[200:203], v[92:95]\n" \
"ds_read_b64_tr_b16 a[152:153], %[vvb] offset:50688\n" \
"global_load_dword v237, %[vct], %[pct]\n" \
"v_mfma_f32_16x16x32_bf16 v[144:147], a[64:67], a[32:35], v[168:171]\n" \
"v_exp_f32_e64 v140, v140\n" \
"v_mfma_f32_16x16x32_bf16 v[144:147], a[68:71], a[36:39], v[144:147]\n" \
"ds_read_b64_tr_b16 a[154:155], %[vvb] offset:54784\n" \
"ds_read_b64_tr_b16 a[156:157], v211 offset:50688\n" \
"v_mfma_f32_16x16x32_bf16 v[144:147], a[72:75], a[40:43], v[144:147]\n" \
"v_exp_f32_e64 v141, v141\n" \
"v_mfma_f32_16x16x32_bf16 v[144:147], a[76:79], a[44:47], v[144:147]\n" \
"v_cvt_pk_bf16_f32 v188, v136, v137\n" \
"v_cvt_pk_bf16_f32 v189, v138, v139\n" \
"v_mfma_f32_16x16x32_bf16 v[148:151], a[92:95], a[44:47], v[168:171]\n" \
"v_exp_f32_e64 v142, v142\n" \
"v_mfma_f32_16x16x32_bf16 v[148:151], a[88:91], a[40:43], v[148:151]\n" \
"ds_read_b64_tr_b16 a[158:159], v211 offset:54784\n" \
"s_waitcnt lgkmcnt(8)\n" \
"v_mfma_f32_16x16x32_bf16 v[148:151], a[84:87], a[36:39], v[148:151]\n" \
"buffer_load_dwordx4 v213, %[srdk], s48 offen offset:2048 lds\n" \
"v_mfma_f32_16x16x32_bf16 v[148:151], a[80:83], a[32:35], v[148:151]\n" \
"v_exp_f32_e32 v143, v143\n" \
"v_mfma_f32_16x16x32_bf16 v[0:3], a[160:163], v[192:195], v[0:3]\n" \
"ds_read_b128 a[96:99], %[kb0] offset:57344\n" \
"v_mfma_f32_16x16x32_bf16 v[0:3], a[128:131], v[184:187], v[0:3]\n" \
"v_exp_f32_e64 v144, v144\n" \
"v_mfma_f32_16x16x32_bf16 v[4:7], a[164:167], v[192:195], v[4:7]\n" \
"v_cvt_pk_bf16_f32 v190, v140, v141\n" \
"v_cvt_pk_bf16_f32 v191, v142, v143\n" \
"v_mfma_f32_16x16x32_bf16 v[4:7], a[132:135], v[184:187], v[4:7]\n" \
"v_exp_f32_e64 v145, v145\n" \
"v_mfma_f32_16x16x32_bf16 v[8:11], a[168:171], v[192:195], v[8:11]\n" \
"ds_read_b128 a[100:103], v208 offset:57344\n" \
"v_mfma_f32_16x16x32_bf16 v[8:11], a[136:139], v[184:187], v[8:11]\n" \
"v_exp_f32_e64 v146, v146\n" \
"v_mfma_f32_16x16x32_bf16 v[12:15], a[172:175], v[192:195], v[12:15]\n" \
"ds_read_b128 a[104:107], v209 offset:57344\n" \
"v_mfma_f32_16x16x32_bf16 v[12:15], a[140:143], v[184:187], v[12:15]\n" \
"v_exp_f32_e64 v147, v147\n" \
"v_smfmac_f32_16x16x64_bf16 v[180:183], a[192:195], v[200:207], v228\n" \
"ds_read_b128 a[108:111], v210 offset:57344\n" \
"v_mfma_f32_16x16x32_bf16 v[152:155], a[64:67], a[48:51], v[172:175]\n" \
"v_exp_f32_e64 v148, v148\n" \
"v_mfma_f32_16x16x32_bf16 v[152:155], a[68:71], a[52:55], v[152:155]\n" \
"ds_read_b128 a[124:127], v210 offset:61440\n" \
"v_mfma_f32_16x16x32_bf16 v[152:155], a[72:75], a[56:59], v[152:155]\n" \
"v_exp_f32_e64 v149, v149\n" \
"v_mfma_f32_16x16x32_bf16 v[152:155], a[76:79], a[60:63], v[152:155]\n" \
"v_cvt_pk_bf16_f32 v216, v144, v145\n" \
"v_cvt_pk_bf16_f32 v217, v146, v147\n" \
"v_mfma_f32_16x16x32_bf16 v[156:159], a[92:95], a[60:63], v[172:175]\n" \
"v_exp_f32_e64 v150, v150\n" \
"v_mfma_f32_16x16x32_bf16 v[156:159], a[88:91], a[56:59], v[156:159]\n" \
"ds_read_b128 a[120:123], v209 offset:61440\n" \
"s_waitcnt lgkmcnt(6)\n" \
"v_mfma_f32_16x16x32_bf16 v[156:159], a[84:87], a[52:55], v[156:159]\n" \
"buffer_load_dwordx4 v214, %[srdk], s48 offen offset:3072 lds\n" \
"v_mfma_f32_16x16x32_bf16 v[156:159], a[80:83], a[48:51], v[156:159]\n" \
"v_exp_f32_e32 v151, v151\n" \
"v_mfma_f32_16x16x32_bf16 v[16:19], a[176:179], v[192:195], v[16:19]\n" \
"ds_read_b128 a[116:119], v208 offset:61440\n" \
"v_mfma_f32_16x16x32_bf16 v[16:19], a[144:147], v[184:187], v[16:19]\n" \
"v_exp_f32_e64 v152, v152\n" \
"v_mfma_f32_16x16x32_bf16 v[20:23], a[180:183], v[192:195], v[20:23]\n" \
"v_cvt_pk_bf16_f32 v218, v148, v149\n" \
"v_cvt_pk_bf16_f32 v219, v150, v151\n" \
"v_mfma_f32_16x16x32_bf16 v[20:23], a[148:151], v[184:187], v[20:23]\n" \
"v_exp_f32_e64 v153, v153\n" \
"v_mfma_f32_16x16x32_bf16 v[24:27], a[184:187], v[192:195], v[24:27]\n" \
"v_exp_f32_e64 v154, v154\n" \
"v_mfma_f32_16x16x32_bf16 v[24:27], a[152:155], v[184:187], v[24:27]\n" \
"ds_read_b128 a[112:115], %[kb0] offset:61440\n" \
"s_add_u32 s48, s48, 0x4000\n" \
"s_mov_b32 m0, s44\n" \
"s_addk_i32 s44, 0x400\n" \
"buffer_load_dwordx4 v229, s[64:67], s45 offen offset:0 lds\n" \
"s_add_u32 s45, s45, 229376\n" \
"v_mfma_f32_16x16x32_bf16 v[28:31], a[188:191], v[192:195], v[28:31]\n" \
"v_exp_f32_e64 v155, v155\n" \
"v_mfma_f32_16x16x32_bf16 v[28:31], a[156:159], v[184:187], v[28:31]\n" \
"s_waitcnt vmcnt(16) lgkmcnt(2)\n" \
"s_barrier\n" \
"v_mfma_f32_16x16x32_bf16 v[128:131], a[96:99], a[0:3], v[160:163]\n" \
"v_exp_f32_e64 v156, v156\n" \
"v_mfma_f32_16x16x32_bf16 v[128:131], a[100:103], a[4:7], v[128:131]\n" \
"s_add_u32 m0, %[m0b], 98304\n" \
"v_mfma_f32_16x16x32_bf16 v[128:131], a[104:107], a[8:11], v[128:131]\n" \
"v_exp_f32_e64 v157, v157\n" \
"v_mfma_f32_16x16x32_bf16 v[128:131], a[108:111], a[12:15], v[128:131]\n" \
"v_cvt_pk_bf16_f32 v220, v152, v153\n" \
"v_cvt_pk_bf16_f32 v221, v154, v155\n" \
"v_mfma_f32_16x16x32_bf16 v[132:135], a[124:127], a[12:15], v[160:163]\n" \
"v_exp_f32_e64 v158, v158\n" \
"v_mfma_f32_16x16x32_bf16 v[132:135], a[120:123], a[8:11], v[132:135]\n" \
"s_waitcnt lgkmcnt(0)\n" \
"v_mfma_f32_16x16x32_bf16 v[132:135], a[116:119], a[4:7], v[132:135]\n" \
"buffer_load_dwordx4 %[vd0], %[srdv], s48 offen offset:0 lds\n" \
"v_mfma_f32_16x16x32_bf16 v[132:135], a[112:115], a[0:3], v[132:135]\n" \
"v_exp_f32_e32 v159, v159\n" \
"v_mfma_f32_16x16x32_bf16 v[32:35], a[160:163], v[196:199], v[32:35]\n" \
"ds_read_b128 a[64:67], %[kb0] offset:0\n" \
"v_mfma_f32_16x16x32_bf16 v[32:35], a[128:131], v[188:191], v[32:35]\n" \
"v_exp_f32_e64 v128, v128\n" \
"v_mfma_f32_16x16x32_bf16 v[36:39], a[164:167], v[196:199], v[36:39]\n" \
"v_cvt_pk_bf16_f32 v222, v156, v157\n" \
"v_cvt_pk_bf16_f32 v223, v158, v159\n" \
"v_mfma_f32_16x16x32_bf16 v[36:39], a[132:135], v[188:191], v[36:39]\n" \
"v_exp_f32_e64 v129, v129\n" \
"v_mfma_f32_16x16x32_bf16 v[40:43], a[168:171], v[196:199], v[40:43]\n" \
"ds_read_b128 a[68:71], v208 offset:0\n" \
"v_mfma_f32_16x16x32_bf16 v[40:43], a[136:139], v[188:191], v[40:43]\n" \
"v_exp_f32_e64 v130, v130\n" \
"v_mfma_f32_16x16x32_bf16 v[44:47], a[172:175], v[196:199], v[44:47]\n" \
"ds_read_b128 a[72:75], v209 offset:0\n" \
"v_mfma_f32_16x16x32_bf16 v[44:47], a[140:143], v[188:191], v[44:47]\n" \
"v_exp_f32_e64 v131, v131\n" \
"v_smfmac_f32_16x16x64_bf16 v[176:179], a[192:195], v[184:191], v228\n" \
"buffer_store_dwordx4 a[196:199], v235, %[srdz], s63 offen offset:2048 sc1\n" \
"s_add_u32 s63, s63, 3072\n" \
"v_mfma_f32_16x16x32_bf16 v[136:139], a[96:99], a[16:19], v[164:167]\n" \
"v_exp_f32_e64 v132, v132\n" \
"v_mfma_f32_16x16x32_bf16 v[136:139], a[100:103], a[20:23], v[136:139]\n" \
"ds_read_b128 a[76:79], v210 offset:0\n" \
"v_mfma_f32_16x16x32_bf16 v[136:139], a[104:107], a[24:27], v[136:139]\n" \
"v_exp_f32_e64 v133, v133\n" \
"v_mfma_f32_16x16x32_bf16 v[136:139], a[108:111], a[28:31], v[136:139]\n" \
"v_cvt_pk_bf16_f32 v192, v128, v129\n" \
"v_cvt_pk_bf16_f32 v193, v130, v131\n" \
"v_mfma_f32_16x16x32_bf16 v[140:143], a[124:127], a[28:31], v[164:167]\n" \
"v_exp_f32_e64 v134, v134\n" \
"v_mfma_f32_16x16x32_bf16 v[140:143], a[120:123], a[24:27], v[140:143]\n" \
"ds_read_b128 a[92:95], v210 offset:4096\n" \
"v_mfma_f32_16x16x32_bf16 v[140:143], a[116:119], a[20:23], v[140:143]\n" \
"buffer_load_dwordx4 v215, %[srdv], s48 offen offset:1024 lds\n" \
"v_mfma_f32_16x16x32_bf16 v[140:143], a[112:115], a[16:19], v[140:143]\n" \
"v_exp_f32_e64 v135, v135\n" \
"v_mfma_f32_16x16x32_bf16 v[48:51], a[176:179], v[196:199], v[48:51]\n" \
"ds_read_b128 a[88:91], v209 offset:4096\n" \
"v_mfma_f32_16x16x32_bf16 v[48:51], a[144:147], v[188:191], v[48:51]\n" \
"v_exp_f32_e64 v136, v136\n" \
"v_mfma_f32_16x16x32_bf16 v[52:55], a[180:183], v[196:199], v[52:55]\n" \
"v_cvt_pk_bf16_f32 v194, v132, v133\n" \
"v_cvt_pk_bf16_f32 v195, v134, v135\n" \
"v_mfma_f32_16x16x32_bf16 v[52:55], a[148:151], v[188:191], v[52:55]\n" \
"v_exp_f32_e64 v137, v137\n" \
"v_mfma_f32_16x16x32_bf16 v[56:59], a[184:187], v[196:199], v[56:59]\n" \
"v_exp_f32_e64 v138, v138\n" \
"v_mfma_f32_16x16x32_bf16 v[56:59], a[152:155], v[188:191], v[56:59]\n" \
"ds_read_b128 a[84:87], v208 offset:4096\n" \
"v_mfma_f32_16x16x32_bf16 v[60:63], a[188:191], v[196:199], v[60:63]\n" \
"v_exp_f32_e64 v139, v139\n" \
"v_mfma_f32_16x16x32_bf16 v[60:63], a[156:159], v[188:191], v[60:63]\n" \
"s_cmp_lg_u32 %[wid], 2\n" \
"s_cbranch_scc1 LtB7_%=\n" \
"global_load_dword v237, v236, s[58:59]\n" \
"s_add_u32 s61, s48, 49152\n" \
"s_mov_b32 exec_hi, 0\n" \
"s_nop 0\n" \
"buffer_load_dword v237, v224, %[srdp], s61 offen\n" \
"s_mov_b32 exec_hi, -1\n" \
"s_nop 0\n" \
"LtB7_%=:\n" \
"v_mfma_f32_16x16x32_bf16 v[144:147], a[96:99], a[32:35], v[168:171]\n" \
"v_exp_f32_e64 v140, v140\n" \
"v_mfma_f32_16x16x32_bf16 v[144:147], a[100:103], a[36:39], v[144:147]\n" \
"ds_read_b128 a[80:83], %[kb0] offset:4096\n" \
"v_mfma_f32_16x16x32_bf16 v[144:147], a[104:107], a[40:43], v[144:147]\n" \
"v_exp_f32_e64 v141, v141\n" \
"v_mfma_f32_16x16x32_bf16 v[144:147], a[108:111], a[44:47], v[144:147]\n" \
"v_cvt_pk_bf16_f32 v196, v136, v137\n" \
"v_cvt_pk_bf16_f32 v197, v138, v139\n" \
"v_mfma_f32_16x16x32_bf16 v[148:151], a[124:127], a[44:47], v[168:171]\n" \
"v_exp_f32_e64 v142, v142\n" \
"v_mfma_f32_16x16x32_bf16 v[148:151], a[120:123], a[40:43], v[148:151]\n" \
"v_mfma_f32_16x16x32_bf16 v[148:151], a[116:119], a[36:39], v[148:151]\n" \
"buffer_load_dwordx4 %[vd0], %[srdv], s48 offen offset:2048 lds\n" \
"v_mfma_f32_16x16x32_bf16 v[148:151], a[112:115], a[32:35], v[148:151]\n" \
"v_exp_f32_e64 v143, v143\n" \
"v_mfma_f32_16x16x32_bf16 v[96:99], a[160:163], v[204:207], v[96:99]\n" \
"ds_read_b64_tr_b16 a[160:161], %[vvb] offset:57344\n" \
"v_mfma_f32_16x16x32_bf16 v[96:99], a[128:131], v[220:223], v[96:99]\n" \
"v_exp_f32_e64 v144, v144\n" \
"v_mfma_f32_16x16x32_bf16 v[100:103], a[164:167], v[204:207], v[100:103]\n" \
"v_cvt_pk_bf16_f32 v198, v140, v141\n" \
"v_cvt_pk_bf16_f32 v199, v142, v143\n" \
"v_mfma_f32_16x16x32_bf16 v[100:103], a[132:135], v[220:223], v[100:103]\n" \
"v_exp_f32_e64 v145, v145\n" \
"v_mfma_f32_16x16x32_bf16 v[104:107], a[168:171], v[204:207], v[104:107]\n" \
"ds_read_b64_tr_b16 a[162:163], %[vvb] offset:61440\n" \
"ds_read_b64_tr_b16 a[164:165], v211 offset:57344\n" \
"v_mfma_f32_16x16x32_bf16 v[104:107], a[136:139], v[220:223], v[104:107]\n" \
"v_exp_f32_e64 v146, v146\n" \
"v_mfma_f32_16x16x32_bf16 v[108:111], a[172:175], v[204:207], v[108:111]\n" \
"ds_read_b64_tr_b16 a[166:167], v211 offset:61440\n" \
"v_mfma_f32_16x16x32_bf16 v[108:111], a[140:143], v[220:223], v[108:111]\n" \
"v_exp_f32_e64 v147, v147\n" \
"v_smfmac_f32_16x16x64_bf16 v[180:183], a[192:195], v[216:223], v228\n" \
"ds_read_b64_tr_b16 a[168:169], %[vvb] offset:57856\n" \
"v_mfma_f32_16x16x32_bf16 v[152:155], a[96:99], a[48:51], v[172:175]\n" \
"v_exp_f32_e64 v148, v148\n" \
"v_mfma_f32_16x16x32_bf16 v[152:155], a[100:103], a[52:55], v[152:155]\n" \
"ds_read_b64_tr_b16 a[170:171], %[vvb] offset:61952\n" \
"v_mfma_f32_16x16x32_bf16 v[152:155], a[104:107], a[56:59], v[152:155]\n" \
"v_exp_f32_e64 v149, v149\n" \
"v_mfma_f32_16x16x32_bf16 v[152:155], a[108:111], a[60:63], v[152:155]\n" \
"v_cvt_pk_bf16_f32 v200, v144, v145\n" \
"v_cvt_pk_bf16_f32 v201, v146, v147\n" \
"v_mfma_f32_16x16x32_bf16 v[156:159], a[124:127], a[60:63], v[172:175]\n" \
"v_exp_f32_e64 v150, v150\n" \
"v_mfma_f32_16x16x32_bf16 v[156:159], a[120:123], a[56:59], v[156:159]\n" \
"ds_read_b64_tr_b16 a[172:173], v211 offset:57856\n" \
"v_mfma_f32_16x16x32_bf16 v[156:159], a[116:119], a[52:55], v[156:159]\n" \
"buffer_load_dwordx4 v215, %[srdv], s48 offen offset:3072 lds\n" \
"v_mfma_f32_16x16x32_bf16 v[156:159], a[112:115], a[48:51], v[156:159]\n" \
"v_exp_f32_e64 v151, v151\n" \
"v_mfma_f32_16x16x32_bf16 v[112:115], a[176:179], v[204:207], v[112:115]\n" \
"ds_read_b64_tr_b16 a[174:175], v211 offset:61952\n" \
"ds_read_b64_tr_b16 a[176:177], %[vvb] offset:58368\n" \
"v_mfma_f32_16x16x32_bf16 v[112:115], a[144:147], v[220:223], v[112:115]\n" \
"v_exp_f32_e64 v152, v152\n" \
"v_mfma_f32_16x16x32_bf16 v[116:119], a[180:183], v[204:207], v[116:119]\n" \
"v_cvt_pk_bf16_f32 v202, v148, v149\n" \
"v_cvt_pk_bf16_f32 v203, v150, v151\n" \
".if . - adaln_body != 28672\n" \
".error \"size model\"\n" \
".endif\n" \
"Lk6_%=:\n" \
"s_cbranch_execz Lk7_%=\n" \
"s_nop 0\n" \
"v_mfma_f32_16x16x32_bf16 v[116:119], a[148:151], v[220:223], v[116:119]\n" \
"v_exp_f32_e64 v153, v153\n" \
"v_mfma_f32_16x16x32_bf16 v[120:123], a[184:187], v[204:207], v[120:123]\n" \
"v_exp_f32_e64 v154, v154\n" \
"v_mfma_f32_16x16x32_bf16 v[120:123], a[152:155], v[220:223], v[120:123]\n" \
"ds_read_b64_tr_b16 a[178:179], %[vvb] offset:62464\n" \
"v_mfma_f32_16x16x32_bf16 v[124:127], a[188:191], v[204:207], v[124:127]\n" \
"v_exp_f32_e64 v155, v155\n" \
"v_mfma_f32_16x16x32_bf16 v[124:127], a[156:159], v[220:223], v[124:127]\n" \
"ds_read_b64_tr_b16 a[180:181], v211 offset:58368\n" \
"s_waitcnt lgkmcnt(13)\n" \
"v_mfma_f32_16x16x32_bf16 v[128:131], a[64:67], a[0:3], v[160:163]\n" \
"v_exp_f32_e32 v156, v156\n" \
"v_mfma_f32_16x16x32_bf16 v[128:131], a[68:71], a[4:7], v[128:131]\n" \
"ds_read_b64_tr_b16 a[182:183], v211 offset:62464\n" \
"s_add_u32 m0, %[m0b], 49152\n" \
"v_mfma_f32_16x16x32_bf16 v[128:131], a[72:75], a[8:11], v[128:131]\n" \
"v_exp_f32_e64 v157, v157\n" \
"v_mfma_f32_16x16x32_bf16 v[128:131], a[76:79], a[12:15], v[128:131]\n" \
"v_cvt_pk_bf16_f32 v204, v152, v153\n" \
"v_cvt_pk_bf16_f32 v205, v154, v155\n" \
"v_mfma_f32_16x16x32_bf16 v[132:135], a[92:95], a[12:15], v[160:163]\n" \
"v_exp_f32_e64 v158, v158\n" \
"v_mfma_f32_16x16x32_bf16 v[132:135], a[88:91], a[8:11], v[132:135]\n" \
"ds_read_b64_tr_b16 a[184:185], %[vvb] offset:58880\n" \
"s_waitcnt lgkmcnt(5)\n" \
"v_mfma_f32_16x16x32_bf16 v[132:135], a[84:87], a[4:7], v[132:135]\n" \
"buffer_load_dwordx4 %[kd0], %[srdk], s48 offen offset:0 lds\n" \
"v_mfma_f32_16x16x32_bf16 v[132:135], a[80:83], a[0:3], v[132:135]\n" \
"v_exp_f32_e32 v159, v159\n" \
"v_mfma_f32_16x16x32_bf16 v[64:67], a[128:131], v[216:219], v[64:67]\n" \
"ds_read_b64_tr_b16 a[186:187], %[vvb] offset:62976\n" \
"ds_read_b64_tr_b16 a[188:189], v211 offset:58880\n" \
"v_mfma_f32_16x16x32_bf16 v[64:67], a[160:163], v[200:203], v[64:67]\n" \
"v_exp_f32_e64 v128, v128\n" \
"v_mfma_f32_16x16x32_bf16 v[68:71], a[132:135], v[216:219], v[68:71]\n" \
"v_cvt_pk_bf16_f32 v206, v156, v157\n" \
"v_cvt_pk_bf16_f32 v207, v158, v159\n" \
"v_mfma_f32_16x16x32_bf16 v[68:71], a[164:167], v[200:203], v[68:71]\n" \
"v_exp_f32_e64 v129, v129\n" \
"v_mfma_f32_16x16x32_bf16 v[72:75], a[136:139], v[216:219], v[72:75]\n" \
"ds_read_b64_tr_b16 a[190:191], v211 offset:62976\n" \
"ds_read_b64_tr_b16 a[128:129], %[vvb] offset:0\n" \
"v_mfma_f32_16x16x32_bf16 v[72:75], a[168:171], v[200:203], v[72:75]\n" \
"v_exp_f32_e64 v130, v130\n" \
"v_mfma_f32_16x16x32_bf16 v[76:79], a[140:143], v[216:219], v[76:79]\n" \
"ds_read_b64_tr_b16 a[130:131], %[vvb] offset:4096\n" \
"ds_read_b64_tr_b16 a[132:133], v211 offset:0\n" \
"v_mfma_f32_16x16x32_bf16 v[76:79], a[172:175], v[200:203], v[76:79]\n" \
"v_exp_f32_e64 v131, v131\n" \
"v_smfmac_f32_16x16x64_bf16 v[176:179], a[192:195], v[192:199], v228\n" \
"ds_read_b64_tr_b16 a[134:135], v211 offset:4096\n" \
"ds_read_b64_tr_b16 a[136:137], %[vvb] offset:512\n" \
"v_mfma_f32_16x16x32_bf16 v[136:139], a[64:67], a[16:19], v[164:167]\n" \
"v_exp_f32_e64 v132, v132\n" \
"v_mfma_f32_16x16x32_bf16 v[136:139], a[68:71], a[20:23], v[136:139]\n" \
"ds_read_b64_tr_b16 a[138:139], %[vvb] offset:4608\n" \
"ds_read_b64_tr_b16 a[140:141], v211 offset:512\n" \
"v_mfma_f32_16x16x32_bf16 v[136:139], a[72:75], a[24:27], v[136:139]\n" \
"v_exp_f32_e64 v133, v133\n" \
"v_mfma_f32_16x16x32_bf16 v[136:139], a[76:79], a[28:31], v[136:139]\n" \
"v_cvt_pk_bf16_f32 v184, v128, v129\n" \
"v_cvt_pk_bf16_f32 v185, v130, v131\n" \
"v_mfma_f32_16x16x32_bf16 v[140:143], a[92:95], a[28:31], v[164:167]\n" \
"v_exp_f32_e64 v134, v134\n" \
"v_mfma_f32_16x16x32_bf16 v[140:143], a[88:91], a[24:27], v[140:143]\n" \
"ds_read_b64_tr_b16 a[142:143], v211 offset:4608\n" \
"s_waitcnt lgkmcnt(8)\n" \
"v_mfma_f32_16x16x32_bf16 v[140:143], a[84:87], a[20:23], v[140:143]\n" \
"buffer_load_dwordx4 v212, %[srdk], s48 offen offset:1024 lds\n" \
"v_mfma_f32_16x16x32_bf16 v[140:143], a[80:83], a[16:19], v[140:143]\n" \
"v_exp_f32_e32 v135, v135\n" \
"v_mfma_f32_16x16x32_bf16 v[80:83], a[144:147], v[216:219], v[80:83]\n" \
"ds_read_b64_tr_b16 a[144:145], %[vvb] offset:1024\n" \
"ds_read_b64_tr_b16 a[146:147], %[vvb] offset:5120\n" \
"v_mfma_f32_16x16x32_bf16 v[80:83], a[176:179], v[200:203], v[80:83]\n" \
"v_exp_f32_e64 v136, v136\n" \
"v_mfma_f32_16x16x32_bf16 v[84:87], a[148:151], v[216:219], v[84:87]\n" \
"v_cvt_pk_bf16_f32 v186, v132, v133\n" \
"v_cvt_pk_bf16_f32 v187, v134, v135\n" \
"v_mfma_f32_16x16x32_bf16 v[84:87], a[180:183], v[200:203], v[84:87]\n" \
"v_exp_f32_e64 v137, v137\n" \
"v_mfma_f32_16x16x32_bf16 v[88:91], a[152:155], v[216:219], v[88:91]\n" \
"v_exp_f32_e64 v138, v138\n" \
"v_mfma_f32_16x16x32_bf16 v[88:91], a[184:187], v[200:203], v[88:91]\n" \
"ds_read_b64_tr_b16 a[148:149], v211 offset:1024\n" \
"ds_read_b64_tr_b16 a[150:151], v211 offset:5120\n" \
"v_mfma_f32_16x16x32_bf16 v[92:95], a[156:159], v[216:219], v[92:95]\n" \
"v_exp_f32_e64 v139, v139\n" \
"v_mfma_f32_16x16x32_bf16 v[92:95], a[188:191], v[200:203], v[92:95]\n" \
"ds_read_b64_tr_b16 a[152:153], %[vvb] offset:1536\n" \
"buffer_store_dwordx4 a[196:199], v235, %[srdz], s62 offen offset:2048 sc1\n" \
"s_add_u32 s62, s62, 3072\n" \
"v_mfma_f32_16x16x32_bf16 v[144:147], a[64:67], a[32:35], v[168:171]\n" \
"v_exp_f32_e64 v140, v140\n" \
"v_mfma_f32_16x16x32_bf16 v[144:147], a[68:71], a[36:39], v[144:147]\n" \
"ds_read_b64_tr_b16 a[154:155], %[vvb] offset:5632\n" \
"ds_read_b64_tr_b16 a[156:157], v211 offset:1536\n" \
"v_mfma_f32_16x16x32_bf16 v[144:147], a[72:75], a[40:43], v[144:147]\n" \
"v_exp_f32_e64 v141, v141\n" \
"v_mfma_f32_16x16x32_bf16 v[144:147], a[76:79], a[44:47], v[144:147]\n" \
"v_cvt_pk_bf16_f32 v188, v136, v137\n" \
"v_cvt_pk_bf16_f32 v189, v138, v139\n" \
"v_mfma_f32_16x16x32_bf16 v[148:151], a[92:95], a[44:47], v[168:171]\n" \
"v_exp_f32_e64 v142, v142\n" \
"v_mfma_f32_16x16x32_bf16 v[148:151], a[88:91], a[40:43], v[148:151]\n" \
"ds_read_b64_tr_b16 a[158:159], v211 offset:5632\n" \
"s_waitcnt lgkmcnt(8)\n" \
"v_mfma_f32_16x16x32_bf16 v[148:151], a[84:87], a[36:39], v[148:151]\n" \
"buffer_load_dwordx4 v213, %[srdk], s48 offen offset:2048 lds\n" \
"v_mfma_f32_16x16x32_bf16 v[148:151], a[80:83], a[32:35], v[148:151]\n" \
"v_exp_f32_e32 v143, v143\n" \
"v_mfma_f32_16x16x32_bf16 v[0:3], a[160:163], v[192:195], v[0:3]\n" \
"ds_read_b128 a[96:99], %[kb0] offset:8192\n" \
"v_mfma_f32_16x16x32_bf16 v[0:3], a[128:131], v[184:187], v[0:3]\n" \
"v_exp_f32_e64 v144, v144\n" \
"v_mfma_f32_16x16x32_bf16 v[4:7], a[164:167], v[192:195], v[4:7]\n" \
"v_cvt_pk_bf16_f32 v190, v140, v141\n" \
"v_cvt_pk_bf16_f32 v191, v142, v143\n" \
"v_mfma_f32_16x16x32_bf16 v[4:7], a[132:135], v[184:187], v[4:7]\n" \
"v_exp_f32_e64 v145, v145\n" \
"v_mfma_f32_16x16x32_bf16 v[8:11], a[168:171], v[192:195], v[8:11]\n" \
"ds_read_b128 a[100:103], v208 offset:8192\n" \
"v_mfma_f32_16x16x32_bf16 v[8:11], a[136:139], v[184:187], v[8:11]\n" \
"v_exp_f32_e64 v146, v146\n" \
"v_mfma_f32_16x16x32_bf16 v[12:15], a[172:175], v[192:195], v[12:15]\n" \
"ds_read_b128 a[104:107], v209 offset:8192\n" \
"v_mfma_f32_16x16x32_bf16 v[12:15], a[140:143], v[184:187], v[12:15]\n" \
"v_exp_f32_e64 v147, v147\n" \
"v_smfmac_f32_16x16x64_bf16 v[180:183], a[192:195], v[200:207], v228\n" \
"ds_read_b128 a[108:111], v210 offset:8192\n" \
"v_mfma_f32_16x16x32_bf16 v[152:155], a[64:67], a[48:51], v[172:175]\n" \
"v_exp_f32_e64 v148, v148\n" \
"v_mfma_f32_16x16x32_bf16 v[152:155], a[68:71], a[52:55], v[152:155]\n" \
"ds_read_b128 a[124:127], v210 offset:12288\n" \
"v_mfma_f32_16x16x32_bf16 v[152:155], a[72:75], a[56:59], v[152:155]\n" \
"v_exp_f32_e64 v149, v149\n" \
"v_mfma_f32_16x16x32_bf16 v[152:155], a[76:79], a[60:63], v[152:155]\n" \
"v_cvt_pk_bf16_f32 v216, v144, v145\n" \
"v_cvt_pk_bf16_f32 v217, v146, v147\n" \
"v_mfma_f32_16x16x32_bf16 v[156:159], a[92:95], a[60:63], v[172:175]\n" \
"v_exp_f32_e64 v150, v150\n" \
"v_mfma_f32_16x16x32_bf16 v[156:159], a[88:91], a[56:59], v[156:159]\n" \
"ds_read_b128 a[120:123], v209 offset:12288\n" \
"s_waitcnt lgkmcnt(6)\n" \
"v_mfma_f32_16x16x32_bf16 v[156:159], a[84:87], a[52:55], v[156:159]\n" \
"buffer_load_dwordx4 v214, %[srdk], s48 offen offset:3072 lds\n" \
"v_mfma_f32_16x16x32_bf16 v[156:159], a[80:83], a[48:51], v[156:159]\n" \
"v_exp_f32_e32 v151, v151\n" \
"v_mfma_f32_16x16x32_bf16 v[16:19], a[176:179], v[192:195], v[16:19]\n" \
"ds_read_b128 a[116:119], v208 offset:12288\n" \
"v_mfma_f32_16x16x32_bf16 v[16:19], a[144:147], v[184:187], v[16:19]\n" \
"v_exp_f32_e64 v152, v152\n" \
"v_mfma_f32_16x16x32_bf16 v[20:23], a[180:183], v[192:195], v[20:23]\n" \
"v_cvt_pk_bf16_f32 v218, v148, v149\n" \
"v_cvt_pk_bf16_f32 v219, v150, v151\n" \
"v_mfma_f32_16x16x32_bf16 v[20:23], a[148:151], v[184:187], v[20:23]\n" \
"v_exp_f32_e64 v153, v153\n" \
"v_mfma_f32_16x16x32_bf16 v[24:27], a[184:187], v[192:195], v[24:27]\n" \
"v_exp_f32_e64 v154, v154\n" \
"v_mfma_f32_16x16x32_bf16 v[24:27], a[152:155], v[184:187], v[24:27]\n" \
"ds_read_b128 a[112:115], %[kb0] offset:12288\n" \
"s_add_u32 s48, s48, 0x4000\n" \
"v_mfma_f32_16x16x32_bf16 v[28:31], a[188:191], v[192:195], v[28:31]\n" \
"v_exp_f32_e64 v155, v155\n" \
"v_mfma_f32_16x16x32_bf16 v[28:31], a[156:159], v[184:187], v[28:31]\n" \
"s_waitcnt vmcnt(17) lgkmcnt(2)\n" \
"s_barrier\n" \
"v_mfma_f32_16x16x32_bf16 v[128:131], a[96:99], a[0:3], v[160:163]\n" \
"v_exp_f32_e64 v156, v156\n" \
"v_mfma_f32_16x16x32_bf16 v[128:131], a[100:103], a[4:7], v[128:131]\n" \
"s_add_u32 m0, %[m0b], 114688\n" \
"v_mfma_f32_16x16x32_bf16 v[128:131], a[104:107], a[8:11], v[128:131]\n" \
"v_exp_f32_e64 v157, v157\n" \
"v_mfma_f32_16x16x32_bf16 v[128:131], a[108:111], a[12:15], v[128:131]\n" \
"v_cvt_pk_bf16_f32 v220, v152, v153\n" \
"v_cvt_pk_bf16_f32 v221, v154, v155\n" \
"v_mfma_f32_16x16x32_bf16 v[132:135], a[124:127], a[12:15], v[160:163]\n" \
"v_exp_f32_e64 v158, v158\n" \
"v_mfma_f32_16x16x32_bf16 v[132:135], a[120:123], a[8:11], v[132:135]\n" \
"s_waitcnt lgkmcnt(0)\n" \
"v_mfma_f32_16x16x32_bf16 v[132:135], a[116:119], a[4:7], v[132:135]\n" \
"buffer_load_dwordx4 %[vd0], %[srdv], s48 offen offset:0 lds\n" \
"v_mfma_f32_16x16x32_bf16 v[132:135], a[112:115], a[0:3], v[132:135]\n" \
"v_exp_f32_e32 v159, v159\n" \
"v_mfma_f32_16x16x32_bf16 v[32:35], a[160:163], v[196:199], v[32:35]\n" \
"ds_read_b128 a[64:67], %[kb0] offset:16384\n" \
"v_mfma_f32_16x16x32_bf16 v[32:35], a[128:131], v[188:191], v[32:35]\n" \
"v_exp_f32_e64 v128, v128\n" \
"v_mfma_f32_16x16x32_bf16 v[36:39], a[164:167], v[196:199], v[36:39]\n" \
"v_cvt_pk_bf16_f32 v222, v156, v157\n" \
"v_cvt_pk_bf16_f32 v223, v158, v159\n" \
"v_mfma_f32_16x16x32_bf16 v[36:39], a[132:135], v[188:191], v[36:39]\n" \
"v_exp_f32_e64 v129, v129\n" \
"v_mfma_f32_16x16x32_bf16 v[40:43], a[168:171], v[196:199], v[40:43]\n" \
"ds_read_b128 a[68:71], v208 offset:16384\n" \
"v_mfma_f32_16x16x32_bf16 v[40:43], a[136:139], v[188:191], v[40:43]\n" \
"v_exp_f32_e64 v130, v130\n" \
"v_mfma_f32_16x16x32_bf16 v[44:47], a[172:175], v[196:199], v[44:47]\n" \
"ds_read_b128 a[72:75], v209 offset:16384\n" \
"v_mfma_f32_16x16x32_bf16 v[44:47], a[140:143], v[188:191], v[44:47]\n" \
"v_exp_f32_e64 v131, v131\n" \
"v_smfmac_f32_16x16x64_bf16 v[176:179], a[192:195], v[184:191], v228\n" \
"v_mfma_f32_16x16x32_bf16 v[136:139], a[96:99], a[16:19], v[164:167]\n" \
"v_exp_f32_e64 v132, v132\n" \
"v_mfma_f32_16x16x32_bf16 v[136:139], a[100:103], a[20:23], v[136:139]\n" \
"ds_read_b128 a[76:79], v210 offset:16384\n" \
"v_mfma_f32_16x16x32_bf16 v[136:139], a[104:107], a[24:27], v[136:139]\n" \
"v_exp_f32_e64 v133, v133\n" \
"v_mfma_f32_16x16x32_bf16 v[136:139], a[108:111], a[28:31], v[136:139]\n" \
"v_cvt_pk_bf16_f32 v192, v128, v129\n" \
"v_cvt_pk_bf16_f32 v193, v130, v131\n" \
"v_mfma_f32_16x16x32_bf16 v[140:143], a[124:127], a[28:31], v[164:167]\n" \
"v_exp_f32_e64 v134, v134\n" \
"v_mfma_f32_16x16x32_bf16 v[140:143], a[120:123], a[24:27], v[140:143]\n" \
"ds_read_b128 a[92:95], v210 offset:20480\n" \
"v_mfma_f32_16x16x32_bf16 v[140:143], a[116:119], a[20:23], v[140:143]\n" \
"buffer_load_dwordx4 v215, %[srdv], s48 offen offset:1024 lds\n" \
"v_mfma_f32_16x16x32_bf16 v[140:143], a[112:115], a[16:19], v[140:143]\n" \
"v_exp_f32_e64 v135, v135\n" \
"v_mfma_f32_16x16x32_bf16 v[48:51], a[176:179], v[196:199], v[48:51]\n" \
"ds_read_b128 a[88:91], v209 offset:20480\n" \
"v_mfma_f32_16x16x32_bf16 v[48:51], a[144:147], v[188:191], v[48:51]\n" \
"v_exp_f32_e64 v136, v136\n" \
"v_mfma_f32_16x16x32_bf16 v[52:55], a[180:183], v[196:199], v[52:55]\n" \
"v_cvt_pk_bf16_f32 v194, v132, v133\n" \
"v_cvt_pk_bf16_f32 v195, v134, v135\n" \
"v_mfma_f32_16x16x32_bf16 v[52:55], a[148:151], v[188:191], v[52:55]\n" \
"v_exp_f32_e64 v137, v137\n" \
"v_mfma_f32_16x16x32_bf16 v[56:59], a[184:187], v[196:199], v[56:59]\n" \
"v_exp_f32_e64 v138, v138\n" \
"v_mfma_f32_16x16x32_bf16 v[56:59], a[152:155], v[188:191], v[56:59]\n" \
"ds_read_b128 a[84:87], v208 offset:20480\n" \
"v_mfma_f32_16x16x32_bf16 v[60:63], a[188:191], v[196:199], v[60:63]\n" \
"v_exp_f32_e64 v139, v139\n" \
"v_mfma_f32_16x16x32_bf16 v[60:63], a[156:159], v[188:191], v[60:63]\n" \
"s_cmp_lg_u32 %[wid], 3\n" \
"s_cbranch_scc1 LtB1_%=\n" \
"global_load_dword v237, v236, s[58:59]\n" \
"s_add_u32 s61, s48, 49152\n" \
"s_mov_b32 exec_hi, 0\n" \
"s_nop 0\n" \
"buffer_load_dword v237, v224, %[srdp], s61 offen\n" \
"s_mov_b32 exec_hi, -1\n" \
"s_nop 0\n" \
"LtB1_%=:\n" \
"v_mfma_f32_16x16x32_bf16 v[144:147], a[96:99], a[32:35], v[168:171]\n" \
"v_exp_f32_e64 v140, v140\n" \
"v_mfma_f32_16x16x32_bf16 v[144:147], a[100:103], a[36:39], v[144:147]\n" \
"ds_read_b128 a[80:83], %[kb0] offset:20480\n" \
"v_mfma_f32_16x16x32_bf16 v[144:147], a[104:107], a[40:43], v[144:147]\n" \
"v_exp_f32_e64 v141, v141\n" \
"v_mfma_f32_16x16x32_bf16 v[144:147], a[108:111], a[44:47], v[144:147]\n" \
"v_cvt_pk_bf16_f32 v196, v136, v137\n" \
"v_cvt_pk_bf16_f32 v197, v138, v139\n" \
"v_mfma_f32_16x16x32_bf16 v[148:151], a[124:127], a[44:47], v[168:171]\n" \
"v_exp_f32_e64 v142, v142\n" \
"v_mfma_f32_16x16x32_bf16 v[148:151], a[120:123], a[40:43], v[148:151]\n" \
"v_mfma_f32_16x16x32_bf16 v[148:151], a[116:119], a[36:39], v[148:151]\n" \
"buffer_load_dwordx4 %[vd0], %[srdv], s48 offen offset:2048 lds\n" \
"v_mfma_f32_16x16x32_bf16 v[148:151], a[112:115], a[32:35], v[148:151]\n" \
"v_exp_f32_e64 v143, v143\n" \
"v_mfma_f32_16x16x32_bf16 v[96:99], a[160:163], v[204:207], v[96:99]\n" \
"ds_read_b64_tr_b16 a[160:161], %[vvb] offset:8192\n" \
"v_mfma_f32_16x16x32_bf16 v[96:99], a[128:131], v[220:223], v[96:99]\n" \
"v_exp_f32_e64 v144, v144\n" \
"v_mfma_f32_16x16x32_bf16 v[100:103], a[164:167], v[204:207], v[100:103]\n" \
"v_cvt_pk_bf16_f32 v198, v140, v141\n" \
"v_cvt_pk_bf16_f32 v199, v142, v143\n" \
"v_mfma_f32_16x16x32_bf16 v[100:103], a[132:135], v[220:223], v[100:103]\n" \
"v_exp_f32_e64 v145, v145\n" \
"v_mfma_f32_16x16x32_bf16 v[104:107], a[168:171], v[204:207], v[104:107]\n" \
"ds_read_b64_tr_b16 a[162:163], %[vvb] offset:12288\n" \
"ds_read_b64_tr_b16 a[164:165], v211 offset:8192\n" \
"v_mfma_f32_16x16x32_bf16 v[104:107], a[136:139], v[220:223], v[104:107]\n" \
"v_exp_f32_e64 v146, v146\n" \
"v_mfma_f32_16x16x32_bf16 v[108:111], a[172:175], v[204:207], v[108:111]\n" \
"ds_read_b64_tr_b16 a[166:167], v211 offset:12288\n" \
"v_mfma_f32_16x16x32_bf16 v[108:111], a[140:143], v[220:223], v[108:111]\n" \
"v_exp_f32_e64 v147, v147\n" \
"v_smfmac_f32_16x16x64_bf16 v[180:183], a[192:195], v[216:223], v228\n" \
"ds_read_b64_tr_b16 a[168:169], %[vvb] offset:8704\n" \
"v_mfma_f32_16x16x32_bf16 v[152:155], a[96:99], a[48:51], v[172:175]\n" \
"v_exp_f32_e64 v148, v148\n" \
"v_mfma_f32_16x16x32_bf16 v[152:155], a[100:103], a[52:55], v[152:155]\n" \
"ds_read_b64_tr_b16 a[170:171], %[vvb] offset:12800\n" \
"v_mfma_f32_16x16x32_bf16 v[152:155], a[104:107], a[56:59], v[152:155]\n" \
"v_exp_f32_e64 v149, v149\n" \
"v_mfma_f32_16x16x32_bf16 v[152:155], a[108:111], a[60:63], v[152:155]\n" \
"v_cvt_pk_bf16_f32 v200, v144, v145\n" \
"v_cvt_pk_bf16_f32 v201, v146, v147\n" \
"v_mfma_f32_16x16x32_bf16 v[156:159], a[124:127], a[60:63], v[172:175]\n" \
"v_exp_f32_e64 v150, v150\n" \
"v_mfma_f32_16x16x32_bf16 v[156:159], a[120:123], a[56:59], v[156:159]\n" \
"ds_read_b64_tr_b16 a[172:173], v211 offset:8704\n" \
"v_mfma_f32_16x16x32_bf16 v[156:159], a[116:119], a[52:55], v[156:159]\n" \
"buffer_load_dwordx4 v215, %[srdv], s48 offen offset:3072 lds\n" \
"v_mfma_f32_16x16x32_bf16 v[156:159], a[112:115], a[48:51], v[156:159]\n" \
"v_exp_f32_e64 v151, v151\n" \
"v_mfma_f32_16x16x32_bf16 v[112:115], a[176:179], v[204:207], v[112:115]\n" \
"ds_read_b64_tr_b16 a[174:175], v211 offset:12800\n" \
"ds_read_b64_tr_b16 a[176:177], %[vvb] offset:9216\n" \
"v_mfma_f32_16x16x32_bf16 v[112:115], a[144:147], v[220:223], v[112:115]\n" \
"v_exp_f32_e64 v152, v152\n" \
"v_mfma_f32_16x16x32_bf16 v[116:119], a[180:183], v[204:207], v[116:119]\n" \
"v_cvt_pk_bf16_f32 v202, v148, v149\n" \
"v_cvt_pk_bf16_f32 v203, v150, v151\n" \
"v_mfma_f32_16x16x32_bf16 v[116:119], a[148:151], v[220:223], v[116:119]\n" \
"v_exp_f32_e64 v153, v153\n" \
"v_mfma_f32_16x16x32_bf16 v[120:123], a[184:187], v[204:207], v[120:123]\n" \
"v_exp_f32_e64 v154, v154\n" \
"v_mfma_f32_16x16x32_bf16 v[120:123], a[152:155], v[220:223], v[120:123]\n" \
"ds_read_b64_tr_b16 a[178:179], %[vvb] offset:13312\n" \
"v_mfma_f32_16x16x32_bf16 v[124:127], a[188:191], v[204:207], v[124:127]\n" \
"v_exp_f32_e64 v155, v155\n" \
"v_mfma_f32_16x16x32_bf16 v[124:127], a[156:159], v[220:223], v[124:127]\n" \
"ds_read_b64_tr_b16 a[180:181], v211 offset:9216\n" \
"s_waitcnt lgkmcnt(13)\n" \
"s_cmp_lg_u32 s60, 7\n" \
"s_cbranch_scc1 LmB_%=\n" \
"s_nop 0\n" \
"v_readlane_b32 s48, %[tlo], 11\n" \
"v_readlane_b32 s58, %[tlo], 12\n" \
"v_mfma_f32_16x16x32_bf16 v[128:131], a[64:67], a[0:3], v[160:163]\n" \
"v_exp_f32_e64 v156, v156\n" \
"v_mfma_f32_16x16x32_bf16 v[128:131], a[68:71], a[4:7], v[128:131]\n" \
"ds_read_b64_tr_b16 a[182:183], v211 offset:13312\n" \
"s_add_u32 m0, %[m0b], 0\n" \
"s_mov_b32 s49, s48\n" \
"v_mfma_f32_16x16x32_bf16 v[128:131], a[72:75], a[8:11], v[128:131]\n" \
"v_exp_f32_e64 v157, v157\n" \
"v_mfma_f32_16x16x32_bf16 v[128:131], a[76:79], a[12:15], v[128:131]\n" \
"v_cvt_pk_bf16_f32 v204, v152, v153\n" \
"v_cvt_pk_bf16_f32 v205, v154, v155\n" \
"v_mfma_f32_16x16x32_bf16 v[132:135], a[92:95], a[12:15], v[160:163]\n" \
"v_exp_f32_e64 v158, v158\n" \
"v_mfma_f32_16x16x32_bf16 v[132:135], a[88:91], a[8:11], v[132:135]\n" \
"ds_read_b64_tr_b16 a[184:185], %[vvb] offset:9728\n" \
"s_lshl_b32 s49, s49, 18\n" \
"s_or_b32 s49, s49, s58\n" \
"s_waitcnt lgkmcnt(5)\n" \
"v_mfma_f32_16x16x32_bf16 v[132:135], a[84:87], a[4:7], v[132:135]\n" \
"buffer_load_dwordx4 %[wo0], %[srdt], s49 offen offset:0 lds\n" \
"v_mfma_f32_16x16x32_bf16 v[132:135], a[80:83], a[0:3], v[132:135]\n" \
"v_exp_f32_e32 v159, v159\n" \
"v_mfma_f32_16x16x32_bf16 v[64:67], a[128:131], v[216:219], v[64:67]\n" \
"ds_read_b64_tr_b16 a[186:187], %[vvb] offset:13824\n" \
"ds_read_b64_tr_b16 a[188:189], v211 offset:9728\n" \
"v_readlane_b32 s60, %[thi], 11\n" \
"v_mfma_f32_16x16x32_bf16 v[64:67], a[160:163], v[200:203], v[64:67]\n" \
"v_exp_f32_e64 v128, v128\n" \
"v_mfma_f32_16x16x32_bf16 v[68:71], a[132:135], v[216:219], v[68:71]\n" \
"v_cvt_pk_bf16_f32 v206, v156, v157\n" \
"v_cvt_pk_bf16_f32 v207, v158, v159\n" \
"v_mfma_f32_16x16x32_bf16 v[68:71], a[164:167], v[200:203], v[68:71]\n" \
"v_exp_f32_e64 v129, v129\n" \
"v_mfma_f32_16x16x32_bf16 v[72:75], a[136:139], v[216:219], v[72:75]\n" \
"ds_read_b64_tr_b16 a[190:191], v211 offset:13824\n" \
"ds_read_b64_tr_b16 a[128:129], %[vvb] offset:16384\n" \
"v_readlane_b32 s59, %[thi], 12\n" \
"v_mfma_f32_16x16x32_bf16 v[72:75], a[168:171], v[200:203], v[72:75]\n" \
"v_exp_f32_e64 v130, v130\n" \
"v_mfma_f32_16x16x32_bf16 v[76:79], a[140:143], v[216:219], v[76:79]\n" \
"ds_read_b64_tr_b16 a[130:131], %[vvb] offset:20480\n" \
"ds_read_b64_tr_b16 a[132:133], v211 offset:16384\n" \
"v_readlane_b32 s53, %[tlo], 13\n" \
"v_mfma_f32_16x16x32_bf16 v[76:79], a[172:175], v[200:203], v[76:79]\n" \
"v_exp_f32_e64 v131, v131\n" \
"v_smfmac_f32_16x16x64_bf16 v[176:179], a[192:195], v[192:199], v228\n" \
"ds_read_b64_tr_b16 a[134:135], v211 offset:20480\n" \
"ds_read_b64_tr_b16 a[136:137], %[vvb] offset:16896\n" \
"v_readlane_b32 s50, %[thi], 13\n" \
"v_mfma_f32_16x16x32_bf16 v[136:139], a[64:67], a[16:19], v[164:167]\n" \
"v_exp_f32_e64 v132, v132\n" \
"v_mfma_f32_16x16x32_bf16 v[136:139], a[68:71], a[20:23], v[136:139]\n" \
"ds_read_b64_tr_b16 a[138:139], %[vvb] offset:20992\n" \
"ds_read_b64_tr_b16 a[140:141], v211 offset:16896\n" \
"v_readlane_b32 s46, %[tlo], 14\n" \
"v_add_u32_e32 v212, 0x3c00, %[wo0]\n" \
"v_mfma_f32_16x16x32_bf16 v[136:139], a[72:75], a[24:27], v[136:139]\n" \
"v_exp_f32_e64 v133, v133\n" \
"v_mfma_f32_16x16x32_bf16 v[136:139], a[76:79], a[28:31], v[136:139]\n" \
"v_cvt_pk_bf16_f32 v184, v128, v129\n" \
"v_cvt_pk_bf16_f32 v185, v130, v131\n" \
"v_mfma_f32_16x16x32_bf16 v[140:143], a[92:95], a[28:31], v[164:167]\n" \
"v_exp_f32_e64 v134, v134\n" \
"v_mfma_f32_16x16x32_bf16 v[140:143], a[88:91], a[24:27], v[140:143]\n" \
"ds_read_b64_tr_b16 a[142:143], v211 offset:20992\n" \
"v_readlane_b32 s47, %[thi], 14\n" \
"v_add_u32_e32 v213, 0x7800, %[wo0]\n" \
"s_waitcnt lgkmcnt(8)\n" \
"v_mfma_f32_16x16x32_bf16 v[140:143], a[84:87], a[20:23], v[140:143]\n" \
"buffer_load_dwordx4 v212, %[srdt], s49 offen offset:1024 lds\n" \
"v_mfma_f32_16x16x32_bf16 v[140:143], a[80:83], a[16:19], v[140:143]\n" \
"v_exp_f32_e32 v135, v135\n" \
"v_mfma_f32_16x16x32_bf16 v[80:83], a[144:147], v[216:219], v[80:83]\n" \
"ds_read_b64_tr_b16 a[144:145], %[vvb] offset:17408\n" \
"ds_read_b64_tr_b16 a[146:147], %[vvb] offset:21504\n" \
"ds_read_b32 v230, %[rd0]\n" \
"v_mfma_f32_16x16x32_bf16 v[80:83], a[176:179], v[200:203], v[80:83]\n" \
"v_exp_f32_e64 v136, v136\n" \
"v_mfma_f32_16x16x32_bf16 v[84:87], a[148:151], v[216:219], v[84:87]\n" \
"v_cvt_pk_bf16_f32 v186, v132, v133\n" \
"v_cvt_pk_bf16_f32 v187, v134, v135\n" \
"v_mfma_f32_16x16x32_bf16 v[84:87], a[180:183], v[200:203], v[84:87]\n" \
"v_exp_f32_e64 v137, v137\n" \
"v_mfma_f32_16x16x32_bf16 v[88:91], a[152:155], v[216:219], v[88:91]\n" \
"v_exp_f32_e64 v138, v138\n" \
"v_mfma_f32_16x16x32_bf16 v[88:91], a[184:187], v[200:203], v[88:91]\n" \
"ds_read_b64_tr_b16 a[148:149], v211 offset:17408\n" \
"v_readlane_b32 s55, %[thi], 15\n" \
"ds_read_b64_tr_b16 a[150:151], v211 offset:21504\n" \
"v_lshrrev_b32_e64 v234, 5, v224\n" \
"v_mfma_f32_16x16x32_bf16 v[92:95], a[156:159], v[216:219], v[92:95]\n" \
"v_exp_f32_e64 v139, v139\n" \
"v_mfma_f32_16x16x32_bf16 v[92:95], a[188:191], v[200:203], v[92:95]\n" \
"ds_read_b64_tr_b16 a[152:153], %[vvb] offset:17920\n" \
"v_mfma_f32_16x16x32_bf16 v[144:147], a[64:67], a[32:35], v[168:171]\n" \
"v_exp_f32_e64 v140, v140\n" \
"v_mfma_f32_16x16x32_bf16 v[144:147], a[68:71], a[36:39], v[144:147]\n" \
"ds_read_b64_tr_b16 a[154:155], %[vvb] offset:22016\n" \
"ds_read_b64_tr_b16 a[156:157], v211 offset:17920\n" \
"s_waitcnt lgkmcnt(5)\n" \
"v_add_f32_dpp v230, v230, v230 row_ror:4 row_mask:0xf bank_mask:0xf\n" \
"v_mfma_f32_16x16x32_bf16 v[144:147], a[72:75], a[40:43], v[144:147]\n" \
"v_exp_f32_e32 v141, v141\n" \
"v_mfma_f32_16x16x32_bf16 v[144:147], a[76:79], a[44:47], v[144:147]\n" \
"v_cvt_pk_bf16_f32 v188, v136, v137\n" \
"v_cvt_pk_bf16_f32 v189, v138, v139\n" \
"v_mfma_f32_16x16x32_bf16 v[148:151], a[92:95], a[44:47], v[168:171]\n" \
"v_exp_f32_e64 v142, v142\n" \
"v_mfma_f32_16x16x32_bf16 v[148:151], a[88:91], a[40:43], v[148:151]\n" \
"ds_read_b64_tr_b16 a[158:159], v211 offset:22016\n" \
"v_add_f32_dpp v230, v230, v230 row_ror:8 row_mask:0xf bank_mask:0xf\n" \
"s_waitcnt lgkmcnt(9)\n" \
"v_mfma_f32_16x16x32_bf16 v[148:151], a[84:87], a[36:39], v[148:151]\n" \
"buffer_load_dwordx4 v213, %[srdt], s49 offen offset:2048 lds\n" \
"v_mfma_f32_16x16x32_bf16 v[148:151], a[80:83], a[32:35], v[148:151]\n" \
"v_exp_f32_e32 v143, v143\n" \
"v_mfma_f32_16x16x32_bf16 v[0:3], a[160:163], v[192:195], v[0:3]\n" \
"ds_read_b128 a[96:99], %[kb0] offset:24576\n" \
"v_cvt_pk_bf16_f32 v230, v230, v230\n" \
"v_mfma_f32_16x16x32_bf16 v[0:3], a[128:131], v[184:187], v[0:3]\n" \
"v_exp_f32_e64 v144, v144\n" \
"v_mfma_f32_16x16x32_bf16 v[4:7], a[164:167], v[192:195], v[4:7]\n" \
"v_cvt_pk_bf16_f32 v190, v140, v141\n" \
"v_cvt_pk_bf16_f32 v191, v142, v143\n" \
"v_mfma_f32_16x16x32_bf16 v[4:7], a[132:135], v[184:187], v[4:7]\n" \
"v_exp_f32_e64 v145, v145\n" \
"v_mfma_f32_16x16x32_bf16 v[8:11], a[168:171], v[192:195], v[8:11]\n" \
"ds_read_b128 a[100:103], v208 offset:24576\n" \
"v_lshlrev_b32_e64 v230, 16, v230\n" \
"v_mfma_f32_16x16x32_bf16 v[8:11], a[136:139], v[184:187], v[8:11]\n" \
"v_exp_f32_e64 v146, v146\n" \
"v_mfma_f32_16x16x32_bf16 v[12:15], a[172:175], v[192:195], v[12:15]\n" \
"ds_read_b128 a[104:107], v209 offset:24576\n" \
"v_add_f32_e64 v230, s55, v230\n" \
"v_add_u32_e32 v214, 0xb400, %[wo0]\n" \
"v_mfma_f32_16x16x32_bf16 v[12:15], a[140:143], v[184:187], v[12:15]\n" \
"v_exp_f32_e64 v147, v147\n" \
"v_smfmac_f32_16x16x64_bf16 v[180:183], a[192:195], v[200:207], v228\n" \
"ds_read_b128 a[108:111], v210 offset:24576\n" \
"v_cvt_pk_bf16_f32 v230, v230, v230\n" \
"v_readlane_b32 s55, %[tlo], 15\n" \
"v_mfma_f32_16x16x32_bf16 v[152:155], a[64:67], a[48:51], v[172:175]\n" \
"v_exp_f32_e64 v148, v148\n" \
"v_mfma_f32_16x16x32_bf16 v[152:155], a[68:71], a[52:55], v[152:155]\n" \
"ds_read_b128 a[124:127], v210 offset:28672\n" \
"v_lshl_or_b32 v230, v230, 16, 1\n" \
"v_mfma_f32_16x16x32_bf16 v[152:155], a[72:75], a[56:59], v[152:155]\n" \
"v_exp_f32_e64 v149, v149\n" \
"v_mfma_f32_16x16x32_bf16 v[152:155], a[76:79], a[60:63], v[152:155]\n" \
"v_cvt_pk_bf16_f32 v216, v144, v145\n" \
"v_cvt_pk_bf16_f32 v217, v146, v147\n" \
"v_mfma_f32_16x16x32_bf16 v[156:159], a[92:95], a[60:63], v[172:175]\n" \
"v_exp_f32_e64 v150, v150\n" \
"v_mfma_f32_16x16x32_bf16 v[156:159], a[88:91], a[56:59], v[156:159]\n" \
"ds_read_b128 a[120:123], v209 offset:28672\n" \
"s_cmp_lg_u32 s52, 0\n" \
"s_cselect_b32 s55, 0x80000000, s55\n" \
"s_mov_b64 exec, 15\n" \
"buffer_store_dword v230, v234, %[srdz], s55 offen sc1\n" \
"s_mov_b64 exec, -1\n" \
"s_waitcnt lgkmcnt(6)\n" \
"v_mfma_f32_16x16x32_bf16 v[156:159], a[84:87], a[52:55], v[156:159]\n" \
"buffer_load_dwordx4 v214, %[srdt], s49 offen offset:3072 lds\n" \
"v_mfma_f32_16x16x32_bf16 v[156:159], a[80:83], a[48:51], v[156:159]\n" \
"v_exp_f32_e64 v151, v151\n" \
"v_mfma_f32_16x16x32_bf16 v[16:19], a[176:179], v[192:195], v[16:19]\n" \
"ds_read_b128 a[116:119], v208 offset:28672\n" \
"v_mfma_f32_16x16x32_bf16 v[16:19], a[144:147], v[184:187], v[16:19]\n" \
"v_exp_f32_e64 v152, v152\n" \
"v_mfma_f32_16x16x32_bf16 v[20:23], a[180:183], v[192:195], v[20:23]\n" \
"v_cvt_pk_bf16_f32 v218, v148, v149\n" \
"v_cvt_pk_bf16_f32 v219, v150, v151\n" \
"v_mfma_f32_16x16x32_bf16 v[20:23], a[148:151], v[184:187], v[20:23]\n" \
"v_exp_f32_e64 v153, v153\n" \
"v_mfma_f32_16x16x32_bf16 v[24:27], a[184:187], v[192:195], v[24:27]\n" \
"v_exp_f32_e64 v154, v154\n" \
"v_mfma_f32_16x16x32_bf16 v[24:27], a[152:155], v[184:187], v[24:27]\n" \
"ds_read_b128 a[112:115], %[kb0] offset:28672\n" \
"s_mov_b32 m0, s44\n" \
"s_addk_i32 s44, 0x400\n" \
"buffer_load_dwordx4 v229, s[64:67], s45 offen offset:0 lds\n" \
"s_add_u32 s45, s45, 229376\n" \
"v_mfma_f32_16x16x32_bf16 v[28:31], a[188:191], v[192:195], v[28:31]\n" \
"v_exp_f32_e64 v155, v155\n" \
"v_mfma_f32_16x16x32_bf16 v[28:31], a[156:159], v[184:187], v[28:31]\n" \
"s_waitcnt vmcnt(6) lgkmcnt(2)\n" \
"s_barrier\n" \
"v_mfma_f32_16x16x32_bf16 v[128:131], a[96:99], a[0:3], v[160:163]\n" \
"v_exp_f32_e64 v156, v156\n" \
"s_mov_b32 m0, s44\n" \
"s_addk_i32 s44, 0x400\n" \
"buffer_load_dwordx4 v229, s[64:67], s45 offen offset:0 lds\n" \
"s_add_u32 s45, s45, 229376\n" \
"v_readlane_b32 s64, %[tlo], 8\n" \
"v_readlane_b32 s65, %[thi], 8\n" \
"v_mfma_f32_16x16x32_bf16 v[128:131], a[100:103], a[4:7], v[128:131]\n" \
"v_mbcnt_lo_u32_b32 v231, -1, 0\n" \
"v_mfma_f32_16x16x32_bf16 v[128:131], a[104:107], a[8:11], v[128:131]\n" \
"v_exp_f32_e64 v157, v157\n" \
"v_mfma_f32_16x16x32_bf16 v[128:131], a[108:111], a[12:15], v[128:131]\n" \
"v_cvt_pk_bf16_f32 v220, v152, v153\n" \
"v_cvt_pk_bf16_f32 v221, v154, v155\n" \
"v_mfma_f32_16x16x32_bf16 v[132:135], a[124:127], a[12:15], v[160:163]\n" \
"v_exp_f32_e64 v158, v158\n" \
"v_mfma_f32_16x16x32_bf16 v[132:135], a[120:123], a[8:11], v[132:135]\n" \
"v_mbcnt_hi_u32_b32 v231, -1, v231\n" \
"s_waitcnt lgkmcnt(0)\n" \
"v_mfma_f32_16x16x32_bf16 v[132:135], a[116:119], a[4:7], v[132:135]\n" \
"s_add_u32 m0, %[m0b], 65536\n" \
"s_mov_b32 s61, 0\n" \
"buffer_load_dwordx4 %[vg0], s[64:67], s61 offen offset:0 lds\n" \
"v_mfma_f32_16x16x32_bf16 v[132:135], a[112:115], a[0:3], v[132:135]\n" \
"v_exp_f32_e64 v159, v159\n" \
".if . - adaln_body != 32768\n" \
".error \"size model\"\n" \
".endif\n" \
"Lk7_%=:\n" \
"s_cbranch_execz Lk8_%=\n" \
"s_nop 0\n" \
"v_mfma_f32_16x16x32_bf16 v[32:35], a[160:163], v[196:199], v[32:35]\n" \
"ds_read_b128 a[64:67], %[kb0] offset:32768\n" \
"v_and_b32_e64 v226, 31, v231\n" \
"v_mfma_f32_16x16x32_bf16 v[32:35], a[128:131], v[188:191], v[32:35]\n" \
"v_exp_f32_e64 v128, v128\n" \
"v_mfma_f32_16x16x32_bf16 v[36:39], a[164:167], v[196:199], v[36:39]\n" \
"v_cvt_pk_bf16_f32 v222, v156, v157\n" \
"v_cvt_pk_bf16_f32 v223, v158, v159\n" \
"v_mfma_f32_16x16x32_bf16 v[36:39], a[132:135], v[188:191], v[36:39]\n" \
"v_exp_f32_e64 v129, v129\n" \
"v_mfma_f32_16x16x32_bf16 v[40:43], a[168:171], v[196:199], v[40:43]\n" \
"ds_read_b128 a[68:71], v208 offset:32768\n" \
"v_lshlrev_b32_e64 v226, 2, v226\n" \
"v_mfma_f32_16x16x32_bf16 v[40:43], a[136:139], v[188:191], v[40:43]\n" \
"v_exp_f32_e64 v130, v130\n" \
"v_mfma_f32_16x16x32_bf16 v[44:47], a[172:175], v[196:199], v[44:47]\n" \
"ds_read_b128 a[72:75], v209 offset:32768\n" \
"v_cmp_gt_u32_e32 vcc, 1, v231\n" \
"v_mfma_f32_16x16x32_bf16 v[44:47], a[140:143], v[188:191], v[44:47]\n" \
"v_exp_f32_e32 v131, v131\n" \
"v_smfmac_f32_16x16x64_bf16 v[176:179], a[192:195], v[184:191], v228\n" \
"v_mov_b32_e32 v225, 0x80000000\n" \
"v_mfma_f32_16x16x32_bf16 v[136:139], a[96:99], a[16:19], v[164:167]\n" \
"v_exp_f32_e64 v132, v132\n" \
"v_mfma_f32_16x16x32_bf16 v[136:139], a[100:103], a[20:23], v[136:139]\n" \
"ds_read_b128 a[76:79], v210 offset:32768\n" \
"v_mfma_f32_16x16x32_bf16 v[136:139], a[104:107], a[24:27], v[136:139]\n" \
"v_exp_f32_e64 v133, v133\n" \
"v_mfma_f32_16x16x32_bf16 v[136:139], a[108:111], a[28:31], v[136:139]\n" \
"v_cvt_pk_bf16_f32 v192, v128, v129\n" \
"v_cvt_pk_bf16_f32 v193, v130, v131\n" \
"v_mfma_f32_16x16x32_bf16 v[140:143], a[124:127], a[28:31], v[164:167]\n" \
"v_exp_f32_e64 v134, v134\n" \
"v_mfma_f32_16x16x32_bf16 v[140:143], a[120:123], a[24:27], v[140:143]\n" \
"ds_read_b128 a[92:95], v210 offset:36864\n" \
"v_cndmask_b32_e32 v225, v225, v224, vcc\n" \
"v_mfma_f32_16x16x32_bf16 v[140:143], a[116:119], a[20:23], v[140:143]\n" \
"s_add_u32 m0, %[m0b], 66560\n" \
"s_mov_b32 s61, 229376\n" \
"buffer_load_dwordx4 %[vg0], s[64:67], s61 offen offset:0 lds\n" \
"v_mfma_f32_16x16x32_bf16 v[140:143], a[112:115], a[16:19], v[140:143]\n" \
"v_exp_f32_e32 v135, v135\n" \
"v_mfma_f32_16x16x32_bf16 v[48:51], a[176:179], v[196:199], v[48:51]\n" \
"ds_read_b128 a[88:91], v209 offset:36864\n" \
"v_mfma_f32_16x16x32_bf16 v[48:51], a[144:147], v[188:191], v[48:51]\n" \
"v_exp_f32_e64 v136, v136\n" \
"v_mfma_f32_16x16x32_bf16 v[52:55], a[180:183], v[196:199], v[52:55]\n" \
"v_cvt_pk_bf16_f32 v194, v132, v133\n" \
"v_cvt_pk_bf16_f32 v195, v134, v135\n" \
"v_mfma_f32_16x16x32_bf16 v[52:55], a[148:151], v[188:191], v[52:55]\n" \
"v_exp_f32_e64 v137, v137\n" \
"v_mfma_f32_16x16x32_bf16 v[56:59], a[184:187], v[196:199], v[56:59]\n" \
"v_exp_f32_e64 v138, v138\n" \
"v_mfma_f32_16x16x32_bf16 v[56:59], a[152:155], v[188:191], v[56:59]\n" \
"ds_read_b128 a[84:87], v208 offset:36864\n" \
"v_mfma_f32_16x16x32_bf16 v[60:63], a[188:191], v[196:199], v[60:63]\n" \
"v_exp_f32_e64 v139, v139\n" \
"v_mfma_f32_16x16x32_bf16 v[60:63], a[156:159], v[188:191], v[60:63]\n" \
"v_mfma_f32_16x16x32_bf16 v[144:147], a[96:99], a[32:35], v[168:171]\n" \
"v_exp_f32_e64 v140, v140\n" \
"v_mfma_f32_16x16x32_bf16 v[144:147], a[100:103], a[36:39], v[144:147]\n" \
"ds_read_b128 a[80:83], %[kb0] offset:36864\n" \
"s_sub_u32 s61, s48, 0\n" \
"s_cmp_lt_u32 s61, 4\n" \
"s_cselect_b64 vcc, -1, 0\n" \
"v_mfma_f32_16x16x32_bf16 v[144:147], a[104:107], a[40:43], v[144:147]\n" \
"v_exp_f32_e32 v141, v141\n" \
"v_mfma_f32_16x16x32_bf16 v[144:147], a[108:111], a[44:47], v[144:147]\n" \
"v_cvt_pk_bf16_f32 v196, v136, v137\n" \
"v_cvt_pk_bf16_f32 v197, v138, v139\n" \
"v_mfma_f32_16x16x32_bf16 v[148:151], a[124:127], a[44:47], v[168:171]\n" \
"v_exp_f32_e64 v142, v142\n" \
"v_mfma_f32_16x16x32_bf16 v[148:151], a[120:123], a[40:43], v[148:151]\n" \
"v_readlane_b32 s55, %[tlo], 16\n" \
"v_mfma_f32_16x16x32_bf16 v[148:151], a[116:119], a[36:39], v[148:151]\n" \
"s_add_u32 m0, %[m0b], 67584\n" \
"s_mov_b32 s61, 458752\n" \
"buffer_load_dwordx4 %[vg0], s[64:67], s61 offen offset:0 lds\n" \
"v_mfma_f32_16x16x32_bf16 v[148:151], a[112:115], a[32:35], v[148:151]\n" \
"v_exp_f32_e64 v143, v143\n" \
"v_mfma_f32_16x16x32_bf16 v[96:99], a[160:163], v[204:207], v[96:99]\n" \
"ds_read_b64_tr_b16 a[160:161], %[vvb] offset:24576\n" \
"v_cmp_gt_u32_e64 s[56:57], 32, v231\n" \
"v_mfma_f32_16x16x32_bf16 v[96:99], a[128:131], v[220:223], v[96:99]\n" \
"v_exp_f32_e64 v144, v144\n" \
"v_mfma_f32_16x16x32_bf16 v[100:103], a[164:167], v[204:207], v[100:103]\n" \
"v_cvt_pk_bf16_f32 v198, v140, v141\n" \
"v_cvt_pk_bf16_f32 v199, v142, v143\n" \
"v_mfma_f32_16x16x32_bf16 v[100:103], a[132:135], v[220:223], v[100:103]\n" \
"v_exp_f32_e64 v145, v145\n" \
"v_mfma_f32_16x16x32_bf16 v[104:107], a[168:171], v[204:207], v[104:107]\n" \
"ds_read_b64_tr_b16 a[162:163], %[vvb] offset:28672\n" \
"ds_read_b64_tr_b16 a[164:165], v211 offset:24576\n" \
"v_lshlrev_b32_e64 v232, 2, v224\n" \
"v_mfma_f32_16x16x32_bf16 v[104:107], a[136:139], v[220:223], v[104:107]\n" \
"v_exp_f32_e64 v146, v146\n" \
"v_mfma_f32_16x16x32_bf16 v[108:111], a[172:175], v[204:207], v[108:111]\n" \
"ds_read_b64_tr_b16 a[166:167], v211 offset:28672\n" \
"v_mov_b32_e32 v233, 0x80000000\n" \
"v_mfma_f32_16x16x32_bf16 v[108:111], a[140:143], v[220:223], v[108:111]\n" \
"v_exp_f32_e64 v147, v147\n" \
"v_smfmac_f32_16x16x64_bf16 v[180:183], a[192:195], v[216:223], v228\n" \
"ds_read_b64_tr_b16 a[168:169], %[vvb] offset:25088\n" \
"v_mfma_f32_16x16x32_bf16 v[152:155], a[96:99], a[48:51], v[172:175]\n" \
"v_exp_f32_e64 v148, v148\n" \
"v_mfma_f32_16x16x32_bf16 v[152:155], a[100:103], a[52:55], v[152:155]\n" \
"ds_read_b64_tr_b16 a[170:171], %[vvb] offset:29184\n" \
"v_cndmask_b32_e64 v232, v233, v232, s[56:57]\n" \
"v_mfma_f32_16x16x32_bf16 v[152:155], a[104:107], a[56:59], v[152:155]\n" \
"v_exp_f32_e64 v149, v149\n" \
"v_mfma_f32_16x16x32_bf16 v[152:155], a[108:111], a[60:63], v[152:155]\n" \
"v_cvt_pk_bf16_f32 v200, v144, v145\n" \
"v_cvt_pk_bf16_f32 v201, v146, v147\n" \
"v_mfma_f32_16x16x32_bf16 v[156:159], a[124:127], a[60:63], v[172:175]\n" \
"v_exp_f32_e64 v150, v150\n" \
"v_mfma_f32_16x16x32_bf16 v[156:159], a[120:123], a[56:59], v[156:159]\n" \
"ds_read_b64_tr_b16 a[172:173], v211 offset:25088\n" \
"v_mov_b32_e64 v230, 1\n" \
"v_mfma_f32_16x16x32_bf16 v[156:159], a[116:119], a[52:55], v[156:159]\n" \
"s_add_u32 m0, %[m0b], 68608\n" \
"s_mov_b32 s61, 688128\n" \
"buffer_load_dwordx4 %[vg0], s[64:67], s61 offen offset:0 lds\n" \
"v_mfma_f32_16x16x32_bf16 v[156:159], a[112:115], a[48:51], v[156:159]\n" \
"v_exp_f32_e64 v151, v151\n" \
"v_mfma_f32_16x16x32_bf16 v[112:115], a[176:179], v[204:207], v[112:115]\n" \
"ds_read_b64_tr_b16 a[174:175], v211 offset:29184\n" \
"ds_read_b64_tr_b16 a[176:177], %[vvb] offset:25600\n" \
"v_mfma_f32_16x16x32_bf16 v[112:115], a[144:147], v[220:223], v[112:115]\n" \
"v_exp_f32_e64 v152, v152\n" \
"v_mfma_f32_16x16x32_bf16 v[116:119], a[180:183], v[204:207], v[116:119]\n" \
"v_cvt_pk_bf16_f32 v202, v148, v149\n" \
"v_cvt_pk_bf16_f32 v203, v150, v151\n" \
"v_mfma_f32_16x16x32_bf16 v[116:119], a[148:151], v[220:223], v[116:119]\n" \
"v_exp_f32_e64 v153, v153\n" \
"v_mfma_f32_16x16x32_bf16 v[120:123], a[184:187], v[204:207], v[120:123]\n" \
"v_exp_f32_e64 v154, v154\n" \
"v_mfma_f32_16x16x32_bf16 v[120:123], a[152:155], v[220:223], v[120:123]\n" \
"ds_read_b64_tr_b16 a[178:179], %[vvb] offset:29696\n" \
"buffer_store_dword v230, v232, %[srdz], s55 offen sc1\n" \
"v_mfma_f32_16x16x32_bf16 v[124:127], a[188:191], v[204:207], v[124:127]\n" \
"v_exp_f32_e64 v155, v155\n" \
"v_mfma_f32_16x16x32_bf16 v[124:127], a[156:159], v[220:223], v[124:127]\n" \
"ds_read_b64_tr_b16 a[180:181], v211 offset:25600\n" \
"s_waitcnt lgkmcnt(13)\n" \
"v_mfma_f32_16x16x32_bf16 v[128:131], a[64:67], a[0:3], v[160:163]\n" \
"v_exp_f32_e32 v156, v156\n" \
"v_mfma_f32_16x16x32_bf16 v[128:131], a[68:71], a[4:7], v[128:131]\n" \
"ds_read_b64_tr_b16 a[182:183], v211 offset:29696\n" \
"s_add_u32 m0, %[m0b], 16384\n" \
"s_mov_b32 s49, s48\n" \
"v_mfma_f32_16x16x32_bf16 v[128:131], a[72:75], a[8:11], v[128:131]\n" \
"v_exp_f32_e32 v157, v157\n" \
"v_mfma_f32_16x16x32_bf16 v[128:131], a[76:79], a[12:15], v[128:131]\n" \
"v_cvt_pk_bf16_f32 v204, v152, v153\n" \
"v_cvt_pk_bf16_f32 v205, v154, v155\n" \
"v_mfma_f32_16x16x32_bf16 v[132:135], a[92:95], a[12:15], v[160:163]\n" \
"v_exp_f32_e64 v158, v158\n" \
"v_mfma_f32_16x16x32_bf16 v[132:135], a[88:91], a[8:11], v[132:135]\n" \
"ds_read_b64_tr_b16 a[184:185], %[vvb] offset:26112\n" \
"s_lshl_b32 s49, s49, 18\n" \
"s_or_b32 s49, s49, s59\n" \
"s_waitcnt lgkmcnt(5)\n" \
"v_mfma_f32_16x16x32_bf16 v[132:135], a[84:87], a[4:7], v[132:135]\n" \
"buffer_load_dwordx4 %[wo0], %[srdt], s49 offen offset:0 lds\n" \
"v_mfma_f32_16x16x32_bf16 v[132:135], a[80:83], a[0:3], v[132:135]\n" \
"v_exp_f32_e32 v159, v159\n" \
"v_mfma_f32_16x16x32_bf16 v[64:67], a[128:131], v[216:219], v[64:67]\n" \
"ds_read_b64_tr_b16 a[186:187], %[vvb] offset:30208\n" \
"ds_read_b64_tr_b16 a[188:189], v211 offset:26112\n" \
"v_xor_b32_e32 v232, 0x80, %[vx0]\n" \
"v_mfma_f32_16x16x32_bf16 v[64:67], a[160:163], v[200:203], v[64:67]\n" \
"v_exp_f32_e64 v128, v128\n" \
"v_mfma_f32_16x16x32_bf16 v[68:71], a[132:135], v[216:219], v[68:71]\n" \
"v_cvt_pk_bf16_f32 v206, v156, v157\n" \
"v_cvt_pk_bf16_f32 v207, v158, v159\n" \
"v_mfma_f32_16x16x32_bf16 v[68:71], a[164:167], v[200:203], v[68:71]\n" \
"v_exp_f32_e64 v129, v129\n" \
"v_mfma_f32_16x16x32_bf16 v[72:75], a[136:139], v[216:219], v[72:75]\n" \
"ds_read_b64_tr_b16 a[190:191], v211 offset:30208\n" \
"ds_read_b64_tr_b16 a[128:129], %[vvb] offset:32768\n" \
"v_mfma_f32_16x16x32_bf16 v[72:75], a[168:171], v[200:203], v[72:75]\n" \
"v_exp_f32_e64 v130, v130\n" \
"v_mfma_f32_16x16x32_bf16 v[76:79], a[140:143], v[216:219], v[76:79]\n" \
"ds_read_b64_tr_b16 a[130:131], %[vvb] offset:36864\n" \
"ds_read_b64_tr_b16 a[132:133], v211 offset:32768\n" \
"v_mfma_f32_16x16x32_bf16 v[76:79], a[172:175], v[200:203], v[76:79]\n" \
"v_exp_f32_e64 v131, v131\n" \
"v_smfmac_f32_16x16x64_bf16 v[176:179], a[192:195], v[192:199], v228\n" \
"ds_read_b64_tr_b16 a[134:135], v211 offset:36864\n" \
"ds_read_b64_tr_b16 a[136:137], %[vvb] offset:33280\n" \
"global_load_dwordx2 a[240:241], %[vx0], %[px] offset:0\n" \
"v_mfma_f32_16x16x32_bf16 v[136:139], a[64:67], a[16:19], v[164:167]\n" \
"v_exp_f32_e64 v132, v132\n" \
"v_mfma_f32_16x16x32_bf16 v[136:139], a[68:71], a[20:23], v[136:139]\n" \
"ds_read_b64_tr_b16 a[138:139], %[vvb] offset:37376\n" \
"ds_read_b64_tr_b16 a[140:141], v211 offset:33280\n" \
"v_mfma_f32_16x16x32_bf16 v[136:139], a[72:75], a[24:27], v[136:139]\n" \
"v_exp_f32_e64 v133, v133\n" \
"v_mfma_f32_16x16x32_bf16 v[136:139], a[76:79], a[28:31], v[136:139]\n" \
"v_cvt_pk_bf16_f32 v184, v128, v129\n" \
"v_cvt_pk_bf16_f32 v185, v130, v131\n" \
"v_mfma_f32_16x16x32_bf16 v[140:143], a[92:95], a[28:31], v[164:167]\n" \
"v_exp_f32_e64 v134, v134\n" \
"v_mfma_f32_16x16x32_bf16 v[140:143], a[88:91], a[24:27], v[140:143]\n" \
"ds_read_b64_tr_b16 a[142:143], v211 offset:37376\n" \
"s_waitcnt lgkmcnt(8)\n" \
"v_mfma_f32_16x16x32_bf16 v[140:143], a[84:87], a[20:23], v[140:143]\n" \
"buffer_load_dwordx4 v212, %[srdt], s49 offen offset:1024 lds\n" \
"v_mfma_f32_16x16x32_bf16 v[140:143], a[80:83], a[16:19], v[140:143]\n" \
"v_exp_f32_e32 v135, v135\n" \
"v_mfma_f32_16x16x32_bf16 v[80:83], a[144:147], v[216:219], v[80:83]\n" \
"ds_read_b64_tr_b16 a[144:145], %[vvb] offset:33792\n" \
"ds_read_b64_tr_b16 a[146:147], %[vvb] offset:37888\n" \
"v_mfma_f32_16x16x32_bf16 v[80:83], a[176:179], v[200:203], v[80:83]\n" \
"v_exp_f32_e64 v136, v136\n" \
"v_mfma_f32_16x16x32_bf16 v[84:87], a[148:151], v[216:219], v[84:87]\n" \
"v_cvt_pk_bf16_f32 v186, v132, v133\n" \
"v_cvt_pk_bf16_f32 v187, v134, v135\n" \
"v_mfma_f32_16x16x32_bf16 v[84:87], a[180:183], v[200:203], v[84:87]\n" \
"v_exp_f32_e64 v137, v137\n" \
"v_mfma_f32_16x16x32_bf16 v[88:91], a[152:155], v[216:219], v[88:91]\n" \
"v_exp_f32_e64 v138, v138\n" \
"v_mfma_f32_16x16x32_bf16 v[88:91], a[184:187], v[200:203], v[88:91]\n" \
"ds_read_b64_tr_b16 a[148:149], v211 offset:33792\n" \
"ds_read_b64_tr_b16 a[150:151], v211 offset:37888\n" \
"v_mfma_f32_16x16x32_bf16 v[92:95], a[156:159], v[216:219], v[92:95]\n" \
"v_exp_f32_e64 v139, v139\n" \
"v_mfma_f32_16x16x32_bf16 v[92:95], a[188:191], v[200:203], v[92:95]\n" \
"ds_read_b64_tr_b16 a[152:153], %[vvb] offset:34304\n" \
"global_load_dwordx2 a[242:243], %[vx0], %[px] offset:1024\n" \
"v_mfma_f32_16x16x32_bf16 v[144:147], a[64:67], a[32:35], v[168:171]\n" \
"v_exp_f32_e64 v140, v140\n" \
"v_mfma_f32_16x16x32_bf16 v[144:147], a[68:71], a[36:39], v[144:147]\n" \
"ds_read_b64_tr_b16 a[154:155], %[vvb] offset:38400\n" \
"ds_read_b64_tr_b16 a[156:157], v211 offset:34304\n" \
"v_mfma_f32_16x16x32_bf16 v[144:147], a[72:75], a[40:43], v[144:147]\n" \
"v_exp_f32_e64 v141, v141\n" \
"v_mfma_f32_16x16x32_bf16 v[144:147], a[76:79], a[44:47], v[144:147]\n" \
"v_cvt_pk_bf16_f32 v188, v136, v137\n" \
"v_cvt_pk_bf16_f32 v189, v138, v139\n" \
"v_mfma_f32_16x16x32_bf16 v[148:151], a[92:95], a[44:47], v[168:171]\n" \
"v_exp_f32_e64 v142, v142\n" \
"v_mfma_f32_16x16x32_bf16 v[148:151], a[88:91], a[40:43], v[148:151]\n" \
"ds_read_b64_tr_b16 a[158:159], v211 offset:38400\n" \
"s_waitcnt lgkmcnt(8)\n" \
"v_mfma_f32_16x16x32_bf16 v[148:151], a[84:87], a[36:39], v[148:151]\n" \
"buffer_load_dwordx4 v213, %[srdt], s49 offen offset:2048 lds\n" \
"v_mfma_f32_16x16x32_bf16 v[148:151], a[80:83], a[32:35], v[148:151]\n" \
"v_exp_f32_e32 v143, v143\n" \
"v_mfma_f32_16x16x32_bf16 v[0:3], a[160:163], v[192:195], v[0:3]\n" \
"ds_read_b128 a[96:99], %[kb0] offset:40960\n" \
"v_mfma_f32_16x16x32_bf16 v[0:3], a[128:131], v[184:187], v[0:3]\n" \
"v_exp_f32_e64 v144, v144\n" \
"v_mfma_f32_16x16x32_bf16 v[4:7], a[164:167], v[192:195], v[4:7]\n" \
"v_cvt_pk_bf16_f32 v190, v140, v141\n" \
"v_cvt_pk_bf16_f32 v191, v142, v143\n" \
"v_mfma_f32_16x16x32_bf16 v[4:7], a[132:135], v[184:187], v[4:7]\n" \
"v_exp_f32_e64 v145, v145\n" \
"v_mfma_f32_16x16x32_bf16 v[8:11], a[168:171], v[192:195], v[8:11]\n" \
"ds_read_b128 a[100:103], v208 offset:40960\n" \
"v_mfma_f32_16x16x32_bf16 v[8:11], a[136:139], v[184:187], v[8:11]\n" \
"v_exp_f32_e64 v146, v146\n" \
"v_mfma_f32_16x16x32_bf16 v[12:15], a[172:175], v[192:195], v[12:15]\n" \
"ds_read_b128 a[104:107], v209 offset:40960\n" \
"v_mfma_f32_16x16x32_bf16 v[12:15], a[140:143], v[184:187], v[12:15]\n" \
"v_exp_f32_e64 v147, v147\n" \
"v_smfmac_f32_16x16x64_bf16 v[180:183], a[192:195], v[200:207], v228\n" \
"ds_read_b128 a[108:111], v210 offset:40960\n" \
"global_load_dwordx2 a[248:249], v232, %[px] offset:0\n" \
"v_mfma_f32_16x16x32_bf16 v[152:155], a[64:67], a[48:51], v[172:175]\n" \
"v_exp_f32_e64 v148, v148\n" \
"v_mfma_f32_16x16x32_bf16 v[152:155], a[68:71], a[52:55], v[152:155]\n" \
"ds_read_b128 a[124:127], v210 offset:45056\n" \
"v_mfma_f32_16x16x32_bf16 v[152:155], a[72:75], a[56:59], v[152:155]\n" \
"v_exp_f32_e64 v149, v149\n" \
"v_mfma_f32_16x16x32_bf16 v[152:155], a[76:79], a[60:63], v[152:155]\n" \
"v_cvt_pk_bf16_f32 v216, v144, v145\n" \
"v_cvt_pk_bf16_f32 v217, v146, v147\n" \
"v_mfma_f32_16x16x32_bf16 v[156:159], a[92:95], a[60:63], v[172:175]\n" \
"v_exp_f32_e64 v150, v150\n" \
"v_mfma_f32_16x16x32_bf16 v[156:159], a[88:91], a[56:59], v[156:159]\n" \
"ds_read_b128 a[120:123], v209 offset:45056\n" \
"s_waitcnt lgkmcnt(6)\n" \
"v_mfma_f32_16x16x32_bf16 v[156:159], a[84:87], a[52:55], v[156:159]\n" \
"buffer_load_dwordx4 v214, %[srdt], s49 offen offset:3072 lds\n" \
"v_mfma_f32_16x16x32_bf16 v[156:159], a[80:83], a[48:51], v[156:159]\n" \
"v_exp_f32_e32 v151, v151\n" \
"v_mfma_f32_16x16x32_bf16 v[16:19], a[176:179], v[192:195], v[16:19]\n" \
"ds_read_b128 a[116:119], v208 offset:45056\n" \
"v_mfma_f32_16x16x32_bf16 v[16:19], a[144:147], v[184:187], v[16:19]\n" \
"v_exp_f32_e64 v152, v152\n" \
"v_mfma_f32_16x16x32_bf16 v[20:23], a[180:183], v[192:195], v[20:23]\n" \
"v_cvt_pk_bf16_f32 v218, v148, v149\n" \
"v_cvt_pk_bf16_f32 v219, v150, v151\n" \
"v_mfma_f32_16x16x32_bf16 v[20:23], a[148:151], v[184:187], v[20:23]\n" \
"v_exp_f32_e64 v153, v153\n" \
"v_mfma_f32_16x16x32_bf16 v[24:27], a[184:187], v[192:195], v[24:27]\n" \
"v_exp_f32_e64 v154, v154\n" \
"v_mfma_f32_16x16x32_bf16 v[24:27], a[152:155], v[184:187], v[24:27]\n" \
"ds_read_b128 a[112:115], %[kb0] offset:45056\n" \
"global_load_dwordx2 a[250:251], v232, %[px] offset:1024\n" \
"v_mfma_f32_16x16x32_bf16 v[28:31], a[188:191], v[192:195], v[28:31]\n" \
"v_exp_f32_e64 v155, v155\n" \
"v_mfma_f32_16x16x32_bf16 v[28:31], a[156:159], v[184:187], v[28:31]\n" \
"s_waitcnt vmcnt(63) lgkmcnt(2)\n" \
"s_barrier\n" \
"v_mfma_f32_16x16x32_bf16 v[128:131], a[96:99], a[0:3], v[160:163]\n" \
"v_exp_f32_e64 v156, v156\n" \
"v_mfma_f32_16x16x32_bf16 v[128:131], a[100:103], a[4:7], v[128:131]\n" \
"v_lshrrev_b32_e64 v227, 5, v231\n" \
"v_mfma_f32_16x16x32_bf16 v[128:131], a[104:107], a[8:11], v[128:131]\n" \
"v_exp_f32_e64 v157, v157\n" \
"v_mfma_f32_16x16x32_bf16 v[128:131], a[108:111], a[12:15], v[128:131]\n" \
"v_cvt_pk_bf16_f32 v220, v152, v153\n" \
"v_cvt_pk_bf16_f32 v221, v154, v155\n" \
"v_mfma_f32_16x16x32_bf16 v[132:135], a[124:127], a[12:15], v[160:163]\n" \
"v_exp_f32_e64 v158, v158\n" \
"v_mfma_f32_16x16x32_bf16 v[132:135], a[120:123], a[8:11], v[132:135]\n" \
"v_lshlrev_b32_e32 v227, 10, v227\n" \
"s_waitcnt lgkmcnt(0)\n" \
"v_mfma_f32_16x16x32_bf16 v[132:135], a[116:119], a[4:7], v[132:135]\n" \
"s_add_u32 m0, %[m0b], 81920\n" \
"s_mov_b32 s61, 917504\n" \
"buffer_load_dwordx4 %[vg0], s[64:67], s61 offen offset:0 lds\n" \
"v_mfma_f32_16x16x32_bf16 v[132:135], a[112:115], a[0:3], v[132:135]\n" \
"v_exp_f32_e64 v159, v159\n" \
"v_mfma_f32_16x16x32_bf16 v[32:35], a[160:163], v[196:199], v[32:35]\n" \
"ds_read_b128 a[64:67], %[kb0] offset:49152\n" \
"v_add_u32_e32 v227, 0x10000, v227\n" \
"v_mfma_f32_16x16x32_bf16 v[32:35], a[128:131], v[188:191], v[32:35]\n" \
"v_exp_f32_e64 v128, v128\n" \
"v_mfma_f32_16x16x32_bf16 v[36:39], a[164:167], v[196:199], v[36:39]\n" \
"v_cvt_pk_bf16_f32 v222, v156, v157\n" \
"v_cvt_pk_bf16_f32 v223, v158, v159\n" \
"v_mfma_f32_16x16x32_bf16 v[36:39], a[132:135], v[188:191], v[36:39]\n" \
"v_exp_f32_e64 v129, v129\n" \
"v_mfma_f32_16x16x32_bf16 v[40:43], a[168:171], v[196:199], v[40:43]\n" \
"ds_read_b128 a[68:71], v208 offset:49152\n" \
"v_sub_u32_e64 v215, %[vvb], v227\n" \
"v_mfma_f32_16x16x32_bf16 v[40:43], a[136:139], v[188:191], v[40:43]\n" \
"v_exp_f32_e64 v130, v130\n" \
"v_mfma_f32_16x16x32_bf16 v[44:47], a[172:175], v[196:199], v[44:47]\n" \
"ds_read_b128 a[72:75], v209 offset:49152\n" \
"v_xor_b32_e64 v227, 32, v215\n" \
"v_mfma_f32_16x16x32_bf16 v[44:47], a[140:143], v[188:191], v[44:47]\n" \
"v_exp_f32_e64 v131, v131\n" \
"v_smfmac_f32_16x16x64_bf16 v[176:179], a[192:195], v[184:191], v228\n" \
"global_load_dwordx2 a[244:245], %[vx0], %[px] offset:64\n" \
"v_mfma_f32_16x16x32_bf16 v[136:139], a[96:99], a[16:19], v[164:167]\n" \
"v_exp_f32_e64 v132, v132\n" \
"v_mfma_f32_16x16x32_bf16 v[136:139], a[100:103], a[20:23], v[136:139]\n" \
"ds_read_b128 a[76:79], v210 offset:49152\n" \
"v_mfma_f32_16x16x32_bf16 v[136:139], a[104:107], a[24:27], v[136:139]\n" \
"v_exp_f32_e64 v133, v133\n" \
"v_mfma_f32_16x16x32_bf16 v[136:139], a[108:111], a[28:31], v[136:139]\n" \
"v_cvt_pk_bf16_f32 v192, v128, v129\n" \
"v_cvt_pk_bf16_f32 v193, v130, v131\n" \
"v_mfma_f32_16x16x32_bf16 v[140:143], a[124:127], a[28:31], v[164:167]\n" \
"v_exp_f32_e64 v134, v134\n" \
"v_mfma_f32_16x16x32_bf16 v[140:143], a[120:123], a[24:27], v[140:143]\n" \
"ds_read_b128 a[92:95], v210 offset:53248\n" \
"v_mfma_f32_16x16x32_bf16 v[140:143], a[116:119], a[20:23], v[140:143]\n" \
"s_add_u32 m0, %[m0b], 82944\n" \
"s_mov_b32 s61, 1146880\n" \
"buffer_load_dwordx4 %[vg0], s[64:67], s61 offen offset:0 lds\n" \
"v_mfma_f32_16x16x32_bf16 v[140:143], a[112:115], a[16:19], v[140:143]\n" \
"v_exp_f32_e64 v135, v135\n" \
"v_mfma_f32_16x16x32_bf16 v[48:51], a[176:179], v[196:199], v[48:51]\n" \
"ds_read_b128 a[88:91], v209 offset:53248\n" \
"v_mfma_f32_16x16x32_bf16 v[48:51], a[144:147], v[188:191], v[48:51]\n" \
"v_exp_f32_e64 v136, v136\n" \
"v_mfma_f32_16x16x32_bf16 v[52:55], a[180:183], v[196:199], v[52:55]\n" \
"v_cvt_pk_bf16_f32 v194, v132, v133\n" \
"v_cvt_pk_bf16_f32 v195, v134, v135\n" \
"v_mfma_f32_16x16x32_bf16 v[52:55], a[148:151], v[188:191], v[52:55]\n" \
"v_exp_f32_e64 v137, v137\n" \
"v_mfma_f32_16x16x32_bf16 v[56:59], a[184:187], v[196:199], v[56:59]\n" \
"v_exp_f32_e64 v138, v138\n" \
"v_mfma_f32_16x16x32_bf16 v[56:59], a[152:155], v[188:191], v[56:59]\n" \
"ds_read_b128 a[84:87], v208 offset:53248\n" \
"v_mfma_f32_16x16x32_bf16 v[60:63], a[188:191], v[196:199], v[60:63]\n" \
"v_exp_f32_e64 v139, v139\n" \
"v_mfma_f32_16x16x32_bf16 v[60:63], a[156:159], v[188:191], v[60:63]\n" \
"global_load_dwordx2 a[246:247], %[vx0], %[px] offset:1088\n" \
"v_mfma_f32_16x16x32_bf16 v[144:147], a[96:99], a[32:35], v[168:171]\n" \
"v_exp_f32_e64 v140, v140\n" \
"v_mfma_f32_16x16x32_bf16 v[144:147], a[100:103], a[36:39], v[144:147]\n" \
"ds_read_b128 a[80:83], %[kb0] offset:53248\n" \
"v_mfma_f32_16x16x32_bf16 v[144:147], a[104:107], a[40:43], v[144:147]\n" \
"v_exp_f32_e64 v141, v141\n" \
"v_mfma_f32_16x16x32_bf16 v[144:147], a[108:111], a[44:47], v[144:147]\n" \
"v_cvt_pk_bf16_f32 v196, v136, v137\n" \
"v_cvt_pk_bf16_f32 v197, v138, v139\n" \
"v_mfma_f32_16x16x32_bf16 v[148:151], a[124:127], a[44:47], v[168:171]\n" \
"v_exp_f32_e64 v142, v142\n" \
"v_mfma_f32_16x16x32_bf16 v[148:151], a[120:123], a[40:43], v[148:151]\n" \
"v_mfma_f32_16x16x32_bf16 v[148:151], a[116:119], a[36:39], v[148:151]\n" \
"s_add_u32 m0, %[m0b], 83968\n" \
"s_mov_b32 s61, 1376256\n" \
"buffer_load_dwordx4 %[vg0], s[64:67], s61 offen offset:0 lds\n" \
"v_mfma_f32_16x16x32_bf16 v[148:151], a[112:115], a[32:35], v[148:151]\n" \
"v_exp_f32_e64 v143, v143\n" \
"v_mfma_f32_16x16x32_bf16 v[96:99], a[160:163], v[204:207], v[96:99]\n" \
"ds_read_b64_tr_b16 a[160:161], %[vvb] offset:40960\n" \
"v_mfma_f32_16x16x32_bf16 v[96:99], a[128:131], v[220:223], v[96:99]\n" \
"v_exp_f32_e64 v144, v144\n" \
"v_mfma_f32_16x16x32_bf16 v[100:103], a[164:167], v[204:207], v[100:103]\n" \
"v_cvt_pk_bf16_f32 v198, v140, v141\n" \
"v_cvt_pk_bf16_f32 v199, v142, v143\n" \
"v_mfma_f32_16x16x32_bf16 v[100:103], a[132:135], v[220:223], v[100:103]\n" \
"v_exp_f32_e64 v145, v145\n" \
"v_mfma_f32_16x16x32_bf16 v[104:107], a[168:171], v[204:207], v[104:107]\n" \
"ds_read_b64_tr_b16 a[162:163], %[vvb] offset:45056\n" \
"ds_read_b64_tr_b16 a[164:165], v211 offset:40960\n" \
"v_mfma_f32_16x16x32_bf16 v[104:107], a[136:139], v[220:223], v[104:107]\n" \
"v_exp_f32_e64 v146, v146\n" \
"v_mfma_f32_16x16x32_bf16 v[108:111], a[172:175], v[204:207], v[108:111]\n" \
"ds_read_b64_tr_b16 a[166:167], v211 offset:45056\n" \
"v_mfma_f32_16x16x32_bf16 v[108:111], a[140:143], v[220:223], v[108:111]\n" \
"v_exp_f32_e64 v147, v147\n" \
"v_smfmac_f32_16x16x64_bf16 v[180:183], a[192:195], v[216:223], v228\n" \
"ds_read_b64_tr_b16 a[168:169], %[vvb] offset:41472\n" \
"global_load_dwordx2 a[252:253], v232, %[px] offset:64\n" \
"v_mfma_f32_16x16x32_bf16 v[152:155], a[96:99], a[48:51], v[172:175]\n" \
"v_exp_f32_e64 v148, v148\n" \
"v_mfma_f32_16x16x32_bf16 v[152:155], a[100:103], a[52:55], v[152:155]\n" \
"ds_read_b64_tr_b16 a[170:171], %[vvb] offset:45568\n" \
"v_mfma_f32_16x16x32_bf16 v[152:155], a[104:107], a[56:59], v[152:155]\n" \
"v_exp_f32_e64 v149, v149\n" \
"v_mfma_f32_16x16x32_bf16 v[152:155], a[108:111], a[60:63], v[152:155]\n" \
"v_cvt_pk_bf16_f32 v200, v144, v145\n" \
"v_cvt_pk_bf16_f32 v201, v146, v147\n" \
"v_mfma_f32_16x16x32_bf16 v[156:159], a[124:127], a[60:63], v[172:175]\n" \
"v_exp_f32_e64 v150, v150\n" \
"v_mfma_f32_16x16x32_bf16 v[156:159], a[120:123], a[56:59], v[156:159]\n" \
"ds_read_b64_tr_b16 a[172:173], v211 offset:41472\n" \
"v_mfma_f32_16x16x32_bf16 v[156:159], a[116:119], a[52:55], v[156:159]\n" \
"s_add_u32 m0, %[m0b], 84992\n" \
"s_mov_b32 s61, 1605632\n" \
"buffer_load_dwordx4 %[vg0], s[64:67], s61 offen offset:0 lds\n" \
"v_mfma_f32_16x16x32_bf16 v[156:159], a[112:115], a[48:51], v[156:159]\n" \
"v_exp_f32_e64 v151, v151\n" \
"v_mfma_f32_16x16x32_bf16 v[112:115], a[176:179], v[204:207], v[112:115]\n" \
"ds_read_b64_tr_b16 a[174:175], v211 offset:45568\n" \
"ds_read_b64_tr_b16 a[176:177], %[vvb] offset:41984\n" \
"v_mfma_f32_16x16x32_bf16 v[112:115], a[144:147], v[220:223], v[112:115]\n" \
"v_exp_f32_e64 v152, v152\n" \
"v_mfma_f32_16x16x32_bf16 v[116:119], a[180:183], v[204:207], v[116:119]\n" \
"v_cvt_pk_bf16_f32 v202, v148, v149\n" \
"v_cvt_pk_bf16_f32 v203, v150, v151\n" \
"v_mfma_f32_16x16x32_bf16 v[116:119], a[148:151], v[220:223], v[116:119]\n" \
"v_exp_f32_e64 v153, v153\n" \
"v_mfma_f32_16x16x32_bf16 v[120:123], a[184:187], v[204:207], v[120:123]\n" \
"v_exp_f32_e64 v154, v154\n" \
"v_mfma_f32_16x16x32_bf16 v[120:123], a[152:155], v[220:223], v[120:123]\n" \
"ds_read_b64_tr_b16 a[178:179], %[vvb] offset:46080\n" \
"global_load_dwordx2 a[254:255], v232, %[px] offset:1088\n" \
"v_mfma_f32_16x16x32_bf16 v[124:127], a[188:191], v[204:207], v[124:127]\n" \
"v_exp_f32_e64 v155, v155\n" \
"v_mfma_f32_16x16x32_bf16 v[124:127], a[156:159], v[220:223], v[124:127]\n" \
"ds_read_b64_tr_b16 a[180:181], v211 offset:41984\n" \
"s_waitcnt lgkmcnt(13)\n" \
"v_mfma_f32_16x16x32_bf16 v[128:131], a[64:67], a[0:3], v[160:163]\n" \
"v_exp_f32_e32 v156, v156\n" \
"v_mfma_f32_16x16x32_bf16 v[128:131], a[68:71], a[4:7], v[128:131]\n" \
"ds_read_b64_tr_b16 a[182:183], v211 offset:46080\n" \
"s_add_u32 m0, %[m0b], 32768\n" \
"s_cmp_gt_u32 s48, 0\n" \
"v_mfma_f32_16x16x32_bf16 v[128:131], a[72:75], a[8:11], v[128:131]\n" \
"v_exp_f32_e32 v157, v157\n" \
"v_mfma_f32_16x16x32_bf16 v[128:131], a[76:79], a[12:15], v[128:131]\n" \
"v_cvt_pk_bf16_f32 v204, v152, v153\n" \
"v_cvt_pk_bf16_f32 v205, v154, v155\n" \
"v_mfma_f32_16x16x32_bf16 v[132:135], a[92:95], a[12:15], v[160:163]\n" \
"v_exp_f32_e64 v158, v158\n" \
"v_mfma_f32_16x16x32_bf16 v[132:135], a[88:91], a[8:11], v[132:135]\n" \
"ds_read_b64_tr_b16 a[184:185], %[vvb] offset:42496\n" \
"s_cselect_b32 s49, 0, 1\n" \
"s_lshl_b32 s49, s49, 18\n" \
"s_or_b32 s49, s49, s58\n" \
"s_waitcnt lgkmcnt(5)\n" \
"v_mfma_f32_16x16x32_bf16 v[132:135], a[84:87], a[4:7], v[132:135]\n" \
"buffer_load_dwordx4 %[wo0], %[srdt], s49 offen offset:0 lds\n" \
"v_mfma_f32_16x16x32_bf16 v[132:135], a[80:83], a[0:3], v[132:135]\n" \
"v_exp_f32_e64 v159, v159\n" \
"v_mfma_f32_16x16x32_bf16 v[64:67], a[128:131], v[216:219], v[64:67]\n" \
"ds_read_b64_tr_b16 a[186:187], %[vvb] offset:46592\n" \
"ds_read_b64_tr_b16 a[188:189], v211 offset:42496\n" \
"v_mfma_f32_16x16x32_bf16 v[64:67], a[160:163], v[200:203], v[64:67]\n" \
"v_exp_f32_e64 v128, v128\n" \
"v_mfma_f32_16x16x32_bf16 v[68:71], a[132:135], v[216:219], v[68:71]\n" \
"v_cvt_pk_bf16_f32 v206, v156, v157\n" \
"v_cvt_pk_bf16_f32 v207, v158, v159\n" \
"v_mfma_f32_16x16x32_bf16 v[68:71], a[164:167], v[200:203], v[68:71]\n" \
"v_exp_f32_e64 v129, v129\n" \
"v_mfma_f32_16x16x32_bf16 v[72:75], a[136:139], v[216:219], v[72:75]\n" \
"ds_read_b64_tr_b16 a[190:191], v211 offset:46592\n" \
"ds_read_b64_tr_b16 a[128:129], %[vvb] offset:49152\n" \
"v_mfma_f32_16x16x32_bf16 v[72:75], a[168:171], v[200:203], v[72:75]\n" \
"v_exp_f32_e64 v130, v130\n" \
"v_mfma_f32_16x16x32_bf16 v[76:79], a[140:143], v[216:219], v[76:79]\n" \
"ds_read_b64_tr_b16 a[130:131], %[vvb] offset:53248\n" \
"ds_read_b64_tr_b16 a[132:133], v211 offset:49152\n" \
"v_mfma_f32_16x16x32_bf16 v[76:79], a[172:175], v[200:203], v[76:79]\n" \
"v_exp_f32_e64 v131, v131\n" \
"v_smfmac_f32_16x16x64_bf16 v[176:179], a[192:195], v[192:199], v228\n" \
"ds_read_b64_tr_b16 a[134:135], v211 offset:53248\n" \
"ds_read_b64_tr_b16 a[136:137], %[vvb] offset:49664\n" \
"v_mfma_f32_16x16x32_bf16 v[136:139], a[64:67], a[16:19], v[164:167]\n" \
"v_exp_f32_e64 v132, v132\n" \
"v_mfma_f32_16x16x32_bf16 v[136:139], a[68:71], a[20:23], v[136:139]\n" \
"ds_read_b64_tr_b16 a[138:139], %[vvb] offset:53760\n" \
"ds_read_b64_tr_b16 a[140:141], v211 offset:49664\n" \
"v_mfma_f32_16x16x32_bf16 v[136:139], a[72:75], a[24:27], v[136:139]\n" \
"v_exp_f32_e64 v133, v133\n" \
"v_mfma_f32_16x16x32_bf16 v[136:139], a[76:79], a[28:31], v[136:139]\n" \
"v_cvt_pk_bf16_f32 v184, v128, v129\n" \
"v_cvt_pk_bf16_f32 v185, v130, v131\n" \
"v_mfma_f32_16x16x32_bf16 v[140:143], a[92:95], a[28:31], v[164:167]\n" \
"v_exp_f32_e64 v134, v134\n" \
"v_mfma_f32_16x16x32_bf16 v[140:143], a[88:91], a[24:27], v[140:143]\n" \
"ds_read_b64_tr_b16 a[142:143], v211 offset:53760\n" \
"s_waitcnt lgkmcnt(8)\n" \
"v_mfma_f32_16x16x32_bf16 v[140:143], a[84:87], a[20:23], v[140:143]\n" \
"buffer_load_dwordx4 v212, %[srdt], s49 offen offset:1024 lds\n" \
"v_mfma_f32_16x16x32_bf16 v[140:143], a[80:83], a[16:19], v[140:143]\n" \
"v_exp_f32_e32 v135, v135\n" \
"v_mfma_f32_16x16x32_bf16 v[80:83], a[144:147], v[216:219], v[80:83]\n" \
"ds_read_b64_tr_b16 a[144:145], %[vvb] offset:50176\n" \
".if . - adaln_body != 36864\n" \
".error \"size model\"\n" \
".endif\n" \
"Lk8_%=:\n" \
"s_cbranch_execz Lk9_%=\n" \
"s_nop 0\n" \
"ds_read_b64_tr_b16 a[146:147], %[vvb] offset:54272\n" \
"v_mfma_f32_16x16x32_bf16 v[80:83], a[176:179], v[200:203], v[80:83]\n" \
"v_exp_f32_e64 v136, v136\n" \
"v_mfma_f32_16x16x32_bf16 v[84:87], a[148:151], v[216:219], v[84:87]\n" \
"v_cvt_pk_bf16_f32 v186, v132, v133\n" \
"v_cvt_pk_bf16_f32 v187, v134, v135\n" \
"v_mfma_f32_16x16x32_bf16 v[84:87], a[180:183], v[200:203], v[84:87]\n" \
"v_exp_f32_e64 v137, v137\n" \
"v_mfma_f32_16x16x32_bf16 v[88:91], a[152:155], v[216:219], v[88:91]\n" \
"v_exp_f32_e64 v138, v138\n" \
"v_mfma_f32_16x16x32_bf16 v[88:91], a[184:187], v[200:203], v[88:91]\n" \
"ds_read_b64_tr_b16 a[148:149], v211 offset:50176\n" \
"ds_read_b64_tr_b16 a[150:151], v211 offset:54272\n" \
"v_readlane_b32 s64, %[tlo], 10\n" \
"v_mfma_f32_16x16x32_bf16 v[92:95], a[156:159], v[216:219], v[92:95]\n" \
"v_exp_f32_e64 v139, v139\n" \
"v_mfma_f32_16x16x32_bf16 v[92:95], a[188:191], v[200:203], v[92:95]\n" \
"ds_read_b64_tr_b16 a[152:153], %[vvb] offset:50688\n" \
"v_readlane_b32 s65, %[thi], 10\n" \
"v_mfma_f32_16x16x32_bf16 v[144:147], a[64:67], a[32:35], v[168:171]\n" \
"v_exp_f32_e64 v140, v140\n" \
"v_mfma_f32_16x16x32_bf16 v[144:147], a[68:71], a[36:39], v[144:147]\n" \
"ds_read_b64_tr_b16 a[154:155], %[vvb] offset:54784\n" \
"ds_read_b64_tr_b16 a[156:157], v211 offset:50688\n" \
"v_mfma_f32_16x16x32_bf16 v[144:147], a[72:75], a[40:43], v[144:147]\n" \
"v_exp_f32_e64 v141, v141\n" \
"v_mfma_f32_16x16x32_bf16 v[144:147], a[76:79], a[44:47], v[144:147]\n" \
"v_cvt_pk_bf16_f32 v188, v136, v137\n" \
"v_cvt_pk_bf16_f32 v189, v138, v139\n" \
"v_mfma_f32_16x16x32_bf16 v[148:151], a[92:95], a[44:47], v[168:171]\n" \
"v_exp_f32_e64 v142, v142\n" \
"v_mfma_f32_16x16x32_bf16 v[148:151], a[88:91], a[40:43], v[148:151]\n" \
"ds_read_b64_tr_b16 a[158:159], v211 offset:54784\n" \
"s_waitcnt lgkmcnt(8)\n" \
"v_mfma_f32_16x16x32_bf16 v[148:151], a[84:87], a[36:39], v[148:151]\n" \
"buffer_load_dwordx4 v213, %[srdt], s49 offen offset:2048 lds\n" \
"v_mfma_f32_16x16x32_bf16 v[148:151], a[80:83], a[32:35], v[148:151]\n" \
"v_exp_f32_e32 v143, v143\n" \
"v_mfma_f32_16x16x32_bf16 v[0:3], a[160:163], v[192:195], v[0:3]\n" \
"ds_read_b128 a[96:99], %[kb0] offset:57344\n" \
"v_mfma_f32_16x16x32_bf16 v[0:3], a[128:131], v[184:187], v[0:3]\n" \
"v_exp_f32_e64 v144, v144\n" \
"v_mfma_f32_16x16x32_bf16 v[4:7], a[164:167], v[192:195], v[4:7]\n" \
"v_cvt_pk_bf16_f32 v190, v140, v141\n" \
"v_cvt_pk_bf16_f32 v191, v142, v143\n" \
"v_mfma_f32_16x16x32_bf16 v[4:7], a[132:135], v[184:187], v[4:7]\n" \
"v_exp_f32_e64 v145, v145\n" \
"v_mfma_f32_16x16x32_bf16 v[8:11], a[168:171], v[192:195], v[8:11]\n" \
"ds_read_b128 a[100:103], v208 offset:57344\n" \
"v_mfma_f32_16x16x32_bf16 v[8:11], a[136:139], v[184:187], v[8:11]\n" \
"v_exp_f32_e64 v146, v146\n" \
"v_mfma_f32_16x16x32_bf16 v[12:15], a[172:175], v[192:195], v[12:15]\n" \
"ds_read_b128 a[104:107], v209 offset:57344\n" \
"v_mfma_f32_16x16x32_bf16 v[12:15], a[140:143], v[184:187], v[12:15]\n" \
"v_exp_f32_e64 v147, v147\n" \
"v_smfmac_f32_16x16x64_bf16 v[180:183], a[192:195], v[200:207], v228\n" \
"ds_read_b128 a[108:111], v210 offset:57344\n" \
"buffer_load_dword v229, v226, %[srdz], s50 offen sc1\n" \
"v_mfma_f32_16x16x32_bf16 v[152:155], a[64:67], a[48:51], v[172:175]\n" \
"v_exp_f32_e64 v148, v148\n" \
"v_mfma_f32_16x16x32_bf16 v[152:155], a[68:71], a[52:55], v[152:155]\n" \
"ds_read_b128 a[124:127], v210 offset:61440\n" \
"v_mfma_f32_16x16x32_bf16 v[152:155], a[72:75], a[56:59], v[152:155]\n" \
"v_exp_f32_e64 v149, v149\n" \
"v_mfma_f32_16x16x32_bf16 v[152:155], a[76:79], a[60:63], v[152:155]\n" \
"v_cvt_pk_bf16_f32 v216, v144, v145\n" \
"v_cvt_pk_bf16_f32 v217, v146, v147\n" \
"v_mfma_f32_16x16x32_bf16 v[156:159], a[92:95], a[60:63], v[172:175]\n" \
"v_exp_f32_e64 v150, v150\n" \
"v_mfma_f32_16x16x32_bf16 v[156:159], a[88:91], a[56:59], v[156:159]\n" \
"ds_read_b128 a[120:123], v209 offset:61440\n" \
"s_waitcnt lgkmcnt(6)\n" \
"v_mfma_f32_16x16x32_bf16 v[156:159], a[84:87], a[52:55], v[156:159]\n" \
"buffer_load_dwordx4 v214, %[srdt], s49 offen offset:3072 lds\n" \
"v_mfma_f32_16x16x32_bf16 v[156:159], a[80:83], a[48:51], v[156:159]\n" \
"v_exp_f32_e32 v151, v151\n" \
"v_mfma_f32_16x16x32_bf16 v[16:19], a[176:179], v[192:195], v[16:19]\n" \
"ds_read_b128 a[116:119], v208 offset:61440\n" \
"v_mfma_f32_16x16x32_bf16 v[16:19], a[144:147], v[184:187], v[16:19]\n" \
"v_exp_f32_e64 v152, v152\n" \
"v_mfma_f32_16x16x32_bf16 v[20:23], a[180:183], v[192:195], v[20:23]\n" \
"v_cvt_pk_bf16_f32 v218, v148, v149\n" \
"v_cvt_pk_bf16_f32 v219, v150, v151\n" \
"v_mfma_f32_16x16x32_bf16 v[20:23], a[148:151], v[184:187], v[20:23]\n" \
"v_exp_f32_e64 v153, v153\n" \
"v_mfma_f32_16x16x32_bf16 v[24:27], a[184:187], v[192:195], v[24:27]\n" \
"v_exp_f32_e64 v154, v154\n" \
"v_mfma_f32_16x16x32_bf16 v[24:27], a[152:155], v[184:187], v[24:27]\n" \
"ds_read_b128 a[112:115], %[kb0] offset:61440\n" \
"v_mfma_f32_16x16x32_bf16 v[28:31], a[188:191], v[192:195], v[28:31]\n" \
"v_exp_f32_e64 v155, v155\n" \
"v_mfma_f32_16x16x32_bf16 v[28:31], a[156:159], v[184:187], v[28:31]\n" \
"s_waitcnt vmcnt(28) lgkmcnt(2)\n" \
"s_barrier\n" \
"v_mfma_f32_16x16x32_bf16 v[128:131], a[96:99], a[0:3], v[160:163]\n" \
"v_exp_f32_e64 v156, v156\n" \
"v_mfma_f32_16x16x32_bf16 v[128:131], a[100:103], a[4:7], v[128:131]\n" \
"s_add_u32 m0, %[m0b], 98304\n" \
"s_cmp_gt_u32 s48, 0\n" \
"v_mfma_f32_16x16x32_bf16 v[128:131], a[104:107], a[8:11], v[128:131]\n" \
"v_exp_f32_e32 v157, v157\n" \
"v_mfma_f32_16x16x32_bf16 v[128:131], a[108:111], a[12:15], v[128:131]\n" \
"v_cvt_pk_bf16_f32 v220, v152, v153\n" \
"v_cvt_pk_bf16_f32 v221, v154, v155\n" \
"v_mfma_f32_16x16x32_bf16 v[132:135], a[124:127], a[12:15], v[160:163]\n" \
"v_exp_f32_e64 v158, v158\n" \
"v_mfma_f32_16x16x32_bf16 v[132:135], a[120:123], a[8:11], v[132:135]\n" \
"s_cselect_b32 s49, 0, 1\n" \
"s_lshl_b32 s49, s49, 18\n" \
"s_or_b32 s49, s49, s59\n" \
"s_waitcnt lgkmcnt(0)\n" \
"v_mfma_f32_16x16x32_bf16 v[132:135], a[116:119], a[4:7], v[132:135]\n" \
"buffer_load_dwordx4 %[wo0], %[srdt], s49 offen offset:0 lds\n" \
"v_mfma_f32_16x16x32_bf16 v[132:135], a[112:115], a[0:3], v[132:135]\n" \
"v_exp_f32_e64 v159, v159\n" \
"v_mfma_f32_16x16x32_bf16 v[32:35], a[160:163], v[196:199], v[32:35]\n" \
"ds_read_b64_tr_b16 a[196:197], v215 offset:0\n" \
"v_mfma_f32_16x16x32_bf16 v[32:35], a[128:131], v[188:191], v[32:35]\n" \
"v_exp_f32_e64 v128, v128\n" \
"v_mfma_f32_16x16x32_bf16 v[36:39], a[164:167], v[196:199], v[36:39]\n" \
"v_cvt_pk_bf16_f32 v222, v156, v157\n" \
"v_cvt_pk_bf16_f32 v223, v158, v159\n" \
"v_mfma_f32_16x16x32_bf16 v[36:39], a[132:135], v[188:191], v[36:39]\n" \
"v_exp_f32_e64 v129, v129\n" \
"v_mfma_f32_16x16x32_bf16 v[40:43], a[168:171], v[196:199], v[40:43]\n" \
"ds_read_b64_tr_b16 a[198:199], v215 offset:2048\n" \
"v_mfma_f32_16x16x32_bf16 v[40:43], a[136:139], v[188:191], v[40:43]\n" \
"v_exp_f32_e64 v130, v130\n" \
"v_mfma_f32_16x16x32_bf16 v[44:47], a[172:175], v[196:199], v[44:47]\n" \
"ds_read_b64_tr_b16 a[200:201], v215 offset:4096\n" \
"v_mfma_f32_16x16x32_bf16 v[44:47], a[140:143], v[188:191], v[44:47]\n" \
"v_exp_f32_e64 v131, v131\n" \
"v_smfmac_f32_16x16x64_bf16 v[176:179], a[192:195], v[184:191], v228\n" \
"v_mfma_f32_16x16x32_bf16 v[136:139], a[96:99], a[16:19], v[164:167]\n" \
"v_exp_f32_e64 v132, v132\n" \
"v_mfma_f32_16x16x32_bf16 v[136:139], a[100:103], a[20:23], v[136:139]\n" \
"ds_read_b64_tr_b16 a[202:203], v215 offset:6144\n" \
"v_mfma_f32_16x16x32_bf16 v[136:139], a[104:107], a[24:27], v[136:139]\n" \
"v_exp_f32_e64 v133, v133\n" \
"v_mfma_f32_16x16x32_bf16 v[136:139], a[108:111], a[28:31], v[136:139]\n" \
"v_cvt_pk_bf16_f32 v192, v128, v129\n" \
"v_cvt_pk_bf16_f32 v193, v130, v131\n" \
"v_mfma_f32_16x16x32_bf16 v[140:143], a[124:127], a[28:31], v[164:167]\n" \
"v_exp_f32_e64 v134, v134\n" \
"v_mfma_f32_16x16x32_bf16 v[140:143], a[120:123], a[24:27], v[140:143]\n" \
"ds_read_b64_tr_b16 a[204:205], v215 offset:8192\n" \
"v_mfma_f32_16x16x32_bf16 v[140:143], a[116:119], a[20:23], v[140:143]\n" \
"buffer_load_dwordx4 v212, %[srdt], s49 offen offset:1024 lds\n" \
"v_mfma_f32_16x16x32_bf16 v[140:143], a[112:115], a[16:19], v[140:143]\n" \
"v_exp_f32_e64 v135, v135\n" \
"v_mfma_f32_16x16x32_bf16 v[48:51], a[176:179], v[196:199], v[48:51]\n" \
"ds_read_b64_tr_b16 a[206:207], v215 offset:10240\n" \
"v_add_u32_e32 v208, 0x1000, %[oq0]\n" \
"v_mfma_f32_16x16x32_bf16 v[48:51], a[144:147], v[188:191], v[48:51]\n" \
"v_exp_f32_e64 v136, v136\n" \
"v_mfma_f32_16x16x32_bf16 v[52:55], a[180:183], v[196:199], v[52:55]\n" \
"v_cvt_pk_bf16_f32 v194, v132, v133\n" \
"v_cvt_pk_bf16_f32 v195, v134, v135\n" \
"v_mfma_f32_16x16x32_bf16 v[52:55], a[148:151], v[188:191], v[52:55]\n" \
"v_exp_f32_e64 v137, v137\n" \
"v_mfma_f32_16x16x32_bf16 v[56:59], a[184:187], v[196:199], v[56:59]\n" \
"v_exp_f32_e64 v138, v138\n" \
"v_mfma_f32_16x16x32_bf16 v[56:59], a[152:155], v[188:191], v[56:59]\n" \
"ds_read_b64_tr_b16 a[208:209], v215 offset:12288\n" \
"v_add_u32_e32 v209, 0x2000, %[oq0]\n" \
"v_mfma_f32_16x16x32_bf16 v[60:63], a[188:191], v[196:199], v[60:63]\n" \
"v_exp_f32_e64 v139, v139\n" \
"v_mfma_f32_16x16x32_bf16 v[60:63], a[156:159], v[188:191], v[60:63]\n" \
"v_add_u32_e32 v210, 0x3000, %[oq0]\n" \
"v_mfma_f32_16x16x32_bf16 v[144:147], a[96:99], a[32:35], v[168:171]\n" \
"v_exp_f32_e64 v140, v140\n" \
"v_mfma_f32_16x16x32_bf16 v[144:147], a[100:103], a[36:39], v[144:147]\n" \
"ds_read_b64_tr_b16 a[210:211], v215 offset:14336\n" \
"v_mfma_f32_16x16x32_bf16 v[144:147], a[104:107], a[40:43], v[144:147]\n" \
"v_exp_f32_e64 v141, v141\n" \
"v_mfma_f32_16x16x32_bf16 v[144:147], a[108:111], a[44:47], v[144:147]\n" \
"v_cvt_pk_bf16_f32 v196, v136, v137\n" \
"v_cvt_pk_bf16_f32 v197, v138, v139\n" \
"v_mfma_f32_16x16x32_bf16 v[148:151], a[124:127], a[44:47], v[168:171]\n" \
"v_exp_f32_e64 v142, v142\n" \
"v_mfma_f32_16x16x32_bf16 v[148:151], a[120:123], a[40:43], v[148:151]\n" \
"v_mfma_f32_16x16x32_bf16 v[148:151], a[116:119], a[36:39], v[148:151]\n" \
"buffer_load_dwordx4 v213, %[srdt], s49 offen offset:2048 lds\n" \
"v_mfma_f32_16x16x32_bf16 v[148:151], a[112:115], a[32:35], v[148:151]\n" \
"v_exp_f32_e64 v143, v143\n" \
"v_mfma_f32_16x16x32_bf16 v[96:99], a[160:163], v[204:207], v[96:99]\n" \
"ds_read_b64_tr_b16 a[160:161], %[vvb] offset:57344\n" \
"v_mfma_f32_16x16x32_bf16 v[96:99], a[128:131], v[220:223], v[96:99]\n" \
"v_exp_f32_e64 v144, v144\n" \
"v_mfma_f32_16x16x32_bf16 v[100:103], a[164:167], v[204:207], v[100:103]\n" \
"v_cvt_pk_bf16_f32 v198, v140, v141\n" \
"v_cvt_pk_bf16_f32 v199, v142, v143\n" \
"v_mfma_f32_16x16x32_bf16 v[100:103], a[132:135], v[220:223], v[100:103]\n" \
"v_exp_f32_e64 v145, v145\n" \
"v_mfma_f32_16x16x32_bf16 v[104:107], a[168:171], v[204:207], v[104:107]\n" \
"ds_read_b64_tr_b16 a[162:163], %[vvb] offset:61440\n" \
"ds_read_b64_tr_b16 a[164:165], v211 offset:57344\n" \
"v_mfma_f32_16x16x32_bf16 v[104:107], a[136:139], v[220:223], v[104:107]\n" \
"v_exp_f32_e64 v146, v146\n" \
"v_mfma_f32_16x16x32_bf16 v[108:111], a[172:175], v[204:207], v[108:111]\n" \
"ds_read_b64_tr_b16 a[166:167], v211 offset:61440\n" \
"v_mfma_f32_16x16x32_bf16 v[108:111], a[140:143], v[220:223], v[108:111]\n" \
"v_exp_f32_e64 v147, v147\n" \
"v_smfmac_f32_16x16x64_bf16 v[180:183], a[192:195], v[216:223], v228\n" \
"ds_read_b64_tr_b16 a[168:169], %[vvb] offset:57856\n" \
"v_mfma_f32_16x16x32_bf16 v[152:155], a[96:99], a[48:51], v[172:175]\n" \
"v_exp_f32_e64 v148, v148\n" \
"v_mfma_f32_16x16x32_bf16 v[152:155], a[100:103], a[52:55], v[152:155]\n" \
"ds_read_b64_tr_b16 a[170:171], %[vvb] offset:61952\n" \
"v_mfma_f32_16x16x32_bf16 v[152:155], a[104:107], a[56:59], v[152:155]\n" \
"v_exp_f32_e64 v149, v149\n" \
"v_mfma_f32_16x16x32_bf16 v[152:155], a[108:111], a[60:63], v[152:155]\n" \
"v_cvt_pk_bf16_f32 v200, v144, v145\n" \
"v_cvt_pk_bf16_f32 v201, v146, v147\n" \
"v_mfma_f32_16x16x32_bf16 v[156:159], a[124:127], a[60:63], v[172:175]\n" \
"v_exp_f32_e64 v150, v150\n" \
"v_mfma_f32_16x16x32_bf16 v[156:159], a[120:123], a[56:59], v[156:159]\n" \
"ds_read_b64_tr_b16 a[172:173], v211 offset:57856\n" \
"v_mfma_f32_16x16x32_bf16 v[156:159], a[116:119], a[52:55], v[156:159]\n" \
"buffer_load_dwordx4 v214, %[srdt], s49 offen offset:3072 lds\n" \
"v_mfma_f32_16x16x32_bf16 v[156:159], a[112:115], a[48:51], v[156:159]\n" \
"v_exp_f32_e64 v151, v151\n" \
"v_mfma_f32_16x16x32_bf16 v[112:115], a[176:179], v[204:207], v[112:115]\n" \
"ds_read_b64_tr_b16 a[174:175], v211 offset:61952\n" \
"ds_read_b64_tr_b16 a[176:177], %[vvb] offset:58368\n" \
"v_mfma_f32_16x16x32_bf16 v[112:115], a[144:147], v[220:223], v[112:115]\n" \
"v_exp_f32_e64 v152, v152\n" \
"v_mfma_f32_16x16x32_bf16 v[116:119], a[180:183], v[204:207], v[116:119]\n" \
"v_cvt_pk_bf16_f32 v202, v148, v149\n" \
"v_cvt_pk_bf16_f32 v203, v150, v151\n" \
"v_mfma_f32_16x16x32_bf16 v[116:119], a[148:151], v[220:223], v[116:119]\n" \
"v_exp_f32_e64 v153, v153\n" \
"v_mfma_f32_16x16x32_bf16 v[120:123], a[184:187], v[204:207], v[120:123]\n" \
"v_exp_f32_e64 v154, v154\n" \
"v_mfma_f32_16x16x32_bf16 v[120:123], a[152:155], v[220:223], v[120:123]\n" \
"ds_read_b64_tr_b16 a[178:179], %[vvb] offset:62464\n" \
"s_waitcnt vmcnt(5)\n" \
"v_cmp_eq_u32_e64 s[56:57], 0, v229\n" \
"s_cmp_lg_u64 s[56:57], 0\n" \
"s_cbranch_scc1 Lspk_%=\n" \
"Lbkk_%=:\n" \
"v_mfma_f32_16x16x32_bf16 v[124:127], a[188:191], v[204:207], v[124:127]\n" \
"v_exp_f32_e32 v155, v155\n" \
"v_mfma_f32_16x16x32_bf16 v[124:127], a[156:159], v[220:223], v[124:127]\n" \
"ds_read_b64_tr_b16 a[180:181], v211 offset:58368\n" \
"ds_read_b64_tr_b16 a[182:183], v211 offset:62464\n" \
"ds_read_b64_tr_b16 a[184:185], %[vvb] offset:58880\n" \
"ds_read_b64_tr_b16 a[186:187], %[vvb] offset:62976\n" \
"ds_read_b64_tr_b16 a[188:189], v211 offset:58880\n" \
"ds_read_b64_tr_b16 a[190:191], v211 offset:62976\n" \
"s_waitcnt lgkmcnt(8)\n" \
"s_nop 0\n" \
"v_smfmac_f32_16x16x64_bf16 v[176:179], a[192:195], v[192:199], v228\n" \
"v_exp_f32_e64 v156, v156\n" \
"ds_read_b64_tr_b16 a[212:213], v227 offset:0\n" \
"v_mfma_f32_16x16x32_bf16 v[0:3], a[160:163], v[192:195], v[0:3]\n" \
"v_exp_f32_e64 v157, v157\n" \
"ds_read_b64_tr_b16 a[214:215], v227 offset:2048\n" \
"v_mfma_f32_16x16x32_bf16 v[4:7], a[164:167], v[192:195], v[4:7]\n" \
"v_cvt_pk_bf16_f32 v204, v152, v153\n" \
"ds_read_b64_tr_b16 a[216:217], v227 offset:4096\n" \
"v_mfma_f32_16x16x32_bf16 v[8:11], a[168:171], v[192:195], v[8:11]\n" \
"v_cvt_pk_bf16_f32 v205, v154, v155\n" \
"ds_read_b64_tr_b16 a[218:219], v227 offset:6144\n" \
"v_mfma_f32_16x16x32_bf16 v[12:15], a[172:175], v[192:195], v[12:15]\n" \
"v_exp_f32_e64 v158, v158\n" \
"ds_read_b64_tr_b16 a[220:221], v227 offset:8192\n" \
"s_waitcnt lgkmcnt(5)\n" \
"s_nop 0\n" \
"v_mfma_f32_16x16x32_bf16 v[16:19], a[176:179], v[192:195], v[16:19]\n" \
"v_exp_f32_e64 v159, v159\n" \
"ds_read_b64_tr_b16 a[222:223], v227 offset:10240\n" \
"v_mfma_f32_16x16x32_bf16 v[20:23], a[180:183], v[192:195], v[20:23]\n" \
"v_cvt_pk_bf16_f32 v206, v156, v157\n" \
"ds_read_b64_tr_b16 a[224:225], v227 offset:12288\n" \
"v_mfma_f32_16x16x32_bf16 v[24:27], a[184:187], v[192:195], v[24:27]\n" \
"v_cvt_pk_bf16_f32 v207, v158, v159\n" \
"ds_read_b64_tr_b16 a[226:227], v227 offset:14336\n" \
"v_mfma_f32_16x16x32_bf16 v[28:31], a[188:191], v[192:195], v[28:31]\n" \
"v_add_f32_e64 v176, v176, v177\n" \
"v_smfmac_f32_16x16x64_bf16 v[180:183], a[192:195], v[200:207], v228\n" \
"v_add_f32_e32 v178, v178, v179\n" \
"v_add_f32_e32 v231, v176, v178\n" \
"v_rcp_f32_e32 v176, v176\n" \
"v_rcp_f32_e32 v178, v178\n" \
"v_mfma_f32_16x16x32_bf16 v[32:35], a[160:163], v[196:199], v[32:35]\n" \
"v_pk_mul_f32 v[0:1], v[0:1], v[176:177] op_sel_hi:[1,0]\n" \
"v_pk_mul_f32 v[2:3], v[2:3], v[176:177] op_sel_hi:[1,0]\n" \
"v_pk_mul_f32 v[4:5], v[4:5], v[176:177] op_sel_hi:[1,0]\n" \
"v_pk_mul_f32 v[6:7], v[6:7], v[176:177] op_sel_hi:[1,0]\n" \
"v_cvt_pk_bf16_f32 v0, v0, v1\n" \
"v_cvt_pk_bf16_f32 v1, v2, v3\n" \
"v_cvt_pk_bf16_f32 v2, v4, v5\n" \
"v_mfma_f32_16x16x32_bf16 v[36:39], a[164:167], v[196:199], v[36:39]\n" \
"v_cvt_pk_bf16_f32 v3, v6, v7\n" \
"s_mov_b32 s55, 0x4e800000\n" \
"v_mov_b32_e32 v236, %[flag]\n" \
"v_add_f32_e32 v180, v180, v181\n" \
"v_add_f32_e32 v182, v182, v183\n" \
"v_add_f32_e32 v231, v231, v180\n" \
"v_add_f32_e64 v231, v231, v182\n" \
"v_mfma_f32_16x16x32_bf16 v[40:43], a[168:171], v[196:199], v[40:43]\n" \
"v_rcp_f32_e64 v180, v180\n" \
"v_cmp_nle_f32_e64 s[56:57], v231, s55\n" \
"v_rcp_f32_e32 v182, v182\n" \
"s_nop 3\n" \
"s_cmp_lg_u64 s[56:57], 0\n" \
"s_cselect_b32 s55, 1, 0\n" \
"v_mov_b32_e64 v230, s55\n" \
"v_mfma_f32_16x16x32_bf16 v[44:47], a[172:175], v[196:199], v[44:47]\n" \
"s_mov_b32 s69, s55\n" \
"s_andn2_b32 s55, s55, s52\n" \
"s_cselect_b64 vcc, 0, vcc\n" \
"s_nop 0\n" \
"ds_write_b32 v236, v230\n" \
"v_and_b32_e32 v236, -16, v236\n" \
"s_cbranch_vccz Lg37_%=\n" \
"buffer_store_dwordx4 v[0:3], %[oq0], %[srdz], s60 offen offset:0 sc1\n" \
"Lg37_%=:\n" \
"v_pk_mul_f32 v[8:9], v[8:9], v[176:177] op_sel_hi:[1,0]\n" \
"v_mfma_f32_16x16x32_bf16 v[48:51], a[176:179], v[196:199], v[48:51]\n" \
"v_pk_mul_f32 v[10:11], v[10:11], v[176:177] op_sel_hi:[1,0]\n" \
"v_pk_mul_f32 v[12:13], v[12:13], v[176:177] op_sel_hi:[1,0]\n" \
"v_pk_mul_f32 v[14:15], v[14:15], v[176:177] op_sel_hi:[1,0]\n" \
"v_cvt_pk_bf16_f32 v8, v8, v9\n" \
"v_cvt_pk_bf16_f32 v9, v10, v11\n" \
"v_cvt_pk_bf16_f32 v10, v12, v13\n" \
"v_cvt_pk_bf16_f32 v11, v14, v15\n" \
"v_mfma_f32_16x16x32_bf16 v[52:55], a[180:183], v[196:199], v[52:55]\n" \
"s_cbranch_vccz Lg38_%=\n" \
"s_nop 0\n" \
"buffer_store_dwordx4 v[8:11], %[oq0], %[srdz], s60 offen offset:1024 sc1\n" \
"Lg38_%=:\n" \
"v_pk_mul_f32 v[16:17], v[16:17], v[176:177] op_sel_hi:[1,0]\n" \
"v_pk_mul_f32 v[18:19], v[18:19], v[176:177] op_sel_hi:[1,0]\n" \
"v_pk_mul_f32 v[20:21], v[20:21], v[176:177] op_sel_hi:[1,0]\n" \
"v_pk_mul_f32 v[22:23], v[22:23], v[176:177] op_sel_hi:[1,0]\n" \
"v_cvt_pk_bf16_f32 v16, v16, v17\n" \
"v_cvt_pk_bf16_f32 v17, v18, v19\n" \
"v_mfma_f32_16x16x32_bf16 v[56:59], a[184:187], v[196:199], v[56:59]\n" \
"v_cvt_pk_bf16_f32 v18, v20, v21\n" \
"v_cvt_pk_bf16_f32 v19, v22, v23\n" \
"s_cbranch_vccz Lg39_%=\n" \
"s_nop 0\n" \
"buffer_store_dwordx4 v[16:19], %[oq0], %[srdz], s60 offen offset:2048 sc1\n" \
"Lg39_%=:\n" \
"v_pk_mul_f32 v[24:25], v[24:25], v[176:177] op_sel_hi:[1,0]\n" \
"v_pk_mul_f32 v[26:27], v[26:27], v[176:177] op_sel_hi:[1,0]\n" \
"v_pk_mul_f32 v[28:29], v[28:29], v[176:177] op_sel_hi:[1,0]\n" \
"v_pk_mul_f32 v[30:31], v[30:31], v[176:177] op_sel_hi:[1,0]\n" \
"v_mfma_f32_16x16x32_bf16 v[60:63], a[188:191], v[196:199], v[60:63]\n" \
"v_cvt_pk_bf16_f32 v24, v24, v25\n" \
"v_cvt_pk_bf16_f32 v25, v26, v27\n" \
"v_cvt_pk_bf16_f32 v26, v28, v29\n" \
"v_cvt_pk_bf16_f32 v27, v30, v31\n" \
"s_cbranch_vccz Lg40_%=\n" \
"s_nop 0\n" \
"buffer_store_dwordx4 v[24:27], %[oq0], %[srdz], s60 offen offset:3072 sc1\n" \
"Lg40_%=:\n" \
"v_mfma_f32_16x16x32_bf16 v[64:67], a[128:131], v[216:219], v[64:67]\n" \
"v_pk_mul_f32 v[32:33], v[32:33], v[178:179] op_sel_hi:[1,0]\n" \
"v_pk_mul_f32 v[34:35], v[34:35], v[178:179] op_sel_hi:[1,0]\n" \
"v_pk_mul_f32 v[36:37], v[36:37], v[178:179] op_sel_hi:[1,0]\n" \
"v_mfma_f32_16x16x32_bf16 v[64:67], a[160:163], v[200:203], v[64:67]\n" \
"v_pk_mul_f32 v[38:39], v[38:39], v[178:179] op_sel_hi:[1,0]\n" \
"v_cvt_pk_bf16_f32 v32, v32, v33\n" \
"v_cvt_pk_bf16_f32 v33, v34, v35\n" \
"v_mfma_f32_16x16x32_bf16 v[68:71], a[132:135], v[216:219], v[68:71]\n" \
"v_cvt_pk_bf16_f32 v34, v36, v37\n" \
"v_cvt_pk_bf16_f32 v35, v38, v39\n" \
"s_cbranch_vccz Lg41_%=\n" \
"s_nop 0\n" \
"buffer_store_dwordx4 v[32:35], v208, %[srdz], s60 offen offset:0 sc1\n" \
"Lg41_%=:\n" \
"v_mfma_f32_16x16x32_bf16 v[68:71], a[164:167], v[200:203], v[68:71]\n" \
"v_pk_mul_f32 v[40:41], v[40:41], v[178:179] op_sel_hi:[1,0]\n" \
"v_pk_mul_f32 v[42:43], v[42:43], v[178:179] op_sel_hi:[1,0]\n" \
"v_pk_mul_f32 v[44:45], v[44:45], v[178:179] op_sel_hi:[1,0]\n" \
"v_mfma_f32_16x16x32_bf16 v[72:75], a[136:139], v[216:219], v[72:75]\n" \
"v_pk_mul_f32 v[46:47], v[46:47], v[178:179] op_sel_hi:[1,0]\n" \
"v_cvt_pk_bf16_f32 v40, v40, v41\n" \
"v_cvt_pk_bf16_f32 v41, v42, v43\n" \
"v_mfma_f32_16x16x32_bf16 v[72:75], a[168:171], v[200:203], v[72:75]\n" \
"v_cvt_pk_bf16_f32 v42, v44, v45\n" \
"v_cvt_pk_bf16_f32 v43, v46, v47\n" \
"s_cbranch_vccz Lg42_%=\n" \
"s_nop 0\n" \
"buffer_store_dwordx4 v[40:43], v208, %[srdz], s60 offen offset:1024 sc1\n" \
"Lg42_%=:\n" \
"v_mfma_f32_16x16x32_bf16 v[76:79], a[140:143], v[216:219], v[76:79]\n" \
"v_pk_mul_f32 v[48:49], v[48:49], v[178:179] op_sel_hi:[1,0]\n" \
"v_pk_mul_f32 v[50:51], v[50:51], v[178:179] op_sel_hi:[1,0]\n" \
"v_pk_mul_f32 v[52:53], v[52:53], v[178:179] op_sel_hi:[1,0]\n" \
"v_mfma_f32_16x16x32_bf16 v[76:79], a[172:175], v[200:203], v[76:79]\n" \
"v_pk_mul_f32 v[54:55], v[54:55], v[178:179] op_sel_hi:[1,0]\n" \
"v_cvt_pk_bf16_f32 v48, v48, v49\n" \
"v_cvt_pk_bf16_f32 v49, v50, v51\n" \
"v_mfma_f32_16x16x32_bf16 v[80:83], a[144:147], v[216:219], v[80:83]\n" \
"v_cvt_pk_bf16_f32 v50, v52, v53\n" \
"v_cvt_pk_bf16_f32 v51, v54, v55\n" \
"s_cbranch_vccz Lg43_%=\n" \
"s_nop 0\n" \
"buffer_store_dwordx4 v[48:51], v208, %[srdz], s60 offen offset:2048 sc1\n" \
"Lg43_%=:\n" \
"v_mfma_f32_16x16x32_bf16 v[80:83], a[176:179], v[200:203], v[80:83]\n" \
"v_pk_mul_f32 v[56:57], v[56:57], v[178:179] op_sel_hi:[1,0]\n" \
"v_pk_mul_f32 v[58:59], v[58:59], v[178:179] op_sel_hi:[1,0]\n" \
"v_pk_mul_f32 v[60:61], v[60:61], v[178:179] op_sel_hi:[1,0]\n" \
"v_mfma_f32_16x16x32_bf16 v[84:87], a[148:151], v[216:219], v[84:87]\n" \
"v_pk_mul_f32 v[62:63], v[62:63], v[178:179] op_sel_hi:[1,0]\n" \
"v_cvt_pk_bf16_f32 v56, v56, v57\n" \
"v_cvt_pk_bf16_f32 v57, v58, v59\n" \
"v_mfma_f32_16x16x32_bf16 v[84:87], a[180:183], v[200:203], v[84:87]\n" \
"v_cvt_pk_bf16_f32 v58, v60, v61\n" \
"v_cvt_pk_bf16_f32 v59, v62, v63\n" \
"s_cbranch_vccz Lg44_%=\n" \
"s_nop 0\n" \
"buffer_store_dwordx4 v[56:59], v208, %[srdz], s60 offen offset:3072 sc1\n" \
"Lg44_%=:\n" \
"v_mfma_f32_16x16x32_bf16 v[88:91], a[152:155], v[216:219], v[88:91]\n" \
"v_mfma_f32_16x16x32_bf16 v[88:91], a[184:187], v[200:203], v[88:91]\n" \
"v_mfma_f32_16x16x32_bf16 v[92:95], a[156:159], v[216:219], v[92:95]\n" \
"v_mfma_f32_16x16x32_bf16 v[92:95], a[188:191], v[200:203], v[92:95]\n" \
"s_waitcnt vmcnt(12) lgkmcnt(0)\n" \
"s_bitcmp1_b32 0xf, s48\n" \
"s_cbranch_scc1 Lcw1_%=\n" \
"s_waitcnt vmcnt(4)\n" \
"Lcw1_%=:\n" \
"s_barrier\n" \
"s_nop 0\n" \
"ds_read_b128 v[232:235], v236\n" \
"v_mfma_f32_16x16x32_bf16 v[96:99], a[160:163], v[204:207], v[96:99]\n" \
"v_pk_mul_f32 v[64:65], v[64:65], v[180:181] op_sel_hi:[1,0]\n" \
"v_pk_mul_f32 v[66:67], v[66:67], v[180:181] op_sel_hi:[1,0]\n" \
"v_pk_mul_f32 v[68:69], v[68:69], v[180:181] op_sel_hi:[1,0]\n" \
"v_pk_mul_f32 v[70:71], v[70:71], v[180:181] op_sel_hi:[1,0]\n" \
"v_cvt_pk_bf16_f32 v64, v64, v65\n" \
"v_mfma_f32_16x16x32_bf16 v[100:103], a[164:167], v[204:207], v[100:103]\n" \
"v_cvt_pk_bf16_f32 v65, v66, v67\n" \
"v_cvt_pk_bf16_f32 v66, v68, v69\n" \
"v_cvt_pk_bf16_f32 v67, v70, v71\n" \
"s_cbranch_vccz Lg45_%=\n" \
"s_nop 0\n" \
"buffer_store_dwordx4 v[64:67], v209, %[srdz], s60 offen offset:0 sc1\n" \
"Lg45_%=:\n" \
"v_pk_mul_f32 v[72:73], v[72:73], v[180:181] op_sel_hi:[1,0]\n" \
"v_mfma_f32_16x16x32_bf16 v[104:107], a[168:171], v[204:207], v[104:107]\n" \
"v_pk_mul_f32 v[74:75], v[74:75], v[180:181] op_sel_hi:[1,0]\n" \
"v_pk_mul_f32 v[76:77], v[76:77], v[180:181] op_sel_hi:[1,0]\n" \
"v_pk_mul_f32 v[78:79], v[78:79], v[180:181] op_sel_hi:[1,0]\n" \
"v_cvt_pk_bf16_f32 v72, v72, v73\n" \
"v_cvt_pk_bf16_f32 v73, v74, v75\n" \
"v_mfma_f32_16x16x32_bf16 v[108:111], a[172:175], v[204:207], v[108:111]\n" \
"v_cvt_pk_bf16_f32 v74, v76, v77\n" \
"v_cvt_pk_bf16_f32 v75, v78, v79\n" \
"s_cbranch_vccz Lg46_%=\n" \
"s_nop 0\n" \
"buffer_store_dwordx4 v[72:75], v209, %[srdz], s60 offen offset:1024 sc1\n" \
"Lg46_%=:\n" \
"v_pk_mul_f32 v[80:81], v[80:81], v[180:181] op_sel_hi:[1,0]\n" \
"v_pk_mul_f32 v[82:83], v[82:83], v[180:181] op_sel_hi:[1,0]\n" \
"v_mfma_f32_16x16x32_bf16 v[112:115], a[176:179], v[204:207], v[112:115]\n" \
"v_pk_mul_f32 v[84:85], v[84:85], v[180:181] op_sel_hi:[1,0]\n" \
"v_pk_mul_f32 v[86:87], v[86:87], v[180:181] op_sel_hi:[1,0]\n" \
"v_cvt_pk_bf16_f32 v80, v80, v81\n" \
"v_cvt_pk_bf16_f32 v81, v82, v83\n" \
"v_cvt_pk_bf16_f32 v82, v84, v85\n" \
"v_mfma_f32_16x16x32_bf16 v[116:119], a[180:183], v[204:207], v[116:119]\n" \
"v_cvt_pk_bf16_f32 v83, v86, v87\n" \
"s_cbranch_vccz Lg47_%=\n" \
"s_nop 0\n" \
"buffer_store_dwordx4 v[80:83], v209, %[srdz], s60 offen offset:2048 sc1\n" \
"Lg47_%=:\n" \
"v_pk_mul_f32 v[88:89], v[88:89], v[180:181] op_sel_hi:[1,0]\n" \
"v_pk_mul_f32 v[90:91], v[90:91], v[180:181] op_sel_hi:[1,0]\n" \
"v_pk_mul_f32 v[92:93], v[92:93], v[180:181] op_sel_hi:[1,0]\n" \
"v_mfma_f32_16x16x32_bf16 v[120:123], a[184:187], v[204:207], v[120:123]\n" \
"v_pk_mul_f32 v[94:95], v[94:95], v[180:181] op_sel_hi:[1,0]\n" \
"v_cvt_pk_bf16_f32 v88, v88, v89\n" \
"v_cvt_pk_bf16_f32 v89, v90, v91\n" \
"v_cvt_pk_bf16_f32 v90, v92, v93\n" \
"v_cvt_pk_bf16_f32 v91, v94, v95\n" \
"v_mfma_f32_16x16x32_bf16 v[124:127], a[188:191], v[204:207], v[124:127]\n" \
"s_cbranch_vccz Lg48_%=\n" \
"s_nop 0\n" \
"buffer_store_dwordx4 v[88:91], v209, %[srdz], s60 offen offset:3072 sc1\n" \
"Lg48_%=:\n" \
"s_waitcnt lgkmcnt(0)\n" \
"s_nop 0\n" \
"v_or3_b32 v232, v232, v233, v234\n" \
"v_or_b32_e32 v232, v232, v235\n" \
"s_nop 0\n" \
"v_readfirstlane_b32 s55, v232\n" \
"s_nop 3\n" \
"s_andn2_b32 s55, s55, s52\n" \
"s_cbranch_scc1 Lslow_%=\n" \
"v_mfma_f32_16x16x32_bf16 v[192:195], a[196:199], v[0:3], 0\n" \
"v_mfma_f32_16x16x32_bf16 v[192:195], a[200:203], v[8:11], v[192:195]\n" \
"v_pk_mul_f32 v[96:97], v[96:97], v[182:183] op_sel_hi:[1,0]\n" \
"v_pk_mul_f32 v[98:99], v[98:99], v[182:183] op_sel_hi:[1,0]\n" \
"v_mfma_f32_16x16x32_bf16 v[192:195], a[204:207], v[16:19], v[192:195]\n" \
"v_pk_mul_f32 v[100:101], v[100:101], v[182:183] op_sel_hi:[1,0]\n" \
"v_pk_mul_f32 v[102:103], v[102:103], v[182:183] op_sel_hi:[1,0]\n" \
"s_add_u32 m0, %[m0b], 114688\n" \
"s_cmp_gt_u32 s48, 1\n" \
"s_cselect_b32 s49, 1, 2\n" \
"s_lshl_b32 s49, s49, 18\n" \
"s_or_b32 s49, s49, s58\n" \
"buffer_load_dwordx4 %[wo0], %[srdt], s49 offen offset:0 lds\n" \
"v_mfma_f32_16x16x32_bf16 v[192:195], a[208:211], v[24:27], v[192:195]\n" \
"v_cvt_pk_bf16_f32 v96, v96, v97\n" \
"v_cvt_pk_bf16_f32 v97, v98, v99\n" \
"v_mfma_f32_16x16x32_bf16 v[144:147], a[212:215], v[0:3], 0\n" \
"v_cvt_pk_bf16_f32 v98, v100, v101\n" \
"v_mfma_f32_16x16x32_bf16 v[144:147], a[216:219], v[8:11], v[144:147]\n" \
"v_cvt_pk_bf16_f32 v99, v102, v103\n" \
"s_cbranch_vccz Lg1_%=\n" \
"s_nop 0\n" \
"buffer_store_dwordx4 v[96:99], v210, %[srdz], s60 offen offset:0 sc1\n" \
"Lg1_%=:\n" \
"v_mfma_f32_16x16x32_bf16 v[144:147], a[220:223], v[16:19], v[144:147]\n" \
"ds_read_b64_tr_b16 a[228:229], v215 offset:512\n" \
"v_pk_mul_f32 v[104:105], v[104:105], v[182:183] op_sel_hi:[1,0]\n" \
"v_pk_mul_f32 v[106:107], v[106:107], v[182:183] op_sel_hi:[1,0]\n" \
"v_mfma_f32_16x16x32_bf16 v[144:147], a[224:227], v[24:27], v[144:147]\n" \
"ds_read_b64_tr_b16 a[230:231], v215 offset:2560\n" \
"v_pk_mul_f32 v[108:109], v[108:109], v[182:183] op_sel_hi:[1,0]\n" \
"v_mfma_f32_16x16x32_bf16 v[196:199], a[196:199], v[32:35], 0\n" \
"ds_read_b64_tr_b16 a[232:233], v215 offset:4608\n" \
"v_pk_mul_f32 v[110:111], v[110:111], v[182:183] op_sel_hi:[1,0]\n" \
"v_cvt_pk_bf16_f32 v104, v104, v105\n" \
"v_mfma_f32_16x16x32_bf16 v[196:199], a[200:203], v[40:43], v[196:199]\n" \
"ds_read_b64_tr_b16 a[234:235], v215 offset:6656\n" \
"v_cvt_pk_bf16_f32 v105, v106, v107\n" \
"v_cvt_pk_bf16_f32 v106, v108, v109\n" \
"v_mfma_f32_16x16x32_bf16 v[196:199], a[204:207], v[48:51], v[196:199]\n" \
"v_cvt_pk_bf16_f32 v107, v110, v111\n" \
"s_cbranch_vccz Lg2_%=\n" \
"s_nop 0\n" \
"buffer_store_dwordx4 v[104:107], v210, %[srdz], s60 offen offset:1024 sc1\n" \
"Lg2_%=:\n" \
"v_mfma_f32_16x16x32_bf16 v[196:199], a[208:211], v[56:59], v[196:199]\n" \
"v_pk_mul_f32 v[112:113], v[112:113], v[182:183] op_sel_hi:[1,0]\n" \
"v_mfma_f32_16x16x32_bf16 v[148:151], a[212:215], v[32:35], 0\n" \
"v_pk_mul_f32 v[114:115], v[114:115], v[182:183] op_sel_hi:[1,0]\n" \
"v_pk_mul_f32 v[116:117], v[116:117], v[182:183] op_sel_hi:[1,0]\n" \
"buffer_load_dwordx4 v212, %[srdt], s49 offen offset:1024 lds\n" \
"v_mfma_f32_16x16x32_bf16 v[148:151], a[216:219], v[40:43], v[148:151]\n" \
"v_pk_mul_f32 v[118:119], v[118:119], v[182:183] op_sel_hi:[1,0]\n" \
"v_cvt_pk_bf16_f32 v112, v112, v113\n" \
"v_mfma_f32_16x16x32_bf16 v[148:151], a[220:223], v[48:51], v[148:151]\n" \
"ds_read_b64_tr_b16 a[236:237], v215 offset:8704\n" \
"v_cvt_pk_bf16_f32 v113, v114, v115\n" \
"v_mfma_f32_16x16x32_bf16 v[148:151], a[224:227], v[56:59], v[148:151]\n" \
"v_cvt_pk_bf16_f32 v114, v116, v117\n" \
"v_cvt_pk_bf16_f32 v115, v118, v119\n" \
"v_mfma_f32_16x16x32_bf16 v[200:203], a[196:199], v[64:67], 0\n" \
"ds_read_b64_tr_b16 a[238:239], v215 offset:10752\n" \
"s_cbranch_vccz Lg3_%=\n" \
"s_nop 0\n" \
"buffer_store_dwordx4 v[112:115], v210, %[srdz], s60 offen offset:2048 sc1\n" \
"Lg3_%=:\n" \
"v_pk_mul_f32 v[120:121], v[120:121], v[182:183] op_sel_hi:[1,0]\n" \
"v_mfma_f32_16x16x32_bf16 v[200:203], a[200:203], v[72:75], v[200:203]\n" \
"v_pk_mul_f32 v[122:123], v[122:123], v[182:183] op_sel_hi:[1,0]\n" \
"v_pk_mul_f32 v[124:125], v[124:125], v[182:183] op_sel_hi:[1,0]\n" \
"v_mfma_f32_16x16x32_bf16 v[200:203], a[204:207], v[80:83], v[200:203]\n" \
"v_pk_mul_f32 v[126:127], v[126:127], v[182:183] op_sel_hi:[1,0]\n" \
"v_mfma_f32_16x16x32_bf16 v[200:203], a[208:211], v[88:91], v[200:203]\n" \
"v_cvt_pk_bf16_f32 v120, v120, v121\n" \
".if . - adaln_body != 40960\n" \
".error \"size model\"\n" \
".endif\n" \
"Lk9_%=:\n" \
"s_cbranch_execz Lk10_%=\n" \
"s_nop 0\n" \
"v_cvt_pk_bf16_f32 v121, v122, v123\n" \
"v_mfma_f32_16x16x32_bf16 v[152:155], a[212:215], v[64:67], 0\n" \
"v_cvt_pk_bf16_f32 v122, v124, v125\n" \
"v_cvt_pk_bf16_f32 v123, v126, v127\n" \
"v_mfma_f32_16x16x32_bf16 v[152:155], a[216:219], v[72:75], v[152:155]\n" \
"s_cbranch_vccz Lg4_%=\n" \
"s_nop 0\n" \
"buffer_store_dwordx4 v[120:123], v210, %[srdz], s60 offen offset:3072 sc1\n" \
"Lg4_%=:\n" \
"v_mfma_f32_16x16x32_bf16 v[152:155], a[220:223], v[80:83], v[152:155]\n" \
"v_add_u32_e32 v220, 0x10000, v215\n" \
"v_mfma_f32_16x16x32_bf16 v[152:155], a[224:227], v[88:91], v[152:155]\n" \
"v_mov_b32_e64 v230, 1\n" \
"v_readlane_b32 s62, %[tlo], 19\n" \
"v_readlane_b32 s63, %[thi], 19\n" \
"v_and_b32_e32 v233, 0x380, v224\n" \
"v_lshlrev_b32_e64 v233, 2, v233\n" \
"buffer_load_dwordx4 v213, %[srdt], s49 offen offset:2048 lds\n" \
"v_mfma_f32_16x16x32_bf16 v[204:207], a[196:199], v[96:99], 0\n" \
"v_add_u32_e32 v221, 0x10000, v227\n" \
"v_mfma_f32_16x16x32_bf16 v[204:207], a[200:203], v[104:107], v[204:207]\n" \
"ds_read_b64_tr_b16 a[196:197], v215 offset:12800\n" \
"v_mfma_f32_16x16x32_bf16 v[204:207], a[204:207], v[112:115], v[204:207]\n" \
"ds_read_b64_tr_b16 a[198:199], v215 offset:14848\n" \
"v_mfma_f32_16x16x32_bf16 v[204:207], a[208:211], v[120:123], v[204:207]\n" \
"ds_read_b64_tr_b16 a[200:201], v227 offset:512\n" \
"v_mfma_f32_16x16x32_bf16 v[156:159], a[212:215], v[96:99], 0\n" \
"ds_read_b64_tr_b16 a[202:203], v227 offset:2560\n" \
"v_mfma_f32_16x16x32_bf16 v[156:159], a[216:219], v[104:107], v[156:159]\n" \
"ds_read_b64_tr_b16 a[204:205], v227 offset:4608\n" \
"v_mfma_f32_16x16x32_bf16 v[156:159], a[220:223], v[112:115], v[156:159]\n" \
"ds_read_b64_tr_b16 a[206:207], v227 offset:6656\n" \
"v_mfma_f32_16x16x32_bf16 v[156:159], a[224:227], v[120:123], v[156:159]\n" \
"s_waitcnt vmcnt(63) lgkmcnt(4)\n" \
"s_nop 0\n" \
"v_mfma_f32_16x16x32_bf16 v[160:163], a[228:231], v[0:3], 0\n" \
"ds_read_b64_tr_b16 a[208:209], v227 offset:8704\n" \
"v_mfma_f32_16x16x32_bf16 v[160:163], a[232:235], v[8:11], v[160:163]\n" \
"ds_read_b64_tr_b16 a[210:211], v227 offset:10752\n" \
"buffer_load_dwordx4 v214, %[srdt], s49 offen offset:3072 lds\n" \
"v_mfma_f32_16x16x32_bf16 v[164:167], a[228:231], v[32:35], 0\n" \
"ds_read_b64_tr_b16 a[212:213], v227 offset:12800\n" \
"v_mfma_f32_16x16x32_bf16 v[164:167], a[232:235], v[40:43], v[164:167]\n" \
"ds_read_b64_tr_b16 a[214:215], v227 offset:14848\n" \
"v_mfma_f32_16x16x32_bf16 v[168:171], a[228:231], v[64:67], 0\n" \
"v_mfma_f32_16x16x32_bf16 v[168:171], a[232:235], v[72:75], v[168:171]\n" \
"v_mfma_f32_16x16x32_bf16 v[172:175], a[228:231], v[96:99], 0\n" \
"ds_read_b64_tr_b16 a[216:217], v215 offset:16384\n" \
"v_mfma_f32_16x16x32_bf16 v[172:175], a[232:235], v[104:107], v[172:175]\n" \
"ds_read_b64_tr_b16 a[218:219], v215 offset:18432\n" \
"v_mfma_f32_16x16x32_bf16 v[160:163], a[236:239], v[16:19], v[160:163]\n" \
"ds_read_b64_tr_b16 a[220:221], v215 offset:20480\n" \
"v_mfma_f32_16x16x32_bf16 v[160:163], a[196:199], v[24:27], v[160:163]\n" \
"ds_read_b64_tr_b16 a[222:223], v215 offset:22528\n" \
"v_mfma_f32_16x16x32_bf16 v[164:167], a[236:239], v[48:51], v[164:167]\n" \
"v_mfma_f32_16x16x32_bf16 v[164:167], a[196:199], v[56:59], v[164:167]\n" \
"v_mfma_f32_16x16x32_bf16 v[168:171], a[236:239], v[80:83], v[168:171]\n" \
"s_waitcnt vmcnt(63) lgkmcnt(0)\n" \
"s_barrier\n" \
"v_mfma_f32_16x16x32_bf16 v[168:171], a[196:199], v[88:91], v[168:171]\n" \
"v_mfma_f32_16x16x32_bf16 v[172:175], a[236:239], v[112:115], v[172:175]\n" \
"ds_read_b64_tr_b16 a[224:225], v215 offset:24576\n" \
"v_mfma_f32_16x16x32_bf16 v[172:175], a[196:199], v[120:123], v[172:175]\n" \
"s_waitcnt vmcnt(63) lgkmcnt(5)\n" \
"s_nop 0\n" \
"v_mfma_f32_16x16x32_bf16 v[176:179], a[200:203], v[0:3], 0\n" \
"ds_read_b64_tr_b16 a[226:227], v215 offset:26624\n" \
"v_mfma_f32_16x16x32_bf16 v[176:179], a[204:207], v[8:11], v[176:179]\n" \
"ds_read_b64_tr_b16 a[228:229], v215 offset:28672\n" \
"s_add_u32 m0, %[m0b], 49152\n" \
"s_cmp_gt_u32 s48, 1\n" \
"s_cselect_b32 s49, 1, 2\n" \
"s_lshl_b32 s49, s49, 18\n" \
"s_or_b32 s49, s49, s59\n" \
"buffer_load_dwordx4 %[wo0], %[srdt], s49 offen offset:0 lds\n" \
"v_mfma_f32_16x16x32_bf16 v[180:183], a[200:203], v[32:35], 0\n" \
"ds_read_b64_tr_b16 a[230:231], v215 offset:30720\n" \
"v_mfma_f32_16x16x32_bf16 v[180:183], a[204:207], v[40:43], v[180:183]\n" \
"v_mfma_f32_16x16x32_bf16 v[184:187], a[200:203], v[64:67], 0\n" \
"v_mfma_f32_16x16x32_bf16 v[184:187], a[204:207], v[72:75], v[184:187]\n" \
"buffer_load_dwordx4 v212, %[srdt], s49 offen offset:1024 lds\n" \
"v_mfma_f32_16x16x32_bf16 v[188:191], a[200:203], v[96:99], 0\n" \
"ds_read_b64_tr_b16 a[232:233], v227 offset:16384\n" \
"v_mfma_f32_16x16x32_bf16 v[188:191], a[204:207], v[104:107], v[188:191]\n" \
"ds_read_b64_tr_b16 a[234:235], v227 offset:18432\n" \
"v_mfma_f32_16x16x32_bf16 v[176:179], a[208:211], v[16:19], v[176:179]\n" \
"ds_read_b64_tr_b16 a[236:237], v227 offset:20480\n" \
"v_mfma_f32_16x16x32_bf16 v[176:179], a[212:215], v[24:27], v[176:179]\n" \
"ds_read_b64_tr_b16 a[238:239], v227 offset:22528\n" \
"v_mfma_f32_16x16x32_bf16 v[180:183], a[208:211], v[48:51], v[180:183]\n" \
"v_mfma_f32_16x16x32_bf16 v[180:183], a[212:215], v[56:59], v[180:183]\n" \
"v_mfma_f32_16x16x32_bf16 v[184:187], a[208:211], v[80:83], v[184:187]\n" \
"v_mfma_f32_16x16x32_bf16 v[184:187], a[212:215], v[88:91], v[184:187]\n" \
"buffer_load_dwordx4 v213, %[srdt], s49 offen offset:2048 lds\n" \
"v_mfma_f32_16x16x32_bf16 v[188:191], a[208:211], v[112:115], v[188:191]\n" \
"ds_read_b64_tr_b16 a[196:197], v227 offset:24576\n" \
"v_mfma_f32_16x16x32_bf16 v[188:191], a[212:215], v[120:123], v[188:191]\n" \
"s_waitcnt vmcnt(63) lgkmcnt(5)\n" \
"s_nop 0\n" \
"v_mfma_f32_16x16x32_bf16 v[4:7], a[216:219], v[0:3], 0\n" \
"ds_read_b64_tr_b16 a[198:199], v227 offset:26624\n" \
"v_mfma_f32_16x16x32_bf16 v[4:7], a[220:223], v[8:11], v[4:7]\n" \
"ds_read_b64_tr_b16 a[200:201], v227 offset:28672\n" \
"v_mfma_f32_16x16x32_bf16 v[36:39], a[216:219], v[32:35], 0\n" \
"ds_read_b64_tr_b16 a[202:203], v227 offset:30720\n" \
"v_mfma_f32_16x16x32_bf16 v[36:39], a[220:223], v[40:43], v[36:39]\n" \
"v_mfma_f32_16x16x32_bf16 v[68:71], a[216:219], v[64:67], 0\n" \
"buffer_load_dwordx4 v214, %[srdt], s49 offen offset:3072 lds\n" \
"v_mfma_f32_16x16x32_bf16 v[68:71], a[220:223], v[72:75], v[68:71]\n" \
"v_mfma_f32_16x16x32_bf16 v[100:103], a[216:219], v[96:99], 0\n" \
"ds_read_b64_tr_b16 a[204:205], v215 offset:16896\n" \
"v_mfma_f32_16x16x32_bf16 v[100:103], a[220:223], v[104:107], v[100:103]\n" \
"ds_read_b64_tr_b16 a[206:207], v215 offset:18944\n" \
"v_mfma_f32_16x16x32_bf16 v[4:7], a[224:227], v[16:19], v[4:7]\n" \
"ds_read_b64_tr_b16 a[208:209], v215 offset:20992\n" \
"v_mfma_f32_16x16x32_bf16 v[4:7], a[228:231], v[24:27], v[4:7]\n" \
"ds_read_b64_tr_b16 a[210:211], v215 offset:23040\n" \
"v_mfma_f32_16x16x32_bf16 v[36:39], a[224:227], v[48:51], v[36:39]\n" \
"v_mfma_f32_16x16x32_bf16 v[36:39], a[228:231], v[56:59], v[36:39]\n" \
"v_mfma_f32_16x16x32_bf16 v[68:71], a[224:227], v[80:83], v[68:71]\n" \
"v_mfma_f32_16x16x32_bf16 v[68:71], a[228:231], v[88:91], v[68:71]\n" \
"v_mfma_f32_16x16x32_bf16 v[100:103], a[224:227], v[112:115], v[100:103]\n" \
"ds_read_b64_tr_b16 a[212:213], v215 offset:25088\n" \
"v_mfma_f32_16x16x32_bf16 v[100:103], a[228:231], v[120:123], v[100:103]\n" \
"s_waitcnt vmcnt(63) lgkmcnt(5)\n" \
"s_nop 0\n" \
"v_mfma_f32_16x16x32_bf16 v[12:15], a[232:235], v[0:3], 0\n" \
"ds_read_b64_tr_b16 a[214:215], v215 offset:27136\n" \
"s_cmp_gt_u32 s48, 0\n" \
"s_cselect_b32 s50, 0, 1\n" \
"s_mul_i32 s50, s50, 152064\n" \
"buffer_load_dwordx4 a[64:67], %[oq0], %[srdz], s50 offen offset:0 sc1\n" \
"v_mfma_f32_16x16x32_bf16 v[12:15], a[236:239], v[8:11], v[12:15]\n" \
"ds_read_b64_tr_b16 a[216:217], v215 offset:29184\n" \
"v_mfma_f32_16x16x32_bf16 v[44:47], a[232:235], v[32:35], 0\n" \
"ds_read_b64_tr_b16 a[218:219], v215 offset:31232\n" \
"buffer_load_dwordx4 a[68:71], %[oq0], %[srdz], s50 offen offset:1024 sc1\n" \
"v_mfma_f32_16x16x32_bf16 v[44:47], a[236:239], v[40:43], v[44:47]\n" \
"v_mfma_f32_16x16x32_bf16 v[76:79], a[232:235], v[64:67], 0\n" \
"buffer_load_dwordx4 a[80:83], v208, %[srdz], s50 offen offset:0 sc1\n" \
"v_mfma_f32_16x16x32_bf16 v[76:79], a[236:239], v[72:75], v[76:79]\n" \
"v_mfma_f32_16x16x32_bf16 v[108:111], a[232:235], v[96:99], 0\n" \
"ds_read_b64_tr_b16 a[220:221], v227 offset:16896\n" \
"buffer_load_dwordx4 a[84:87], v208, %[srdz], s50 offen offset:1024 sc1\n" \
"v_mfma_f32_16x16x32_bf16 v[108:111], a[236:239], v[104:107], v[108:111]\n" \
"ds_read_b64_tr_b16 a[222:223], v227 offset:18944\n" \
"v_mfma_f32_16x16x32_bf16 v[12:15], a[196:199], v[16:19], v[12:15]\n" \
"ds_read_b64_tr_b16 a[224:225], v227 offset:25088\n" \
"buffer_load_dwordx4 a[96:99], v209, %[srdz], s50 offen offset:0 sc1\n" \
"v_mfma_f32_16x16x32_bf16 v[12:15], a[200:203], v[24:27], v[12:15]\n" \
"ds_read_b64_tr_b16 a[226:227], v227 offset:27136\n" \
"v_mfma_f32_16x16x32_bf16 v[44:47], a[196:199], v[48:51], v[44:47]\n" \
"ds_read_b64_tr_b16 a[228:229], v227 offset:20992\n" \
"buffer_load_dwordx4 a[100:103], v209, %[srdz], s50 offen offset:1024 sc1\n" \
"v_mfma_f32_16x16x32_bf16 v[44:47], a[200:203], v[56:59], v[44:47]\n" \
"ds_read_b64_tr_b16 a[230:231], v227 offset:23040\n" \
"v_mfma_f32_16x16x32_bf16 v[76:79], a[196:199], v[80:83], v[76:79]\n" \
"ds_read_b64_tr_b16 a[232:233], v227 offset:29184\n" \
"buffer_load_dwordx4 a[112:115], v210, %[srdz], s50 offen offset:0 sc1\n" \
"v_mfma_f32_16x16x32_bf16 v[76:79], a[200:203], v[88:91], v[76:79]\n" \
"ds_read_b64_tr_b16 a[234:235], v227 offset:31232\n" \
"v_mfma_f32_16x16x32_bf16 v[108:111], a[196:199], v[112:115], v[108:111]\n" \
"ds_read_b64_tr_b16 a[236:237], v215 offset:32768\n" \
"buffer_load_dwordx4 a[116:119], v210, %[srdz], s50 offen offset:1024 sc1\n" \
"v_mfma_f32_16x16x32_bf16 v[108:111], a[200:203], v[120:123], v[108:111]\n" \
"s_waitcnt vmcnt(63) lgkmcnt(13)\n" \
"s_nop 0\n" \
"v_mfma_f32_16x16x32_bf16 v[20:23], a[204:207], v[0:3], 0\n" \
"ds_read_b64_tr_b16 a[238:239], v215 offset:34816\n" \
"buffer_load_dwordx4 a[72:75], %[oq0], %[srdz], s50 offen offset:2048 sc1\n" \
"v_mfma_f32_16x16x32_bf16 v[20:23], a[208:211], v[8:11], v[20:23]\n" \
"ds_read_b64_tr_b16 a[196:197], v215 offset:36864\n" \
"v_mfma_f32_16x16x32_bf16 v[52:55], a[204:207], v[32:35], 0\n" \
"ds_read_b64_tr_b16 a[198:199], v215 offset:38912\n" \
"buffer_load_dwordx4 a[76:79], %[oq0], %[srdz], s50 offen offset:3072 sc1\n" \
"v_mfma_f32_16x16x32_bf16 v[52:55], a[208:211], v[40:43], v[52:55]\n" \
"v_mfma_f32_16x16x32_bf16 v[84:87], a[204:207], v[64:67], 0\n" \
"s_waitcnt vmcnt(63) lgkmcnt(4)\n" \
"s_barrier\n" \
"v_mfma_f32_16x16x32_bf16 v[84:87], a[208:211], v[72:75], v[84:87]\n" \
"buffer_load_dwordx4 a[88:91], v208, %[srdz], s50 offen offset:2048 sc1\n" \
"v_mfma_f32_16x16x32_bf16 v[116:119], a[204:207], v[96:99], 0\n" \
"ds_read_b64_tr_b16 a[200:201], v215 offset:40960\n" \
"v_mfma_f32_16x16x32_bf16 v[116:119], a[208:211], v[104:107], v[116:119]\n" \
"ds_read_b64_tr_b16 a[202:203], v215 offset:43008\n" \
"buffer_load_dwordx4 a[92:95], v208, %[srdz], s50 offen offset:3072 sc1\n" \
"v_mfma_f32_16x16x32_bf16 v[20:23], a[212:215], v[16:19], v[20:23]\n" \
"ds_read_b64_tr_b16 a[204:205], v215 offset:45056\n" \
"v_mfma_f32_16x16x32_bf16 v[20:23], a[216:219], v[24:27], v[20:23]\n" \
"ds_read_b64_tr_b16 a[206:207], v215 offset:47104\n" \
"buffer_load_dwordx4 a[104:107], v209, %[srdz], s50 offen offset:2048 sc1\n" \
"v_mfma_f32_16x16x32_bf16 v[52:55], a[212:215], v[48:51], v[52:55]\n" \
"v_mfma_f32_16x16x32_bf16 v[52:55], a[216:219], v[56:59], v[52:55]\n" \
"buffer_load_dwordx4 a[108:111], v209, %[srdz], s50 offen offset:3072 sc1\n" \
"v_mfma_f32_16x16x32_bf16 v[84:87], a[212:215], v[80:83], v[84:87]\n" \
"v_mfma_f32_16x16x32_bf16 v[84:87], a[216:219], v[88:91], v[84:87]\n" \
"buffer_load_dwordx4 a[120:123], v210, %[srdz], s50 offen offset:2048 sc1\n" \
"v_mfma_f32_16x16x32_bf16 v[116:119], a[212:215], v[112:115], v[116:119]\n" \
"v_mfma_f32_16x16x32_bf16 v[116:119], a[216:219], v[120:123], v[116:119]\n" \
"s_waitcnt vmcnt(63) lgkmcnt(10)\n" \
"s_nop 0\n" \
"v_mfma_f32_16x16x32_bf16 v[28:31], a[220:223], v[0:3], 0\n" \
"buffer_load_dwordx4 a[124:127], v210, %[srdz], s50 offen offset:3072 sc1\n" \
"v_mfma_f32_16x16x32_bf16 v[28:31], a[228:231], v[8:11], v[28:31]\n" \
"s_sub_u32 s61, s48, 4\n" \
"s_cmp_lt_u32 s61, 2\n" \
"s_cselect_b64 vcc, -1, 0\n" \
"s_nop 0\n" \
"v_mfma_f32_16x16x32_bf16 v[60:63], a[220:223], v[32:35], 0\n" \
"s_cbranch_vccz Lg5_%=\n" \
"s_nop 0\n" \
"buffer_store_dwordx4 v[0:3], %[oq0], %[srdz], s60 offen offset:0 sc1\n" \
"Lg5_%=:\n" \
"v_mfma_f32_16x16x32_bf16 v[60:63], a[228:231], v[40:43], v[60:63]\n" \
"v_mfma_f32_16x16x32_bf16 v[92:95], a[220:223], v[64:67], 0\n" \
"s_cmp_gt_u32 s48, 1\n" \
"s_cselect_b32 s50, 1, 2\n" \
"s_mul_i32 s50, s50, 152064\n" \
"buffer_load_dwordx4 a[0:3], %[oq0], %[srdz], s50 offen offset:0 sc1\n" \
"v_mfma_f32_16x16x32_bf16 v[92:95], a[228:231], v[72:75], v[92:95]\n" \
"v_mfma_f32_16x16x32_bf16 v[124:127], a[220:223], v[96:99], 0\n" \
"ds_read_b64_tr_b16 a[208:209], v227 offset:32768\n" \
"v_mfma_f32_16x16x32_bf16 v[124:127], a[228:231], v[104:107], v[124:127]\n" \
"ds_read_b64_tr_b16 a[210:211], v227 offset:34816\n" \
"s_waitcnt vmcnt(15) lgkmcnt(6)\n" \
"s_nop 0\n" \
"v_mfma_f32_16x16x32_bf16 v[128:131], a[236:239], a[64:67], v[192:195]\n" \
"ds_read_b64_tr_b16 a[212:213], v227 offset:36864\n" \
"v_mfma_f32_16x16x32_bf16 v[128:131], a[196:199], a[68:71], v[128:131]\n" \
"ds_read_b64_tr_b16 a[214:215], v227 offset:38912\n" \
"s_waitcnt vmcnt(13) lgkmcnt(15)\n" \
"s_nop 0\n" \
"v_mfma_f32_16x16x32_bf16 v[132:135], a[236:239], a[80:83], v[196:199]\n" \
"s_cbranch_vccz Lg6_%=\n" \
"s_nop 0\n" \
"buffer_store_dwordx4 v[8:11], %[oq0], %[srdz], s60 offen offset:1024 sc1\n" \
"Lg6_%=:\n" \
"v_mfma_f32_16x16x32_bf16 v[132:135], a[196:199], a[84:87], v[132:135]\n" \
"s_waitcnt vmcnt(13) lgkmcnt(15)\n" \
"s_bitcmp1_b32 0x30, s48\n" \
"s_cbranch_scc1 Lcw2_%=\n" \
"s_waitcnt vmcnt(11)\n" \
"Lcw2_%=:\n" \
"v_mfma_f32_16x16x32_bf16 v[136:139], a[236:239], a[96:99], v[200:203]\n" \
"buffer_load_dwordx4 a[4:7], %[oq0], %[srdz], s50 offen offset:1024 sc1\n" \
"v_mfma_f32_16x16x32_bf16 v[136:139], a[196:199], a[100:103], v[136:139]\n" \
"s_waitcnt vmcnt(12) lgkmcnt(15)\n" \
"s_bitcmp1_b32 0x30, s48\n" \
"s_cbranch_scc1 Lcw3_%=\n" \
"s_waitcnt vmcnt(10)\n" \
"Lcw3_%=:\n" \
"v_mfma_f32_16x16x32_bf16 v[140:143], a[236:239], a[112:115], v[204:207]\n" \
"ds_read_b64_tr_b16 a[216:217], v227 offset:40960\n" \
"v_mfma_f32_16x16x32_bf16 v[140:143], a[196:199], a[116:119], v[140:143]\n" \
"s_waitcnt vmcnt(10) lgkmcnt(5)\n" \
"s_bitcmp1_b32 0x30, s48\n" \
"s_cbranch_scc1 Lcw4_%=\n" \
"s_waitcnt vmcnt(8)\n" \
"Lcw4_%=:\n" \
"v_mfma_f32_16x16x32_bf16 v[128:131], a[200:203], a[72:75], v[128:131]\n" \
"ds_read_b64_tr_b16 a[218:219], v227 offset:43008\n" \
"buffer_load_dwordx4 a[8:11], %[oq0], %[srdz], s50 offen offset:2048 sc1\n" \
"v_mfma_f32_16x16x32_bf16 v[128:131], a[204:207], a[76:79], v[128:131]\n" \
"ds_read_b64_tr_b16 a[220:221], v227 offset:45056\n" \
"s_waitcnt vmcnt(9) lgkmcnt(15)\n" \
"s_bitcmp1_b32 0x30, s48\n" \
"s_cbranch_scc1 Lcw5_%=\n" \
"s_waitcnt vmcnt(7)\n" \
"Lcw5_%=:\n" \
"v_mfma_f32_16x16x32_bf16 v[132:135], a[200:203], a[88:91], v[132:135]\n" \
"ds_read_b64_tr_b16 a[222:223], v227 offset:47104\n" \
"v_mfma_f32_16x16x32_bf16 v[132:135], a[204:207], a[92:95], v[132:135]\n" \
"s_cbranch_vccz Lg7_%=\n" \
"s_nop 0\n" \
"buffer_store_dwordx4 v[16:19], %[oq0], %[srdz], s60 offen offset:2048 sc1\n" \
"Lg7_%=:\n" \
"s_waitcnt vmcnt(6) lgkmcnt(14)\n" \
"s_bitcmp1_b32 0x30, s48\n" \
"s_cbranch_scc1 Lcw6_%=\n" \
"s_waitcnt vmcnt(3)\n" \
"Lcw6_%=:\n" \
"v_mfma_f32_16x16x32_bf16 v[136:139], a[200:203], a[104:107], v[136:139]\n" \
"v_mfma_f32_16x16x32_bf16 v[136:139], a[204:207], a[108:111], v[136:139]\n" \
"buffer_load_dwordx4 a[12:15], %[oq0], %[srdz], s50 offen offset:3072 sc1\n" \
"v_mfma_f32_16x16x32_bf16 v[140:143], a[200:203], a[120:123], v[140:143]\n" \
"ds_read_b64_tr_b16 a[228:229], v215 offset:33280\n" \
"v_mfma_f32_16x16x32_bf16 v[140:143], a[204:207], a[124:127], v[140:143]\n" \
"ds_read_b64_tr_b16 a[230:231], v215 offset:35328\n" \
"v_mfma_f32_16x16x32_bf16 v[28:31], a[224:227], v[16:19], v[28:31]\n" \
"buffer_load_dwordx4 a[16:19], v208, %[srdz], s50 offen offset:0 sc1\n" \
"v_mfma_f32_16x16x32_bf16 v[28:31], a[232:235], v[24:27], v[28:31]\n" \
"v_mfma_f32_16x16x32_bf16 v[60:63], a[224:227], v[48:51], v[60:63]\n" \
"v_mfma_f32_16x16x32_bf16 v[60:63], a[232:235], v[56:59], v[60:63]\n" \
"s_cbranch_vccz Lg8_%=\n" \
"s_nop 0\n" \
"buffer_store_dwordx4 v[24:27], %[oq0], %[srdz], s60 offen offset:3072 sc1\n" \
"Lg8_%=:\n" \
"v_mfma_f32_16x16x32_bf16 v[92:95], a[224:227], v[80:83], v[92:95]\n" \
"v_mfma_f32_16x16x32_bf16 v[92:95], a[232:235], v[88:91], v[92:95]\n" \
"v_cmp_u_f32_e64 s[44:45], v128, v132\n" \
"buffer_load_dwordx4 a[20:23], v208, %[srdz], s50 offen offset:1024 sc1\n" \
"v_mfma_f32_16x16x32_bf16 v[124:127], a[224:227], v[112:115], v[124:127]\n" \
"v_mfma_f32_16x16x32_bf16 v[124:127], a[232:235], v[120:123], v[124:127]\n" \
"v_cmp_u_f32_e64 s[56:57], v136, v140\n" \
"s_or_b64 s[56:57], s[56:57], s[44:45]\n" \
"s_cbranch_scc1 Lsp1_%=\n" \
"Lbk1_%=:\n" \
"s_waitcnt vmcnt(63) lgkmcnt(2)\n" \
"s_nop 0\n" \
"v_mfma_f32_16x16x32_bf16 v[144:147], a[208:211], a[64:67], v[144:147]\n" \
"ds_read_b64_tr_b16 a[224:225], v215 offset:37376\n" \
"v_mfma_f32_16x16x32_bf16 v[144:147], a[212:215], a[68:71], v[144:147]\n" \
"ds_read_b64_tr_b16 a[226:227], v215 offset:39424\n" \
"buffer_load_dwordx4 a[24:27], v208, %[srdz], s50 offen offset:2048 sc1\n" \
"v_mfma_f32_16x16x32_bf16 v[148:151], a[208:211], a[80:83], v[148:151]\n" \
"ds_read_b64_tr_b16 a[196:197], v215 offset:41472\n" \
"v_mfma_f32_16x16x32_bf16 v[148:151], a[212:215], a[84:87], v[148:151]\n" \
"ds_read_b64_tr_b16 a[198:199], v215 offset:43520\n" \
"v_mfma_f32_16x16x32_bf16 v[152:155], a[208:211], a[96:99], v[152:155]\n" \
"ds_read_b64_tr_b16 a[200:201], v215 offset:45568\n" \
"s_cbranch_vccz Lg9_%=\n" \
"s_nop 0\n" \
"buffer_store_dwordx4 v[32:35], v208, %[srdz], s60 offen offset:0 sc1\n" \
"Lg9_%=:\n" \
"v_mfma_f32_16x16x32_bf16 v[152:155], a[212:215], a[100:103], v[152:155]\n" \
"ds_read_b64_tr_b16 a[202:203], v215 offset:47616\n" \
"v_mfma_f32_16x16x32_bf16 v[156:159], a[208:211], a[112:115], v[156:159]\n" \
"ds_read_b64_tr_b16 a[204:205], v227 offset:33280\n" \
"buffer_load_dwordx4 a[28:31], v208, %[srdz], s50 offen offset:3072 sc1\n" \
"v_mfma_f32_16x16x32_bf16 v[156:159], a[212:215], a[116:119], v[156:159]\n" \
"ds_read_b64_tr_b16 a[206:207], v227 offset:35328\n" \
"v_mfma_f32_16x16x32_bf16 v[144:147], a[216:219], a[72:75], v[144:147]\n" \
"ds_read_b64_tr_b16 a[232:233], v227 offset:37376\n" \
"v_mfma_f32_16x16x32_bf16 v[144:147], a[220:223], a[76:79], v[144:147]\n" \
"ds_read_b64_tr_b16 a[234:235], v227 offset:39424\n" \
"v_mfma_f32_16x16x32_bf16 v[148:151], a[216:219], a[88:91], v[148:151]\n" \
"buffer_load_dwordx4 a[32:35], v209, %[srdz], s50 offen offset:0 sc1\n" \
"v_mfma_f32_16x16x32_bf16 v[148:151], a[220:223], a[92:95], v[148:151]\n" \
"v_mfma_f32_16x16x32_bf16 v[152:155], a[216:219], a[104:107], v[152:155]\n" \
"s_cbranch_vccz Lg10_%=\n" \
"s_nop 0\n" \
"buffer_store_dwordx4 v[40:43], v208, %[srdz], s60 offen offset:1024 sc1\n" \
"Lg10_%=:\n" \
"v_mfma_f32_16x16x32_bf16 v[152:155], a[220:223], a[108:111], v[152:155]\n" \
"v_mfma_f32_16x16x32_bf16 v[156:159], a[216:219], a[120:123], v[156:159]\n" \
"ds_read_b64_tr_b16 a[236:237], v227 offset:41472\n" \
"buffer_load_dwordx4 a[36:39], v209, %[srdz], s50 offen offset:1024 sc1\n" \
"v_mfma_f32_16x16x32_bf16 v[156:159], a[220:223], a[124:127], v[156:159]\n" \
"s_waitcnt vmcnt(63) lgkmcnt(5)\n" \
"s_nop 0\n" \
"v_mfma_f32_16x16x32_bf16 v[160:163], a[228:231], a[64:67], v[160:163]\n" \
"ds_read_b64_tr_b16 a[238:239], v227 offset:43520\n" \
"v_mfma_f32_16x16x32_bf16 v[160:163], a[224:227], a[68:71], v[160:163]\n" \
"ds_read_b64_tr_b16 a[208:209], v227 offset:45568\n" \
"v_mfma_f32_16x16x32_bf16 v[164:167], a[228:231], a[80:83], v[164:167]\n" \
"ds_read_b64_tr_b16 a[210:211], v227 offset:47616\n" \
"buffer_load_dwordx4 a[40:43], v209, %[srdz], s50 offen offset:2048 sc1\n" \
"v_mfma_f32_16x16x32_bf16 v[164:167], a[224:227], a[84:87], v[164:167]\n" \
"v_mfma_f32_16x16x32_bf16 v[168:171], a[228:231], a[96:99], v[168:171]\n" \
"v_mfma_f32_16x16x32_bf16 v[168:171], a[224:227], a[100:103], v[168:171]\n" \
"s_cbranch_vccz Lg11_%=\n" \
"s_nop 0\n" \
"buffer_store_dwordx4 v[48:51], v208, %[srdz], s60 offen offset:2048 sc1\n" \
"Lg11_%=:\n" \
"v_mfma_f32_16x16x32_bf16 v[172:175], a[228:231], a[112:115], v[172:175]\n" \
"v_mfma_f32_16x16x32_bf16 v[172:175], a[224:227], a[116:119], v[172:175]\n" \
"buffer_load_dwordx4 a[44:47], v209, %[srdz], s50 offen offset:3072 sc1\n" \
"v_mfma_f32_16x16x32_bf16 v[160:163], a[196:199], a[72:75], v[160:163]\n" \
"v_mfma_f32_16x16x32_bf16 v[160:163], a[200:203], a[76:79], v[160:163]\n" \
"buffer_load_dwordx4 a[48:51], v210, %[srdz], s50 offen offset:0 sc1\n" \
"v_mfma_f32_16x16x32_bf16 v[164:167], a[196:199], a[88:91], v[164:167]\n" \
"v_mfma_f32_16x16x32_bf16 v[164:167], a[200:203], a[92:95], v[164:167]\n" \
"buffer_load_dwordx4 a[52:55], v210, %[srdz], s50 offen offset:1024 sc1\n" \
"v_mfma_f32_16x16x32_bf16 v[168:171], a[196:199], a[104:107], v[168:171]\n" \
"s_waitcnt vmcnt(54) lgkmcnt(0)\n" \
"s_bitcmp1_b32 0xf, s48\n" \
"s_cbranch_scc1 Lcw7_%=\n" \
"s_waitcnt vmcnt(45)\n" \
"s_bitcmp1_b32 0x30, s48\n" \
"s_cbranch_scc1 Lcw7_%=\n" \
"s_waitcnt vmcnt(38)\n" \
"Lcw7_%=:\n" \
"s_barrier\n" \
"v_mfma_f32_16x16x32_bf16 v[168:171], a[200:203], a[108:111], v[168:171]\n" \
"ds_read_b64_tr_b16 a[212:213], v220 offset:32768\n" \
"s_cbranch_vccz Lg12_%=\n" \
"s_nop 0\n" \
"buffer_store_dwordx4 v[56:59], v208, %[srdz], s60 offen offset:3072 sc1\n" \
"Lg12_%=:\n" \
"v_mfma_f32_16x16x32_bf16 v[172:175], a[196:199], a[120:123], v[172:175]\n" \
"ds_read_b64_tr_b16 a[214:215], v220 offset:34816\n" \
"v_mfma_f32_16x16x32_bf16 v[172:175], a[200:203], a[124:127], v[172:175]\n" \
"s_waitcnt vmcnt(63) lgkmcnt(2)\n" \
"s_nop 0\n" \
"v_mfma_f32_16x16x32_bf16 v[176:179], a[204:207], a[64:67], v[176:179]\n" \
"ds_read_b64_tr_b16 a[216:217], v220 offset:36864\n" \
"buffer_load_dwordx4 a[56:59], v210, %[srdz], s50 offen offset:2048 sc1\n" \
"v_mfma_f32_16x16x32_bf16 v[176:179], a[232:235], a[68:71], v[176:179]\n" \
"ds_read_b64_tr_b16 a[218:219], v220 offset:38912\n" \
"v_mfma_f32_16x16x32_bf16 v[180:183], a[204:207], a[80:83], v[180:183]\n" \
"ds_read_b64_tr_b16 a[220:221], v220 offset:40960\n" \
"v_mfma_f32_16x16x32_bf16 v[180:183], a[232:235], a[84:87], v[180:183]\n" \
"ds_read_b64_tr_b16 a[222:223], v220 offset:43008\n" \
"v_mfma_f32_16x16x32_bf16 v[184:187], a[204:207], a[96:99], v[184:187]\n" \
"ds_read_b64_tr_b16 a[228:229], v220 offset:45056\n" \
"buffer_load_dwordx4 a[60:63], v210, %[srdz], s50 offen offset:3072 sc1\n" \
"v_mfma_f32_16x16x32_bf16 v[184:187], a[232:235], a[100:103], v[184:187]\n" \
"ds_read_b64_tr_b16 a[230:231], v220 offset:47104\n" \
"v_mfma_f32_16x16x32_bf16 v[188:191], a[204:207], a[112:115], v[188:191]\n" \
"ds_read_b64_tr_b16 a[224:225], v221 offset:32768\n" \
"s_cbranch_vccz Lg13_%=\n" \
"s_nop 0\n" \
"buffer_store_dwordx4 v[64:67], v209, %[srdz], s60 offen offset:0 sc1\n" \
"Lg13_%=:\n" \
"v_mfma_f32_16x16x32_bf16 v[188:191], a[232:235], a[116:119], v[188:191]\n" \
"ds_read_b64_tr_b16 a[226:227], v221 offset:34816\n" \
"v_mfma_f32_16x16x32_bf16 v[176:179], a[236:239], a[72:75], v[176:179]\n" \
"ds_read_b64_tr_b16 a[196:197], v221 offset:36864\n" \
"v_mfma_f32_16x16x32_bf16 v[176:179], a[208:211], a[76:79], v[176:179]\n" \
"ds_read_b64_tr_b16 a[198:199], v221 offset:38912\n" \
"v_mfma_f32_16x16x32_bf16 v[180:183], a[236:239], a[88:91], v[180:183]\n" \
"s_cmp_gt_u32 s48, 2\n" \
"s_cselect_b32 s50, 2, 3\n" \
"s_mul_i32 s50, s50, 152064\n" \
"buffer_load_dwordx4 a[128:131], %[oq0], %[srdz], s50 offen offset:0 sc1\n" \
"v_mfma_f32_16x16x32_bf16 v[180:183], a[208:211], a[92:95], v[180:183]\n" \
"v_mfma_f32_16x16x32_bf16 v[184:187], a[236:239], a[104:107], v[184:187]\n" \
"v_mfma_f32_16x16x32_bf16 v[184:187], a[208:211], a[108:111], v[184:187]\n" \
"v_mfma_f32_16x16x32_bf16 v[188:191], a[236:239], a[120:123], v[188:191]\n" \
"ds_read_b64_tr_b16 a[200:201], v221 offset:40960\n" \
"s_cbranch_vccz Lg14_%=\n" \
"s_nop 0\n" \
"buffer_store_dwordx4 v[72:75], v209, %[srdz], s60 offen offset:1024 sc1\n" \
"Lg14_%=:\n" \
"v_mfma_f32_16x16x32_bf16 v[188:191], a[208:211], a[124:127], v[188:191]\n" \
"s_waitcnt vmcnt(63) lgkmcnt(5)\n" \
"s_nop 0\n" \
"v_mfma_f32_16x16x32_bf16 v[4:7], a[212:215], a[64:67], v[4:7]\n" \
"ds_read_b64_tr_b16 a[202:203], v221 offset:43008\n" \
"v_mfma_f32_16x16x32_bf16 v[4:7], a[216:219], a[68:71], v[4:7]\n" \
"ds_read_b64_tr_b16 a[204:205], v221 offset:45056\n" \
"s_add_u32 m0, %[m0b], 0\n" \
"s_cmp_gt_u32 s48, 2\n" \
"s_cselect_b32 s49, 2, 3\n" \
"s_lshl_b32 s49, s49, 18\n" \
"s_or_b32 s49, s49, s58\n" \
"s_nop 0\n" \
"buffer_load_dwordx4 %[wo0], %[srdt], s49 offen offset:0 lds\n" \
"v_mfma_f32_16x16x32_bf16 v[36:39], a[212:215], a[80:83], v[36:39]\n" \
"ds_read_b64_tr_b16 a[206:207], v221 offset:47104\n" \
"v_mfma_f32_16x16x32_bf16 v[36:39], a[216:219], a[84:87], v[36:39]\n" \
"buffer_load_dwordx4 a[132:135], %[oq0], %[srdz], s50 offen offset:1024 sc1\n" \
"v_mfma_f32_16x16x32_bf16 v[68:71], a[212:215], a[96:99], v[68:71]\n" \
"v_mfma_f32_16x16x32_bf16 v[68:71], a[216:219], a[100:103], v[68:71]\n" \
"v_mfma_f32_16x16x32_bf16 v[100:103], a[212:215], a[112:115], v[100:103]\n" \
"ds_read_b64_tr_b16 a[232:233], v220 offset:33280\n" \
"v_mfma_f32_16x16x32_bf16 v[100:103], a[216:219], a[116:119], v[100:103]\n" \
"ds_read_b64_tr_b16 a[234:235], v220 offset:35328\n" \
"s_cbranch_vccz Lg15_%=\n" \
"s_nop 0\n" \
"buffer_store_dwordx4 v[80:83], v209, %[srdz], s60 offen offset:2048 sc1\n" \
"Lg15_%=:\n" \
"v_mfma_f32_16x16x32_bf16 v[4:7], a[220:223], a[72:75], v[4:7]\n" \
"ds_read_b64_tr_b16 a[236:237], v220 offset:37376\n" \
"v_mfma_f32_16x16x32_bf16 v[4:7], a[228:231], a[76:79], v[4:7]\n" \
"ds_read_b64_tr_b16 a[238:239], v220 offset:39424\n" \
"buffer_load_dwordx4 a[136:139], %[oq0], %[srdz], s50 offen offset:2048 sc1\n" \
"v_mfma_f32_16x16x32_bf16 v[36:39], a[220:223], a[88:91], v[36:39]\n" \
"v_mfma_f32_16x16x32_bf16 v[36:39], a[228:231], a[92:95], v[36:39]\n" \
"v_mfma_f32_16x16x32_bf16 v[68:71], a[220:223], a[104:107], v[68:71]\n" \
"v_mfma_f32_16x16x32_bf16 v[68:71], a[228:231], a[108:111], v[68:71]\n" \
"buffer_load_dwordx4 a[140:143], %[oq0], %[srdz], s50 offen offset:3072 sc1\n" \
"v_mfma_f32_16x16x32_bf16 v[100:103], a[220:223], a[120:123], v[100:103]\n" \
"ds_read_b64_tr_b16 a[208:209], v220 offset:41472\n" \
"v_mfma_f32_16x16x32_bf16 v[100:103], a[228:231], a[124:127], v[100:103]\n" \
"s_waitcnt vmcnt(63) lgkmcnt(5)\n" \
"s_nop 0\n" \
"v_mfma_f32_16x16x32_bf16 v[12:15], a[224:227], a[64:67], v[12:15]\n" \
"ds_read_b64_tr_b16 a[210:211], v220 offset:43520\n" \
"s_cbranch_vccz Lg16_%=\n" \
"s_nop 0\n" \
"buffer_store_dwordx4 v[88:91], v209, %[srdz], s60 offen offset:3072 sc1\n" \
"Lg16_%=:\n" \
"v_mfma_f32_16x16x32_bf16 v[12:15], a[196:199], a[68:71], v[12:15]\n" \
"ds_read_b64_tr_b16 a[212:213], v220 offset:45568\n" \
"v_mfma_f32_16x16x32_bf16 v[44:47], a[224:227], a[80:83], v[44:47]\n" \
"ds_read_b64_tr_b16 a[214:215], v220 offset:47616\n" \
"buffer_load_dwordx4 v212, %[srdt], s49 offen offset:1024 lds\n" \
"v_mfma_f32_16x16x32_bf16 v[44:47], a[196:199], a[84:87], v[44:47]\n" \
"v_mfma_f32_16x16x32_bf16 v[76:79], a[224:227], a[96:99], v[76:79]\n" \
"v_mfma_f32_16x16x32_bf16 v[76:79], a[196:199], a[100:103], v[76:79]\n" \
"buffer_load_dwordx4 a[144:147], v208, %[srdz], s50 offen offset:0 sc1\n" \
"v_mfma_f32_16x16x32_bf16 v[108:111], a[224:227], a[112:115], v[108:111]\n" \
"ds_read_b64_tr_b16 a[216:217], v221 offset:33280\n" \
"v_mfma_f32_16x16x32_bf16 v[108:111], a[196:199], a[116:119], v[108:111]\n" \
"ds_read_b64_tr_b16 a[218:219], v221 offset:35328\n" \
"v_mfma_f32_16x16x32_bf16 v[12:15], a[200:203], a[72:75], v[12:15]\n" \
"ds_read_b64_tr_b16 a[220:221], v221 offset:41472\n" \
"s_cbranch_vccz Lg17_%=\n" \
"s_nop 0\n" \
"buffer_store_dwordx4 v[96:99], v210, %[srdz], s60 offen offset:0 sc1\n" \
"Lg17_%=:\n" \
"v_mfma_f32_16x16x32_bf16 v[12:15], a[204:207], a[76:79], v[12:15]\n" \
"ds_read_b64_tr_b16 a[222:223], v221 offset:43520\n" \
"v_mfma_f32_16x16x32_bf16 v[44:47], a[200:203], a[88:91], v[44:47]\n" \
"ds_read_b64_tr_b16 a[228:229], v221 offset:37376\n" \
"v_mfma_f32_16x16x32_bf16 v[44:47], a[204:207], a[92:95], v[44:47]\n" \
"ds_read_b64_tr_b16 a[230:231], v221 offset:39424\n" \
"v_mfma_f32_16x16x32_bf16 v[76:79], a[200:203], a[104:107], v[76:79]\n" \
"ds_read_b64_tr_b16 a[224:225], v221 offset:45568\n" \
"v_mfma_f32_16x16x32_bf16 v[76:79], a[204:207], a[108:111], v[76:79]\n" \
"ds_read_b64_tr_b16 a[226:227], v221 offset:47616\n" \
"v_mfma_f32_16x16x32_bf16 v[108:111], a[200:203], a[120:123], v[108:111]\n" \
"buffer_load_dwordx4 a[148:151], v208, %[srdz], s50 offen offset:1024 sc1\n" \
"v_mfma_f32_16x16x32_bf16 v[108:111], a[204:207], a[124:127], v[108:111]\n" \
"s_waitcnt vmcnt(63) lgkmcnt(12)\n" \
"s_nop 0\n" \
"v_mfma_f32_16x16x32_bf16 v[20:23], a[232:235], a[64:67], v[20:23]\n" \
"s_cbranch_vccz Lg18_%=\n" \
"s_nop 0\n" \
"buffer_store_dwordx4 v[104:107], v210, %[srdz], s60 offen offset:1024 sc1\n" \
"Lg18_%=:\n" \
"v_mfma_f32_16x16x32_bf16 v[20:23], a[236:239], a[68:71], v[20:23]\n" \
"v_mfma_f32_16x16x32_bf16 v[52:55], a[232:235], a[80:83], v[52:55]\n" \
"buffer_load_dwordx4 v213, %[srdt], s49 offen offset:2048 lds\n" \
"v_mfma_f32_16x16x32_bf16 v[52:55], a[236:239], a[84:87], v[52:55]\n" \
"v_mfma_f32_16x16x32_bf16 v[84:87], a[232:235], a[96:99], v[84:87]\n" \
"s_waitcnt vmcnt(59) lgkmcnt(0)\n" \
"s_bitcmp1_b32 0x30, s48\n" \
"s_cbranch_scc1 Lcw8_%=\n" \
"s_waitcnt vmcnt(45)\n" \
"Lcw8_%=:\n" \
"s_barrier\n" \
"s_nop 0\n" \
"v_mfma_f32_16x16x32_bf16 v[84:87], a[236:239], a[100:103], v[84:87]\n" \
"ds_read_b64_tr_b16 a[196:197], v220 offset:49152\n" \
"v_mfma_f32_16x16x32_bf16 v[116:119], a[232:235], a[112:115], v[116:119]\n" \
"ds_read_b64_tr_b16 a[198:199], v220 offset:51200\n" \
"buffer_load_dwordx4 a[152:155], v208, %[srdz], s50 offen offset:2048 sc1\n" \
"v_mfma_f32_16x16x32_bf16 v[116:119], a[236:239], a[116:119], v[116:119]\n" \
"ds_read_b64_tr_b16 a[200:201], v220 offset:53248\n" \
"v_mfma_f32_16x16x32_bf16 v[20:23], a[208:211], a[72:75], v[20:23]\n" \
"ds_read_b64_tr_b16 a[202:203], v220 offset:55296\n" \
"v_mfma_f32_16x16x32_bf16 v[20:23], a[212:215], a[76:79], v[20:23]\n" \
"ds_read_b64_tr_b16 a[204:205], v220 offset:57344\n" \
"s_cbranch_vccz Lg19_%=\n" \
"s_nop 0\n" \
"buffer_store_dwordx4 v[112:115], v210, %[srdz], s60 offen offset:2048 sc1\n" \
"Lg19_%=:\n" \
"v_mfma_f32_16x16x32_bf16 v[52:55], a[208:211], a[88:91], v[52:55]\n" \
"ds_read_b64_tr_b16 a[206:207], v220 offset:59392\n" \
"v_mfma_f32_16x16x32_bf16 v[52:55], a[212:215], a[92:95], v[52:55]\n" \
"ds_read_b64_tr_b16 a[232:233], v220 offset:61440\n" \
"v_mfma_f32_16x16x32_bf16 v[84:87], a[208:211], a[104:107], v[84:87]\n" \
"ds_read_b64_tr_b16 a[234:235], v220 offset:63488\n" \
"v_mfma_f32_16x16x32_bf16 v[84:87], a[212:215], a[108:111], v[84:87]\n" \
"buffer_load_dwordx4 a[156:159], v208, %[srdz], s50 offen offset:3072 sc1\n" \
"v_mfma_f32_16x16x32_bf16 v[116:119], a[208:211], a[120:123], v[116:119]\n" \
"v_mfma_f32_16x16x32_bf16 v[116:119], a[212:215], a[124:127], v[116:119]\n" \
"s_waitcnt vmcnt(21) lgkmcnt(4)\n" \
"s_bitcmp1_b32 0x30, s48\n" \
"s_cbranch_scc1 Lcw9_%=\n" \
"s_waitcnt vmcnt(13)\n" \
"Lcw9_%=:\n" \
"v_mfma_f32_16x16x32_bf16 v[28:31], a[216:219], a[64:67], v[28:31]\n" \
"v_mfma_f32_16x16x32_bf16 v[28:31], a[228:231], a[68:71], v[28:31]\n" \
"s_cbranch_vccz Lg20_%=\n" \
"s_nop 0\n" \
"buffer_store_dwordx4 v[120:123], v210, %[srdz], s60 offen offset:3072 sc1\n" \
"Lg20_%=:\n" \
"v_mfma_f32_16x16x32_bf16 v[60:63], a[216:219], a[80:83], v[60:63]\n" \
"v_mfma_f32_16x16x32_bf16 v[60:63], a[228:231], a[84:87], v[60:63]\n" \
"buffer_load_dwordx4 v214, %[srdt], s49 offen offset:3072 lds\n" \
"v_mfma_f32_16x16x32_bf16 v[92:95], a[216:219], a[96:99], v[92:95]\n" \
"v_mfma_f32_16x16x32_bf16 v[92:95], a[228:231], a[100:103], v[92:95]\n" \
"buffer_load_dwordx4 a[160:163], v209, %[srdz], s50 offen offset:0 sc1\n" \
"v_mfma_f32_16x16x32_bf16 v[124:127], a[216:219], a[112:115], v[124:127]\n" \
"ds_read_b64_tr_b16 a[236:237], v221 offset:49152\n" \
"v_mfma_f32_16x16x32_bf16 v[124:127], a[228:231], a[116:119], v[124:127]\n" \
"ds_read_b64_tr_b16 a[238:239], v221 offset:51200\n" \
"v_mfma_f32_16x16x32_bf16 v[192:195], a[196:199], a[0:3], v[128:131]\n" \
"ds_read_b64_tr_b16 a[208:209], v221 offset:53248\n" \
"v_mfma_f32_16x16x32_bf16 v[192:195], a[200:203], a[4:7], v[192:195]\n" \
"ds_read_b64_tr_b16 a[210:211], v221 offset:55296\n" \
"v_mfma_f32_16x16x32_bf16 v[196:199], a[196:199], a[16:19], v[132:135]\n" \
"buffer_load_dwordx4 a[164:167], v209, %[srdz], s50 offen offset:1024 sc1\n" \
"v_mfma_f32_16x16x32_bf16 v[196:199], a[200:203], a[20:23], v[196:199]\n" \
"v_mfma_f32_16x16x32_bf16 v[200:203], a[196:199], a[32:35], v[136:139]\n" \
"v_mfma_f32_16x16x32_bf16 v[200:203], a[200:203], a[36:39], v[200:203]\n" \
"v_mfma_f32_16x16x32_bf16 v[204:207], a[196:199], a[48:51], v[140:143]\n" \
"ds_read_b64_tr_b16 a[212:213], v221 offset:57344\n" \
"s_sub_u32 s61, s48, 6\n" \
"s_cmp_lt_u32 s61, 2\n" \
"s_cselect_b64 vcc, -1, 0\n" \
"s_nop 0\n" \
"v_mfma_f32_16x16x32_bf16 v[204:207], a[200:203], a[52:55], v[204:207]\n" \
"s_waitcnt vmcnt(22) lgkmcnt(5)\n" \
"s_bitcmp1_b32 0x30, s48\n" \
"s_cbranch_scc1 Lcw10_%=\n" \
"s_waitcnt vmcnt(14)\n" \
"Lcw10_%=:\n" \
"v_mfma_f32_16x16x32_bf16 v[192:195], a[204:207], a[8:11], v[192:195]\n" \
"ds_read_b64_tr_b16 a[214:215], v221 offset:59392\n" \
"s_cbranch_vccz Lg21_%=\n" \
"s_nop 0\n" \
"buffer_store_dwordx4 v[0:3], %[oq0], %[srdz], s60 offen offset:0 sc1\n" \
"Lg21_%=:\n" \
"v_mfma_f32_16x16x32_bf16 v[192:195], a[232:235], a[12:15], v[192:195]\n" \
".if . - adaln_body != 45064\n" \
".error \"size model\"\n" \
".endif\n" \
"Lk10_%=:\n" \
"s_cbranch_execz Lk11_%=\n" \
"s_nop 0\n" \
"ds_read_b64_tr_b16 a[216:217], v221 offset:61440\n" \
"v_mfma_f32_16x16x32_bf16 v[196:199], a[204:207], a[24:27], v[196:199]\n" \
"ds_read_b64_tr_b16 a[218:219], v221 offset:63488\n" \
"v_mfma_f32_16x16x32_bf16 v[196:199], a[232:235], a[28:31], v[196:199]\n" \
"buffer_load_dwordx4 a[168:171], v209, %[srdz], s50 offen offset:2048 sc1\n" \
"v_mfma_f32_16x16x32_bf16 v[200:203], a[204:207], a[40:43], v[200:203]\n" \
"v_mfma_f32_16x16x32_bf16 v[200:203], a[232:235], a[44:47], v[200:203]\n" \
"v_mfma_f32_16x16x32_bf16 v[204:207], a[204:207], a[56:59], v[204:207]\n" \
"ds_read_b64_tr_b16 a[228:229], v220 offset:49664\n" \
"v_mfma_f32_16x16x32_bf16 v[204:207], a[232:235], a[60:63], v[204:207]\n" \
"ds_read_b64_tr_b16 a[230:231], v220 offset:51712\n" \
"v_mfma_f32_16x16x32_bf16 v[28:31], a[220:223], a[72:75], v[28:31]\n" \
"s_cbranch_vccz Lg22_%=\n" \
"s_nop 0\n" \
"buffer_store_dwordx4 v[8:11], %[oq0], %[srdz], s60 offen offset:1024 sc1\n" \
"Lg22_%=:\n" \
"v_mfma_f32_16x16x32_bf16 v[28:31], a[224:227], a[76:79], v[28:31]\n" \
"v_mfma_f32_16x16x32_bf16 v[60:63], a[220:223], a[88:91], v[60:63]\n" \
"buffer_load_dwordx4 a[172:175], v209, %[srdz], s50 offen offset:3072 sc1\n" \
"v_mfma_f32_16x16x32_bf16 v[60:63], a[224:227], a[92:95], v[60:63]\n" \
"v_mfma_f32_16x16x32_bf16 v[92:95], a[220:223], a[104:107], v[92:95]\n" \
"v_mfma_f32_16x16x32_bf16 v[92:95], a[224:227], a[108:111], v[92:95]\n" \
"v_cmp_u_f32_e64 s[44:45], v192, v196\n" \
"v_mfma_f32_16x16x32_bf16 v[124:127], a[220:223], a[120:123], v[124:127]\n" \
"s_add_u32 m0, %[m0b], 16384\n" \
"s_cmp_gt_u32 s48, 2\n" \
"s_cselect_b32 s49, 2, 3\n" \
"s_lshl_b32 s49, s49, 18\n" \
"s_or_b32 s49, s49, s59\n" \
"buffer_load_dwordx4 %[wo0], %[srdt], s49 offen offset:0 lds\n" \
"v_mfma_f32_16x16x32_bf16 v[124:127], a[224:227], a[124:127], v[124:127]\n" \
"v_cmp_u_f32_e64 s[56:57], v200, v204\n" \
"s_or_b64 s[56:57], s[56:57], s[44:45]\n" \
"s_cbranch_scc1 Lsp2_%=\n" \
"Lbk2_%=:\n" \
"s_waitcnt vmcnt(63) lgkmcnt(2)\n" \
"s_nop 0\n" \
"v_mfma_f32_16x16x32_bf16 v[144:147], a[236:239], a[0:3], v[144:147]\n" \
"ds_read_b64_tr_b16 a[220:221], v220 offset:53760\n" \
"v_mfma_f32_16x16x32_bf16 v[144:147], a[208:211], a[4:7], v[144:147]\n" \
"ds_read_b64_tr_b16 a[222:223], v220 offset:55808\n" \
"s_cbranch_vccz Lg23_%=\n" \
"s_nop 0\n" \
"buffer_store_dwordx4 v[16:19], %[oq0], %[srdz], s60 offen offset:2048 sc1\n" \
"Lg23_%=:\n" \
"v_mfma_f32_16x16x32_bf16 v[148:151], a[236:239], a[16:19], v[148:151]\n" \
"ds_read_b64_tr_b16 a[196:197], v220 offset:57856\n" \
"v_mfma_f32_16x16x32_bf16 v[148:151], a[208:211], a[20:23], v[148:151]\n" \
"ds_read_b64_tr_b16 a[198:199], v220 offset:59904\n" \
"buffer_load_dwordx4 a[176:179], v210, %[srdz], s50 offen offset:0 sc1\n" \
"v_mfma_f32_16x16x32_bf16 v[152:155], a[236:239], a[32:35], v[152:155]\n" \
"ds_read_b64_tr_b16 a[200:201], v220 offset:61952\n" \
"v_mfma_f32_16x16x32_bf16 v[152:155], a[208:211], a[36:39], v[152:155]\n" \
"ds_read_b64_tr_b16 a[202:203], v220 offset:64000\n" \
"v_mfma_f32_16x16x32_bf16 v[156:159], a[236:239], a[48:51], v[156:159]\n" \
"ds_read_b64_tr_b16 a[204:205], v221 offset:49664\n" \
"v_mfma_f32_16x16x32_bf16 v[156:159], a[208:211], a[52:55], v[156:159]\n" \
"ds_read_b64_tr_b16 a[206:207], v221 offset:51712\n" \
"v_mfma_f32_16x16x32_bf16 v[144:147], a[212:215], a[8:11], v[144:147]\n" \
"ds_read_b64_tr_b16 a[224:225], v221 offset:53760\n" \
"v_mfma_f32_16x16x32_bf16 v[144:147], a[216:219], a[12:15], v[144:147]\n" \
"ds_read_b64_tr_b16 a[226:227], v221 offset:55808\n" \
"v_mfma_f32_16x16x32_bf16 v[148:151], a[212:215], a[24:27], v[148:151]\n" \
"s_cbranch_vccz Lg24_%=\n" \
"s_nop 0\n" \
"buffer_store_dwordx4 v[24:27], %[oq0], %[srdz], s60 offen offset:3072 sc1\n" \
"Lg24_%=:\n" \
"v_mfma_f32_16x16x32_bf16 v[148:151], a[216:219], a[28:31], v[148:151]\n" \
"v_mfma_f32_16x16x32_bf16 v[152:155], a[212:215], a[40:43], v[152:155]\n" \
"buffer_load_dwordx4 a[180:183], v210, %[srdz], s50 offen offset:1024 sc1\n" \
"v_mfma_f32_16x16x32_bf16 v[152:155], a[216:219], a[44:47], v[152:155]\n" \
"v_mfma_f32_16x16x32_bf16 v[156:159], a[212:215], a[56:59], v[156:159]\n" \
"ds_read_b64_tr_b16 a[232:233], v221 offset:57856\n" \
"v_mfma_f32_16x16x32_bf16 v[156:159], a[216:219], a[60:63], v[156:159]\n" \
"s_waitcnt vmcnt(63) lgkmcnt(5)\n" \
"s_nop 0\n" \
"v_mfma_f32_16x16x32_bf16 v[160:163], a[228:231], a[0:3], v[160:163]\n" \
"ds_read_b64_tr_b16 a[234:235], v221 offset:59904\n" \
"buffer_load_dwordx4 v212, %[srdt], s49 offen offset:1024 lds\n" \
"v_mfma_f32_16x16x32_bf16 v[160:163], a[220:223], a[4:7], v[160:163]\n" \
"ds_read_b64_tr_b16 a[236:237], v221 offset:61952\n" \
"v_mfma_f32_16x16x32_bf16 v[164:167], a[228:231], a[16:19], v[164:167]\n" \
"ds_read_b64_tr_b16 a[238:239], v221 offset:64000\n" \
"v_mfma_f32_16x16x32_bf16 v[164:167], a[220:223], a[20:23], v[164:167]\n" \
"s_cbranch_vccz Lg25_%=\n" \
"s_nop 0\n" \
"buffer_store_dwordx4 v[32:35], v208, %[srdz], s60 offen offset:0 sc1\n" \
"Lg25_%=:\n" \
"v_mfma_f32_16x16x32_bf16 v[168:171], a[228:231], a[32:35], v[168:171]\n" \
"v_mfma_f32_16x16x32_bf16 v[168:171], a[220:223], a[36:39], v[168:171]\n" \
"buffer_load_dwordx4 a[184:187], v210, %[srdz], s50 offen offset:2048 sc1\n" \
"v_mfma_f32_16x16x32_bf16 v[172:175], a[228:231], a[48:51], v[172:175]\n" \
"v_mfma_f32_16x16x32_bf16 v[172:175], a[220:223], a[52:55], v[172:175]\n" \
"buffer_load_dwordx4 a[188:191], v210, %[srdz], s50 offen offset:3072 sc1\n" \
"v_mfma_f32_16x16x32_bf16 v[160:163], a[196:199], a[8:11], v[160:163]\n" \
"v_mfma_f32_16x16x32_bf16 v[160:163], a[200:203], a[12:15], v[160:163]\n" \
"v_mfma_f32_16x16x32_bf16 v[164:167], a[196:199], a[24:27], v[164:167]\n" \
"v_mfma_f32_16x16x32_bf16 v[164:167], a[200:203], a[28:31], v[164:167]\n" \
"v_mfma_f32_16x16x32_bf16 v[168:171], a[196:199], a[40:43], v[168:171]\n" \
"s_waitcnt vmcnt(63) lgkmcnt(0)\n" \
"s_bitcmp1_b32 0x30, s48\n" \
"s_cbranch_scc1 Lcw11_%=\n" \
"s_waitcnt vmcnt(59)\n" \
"s_bitcmp1_b32 0xc0, s48\n" \
"s_cbranch_scc1 Lcw11_%=\n" \
"s_waitcnt vmcnt(54)\n" \
"Lcw11_%=:\n" \
"s_barrier\n" \
"s_nop 0\n" \
"v_mfma_f32_16x16x32_bf16 v[168:171], a[200:203], a[44:47], v[168:171]\n" \
"ds_read_b64_tr_b16 a[208:209], v215 offset:49152\n" \
"s_cbranch_vccz Lg26_%=\n" \
"s_nop 0\n" \
"buffer_store_dwordx4 v[40:43], v208, %[srdz], s60 offen offset:1024 sc1\n" \
"Lg26_%=:\n" \
"v_mfma_f32_16x16x32_bf16 v[172:175], a[196:199], a[56:59], v[172:175]\n" \
"ds_read_b64_tr_b16 a[210:211], v215 offset:51200\n" \
"v_mfma_f32_16x16x32_bf16 v[172:175], a[200:203], a[60:63], v[172:175]\n" \
"s_waitcnt vmcnt(63) lgkmcnt(2)\n" \
"s_nop 0\n" \
"v_mfma_f32_16x16x32_bf16 v[176:179], a[204:207], a[0:3], v[176:179]\n" \
"ds_read_b64_tr_b16 a[212:213], v215 offset:53248\n" \
"global_load_dword v237, %[vct], %[pct]\n" \
"v_mfma_f32_16x16x32_bf16 v[176:179], a[224:227], a[4:7], v[176:179]\n" \
"ds_read_b64_tr_b16 a[214:215], v215 offset:55296\n" \
"v_mfma_f32_16x16x32_bf16 v[180:183], a[204:207], a[16:19], v[180:183]\n" \
"ds_read_b64_tr_b16 a[216:217], v215 offset:57344\n" \
"buffer_load_dwordx4 v213, %[srdt], s49 offen offset:2048 lds\n" \
"v_mfma_f32_16x16x32_bf16 v[180:183], a[224:227], a[20:23], v[180:183]\n" \
"ds_read_b64_tr_b16 a[218:219], v215 offset:59392\n" \
"v_mfma_f32_16x16x32_bf16 v[184:187], a[204:207], a[32:35], v[184:187]\n" \
"ds_read_b64_tr_b16 a[228:229], v215 offset:61440\n" \
"v_mfma_f32_16x16x32_bf16 v[184:187], a[224:227], a[36:39], v[184:187]\n" \
"ds_read_b64_tr_b16 a[230:231], v215 offset:63488\n" \
"s_cbranch_vccz Lg27_%=\n" \
"s_nop 0\n" \
"buffer_store_dwordx4 v[48:51], v208, %[srdz], s60 offen offset:2048 sc1\n" \
"Lg27_%=:\n" \
"v_mfma_f32_16x16x32_bf16 v[188:191], a[204:207], a[48:51], v[188:191]\n" \
"ds_read_b64_tr_b16 a[220:221], v227 offset:49152\n" \
"v_mfma_f32_16x16x32_bf16 v[188:191], a[224:227], a[52:55], v[188:191]\n" \
"ds_read_b64_tr_b16 a[222:223], v227 offset:51200\n" \
"v_mfma_f32_16x16x32_bf16 v[176:179], a[232:235], a[8:11], v[176:179]\n" \
"ds_read_b64_tr_b16 a[196:197], v227 offset:53248\n" \
"v_mfma_f32_16x16x32_bf16 v[176:179], a[236:239], a[12:15], v[176:179]\n" \
"ds_read_b64_tr_b16 a[198:199], v227 offset:55296\n" \
"s_cmp_gt_u32 s48, 3\n" \
"s_cselect_b32 s50, 3, 4\n" \
"s_mul_i32 s50, s50, 152064\n" \
"buffer_load_dwordx4 a[64:67], %[oq0], %[srdz], s50 offen offset:0 sc1\n" \
"v_mfma_f32_16x16x32_bf16 v[180:183], a[232:235], a[24:27], v[180:183]\n" \
"v_mfma_f32_16x16x32_bf16 v[180:183], a[236:239], a[28:31], v[180:183]\n" \
"buffer_load_dwordx4 v214, %[srdt], s49 offen offset:3072 lds\n" \
"v_mfma_f32_16x16x32_bf16 v[184:187], a[232:235], a[40:43], v[184:187]\n" \
"v_mfma_f32_16x16x32_bf16 v[184:187], a[236:239], a[44:47], v[184:187]\n" \
"v_mfma_f32_16x16x32_bf16 v[188:191], a[232:235], a[56:59], v[188:191]\n" \
"ds_read_b64_tr_b16 a[200:201], v227 offset:57344\n" \
"s_cbranch_vccz Lg28_%=\n" \
"s_nop 0\n" \
"buffer_store_dwordx4 v[56:59], v208, %[srdz], s60 offen offset:3072 sc1\n" \
"Lg28_%=:\n" \
"v_mfma_f32_16x16x32_bf16 v[188:191], a[236:239], a[60:63], v[188:191]\n" \
"s_waitcnt vmcnt(63) lgkmcnt(5)\n" \
"s_nop 0\n" \
"v_mfma_f32_16x16x32_bf16 v[4:7], a[208:211], a[0:3], v[4:7]\n" \
"ds_read_b64_tr_b16 a[202:203], v227 offset:59392\n" \
"buffer_load_dwordx4 a[68:71], %[oq0], %[srdz], s50 offen offset:1024 sc1\n" \
"v_mfma_f32_16x16x32_bf16 v[4:7], a[212:215], a[4:7], v[4:7]\n" \
"ds_read_b64_tr_b16 a[204:205], v227 offset:61440\n" \
"v_mfma_f32_16x16x32_bf16 v[36:39], a[208:211], a[16:19], v[36:39]\n" \
"ds_read_b64_tr_b16 a[206:207], v227 offset:63488\n" \
"v_mfma_f32_16x16x32_bf16 v[36:39], a[212:215], a[20:23], v[36:39]\n" \
"v_mfma_f32_16x16x32_bf16 v[68:71], a[208:211], a[32:35], v[68:71]\n" \
"buffer_load_dwordx4 a[72:75], %[oq0], %[srdz], s50 offen offset:2048 sc1\n" \
"v_mfma_f32_16x16x32_bf16 v[68:71], a[212:215], a[36:39], v[68:71]\n" \
"v_mfma_f32_16x16x32_bf16 v[100:103], a[208:211], a[48:51], v[100:103]\n" \
"ds_read_b64_tr_b16 a[224:225], v215 offset:49664\n" \
"v_mfma_f32_16x16x32_bf16 v[100:103], a[212:215], a[52:55], v[100:103]\n" \
"ds_read_b64_tr_b16 a[226:227], v215 offset:51712\n" \
"s_cbranch_vccz Lg29_%=\n" \
"s_nop 0\n" \
"buffer_store_dwordx4 v[64:67], v209, %[srdz], s60 offen offset:0 sc1\n" \
"Lg29_%=:\n" \
"v_mfma_f32_16x16x32_bf16 v[4:7], a[216:219], a[8:11], v[4:7]\n" \
"ds_read_b64_tr_b16 a[232:233], v215 offset:53760\n" \
"v_mfma_f32_16x16x32_bf16 v[4:7], a[228:231], a[12:15], v[4:7]\n" \
"ds_read_b64_tr_b16 a[234:235], v215 offset:55808\n" \
"v_mfma_f32_16x16x32_bf16 v[36:39], a[216:219], a[24:27], v[36:39]\n" \
"v_mfma_f32_16x16x32_bf16 v[36:39], a[228:231], a[28:31], v[36:39]\n" \
"buffer_load_dwordx4 a[76:79], %[oq0], %[srdz], s50 offen offset:3072 sc1\n" \
"v_mfma_f32_16x16x32_bf16 v[68:71], a[216:219], a[40:43], v[68:71]\n" \
"v_mfma_f32_16x16x32_bf16 v[68:71], a[228:231], a[44:47], v[68:71]\n" \
"v_mfma_f32_16x16x32_bf16 v[100:103], a[216:219], a[56:59], v[100:103]\n" \
"ds_read_b64_tr_b16 a[236:237], v215 offset:57856\n" \
"v_mfma_f32_16x16x32_bf16 v[100:103], a[228:231], a[60:63], v[100:103]\n" \
"s_waitcnt vmcnt(63) lgkmcnt(5)\n" \
"s_nop 0\n" \
"v_mfma_f32_16x16x32_bf16 v[12:15], a[220:223], a[0:3], v[12:15]\n" \
"ds_read_b64_tr_b16 a[238:239], v215 offset:59904\n" \
"s_cbranch_vccz Lg30_%=\n" \
"s_nop 0\n" \
"buffer_store_dwordx4 v[72:75], v209, %[srdz], s60 offen offset:1024 sc1\n" \
"Lg30_%=:\n" \
"v_mfma_f32_16x16x32_bf16 v[12:15], a[196:199], a[4:7], v[12:15]\n" \
"ds_read_b64_tr_b16 a[208:209], v215 offset:61952\n" \
"v_mfma_f32_16x16x32_bf16 v[44:47], a[220:223], a[16:19], v[44:47]\n" \
"ds_read_b64_tr_b16 a[210:211], v215 offset:64000\n" \
"v_mfma_f32_16x16x32_bf16 v[44:47], a[196:199], a[20:23], v[44:47]\n" \
"buffer_load_dwordx4 a[80:83], v208, %[srdz], s50 offen offset:0 sc1\n" \
"v_mfma_f32_16x16x32_bf16 v[76:79], a[220:223], a[32:35], v[76:79]\n" \
"v_mfma_f32_16x16x32_bf16 v[76:79], a[196:199], a[36:39], v[76:79]\n" \
"s_add_u32 m0, %[m0b], 32768\n" \
"s_cmp_gt_u32 s48, 3\n" \
"s_cselect_b32 s49, 3, 4\n" \
"s_lshl_b32 s49, s49, 18\n" \
"s_or_b32 s49, s49, s58\n" \
"buffer_load_dwordx4 %[wo0], %[srdt], s49 offen offset:0 lds\n" \
"v_mfma_f32_16x16x32_bf16 v[108:111], a[220:223], a[48:51], v[108:111]\n" \
"ds_read_b64_tr_b16 a[212:213], v227 offset:49664\n" \
"v_mfma_f32_16x16x32_bf16 v[108:111], a[196:199], a[52:55], v[108:111]\n" \
"ds_read_b64_tr_b16 a[214:215], v227 offset:51712\n" \
"v_mfma_f32_16x16x32_bf16 v[12:15], a[200:203], a[8:11], v[12:15]\n" \
"ds_read_b64_tr_b16 a[216:217], v227 offset:57856\n" \
"v_mfma_f32_16x16x32_bf16 v[12:15], a[204:207], a[12:15], v[12:15]\n" \
"ds_read_b64_tr_b16 a[218:219], v227 offset:59904\n" \
"s_cbranch_vccz Lg31_%=\n" \
"s_nop 0\n" \
"buffer_store_dwordx4 v[80:83], v209, %[srdz], s60 offen offset:2048 sc1\n" \
"Lg31_%=:\n" \
"v_mfma_f32_16x16x32_bf16 v[44:47], a[200:203], a[24:27], v[44:47]\n" \
"ds_read_b64_tr_b16 a[228:229], v227 offset:53760\n" \
"v_mfma_f32_16x16x32_bf16 v[44:47], a[204:207], a[28:31], v[44:47]\n" \
"ds_read_b64_tr_b16 a[230:231], v227 offset:55808\n" \
"v_mfma_f32_16x16x32_bf16 v[76:79], a[200:203], a[40:43], v[76:79]\n" \
"ds_read_b64_tr_b16 a[220:221], v227 offset:61952\n" \
"buffer_load_dwordx4 a[84:87], v208, %[srdz], s50 offen offset:1024 sc1\n" \
"v_mfma_f32_16x16x32_bf16 v[76:79], a[204:207], a[44:47], v[76:79]\n" \
"ds_read_b64_tr_b16 a[222:223], v227 offset:64000\n" \
"v_mfma_f32_16x16x32_bf16 v[108:111], a[200:203], a[56:59], v[108:111]\n" \
"v_mfma_f32_16x16x32_bf16 v[108:111], a[204:207], a[60:63], v[108:111]\n" \
"s_waitcnt vmcnt(63) lgkmcnt(12)\n" \
"s_nop 0\n" \
"v_mfma_f32_16x16x32_bf16 v[20:23], a[224:227], a[0:3], v[20:23]\n" \
"v_mfma_f32_16x16x32_bf16 v[20:23], a[232:235], a[4:7], v[20:23]\n" \
"v_mfma_f32_16x16x32_bf16 v[52:55], a[224:227], a[16:19], v[52:55]\n" \
"s_cbranch_vccz Lg32_%=\n" \
"s_nop 0\n" \
"buffer_store_dwordx4 v[88:91], v209, %[srdz], s60 offen offset:3072 sc1\n" \
"Lg32_%=:\n" \
"v_mfma_f32_16x16x32_bf16 v[52:55], a[232:235], a[20:23], v[52:55]\n" \
"v_mfma_f32_16x16x32_bf16 v[84:87], a[224:227], a[32:35], v[84:87]\n" \
"s_waitcnt vmcnt(32) lgkmcnt(0)\n" \
"s_nop 0\n" \
"s_bitcmp1_b32 0xc0, s48\n" \
"s_cbranch_scc1 Lcw12_%=\n" \
"s_waitcnt vmcnt(20)\n" \
"Lcw12_%=:\n" \
"s_barrier\n" \
"s_nop 0\n" \
"v_mfma_f32_16x16x32_bf16 v[84:87], a[232:235], a[36:39], v[84:87]\n" \
"ds_read_b64_tr_b16 a[196:197], v215 offset:0\n" \
"buffer_load_dwordx4 a[88:91], v208, %[srdz], s50 offen offset:2048 sc1\n" \
"v_mfma_f32_16x16x32_bf16 v[116:119], a[224:227], a[48:51], v[116:119]\n" \
"ds_read_b64_tr_b16 a[198:199], v215 offset:2048\n" \
"v_mfma_f32_16x16x32_bf16 v[116:119], a[232:235], a[52:55], v[116:119]\n" \
"ds_read_b64_tr_b16 a[200:201], v215 offset:4096\n" \
"buffer_load_dwordx4 v212, %[srdt], s49 offen offset:1024 lds\n" \
"v_mfma_f32_16x16x32_bf16 v[20:23], a[236:239], a[8:11], v[20:23]\n" \
"ds_read_b64_tr_b16 a[202:203], v215 offset:6144\n" \
"v_mfma_f32_16x16x32_bf16 v[20:23], a[208:211], a[12:15], v[20:23]\n" \
"ds_read_b64_tr_b16 a[204:205], v215 offset:8192\n" \
"v_mfma_f32_16x16x32_bf16 v[52:55], a[236:239], a[24:27], v[52:55]\n" \
"ds_read_b64_tr_b16 a[206:207], v215 offset:10240\n" \
"v_mfma_f32_16x16x32_bf16 v[52:55], a[208:211], a[28:31], v[52:55]\n" \
"ds_read_b64_tr_b16 a[224:225], v215 offset:12288\n" \
"s_cbranch_vccz Lg33_%=\n" \
"s_nop 0\n" \
"buffer_store_dwordx4 v[96:99], v210, %[srdz], s60 offen offset:0 sc1\n" \
"Lg33_%=:\n" \
"v_mfma_f32_16x16x32_bf16 v[84:87], a[236:239], a[40:43], v[84:87]\n" \
"ds_read_b64_tr_b16 a[226:227], v215 offset:14336\n" \
"v_mfma_f32_16x16x32_bf16 v[84:87], a[208:211], a[44:47], v[84:87]\n" \
"buffer_load_dwordx4 a[92:95], v208, %[srdz], s50 offen offset:3072 sc1\n" \
"v_mfma_f32_16x16x32_bf16 v[116:119], a[236:239], a[56:59], v[116:119]\n" \
"v_mfma_f32_16x16x32_bf16 v[116:119], a[208:211], a[60:63], v[116:119]\n" \
"s_waitcnt vmcnt(25) lgkmcnt(4)\n" \
"s_nop 0\n" \
"s_bitcmp1_b32 0xc0, s48\n" \
"s_cbranch_scc1 Lcw13_%=\n" \
"s_waitcnt vmcnt(16)\n" \
"Lcw13_%=:\n" \
"v_mfma_f32_16x16x32_bf16 v[28:31], a[212:215], a[0:3], v[28:31]\n" \
"buffer_load_dwordx4 a[96:99], v209, %[srdz], s50 offen offset:0 sc1\n" \
"v_mfma_f32_16x16x32_bf16 v[28:31], a[228:231], a[4:7], v[28:31]\n" \
"v_mfma_f32_16x16x32_bf16 v[60:63], a[212:215], a[16:19], v[60:63]\n" \
"buffer_load_dwordx4 v213, %[srdt], s49 offen offset:2048 lds\n" \
"v_mfma_f32_16x16x32_bf16 v[60:63], a[228:231], a[20:23], v[60:63]\n" \
"v_mfma_f32_16x16x32_bf16 v[92:95], a[212:215], a[32:35], v[92:95]\n" \
"s_cbranch_vccz Lg34_%=\n" \
"s_nop 0\n" \
"buffer_store_dwordx4 v[104:107], v210, %[srdz], s60 offen offset:1024 sc1\n" \
"Lg34_%=:\n" \
"v_mfma_f32_16x16x32_bf16 v[92:95], a[228:231], a[36:39], v[92:95]\n" \
"v_mfma_f32_16x16x32_bf16 v[124:127], a[212:215], a[48:51], v[124:127]\n" \
"ds_read_b64_tr_b16 a[232:233], v227 offset:0\n" \
"v_mfma_f32_16x16x32_bf16 v[124:127], a[228:231], a[52:55], v[124:127]\n" \
"ds_read_b64_tr_b16 a[234:235], v227 offset:2048\n" \
"v_mfma_f32_16x16x32_bf16 v[128:131], a[196:199], a[128:131], v[192:195]\n" \
"ds_read_b64_tr_b16 a[236:237], v227 offset:4096\n" \
"v_mfma_f32_16x16x32_bf16 v[128:131], a[200:203], a[132:135], v[128:131]\n" \
"ds_read_b64_tr_b16 a[238:239], v227 offset:6144\n" \
"buffer_load_dwordx4 a[100:103], v209, %[srdz], s50 offen offset:1024 sc1\n" \
"v_mfma_f32_16x16x32_bf16 v[132:135], a[196:199], a[144:147], v[196:199]\n" \
"v_mfma_f32_16x16x32_bf16 v[132:135], a[200:203], a[148:151], v[132:135]\n" \
"v_mfma_f32_16x16x32_bf16 v[136:139], a[196:199], a[160:163], v[200:203]\n" \
"v_mfma_f32_16x16x32_bf16 v[136:139], a[200:203], a[164:167], v[136:139]\n" \
"s_cbranch_vccz Lg35_%=\n" \
"s_nop 0\n" \
"buffer_store_dwordx4 v[112:115], v210, %[srdz], s60 offen offset:2048 sc1\n" \
"Lg35_%=:\n" \
"v_mfma_f32_16x16x32_bf16 v[140:143], a[196:199], a[176:179], v[204:207]\n" \
"ds_read_b64_tr_b16 a[208:209], v227 offset:8192\n" \
"v_mfma_f32_16x16x32_bf16 v[140:143], a[200:203], a[180:183], v[140:143]\n" \
"s_waitcnt vmcnt(26) lgkmcnt(5)\n" \
"s_nop 0\n" \
"s_bitcmp1_b32 0xc0, s48\n" \
"s_cbranch_scc1 Lcw14_%=\n" \
"s_waitcnt vmcnt(16)\n" \
"Lcw14_%=:\n" \
"v_mfma_f32_16x16x32_bf16 v[128:131], a[204:207], a[136:139], v[128:131]\n" \
"ds_read_b64_tr_b16 a[210:211], v227 offset:10240\n" \
"buffer_load_dwordx4 a[104:107], v209, %[srdz], s50 offen offset:2048 sc1\n" \
"v_mfma_f32_16x16x32_bf16 v[128:131], a[224:227], a[140:143], v[128:131]\n" \
"ds_read_b64_tr_b16 a[212:213], v227 offset:12288\n" \
"v_mfma_f32_16x16x32_bf16 v[132:135], a[204:207], a[152:155], v[132:135]\n" \
"ds_read_b64_tr_b16 a[214:215], v227 offset:14336\n" \
"v_mfma_f32_16x16x32_bf16 v[132:135], a[224:227], a[156:159], v[132:135]\n" \
"buffer_load_dwordx4 v214, %[srdt], s49 offen offset:3072 lds\n" \
"v_mfma_f32_16x16x32_bf16 v[136:139], a[204:207], a[168:171], v[136:139]\n" \
"v_mfma_f32_16x16x32_bf16 v[136:139], a[224:227], a[172:175], v[136:139]\n" \
"v_mfma_f32_16x16x32_bf16 v[140:143], a[204:207], a[184:187], v[140:143]\n" \
"ds_read_b64_tr_b16 a[228:229], v215 offset:512\n" \
"s_cbranch_vccz Lg36_%=\n" \
"s_nop 0\n" \
"buffer_store_dwordx4 v[120:123], v210, %[srdz], s60 offen offset:3072 sc1\n" \
"Lg36_%=:\n" \
"v_mfma_f32_16x16x32_bf16 v[140:143], a[224:227], a[188:191], v[140:143]\n" \
"ds_read_b64_tr_b16 a[230:231], v215 offset:2560\n" \
"v_mfma_f32_16x16x32_bf16 v[28:31], a[216:219], a[8:11], v[28:31]\n" \
"buffer_load_dwordx4 a[108:111], v209, %[srdz], s50 offen offset:3072 sc1\n" \
"v_mfma_f32_16x16x32_bf16 v[28:31], a[220:223], a[12:15], v[28:31]\n" \
"v_mfma_f32_16x16x32_bf16 v[60:63], a[216:219], a[24:27], v[60:63]\n" \
"v_mfma_f32_16x16x32_bf16 v[60:63], a[220:223], a[28:31], v[60:63]\n" \
"v_mfma_f32_16x16x32_bf16 v[92:95], a[216:219], a[40:43], v[92:95]\n" \
"buffer_load_dwordx4 a[112:115], v210, %[srdz], s50 offen offset:0 sc1\n" \
"v_mfma_f32_16x16x32_bf16 v[92:95], a[220:223], a[44:47], v[92:95]\n" \
"v_cmp_u_f32_e64 s[44:45], v128, v132\n" \
"v_mfma_f32_16x16x32_bf16 v[124:127], a[216:219], a[56:59], v[124:127]\n" \
"v_mfma_f32_16x16x32_bf16 v[124:127], a[220:223], a[60:63], v[124:127]\n" \
"v_cmp_u_f32_e64 s[56:57], v136, v140\n" \
"s_or_b64 s[56:57], s[56:57], s[44:45]\n" \
"s_cbranch_scc1 Lsp3_%=\n" \
"Lbk3_%=:\n" \
"s_waitcnt vmcnt(63) lgkmcnt(2)\n" \
"s_nop 0\n" \
"v_mfma_f32_16x16x32_bf16 v[144:147], a[232:235], a[128:131], v[144:147]\n" \
"ds_read_b64_tr_b16 a[216:217], v215 offset:4608\n" \
"v_mfma_f32_16x16x32_bf16 v[144:147], a[236:239], a[132:135], v[144:147]\n" \
"ds_read_b64_tr_b16 a[218:219], v215 offset:6656\n" \
"v_mfma_f32_16x16x32_bf16 v[148:151], a[232:235], a[144:147], v[148:151]\n" \
"ds_read_b64_tr_b16 a[196:197], v215 offset:8704\n" \
"v_mfma_f32_16x16x32_bf16 v[148:151], a[236:239], a[148:151], v[148:151]\n" \
"ds_read_b64_tr_b16 a[198:199], v215 offset:10752\n" \
"v_mfma_f32_16x16x32_bf16 v[152:155], a[232:235], a[160:163], v[152:155]\n" \
"ds_read_b64_tr_b16 a[200:201], v215 offset:12800\n" \
"v_mfma_f32_16x16x32_bf16 v[152:155], a[236:239], a[164:167], v[152:155]\n" \
"ds_read_b64_tr_b16 a[202:203], v215 offset:14848\n" \
"v_mfma_f32_16x16x32_bf16 v[156:159], a[232:235], a[176:179], v[156:159]\n" \
"ds_read_b64_tr_b16 a[204:205], v227 offset:512\n" \
"buffer_load_dwordx4 a[116:119], v210, %[srdz], s50 offen offset:1024 sc1\n" \
"v_mfma_f32_16x16x32_bf16 v[156:159], a[236:239], a[180:183], v[156:159]\n" \
"ds_read_b64_tr_b16 a[206:207], v227 offset:2560\n" \
"v_mfma_f32_16x16x32_bf16 v[144:147], a[208:211], a[136:139], v[144:147]\n" \
"ds_read_b64_tr_b16 a[220:221], v227 offset:4608\n" \
"v_mfma_f32_16x16x32_bf16 v[144:147], a[212:215], a[140:143], v[144:147]\n" \
"ds_read_b64_tr_b16 a[222:223], v227 offset:6656\n" \
"v_mfma_f32_16x16x32_bf16 v[148:151], a[208:211], a[152:155], v[148:151]\n" \
"s_add_u32 m0, %[m0b], 98304\n" \
"s_cmp_gt_u32 s48, 4\n" \
"s_cselect_b32 s49, 4, 5\n" \
"s_lshl_b32 s49, s49, 18\n" \
"s_or_b32 s49, s49, s58\n" \
"buffer_load_dwordx4 %[wo0], %[srdt], s49 offen offset:0 lds\n" \
"v_mfma_f32_16x16x32_bf16 v[148:151], a[212:215], a[156:159], v[148:151]\n" \
"v_mfma_f32_16x16x32_bf16 v[152:155], a[208:211], a[168:171], v[152:155]\n" \
"buffer_load_dwordx4 a[120:123], v210, %[srdz], s50 offen offset:2048 sc1\n" \
"v_mfma_f32_16x16x32_bf16 v[152:155], a[212:215], a[172:175], v[152:155]\n" \
"v_mfma_f32_16x16x32_bf16 v[156:159], a[208:211], a[184:187], v[156:159]\n" \
"ds_read_b64_tr_b16 a[224:225], v227 offset:8704\n" \
"v_mfma_f32_16x16x32_bf16 v[156:159], a[212:215], a[188:191], v[156:159]\n" \
"s_waitcnt vmcnt(63) lgkmcnt(5)\n" \
"s_nop 0\n" \
"v_mfma_f32_16x16x32_bf16 v[160:163], a[228:231], a[128:131], v[160:163]\n" \
"ds_read_b64_tr_b16 a[226:227], v227 offset:10752\n" \
"v_mfma_f32_16x16x32_bf16 v[160:163], a[216:219], a[132:135], v[160:163]\n" \
"ds_read_b64_tr_b16 a[232:233], v227 offset:12800\n" \
"v_mfma_f32_16x16x32_bf16 v[164:167], a[228:231], a[144:147], v[164:167]\n" \
"ds_read_b64_tr_b16 a[234:235], v227 offset:14848\n" \
"v_mfma_f32_16x16x32_bf16 v[164:167], a[216:219], a[148:151], v[164:167]\n" \
"buffer_load_dwordx4 a[124:127], v210, %[srdz], s50 offen offset:3072 sc1\n" \
"v_mfma_f32_16x16x32_bf16 v[168:171], a[228:231], a[160:163], v[168:171]\n" \
"v_mfma_f32_16x16x32_bf16 v[168:171], a[216:219], a[164:167], v[168:171]\n" \
"v_mfma_f32_16x16x32_bf16 v[172:175], a[228:231], a[176:179], v[172:175]\n" \
"v_mfma_f32_16x16x32_bf16 v[172:175], a[216:219], a[180:183], v[172:175]\n" \
"v_mfma_f32_16x16x32_bf16 v[160:163], a[196:199], a[136:139], v[160:163]\n" \
"v_mfma_f32_16x16x32_bf16 v[160:163], a[200:203], a[140:143], v[160:163]\n" \
"buffer_load_dwordx4 v212, %[srdt], s49 offen offset:1024 lds\n" \
"v_mfma_f32_16x16x32_bf16 v[164:167], a[196:199], a[152:155], v[164:167]\n" \
"v_mfma_f32_16x16x32_bf16 v[164:167], a[200:203], a[156:159], v[164:167]\n" \
"v_mfma_f32_16x16x32_bf16 v[168:171], a[196:199], a[168:171], v[168:171]\n" \
"s_waitcnt vmcnt(30) lgkmcnt(0)\n" \
"s_nop 0\n" \
"s_bitcmp1_b32 0xc0, s48\n" \
"s_cbranch_scc1 Lcw15_%=\n" \
"s_waitcnt vmcnt(21)\n" \
"Lcw15_%=:\n" \
"s_barrier\n" \
"s_nop 0\n" \
"v_mfma_f32_16x16x32_bf16 v[168:171], a[200:203], a[172:175], v[168:171]\n" \
"ds_read_b64_tr_b16 a[236:237], v215 offset:16384\n" \
"v_mfma_f32_16x16x32_bf16 v[172:175], a[196:199], a[184:187], v[172:175]\n" \
"ds_read_b64_tr_b16 a[238:239], v215 offset:18432\n" \
"v_mfma_f32_16x16x32_bf16 v[172:175], a[200:203], a[188:191], v[172:175]\n" \
"s_waitcnt vmcnt(63) lgkmcnt(2)\n" \
"s_nop 0\n" \
"v_mfma_f32_16x16x32_bf16 v[176:179], a[204:207], a[128:131], v[176:179]\n" \
"ds_read_b64_tr_b16 a[208:209], v215 offset:20480\n" \
"s_cmp_gt_u32 s48, 4\n" \
"s_cselect_b32 s50, 4, 5\n" \
"s_mul_i32 s50, s50, 152064\n" \
"buffer_load_dwordx4 v[0:3], %[oq0], %[srdz], s50 offen offset:0 sc1\n" \
"v_mfma_f32_16x16x32_bf16 v[176:179], a[220:223], a[132:135], v[176:179]\n" \
"ds_read_b64_tr_b16 a[210:211], v215 offset:22528\n" \
"v_mfma_f32_16x16x32_bf16 v[180:183], a[204:207], a[144:147], v[180:183]\n" \
"ds_read_b64_tr_b16 a[212:213], v215 offset:24576\n" \
"v_mfma_f32_16x16x32_bf16 v[180:183], a[220:223], a[148:151], v[180:183]\n" \
"ds_read_b64_tr_b16 a[214:215], v215 offset:26624\n" \
"v_mfma_f32_16x16x32_bf16 v[184:187], a[204:207], a[160:163], v[184:187]\n" \
"ds_read_b64_tr_b16 a[228:229], v215 offset:28672\n" \
"buffer_load_dwordx4 v[8:11], %[oq0], %[srdz], s50 offen offset:1024 sc1\n" \
"v_mfma_f32_16x16x32_bf16 v[184:187], a[220:223], a[164:167], v[184:187]\n" \
"ds_read_b64_tr_b16 a[230:231], v215 offset:30720\n" \
"v_mfma_f32_16x16x32_bf16 v[188:191], a[204:207], a[176:179], v[188:191]\n" \
"ds_read_b64_tr_b16 a[216:217], v227 offset:16384\n" \
"v_mfma_f32_16x16x32_bf16 v[188:191], a[220:223], a[180:183], v[188:191]\n" \
"ds_read_b64_tr_b16 a[218:219], v227 offset:18432\n" \
"v_mfma_f32_16x16x32_bf16 v[176:179], a[224:227], a[136:139], v[176:179]\n" \
"ds_read_b64_tr_b16 a[196:197], v227 offset:20480\n" \
"buffer_load_dwordx4 v[16:19], %[oq0], %[srdz], s50 offen offset:2048 sc1\n" \
"v_mfma_f32_16x16x32_bf16 v[176:179], a[232:235], a[140:143], v[176:179]\n" \
"ds_read_b64_tr_b16 a[198:199], v227 offset:22528\n" \
"v_mfma_f32_16x16x32_bf16 v[180:183], a[224:227], a[152:155], v[180:183]\n" \
"buffer_load_dwordx4 v213, %[srdt], s49 offen offset:2048 lds\n" \
"v_mfma_f32_16x16x32_bf16 v[180:183], a[232:235], a[156:159], v[180:183]\n" \
"v_mfma_f32_16x16x32_bf16 v[184:187], a[224:227], a[168:171], v[184:187]\n" \
"buffer_load_dwordx4 v[24:27], %[oq0], %[srdz], s50 offen offset:3072 sc1\n" \
"v_mfma_f32_16x16x32_bf16 v[184:187], a[232:235], a[172:175], v[184:187]\n" \
"v_mfma_f32_16x16x32_bf16 v[188:191], a[224:227], a[184:187], v[188:191]\n" \
"ds_read_b64_tr_b16 a[200:201], v227 offset:24576\n" \
"v_mfma_f32_16x16x32_bf16 v[188:191], a[232:235], a[188:191], v[188:191]\n" \
"s_waitcnt vmcnt(63) lgkmcnt(5)\n" \
"s_nop 0\n" \
"v_mfma_f32_16x16x32_bf16 v[4:7], a[236:239], a[128:131], v[4:7]\n" \
"ds_read_b64_tr_b16 a[202:203], v227 offset:26624\n" \
"buffer_load_dwordx4 v[32:35], v208, %[srdz], s50 offen offset:0 sc1\n" \
"v_mfma_f32_16x16x32_bf16 v[4:7], a[208:211], a[132:135], v[4:7]\n" \
"ds_read_b64_tr_b16 a[204:205], v227 offset:28672\n" \
"v_mfma_f32_16x16x32_bf16 v[36:39], a[236:239], a[144:147], v[36:39]\n" \
"ds_read_b64_tr_b16 a[206:207], v227 offset:30720\n" \
"v_mfma_f32_16x16x32_bf16 v[36:39], a[208:211], a[148:151], v[36:39]\n" \
"buffer_load_dwordx4 v[40:43], v208, %[srdz], s50 offen offset:1024 sc1\n" \
"v_mfma_f32_16x16x32_bf16 v[68:71], a[236:239], a[160:163], v[68:71]\n" \
"v_mfma_f32_16x16x32_bf16 v[68:71], a[208:211], a[164:167], v[68:71]\n" \
"buffer_load_dwordx4 v[48:51], v208, %[srdz], s50 offen offset:2048 sc1\n" \
"v_mfma_f32_16x16x32_bf16 v[100:103], a[236:239], a[176:179], v[100:103]\n" \
"ds_read_b64_tr_b16 a[220:221], v215 offset:16896\n" \
"v_mfma_f32_16x16x32_bf16 v[100:103], a[208:211], a[180:183], v[100:103]\n" \
"ds_read_b64_tr_b16 a[222:223], v215 offset:18944\n" \
"v_mfma_f32_16x16x32_bf16 v[4:7], a[212:215], a[136:139], v[4:7]\n" \
"ds_read_b64_tr_b16 a[224:225], v215 offset:20992\n" \
"v_mfma_f32_16x16x32_bf16 v[4:7], a[228:231], a[140:143], v[4:7]\n" \
"ds_read_b64_tr_b16 a[226:227], v215 offset:23040\n" \
"v_mfma_f32_16x16x32_bf16 v[36:39], a[212:215], a[152:155], v[36:39]\n" \
"buffer_load_dwordx4 v214, %[srdt], s49 offen offset:3072 lds\n" \
"v_mfma_f32_16x16x32_bf16 v[36:39], a[228:231], a[156:159], v[36:39]\n" \
"v_mfma_f32_16x16x32_bf16 v[68:71], a[212:215], a[168:171], v[68:71]\n" \
"buffer_load_dwordx4 v[56:59], v208, %[srdz], s50 offen offset:3072 sc1\n" \
"v_mfma_f32_16x16x32_bf16 v[68:71], a[228:231], a[172:175], v[68:71]\n" \
"v_mfma_f32_16x16x32_bf16 v[100:103], a[212:215], a[184:187], v[100:103]\n" \
"ds_read_b64_tr_b16 a[232:233], v215 offset:25088\n" \
"v_mfma_f32_16x16x32_bf16 v[100:103], a[228:231], a[188:191], v[100:103]\n" \
"s_waitcnt vmcnt(63) lgkmcnt(5)\n" \
"s_nop 0\n" \
"v_mfma_f32_16x16x32_bf16 v[12:15], a[216:219], a[128:131], v[12:15]\n" \
"ds_read_b64_tr_b16 a[234:235], v215 offset:27136\n" \
"buffer_load_dwordx4 v[64:67], v209, %[srdz], s50 offen offset:0 sc1\n" \
"v_mfma_f32_16x16x32_bf16 v[12:15], a[196:199], a[132:135], v[12:15]\n" \
"ds_read_b64_tr_b16 a[236:237], v215 offset:29184\n" \
"v_mfma_f32_16x16x32_bf16 v[44:47], a[216:219], a[144:147], v[44:47]\n" \
"ds_read_b64_tr_b16 a[238:239], v215 offset:31232\n" \
"v_mfma_f32_16x16x32_bf16 v[44:47], a[196:199], a[148:151], v[44:47]\n" \
"buffer_load_dwordx4 v[72:75], v209, %[srdz], s50 offen offset:1024 sc1\n" \
"v_mfma_f32_16x16x32_bf16 v[76:79], a[216:219], a[160:163], v[76:79]\n" \
"v_mfma_f32_16x16x32_bf16 v[76:79], a[196:199], a[164:167], v[76:79]\n" \
"buffer_load_dwordx4 v[80:83], v209, %[srdz], s50 offen offset:2048 sc1\n" \
"v_mfma_f32_16x16x32_bf16 v[108:111], a[216:219], a[176:179], v[108:111]\n" \
"ds_read_b64_tr_b16 a[208:209], v227 offset:16896\n" \
"v_mfma_f32_16x16x32_bf16 v[108:111], a[196:199], a[180:183], v[108:111]\n" \
"ds_read_b64_tr_b16 a[210:211], v227 offset:18944\n" \
"v_mfma_f32_16x16x32_bf16 v[12:15], a[200:203], a[136:139], v[12:15]\n" \
"ds_read_b64_tr_b16 a[212:213], v227 offset:25088\n" \
"v_mfma_f32_16x16x32_bf16 v[12:15], a[204:207], a[140:143], v[12:15]\n" \
"ds_read_b64_tr_b16 a[214:215], v227 offset:27136\n" \
"v_mfma_f32_16x16x32_bf16 v[44:47], a[200:203], a[152:155], v[44:47]\n" \
"ds_read_b64_tr_b16 a[228:229], v227 offset:20992\n" \
"v_mfma_f32_16x16x32_bf16 v[44:47], a[204:207], a[156:159], v[44:47]\n" \
"ds_read_b64_tr_b16 a[230:231], v227 offset:23040\n" \
"buffer_load_dwordx4 v[88:91], v209, %[srdz], s50 offen offset:3072 sc1\n" \
"v_mfma_f32_16x16x32_bf16 v[76:79], a[200:203], a[168:171], v[76:79]\n" \
"ds_read_b64_tr_b16 a[216:217], v227 offset:29184\n" \
"v_mfma_f32_16x16x32_bf16 v[76:79], a[204:207], a[172:175], v[76:79]\n" \
"ds_read_b64_tr_b16 a[218:219], v227 offset:31232\n" \
"v_mfma_f32_16x16x32_bf16 v[108:111], a[200:203], a[184:187], v[108:111]\n" \
"buffer_load_dwordx4 v[96:99], v210, %[srdz], s50 offen offset:0 sc1\n" \
"v_mfma_f32_16x16x32_bf16 v[108:111], a[204:207], a[188:191], v[108:111]\n" \
"s_waitcnt vmcnt(63) lgkmcnt(12)\n" \
"s_nop 0\n" \
"v_mfma_f32_16x16x32_bf16 v[20:23], a[220:223], a[128:131], v[20:23]\n" \
"s_add_u32 m0, %[m0b], 114688\n" \
"s_cmp_gt_u32 s48, 5\n" \
"s_cselect_b32 s49, 5, 6\n" \
"s_lshl_b32 s49, s49, 18\n" \
"s_or_b32 s49, s49, s58\n" \
"buffer_load_dwordx4 %[wo0], %[srdt], s49 offen offset:0 lds\n" \
"v_mfma_f32_16x16x32_bf16 v[20:23], a[224:227], a[132:135], v[20:23]\n" \
"v_mfma_f32_16x16x32_bf16 v[52:55], a[220:223], a[144:147], v[52:55]\n" \
"buffer_load_dwordx4 v[104:107], v210, %[srdz], s50 offen offset:1024 sc1\n" \
"v_mfma_f32_16x16x32_bf16 v[52:55], a[224:227], a[148:151], v[52:55]\n" \
"v_mfma_f32_16x16x32_bf16 v[84:87], a[220:223], a[160:163], v[84:87]\n" \
"s_waitcnt vmcnt(24) lgkmcnt(0)\n" \
"s_barrier\n" \
"v_mfma_f32_16x16x32_bf16 v[84:87], a[224:227], a[164:167], v[84:87]\n" \
"ds_read_b64_tr_b16 a[196:197], v215 offset:32768\n" \
"v_mfma_f32_16x16x32_bf16 v[116:119], a[220:223], a[176:179], v[116:119]\n" \
"ds_read_b64_tr_b16 a[198:199], v215 offset:34816\n" \
"v_mfma_f32_16x16x32_bf16 v[116:119], a[224:227], a[180:183], v[116:119]\n" \
"ds_read_b64_tr_b16 a[200:201], v215 offset:36864\n" \
"buffer_load_dwordx4 v[112:115], v210, %[srdz], s50 offen offset:2048 sc1\n" \
"v_mfma_f32_16x16x32_bf16 v[20:23], a[232:235], a[136:139], v[20:23]\n" \
"ds_read_b64_tr_b16 a[202:203], v215 offset:38912\n" \
"v_mfma_f32_16x16x32_bf16 v[20:23], a[236:239], a[140:143], v[20:23]\n" \
"ds_read_b64_tr_b16 a[204:205], v215 offset:40960\n" \
"v_mfma_f32_16x16x32_bf16 v[52:55], a[232:235], a[152:155], v[52:55]\n" \
"ds_read_b64_tr_b16 a[206:207], v215 offset:43008\n" \
"v_mfma_f32_16x16x32_bf16 v[52:55], a[236:239], a[156:159], v[52:55]\n" \
"ds_read_b64_tr_b16 a[220:221], v215 offset:45056\n" \
"buffer_load_dwordx4 v[120:123], v210, %[srdz], s50 offen offset:3072 sc1\n" \
"v_mfma_f32_16x16x32_bf16 v[84:87], a[232:235], a[168:171], v[84:87]\n" \
"ds_read_b64_tr_b16 a[222:223], v215 offset:47104\n" \
"v_readlane_b32 s54, %[thi], 16\n" \
"v_mfma_f32_16x16x32_bf16 v[84:87], a[236:239], a[172:175], v[84:87]\n" \
"v_mfma_f32_16x16x32_bf16 v[116:119], a[232:235], a[184:187], v[116:119]\n" \
"v_mfma_f32_16x16x32_bf16 v[116:119], a[236:239], a[188:191], v[116:119]\n" \
"s_waitcnt vmcnt(23) lgkmcnt(4)\n" \
"s_nop 0\n" \
"v_mfma_f32_16x16x32_bf16 v[28:31], a[208:211], a[128:131], v[28:31]\n" \
"buffer_load_dwordx4 v212, %[srdt], s49 offen offset:1024 lds\n" \
"v_mfma_f32_16x16x32_bf16 v[28:31], a[228:231], a[132:135], v[28:31]\n" \
"v_mfma_f32_16x16x32_bf16 v[60:63], a[208:211], a[144:147], v[60:63]\n" \
"s_cmp_gt_u32 s48, 5\n" \
"s_cselect_b32 s50, 5, 6\n" \
"s_mul_i32 s50, s50, 152064\n" \
"buffer_load_dwordx4 a[0:3], %[oq0], %[srdz], s50 offen offset:0 sc1\n" \
"v_mfma_f32_16x16x32_bf16 v[60:63], a[228:231], a[148:151], v[60:63]\n" \
"v_mfma_f32_16x16x32_bf16 v[92:95], a[208:211], a[160:163], v[92:95]\n" \
"buffer_load_dword v232, v226, %[srdz], s54 offen sc1\n" \
"v_mfma_f32_16x16x32_bf16 v[92:95], a[228:231], a[164:167], v[92:95]\n" \
"v_mfma_f32_16x16x32_bf16 v[124:127], a[208:211], a[176:179], v[124:127]\n" \
".if . - adaln_body != 49152\n" \
".error \"size model\"\n" \
".endif\n" \
"Lk11_%=:\n" \
"s_cbranch_execz adaln_kr\n" \
"s_nop 0\n" \
"ds_read_b64_tr_b16 a[224:225], v227 offset:32768\n" \
"buffer_load_dwordx4 a[4:7], %[oq0], %[srdz], s50 offen offset:1024 sc1\n" \
"v_mfma_f32_16x16x32_bf16 v[124:127], a[228:231], a[180:183], v[124:127]\n" \
"ds_read_b64_tr_b16 a[226:227], v227 offset:34816\n" \
"v_mfma_f32_16x16x32_bf16 v[192:195], a[196:199], a[64:67], v[128:131]\n" \
"ds_read_b64_tr_b16 a[232:233], v227 offset:36864\n" \
"v_mfma_f32_16x16x32_bf16 v[192:195], a[200:203], a[68:71], v[192:195]\n" \
"ds_read_b64_tr_b16 a[234:235], v227 offset:38912\n" \
"v_mfma_f32_16x16x32_bf16 v[196:199], a[196:199], a[80:83], v[132:135]\n" \
"buffer_load_dwordx4 a[8:11], %[oq0], %[srdz], s50 offen offset:2048 sc1\n" \
"v_mfma_f32_16x16x32_bf16 v[196:199], a[200:203], a[84:87], v[196:199]\n" \
"v_mfma_f32_16x16x32_bf16 v[200:203], a[196:199], a[96:99], v[136:139]\n" \
"buffer_load_dwordx4 a[12:15], %[oq0], %[srdz], s50 offen offset:3072 sc1\n" \
"v_mfma_f32_16x16x32_bf16 v[200:203], a[200:203], a[100:103], v[200:203]\n" \
"v_mfma_f32_16x16x32_bf16 v[204:207], a[196:199], a[112:115], v[140:143]\n" \
"ds_read_b64_tr_b16 a[236:237], v227 offset:40960\n" \
"v_mfma_f32_16x16x32_bf16 v[204:207], a[200:203], a[116:119], v[204:207]\n" \
"s_waitcnt vmcnt(26) lgkmcnt(5)\n" \
"s_nop 0\n" \
"v_mfma_f32_16x16x32_bf16 v[192:195], a[204:207], a[72:75], v[192:195]\n" \
"ds_read_b64_tr_b16 a[238:239], v227 offset:43008\n" \
"v_mfma_f32_16x16x32_bf16 v[192:195], a[220:223], a[76:79], v[192:195]\n" \
"ds_read_b64_tr_b16 a[208:209], v227 offset:45056\n" \
"buffer_load_dwordx4 v213, %[srdt], s49 offen offset:2048 lds\n" \
"v_mfma_f32_16x16x32_bf16 v[196:199], a[204:207], a[88:91], v[196:199]\n" \
"ds_read_b64_tr_b16 a[210:211], v227 offset:47104\n" \
"v_mfma_f32_16x16x32_bf16 v[196:199], a[220:223], a[92:95], v[196:199]\n" \
"buffer_load_dwordx4 a[16:19], v208, %[srdz], s50 offen offset:0 sc1\n" \
"v_mfma_f32_16x16x32_bf16 v[200:203], a[204:207], a[104:107], v[200:203]\n" \
"v_mfma_f32_16x16x32_bf16 v[200:203], a[220:223], a[108:111], v[200:203]\n" \
"buffer_load_dwordx4 a[20:23], v208, %[srdz], s50 offen offset:1024 sc1\n" \
"v_mfma_f32_16x16x32_bf16 v[204:207], a[204:207], a[120:123], v[204:207]\n" \
"ds_read_b64_tr_b16 a[228:229], v215 offset:33280\n" \
"v_mfma_f32_16x16x32_bf16 v[204:207], a[220:223], a[124:127], v[204:207]\n" \
"ds_read_b64_tr_b16 a[230:231], v215 offset:35328\n" \
"v_mfma_f32_16x16x32_bf16 v[28:31], a[212:215], a[136:139], v[28:31]\n" \
"buffer_load_dwordx4 a[24:27], v208, %[srdz], s50 offen offset:2048 sc1\n" \
"v_mfma_f32_16x16x32_bf16 v[28:31], a[216:219], a[140:143], v[28:31]\n" \
"v_mfma_f32_16x16x32_bf16 v[60:63], a[212:215], a[152:155], v[60:63]\n" \
"buffer_load_dwordx4 a[28:31], v208, %[srdz], s50 offen offset:3072 sc1\n" \
"v_mfma_f32_16x16x32_bf16 v[60:63], a[216:219], a[156:159], v[60:63]\n" \
"v_mfma_f32_16x16x32_bf16 v[92:95], a[212:215], a[168:171], v[92:95]\n" \
"v_mfma_f32_16x16x32_bf16 v[92:95], a[216:219], a[172:175], v[92:95]\n" \
"v_cmp_u_f32_e64 s[44:45], v192, v196\n" \
"buffer_load_dwordx4 v214, %[srdt], s49 offen offset:3072 lds\n" \
"v_mfma_f32_16x16x32_bf16 v[124:127], a[212:215], a[184:187], v[124:127]\n" \
"v_mfma_f32_16x16x32_bf16 v[124:127], a[216:219], a[188:191], v[124:127]\n" \
"v_cmp_u_f32_e64 s[56:57], v200, v204\n" \
"s_or_b64 s[56:57], s[56:57], s[44:45]\n" \
"s_cbranch_scc1 Lsp4_%=\n" \
"Lbk4_%=:\n" \
"s_waitcnt vmcnt(63) lgkmcnt(2)\n" \
"s_nop 0\n" \
"v_mfma_f32_16x16x32_bf16 v[144:147], a[224:227], a[64:67], v[144:147]\n" \
"ds_read_b64_tr_b16 a[212:213], v215 offset:37376\n" \
"v_mfma_f32_16x16x32_bf16 v[144:147], a[232:235], a[68:71], v[144:147]\n" \
"ds_read_b64_tr_b16 a[214:215], v215 offset:39424\n" \
"buffer_load_dwordx4 a[32:35], v209, %[srdz], s50 offen offset:0 sc1\n" \
"v_mfma_f32_16x16x32_bf16 v[148:151], a[224:227], a[80:83], v[148:151]\n" \
"ds_read_b64_tr_b16 a[196:197], v215 offset:41472\n" \
"v_mfma_f32_16x16x32_bf16 v[148:151], a[232:235], a[84:87], v[148:151]\n" \
"ds_read_b64_tr_b16 a[198:199], v215 offset:43520\n" \
"v_mfma_f32_16x16x32_bf16 v[152:155], a[224:227], a[96:99], v[152:155]\n" \
"ds_read_b64_tr_b16 a[200:201], v215 offset:45568\n" \
"v_mfma_f32_16x16x32_bf16 v[152:155], a[232:235], a[100:103], v[152:155]\n" \
"ds_read_b64_tr_b16 a[202:203], v215 offset:47616\n" \
"buffer_load_dwordx4 a[36:39], v209, %[srdz], s50 offen offset:1024 sc1\n" \
"v_mfma_f32_16x16x32_bf16 v[156:159], a[224:227], a[112:115], v[156:159]\n" \
"ds_read_b64_tr_b16 a[204:205], v227 offset:33280\n" \
"v_mfma_f32_16x16x32_bf16 v[156:159], a[232:235], a[116:119], v[156:159]\n" \
"ds_read_b64_tr_b16 a[206:207], v227 offset:35328\n" \
"v_mfma_f32_16x16x32_bf16 v[144:147], a[236:239], a[72:75], v[144:147]\n" \
"ds_read_b64_tr_b16 a[216:217], v227 offset:41472\n" \
"v_mfma_f32_16x16x32_bf16 v[144:147], a[208:211], a[76:79], v[144:147]\n" \
"ds_read_b64_tr_b16 a[218:219], v227 offset:43520\n" \
"buffer_load_dwordx4 a[40:43], v209, %[srdz], s50 offen offset:2048 sc1\n" \
"v_mfma_f32_16x16x32_bf16 v[148:151], a[236:239], a[88:91], v[148:151]\n" \
"ds_read_b64_tr_b16 a[220:221], v227 offset:37376\n" \
"v_mfma_f32_16x16x32_bf16 v[148:151], a[208:211], a[92:95], v[148:151]\n" \
"ds_read_b64_tr_b16 a[222:223], v227 offset:39424\n" \
"v_mfma_f32_16x16x32_bf16 v[152:155], a[236:239], a[104:107], v[152:155]\n" \
"ds_read_b64_tr_b16 a[224:225], v227 offset:45568\n" \
"v_mfma_f32_16x16x32_bf16 v[152:155], a[208:211], a[108:111], v[152:155]\n" \
"ds_read_b64_tr_b16 a[226:227], v227 offset:47616\n" \
"buffer_load_dwordx4 a[44:47], v209, %[srdz], s50 offen offset:3072 sc1\n" \
"v_mfma_f32_16x16x32_bf16 v[156:159], a[236:239], a[120:123], v[156:159]\n" \
"v_mfma_f32_16x16x32_bf16 v[156:159], a[208:211], a[124:127], v[156:159]\n" \
"s_waitcnt vmcnt(63) lgkmcnt(12)\n" \
"s_nop 0\n" \
"v_mfma_f32_16x16x32_bf16 v[160:163], a[228:231], a[64:67], v[160:163]\n" \
"buffer_load_dwordx4 a[48:51], v210, %[srdz], s50 offen offset:0 sc1\n" \
"v_mfma_f32_16x16x32_bf16 v[160:163], a[212:215], a[68:71], v[160:163]\n" \
"v_mfma_f32_16x16x32_bf16 v[164:167], a[228:231], a[80:83], v[164:167]\n" \
"buffer_load_dwordx4 a[52:55], v210, %[srdz], s50 offen offset:1024 sc1\n" \
"v_mfma_f32_16x16x32_bf16 v[164:167], a[212:215], a[84:87], v[164:167]\n" \
"v_mfma_f32_16x16x32_bf16 v[168:171], a[228:231], a[96:99], v[168:171]\n" \
"s_waitcnt vmcnt(28) lgkmcnt(0)\n" \
"s_barrier\n" \
"v_mfma_f32_16x16x32_bf16 v[168:171], a[212:215], a[100:103], v[168:171]\n" \
"ds_read_b64_tr_b16 a[232:233], v220 offset:32768\n" \
"v_mfma_f32_16x16x32_bf16 v[172:175], a[228:231], a[112:115], v[172:175]\n" \
"ds_read_b64_tr_b16 a[234:235], v220 offset:34816\n" \
"v_mfma_f32_16x16x32_bf16 v[172:175], a[212:215], a[116:119], v[172:175]\n" \
"ds_read_b64_tr_b16 a[236:237], v220 offset:36864\n" \
"v_mfma_f32_16x16x32_bf16 v[160:163], a[196:199], a[72:75], v[160:163]\n" \
"ds_read_b64_tr_b16 a[238:239], v220 offset:38912\n" \
"v_mfma_f32_16x16x32_bf16 v[160:163], a[200:203], a[76:79], v[160:163]\n" \
"ds_read_b64_tr_b16 a[208:209], v220 offset:40960\n" \
"s_add_u32 m0, %[m0b], 49152\n" \
"s_cmp_gt_u32 s48, 6\n" \
"s_cselect_b32 s49, 6, 7\n" \
"s_lshl_b32 s49, s49, 18\n" \
"s_or_b32 s49, s49, s58\n" \
"buffer_load_dwordx4 %[wo0], %[srdt], s49 offen offset:0 lds\n" \
"v_mfma_f32_16x16x32_bf16 v[164:167], a[196:199], a[88:91], v[164:167]\n" \
"ds_read_b64_tr_b16 a[210:211], v220 offset:43008\n" \
"v_mfma_f32_16x16x32_bf16 v[164:167], a[200:203], a[92:95], v[164:167]\n" \
"ds_read_b64_tr_b16 a[228:229], v220 offset:45056\n" \
"buffer_load_dwordx4 a[56:59], v210, %[srdz], s50 offen offset:2048 sc1\n" \
"v_mfma_f32_16x16x32_bf16 v[168:171], a[196:199], a[104:107], v[168:171]\n" \
"ds_read_b64_tr_b16 a[230:231], v220 offset:47104\n" \
"v_mfma_f32_16x16x32_bf16 v[168:171], a[200:203], a[108:111], v[168:171]\n" \
"buffer_load_dwordx4 a[60:63], v210, %[srdz], s50 offen offset:3072 sc1\n" \
"v_mfma_f32_16x16x32_bf16 v[172:175], a[196:199], a[120:123], v[172:175]\n" \
"v_mfma_f32_16x16x32_bf16 v[172:175], a[200:203], a[124:127], v[172:175]\n" \
"s_waitcnt vmcnt(23) lgkmcnt(4)\n" \
"s_nop 0\n" \
"v_mfma_f32_16x16x32_bf16 v[176:179], a[204:207], a[64:67], v[176:179]\n" \
"v_mfma_f32_16x16x32_bf16 v[176:179], a[220:223], a[68:71], v[176:179]\n" \
"s_cmp_gt_u32 s48, 6\n" \
"s_cselect_b32 s50, 6, 7\n" \
"s_mul_i32 s50, s50, 152064\n" \
"buffer_load_dwordx4 a[128:131], %[oq0], %[srdz], s50 offen offset:0 sc1\n" \
"v_mfma_f32_16x16x32_bf16 v[180:183], a[204:207], a[80:83], v[180:183]\n" \
"v_mfma_f32_16x16x32_bf16 v[180:183], a[220:223], a[84:87], v[180:183]\n" \
"v_mfma_f32_16x16x32_bf16 v[184:187], a[204:207], a[96:99], v[184:187]\n" \
"v_mfma_f32_16x16x32_bf16 v[184:187], a[220:223], a[100:103], v[184:187]\n" \
"buffer_load_dwordx4 v212, %[srdt], s49 offen offset:1024 lds\n" \
"v_mfma_f32_16x16x32_bf16 v[188:191], a[204:207], a[112:115], v[188:191]\n" \
"ds_read_b64_tr_b16 a[212:213], v221 offset:32768\n" \
"v_mfma_f32_16x16x32_bf16 v[188:191], a[220:223], a[116:119], v[188:191]\n" \
"ds_read_b64_tr_b16 a[214:215], v221 offset:34816\n" \
"v_mfma_f32_16x16x32_bf16 v[128:131], a[232:235], v[0:3], v[192:195]\n" \
"ds_read_b64_tr_b16 a[196:197], v221 offset:36864\n" \
"buffer_load_dwordx4 a[132:135], %[oq0], %[srdz], s50 offen offset:1024 sc1\n" \
"v_mfma_f32_16x16x32_bf16 v[128:131], a[236:239], v[8:11], v[128:131]\n" \
"ds_read_b64_tr_b16 a[198:199], v221 offset:38912\n" \
"v_mfma_f32_16x16x32_bf16 v[132:135], a[232:235], v[32:35], v[196:199]\n" \
"buffer_load_dwordx4 a[136:139], %[oq0], %[srdz], s50 offen offset:2048 sc1\n" \
"v_mfma_f32_16x16x32_bf16 v[132:135], a[236:239], v[40:43], v[132:135]\n" \
"v_mfma_f32_16x16x32_bf16 v[136:139], a[232:235], v[64:67], v[200:203]\n" \
"v_mfma_f32_16x16x32_bf16 v[136:139], a[236:239], v[72:75], v[136:139]\n" \
"buffer_load_dwordx4 a[140:143], %[oq0], %[srdz], s50 offen offset:3072 sc1\n" \
"v_mfma_f32_16x16x32_bf16 v[140:143], a[232:235], v[96:99], v[204:207]\n" \
"ds_read_b64_tr_b16 a[200:201], v221 offset:40960\n" \
"v_mfma_f32_16x16x32_bf16 v[140:143], a[236:239], v[104:107], v[140:143]\n" \
"s_waitcnt vmcnt(26) lgkmcnt(5)\n" \
"s_nop 0\n" \
"v_mfma_f32_16x16x32_bf16 v[128:131], a[208:211], v[16:19], v[128:131]\n" \
"ds_read_b64_tr_b16 a[202:203], v221 offset:43008\n" \
"v_mfma_f32_16x16x32_bf16 v[128:131], a[228:231], v[24:27], v[128:131]\n" \
"ds_read_b64_tr_b16 a[204:205], v221 offset:45056\n" \
"v_mfma_f32_16x16x32_bf16 v[132:135], a[208:211], v[48:51], v[132:135]\n" \
"ds_read_b64_tr_b16 a[206:207], v221 offset:47104\n" \
"v_mfma_f32_16x16x32_bf16 v[132:135], a[228:231], v[56:59], v[132:135]\n" \
"buffer_load_dwordx4 a[144:147], v208, %[srdz], s50 offen offset:0 sc1\n" \
"v_mfma_f32_16x16x32_bf16 v[136:139], a[208:211], v[80:83], v[136:139]\n" \
"v_mfma_f32_16x16x32_bf16 v[136:139], a[228:231], v[88:91], v[136:139]\n" \
"buffer_load_dwordx4 v213, %[srdt], s49 offen offset:2048 lds\n" \
"v_mfma_f32_16x16x32_bf16 v[140:143], a[208:211], v[112:115], v[140:143]\n" \
"ds_read_b64_tr_b16 a[220:221], v220 offset:33280\n" \
"v_mfma_f32_16x16x32_bf16 v[140:143], a[228:231], v[120:123], v[140:143]\n" \
"ds_read_b64_tr_b16 a[222:223], v220 offset:35328\n" \
"v_mfma_f32_16x16x32_bf16 v[176:179], a[216:219], a[72:75], v[176:179]\n" \
"buffer_load_dwordx4 a[148:151], v208, %[srdz], s50 offen offset:1024 sc1\n" \
"v_mfma_f32_16x16x32_bf16 v[176:179], a[224:227], a[76:79], v[176:179]\n" \
"v_mfma_f32_16x16x32_bf16 v[180:183], a[216:219], a[88:91], v[180:183]\n" \
"buffer_load_dwordx4 a[152:155], v208, %[srdz], s50 offen offset:2048 sc1\n" \
"v_mfma_f32_16x16x32_bf16 v[180:183], a[224:227], a[92:95], v[180:183]\n" \
"v_mfma_f32_16x16x32_bf16 v[184:187], a[216:219], a[104:107], v[184:187]\n" \
"v_mfma_f32_16x16x32_bf16 v[184:187], a[224:227], a[108:111], v[184:187]\n" \
"v_cmp_u_f32_e64 s[44:45], v128, v132\n" \
"buffer_load_dwordx4 a[156:159], v208, %[srdz], s50 offen offset:3072 sc1\n" \
"v_mfma_f32_16x16x32_bf16 v[188:191], a[216:219], a[120:123], v[188:191]\n" \
"v_mfma_f32_16x16x32_bf16 v[188:191], a[224:227], a[124:127], v[188:191]\n" \
"v_cmp_u_f32_e64 s[56:57], v136, v140\n" \
"s_or_b64 s[56:57], s[56:57], s[44:45]\n" \
"s_cbranch_scc1 Lsp5_%=\n" \
"Lbk5_%=:\n" \
"s_waitcnt vmcnt(63) lgkmcnt(2)\n" \
"s_nop 0\n" \
"v_mfma_f32_16x16x32_bf16 v[144:147], a[212:215], v[0:3], v[144:147]\n" \
"ds_read_b64_tr_b16 a[216:217], v220 offset:37376\n" \
"v_mfma_f32_16x16x32_bf16 v[144:147], a[196:199], v[8:11], v[144:147]\n" \
"ds_read_b64_tr_b16 a[218:219], v220 offset:39424\n" \
"v_mfma_f32_16x16x32_bf16 v[148:151], a[212:215], v[32:35], v[148:151]\n" \
"ds_read_b64_tr_b16 a[208:209], v220 offset:41472\n" \
"v_mfma_f32_16x16x32_bf16 v[148:151], a[196:199], v[40:43], v[148:151]\n" \
"ds_read_b64_tr_b16 a[210:211], v220 offset:43520\n" \
"v_mfma_f32_16x16x32_bf16 v[152:155], a[212:215], v[64:67], v[152:155]\n" \
"ds_read_b64_tr_b16 a[224:225], v220 offset:45568\n" \
"buffer_load_dwordx4 a[160:163], v209, %[srdz], s50 offen offset:0 sc1\n" \
"v_mfma_f32_16x16x32_bf16 v[152:155], a[196:199], v[72:75], v[152:155]\n" \
"ds_read_b64_tr_b16 a[226:227], v220 offset:47616\n" \
"v_mfma_f32_16x16x32_bf16 v[156:159], a[212:215], v[96:99], v[156:159]\n" \
"ds_read_b64_tr_b16 a[228:229], v221 offset:33280\n" \
"buffer_load_dwordx4 v214, %[srdt], s49 offen offset:3072 lds\n" \
"v_mfma_f32_16x16x32_bf16 v[156:159], a[196:199], v[104:107], v[156:159]\n" \
"ds_read_b64_tr_b16 a[230:231], v221 offset:35328\n" \
"v_mfma_f32_16x16x32_bf16 v[144:147], a[200:203], v[16:19], v[144:147]\n" \
"ds_read_b64_tr_b16 a[232:233], v221 offset:41472\n" \
"buffer_load_dwordx4 a[164:167], v209, %[srdz], s50 offen offset:1024 sc1\n" \
"v_mfma_f32_16x16x32_bf16 v[144:147], a[204:207], v[24:27], v[144:147]\n" \
"ds_read_b64_tr_b16 a[234:235], v221 offset:43520\n" \
"v_mfma_f32_16x16x32_bf16 v[148:151], a[200:203], v[48:51], v[148:151]\n" \
"ds_read_b64_tr_b16 a[236:237], v221 offset:37376\n" \
"v_mfma_f32_16x16x32_bf16 v[148:151], a[204:207], v[56:59], v[148:151]\n" \
"ds_read_b64_tr_b16 a[238:239], v221 offset:39424\n" \
"v_mfma_f32_16x16x32_bf16 v[152:155], a[200:203], v[80:83], v[152:155]\n" \
"ds_read_b64_tr_b16 a[212:213], v221 offset:45568\n" \
"buffer_load_dwordx4 a[168:171], v209, %[srdz], s50 offen offset:2048 sc1\n" \
"v_mfma_f32_16x16x32_bf16 v[152:155], a[204:207], v[88:91], v[152:155]\n" \
"ds_read_b64_tr_b16 a[214:215], v221 offset:47616\n" \
"v_mfma_f32_16x16x32_bf16 v[156:159], a[200:203], v[112:115], v[156:159]\n" \
"buffer_load_dwordx4 a[172:175], v209, %[srdz], s50 offen offset:3072 sc1\n" \
"v_mfma_f32_16x16x32_bf16 v[156:159], a[204:207], v[120:123], v[156:159]\n" \
"s_waitcnt vmcnt(63) lgkmcnt(12)\n" \
"s_nop 0\n" \
"v_mfma_f32_16x16x32_bf16 v[160:163], a[220:223], v[0:3], v[160:163]\n" \
"v_mfma_f32_16x16x32_bf16 v[160:163], a[216:219], v[8:11], v[160:163]\n" \
"buffer_load_dwordx4 a[176:179], v210, %[srdz], s50 offen offset:0 sc1\n" \
"v_mfma_f32_16x16x32_bf16 v[164:167], a[220:223], v[32:35], v[164:167]\n" \
"v_mfma_f32_16x16x32_bf16 v[164:167], a[216:219], v[40:43], v[164:167]\n" \
"v_mfma_f32_16x16x32_bf16 v[168:171], a[220:223], v[64:67], v[168:171]\n" \
"s_waitcnt vmcnt(25) lgkmcnt(0)\n" \
"s_barrier\n" \
"v_mfma_f32_16x16x32_bf16 v[168:171], a[216:219], v[72:75], v[168:171]\n" \
"ds_read_b64_tr_b16 a[196:197], v220 offset:49152\n" \
"v_mfma_f32_16x16x32_bf16 v[172:175], a[220:223], v[96:99], v[172:175]\n" \
"ds_read_b64_tr_b16 a[198:199], v220 offset:51200\n" \
"v_mfma_f32_16x16x32_bf16 v[172:175], a[216:219], v[104:107], v[172:175]\n" \
"ds_read_b64_tr_b16 a[200:201], v220 offset:53248\n" \
"v_mfma_f32_16x16x32_bf16 v[160:163], a[208:211], v[16:19], v[160:163]\n" \
"ds_read_b64_tr_b16 a[202:203], v220 offset:55296\n" \
"buffer_load_dwordx4 a[180:183], v210, %[srdz], s50 offen offset:1024 sc1\n" \
"v_mfma_f32_16x16x32_bf16 v[160:163], a[224:227], v[24:27], v[160:163]\n" \
"ds_read_b64_tr_b16 a[204:205], v220 offset:57344\n" \
"v_mfma_f32_16x16x32_bf16 v[164:167], a[208:211], v[48:51], v[164:167]\n" \
"ds_read_b64_tr_b16 a[206:207], v220 offset:59392\n" \
"v_mfma_f32_16x16x32_bf16 v[164:167], a[224:227], v[56:59], v[164:167]\n" \
"ds_read_b64_tr_b16 a[220:221], v220 offset:61440\n" \
"v_mfma_f32_16x16x32_bf16 v[168:171], a[208:211], v[80:83], v[168:171]\n" \
"ds_read_b64_tr_b16 a[222:223], v220 offset:63488\n" \
"buffer_load_dwordx4 a[184:187], v210, %[srdz], s50 offen offset:2048 sc1\n" \
"v_mfma_f32_16x16x32_bf16 v[168:171], a[224:227], v[88:91], v[168:171]\n" \
"v_mfma_f32_16x16x32_bf16 v[172:175], a[208:211], v[112:115], v[172:175]\n" \
"buffer_load_dwordx4 a[188:191], v210, %[srdz], s50 offen offset:3072 sc1\n" \
"v_mfma_f32_16x16x32_bf16 v[172:175], a[224:227], v[120:123], v[172:175]\n" \
"s_waitcnt vmcnt(63) lgkmcnt(4)\n" \
"s_nop 0\n" \
"v_mfma_f32_16x16x32_bf16 v[176:179], a[228:231], v[0:3], v[176:179]\n" \
"v_mfma_f32_16x16x32_bf16 v[176:179], a[236:239], v[8:11], v[176:179]\n" \
"v_mfma_f32_16x16x32_bf16 v[180:183], a[228:231], v[32:35], v[180:183]\n" \
"v_mfma_f32_16x16x32_bf16 v[180:183], a[236:239], v[40:43], v[180:183]\n" \
"v_mfma_f32_16x16x32_bf16 v[184:187], a[228:231], v[64:67], v[184:187]\n" \
"v_mfma_f32_16x16x32_bf16 v[184:187], a[236:239], v[72:75], v[184:187]\n" \
"v_mfma_f32_16x16x32_bf16 v[188:191], a[228:231], v[96:99], v[188:191]\n" \
"ds_read_b64_tr_b16 a[216:217], v221 offset:49152\n" \
"s_waitcnt vmcnt(22)\n" \
"v_cmp_eq_u32_e32 vcc, 0, v232\n" \
"s_cbranch_vccnz Lspp_%=\n" \
"Lbkp_%=:\n" \
"s_nop 0\n" \
"v_mfma_f32_16x16x32_bf16 v[188:191], a[236:239], v[104:107], v[188:191]\n" \
"ds_read_b64_tr_b16 a[218:219], v221 offset:51200\n" \
"v_mfma_f32_16x16x32_bf16 v[192:195], a[196:199], a[0:3], v[128:131]\n" \
"ds_read_b64_tr_b16 a[208:209], v221 offset:53248\n" \
"v_mfma_f32_16x16x32_bf16 v[192:195], a[200:203], a[4:7], v[192:195]\n" \
"ds_read_b64_tr_b16 a[210:211], v221 offset:55296\n" \
"s_add_u32 m0, %[m0b], 0\n" \
"s_cmp_gt_u32 s48, 3\n" \
"s_cselect_b32 s49, 3, 4\n" \
"s_lshl_b32 s49, s49, 18\n" \
"s_or_b32 s49, s49, s59\n" \
"s_nop 0\n" \
"buffer_load_dwordx4 %[wo0], %[srdt], s49 offen offset:0 lds\n" \
"v_mfma_f32_16x16x32_bf16 v[196:199], a[196:199], a[16:19], v[132:135]\n" \
"v_mfma_f32_16x16x32_bf16 v[196:199], a[200:203], a[20:23], v[196:199]\n" \
"v_mfma_f32_16x16x32_bf16 v[200:203], a[196:199], a[32:35], v[136:139]\n" \
"v_mfma_f32_16x16x32_bf16 v[200:203], a[200:203], a[36:39], v[200:203]\n" \
"v_mfma_f32_16x16x32_bf16 v[204:207], a[196:199], a[48:51], v[140:143]\n" \
"ds_read_b64_tr_b16 a[224:225], v221 offset:57344\n" \
"v_mfma_f32_16x16x32_bf16 v[204:207], a[200:203], a[52:55], v[204:207]\n" \
"s_waitcnt vmcnt(20) lgkmcnt(5)\n" \
"s_nop 0\n" \
"v_mfma_f32_16x16x32_bf16 v[192:195], a[204:207], a[8:11], v[192:195]\n" \
"ds_read_b64_tr_b16 a[226:227], v221 offset:59392\n" \
"v_mfma_f32_16x16x32_bf16 v[192:195], a[220:223], a[12:15], v[192:195]\n" \
"ds_read_b64_tr_b16 a[228:229], v221 offset:61440\n" \
"v_mfma_f32_16x16x32_bf16 v[196:199], a[204:207], a[24:27], v[196:199]\n" \
"ds_read_b64_tr_b16 a[230:231], v221 offset:63488\n" \
"v_mfma_f32_16x16x32_bf16 v[196:199], a[220:223], a[28:31], v[196:199]\n" \
"buffer_load_dwordx4 v212, %[srdt], s49 offen offset:1024 lds\n" \
"v_mfma_f32_16x16x32_bf16 v[200:203], a[204:207], a[40:43], v[200:203]\n" \
"v_mfma_f32_16x16x32_bf16 v[200:203], a[220:223], a[44:47], v[200:203]\n" \
"v_mfma_f32_16x16x32_bf16 v[204:207], a[204:207], a[56:59], v[204:207]\n" \
"ds_read_b64_tr_b16 a[236:237], v220 offset:49664\n" \
"v_mfma_f32_16x16x32_bf16 v[204:207], a[220:223], a[60:63], v[204:207]\n" \
"ds_read_b64_tr_b16 a[238:239], v220 offset:51712\n" \
"v_mfma_f32_16x16x32_bf16 v[176:179], a[232:235], v[16:19], v[176:179]\n" \
"v_mfma_f32_16x16x32_bf16 v[176:179], a[212:215], v[24:27], v[176:179]\n" \
"v_mfma_f32_16x16x32_bf16 v[180:183], a[232:235], v[48:51], v[180:183]\n" \
"buffer_load_dwordx4 v213, %[srdt], s49 offen offset:2048 lds\n" \
"v_mfma_f32_16x16x32_bf16 v[180:183], a[212:215], v[56:59], v[180:183]\n" \
"v_mfma_f32_16x16x32_bf16 v[184:187], a[232:235], v[80:83], v[184:187]\n" \
"s_add_u32 m0, %[m0b], 98304\n" \
"s_mov_b32 s61, 0\n" \
"s_nop 0\n" \
"buffer_load_dwordx4 %[vdn], s[64:67], s61 offen offset:0 lds\n" \
"v_mfma_f32_16x16x32_bf16 v[184:187], a[212:215], v[88:91], v[184:187]\n" \
"v_cmp_u_f32_e64 s[44:45], v192, v196\n" \
"v_mfma_f32_16x16x32_bf16 v[188:191], a[232:235], v[112:115], v[188:191]\n" \
"v_mfma_f32_16x16x32_bf16 v[188:191], a[212:215], v[120:123], v[188:191]\n" \
"v_cmp_u_f32_e64 s[56:57], v200, v204\n" \
"s_or_b64 s[56:57], s[56:57], s[44:45]\n" \
"s_cbranch_scc1 Lsp6_%=\n" \
"Lbk6_%=:\n" \
"s_waitcnt vmcnt(63) lgkmcnt(2)\n" \
"s_nop 0\n" \
"v_mfma_f32_16x16x32_bf16 v[144:147], a[216:219], a[0:3], v[144:147]\n" \
"ds_read_b64_tr_b16 a[232:233], v220 offset:53760\n" \
"v_mfma_f32_16x16x32_bf16 v[144:147], a[208:211], a[4:7], v[144:147]\n" \
"ds_read_b64_tr_b16 a[234:235], v220 offset:55808\n" \
"v_mfma_f32_16x16x32_bf16 v[148:151], a[216:219], a[16:19], v[148:151]\n" \
"ds_read_b64_tr_b16 a[196:197], v220 offset:57856\n" \
"v_mfma_f32_16x16x32_bf16 v[148:151], a[208:211], a[20:23], v[148:151]\n" \
"ds_read_b64_tr_b16 a[198:199], v220 offset:59904\n" \
"v_mfma_f32_16x16x32_bf16 v[152:155], a[216:219], a[32:35], v[152:155]\n" \
"ds_read_b64_tr_b16 a[200:201], v220 offset:61952\n" \
"v_mfma_f32_16x16x32_bf16 v[152:155], a[208:211], a[36:39], v[152:155]\n" \
"ds_read_b64_tr_b16 a[202:203], v220 offset:64000\n" \
"v_mfma_f32_16x16x32_bf16 v[156:159], a[216:219], a[48:51], v[156:159]\n" \
"ds_read_b64_tr_b16 a[204:205], v221 offset:49664\n" \
"v_mfma_f32_16x16x32_bf16 v[156:159], a[208:211], a[52:55], v[156:159]\n" \
"ds_read_b64_tr_b16 a[206:207], v221 offset:51712\n" \
"s_add_u32 m0, %[m0b], 0\n" \
"s_nop 0\n" \
"buffer_load_dwordx4 v214, %[srdt], s49 offen offset:3072 lds\n" \
"v_mfma_f32_16x16x32_bf16 v[144:147], a[224:227], a[8:11], v[144:147]\n" \
"ds_read_b64_tr_b16 a[212:213], v221 offset:57856\n" \
"v_mfma_f32_16x16x32_bf16 v[144:147], a[228:231], a[12:15], v[144:147]\n" \
"ds_read_b64_tr_b16 a[214:215], v221 offset:59904\n" \
"v_mfma_f32_16x16x32_bf16 v[148:151], a[224:227], a[24:27], v[148:151]\n" \
"ds_read_b64_tr_b16 a[220:221], v221 offset:53760\n" \
"v_mfma_f32_16x16x32_bf16 v[148:151], a[228:231], a[28:31], v[148:151]\n" \
"ds_read_b64_tr_b16 a[222:223], v221 offset:55808\n" \
"v_mfma_f32_16x16x32_bf16 v[152:155], a[224:227], a[40:43], v[152:155]\n" \
"ds_read_b64_tr_b16 a[216:217], v221 offset:61952\n" \
"v_mfma_f32_16x16x32_bf16 v[152:155], a[228:231], a[44:47], v[152:155]\n" \
"ds_read_b64_tr_b16 a[218:219], v221 offset:64000\n" \
"v_mfma_f32_16x16x32_bf16 v[156:159], a[224:227], a[56:59], v[156:159]\n" \
"s_add_u32 m0, %[m0b], 99328\n" \
"s_mov_b32 s61, 4096\n" \
"buffer_load_dwordx4 %[vdn], s[64:67], s61 offen offset:0 lds\n" \
"v_mfma_f32_16x16x32_bf16 v[156:159], a[228:231], a[60:63], v[156:159]\n" \
"s_waitcnt vmcnt(63) lgkmcnt(12)\n" \
"s_nop 0\n" \
"v_mfma_f32_16x16x32_bf16 v[160:163], a[236:239], a[0:3], v[160:163]\n" \
"v_mfma_f32_16x16x32_bf16 v[160:163], a[232:235], a[4:7], v[160:163]\n" \
"v_mfma_f32_16x16x32_bf16 v[164:167], a[236:239], a[16:19], v[164:167]\n" \
"v_mfma_f32_16x16x32_bf16 v[164:167], a[232:235], a[20:23], v[164:167]\n" \
"s_add_u32 m0, %[m0b], 16384\n" \
"s_cmp_gt_u32 s48, 4\n" \
"s_cselect_b32 s49, 4, 5\n" \
"s_lshl_b32 s49, s49, 18\n" \
"s_or_b32 s49, s49, s59\n" \
"buffer_load_dwordx4 %[wo0], %[srdt], s49 offen offset:0 lds\n" \
"v_mfma_f32_16x16x32_bf16 v[168:171], a[236:239], a[32:35], v[168:171]\n" \
"s_waitcnt vmcnt(14) lgkmcnt(0)\n" \
"s_barrier\n" \
"v_mfma_f32_16x16x32_bf16 v[168:171], a[232:235], a[36:39], v[168:171]\n" \
"ds_read_b64_tr_b16 a[208:209], v215 offset:49152\n" \
"v_mfma_f32_16x16x32_bf16 v[172:175], a[236:239], a[48:51], v[172:175]\n" \
"ds_read_b64_tr_b16 a[210:211], v215 offset:51200\n" \
"v_mfma_f32_16x16x32_bf16 v[172:175], a[232:235], a[52:55], v[172:175]\n" \
"ds_read_b64_tr_b16 a[224:225], v215 offset:53248\n" \
"v_mfma_f32_16x16x32_bf16 v[160:163], a[196:199], a[8:11], v[160:163]\n" \
"ds_read_b64_tr_b16 a[226:227], v215 offset:55296\n" \
"v_mfma_f32_16x16x32_bf16 v[160:163], a[200:203], a[12:15], v[160:163]\n" \
"ds_read_b64_tr_b16 a[228:229], v215 offset:57344\n" \
"v_mfma_f32_16x16x32_bf16 v[164:167], a[196:199], a[24:27], v[164:167]\n" \
"ds_read_b64_tr_b16 a[230:231], v215 offset:59392\n" \
"v_mfma_f32_16x16x32_bf16 v[164:167], a[200:203], a[28:31], v[164:167]\n" \
"ds_read_b64_tr_b16 a[236:237], v215 offset:61440\n" \
"v_mfma_f32_16x16x32_bf16 v[168:171], a[196:199], a[40:43], v[168:171]\n" \
"ds_read_b64_tr_b16 a[238:239], v215 offset:63488\n" \
"v_mfma_f32_16x16x32_bf16 v[168:171], a[200:203], a[44:47], v[168:171]\n" \
"buffer_load_dwordx4 v212, %[srdt], s49 offen offset:1024 lds\n" \
"v_mfma_f32_16x16x32_bf16 v[172:175], a[196:199], a[56:59], v[172:175]\n" \
"v_mfma_f32_16x16x32_bf16 v[172:175], a[200:203], a[60:63], v[172:175]\n" \
"s_waitcnt vmcnt(10) lgkmcnt(4)\n" \
"s_nop 0\n" \
"v_mfma_f32_16x16x32_bf16 v[176:179], a[204:207], a[0:3], v[176:179]\n" \
"s_add_u32 m0, %[m0b], 100352\n" \
"s_mov_b32 s61, 8192\n" \
"buffer_load_dwordx4 %[vdn], s[64:67], s61 offen offset:0 lds\n" \
"v_mfma_f32_16x16x32_bf16 v[176:179], a[220:223], a[4:7], v[176:179]\n" \
"v_mfma_f32_16x16x32_bf16 v[180:183], a[204:207], a[16:19], v[180:183]\n" \
"v_mfma_f32_16x16x32_bf16 v[180:183], a[220:223], a[20:23], v[180:183]\n" \
"v_mfma_f32_16x16x32_bf16 v[184:187], a[204:207], a[32:35], v[184:187]\n" \
"v_mfma_f32_16x16x32_bf16 v[184:187], a[220:223], a[36:39], v[184:187]\n" \
"v_mfma_f32_16x16x32_bf16 v[188:191], a[204:207], a[48:51], v[188:191]\n" \
"ds_read_b64_tr_b16 a[232:233], v227 offset:49152\n" \
"v_mfma_f32_16x16x32_bf16 v[188:191], a[220:223], a[52:55], v[188:191]\n" \
"ds_read_b64_tr_b16 a[234:235], v227 offset:51200\n" \
"v_mfma_f32_16x16x32_bf16 v[128:131], a[208:211], a[128:131], v[192:195]\n" \
"ds_read_b64_tr_b16 a[196:197], v227 offset:53248\n" \
"v_mfma_f32_16x16x32_bf16 v[128:131], a[224:227], a[132:135], v[128:131]\n" \
"ds_read_b64_tr_b16 a[198:199], v227 offset:55296\n" \
"v_mfma_f32_16x16x32_bf16 v[132:135], a[208:211], a[144:147], v[196:199]\n" \
"s_add_u32 m0, %[m0b], 16384\n" \
"s_nop 0\n" \
"s_nop 0\n" \
"buffer_load_dwordx4 v213, %[srdt], s49 offen offset:2048 lds\n" \
"v_mfma_f32_16x16x32_bf16 v[132:135], a[224:227], a[148:151], v[132:135]\n" \
"v_mfma_f32_16x16x32_bf16 v[136:139], a[208:211], a[160:163], v[200:203]\n" \
"v_mfma_f32_16x16x32_bf16 v[136:139], a[224:227], a[164:167], v[136:139]\n" \
"v_mfma_f32_16x16x32_bf16 v[140:143], a[208:211], a[176:179], v[204:207]\n" \
"ds_read_b64_tr_b16 a[200:201], v227 offset:57344\n" \
"v_mfma_f32_16x16x32_bf16 v[140:143], a[224:227], a[180:183], v[140:143]\n" \
"s_waitcnt vmcnt(10) lgkmcnt(5)\n" \
"s_nop 0\n" \
"v_mfma_f32_16x16x32_bf16 v[128:131], a[228:231], a[136:139], v[128:131]\n" \
"ds_read_b64_tr_b16 a[202:203], v227 offset:59392\n" \
"v_mfma_f32_16x16x32_bf16 v[128:131], a[236:239], a[140:143], v[128:131]\n" \
"ds_read_b64_tr_b16 a[204:205], v227 offset:61440\n" \
"s_add_u32 m0, %[m0b], 101376\n" \
"s_mov_b32 s61, 12288\n" \
"buffer_load_dwordx4 %[vdn], s[64:67], s61 offen offset:0 lds\n" \
"v_mfma_f32_16x16x32_bf16 v[132:135], a[228:231], a[152:155], v[132:135]\n" \
"ds_read_b64_tr_b16 a[206:207], v227 offset:63488\n" \
"v_mfma_f32_16x16x32_bf16 v[132:135], a[236:239], a[156:159], v[132:135]\n" \
"s_add_u32 m0, %[m0b], 16384\n" \
"s_nop 0\n" \
"s_nop 0\n" \
"buffer_load_dwordx4 v214, %[srdt], s49 offen offset:3072 lds\n" \
"v_mfma_f32_16x16x32_bf16 v[136:139], a[228:231], a[168:171], v[136:139]\n" \
"v_mfma_f32_16x16x32_bf16 v[136:139], a[236:239], a[172:175], v[136:139]\n" \
"v_mfma_f32_16x16x32_bf16 v[140:143], a[228:231], a[184:187], v[140:143]\n" \
"ds_read_b64_tr_b16 a[220:221], v215 offset:49664\n" \
"v_mfma_f32_16x16x32_bf16 v[140:143], a[236:239], a[188:191], v[140:143]\n" \
"ds_read_b64_tr_b16 a[222:223], v215 offset:51712\n" \
"v_mfma_f32_16x16x32_bf16 v[176:179], a[212:215], a[8:11], v[176:179]\n" \
"v_mfma_f32_16x16x32_bf16 v[176:179], a[216:219], a[12:15], v[176:179]\n" \
"v_mfma_f32_16x16x32_bf16 v[180:183], a[212:215], a[24:27], v[180:183]\n" \
"v_mfma_f32_16x16x32_bf16 v[180:183], a[216:219], a[28:31], v[180:183]\n" \
"v_mfma_f32_16x16x32_bf16 v[184:187], a[212:215], a[40:43], v[184:187]\n" \
"v_mfma_f32_16x16x32_bf16 v[184:187], a[216:219], a[44:47], v[184:187]\n" \
"v_cmp_u_f32_e64 s[44:45], v128, v132\n" \
"v_mfma_f32_16x16x32_bf16 v[188:191], a[212:215], a[56:59], v[188:191]\n" \
"v_mfma_f32_16x16x32_bf16 v[188:191], a[216:219], a[60:63], v[188:191]\n" \
"v_cmp_u_f32_e64 s[56:57], v136, v140\n" \
"s_or_b64 s[56:57], s[56:57], s[44:45]\n" \
"s_cbranch_scc1 Lsp7_%=\n" \
"Lbk7_%=:\n" \
"s_waitcnt vmcnt(63) lgkmcnt(2)\n" \
"s_nop 0\n" \
"v_mfma_f32_16x16x32_bf16 v[144:147], a[232:235], a[128:131], v[144:147]\n" \
"ds_read_b64_tr_b16 a[212:213], v215 offset:53760\n" \
"v_mfma_f32_16x16x32_bf16 v[144:147], a[196:199], a[132:135], v[144:147]\n" \
"ds_read_b64_tr_b16 a[214:215], v215 offset:55808\n" \
"v_mfma_f32_16x16x32_bf16 v[148:151], a[232:235], a[144:147], v[148:151]\n" \
"ds_read_b64_tr_b16 a[208:209], v215 offset:57856\n" \
"v_mfma_f32_16x16x32_bf16 v[148:151], a[196:199], a[148:151], v[148:151]\n" \
"ds_read_b64_tr_b16 a[210:211], v215 offset:59904\n" \
"s_lshl_b32 s61, %[wid], 10\n" \
"v_lshrrev_b32_e32 v231, 3, v224\n" \
"v_add_u32_e64 v231, s61, v231\n" \
"s_add_u32 m0, %[m0b], 32768\n" \
"s_cmp_gt_u32 s48, 5\n" \
"s_cselect_b32 s49, 5, 6\n" \
"s_lshl_b32 s49, s49, 18\n" \
"s_or_b32 s49, s49, s59\n" \
"buffer_load_dwordx4 %[wo0], %[srdt], s49 offen offset:0 lds\n" \
"v_mfma_f32_16x16x32_bf16 v[152:155], a[232:235], a[160:163], v[152:155]\n" \
"ds_read_b64_tr_b16 a[216:217], v215 offset:61952\n" \
"v_readlane_b32 s44, %[tlo], 20\n" \
"v_readlane_b32 s45, %[thi], 20\n" \
"v_lshrrev_b32_e64 v236, 1, v231\n" \
"v_mfma_f32_16x16x32_bf16 v[152:155], a[196:199], a[164:167], v[152:155]\n" \
"ds_read_b64_tr_b16 a[218:219], v215 offset:64000\n" \
"v_mfma_f32_16x16x32_bf16 v[156:159], a[232:235], a[176:179], v[156:159]\n" \
"ds_read_b64_tr_b16 a[224:225], v227 offset:49664\n" \
"v_mfma_f32_16x16x32_bf16 v[156:159], a[196:199], a[180:183], v[156:159]\n" \
"ds_read_b64_tr_b16 a[226:227], v227 offset:51712\n" \
"v_mfma_f32_16x16x32_bf16 v[144:147], a[200:203], a[136:139], v[144:147]\n" \
"ds_read_b64_tr_b16 a[228:229], v227 offset:53760\n" \
"v_mfma_f32_16x16x32_bf16 v[144:147], a[204:207], a[140:143], v[144:147]\n" \
"ds_read_b64_tr_b16 a[230:231], v227 offset:55808\n" \
"v_mfma_f32_16x16x32_bf16 v[148:151], a[200:203], a[152:155], v[148:151]\n" \
"s_mov_b32 s61, 262144\n" \
"buffer_load_dwordx4 v[192:195], v231, %[srdz], s61 offen offset:0 sc1\n" \
"v_mfma_f32_16x16x32_bf16 v[148:151], a[204:207], a[156:159], v[148:151]\n" \
"v_mfma_f32_16x16x32_bf16 v[152:155], a[200:203], a[168:171], v[152:155]\n" \
"buffer_load_dwordx4 v212, %[srdt], s49 offen offset:1024 lds\n" \
"v_mfma_f32_16x16x32_bf16 v[152:155], a[204:207], a[172:175], v[152:155]\n" \
"v_mfma_f32_16x16x32_bf16 v[156:159], a[200:203], a[184:187], v[156:159]\n" \
"ds_read_b64_tr_b16 a[236:237], v227 offset:57856\n" \
"v_mfma_f32_16x16x32_bf16 v[156:159], a[204:207], a[188:191], v[156:159]\n" \
"s_waitcnt vmcnt(63) lgkmcnt(5)\n" \
"s_nop 0\n" \
"v_mfma_f32_16x16x32_bf16 v[160:163], a[220:223], a[128:131], v[160:163]\n" \
"ds_read_b64_tr_b16 a[238:239], v227 offset:59904\n" \
"v_mfma_f32_16x16x32_bf16 v[160:163], a[212:215], a[132:135], v[160:163]\n" \
"ds_read_b64_tr_b16 a[232:233], v227 offset:61952\n" \
"v_mfma_f32_16x16x32_bf16 v[164:167], a[220:223], a[144:147], v[164:167]\n" \
"ds_read_b64_tr_b16 a[234:235], v227 offset:64000\n" \
"s_mov_b32 s61, 266240\n" \
"buffer_load_dwordx4 v[196:199], v231, %[srdz], s61 offen offset:0 sc1\n" \
"v_mfma_f32_16x16x32_bf16 v[164:167], a[212:215], a[148:151], v[164:167]\n" \
"v_mfma_f32_16x16x32_bf16 v[168:171], a[220:223], a[160:163], v[168:171]\n" \
"v_cvt_pk_bf16_f32 v128, v128, v129\n" \
"buffer_load_dwordx4 v213, %[srdt], s49 offen offset:2048 lds\n" \
"v_mfma_f32_16x16x32_bf16 v[168:171], a[212:215], a[164:167], v[168:171]\n" \
"v_cvt_pk_bf16_f32 v129, v130, v131\n" \
"v_mfma_f32_16x16x32_bf16 v[172:175], a[220:223], a[176:179], v[172:175]\n" \
"v_cvt_pk_bf16_f32 v130, v144, v145\n" \
"v_mfma_f32_16x16x32_bf16 v[172:175], a[212:215], a[180:183], v[172:175]\n" \
"v_cvt_pk_bf16_f32 v131, v146, v147\n" \
"v_mfma_f32_16x16x32_bf16 v[160:163], a[208:211], a[136:139], v[160:163]\n" \
"s_add_u32 s55, s46, 0\n" \
"s_nop 0\n" \
"buffer_store_dwordx4 v[128:131], %[vxs], %[srdz], s55 offen offset:0 sc1\n" \
"v_mfma_f32_16x16x32_bf16 v[160:163], a[216:219], a[140:143], v[160:163]\n" \
"v_mfma_f32_16x16x32_bf16 v[164:167], a[208:211], a[152:155], v[164:167]\n" \
"v_cvt_pk_bf16_f32 v132, v132, v133\n" \
"s_mov_b32 s61, 270336\n" \
"buffer_load_dwordx4 v[200:203], v231, %[srdz], s61 offen offset:0 sc1\n" \
"v_mfma_f32_16x16x32_bf16 v[164:167], a[216:219], a[156:159], v[164:167]\n" \
"v_cvt_pk_bf16_f32 v133, v134, v135\n" \
"v_mfma_f32_16x16x32_bf16 v[168:171], a[208:211], a[168:171], v[168:171]\n" \
"s_waitcnt vmcnt(14) lgkmcnt(0)\n" \
"s_barrier\n" \
"v_mfma_f32_16x16x32_bf16 v[168:171], a[216:219], a[172:175], v[168:171]\n" \
"ds_read_b64_tr_b16 a[196:197], v215 offset:0\n" \
"v_cvt_pk_bf16_f32 v134, v148, v149\n" \
"v_mfma_f32_16x16x32_bf16 v[172:175], a[208:211], a[184:187], v[172:175]\n" \
"ds_read_b64_tr_b16 a[198:199], v215 offset:2048\n" \
"v_cvt_pk_bf16_f32 v135, v150, v151\n" \
"v_mfma_f32_16x16x32_bf16 v[172:175], a[216:219], a[188:191], v[172:175]\n" \
"s_waitcnt vmcnt(63) lgkmcnt(2)\n" \
"s_nop 0\n" \
"v_mfma_f32_16x16x32_bf16 v[176:179], a[224:227], a[128:131], v[176:179]\n" \
"ds_read_b64_tr_b16 a[200:201], v215 offset:4096\n" \
"v_cvt_pk_bf16_f32 v136, v136, v137\n" \
"v_mfma_f32_16x16x32_bf16 v[176:179], a[228:231], a[132:135], v[176:179]\n" \
"ds_read_b64_tr_b16 a[202:203], v215 offset:6144\n" \
"v_cvt_pk_bf16_f32 v137, v138, v139\n" \
".if . - adaln_body != 53248\n" \
".error \"size model\"\n" \
".endif\n" \
"Lk12_%=:\n" \
"s_cbranch_execz Lk13_%=\n" \
"s_nop 0\n" \
"v_mfma_f32_16x16x32_bf16 v[180:183], a[224:227], a[144:147], v[180:183]\n" \
"ds_read_b64_tr_b16 a[204:205], v215 offset:8192\n" \
"s_add_u32 s55, s46, 304128\n" \
"buffer_store_dwordx4 v[132:135], %[vxs], %[srdz], s55 offen offset:0 sc1\n" \
"v_mfma_f32_16x16x32_bf16 v[180:183], a[228:231], a[148:151], v[180:183]\n" \
"ds_read_b64_tr_b16 a[206:207], v215 offset:10240\n" \
"v_cvt_pk_bf16_f32 v138, v152, v153\n" \
"v_mfma_f32_16x16x32_bf16 v[184:187], a[224:227], a[160:163], v[184:187]\n" \
"ds_read_b64_tr_b16 a[220:221], v215 offset:12288\n" \
"v_cvt_pk_bf16_f32 v139, v154, v155\n" \
"s_mov_b32 s61, 274432\n" \
"buffer_load_dwordx4 v[204:207], v231, %[srdz], s61 offen offset:0 sc1\n" \
"v_mfma_f32_16x16x32_bf16 v[184:187], a[228:231], a[164:167], v[184:187]\n" \
"ds_read_b64_tr_b16 a[222:223], v215 offset:14336\n" \
"v_mfma_f32_16x16x32_bf16 v[188:191], a[224:227], a[176:179], v[188:191]\n" \
"ds_read_b64_tr_b16 a[212:213], v227 offset:0\n" \
"v_cvt_pk_bf16_f32 v140, v140, v141\n" \
"buffer_load_dwordx4 v214, %[srdt], s49 offen offset:3072 lds\n" \
"v_mfma_f32_16x16x32_bf16 v[188:191], a[228:231], a[180:183], v[188:191]\n" \
"ds_read_b64_tr_b16 a[214:215], v227 offset:2048\n" \
"v_cvt_pk_bf16_f32 v141, v142, v143\n" \
"v_mfma_f32_16x16x32_bf16 v[176:179], a[236:239], a[136:139], v[176:179]\n" \
"ds_read_b64_tr_b16 a[208:209], v227 offset:4096\n" \
"s_add_u32 s55, s46, 608256\n" \
"buffer_store_dwordx4 v[136:139], %[vxs], %[srdz], s55 offen offset:0 sc1\n" \
"v_mfma_f32_16x16x32_bf16 v[176:179], a[232:235], a[140:143], v[176:179]\n" \
"ds_read_b64_tr_b16 a[210:211], v227 offset:6144\n" \
"v_cvt_pk_bf16_f32 v142, v156, v157\n" \
"v_mfma_f32_16x16x32_bf16 v[180:183], a[236:239], a[152:155], v[180:183]\n" \
"v_cvt_pk_bf16_f32 v143, v158, v159\n" \
"v_mfma_f32_16x16x32_bf16 v[180:183], a[232:235], a[156:159], v[180:183]\n" \
"s_add_u32 s55, s46, 912384\n" \
"buffer_store_dwordx4 v[140:143], %[vxs], %[srdz], s55 offen offset:0 sc1\n" \
"v_mfma_f32_16x16x32_bf16 v[184:187], a[236:239], a[168:171], v[184:187]\n" \
"v_mfma_f32_16x16x32_bf16 v[184:187], a[232:235], a[172:175], v[184:187]\n" \
"global_load_dwordx2 v[232:233], v236, s[44:45]\n" \
"v_mfma_f32_16x16x32_bf16 v[188:191], a[236:239], a[184:187], v[188:191]\n" \
"ds_read_b64_tr_b16 a[216:217], v227 offset:8192\n" \
"v_mfma_f32_16x16x32_bf16 v[188:191], a[232:235], a[188:191], v[188:191]\n" \
"s_waitcnt vmcnt(63) lgkmcnt(5)\n" \
"s_nop 0\n" \
"v_mfma_f32_16x16x32_bf16 v[4:7], a[196:199], a[64:67], v[4:7]\n" \
"ds_read_b64_tr_b16 a[218:219], v227 offset:10240\n" \
"v_mfma_f32_16x16x32_bf16 v[4:7], a[200:203], a[68:71], v[4:7]\n" \
"ds_read_b64_tr_b16 a[224:225], v227 offset:12288\n" \
"v_mfma_f32_16x16x32_bf16 v[36:39], a[196:199], a[80:83], v[36:39]\n" \
"ds_read_b64_tr_b16 a[226:227], v227 offset:14336\n" \
"v_mfma_f32_16x16x32_bf16 v[36:39], a[200:203], a[84:87], v[36:39]\n" \
"v_mfma_f32_16x16x32_bf16 v[68:71], a[196:199], a[96:99], v[68:71]\n" \
"v_cvt_pk_bf16_f32 v160, v160, v161\n" \
"v_mfma_f32_16x16x32_bf16 v[68:71], a[200:203], a[100:103], v[68:71]\n" \
"v_cvt_pk_bf16_f32 v161, v162, v163\n" \
"v_mfma_f32_16x16x32_bf16 v[100:103], a[196:199], a[112:115], v[100:103]\n" \
"ds_read_b64_tr_b16 a[228:229], v215 offset:512\n" \
"v_cvt_pk_bf16_f32 v162, v176, v177\n" \
"v_mfma_f32_16x16x32_bf16 v[100:103], a[200:203], a[116:119], v[100:103]\n" \
"ds_read_b64_tr_b16 a[230:231], v215 offset:2560\n" \
"v_cvt_pk_bf16_f32 v163, v178, v179\n" \
"v_mfma_f32_16x16x32_bf16 v[4:7], a[204:207], a[72:75], v[4:7]\n" \
"ds_read_b64_tr_b16 a[236:237], v215 offset:4608\n" \
"v_mfma_f32_16x16x32_bf16 v[4:7], a[220:223], a[76:79], v[4:7]\n" \
"ds_read_b64_tr_b16 a[238:239], v215 offset:6656\n" \
"v_mfma_f32_16x16x32_bf16 v[36:39], a[204:207], a[88:91], v[36:39]\n" \
"s_add_u32 s55, s46, 4096\n" \
"buffer_store_dwordx4 v[160:163], %[vxs], %[srdz], s55 offen offset:0 sc1\n" \
"v_mfma_f32_16x16x32_bf16 v[36:39], a[220:223], a[92:95], v[36:39]\n" \
"v_cvt_pk_bf16_f32 v164, v164, v165\n" \
"v_cvt_pk_bf16_f32 v165, v166, v167\n" \
"v_mfma_f32_16x16x32_bf16 v[68:71], a[204:207], a[104:107], v[68:71]\n" \
"v_cvt_pk_bf16_f32 v166, v180, v181\n" \
"s_add_u32 m0, %[m0b], 49152\n" \
"s_cmp_gt_u32 s48, 6\n" \
"s_cselect_b32 s49, 6, 7\n" \
"s_lshl_b32 s49, s49, 18\n" \
"s_or_b32 s49, s49, s59\n" \
"buffer_load_dwordx4 %[wo0], %[srdt], s49 offen offset:0 lds\n" \
"v_mfma_f32_16x16x32_bf16 v[68:71], a[220:223], a[108:111], v[68:71]\n" \
"v_cvt_pk_bf16_f32 v167, v182, v183\n" \
"v_mfma_f32_16x16x32_bf16 v[100:103], a[204:207], a[120:123], v[100:103]\n" \
"ds_read_b64_tr_b16 a[232:233], v215 offset:8704\n" \
"v_mfma_f32_16x16x32_bf16 v[100:103], a[220:223], a[124:127], v[100:103]\n" \
"s_waitcnt vmcnt(63) lgkmcnt(5)\n" \
"s_nop 0\n" \
"v_mfma_f32_16x16x32_bf16 v[12:15], a[212:215], a[64:67], v[12:15]\n" \
"ds_read_b64_tr_b16 a[234:235], v215 offset:10752\n" \
"v_cvt_pk_bf16_f32 v168, v168, v169\n" \
"v_mfma_f32_16x16x32_bf16 v[12:15], a[208:211], a[68:71], v[12:15]\n" \
"ds_read_b64_tr_b16 a[196:197], v215 offset:12800\n" \
"s_add_u32 s55, s46, 308224\n" \
"buffer_store_dwordx4 v[164:167], %[vxs], %[srdz], s55 offen offset:0 sc1\n" \
"v_mfma_f32_16x16x32_bf16 v[44:47], a[212:215], a[80:83], v[44:47]\n" \
"ds_read_b64_tr_b16 a[198:199], v215 offset:14848\n" \
"v_cvt_pk_bf16_f32 v169, v170, v171\n" \
"v_mfma_f32_16x16x32_bf16 v[44:47], a[208:211], a[84:87], v[44:47]\n" \
"v_cvt_pk_bf16_f32 v170, v184, v185\n" \
"v_cvt_pk_bf16_f32 v171, v186, v187\n" \
"v_mfma_f32_16x16x32_bf16 v[76:79], a[212:215], a[96:99], v[76:79]\n" \
"s_add_u32 s55, s46, 612352\n" \
"buffer_store_dwordx4 v[168:171], %[vxs], %[srdz], s55 offen offset:0 sc1\n" \
"v_mfma_f32_16x16x32_bf16 v[76:79], a[208:211], a[100:103], v[76:79]\n" \
"v_mfma_f32_16x16x32_bf16 v[108:111], a[212:215], a[112:115], v[108:111]\n" \
"ds_read_b64_tr_b16 a[200:201], v227 offset:512\n" \
"v_cvt_pk_bf16_f32 v172, v172, v173\n" \
"buffer_load_dwordx4 v212, %[srdt], s49 offen offset:1024 lds\n" \
"v_mfma_f32_16x16x32_bf16 v[108:111], a[208:211], a[116:119], v[108:111]\n" \
"ds_read_b64_tr_b16 a[202:203], v227 offset:2560\n" \
"v_cvt_pk_bf16_f32 v173, v174, v175\n" \
"v_mfma_f32_16x16x32_bf16 v[12:15], a[216:219], a[72:75], v[12:15]\n" \
"ds_read_b64_tr_b16 a[204:205], v227 offset:4608\n" \
"v_cvt_pk_bf16_f32 v174, v188, v189\n" \
"v_mfma_f32_16x16x32_bf16 v[12:15], a[224:227], a[76:79], v[12:15]\n" \
"ds_read_b64_tr_b16 a[206:207], v227 offset:6656\n" \
"v_cvt_pk_bf16_f32 v175, v190, v191\n" \
"v_mfma_f32_16x16x32_bf16 v[44:47], a[216:219], a[88:91], v[44:47]\n" \
"s_add_u32 s55, s46, 916480\n" \
"buffer_store_dwordx4 v[172:175], %[vxs], %[srdz], s55 offen offset:0 sc1\n" \
"v_mfma_f32_16x16x32_bf16 v[44:47], a[224:227], a[92:95], v[44:47]\n" \
"v_mfma_f32_16x16x32_bf16 v[76:79], a[216:219], a[104:107], v[76:79]\n" \
"buffer_load_dwordx4 v213, %[srdt], s49 offen offset:2048 lds\n" \
"v_mfma_f32_16x16x32_bf16 v[76:79], a[224:227], a[108:111], v[76:79]\n" \
"v_mfma_f32_16x16x32_bf16 v[108:111], a[216:219], a[120:123], v[108:111]\n" \
"ds_read_b64_tr_b16 a[220:221], v227 offset:8704\n" \
"v_accvgpr_read_b32 v129, a240\n" \
"v_mfma_f32_16x16x32_bf16 v[108:111], a[224:227], a[124:127], v[108:111]\n" \
"s_waitcnt vmcnt(63) lgkmcnt(5)\n" \
"s_nop 0\n" \
"v_mfma_f32_16x16x32_bf16 v[20:23], a[228:231], a[64:67], v[20:23]\n" \
"ds_read_b64_tr_b16 a[222:223], v227 offset:10752\n" \
"v_lshlrev_b32_e64 v128, 16, v129\n" \
"v_mfma_f32_16x16x32_bf16 v[20:23], a[236:239], a[68:71], v[20:23]\n" \
"ds_read_b64_tr_b16 a[212:213], v227 offset:12800\n" \
"v_and_b32_e32 v129, 0xffff0000, v129\n" \
"v_mfma_f32_16x16x32_bf16 v[52:55], a[228:231], a[80:83], v[52:55]\n" \
"ds_read_b64_tr_b16 a[214:215], v227 offset:14848\n" \
"v_accvgpr_read_b32 v131, a241\n" \
"v_mfma_f32_16x16x32_bf16 v[52:55], a[236:239], a[84:87], v[52:55]\n" \
"v_lshlrev_b32_e64 v130, 16, v131\n" \
"v_and_b32_e32 v131, 0xffff0000, v131\n" \
"global_load_dword v237, %[vct], %[pct]\n" \
"v_mfma_f32_16x16x32_bf16 v[84:87], a[228:231], a[96:99], v[84:87]\n" \
"v_accvgpr_read_b32 v133, a242\n" \
"v_mfma_f32_16x16x32_bf16 v[84:87], a[236:239], a[100:103], v[84:87]\n" \
"v_lshlrev_b32_e64 v132, 16, v133\n" \
"s_add_u32 m0, %[m0b], 114688\n" \
"s_mov_b32 s61, 16384\n" \
"buffer_load_dwordx4 %[vdn], s[64:67], s61 offen offset:0 lds\n" \
"v_mfma_f32_16x16x32_bf16 v[116:119], a[228:231], a[112:115], v[116:119]\n" \
"v_and_b32_e32 v133, 0xffff0000, v133\n" \
"v_mfma_f32_16x16x32_bf16 v[116:119], a[236:239], a[116:119], v[116:119]\n" \
"v_accvgpr_read_b32 v135, a243\n" \
"s_add_u32 m0, %[m0b], 49152\n" \
"s_nop 0\n" \
"s_nop 0\n" \
"buffer_load_dwordx4 v214, %[srdt], s49 offen offset:3072 lds\n" \
"v_mfma_f32_16x16x32_bf16 v[20:23], a[232:235], a[72:75], v[20:23]\n" \
"v_lshlrev_b32_e64 v134, 16, v135\n" \
"v_mfma_f32_16x16x32_bf16 v[20:23], a[196:199], a[76:79], v[20:23]\n" \
"v_and_b32_e32 v135, 0xffff0000, v135\n" \
"v_mfma_f32_16x16x32_bf16 v[52:55], a[232:235], a[88:91], v[52:55]\n" \
"v_accvgpr_read_b32 v137, a244\n" \
"v_mfma_f32_16x16x32_bf16 v[52:55], a[196:199], a[92:95], v[52:55]\n" \
"v_lshlrev_b32_e64 v136, 16, v137\n" \
"v_mfma_f32_16x16x32_bf16 v[84:87], a[232:235], a[104:107], v[84:87]\n" \
"s_waitcnt vmcnt(23) lgkmcnt(0)\n" \
"s_barrier\n" \
"v_mfma_f32_16x16x32_bf16 v[84:87], a[196:199], a[108:111], v[84:87]\n" \
"ds_read_b64_tr_b16 a[208:209], v215 offset:16384\n" \
"v_and_b32_e32 v137, 0xffff0000, v137\n" \
"v_mfma_f32_16x16x32_bf16 v[116:119], a[232:235], a[120:123], v[116:119]\n" \
"ds_read_b64_tr_b16 a[210:211], v215 offset:18432\n" \
"v_accvgpr_read_b32 v139, a245\n" \
"v_mfma_f32_16x16x32_bf16 v[116:119], a[196:199], a[124:127], v[116:119]\n" \
"s_waitcnt vmcnt(63) lgkmcnt(2)\n" \
"s_nop 0\n" \
"v_mfma_f32_16x16x32_bf16 v[28:31], a[200:203], a[64:67], v[28:31]\n" \
"ds_read_b64_tr_b16 a[216:217], v215 offset:20480\n" \
"v_lshlrev_b32_e64 v138, 16, v139\n" \
"v_mfma_f32_16x16x32_bf16 v[28:31], a[204:207], a[68:71], v[28:31]\n" \
"ds_read_b64_tr_b16 a[218:219], v215 offset:22528\n" \
"v_and_b32_e32 v139, 0xffff0000, v139\n" \
"v_mfma_f32_16x16x32_bf16 v[60:63], a[200:203], a[80:83], v[60:63]\n" \
"ds_read_b64_tr_b16 a[224:225], v215 offset:24576\n" \
"v_accvgpr_read_b32 v141, a246\n" \
"v_mfma_f32_16x16x32_bf16 v[60:63], a[204:207], a[84:87], v[60:63]\n" \
"ds_read_b64_tr_b16 a[226:227], v215 offset:26624\n" \
"v_lshlrev_b32_e64 v140, 16, v141\n" \
"v_mfma_f32_16x16x32_bf16 v[92:95], a[200:203], a[96:99], v[92:95]\n" \
"ds_read_b64_tr_b16 a[228:229], v215 offset:28672\n" \
"v_and_b32_e32 v141, 0xffff0000, v141\n" \
"s_add_u32 m0, %[m0b], 115712\n" \
"s_mov_b32 s61, 20480\n" \
"buffer_load_dwordx4 %[vdn], s[64:67], s61 offen offset:0 lds\n" \
"v_mfma_f32_16x16x32_bf16 v[92:95], a[204:207], a[100:103], v[92:95]\n" \
"ds_read_b64_tr_b16 a[230:231], v215 offset:30720\n" \
"v_accvgpr_read_b32 v143, a247\n" \
"v_mfma_f32_16x16x32_bf16 v[124:127], a[200:203], a[112:115], v[124:127]\n" \
"ds_read_b64_tr_b16 a[236:237], v227 offset:16384\n" \
"v_lshlrev_b32_e64 v142, 16, v143\n" \
"v_mfma_f32_16x16x32_bf16 v[124:127], a[204:207], a[116:119], v[124:127]\n" \
"ds_read_b64_tr_b16 a[238:239], v227 offset:18432\n" \
"v_and_b32_e32 v143, 0xffff0000, v143\n" \
"v_mfma_f32_16x16x32_bf16 v[28:31], a[220:223], a[72:75], v[28:31]\n" \
"ds_read_b64_tr_b16 a[232:233], v227 offset:20480\n" \
"v_accvgpr_read_b32 v145, a248\n" \
"v_mfma_f32_16x16x32_bf16 v[28:31], a[212:215], a[76:79], v[28:31]\n" \
"ds_read_b64_tr_b16 a[234:235], v227 offset:22528\n" \
"v_lshlrev_b32_e64 v144, 16, v145\n" \
"v_mfma_f32_16x16x32_bf16 v[60:63], a[220:223], a[88:91], v[60:63]\n" \
"v_and_b32_e32 v145, 0xffff0000, v145\n" \
"v_accvgpr_read_b32 v147, a249\n" \
"v_mfma_f32_16x16x32_bf16 v[60:63], a[212:215], a[92:95], v[60:63]\n" \
"v_lshlrev_b32_e64 v146, 16, v147\n" \
"v_and_b32_e32 v147, 0xffff0000, v147\n" \
"v_mfma_f32_16x16x32_bf16 v[92:95], a[220:223], a[104:107], v[92:95]\n" \
"v_accvgpr_read_b32 v149, a250\n" \
"v_mfma_f32_16x16x32_bf16 v[92:95], a[212:215], a[108:111], v[92:95]\n" \
"v_lshlrev_b32_e64 v148, 16, v149\n" \
"v_mfma_f32_16x16x32_bf16 v[124:127], a[220:223], a[120:123], v[124:127]\n" \
"ds_read_b64_tr_b16 a[196:197], v227 offset:24576\n" \
"v_and_b32_e32 v149, 0xffff0000, v149\n" \
"v_mfma_f32_16x16x32_bf16 v[124:127], a[212:215], a[124:127], v[124:127]\n" \
"s_waitcnt vmcnt(63) lgkmcnt(5)\n" \
"s_nop 0\n" \
"v_mfma_f32_16x16x32_bf16 v[4:7], a[208:211], v[0:3], v[4:7]\n" \
"ds_read_b64_tr_b16 a[198:199], v227 offset:26624\n" \
"v_accvgpr_read_b32 v151, a251\n" \
"v_mfma_f32_16x16x32_bf16 v[4:7], a[216:219], v[8:11], v[4:7]\n" \
"ds_read_b64_tr_b16 a[200:201], v227 offset:28672\n" \
"v_lshlrev_b32_e64 v150, 16, v151\n" \
"v_mfma_f32_16x16x32_bf16 v[36:39], a[208:211], v[32:35], v[36:39]\n" \
"ds_read_b64_tr_b16 a[202:203], v227 offset:30720\n" \
"v_and_b32_e32 v151, 0xffff0000, v151\n" \
"v_mfma_f32_16x16x32_bf16 v[36:39], a[216:219], v[40:43], v[36:39]\n" \
"v_accvgpr_read_b32 v153, a252\n" \
"v_lshlrev_b32_e64 v152, 16, v153\n" \
"s_add_u32 m0, %[m0b], 116736\n" \
"s_mov_b32 s61, 24576\n" \
"buffer_load_dwordx4 %[vdn], s[64:67], s61 offen offset:0 lds\n" \
"v_mfma_f32_16x16x32_bf16 v[68:71], a[208:211], v[64:67], v[68:71]\n" \
"v_and_b32_e32 v153, 0xffff0000, v153\n" \
"v_mfma_f32_16x16x32_bf16 v[68:71], a[216:219], v[72:75], v[68:71]\n" \
"v_accvgpr_read_b32 v155, a253\n" \
"v_mfma_f32_16x16x32_bf16 v[100:103], a[208:211], v[96:99], v[100:103]\n" \
"v_lshlrev_b32_e64 v154, 16, v155\n" \
"v_mfma_f32_16x16x32_bf16 v[100:103], a[216:219], v[104:107], v[100:103]\n" \
"v_and_b32_e32 v155, 0xffff0000, v155\n" \
"v_mfma_f32_16x16x32_bf16 v[4:7], a[224:227], v[16:19], v[4:7]\n" \
"v_accvgpr_read_b32 v157, a254\n" \
"v_mfma_f32_16x16x32_bf16 v[4:7], a[228:231], v[24:27], v[4:7]\n" \
"v_lshlrev_b32_e64 v156, 16, v157\n" \
"v_mfma_f32_16x16x32_bf16 v[36:39], a[224:227], v[48:51], v[36:39]\n" \
"v_and_b32_e32 v157, 0xffff0000, v157\n" \
"v_mfma_f32_16x16x32_bf16 v[36:39], a[228:231], v[56:59], v[36:39]\n" \
"v_accvgpr_read_b32 v159, a255\n" \
"v_mfma_f32_16x16x32_bf16 v[68:71], a[224:227], v[80:83], v[68:71]\n" \
"s_waitcnt vmcnt(15) lgkmcnt(15)\n" \
"s_barrier\n" \
"v_mfma_f32_16x16x32_bf16 v[68:71], a[228:231], v[88:91], v[68:71]\n" \
"ds_read_b64_tr_b16 a[204:205], v215 offset:32768\n" \
"v_lshlrev_b32_e64 v158, 16, v159\n" \
"v_mfma_f32_16x16x32_bf16 v[100:103], a[224:227], v[112:115], v[100:103]\n" \
"ds_read_b64_tr_b16 a[206:207], v215 offset:34816\n" \
"v_and_b32_e32 v159, 0xffff0000, v159\n" \
"v_mfma_f32_16x16x32_bf16 v[100:103], a[228:231], v[120:123], v[100:103]\n" \
"s_waitcnt vmcnt(63) lgkmcnt(2)\n" \
"s_nop 0\n" \
"v_mfma_f32_16x16x32_bf16 v[12:15], a[236:239], v[0:3], v[12:15]\n" \
"ds_read_b64_tr_b16 a[220:221], v215 offset:36864\n" \
"v_and_b32_e32 v208, 0x7ff, %[vx0]\n" \
"v_mfma_f32_16x16x32_bf16 v[12:15], a[232:235], v[8:11], v[12:15]\n" \
"ds_read_b64_tr_b16 a[222:223], v215 offset:38912\n" \
"v_lshlrev_b32_e64 v208, 1, v208\n" \
"v_mfma_f32_16x16x32_bf16 v[44:47], a[236:239], v[32:35], v[44:47]\n" \
"ds_read_b64_tr_b16 a[212:213], v215 offset:40960\n" \
"v_xor_b32_e32 v209, 0x100, v208\n" \
"v_mfma_f32_16x16x32_bf16 v[44:47], a[232:235], v[40:43], v[44:47]\n" \
"ds_read_b64_tr_b16 a[214:215], v215 offset:43008\n" \
"v_lshrrev_b32_e64 v210, 3, v224\n" \
"v_mfma_f32_16x16x32_bf16 v[76:79], a[236:239], v[64:67], v[76:79]\n" \
"ds_read_b64_tr_b16 a[208:209], v215 offset:45056\n" \
"v_and_b32_e64 v210, 48, v210\n" \
"v_mfma_f32_16x16x32_bf16 v[76:79], a[232:235], v[72:75], v[76:79]\n" \
"ds_read_b64_tr_b16 a[210:211], v215 offset:47104\n" \
"s_lshl_b32 s61, %[wid], 12\n" \
"s_sub_u32 s61, %[m0b], s61\n" \
"v_add_u32_e32 v208, s61, v208\n" \
"v_add_u32_e32 v209, s61, v209\n" \
"v_add_u32_e64 v210, s61, v210\n" \
"v_mfma_f32_16x16x32_bf16 v[108:111], a[236:239], v[96:99], v[108:111]\n" \
"ds_read_b64_tr_b16 a[216:217], v227 offset:32768\n" \
"v_mfma_f32_16x16x32_bf16 v[108:111], a[232:235], v[104:107], v[108:111]\n" \
"ds_read_b64_tr_b16 a[218:219], v227 offset:34816\n" \
"s_waitcnt vmcnt(12)\n" \
"s_nop 0\n" \
"v_and_or_b32 v236, v192, v193, 0\n" \
"s_add_u32 m0, %[m0b], 117760\n" \
"s_mov_b32 s61, 28672\n" \
"buffer_load_dwordx4 %[vdn], s[64:67], s61 offen offset:0 lds\n" \
"v_mfma_f32_16x16x32_bf16 v[12:15], a[196:199], v[16:19], v[12:15]\n" \
"ds_read_b64_tr_b16 a[224:225], v227 offset:36864\n" \
"v_bitop3_b32 v236, v236, v194, v195 bitop3:0x80\n" \
"v_mfma_f32_16x16x32_bf16 v[12:15], a[200:203], v[24:27], v[12:15]\n" \
"ds_read_b64_tr_b16 a[226:227], v227 offset:38912\n" \
"v_bitop3_b32 v236, v236, v196, v197 bitop3:0x80\n" \
"v_mfma_f32_16x16x32_bf16 v[44:47], a[196:199], v[48:51], v[44:47]\n" \
"v_bitop3_b32 v236, v236, v198, v199 bitop3:0x80\n" \
"v_mfma_f32_16x16x32_bf16 v[44:47], a[200:203], v[56:59], v[44:47]\n" \
"v_bitop3_b32 v236, v236, v200, v201 bitop3:0x80\n" \
"v_mfma_f32_16x16x32_bf16 v[76:79], a[196:199], v[80:83], v[76:79]\n" \
"v_bitop3_b32 v236, v236, v202, v203 bitop3:0x80\n" \
"v_mfma_f32_16x16x32_bf16 v[76:79], a[200:203], v[88:91], v[76:79]\n" \
"v_bitop3_b32 v236, v236, v204, v205 bitop3:0x80\n" \
"v_mfma_f32_16x16x32_bf16 v[108:111], a[196:199], v[112:115], v[108:111]\n" \
"ds_read_b64_tr_b16 a[228:229], v227 offset:40960\n" \
"v_bitop3_b32 v236, v236, v206, v207 bitop3:0x80\n" \
"v_mfma_f32_16x16x32_bf16 v[108:111], a[200:203], v[120:123], v[108:111]\n" \
"s_waitcnt vmcnt(63) lgkmcnt(5)\n" \
"s_nop 0\n" \
"v_mfma_f32_16x16x32_bf16 v[4:7], a[204:207], a[0:3], v[4:7]\n" \
"ds_read_b64_tr_b16 a[230:231], v227 offset:43008\n" \
"s_mov_b32 s54, 0x07060302\n" \
"v_and_b32_e64 v236, 1, v236\n" \
"v_mfma_f32_16x16x32_bf16 v[4:7], a[220:223], a[4:7], v[4:7]\n" \
"ds_read_b64_tr_b16 a[236:237], v227 offset:45056\n" \
"v_lshrrev_b32_e64 v218, 1, v231\n" \
"v_mfma_f32_16x16x32_bf16 v[36:39], a[204:207], a[16:19], v[36:39]\n" \
"ds_read_b64_tr_b16 a[238:239], v227 offset:47104\n" \
"v_cmp_eq_u32_e64 s[56:57], 0, v236\n" \
"v_mfma_f32_16x16x32_bf16 v[36:39], a[220:223], a[20:23], v[36:39]\n" \
"s_waitcnt vmcnt(7)\n" \
"v_mov_b32_e32 v230, 1\n" \
"buffer_store_dword v230, v225, %[srdz], s62 offen offset:68 sc1\n" \
"v_mfma_f32_16x16x32_bf16 v[68:71], a[204:207], a[32:35], v[68:71]\n" \
"s_lshl_b32 s61, %[wid], 12\n" \
"s_sub_u32 s61, %[m0b], s61\n" \
"v_add_u32_e32 v231, s61, v231\n" \
"v_add_u32_e32 v218, s61, v218\n" \
"v_mov_b32_e64 v219, s61\n" \
"ds_write_b128 v231, v[192:195] offset:0\n" \
"v_mfma_f32_16x16x32_bf16 v[68:71], a[220:223], a[36:39], v[68:71]\n" \
"v_lshlrev_b32_e64 v234, 16, v233\n" \
"v_and_b32_e32 v235, 0xffff0000, v233\n" \
"v_mfma_f32_16x16x32_bf16 v[100:103], a[204:207], a[48:51], v[100:103]\n" \
"v_and_b32_e32 v233, 0xffff0000, v232\n" \
"v_mfma_f32_16x16x32_bf16 v[100:103], a[220:223], a[52:55], v[100:103]\n" \
"v_lshlrev_b32_e64 v232, 16, v232\n" \
"v_mfma_f32_16x16x32_bf16 v[4:7], a[212:215], a[8:11], v[4:7]\n" \
"v_mul_f32_e64 v234, v202, v234\n" \
"v_mfma_f32_16x16x32_bf16 v[4:7], a[208:211], a[12:15], v[4:7]\n" \
"v_mul_f32_e64 v235, v203, v235\n" \
"v_mfma_f32_16x16x32_bf16 v[36:39], a[212:215], a[24:27], v[36:39]\n" \
"v_mul_f32_e64 v233, v201, v233\n" \
"v_mfma_f32_16x16x32_bf16 v[36:39], a[208:211], a[28:31], v[36:39]\n" \
"v_mul_f32_e64 v232, v200, v232\n" \
"v_mfma_f32_16x16x32_bf16 v[68:71], a[212:215], a[40:43], v[68:71]\n" \
"s_waitcnt vmcnt(4) lgkmcnt(15)\n" \
"s_barrier\n" \
"v_mfma_f32_16x16x32_bf16 v[68:71], a[208:211], a[44:47], v[68:71]\n" \
"ds_read_b64_tr_b16 a[232:233], v215 offset:49152\n" \
"v_add_f32_e64 v223, v234, v235\n" \
"v_mfma_f32_16x16x32_bf16 v[100:103], a[212:215], a[56:59], v[100:103]\n" \
"ds_read_b64_tr_b16 a[234:235], v215 offset:51200\n" \
"v_add_f32_e64 v222, v232, v233\n" \
"v_mfma_f32_16x16x32_bf16 v[100:103], a[208:211], a[60:63], v[100:103]\n" \
"s_waitcnt vmcnt(63) lgkmcnt(3)\n" \
"s_nop 0\n" \
"v_mfma_f32_16x16x32_bf16 v[12:15], a[216:219], a[0:3], v[12:15]\n" \
"ds_read_b64_tr_b16 a[196:197], v215 offset:53248\n" \
"ds_write_b128 v231, v[232:235] offset:4096\n" \
"v_mfma_f32_16x16x32_bf16 v[12:15], a[224:227], a[4:7], v[12:15]\n" \
"ds_read_b64_tr_b16 a[198:199], v215 offset:55296\n" \
"v_add_f32_e64 v222, v222, v223\n" \
"v_mfma_f32_16x16x32_bf16 v[44:47], a[216:219], a[16:19], v[44:47]\n" \
"ds_read_b64_tr_b16 a[200:201], v215 offset:57344\n" \
"v_perm_b32 v196, v197, v196, s54\n" \
"v_mfma_f32_16x16x32_bf16 v[44:47], a[224:227], a[20:23], v[44:47]\n" \
"ds_read_b64_tr_b16 a[202:203], v215 offset:59392\n" \
"v_add_f32_dpp v222, v222, v222 quad_perm:[1,0,3,2] row_mask:0xf bank_mask:0xf\n" \
"v_mfma_f32_16x16x32_bf16 v[76:79], a[216:219], a[32:35], v[76:79]\n" \
"ds_read_b64_tr_b16 a[204:205], v215 offset:61440\n" \
"v_perm_b32 v197, v199, v198, s54\n" \
"v_mfma_f32_16x16x32_bf16 v[76:79], a[224:227], a[36:39], v[76:79]\n" \
"ds_read_b64_tr_b16 a[206:207], v215 offset:63488\n" \
"v_add_f32_dpp v222, v222, v222 quad_perm:[2,3,0,1] row_mask:0xf bank_mask:0xf\n" \
"v_mfma_f32_16x16x32_bf16 v[108:111], a[216:219], a[48:51], v[108:111]\n" \
"ds_read_b64_tr_b16 a[220:221], v227 offset:49152\n" \
"ds_write_b64 v218, v[196:197] offset:8192\n" \
"v_mfma_f32_16x16x32_bf16 v[108:111], a[224:227], a[52:55], v[108:111]\n" \
"ds_read_b64_tr_b16 a[222:223], v227 offset:51200\n" \
"v_add_f32_dpp v222, v222, v222 row_ror:4 row_mask:0xf bank_mask:0xf\n" \
"v_mfma_f32_16x16x32_bf16 v[12:15], a[228:231], a[8:11], v[12:15]\n" \
"ds_read_b64_tr_b16 a[212:213], v227 offset:53248\n" \
"v_perm_b32 v200, v201, v200, s54\n" \
"v_mfma_f32_16x16x32_bf16 v[12:15], a[236:239], a[12:15], v[12:15]\n" \
"ds_read_b64_tr_b16 a[214:215], v227 offset:55296\n" \
"v_add_f32_dpp v222, v222, v222 row_ror:8 row_mask:0xf bank_mask:0xf\n" \
"v_mfma_f32_16x16x32_bf16 v[44:47], a[228:231], a[24:27], v[44:47]\n" \
"v_perm_b32 v201, v203, v202, s54\n" \
"v_mov_b32_e64 v223, v222\n" \
"v_mfma_f32_16x16x32_bf16 v[44:47], a[236:239], a[28:31], v[44:47]\n" \
"ds_write_b64 v218, v[200:201] offset:10240\n" \
"s_nop 0\n" \
"v_permlane16_swap_b32_e32 v222, v223\n" \
"v_mfma_f32_16x16x32_bf16 v[76:79], a[228:231], a[40:43], v[76:79]\n" \
"v_perm_b32 v204, v205, v204, s54\n" \
"v_mfma_f32_16x16x32_bf16 v[76:79], a[236:239], a[44:47], v[76:79]\n" \
"v_add_f32_e64 v222, v222, v223\n" \
"v_mfma_f32_16x16x32_bf16 v[108:111], a[228:231], a[56:59], v[108:111]\n" \
"ds_read_b64_tr_b16 a[208:209], v227 offset:57344\n" \
"v_perm_b32 v205, v207, v206, s54\n" \
"v_mfma_f32_16x16x32_bf16 v[108:111], a[236:239], a[60:63], v[108:111]\n" \
"s_waitcnt vmcnt(63) lgkmcnt(7)\n" \
"s_nop 0\n" \
"v_mfma_f32_16x16x32_bf16 v[4:7], a[232:235], a[128:131], v[4:7]\n" \
"ds_read_b64_tr_b16 a[210:211], v227 offset:59392\n" \
"v_mov_b32_e64 v223, v222\n" \
"v_mfma_f32_16x16x32_bf16 v[4:7], a[196:199], a[132:135], v[4:7]\n" \
"ds_read_b64_tr_b16 a[216:217], v227 offset:61440\n" \
"ds_write_b64 v218, v[204:205] offset:14336\n" \
"v_mfma_f32_16x16x32_bf16 v[36:39], a[232:235], a[144:147], v[36:39]\n" \
"ds_read_b64_tr_b16 a[218:219], v227 offset:63488\n" \
"s_nop 0\n" \
"v_permlane32_swap_b32_e32 v222, v223\n" \
"v_mfma_f32_16x16x32_bf16 v[36:39], a[196:199], a[148:151], v[36:39]\n" \
"v_lshl_add_u32 v219, %[wid], 4, v219\n" \
"v_add_f32_e64 v222, v222, v223\n" \
"v_mfma_f32_16x16x32_bf16 v[68:71], a[232:235], a[160:163], v[68:71]\n" \
"s_cmp_lg_u64 s[56:57], 0\n" \
"s_cselect_b32 s61, 1, 0\n" \
"v_mov_b32_e64 v192, s61\n" \
"v_mfma_f32_16x16x32_bf16 v[68:71], a[196:199], a[164:167], v[68:71]\n" \
"v_mov_b32_e64 v193, v192\n" \
"v_mfma_f32_16x16x32_bf16 v[100:103], a[232:235], a[176:179], v[100:103]\n" \
"ds_read_b64_tr_b16 a[224:225], v215 offset:16896\n" \
"ds_write_b32 v219, v222 offset:12424\n" \
"v_mfma_f32_16x16x32_bf16 v[100:103], a[196:199], a[180:183], v[100:103]\n" \
"ds_read_b64_tr_b16 a[226:227], v215 offset:18944\n" \
"ds_write_b64 v219, v[192:193] offset:12416\n" \
"v_mfma_f32_16x16x32_bf16 v[4:7], a[200:203], a[136:139], v[4:7]\n" \
"ds_read_b64_tr_b16 a[228:229], v215 offset:20992\n" \
"v_mfma_f32_16x16x32_bf16 v[4:7], a[204:207], a[140:143], v[4:7]\n" \
"ds_read_b64_tr_b16 a[230:231], v215 offset:23040\n" \
"v_mfma_f32_16x16x32_bf16 v[36:39], a[200:203], a[152:155], v[36:39]\n" \
"v_mfma_f32_16x16x32_bf16 v[36:39], a[204:207], a[156:159], v[36:39]\n" \
"v_mfma_f32_16x16x32_bf16 v[68:71], a[200:203], a[168:171], v[68:71]\n" \
"v_mfma_f32_16x16x32_bf16 v[68:71], a[204:207], a[172:175], v[68:71]\n" \
"v_mfma_f32_16x16x32_bf16 v[100:103], a[200:203], a[184:187], v[100:103]\n" \
"ds_read_b64_tr_b16 a[236:237], v215 offset:25088\n" \
"v_mfma_f32_16x16x32_bf16 v[100:103], a[204:207], a[188:191], v[100:103]\n" \
"s_waitcnt vmcnt(63) lgkmcnt(3)\n" \
"s_barrier\n" \
"v_mfma_f32_16x16x32_bf16 v[12:15], a[220:223], a[128:131], v[12:15]\n" \
"ds_read_b64_tr_b16 a[238:239], v215 offset:27136\n" \
"ds_read_b128 v[204:207], v210 offset:12416\n" \
"v_mfma_f32_16x16x32_bf16 v[12:15], a[212:215], a[132:135], v[12:15]\n" \
"ds_read_b64_tr_b16 a[232:233], v215 offset:29184\n" \
"v_lshrrev_b32_e64 v200, 3, v224\n" \
"v_mfma_f32_16x16x32_bf16 v[44:47], a[220:223], a[144:147], v[44:47]\n" \
"ds_read_b64_tr_b16 a[234:235], v215 offset:31232\n" \
"ds_read_b128 v[160:163], v208 offset:0\n" \
"v_mfma_f32_16x16x32_bf16 v[44:47], a[212:215], a[148:151], v[44:47]\n" \
"v_bfe_u32 v201, v224, 8, 3\n" \
"v_and_b32_e64 v200, 16, v200\n" \
"v_mfma_f32_16x16x32_bf16 v[76:79], a[220:223], a[160:163], v[76:79]\n" \
"ds_read_b128 v[164:167], v208 offset:2048\n" \
"v_lshlrev_b32_e64 v201, v201, 1\n" \
"v_mfma_f32_16x16x32_bf16 v[76:79], a[212:215], a[164:167], v[76:79]\n" \
"s_movk_i32 s61, 0x3f80\n" \
"s_nop 0\n" \
"v_lshlrev_b32_e64 v200, v200, s61\n" \
"v_readlane_b32 s68, %[thi], 18\n" \
"v_lshlrev_b32_e64 v226, 7, v226\n" \
"v_mfma_f32_16x16x32_bf16 v[108:111], a[220:223], a[176:179], v[108:111]\n" \
"ds_read_b64_tr_b16 a[196:197], v227 offset:16896\n" \
"ds_read_b128 v[168:171], v208 offset:128\n" \
"v_mfma_f32_16x16x32_bf16 v[108:111], a[212:215], a[180:183], v[108:111]\n" \
"ds_read_b64_tr_b16 a[198:199], v227 offset:18944\n" \
"v_bfe_i32 v192, v201, 0, 1\n" \
"v_mfma_f32_16x16x32_bf16 v[12:15], a[208:211], a[136:139], v[12:15]\n" \
"ds_read_b64_tr_b16 a[200:201], v227 offset:20992\n" \
"ds_read_b128 v[172:175], v208 offset:2176\n" \
"v_mfma_f32_16x16x32_bf16 v[12:15], a[216:219], a[140:143], v[12:15]\n" \
"ds_read_b64_tr_b16 a[202:203], v227 offset:23040\n" \
"v_and_b32_e64 v192, v200, v192\n" \
"v_mfma_f32_16x16x32_bf16 v[44:47], a[208:211], a[152:155], v[44:47]\n" \
"ds_read_b128 v[176:179], v209 offset:0\n" \
"v_bfe_i32 v193, v201, 1, 1\n" \
"v_mfma_f32_16x16x32_bf16 v[44:47], a[216:219], a[156:159], v[44:47]\n" \
"v_and_b32_e64 v193, v200, v193\n" \
"v_bfe_i32 v194, v201, 2, 1\n" \
"v_mfma_f32_16x16x32_bf16 v[76:79], a[208:211], a[168:171], v[76:79]\n" \
"ds_read_b128 v[180:183], v209 offset:2048\n" \
"v_and_b32_e64 v194, v200, v194\n" \
"v_mfma_f32_16x16x32_bf16 v[76:79], a[216:219], a[172:175], v[76:79]\n" \
"v_bfe_i32 v195, v201, 3, 1\n" \
"v_and_b32_e64 v195, v200, v195\n" \
"buffer_load_dword v216, v226, %[srdz], s68 offen offset:68 sc1\n" \
"v_mfma_f32_16x16x32_bf16 v[108:111], a[208:211], a[184:187], v[108:111]\n" \
"ds_read_b64_tr_b16 a[204:205], v227 offset:25088\n" \
"ds_read_b128 v[184:187], v209 offset:128\n" \
"v_lshrrev_b32_e64 v217, 3, v224\n" \
"s_add_u32 s51, %[sin], 4096\n" \
"v_mfma_f32_16x16x32_bf16 v[108:111], a[216:219], a[188:191], v[108:111]\n" \
"s_waitcnt vmcnt(63) lgkmcnt(12)\n" \
"s_nop 0\n" \
"v_mfma_f32_16x16x32_bf16 v[20:23], a[224:227], v[0:3], v[20:23]\n" \
"ds_read_b64_tr_b16 a[206:207], v227 offset:27136\n" \
"ds_read_b128 v[188:191], v209 offset:2176\n" \
"v_mfma_f32_16x16x32_bf16 v[20:23], a[228:231], v[8:11], v[20:23]\n" \
"ds_read_b64_tr_b16 a[220:221], v227 offset:29184\n" \
"v_bfe_i32 v196, v201, 4, 1\n" \
"v_mfma_f32_16x16x32_bf16 v[52:55], a[224:227], v[32:35], v[52:55]\n" \
"ds_read_b64_tr_b16 a[222:223], v227 offset:31232\n" \
"v_and_b32_e64 v196, v200, v196\n" \
"v_mfma_f32_16x16x32_bf16 v[52:55], a[228:231], v[40:43], v[52:55]\n" \
"v_bfe_i32 v197, v201, 5, 1\n" \
"v_and_b32_e64 v197, v200, v197\n" \
"v_mfma_f32_16x16x32_bf16 v[84:87], a[224:227], v[64:67], v[84:87]\n" \
"v_cvt_pk_bf16_f32 v4, v4, v5\n" \
"v_bfe_i32 v198, v201, 6, 1\n" \
"v_mfma_f32_16x16x32_bf16 v[84:87], a[228:231], v[72:75], v[84:87]\n" \
"v_cvt_pk_bf16_f32 v5, v6, v7\n" \
"v_and_b32_e64 v198, v200, v198\n" \
"v_mfma_f32_16x16x32_bf16 v[116:119], a[224:227], v[96:99], v[116:119]\n" \
"ds_read_b64_tr_b16 a[212:213], v215 offset:33280\n" \
"v_cvt_pk_bf16_f32 v6, v12, v13\n" \
"v_mfma_f32_16x16x32_bf16 v[116:119], a[228:231], v[104:107], v[116:119]\n" \
"ds_read_b64_tr_b16 a[214:215], v215 offset:35328\n" \
"v_cvt_pk_bf16_f32 v7, v14, v15\n" \
"v_mfma_f32_16x16x32_bf16 v[20:23], a[236:239], v[16:19], v[20:23]\n" \
"ds_read_b64_tr_b16 a[208:209], v215 offset:37376\n" \
"v_bfe_i32 v199, v201, 7, 1\n" \
"v_mfma_f32_16x16x32_bf16 v[20:23], a[232:235], v[24:27], v[20:23]\n" \
"ds_read_b64_tr_b16 a[210:211], v215 offset:39424\n" \
"v_and_b32_e64 v199, v200, v199\n" \
"v_mfma_f32_16x16x32_bf16 v[52:55], a[236:239], v[48:51], v[52:55]\n" \
"s_add_u32 s55, s46, 8192\n" \
"buffer_store_dwordx4 v[4:7], %[vxs], %[srdz], s55 offen offset:0 sc1\n" \
".if . - adaln_body != 57344\n" \
".error \"size model\"\n" \
".endif\n" \
"Lk13_%=:\n" \
"s_cbranch_execz adaln_pg0\n" \
"s_nop 0\n" \
"v_mfma_f32_16x16x32_bf16 v[52:55], a[232:235], v[56:59], v[52:55]\n" \
"v_cvt_pk_bf16_f32 v36, v36, v37\n" \
"v_cvt_pk_bf16_f32 v37, v38, v39\n" \
"v_mfma_f32_16x16x32_bf16 v[84:87], a[236:239], v[80:83], v[84:87]\n" \
"v_cvt_pk_bf16_f32 v38, v44, v45\n" \
"s_waitcnt lgkmcnt(14)\n" \
"s_nop 0\n" \
"v_add_f32_dpp v206, v206, v206 quad_perm:[1,0,3,2] row_mask:0xf bank_mask:0xf\n" \
"v_mfma_f32_16x16x32_bf16 v[84:87], a[232:235], v[88:91], v[84:87]\n" \
"v_cvt_pk_bf16_f32 v39, v46, v47\n" \
"v_mfma_f32_16x16x32_bf16 v[116:119], a[236:239], v[112:115], v[116:119]\n" \
"ds_read_b64_tr_b16 a[216:217], v215 offset:41472\n" \
"v_add_f32_dpp v206, v206, v206 quad_perm:[2,3,0,1] row_mask:0xf bank_mask:0xf\n" \
"v_mfma_f32_16x16x32_bf16 v[116:119], a[232:235], v[120:123], v[116:119]\n" \
"s_waitcnt vmcnt(63) lgkmcnt(5)\n" \
"s_nop 0\n" \
"v_mfma_f32_16x16x32_bf16 v[28:31], a[196:199], v[0:3], v[28:31]\n" \
"ds_read_b64_tr_b16 a[218:219], v215 offset:43520\n" \
"v_cvt_pk_bf16_f32 v68, v68, v69\n" \
"v_mfma_f32_16x16x32_bf16 v[28:31], a[200:203], v[8:11], v[28:31]\n" \
"ds_read_b64_tr_b16 a[224:225], v215 offset:45568\n" \
"s_add_u32 s55, s46, 312320\n" \
"buffer_store_dwordx4 v[36:39], %[vxs], %[srdz], s55 offen offset:0 sc1\n" \
"v_mfma_f32_16x16x32_bf16 v[60:63], a[196:199], v[32:35], v[60:63]\n" \
"ds_read_b64_tr_b16 a[226:227], v215 offset:47616\n" \
"v_cvt_pk_bf16_f32 v69, v70, v71\n" \
"v_mfma_f32_16x16x32_bf16 v[60:63], a[200:203], v[40:43], v[60:63]\n" \
"v_cvt_pk_bf16_f32 v70, v76, v77\n" \
"v_cvt_pk_bf16_f32 v71, v78, v79\n" \
"v_mfma_f32_16x16x32_bf16 v[92:95], a[196:199], v[64:67], v[92:95]\n" \
"s_add_u32 s55, s46, 616448\n" \
"buffer_store_dwordx4 v[68:71], %[vxs], %[srdz], s55 offen offset:0 sc1\n" \
"v_mfma_f32_16x16x32_bf16 v[92:95], a[200:203], v[72:75], v[92:95]\n" \
"v_mfma_f32_16x16x32_bf16 v[124:127], a[196:199], v[96:99], v[124:127]\n" \
"ds_read_b64_tr_b16 a[228:229], v227 offset:33280\n" \
"v_cvt_pk_bf16_f32 v100, v100, v101\n" \
"v_mfma_f32_16x16x32_bf16 v[124:127], a[200:203], v[104:107], v[124:127]\n" \
"ds_read_b64_tr_b16 a[230:231], v227 offset:35328\n" \
"v_cvt_pk_bf16_f32 v101, v102, v103\n" \
"v_mfma_f32_16x16x32_bf16 v[28:31], a[204:207], v[16:19], v[28:31]\n" \
"ds_read_b64_tr_b16 a[236:237], v227 offset:37376\n" \
"v_cvt_pk_bf16_f32 v102, v108, v109\n" \
"v_mfma_f32_16x16x32_bf16 v[28:31], a[220:223], v[24:27], v[28:31]\n" \
"ds_read_b64_tr_b16 a[238:239], v227 offset:39424\n" \
"v_cvt_pk_bf16_f32 v103, v110, v111\n" \
"v_mfma_f32_16x16x32_bf16 v[60:63], a[204:207], v[48:51], v[60:63]\n" \
"s_add_u32 s55, s46, 920576\n" \
"buffer_store_dwordx4 v[100:103], %[vxs], %[srdz], s55 offen offset:0 sc1\n" \
"v_mfma_f32_16x16x32_bf16 v[60:63], a[220:223], v[56:59], v[60:63]\n" \
"v_mfma_f32_16x16x32_bf16 v[92:95], a[204:207], v[80:83], v[92:95]\n" \
"v_mov_b32_e64 v230, 1\n" \
"buffer_store_dword v230, v225, %[srdz], s62 offen offset:64 sc1\n" \
"v_mfma_f32_16x16x32_bf16 v[92:95], a[220:223], v[88:91], v[92:95]\n" \
"ds_read_b128 v[4:7], v208 offset:4096\n" \
"v_mfma_f32_16x16x32_bf16 v[124:127], a[204:207], v[112:115], v[124:127]\n" \
"ds_read_b64_tr_b16 a[232:233], v227 offset:41472\n" \
"v_mfma_f32_16x16x32_bf16 v[124:127], a[220:223], v[120:123], v[124:127]\n" \
"s_waitcnt vmcnt(63) lgkmcnt(6)\n" \
"s_nop 0\n" \
"v_mfma_f32_16x16x32_bf16 v[20:23], a[212:215], a[0:3], v[20:23]\n" \
"ds_read_b64_tr_b16 a[234:235], v227 offset:43520\n" \
"ds_read_b128 v[12:15], v208 offset:6144\n" \
"v_mfma_f32_16x16x32_bf16 v[20:23], a[208:211], a[4:7], v[20:23]\n" \
"ds_read_b64_tr_b16 a[196:197], v227 offset:45568\n" \
"v_mfma_f32_16x16x32_bf16 v[52:55], a[212:215], a[16:19], v[52:55]\n" \
"ds_read_b64_tr_b16 a[198:199], v227 offset:47616\n" \
"v_mfma_f32_16x16x32_bf16 v[52:55], a[208:211], a[20:23], v[52:55]\n" \
"ds_read_b128 v[36:39], v208 offset:4224\n" \
"v_mfma_f32_16x16x32_bf16 v[84:87], a[212:215], a[32:35], v[84:87]\n" \
"v_mfma_f32_16x16x32_bf16 v[84:87], a[208:211], a[36:39], v[84:87]\n" \
"v_mfma_f32_16x16x32_bf16 v[116:119], a[212:215], a[48:51], v[116:119]\n" \
"ds_read_b64_tr_b16 a[200:201], v215 offset:49664\n" \
"ds_read_b128 v[44:47], v208 offset:6272\n" \
"v_mfma_f32_16x16x32_bf16 v[116:119], a[208:211], a[52:55], v[116:119]\n" \
"ds_read_b64_tr_b16 a[202:203], v215 offset:51712\n" \
"v_mfma_f32_16x16x32_bf16 v[20:23], a[216:219], a[8:11], v[20:23]\n" \
"ds_read_b64_tr_b16 a[204:205], v215 offset:53760\n" \
"v_mfma_f32_16x16x32_bf16 v[20:23], a[224:227], a[12:15], v[20:23]\n" \
"ds_read_b64_tr_b16 a[206:207], v215 offset:55808\n" \
"ds_read_b128 v[68:71], v209 offset:4096\n" \
"v_mfma_f32_16x16x32_bf16 v[52:55], a[216:219], a[24:27], v[52:55]\n" \
"ds_read_b64_tr_b16 a[220:221], v215 offset:57856\n" \
"v_mfma_f32_16x16x32_bf16 v[52:55], a[224:227], a[28:31], v[52:55]\n" \
"ds_read_b64_tr_b16 a[222:223], v215 offset:59904\n" \
"v_mfma_f32_16x16x32_bf16 v[84:87], a[216:219], a[40:43], v[84:87]\n" \
"ds_read_b64_tr_b16 a[212:213], v215 offset:61952\n" \
"ds_read_b128 v[76:79], v209 offset:6144\n" \
"v_mfma_f32_16x16x32_bf16 v[84:87], a[224:227], a[44:47], v[84:87]\n" \
"ds_read_b64_tr_b16 a[214:215], v215 offset:64000\n" \
"v_mfma_f32_16x16x32_bf16 v[116:119], a[216:219], a[56:59], v[116:119]\n" \
"ds_read_b64_tr_b16 a[208:209], v227 offset:49664\n" \
"v_mfma_f32_16x16x32_bf16 v[116:119], a[224:227], a[60:63], v[116:119]\n" \
"s_waitcnt vmcnt(63) lgkmcnt(13)\n" \
"s_nop 0\n" \
"v_mfma_f32_16x16x32_bf16 v[28:31], a[228:231], a[0:3], v[28:31]\n" \
"ds_read_b64_tr_b16 a[210:211], v227 offset:51712\n" \
"ds_read_b128 v[100:103], v209 offset:4224\n" \
"v_mfma_f32_16x16x32_bf16 v[28:31], a[236:239], a[4:7], v[28:31]\n" \
"ds_read_b64_tr_b16 a[216:217], v227 offset:53760\n" \
"v_mfma_f32_16x16x32_bf16 v[60:63], a[228:231], a[16:19], v[60:63]\n" \
"ds_read_b64_tr_b16 a[218:219], v227 offset:55808\n" \
"ds_read_b128 v[108:111], v209 offset:6272\n" \
"v_mfma_f32_16x16x32_bf16 v[60:63], a[236:239], a[20:23], v[60:63]\n" \
"ds_read_b64_tr_b16 a[224:225], v227 offset:57856\n" \
"v_mfma_f32_16x16x32_bf16 v[92:95], a[228:231], a[32:35], v[92:95]\n" \
"ds_read_b64_tr_b16 a[226:227], v227 offset:59904\n" \
"v_mfma_f32_16x16x32_bf16 v[92:95], a[236:239], a[36:39], v[92:95]\n" \
"v_mfma_f32_16x16x32_bf16 v[124:127], a[228:231], a[48:51], v[124:127]\n" \
"v_mfma_f32_16x16x32_bf16 v[124:127], a[236:239], a[52:55], v[124:127]\n" \
"ds_read_b64_tr_b16 a[228:229], v227 offset:61952\n" \
"v_mfma_f32_16x16x32_bf16 v[28:31], a[232:235], a[8:11], v[28:31]\n" \
"ds_read_b64_tr_b16 a[230:231], v227 offset:64000\n" \
"s_waitcnt vmcnt(5)\n" \
"v_cmp_eq_u32_e32 vcc, 0, v216\n" \
"s_nop 0\n" \
"s_cmp_eq_u64 vcc, 0\n" \
"s_cselect_b32 %[got], 1, 0\n" \
"s_nop 0\n" \
"s_cselect_b32 s54, %[sin], 0x80000000\n" \
"s_cselect_b32 s51, s51, 0x80000000\n" \
"v_mfma_f32_16x16x32_bf16 v[28:31], a[196:199], a[12:15], v[28:31]\n" \
"v_mfma_f32_16x16x32_bf16 v[60:63], a[232:235], a[24:27], v[60:63]\n" \
"buffer_load_dwordx4 a[64:67], v217, %[srdz], s54 offen offset:0 sc1\n" \
"v_mfma_f32_16x16x32_bf16 v[60:63], a[196:199], a[28:31], v[60:63]\n" \
"v_mfma_f32_16x16x32_bf16 v[92:95], a[232:235], a[40:43], v[92:95]\n" \
"buffer_load_dwordx4 a[68:71], v217, %[srdz], s54 offen offset:1024 sc1\n" \
"v_mfma_f32_16x16x32_bf16 v[92:95], a[196:199], a[44:47], v[92:95]\n" \
"v_mfma_f32_16x16x32_bf16 v[124:127], a[232:235], a[56:59], v[124:127]\n" \
"buffer_load_dwordx4 a[72:75], v217, %[srdz], s54 offen offset:2048 sc1\n" \
"v_mfma_f32_16x16x32_bf16 v[124:127], a[196:199], a[60:63], v[124:127]\n" \
"s_waitcnt vmcnt(63) lgkmcnt(0)\n" \
"s_nop 0\n" \
"v_mfma_f32_16x16x32_bf16 v[20:23], a[200:203], a[128:131], v[20:23]\n" \
"buffer_load_dwordx4 a[76:79], v217, %[srdz], s54 offen offset:3072 sc1\n" \
"v_mfma_f32_16x16x32_bf16 v[20:23], a[204:207], a[132:135], v[20:23]\n" \
"v_mfma_f32_16x16x32_bf16 v[20:23], a[220:223], a[136:139], v[20:23]\n" \
"buffer_load_dwordx4 a[80:83], v217, %[srdz], s51 offen offset:0 sc1\n" \
"v_mfma_f32_16x16x32_bf16 v[20:23], a[212:215], a[140:143], v[20:23]\n" \
"v_mfma_f32_16x16x32_bf16 v[28:31], a[208:211], a[128:131], v[28:31]\n" \
"buffer_load_dwordx4 a[84:87], v217, %[srdz], s51 offen offset:1024 sc1\n" \
"v_mfma_f32_16x16x32_bf16 v[28:31], a[216:219], a[132:135], v[28:31]\n" \
"v_mfma_f32_16x16x32_bf16 v[28:31], a[224:227], a[136:139], v[28:31]\n" \
"buffer_load_dwordx4 a[88:91], v217, %[srdz], s51 offen offset:2048 sc1\n" \
"v_mfma_f32_16x16x32_bf16 v[28:31], a[228:231], a[140:143], v[28:31]\n" \
"v_mfma_f32_16x16x32_bf16 v[52:55], a[200:203], a[144:147], v[52:55]\n" \
"buffer_load_dwordx4 a[92:95], v217, %[srdz], s51 offen offset:3072 sc1\n" \
"v_mfma_f32_16x16x32_bf16 v[52:55], a[204:207], a[148:151], v[52:55]\n" \
"v_mfma_f32_16x16x32_bf16 v[52:55], a[220:223], a[152:155], v[52:55]\n" \
"v_mfma_f32_16x16x32_bf16 v[52:55], a[212:215], a[156:159], v[52:55]\n" \
"v_cvt_pk_bf16_f32 v20, v20, v21\n" \
"v_cvt_pk_bf16_f32 v21, v22, v23\n" \
"v_mfma_f32_16x16x32_bf16 v[60:63], a[208:211], a[144:147], v[60:63]\n" \
"buffer_load_dwordx2 v[228:229], v226, %[srdz], s68 offen offset:64 sc1\n" \
"v_mfma_f32_16x16x32_bf16 v[60:63], a[216:219], a[148:151], v[60:63]\n" \
"v_cvt_pk_bf16_f32 v22, v28, v29\n" \
"v_cvt_pk_bf16_f32 v23, v30, v31\n" \
"v_mfma_f32_16x16x32_bf16 v[60:63], a[224:227], a[152:155], v[60:63]\n" \
"s_add_u32 s55, s46, 12288\n" \
"buffer_store_dwordx4 v[20:23], %[vxs], %[srdz], s55 offen offset:0 sc1\n" \
"v_mfma_f32_16x16x32_bf16 v[60:63], a[228:231], a[156:159], v[60:63]\n" \
"v_mfma_f32_16x16x32_bf16 v[84:87], a[200:203], a[160:163], v[84:87]\n" \
"v_mfma_f32_16x16x32_bf16 v[84:87], a[204:207], a[164:167], v[84:87]\n" \
"v_mfma_f32_16x16x32_bf16 v[84:87], a[220:223], a[168:171], v[84:87]\n" \
"v_mfma_f32_16x16x32_bf16 v[84:87], a[212:215], a[172:175], v[84:87]\n" \
"v_cvt_pk_bf16_f32 v52, v52, v53\n" \
"v_cvt_pk_bf16_f32 v53, v54, v55\n" \
"v_mfma_f32_16x16x32_bf16 v[92:95], a[208:211], a[160:163], v[92:95]\n" \
"v_mfma_f32_16x16x32_bf16 v[92:95], a[216:219], a[164:167], v[92:95]\n" \
"v_cvt_pk_bf16_f32 v54, v60, v61\n" \
"v_cvt_pk_bf16_f32 v55, v62, v63\n" \
"v_mfma_f32_16x16x32_bf16 v[92:95], a[224:227], a[168:171], v[92:95]\n" \
"s_add_u32 s55, s46, 316416\n" \
"buffer_store_dwordx4 v[52:55], %[vxs], %[srdz], s55 offen offset:0 sc1\n" \
"v_mfma_f32_16x16x32_bf16 v[92:95], a[228:231], a[172:175], v[92:95]\n" \
"v_mfma_f32_16x16x32_bf16 v[116:119], a[200:203], a[176:179], v[116:119]\n" \
"v_mfma_f32_16x16x32_bf16 v[116:119], a[204:207], a[180:183], v[116:119]\n" \
"v_mfma_f32_16x16x32_bf16 v[116:119], a[220:223], a[184:187], v[116:119]\n" \
"v_mfma_f32_16x16x32_bf16 v[116:119], a[212:215], a[188:191], v[116:119]\n" \
"v_cvt_pk_bf16_f32 v84, v84, v85\n" \
"v_cvt_pk_bf16_f32 v85, v86, v87\n" \
"v_mfma_f32_16x16x32_bf16 v[124:127], a[208:211], a[176:179], v[124:127]\n" \
"v_mfma_f32_16x16x32_bf16 v[124:127], a[216:219], a[180:183], v[124:127]\n" \
"v_cvt_pk_bf16_f32 v86, v92, v93\n" \
"v_cvt_pk_bf16_f32 v87, v94, v95\n" \
"v_mfma_f32_16x16x32_bf16 v[124:127], a[224:227], a[184:187], v[124:127]\n" \
"s_add_u32 s55, s46, 620544\n" \
"buffer_store_dwordx4 v[84:87], %[vxs], %[srdz], s55 offen offset:0 sc1\n" \
"v_mfma_f32_16x16x32_bf16 v[124:127], a[228:231], a[188:191], v[124:127]\n" \
"v_cvt_pk_bf16_f32 v116, v116, v117\n" \
"v_cvt_pk_bf16_f32 v117, v118, v119\n" \
"s_waitcnt lgkmcnt(0)\n" \
"s_nop 4\n" \
"v_cvt_pk_bf16_f32 v118, v124, v125\n" \
"v_cvt_pk_bf16_f32 v119, v126, v127\n" \
"s_add_u32 s55, s46, 924672\n" \
"buffer_store_dwordx4 v[116:119], %[vxs], %[srdz], s55 offen offset:0 sc1\n" \
"s_branch Lend_%=\n" \
"s_nop 0\n" \
"Lsp1_%=:\n" \
"s_movk_i32 s51, 0x1000\n" \
"s_cmp_gt_u32 s48, 0\n" \
"s_cselect_b32 s61, 0, 1\n" \
"s_nop 0\n" \
"s_mul_i32 s61, s61, 152064\n" \
"Lsq1_%=:\n" \
"buffer_load_dwordx4 a[64:67], %[oq0], %[srdz], s61 offen offset:0 sc1\n" \
"buffer_load_dwordx4 a[68:71], %[oq0], %[srdz], s61 offen offset:1024 sc1\n" \
"buffer_load_dwordx4 a[72:75], %[oq0], %[srdz], s61 offen offset:2048 sc1\n" \
"buffer_load_dwordx4 a[76:79], %[oq0], %[srdz], s61 offen offset:3072 sc1\n" \
"buffer_load_dwordx4 a[80:83], v208, %[srdz], s61 offen offset:0 sc1\n" \
"buffer_load_dwordx4 a[84:87], v208, %[srdz], s61 offen offset:1024 sc1\n" \
"buffer_load_dwordx4 a[88:91], v208, %[srdz], s61 offen offset:2048 sc1\n" \
"buffer_load_dwordx4 a[92:95], v208, %[srdz], s61 offen offset:3072 sc1\n" \
"buffer_load_dwordx4 a[96:99], v209, %[srdz], s61 offen offset:0 sc1\n" \
"buffer_load_dwordx4 a[100:103], v209, %[srdz], s61 offen offset:1024 sc1\n" \
"buffer_load_dwordx4 a[104:107], v209, %[srdz], s61 offen offset:2048 sc1\n" \
"buffer_load_dwordx4 a[108:111], v209, %[srdz], s61 offen offset:3072 sc1\n" \
"buffer_load_dwordx4 a[112:115], v210, %[srdz], s61 offen offset:0 sc1\n" \
"buffer_load_dwordx4 a[116:119], v210, %[srdz], s61 offen offset:1024 sc1\n" \
"buffer_load_dwordx4 a[120:123], v210, %[srdz], s61 offen offset:2048 sc1\n" \
"buffer_load_dwordx4 a[124:127], v210, %[srdz], s61 offen offset:3072 sc1\n" \
"s_waitcnt vmcnt(0)\n" \
"s_nop 0\n" \
"v_mfma_f32_16x16x32_bf16 v[128:131], a[236:239], a[64:67], v[192:195]\n" \
"v_mfma_f32_16x16x32_bf16 v[128:131], a[196:199], a[68:71], v[128:131]\n" \
"v_mfma_f32_16x16x32_bf16 v[128:131], a[200:203], a[72:75], v[128:131]\n" \
"v_mfma_f32_16x16x32_bf16 v[128:131], a[204:207], a[76:79], v[128:131]\n" \
"v_mfma_f32_16x16x32_bf16 v[132:135], a[236:239], a[80:83], v[196:199]\n" \
"v_mfma_f32_16x16x32_bf16 v[132:135], a[196:199], a[84:87], v[132:135]\n" \
"v_mfma_f32_16x16x32_bf16 v[132:135], a[200:203], a[88:91], v[132:135]\n" \
"v_mfma_f32_16x16x32_bf16 v[132:135], a[204:207], a[92:95], v[132:135]\n" \
"v_mfma_f32_16x16x32_bf16 v[136:139], a[236:239], a[96:99], v[200:203]\n" \
"v_mfma_f32_16x16x32_bf16 v[136:139], a[196:199], a[100:103], v[136:139]\n" \
"v_mfma_f32_16x16x32_bf16 v[136:139], a[200:203], a[104:107], v[136:139]\n" \
"v_mfma_f32_16x16x32_bf16 v[136:139], a[204:207], a[108:111], v[136:139]\n" \
"v_mfma_f32_16x16x32_bf16 v[140:143], a[236:239], a[112:115], v[204:207]\n" \
"v_mfma_f32_16x16x32_bf16 v[140:143], a[196:199], a[116:119], v[140:143]\n" \
"v_mfma_f32_16x16x32_bf16 v[140:143], a[200:203], a[120:123], v[140:143]\n" \
"v_mfma_f32_16x16x32_bf16 v[140:143], a[204:207], a[124:127], v[140:143]\n" \
"s_nop 7\n" \
"s_nop 0\n" \
"v_cmp_u_f32_e64 s[44:45], v128, v132\n" \
"v_cmp_u_f32_e64 s[56:57], v136, v140\n" \
"s_or_b64 s[56:57], s[56:57], s[44:45]\n" \
"s_cbranch_scc0 Lbk1_%=\n" \
"s_sub_u32 s51, s51, 1\n" \
"s_cbranch_scc0 Lsq1_%=\n" \
"s_branch Lbk1_%=\n" \
"Lsp2_%=:\n" \
"s_movk_i32 s51, 0x1000\n" \
"s_cmp_gt_u32 s48, 1\n" \
"s_cselect_b32 s61, 1, 2\n" \
"s_mul_i32 s61, s61, 152064\n" \
"Lsq2_%=:\n" \
"buffer_load_dwordx4 a[0:3], %[oq0], %[srdz], s61 offen offset:0 sc1\n" \
"buffer_load_dwordx4 a[4:7], %[oq0], %[srdz], s61 offen offset:1024 sc1\n" \
"buffer_load_dwordx4 a[8:11], %[oq0], %[srdz], s61 offen offset:2048 sc1\n" \
"buffer_load_dwordx4 a[12:15], %[oq0], %[srdz], s61 offen offset:3072 sc1\n" \
"buffer_load_dwordx4 a[16:19], v208, %[srdz], s61 offen offset:0 sc1\n" \
"buffer_load_dwordx4 a[20:23], v208, %[srdz], s61 offen offset:1024 sc1\n" \
"buffer_load_dwordx4 a[24:27], v208, %[srdz], s61 offen offset:2048 sc1\n" \
"buffer_load_dwordx4 a[28:31], v208, %[srdz], s61 offen offset:3072 sc1\n" \
"buffer_load_dwordx4 a[32:35], v209, %[srdz], s61 offen offset:0 sc1\n" \
"buffer_load_dwordx4 a[36:39], v209, %[srdz], s61 offen offset:1024 sc1\n" \
"buffer_load_dwordx4 a[40:43], v209, %[srdz], s61 offen offset:2048 sc1\n" \
"buffer_load_dwordx4 a[44:47], v209, %[srdz], s61 offen offset:3072 sc1\n" \
"buffer_load_dwordx4 a[48:51], v210, %[srdz], s61 offen offset:0 sc1\n" \
"buffer_load_dwordx4 a[52:55], v210, %[srdz], s61 offen offset:1024 sc1\n" \
"buffer_load_dwordx4 a[56:59], v210, %[srdz], s61 offen offset:2048 sc1\n" \
"buffer_load_dwordx4 a[60:63], v210, %[srdz], s61 offen offset:3072 sc1\n" \
"s_waitcnt vmcnt(0)\n" \
"s_nop 0\n" \
"v_mfma_f32_16x16x32_bf16 v[192:195], a[196:199], a[0:3], v[128:131]\n" \
"v_mfma_f32_16x16x32_bf16 v[192:195], a[200:203], a[4:7], v[192:195]\n" \
"v_mfma_f32_16x16x32_bf16 v[192:195], a[204:207], a[8:11], v[192:195]\n" \
"v_mfma_f32_16x16x32_bf16 v[192:195], a[232:235], a[12:15], v[192:195]\n" \
"v_mfma_f32_16x16x32_bf16 v[196:199], a[196:199], a[16:19], v[132:135]\n" \
"v_mfma_f32_16x16x32_bf16 v[196:199], a[200:203], a[20:23], v[196:199]\n" \
"v_mfma_f32_16x16x32_bf16 v[196:199], a[204:207], a[24:27], v[196:199]\n" \
"v_mfma_f32_16x16x32_bf16 v[196:199], a[232:235], a[28:31], v[196:199]\n" \
"v_mfma_f32_16x16x32_bf16 v[200:203], a[196:199], a[32:35], v[136:139]\n" \
"v_mfma_f32_16x16x32_bf16 v[200:203], a[200:203], a[36:39], v[200:203]\n" \
"v_mfma_f32_16x16x32_bf16 v[200:203], a[204:207], a[40:43], v[200:203]\n" \
"v_mfma_f32_16x16x32_bf16 v[200:203], a[232:235], a[44:47], v[200:203]\n" \
"v_mfma_f32_16x16x32_bf16 v[204:207], a[196:199], a[48:51], v[140:143]\n" \
"v_mfma_f32_16x16x32_bf16 v[204:207], a[200:203], a[52:55], v[204:207]\n" \
"v_mfma_f32_16x16x32_bf16 v[204:207], a[204:207], a[56:59], v[204:207]\n" \
"v_mfma_f32_16x16x32_bf16 v[204:207], a[232:235], a[60:63], v[204:207]\n" \
"s_nop 7\n" \
"s_nop 0\n" \
"v_cmp_u_f32_e64 s[44:45], v192, v196\n" \
"v_cmp_u_f32_e64 s[56:57], v200, v204\n" \
"s_or_b64 s[56:57], s[56:57], s[44:45]\n" \
"s_cbranch_scc0 Lbk2_%=\n" \
"s_sub_u32 s51, s51, 1\n" \
"s_cbranch_scc0 Lsq2_%=\n" \
"s_branch Lbk2_%=\n" \
"Lsp3_%=:\n" \
"s_movk_i32 s51, 0x1000\n" \
"s_cmp_gt_u32 s48, 2\n" \
"s_cselect_b32 s61, 2, 3\n" \
"s_mul_i32 s61, s61, 152064\n" \
"Lsq3_%=:\n" \
"buffer_load_dwordx4 a[128:131], %[oq0], %[srdz], s61 offen offset:0 sc1\n" \
"buffer_load_dwordx4 a[132:135], %[oq0], %[srdz], s61 offen offset:1024 sc1\n" \
"buffer_load_dwordx4 a[136:139], %[oq0], %[srdz], s61 offen offset:2048 sc1\n" \
"buffer_load_dwordx4 a[140:143], %[oq0], %[srdz], s61 offen offset:3072 sc1\n" \
"buffer_load_dwordx4 a[144:147], v208, %[srdz], s61 offen offset:0 sc1\n" \
"buffer_load_dwordx4 a[148:151], v208, %[srdz], s61 offen offset:1024 sc1\n" \
"buffer_load_dwordx4 a[152:155], v208, %[srdz], s61 offen offset:2048 sc1\n" \
"buffer_load_dwordx4 a[156:159], v208, %[srdz], s61 offen offset:3072 sc1\n" \
"buffer_load_dwordx4 a[160:163], v209, %[srdz], s61 offen offset:0 sc1\n" \
"buffer_load_dwordx4 a[164:167], v209, %[srdz], s61 offen offset:1024 sc1\n" \
"buffer_load_dwordx4 a[168:171], v209, %[srdz], s61 offen offset:2048 sc1\n" \
"buffer_load_dwordx4 a[172:175], v209, %[srdz], s61 offen offset:3072 sc1\n" \
"buffer_load_dwordx4 a[176:179], v210, %[srdz], s61 offen offset:0 sc1\n" \
"buffer_load_dwordx4 a[180:183], v210, %[srdz], s61 offen offset:1024 sc1\n" \
"buffer_load_dwordx4 a[184:187], v210, %[srdz], s61 offen offset:2048 sc1\n" \
"buffer_load_dwordx4 a[188:191], v210, %[srdz], s61 offen offset:3072 sc1\n" \
"s_waitcnt vmcnt(0)\n" \
"s_nop 0\n" \
"v_mfma_f32_16x16x32_bf16 v[128:131], a[196:199], a[128:131], v[192:195]\n" \
"v_mfma_f32_16x16x32_bf16 v[128:131], a[200:203], a[132:135], v[128:131]\n" \
"v_mfma_f32_16x16x32_bf16 v[128:131], a[204:207], a[136:139], v[128:131]\n" \
"v_mfma_f32_16x16x32_bf16 v[128:131], a[224:227], a[140:143], v[128:131]\n" \
"v_mfma_f32_16x16x32_bf16 v[132:135], a[196:199], a[144:147], v[196:199]\n" \
"v_mfma_f32_16x16x32_bf16 v[132:135], a[200:203], a[148:151], v[132:135]\n" \
"v_mfma_f32_16x16x32_bf16 v[132:135], a[204:207], a[152:155], v[132:135]\n" \
"v_mfma_f32_16x16x32_bf16 v[132:135], a[224:227], a[156:159], v[132:135]\n" \
"v_mfma_f32_16x16x32_bf16 v[136:139], a[196:199], a[160:163], v[200:203]\n" \
"v_mfma_f32_16x16x32_bf16 v[136:139], a[200:203], a[164:167], v[136:139]\n" \
"v_mfma_f32_16x16x32_bf16 v[136:139], a[204:207], a[168:171], v[136:139]\n" \
"v_mfma_f32_16x16x32_bf16 v[136:139], a[224:227], a[172:175], v[136:139]\n" \
"v_mfma_f32_16x16x32_bf16 v[140:143], a[196:199], a[176:179], v[204:207]\n" \
"v_mfma_f32_16x16x32_bf16 v[140:143], a[200:203], a[180:183], v[140:143]\n" \
"v_mfma_f32_16x16x32_bf16 v[140:143], a[204:207], a[184:187], v[140:143]\n" \
"v_mfma_f32_16x16x32_bf16 v[140:143], a[224:227], a[188:191], v[140:143]\n" \
"s_nop 7\n" \
"s_nop 0\n" \
"v_cmp_u_f32_e64 s[44:45], v128, v132\n" \
"v_cmp_u_f32_e64 s[56:57], v136, v140\n" \
"s_or_b64 s[56:57], s[56:57], s[44:45]\n" \
"s_cbranch_scc0 Lbk3_%=\n" \
"s_sub_u32 s51, s51, 1\n" \
"s_cbranch_scc0 Lsq3_%=\n" \
"s_branch Lbk3_%=\n" \
"Lsp4_%=:\n" \
"s_movk_i32 s51, 0x1000\n" \
"s_cmp_gt_u32 s48, 3\n" \
"s_cselect_b32 s61, 3, 4\n" \
"s_mul_i32 s61, s61, 152064\n" \
"Lsq4_%=:\n" \
"buffer_load_dwordx4 a[64:67], %[oq0], %[srdz], s61 offen offset:0 sc1\n" \
"buffer_load_dwordx4 a[68:71], %[oq0], %[srdz], s61 offen offset:1024 sc1\n" \
"buffer_load_dwordx4 a[72:75], %[oq0], %[srdz], s61 offen offset:2048 sc1\n" \
"buffer_load_dwordx4 a[76:79], %[oq0], %[srdz], s61 offen offset:3072 sc1\n" \
"buffer_load_dwordx4 a[80:83], v208, %[srdz], s61 offen offset:0 sc1\n" \
"buffer_load_dwordx4 a[84:87], v208, %[srdz], s61 offen offset:1024 sc1\n" \
"buffer_load_dwordx4 a[88:91], v208, %[srdz], s61 offen offset:2048 sc1\n" \
"buffer_load_dwordx4 a[92:95], v208, %[srdz], s61 offen offset:3072 sc1\n" \
"buffer_load_dwordx4 a[96:99], v209, %[srdz], s61 offen offset:0 sc1\n" \
"buffer_load_dwordx4 a[100:103], v209, %[srdz], s61 offen offset:1024 sc1\n" \
"buffer_load_dwordx4 a[104:107], v209, %[srdz], s61 offen offset:2048 sc1\n" \
"buffer_load_dwordx4 a[108:111], v209, %[srdz], s61 offen offset:3072 sc1\n" \
"buffer_load_dwordx4 a[112:115], v210, %[srdz], s61 offen offset:0 sc1\n" \
"buffer_load_dwordx4 a[116:119], v210, %[srdz], s61 offen offset:1024 sc1\n" \
"buffer_load_dwordx4 a[120:123], v210, %[srdz], s61 offen offset:2048 sc1\n" \
"buffer_load_dwordx4 a[124:127], v210, %[srdz], s61 offen offset:3072 sc1\n" \
"s_waitcnt vmcnt(0)\n" \
"s_nop 0\n" \
"v_mfma_f32_16x16x32_bf16 v[192:195], a[196:199], a[64:67], v[128:131]\n" \
"v_mfma_f32_16x16x32_bf16 v[192:195], a[200:203], a[68:71], v[192:195]\n" \
"v_mfma_f32_16x16x32_bf16 v[192:195], a[204:207], a[72:75], v[192:195]\n" \
"v_mfma_f32_16x16x32_bf16 v[192:195], a[220:223], a[76:79], v[192:195]\n" \
"v_mfma_f32_16x16x32_bf16 v[196:199], a[196:199], a[80:83], v[132:135]\n" \
"v_mfma_f32_16x16x32_bf16 v[196:199], a[200:203], a[84:87], v[196:199]\n" \
"v_mfma_f32_16x16x32_bf16 v[196:199], a[204:207], a[88:91], v[196:199]\n" \
"v_mfma_f32_16x16x32_bf16 v[196:199], a[220:223], a[92:95], v[196:199]\n" \
"v_mfma_f32_16x16x32_bf16 v[200:203], a[196:199], a[96:99], v[136:139]\n" \
"v_mfma_f32_16x16x32_bf16 v[200:203], a[200:203], a[100:103], v[200:203]\n" \
"v_mfma_f32_16x16x32_bf16 v[200:203], a[204:207], a[104:107], v[200:203]\n" \
"v_mfma_f32_16x16x32_bf16 v[200:203], a[220:223], a[108:111], v[200:203]\n" \
"v_mfma_f32_16x16x32_bf16 v[204:207], a[196:199], a[112:115], v[140:143]\n" \
"v_mfma_f32_16x16x32_bf16 v[204:207], a[200:203], a[116:119], v[204:207]\n" \
"v_mfma_f32_16x16x32_bf16 v[204:207], a[204:207], a[120:123], v[204:207]\n" \
"v_mfma_f32_16x16x32_bf16 v[204:207], a[220:223], a[124:127], v[204:207]\n" \
"s_nop 7\n" \
"s_nop 0\n" \
"v_cmp_u_f32_e64 s[44:45], v192, v196\n" \
"v_cmp_u_f32_e64 s[56:57], v200, v204\n" \
"s_or_b64 s[56:57], s[56:57], s[44:45]\n" \
"s_cbranch_scc0 Lbk4_%=\n" \
"s_sub_u32 s51, s51, 1\n" \
"s_cbranch_scc0 Lsq4_%=\n" \
"s_branch Lbk4_%=\n" \
"Lsp5_%=:\n" \
"s_movk_i32 s51, 0x1000\n" \
"s_cmp_gt_u32 s48, 4\n" \
"s_cselect_b32 s61, 4, 5\n" \
"s_mul_i32 s61, s61, 152064\n" \
"Lsq5_%=:\n" \
"buffer_load_dwordx4 v[0:3], %[oq0], %[srdz], s61 offen offset:0 sc1\n" \
"buffer_load_dwordx4 v[8:11], %[oq0], %[srdz], s61 offen offset:1024 sc1\n" \
"buffer_load_dwordx4 v[16:19], %[oq0], %[srdz], s61 offen offset:2048 sc1\n" \
"buffer_load_dwordx4 v[24:27], %[oq0], %[srdz], s61 offen offset:3072 sc1\n" \
"buffer_load_dwordx4 v[32:35], v208, %[srdz], s61 offen offset:0 sc1\n" \
"buffer_load_dwordx4 v[40:43], v208, %[srdz], s61 offen offset:1024 sc1\n" \
"buffer_load_dwordx4 v[48:51], v208, %[srdz], s61 offen offset:2048 sc1\n" \
"buffer_load_dwordx4 v[56:59], v208, %[srdz], s61 offen offset:3072 sc1\n" \
"buffer_load_dwordx4 v[64:67], v209, %[srdz], s61 offen offset:0 sc1\n" \
"buffer_load_dwordx4 v[72:75], v209, %[srdz], s61 offen offset:1024 sc1\n" \
"buffer_load_dwordx4 v[80:83], v209, %[srdz], s61 offen offset:2048 sc1\n" \
"buffer_load_dwordx4 v[88:91], v209, %[srdz], s61 offen offset:3072 sc1\n" \
"buffer_load_dwordx4 v[96:99], v210, %[srdz], s61 offen offset:0 sc1\n" \
"buffer_load_dwordx4 v[104:107], v210, %[srdz], s61 offen offset:1024 sc1\n" \
"buffer_load_dwordx4 v[112:115], v210, %[srdz], s61 offen offset:2048 sc1\n" \
"buffer_load_dwordx4 v[120:123], v210, %[srdz], s61 offen offset:3072 sc1\n" \
"s_waitcnt vmcnt(0)\n" \
"s_nop 0\n" \
"v_mfma_f32_16x16x32_bf16 v[128:131], a[232:235], v[0:3], v[192:195]\n" \
"v_mfma_f32_16x16x32_bf16 v[128:131], a[236:239], v[8:11], v[128:131]\n" \
"v_mfma_f32_16x16x32_bf16 v[128:131], a[208:211], v[16:19], v[128:131]\n" \
"v_mfma_f32_16x16x32_bf16 v[128:131], a[228:231], v[24:27], v[128:131]\n" \
"v_mfma_f32_16x16x32_bf16 v[132:135], a[232:235], v[32:35], v[196:199]\n" \
"v_mfma_f32_16x16x32_bf16 v[132:135], a[236:239], v[40:43], v[132:135]\n" \
"v_mfma_f32_16x16x32_bf16 v[132:135], a[208:211], v[48:51], v[132:135]\n" \
"v_mfma_f32_16x16x32_bf16 v[132:135], a[228:231], v[56:59], v[132:135]\n" \
"v_mfma_f32_16x16x32_bf16 v[136:139], a[232:235], v[64:67], v[200:203]\n" \
"v_mfma_f32_16x16x32_bf16 v[136:139], a[236:239], v[72:75], v[136:139]\n" \
"v_mfma_f32_16x16x32_bf16 v[136:139], a[208:211], v[80:83], v[136:139]\n" \
"v_mfma_f32_16x16x32_bf16 v[136:139], a[228:231], v[88:91], v[136:139]\n" \
"v_mfma_f32_16x16x32_bf16 v[140:143], a[232:235], v[96:99], v[204:207]\n" \
"v_mfma_f32_16x16x32_bf16 v[140:143], a[236:239], v[104:107], v[140:143]\n" \
"v_mfma_f32_16x16x32_bf16 v[140:143], a[208:211], v[112:115], v[140:143]\n" \
"v_mfma_f32_16x16x32_bf16 v[140:143], a[228:231], v[120:123], v[140:143]\n" \
"s_nop 7\n" \
"s_nop 0\n" \
"v_cmp_u_f32_e64 s[44:45], v128, v132\n" \
"v_cmp_u_f32_e64 s[56:57], v136, v140\n" \
"s_or_b64 s[56:57], s[56:57], s[44:45]\n" \
"s_cbranch_scc0 Lbk5_%=\n" \
"s_sub_u32 s51, s51, 1\n" \
"s_cbranch_scc0 Lsq5_%=\n" \
"s_branch Lbk5_%=\n" \
"Lsp6_%=:\n" \
"s_movk_i32 s51, 0x1000\n" \
"s_cmp_gt_u32 s48, 5\n" \
"s_cselect_b32 s61, 5, 6\n" \
"s_mul_i32 s61, s61, 152064\n" \
"Lsq6_%=:\n" \
"buffer_load_dwordx4 a[0:3], %[oq0], %[srdz], s61 offen offset:0 sc1\n" \
"buffer_load_dwordx4 a[4:7], %[oq0], %[srdz], s61 offen offset:1024 sc1\n" \
"buffer_load_dwordx4 a[8:11], %[oq0], %[srdz], s61 offen offset:2048 sc1\n" \
"buffer_load_dwordx4 a[12:15], %[oq0], %[srdz], s61 offen offset:3072 sc1\n" \
"buffer_load_dwordx4 a[16:19], v208, %[srdz], s61 offen offset:0 sc1\n" \
"buffer_load_dwordx4 a[20:23], v208, %[srdz], s61 offen offset:1024 sc1\n" \
"buffer_load_dwordx4 a[24:27], v208, %[srdz], s61 offen offset:2048 sc1\n" \
"buffer_load_dwordx4 a[28:31], v208, %[srdz], s61 offen offset:3072 sc1\n" \
"buffer_load_dwordx4 a[32:35], v209, %[srdz], s61 offen offset:0 sc1\n" \
"buffer_load_dwordx4 a[36:39], v209, %[srdz], s61 offen offset:1024 sc1\n" \
"buffer_load_dwordx4 a[40:43], v209, %[srdz], s61 offen offset:2048 sc1\n" \
"buffer_load_dwordx4 a[44:47], v209, %[srdz], s61 offen offset:3072 sc1\n" \
"buffer_load_dwordx4 a[48:51], v210, %[srdz], s61 offen offset:0 sc1\n" \
"buffer_load_dwordx4 a[52:55], v210, %[srdz], s61 offen offset:1024 sc1\n" \
"buffer_load_dwordx4 a[56:59], v210, %[srdz], s61 offen offset:2048 sc1\n" \
"buffer_load_dwordx4 a[60:63], v210, %[srdz], s61 offen offset:3072 sc1\n" \
"s_waitcnt vmcnt(0)\n" \
"s_nop 0\n" \
"v_mfma_f32_16x16x32_bf16 v[192:195], a[196:199], a[0:3], v[128:131]\n" \
"v_mfma_f32_16x16x32_bf16 v[192:195], a[200:203], a[4:7], v[192:195]\n" \
"v_mfma_f32_16x16x32_bf16 v[192:195], a[204:207], a[8:11], v[192:195]\n" \
"v_mfma_f32_16x16x32_bf16 v[192:195], a[220:223], a[12:15], v[192:195]\n" \
"v_mfma_f32_16x16x32_bf16 v[196:199], a[196:199], a[16:19], v[132:135]\n" \
"v_mfma_f32_16x16x32_bf16 v[196:199], a[200:203], a[20:23], v[196:199]\n" \
"v_mfma_f32_16x16x32_bf16 v[196:199], a[204:207], a[24:27], v[196:199]\n" \
"v_mfma_f32_16x16x32_bf16 v[196:199], a[220:223], a[28:31], v[196:199]\n" \
"v_mfma_f32_16x16x32_bf16 v[200:203], a[196:199], a[32:35], v[136:139]\n" \
"v_mfma_f32_16x16x32_bf16 v[200:203], a[200:203], a[36:39], v[200:203]\n" \
"v_mfma_f32_16x16x32_bf16 v[200:203], a[204:207], a[40:43], v[200:203]\n" \
"v_mfma_f32_16x16x32_bf16 v[200:203], a[220:223], a[44:47], v[200:203]\n" \
"v_mfma_f32_16x16x32_bf16 v[204:207], a[196:199], a[48:51], v[140:143]\n" \
"v_mfma_f32_16x16x32_bf16 v[204:207], a[200:203], a[52:55], v[204:207]\n" \
"v_mfma_f32_16x16x32_bf16 v[204:207], a[204:207], a[56:59], v[204:207]\n" \
"v_mfma_f32_16x16x32_bf16 v[204:207], a[220:223], a[60:63], v[204:207]\n" \
"s_nop 7\n" \
"s_nop 0\n" \
"v_cmp_u_f32_e64 s[44:45], v192, v196\n" \
"v_cmp_u_f32_e64 s[56:57], v200, v204\n" \
"s_or_b64 s[56:57], s[56:57], s[44:45]\n" \
"s_cbranch_scc0 Lbk6_%=\n" \
"s_sub_u32 s51, s51, 1\n" \
"s_cbranch_scc0 Lsq6_%=\n" \
"s_branch Lbk6_%=\n" \
"Lsp7_%=:\n" \
"s_movk_i32 s51, 0x1000\n" \
"s_cmp_gt_u32 s48, 6\n" \
"s_cselect_b32 s61, 6, 7\n" \
"s_mul_i32 s61, s61, 152064\n" \
"Lsq7_%=:\n" \
"buffer_load_dwordx4 a[128:131], %[oq0], %[srdz], s61 offen offset:0 sc1\n" \
"buffer_load_dwordx4 a[132:135], %[oq0], %[srdz], s61 offen offset:1024 sc1\n" \
"buffer_load_dwordx4 a[136:139], %[oq0], %[srdz], s61 offen offset:2048 sc1\n" \
"buffer_load_dwordx4 a[140:143], %[oq0], %[srdz], s61 offen offset:3072 sc1\n" \
"buffer_load_dwordx4 a[144:147], v208, %[srdz], s61 offen offset:0 sc1\n" \
"buffer_load_dwordx4 a[148:151], v208, %[srdz], s61 offen offset:1024 sc1\n" \
"buffer_load_dwordx4 a[152:155], v208, %[srdz], s61 offen offset:2048 sc1\n" \
"buffer_load_dwordx4 a[156:159], v208, %[srdz], s61 offen offset:3072 sc1\n" \
"buffer_load_dwordx4 a[160:163], v209, %[srdz], s61 offen offset:0 sc1\n" \
"buffer_load_dwordx4 a[164:167], v209, %[srdz], s61 offen offset:1024 sc1\n" \
"buffer_load_dwordx4 a[168:171], v209, %[srdz], s61 offen offset:2048 sc1\n" \
"buffer_load_dwordx4 a[172:175], v209, %[srdz], s61 offen offset:3072 sc1\n" \
"buffer_load_dwordx4 a[176:179], v210, %[srdz], s61 offen offset:0 sc1\n" \
"buffer_load_dwordx4 a[180:183], v210, %[srdz], s61 offen offset:1024 sc1\n" \
"buffer_load_dwordx4 a[184:187], v210, %[srdz], s61 offen offset:2048 sc1\n" \
"buffer_load_dwordx4 a[188:191], v210, %[srdz], s61 offen offset:3072 sc1\n" \
"s_waitcnt vmcnt(0)\n" \
"s_nop 0\n" \
"v_mfma_f32_16x16x32_bf16 v[128:131], a[208:211], a[128:131], v[192:195]\n" \
"v_mfma_f32_16x16x32_bf16 v[128:131], a[224:227], a[132:135], v[128:131]\n" \
"v_mfma_f32_16x16x32_bf16 v[128:131], a[228:231], a[136:139], v[128:131]\n" \
"v_mfma_f32_16x16x32_bf16 v[128:131], a[236:239], a[140:143], v[128:131]\n" \
"v_mfma_f32_16x16x32_bf16 v[132:135], a[208:211], a[144:147], v[196:199]\n" \
"v_mfma_f32_16x16x32_bf16 v[132:135], a[224:227], a[148:151], v[132:135]\n" \
"v_mfma_f32_16x16x32_bf16 v[132:135], a[228:231], a[152:155], v[132:135]\n" \
"v_mfma_f32_16x16x32_bf16 v[132:135], a[236:239], a[156:159], v[132:135]\n" \
"v_mfma_f32_16x16x32_bf16 v[136:139], a[208:211], a[160:163], v[200:203]\n" \
"v_mfma_f32_16x16x32_bf16 v[136:139], a[224:227], a[164:167], v[136:139]\n" \
"v_mfma_f32_16x16x32_bf16 v[136:139], a[228:231], a[168:171], v[136:139]\n" \
"v_mfma_f32_16x16x32_bf16 v[136:139], a[236:239], a[172:175], v[136:139]\n" \
"v_mfma_f32_16x16x32_bf16 v[140:143], a[208:211], a[176:179], v[204:207]\n" \
"v_mfma_f32_16x16x32_bf16 v[140:143], a[224:227], a[180:183], v[140:143]\n" \
"v_mfma_f32_16x16x32_bf16 v[140:143], a[228:231], a[184:187], v[140:143]\n" \
"v_mfma_f32_16x16x32_bf16 v[140:143], a[236:239], a[188:191], v[140:143]\n" \
"s_nop 7\n" \
"s_nop 0\n" \
"v_cmp_u_f32_e64 s[44:45], v128, v132\n" \
"v_cmp_u_f32_e64 s[56:57], v136, v140\n" \
"s_or_b64 s[56:57], s[56:57], s[44:45]\n" \
"s_cbranch_scc0 Lbk7_%=\n" \
"s_sub_u32 s51, s51, 1\n" \
"s_cbranch_scc0 Lsq7_%=\n" \
"s_branch Lbk7_%=\n" \
"Lspk_%=:\n" \
"s_nop 0\n" \
"s_mov_b32 s51, 0x10000\n" \
"Lsqk_%=:\n" \
"buffer_load_dword v229, v226, %[srdz], s50 offen sc1\n" \
"s_waitcnt vmcnt(0)\n" \
"s_nop 0\n" \
"v_cmp_eq_u32_e64 s[56:57], 0, v229\n" \
"s_cmp_lg_u64 s[56:57], 0\n" \
"s_cbranch_scc0 Lbkk_%=\n" \
"s_sub_u32 s51, s51, 1\n" \
"s_cbranch_scc0 Lsqk_%=\n" \
"s_branch Lbkk_%=\n" \
"Lspp_%=:\n" \
"s_nop 0\n" \
"s_mov_b32 s51, 0x10000\n" \
"Lsqp_%=:\n" \
"buffer_load_dword v232, v226, %[srdz], s54 offen sc1\n" \
"s_waitcnt vmcnt(0)\n" \
"v_cmp_eq_u32_e32 vcc, 0, v232\n" \
"s_cbranch_vccz Lbkp_%=\n" \
"s_sub_u32 s51, s51, 1\n" \
"s_cbranch_scc0 Lsqp_%=\n" \
"s_branch Lbkp_%=\n" \
"Lwalk_%=:\n" \
"s_mov_b64 exec, 0\n" \
"s_cmp_eq_u32 %[wid], 1\n" \
"s_cbranch_scc1 Lk0_%=\n" \
"s_cmp_eq_u32 %[wid], 2\n" \
"s_cbranch_scc1 Lk6_%=\n" \
"s_branch Lk12_%=\n" \
"adaln_kr:\n" \
"s_mov_b64 exec, -1\n" \
"s_branch Lgo_%=\n" \
"Lslow_%=:\n" \
"s_mov_b32 s52, 1\n" \
"s_mov_b32 s48, 0\n" \
"s_mov_b32 s51, 32\n" \
"s_nop 0\n" \
"s_mov_b32 s62, 0x80000000\n" \
"s_mov_b32 s63, 0x80000000\n" \
"s_waitcnt vmcnt(0)\n" \
"s_nop 0\n" \
"v_readlane_b32 s64, %[tlo], 9\n" \
"v_readlane_b32 s65, %[thi], 9\n" \
"s_mov_b32 s66, %[wbytes]\n" \
"v_mov_b32_e32 v229, %[vg0]\n" \
"s_mov_b32 s44, %[m0t]\n" \
"s_mov_b32 s45, 0\n" \
"v_xor_b32_e64 v208, 64, %[kb0]\n" \
"v_xor_b32_e32 v209, 0x80, %[kb0]\n" \
"v_xor_b32_e32 v210, 0xc0, %[kb0]\n" \
"v_xor_b32_e64 v212, 64, %[kd0]\n" \
"v_xor_b32_e32 v213, 0x80, %[kd0]\n" \
"v_xor_b32_e32 v214, 0xc0, %[kd0]\n" \
"v_xor_b32_e64 v211, 32, %[vvb]\n" \
"v_add_u32_e32 v215, 0xfffffc80, %[vd0]\n" \
"s_mov_b32 s66, 0\n" \
"s_add_u32 s40, %[m0b], 0\n" \
"s_add_u32 s41, %[m0b], 16384\n" \
"s_add_u32 s42, %[m0b], 32768\n" \
"s_add_u32 s43, %[m0b], 49152\n" \
"v_mov_b32_e32 v225, 0xff800000\n" \
"v_mov_b32_e32 v226, 0xff800000\n" \
"v_mov_b32_e32 v227, 0xff800000\n" \
"v_mov_b32_e32 v228, 0xff800000\n" \
"s_waitcnt vmcnt(0) lgkmcnt(0)\n" \
"s_barrier\n" \
"Lsl_%=:\n" \
"s_mov_b32 m0, s40\n" \
"s_nop 0\n" \
"buffer_load_dwordx4 %[kd0], %[srdk], s48 offen offset:0 lds\n" \
"buffer_load_dwordx4 v212, %[srdk], s48 offen offset:1024 lds\n" \
"buffer_load_dwordx4 v213, %[srdk], s48 offen offset:2048 lds\n" \
"buffer_load_dwordx4 v214, %[srdk], s48 offen offset:3072 lds\n" \
"s_waitcnt vmcnt(0)\n" \
"s_barrier\n" \
"ds_read_b128 a[64:67], %[kb0] offset:0\n" \
"ds_read_b128 a[68:71], v208 offset:0\n" \
"ds_read_b128 a[72:75], v209 offset:0\n" \
"ds_read_b128 a[76:79], v210 offset:0\n" \
"ds_read_b128 a[92:95], v210 offset:4096\n" \
"ds_read_b128 a[88:91], v209 offset:4096\n" \
"ds_read_b128 a[84:87], v208 offset:4096\n" \
"ds_read_b128 a[80:83], %[kb0] offset:4096\n" \
"ds_read_b128 a[96:99], %[kb0] offset:8192\n" \
"ds_read_b128 a[100:103], v208 offset:8192\n" \
"ds_read_b128 a[104:107], v209 offset:8192\n" \
"ds_read_b128 a[108:111], v210 offset:8192\n" \
"ds_read_b128 a[124:127], v210 offset:12288\n" \
"ds_read_b128 a[120:123], v209 offset:12288\n" \
"ds_read_b128 a[116:119], v208 offset:12288\n" \
"ds_read_b128 a[112:115], %[kb0] offset:12288\n" \
"s_waitcnt lgkmcnt(0)\n" \
"s_barrier\n" \
"v_mfma_f32_16x16x32_bf16 v[128:131], a[64:67], a[0:3], 0\n" \
"v_mfma_f32_16x16x32_bf16 v[128:131], a[68:71], a[4:7], v[128:131]\n" \
"v_mfma_f32_16x16x32_bf16 v[128:131], a[72:75], a[8:11], v[128:131]\n" \
"v_mfma_f32_16x16x32_bf16 v[128:131], a[76:79], a[12:15], v[128:131]\n" \
"v_mfma_f32_16x16x32_bf16 v[132:135], a[92:95], a[12:15], 0\n" \
"v_mfma_f32_16x16x32_bf16 v[132:135], a[88:91], a[8:11], v[132:135]\n" \
"v_mfma_f32_16x16x32_bf16 v[132:135], a[84:87], a[4:7], v[132:135]\n" \
"v_mfma_f32_16x16x32_bf16 v[132:135], a[80:83], a[0:3], v[132:135]\n" \
"v_mfma_f32_16x16x32_bf16 v[136:139], a[64:67], a[16:19], 0\n" \
"v_mfma_f32_16x16x32_bf16 v[136:139], a[68:71], a[20:23], v[136:139]\n" \
"v_mfma_f32_16x16x32_bf16 v[136:139], a[72:75], a[24:27], v[136:139]\n" \
"v_mfma_f32_16x16x32_bf16 v[136:139], a[76:79], a[28:31], v[136:139]\n" \
"v_mfma_f32_16x16x32_bf16 v[140:143], a[92:95], a[28:31], 0\n" \
"v_mfma_f32_16x16x32_bf16 v[140:143], a[88:91], a[24:27], v[140:143]\n" \
"v_mfma_f32_16x16x32_bf16 v[140:143], a[84:87], a[20:23], v[140:143]\n" \
"v_mfma_f32_16x16x32_bf16 v[140:143], a[80:83], a[16:19], v[140:143]\n" \
"v_mfma_f32_16x16x32_bf16 v[144:147], a[64:67], a[32:35], 0\n" \
"v_mfma_f32_16x16x32_bf16 v[144:147], a[68:71], a[36:39], v[144:147]\n" \
"v_mfma_f32_16x16x32_bf16 v[144:147], a[72:75], a[40:43], v[144:147]\n" \
"v_mfma_f32_16x16x32_bf16 v[144:147], a[76:79], a[44:47], v[144:147]\n" \
"v_mfma_f32_16x16x32_bf16 v[148:151], a[92:95], a[44:47], 0\n" \
"v_mfma_f32_16x16x32_bf16 v[148:151], a[88:91], a[40:43], v[148:151]\n" \
"v_mfma_f32_16x16x32_bf16 v[148:151], a[84:87], a[36:39], v[148:151]\n" \
"v_mfma_f32_16x16x32_bf16 v[148:151], a[80:83], a[32:35], v[148:151]\n" \
"v_mfma_f32_16x16x32_bf16 v[152:155], a[64:67], a[48:51], 0\n" \
"v_mfma_f32_16x16x32_bf16 v[152:155], a[68:71], a[52:55], v[152:155]\n" \
"v_mfma_f32_16x16x32_bf16 v[152:155], a[72:75], a[56:59], v[152:155]\n" \
"v_mfma_f32_16x16x32_bf16 v[152:155], a[76:79], a[60:63], v[152:155]\n" \
"v_mfma_f32_16x16x32_bf16 v[156:159], a[92:95], a[60:63], 0\n" \
"v_mfma_f32_16x16x32_bf16 v[156:159], a[88:91], a[56:59], v[156:159]\n" \
"v_mfma_f32_16x16x32_bf16 v[156:159], a[84:87], a[52:55], v[156:159]\n" \
"v_mfma_f32_16x16x32_bf16 v[156:159], a[80:83], a[48:51], v[156:159]\n" \
"s_nop 7\n" \
"s_nop 0\n" \
"v_max3_f32 v230, v128, v129, v130\n" \
"v_max3_f32 v231, v131, v132, v133\n" \
"v_max3_f32 v230, v230, v134, v135\n" \
"v_max3_f32 v225, v230, v231, v225\n" \
"v_max3_f32 v230, v136, v137, v138\n" \
"v_max3_f32 v231, v139, v140, v141\n" \
"v_max3_f32 v230, v230, v142, v143\n" \
"v_max3_f32 v226, v230, v231, v226\n" \
"v_max3_f32 v230, v144, v145, v146\n" \
"v_max3_f32 v231, v147, v148, v149\n" \
"v_max3_f32 v230, v230, v150, v151\n" \
"v_max3_f32 v227, v230, v231, v227\n" \
"v_max3_f32 v230, v152, v153, v154\n" \
"v_max3_f32 v231, v155, v156, v157\n" \
"v_max3_f32 v230, v230, v158, v159\n" \
"v_max3_f32 v228, v230, v231, v228\n" \
"s_nop 3\n" \
"s_nop 0\n" \
"v_mfma_f32_16x16x32_bf16 v[128:131], a[96:99], a[0:3], 0\n" \
"v_mfma_f32_16x16x32_bf16 v[128:131], a[100:103], a[4:7], v[128:131]\n" \
"v_mfma_f32_16x16x32_bf16 v[128:131], a[104:107], a[8:11], v[128:131]\n" \
"v_mfma_f32_16x16x32_bf16 v[128:131], a[108:111], a[12:15], v[128:131]\n" \
"v_mfma_f32_16x16x32_bf16 v[132:135], a[124:127], a[12:15], 0\n" \
"v_mfma_f32_16x16x32_bf16 v[132:135], a[120:123], a[8:11], v[132:135]\n" \
"v_mfma_f32_16x16x32_bf16 v[132:135], a[116:119], a[4:7], v[132:135]\n" \
"v_mfma_f32_16x16x32_bf16 v[132:135], a[112:115], a[0:3], v[132:135]\n" \
"v_mfma_f32_16x16x32_bf16 v[136:139], a[96:99], a[16:19], 0\n" \
"v_mfma_f32_16x16x32_bf16 v[136:139], a[100:103], a[20:23], v[136:139]\n" \
"v_mfma_f32_16x16x32_bf16 v[136:139], a[104:107], a[24:27], v[136:139]\n" \
"v_mfma_f32_16x16x32_bf16 v[136:139], a[108:111], a[28:31], v[136:139]\n" \
"v_mfma_f32_16x16x32_bf16 v[140:143], a[124:127], a[28:31], 0\n" \
"v_mfma_f32_16x16x32_bf16 v[140:143], a[120:123], a[24:27], v[140:143]\n" \
"v_mfma_f32_16x16x32_bf16 v[140:143], a[116:119], a[20:23], v[140:143]\n" \
"v_mfma_f32_16x16x32_bf16 v[140:143], a[112:115], a[16:19], v[140:143]\n" \
"v_mfma_f32_16x16x32_bf16 v[144:147], a[96:99], a[32:35], 0\n" \
"v_mfma_f32_16x16x32_bf16 v[144:147], a[100:103], a[36:39], v[144:147]\n" \
"v_mfma_f32_16x16x32_bf16 v[144:147], a[104:107], a[40:43], v[144:147]\n" \
"v_mfma_f32_16x16x32_bf16 v[144:147], a[108:111], a[44:47], v[144:147]\n" \
"v_mfma_f32_16x16x32_bf16 v[148:151], a[124:127], a[44:47], 0\n" \
"v_mfma_f32_16x16x32_bf16 v[148:151], a[120:123], a[40:43], v[148:151]\n" \
"v_mfma_f32_16x16x32_bf16 v[148:151], a[116:119], a[36:39], v[148:151]\n" \
"v_mfma_f32_16x16x32_bf16 v[148:151], a[112:115], a[32:35], v[148:151]\n" \
"v_mfma_f32_16x16x32_bf16 v[152:155], a[96:99], a[48:51], 0\n" \
"v_mfma_f32_16x16x32_bf16 v[152:155], a[100:103], a[52:55], v[152:155]\n" \
"v_mfma_f32_16x16x32_bf16 v[152:155], a[104:107], a[56:59], v[152:155]\n" \
"v_mfma_f32_16x16x32_bf16 v[152:155], a[108:111], a[60:63], v[152:155]\n" \
"v_mfma_f32_16x16x32_bf16 v[156:159], a[124:127], a[60:63], 0\n" \
"v_mfma_f32_16x16x32_bf16 v[156:159], a[120:123], a[56:59], v[156:159]\n" \
"v_mfma_f32_16x16x32_bf16 v[156:159], a[116:119], a[52:55], v[156:159]\n" \
"v_mfma_f32_16x16x32_bf16 v[156:159], a[112:115], a[48:51], v[156:159]\n" \
"s_nop 7\n" \
"s_nop 0\n" \
"v_max3_f32 v230, v128, v129, v130\n" \
"v_max3_f32 v231, v131, v132, v133\n" \
"v_max3_f32 v230, v230, v134, v135\n" \
"v_max3_f32 v225, v230, v231, v225\n" \
"v_max3_f32 v230, v136, v137, v138\n" \
"v_max3_f32 v231, v139, v140, v141\n" \
"v_max3_f32 v230, v230, v142, v143\n" \
"v_max3_f32 v226, v230, v231, v226\n" \
"v_max3_f32 v230, v144, v145, v146\n" \
"v_max3_f32 v231, v147, v148, v149\n" \
"v_max3_f32 v230, v230, v150, v151\n" \
"v_max3_f32 v227, v230, v231, v227\n" \
"v_max3_f32 v230, v152, v153, v154\n" \
"v_max3_f32 v231, v155, v156, v157\n" \
"v_max3_f32 v230, v230, v158, v159\n" \
"v_max3_f32 v228, v230, v231, v228\n" \
"s_nop 3\n" \
"s_add_u32 s48, s48, s54\n" \
"s_sub_u32 s51, s51, 1\n" \
"s_cmp_lg_u32 s51, 0\n" \
"s_cbranch_scc1 Lsl_%=\n" \
"s_nop 0\n" \
"v_mov_b32_e32 v232, v225\n" \
"v_mov_b32_e32 v233, v226\n" \
"v_mov_b32_e32 v234, v227\n" \
"v_mov_b32_e32 v235, v228\n" \
"s_nop 0\n" \
"v_permlane16_swap_b32_e32 v225, v232\n" \
"v_permlane16_swap_b32_e32 v226, v233\n" \
"v_permlane16_swap_b32_e32 v227, v234\n" \
"v_permlane16_swap_b32_e32 v228, v235\n" \
"v_max_f32_e32 v225, v225, v232\n" \
"v_max_f32_e32 v226, v226, v233\n" \
"v_max_f32_e32 v227, v227, v234\n" \
"v_max_f32_e32 v228, v228, v235\n" \
"v_mov_b32_e32 v232, v225\n" \
"v_mov_b32_e32 v233, v226\n" \
"v_mov_b32_e32 v234, v227\n" \
"v_mov_b32_e32 v235, v228\n" \
"s_nop 0\n" \
"v_permlane32_swap_b32_e32 v225, v232\n" \
"v_permlane32_swap_b32_e32 v226, v233\n" \
"v_permlane32_swap_b32_e32 v227, v234\n" \
"v_permlane32_swap_b32_e32 v228, v235\n" \
"v_max_f32_e32 v225, v225, v232\n" \
"v_max_f32_e32 v226, v226, v233\n" \
"v_max_f32_e32 v227, v227, v234\n" \
"v_max_f32_e32 v228, v228, v235\n" \
"s_cmp_lg_u32 s69, 0\n" \
"s_cbranch_scc1 Lnewm_%=\n" \
"v_sub_f32_e32 v225, 0, v160\n" \
"v_sub_f32_e32 v226, 0, v164\n" \
"v_sub_f32_e64 v227, 0, v168\n" \
"v_sub_f32_e32 v228, 0, v172\n" \
"Lnewm_%=:\n" \
"s_mov_b32 s48, 0\n" \
"s_mov_b32 m0, s40\n" \
"s_nop 0\n" \
"buffer_load_dwordx4 %[kd0], %[srdk], s48 offen offset:0 lds\n" \
"buffer_load_dwordx4 v212, %[srdk], s48 offen offset:1024 lds\n" \
"buffer_load_dwordx4 v213, %[srdk], s48 offen offset:2048 lds\n" \
"buffer_load_dwordx4 v214, %[srdk], s48 offen offset:3072 lds\n" \
"s_mov_b32 s48, 16384\n" \
"s_mov_b32 m0, s41\n" \
"s_nop 0\n" \
"buffer_load_dwordx4 %[kd0], %[srdk], s48 offen offset:0 lds\n" \
"buffer_load_dwordx4 v212, %[srdk], s48 offen offset:1024 lds\n" \
"buffer_load_dwordx4 v213, %[srdk], s48 offen offset:2048 lds\n" \
"buffer_load_dwordx4 v214, %[srdk], s48 offen offset:3072 lds\n" \
"s_add_u32 m0, %[m0b], 65536\n" \
"s_nop 0\n" \
"s_nop 0\n" \
"buffer_load_dwordx4 %[vd0], %[srdv], s48 offen offset:0 lds\n" \
"buffer_load_dwordx4 v215, %[srdv], s48 offen offset:1024 lds\n" \
"buffer_load_dwordx4 %[vd0], %[srdv], s48 offen offset:2048 lds\n" \
"buffer_load_dwordx4 v215, %[srdv], s48 offen offset:3072 lds\n" \
"s_mov_b32 s48, 32768\n" \
"s_mov_b32 m0, s42\n" \
"s_nop 0\n" \
"buffer_load_dwordx4 %[kd0], %[srdk], s48 offen offset:0 lds\n" \
"buffer_load_dwordx4 v212, %[srdk], s48 offen offset:1024 lds\n" \
"buffer_load_dwordx4 v213, %[srdk], s48 offen offset:2048 lds\n" \
"buffer_load_dwordx4 v214, %[srdk], s48 offen offset:3072 lds\n" \
"s_add_u32 m0, %[m0b], 81920\n" \
"s_nop 0\n" \
"s_nop 0\n" \
"buffer_load_dwordx4 %[vd0], %[srdv], s48 offen offset:0 lds\n" \
"buffer_load_dwordx4 v215, %[srdv], s48 offen offset:1024 lds\n" \
"buffer_load_dwordx4 %[vd0], %[srdv], s48 offen offset:2048 lds\n" \
"buffer_load_dwordx4 v215, %[srdv], s48 offen offset:3072 lds\n" \
"s_mov_b32 s48, 49152\n" \
"s_mov_b32 m0, s43\n" \
"s_nop 0\n" \
"buffer_load_dwordx4 %[kd0], %[srdk], s48 offen offset:0 lds\n" \
"buffer_load_dwordx4 v212, %[srdk], s48 offen offset:1024 lds\n" \
"buffer_load_dwordx4 v213, %[srdk], s48 offen offset:2048 lds\n" \
"buffer_load_dwordx4 v214, %[srdk], s48 offen offset:3072 lds\n" \
"s_add_u32 m0, %[m0b], 98304\n" \
"s_nop 0\n" \
"s_nop 0\n" \
"buffer_load_dwordx4 %[vd0], %[srdv], s48 offen offset:0 lds\n" \
"buffer_load_dwordx4 v215, %[srdv], s48 offen offset:1024 lds\n" \
"buffer_load_dwordx4 %[vd0], %[srdv], s48 offen offset:2048 lds\n" \
"buffer_load_dwordx4 v215, %[srdv], s48 offen offset:3072 lds\n" \
"s_waitcnt vmcnt(0)\n" \
"s_barrier\n" \
"ds_read_b128 a[64:67], %[kb0] offset:0\n" \
"ds_read_b128 a[68:71], v208 offset:0\n" \
"ds_read_b128 a[72:75], v209 offset:0\n" \
"ds_read_b128 a[76:79], v210 offset:0\n" \
"ds_read_b128 a[92:95], v210 offset:4096\n" \
"ds_read_b128 a[88:91], v209 offset:4096\n" \
"ds_read_b128 a[84:87], v208 offset:4096\n" \
"ds_read_b128 a[80:83], %[kb0] offset:4096\n" \
"s_mov_b32 s48, 65536\n" \
"s_mov_b32 s60, 0\n" \
"s_waitcnt lgkmcnt(0)\n" \
"v_mfma_f32_16x16x32_bf16 v[128:131], a[64:67], a[0:3], 0\n" \
"ds_read_b128 a[96:99], %[kb0] offset:8192\n" \
"v_mfma_f32_16x16x32_bf16 v[128:131], a[68:71], a[4:7], v[128:131]\n" \
"ds_read_b128 a[100:103], v208 offset:8192\n" \
"v_mfma_f32_16x16x32_bf16 v[128:131], a[72:75], a[8:11], v[128:131]\n" \
"ds_read_b128 a[104:107], v209 offset:8192\n" \
"v_mfma_f32_16x16x32_bf16 v[128:131], a[76:79], a[12:15], v[128:131]\n" \
"ds_read_b128 a[108:111], v210 offset:8192\n" \
"v_mfma_f32_16x16x32_bf16 v[132:135], a[92:95], a[12:15], 0\n" \
"ds_read_b128 a[124:127], v210 offset:12288\n" \
"v_mfma_f32_16x16x32_bf16 v[132:135], a[88:91], a[8:11], v[132:135]\n" \
"ds_read_b128 a[120:123], v209 offset:12288\n" \
"v_mfma_f32_16x16x32_bf16 v[132:135], a[84:87], a[4:7], v[132:135]\n" \
"ds_read_b128 a[116:119], v208 offset:12288\n" \
"v_mfma_f32_16x16x32_bf16 v[132:135], a[80:83], a[0:3], v[132:135]\n" \
"ds_read_b128 a[112:115], %[kb0] offset:12288\n" \
"v_mfma_f32_16x16x32_bf16 v[136:139], a[64:67], a[16:19], 0\n" \
"ds_read_b64_tr_b16 a[128:129], %[vvb] offset:0\n" \
"v_mfma_f32_16x16x32_bf16 v[136:139], a[68:71], a[20:23], v[136:139]\n" \
"ds_read_b64_tr_b16 a[130:131], %[vvb] offset:4096\n" \
"v_mfma_f32_16x16x32_bf16 v[136:139], a[72:75], a[24:27], v[136:139]\n" \
"ds_read_b64_tr_b16 a[132:133], v211 offset:0\n" \
"v_mfma_f32_16x16x32_bf16 v[136:139], a[76:79], a[28:31], v[136:139]\n" \
"ds_read_b64_tr_b16 a[134:135], v211 offset:4096\n" \
"v_mfma_f32_16x16x32_bf16 v[140:143], a[92:95], a[28:31], 0\n" \
"ds_read_b64_tr_b16 a[136:137], %[vvb] offset:512\n" \
"v_mfma_f32_16x16x32_bf16 v[140:143], a[88:91], a[24:27], v[140:143]\n" \
"ds_read_b64_tr_b16 a[138:139], %[vvb] offset:4608\n" \
"v_mfma_f32_16x16x32_bf16 v[140:143], a[84:87], a[20:23], v[140:143]\n" \
"ds_read_b64_tr_b16 a[140:141], v211 offset:512\n" \
"v_mfma_f32_16x16x32_bf16 v[140:143], a[80:83], a[16:19], v[140:143]\n" \
"ds_read_b64_tr_b16 a[142:143], v211 offset:4608\n" \
"v_mfma_f32_16x16x32_bf16 v[144:147], a[64:67], a[32:35], 0\n" \
"ds_read_b64_tr_b16 a[144:145], %[vvb] offset:1024\n" \
"v_mfma_f32_16x16x32_bf16 v[144:147], a[68:71], a[36:39], v[144:147]\n" \
"ds_read_b64_tr_b16 a[146:147], %[vvb] offset:5120\n" \
"v_mfma_f32_16x16x32_bf16 v[144:147], a[72:75], a[40:43], v[144:147]\n" \
"ds_read_b64_tr_b16 a[148:149], v211 offset:1024\n" \
"v_mfma_f32_16x16x32_bf16 v[144:147], a[76:79], a[44:47], v[144:147]\n" \
"ds_read_b64_tr_b16 a[150:151], v211 offset:5120\n" \
"v_mfma_f32_16x16x32_bf16 v[148:151], a[92:95], a[44:47], 0\n" \
"ds_read_b64_tr_b16 a[152:153], %[vvb] offset:1536\n" \
"v_mfma_f32_16x16x32_bf16 v[148:151], a[88:91], a[40:43], v[148:151]\n" \
"ds_read_b64_tr_b16 a[154:155], %[vvb] offset:5632\n" \
"v_mfma_f32_16x16x32_bf16 v[148:151], a[84:87], a[36:39], v[148:151]\n" \
"ds_read_b64_tr_b16 a[156:157], v211 offset:1536\n" \
"v_mfma_f32_16x16x32_bf16 v[148:151], a[80:83], a[32:35], v[148:151]\n" \
"ds_read_b64_tr_b16 a[158:159], v211 offset:5632\n" \
"v_mfma_f32_16x16x32_bf16 v[152:155], a[64:67], a[48:51], 0\n" \
"v_mfma_f32_16x16x32_bf16 v[152:155], a[68:71], a[52:55], v[152:155]\n" \
"v_mfma_f32_16x16x32_bf16 v[152:155], a[72:75], a[56:59], v[152:155]\n" \
"v_mfma_f32_16x16x32_bf16 v[152:155], a[76:79], a[60:63], v[152:155]\n" \
"v_mfma_f32_16x16x32_bf16 v[156:159], a[92:95], a[60:63], 0\n" \
"v_mfma_f32_16x16x32_bf16 v[156:159], a[88:91], a[56:59], v[156:159]\n" \
"v_mfma_f32_16x16x32_bf16 v[156:159], a[84:87], a[52:55], v[156:159]\n" \
"v_mfma_f32_16x16x32_bf16 v[156:159], a[80:83], a[48:51], v[156:159]\n" \
"s_branch Lhavem_%=\n" \
"s_nop 0\n" \
".p2align 12\n" \
"adaln_pg0:\n" \
"s_cbranch_execz adaln_pg1\n" \
"Lend_%=:\n" \
""

#define SRD_WORD3 0x00020000
DI u32x4 ld16_sc1(const void* p) { u32x4 v; asm volatile("global_load_dwordx4 %0, %1, off sc1" : "=v"(v) : "v"(p) : "memory"); return v; }
DI unsigned ord_key(float r, unsigned row) {
  const unsigned b = __float_as_uint(r), o = (b & 0x80000000u) ? ~b : (b | 0x80000000u);
  return max(o & 0xfffff800u, 0x800u) | (2047u - row);
}
DI void top2_merge(unsigned& a1, unsigned& a2, unsigned b1, unsigned b2) {
  const unsigned n1 = max(a1, b1), n2 = max(min(a1, b1), max(a2, b2)); a1 = n1; a2 = n2;
}
template <int CTRL> DI void top2_dpp(unsigned& k1, unsigned& k2) {
  const unsigned p1 = __builtin_amdgcn_update_dpp(0u, k1, CTRL, 0xf, 0xf, true), p2 = __builtin_amdgcn_update_dpp(0u, k2, CTRL, 0xf, 0xf, true); top2_merge(k1, k2, p1, p2);
}
DI float row_sum(float v) {
  v = dpp_add<DPP_XOR1>(v); v = dpp_add<DPP_XOR2>(v); v = dpp_add<DPP_ROR8>(v);
  v = swap16_add(v, v); return swap32_add(v, v);
}
DI unsigned h2_pair(float x0, float x1, float m, float rden, unsigned c1w, unsigned sbw) { return ln_pair(x0, x1, m, rden, lo16(c1w), hi16(c1w), lo16(sbw), hi16(sbw)); }
#define PAGE(n, nx) asm volatile("s_branch adaln_pe" #n "\n.p2align 12\nadaln_pg" #n ":\n\ts_cbranch_execz " nx "\nadaln_pe" #n ":")
#define STR_(x) #x
#define STR(x) STR_(x)
constexpr int MOE_WGS = DFF / 16;
constexpr unsigned RETRY_CAP = 4096;
#define ASLEEP 2
#define KLOOK 6
#define YSPLIT 8
#define FSLEEP 14
#define KSLEEP 3
#define YEARLY 8
#define FREST 0
#define KL0 1u
#define KLS 2u
#define KST 2u
#define KCHK 8
#define KPAUSE 2
#define KEY_SEL b

#define WBYTES (moe ? DM * DFF * 2 : 0)
namespace fa {
constexpr unsigned LDS_BYTES = 163840;
constexpr float QSCALE = 0.12751743082f;
struct Args { const u16 *x, *wmod, *wq, *kc, *vc, *wo, *wg, *wu, *wd, *c, *wr; char* ws; u16* out;
};
__global__ __launch_bounds__(256, 1) void fattn_k(const Args p) {
  extern __shared__ __attribute__((aligned(16))) char lds[];
  const unsigned wid = __builtin_amdgcn_readfirstlane(threadIdx.x >> 6);
  const unsigned b = blockIdx.x, idx = b >> 3, kg = b & 3u, m = idx & 7u, hd = 8u * kg + m, y = (b & 4u) + (idx >> 3);
  const unsigned ldsb = (unsigned)(size_t)lds;
  const unsigned m0b = ldsb + 4096u * wid, m0t = ldsb + 131072u + 8192u * wid;
  const __amdgpu_buffer_rsrc_t srdz = __builtin_amdgcn_make_buffer_rsrc((void*)p.ws, (short)0, (int)WS_END, SRD_WORD3);
  const bool moe = b < MOE_WGS;
  const unsigned sl = 4u * ((b & 7u) * 7u + (idx >> 2)) + (idx & 3u), yp2 = (y & 1u) * 2u;
  const unsigned sin = (unsigned)WS_XIN + ((y * 32u + hd) * 4u + wid) * BOX;
  u32x2 xq[8]; unsigned fl, sd0, got, got2; unsigned long long tag; u16* pout;
  f32x2 xf[16]; f32x4 gaf[8], aw[8]; u32x4 alo, ahi; unsigned late; float arow;
#define FQ(i)
#define TQ(i)
  {
    const unsigned tid = threadIdx.x, lane = tid & 63u, l16 = lane & 15u, g = lane >> 4, hi = lane >> 5, r8 = (lane >> 2) & 7u;
    unsigned kb0 = ldsb + 256u * l16 + 16u * (g ^ l16);
    unsigned vvb = ldsb + 65536u + 2048u * hi + 288u * (g & 1u) + 64u * ((lane >> 2) & 3u) + 8u * (lane & 3u);
    unsigned vwb = ldsb + 65536u + 2048u * g + 64u * ((lane >> 2) & 3u) + 16u * (lane & 3u);
    unsigned kd0 = 4096u * wid + 256u * g + 16u * (l16 ^ g);
    unsigned vd0 = 4096u * wid + 256u * r8 + 64u * hi + 16u * ((lane & 3u) ^ (2u * (r8 >> 2))) + 1024u;
    unsigned wd0 = 8192u * (16u * wid + r8) + 64u * hi + 16u * (lane & 3u) + 4096u;
    unsigned hq0 = (unsigned)WS_H + (16u * y + 4u * wid) * 32768u + 16u * lane;
    unsigned oq0 = (unsigned)WS_XIN + ((y * 32u + kg * 8u) * 4u + wid) * BOX + 20480u + 16u * lane;
    unsigned flag = ldsb + 49152u + 4u * wid, wbytes = moe ? DM * DFF * 2 : 0;
    const __amdgpu_buffer_rsrc_t srdw = __builtin_amdgcn_make_buffer_rsrc((void*)((const char*)p.wq + 256u * hd - 4096), (short)0, (int)(DM * DQ * 2 - 256u * hd + 4096u), SRD_WORD3);
    __amdgpu_buffer_rsrc_t srdk = __builtin_amdgcn_make_buffer_rsrc((void*)((const char*)p.kc + (size_t)hd * (SEQ * 256)), (short)0, SEQ * 256, SRD_WORD3);
    __amdgpu_buffer_rsrc_t srdv = __builtin_amdgcn_make_buffer_rsrc((void*)((const char*)p.vc + (size_t)hd * (SEQ * 256) - 17408), (short)0, SEQ * 256 + 17408, SRD_WORD3);
    unsigned long long pct; unsigned clen;
    asm volatile("s_getpc_b64 %0\n\ts_mov_b32 %1, adaln_text_end@rel32@lo+4" : "=s"(pct), "=s"(clen));
    const unsigned vct = min(((idx * 4u + wid) * 6u + min(lane, 5u)) * 128u, (clen - 4u) & ~3u);
      const unsigned gr = 128u * (b & 7u) + 4u * (tid >> 3), gc = 64u * (b >> 3) + 8u * (tid & 7u);
      u32x4 w[4], xs[4]; u32x2 ep, cv;
#define XV(i) ({ u32x4 v_; asm volatile("v_accvgpr_read_b32 %0, a[%4]\n\tv_accvgpr_read_b32 %1, a[%4 + 1]\n\tv_accvgpr_read_b32 %2, a[%4 + 2]\n\tv_accvgpr_read_b32 %3, a[%4 + 3]" : "=v"(v_[0]), "=v"(v_[1]), "=v"(v_[2]), "=v"(v_[3]) : "n"(200 + 4 * (i))); v_; })
      asm volatile("global_load_dwordx2 %0, %1, %2" : "=v"(cv) : "v"(gr * 2u), "s"(p.c) : "memory");
      #pragma unroll
      for (int j = 0; j < 4; ++j) asm volatile("global_load_dwordx4 %0, %1, %2 nt" : "=v"(w[j]) : "v"(((gr + j) * NMOD + gc) * 2u), "s"(p.wmod) : "memory");
      #pragma unroll
      for (int q = 0; q < 4; ++q) asm volatile("global_load_dwordx4 %0, %1, %2 offset:%3 nt" : "=v"(xs[q]) : "v"((8u * (y * 32u + hd) + 2u * wid) * 2048u + 16u * lane), "s"(p.x), "n"(1024 * q) : "memory");
      asm volatile("global_load_dwordx2 %0, %1, %2 sc1" : "=v"(ep) : "v"(0u), "s"(p.ws + WS_EP) : "memory");
      asm volatile("buffer_store_dwordx4 %0, %1, %2, 0 offen sc1\n\ts_nop 1" :: "v"(u32x4{0u, 0u, 0u, 0u}), "v"(tid < Z_SHARE / 16u ? (unsigned)WS_Z + b * Z_SHARE + tid * 16u : 0x80000000u), "s"(srdz) : "memory");
      FQ(6);
      asm volatile("global_load_dword a255, %0, %1" :: "v"(vct), "s"(pct) : "memory");
      asm volatile("s_mov_b64 exec, 0\n\ts_cmp_eq_u32 %0, 1\n\ts_cbranch_scc1 adaln_pg5\n\ts_cmp_eq_u32 %0, 2\n\ts_cbranch_scc1 adaln_pg6\n\ts_cmp_eq_u32 %0, 3\n\ts_cbranch_scc1 adaln_pg7\nadaln_fr:\n\ts_mov_b64 exec, -1" :: "s"(wid) : "scc");
      FQ(7);
      unsigned tq0 = (idx * 8u + wid * 2u + ((lane >> 3) & 1u)) * 8192u + (b & 4u) * 256u + (lane & 7u) * 128u + 4096u - 256u * m;
      const unsigned rb0 = 16u * y + 4u * (hd >> 3), ch = 16u * (hd & 7u) + (tid >> 4);
      unsigned vrw = ldsb + 135168u + wid * 256u + 32u * (tid & 7u) + 8u * g, vrr = ldsb + 135168u + 4u * lane;
      unsigned vmo = wid ? 0x80000000u : (unsigned)WS_MOD + b * 512u + lane * 8u, vst = (lane & 31u) ? 0x80000000u : (unsigned)WS_RST + (8u * (y * 32u + hd) + 2u * wid + hi) * 16u;
      unsigned vx2 = (rb0 * 16u + l16) * 2048u + ch * 16u;
      pout = p.out;
      asm volatile("s_waitcnt vmcnt(11)" : "+v"(cv), "+v"(tq0), "+v"(vrw), "+v"(vrr), "+v"(vmo), "+v"(vst), "+v"(vx2), "+s"(pout) :: "memory");
      auto silu2 = [](unsigned v) { const f32x2 f = unpk(v); return cvtpk(f[0] * __builtin_amdgcn_rcpf(1.f + __builtin_amdgcn_exp2f(f[0] * -1.44269504089f)), f[1] * __builtin_amdgcn_rcpf(1.f + __builtin_amdgcn_exp2f(f[1] * -1.44269504089f))); };
      auto dot2 = [](unsigned x, unsigned z, float c) { return __builtin_amdgcn_fdot2_f32_bf16(__builtin_bit_cast(bf16x2, x), __builtin_bit_cast(bf16x2, z), c, false); };
      unsigned s01 = silu2(cv[0]), s23 = silu2(cv[1]);
      asm volatile("s_waitcnt vmcnt(7)" : "+v"(s01), "+v"(s23), "+v"(w[0]), "+v"(w[1]), "+v"(w[2]), "+v"(w[3]) :: "memory");
      FQ(4);
      asm volatile("buffer_load_dword a255, %0, %1, 0 offen" :: "v"(tq0), "s"(srdw) : "memory");
      {
        float acc[8];
        #pragma unroll
        for (int e = 0; e < 4; ++e) {
          acc[2 * e]     = dot2(__builtin_amdgcn_perm(w[3][e], w[2][e], 0x05040100u), s23, dot2(__builtin_amdgcn_perm(w[1][e], w[0][e], 0x05040100u), s01, 0.f));
          acc[2 * e + 1] = dot2(__builtin_amdgcn_perm(w[3][e], w[2][e], 0x07060302u), s23, dot2(__builtin_amdgcn_perm(w[1][e], w[0][e], 0x07060302u), s01, 0.f));
        }
        float t[4], u[2];
        #pragma unroll
        for (int e = 0; e < 4; ++e) t[e] = swap32_add(acc[e], acc[e + 4]);
        #pragma unroll
        for (int e = 0; e < 2; ++e) u[e] = dpp_add<DPP_ROR8>(swap16_add(t[e], t[e + 2]));
        asm volatile("ds_write_b64 %5, %6\n\ts_waitcnt vmcnt(3)" : "+v"(xs[0]), "+v"(xs[1]), "+v"(xs[2]), "+v"(xs[3]), "+v"(ep) : "v"(vrw), "v"(f32x2{u[0], u[1]}) : "memory");
      }
      tag = (((unsigned long long)(unsigned)__builtin_amdgcn_readfirstlane(ep[1]) << 32) | (unsigned)__builtin_amdgcn_readfirstlane(ep[0])) + 1ull;
      const unsigned tl = (unsigned)tag & 0xffu, th = (unsigned)(tag >> 8);
      u32x4 sg;
      {
        float t4[4] = {0.f, 0.f, 0.f, 0.f};
        #pragma unroll
        for (int e = 0; e < 16; ++e) { const unsigned xw = xs[e >> 2][e & 3]; t4[2 * (e >> 3)] = dot2(xw, 0x3f803f80u, t4[2 * (e >> 3)]); t4[2 * (e >> 3) + 1] = dot2(xw, xw, t4[2 * (e >> 3) + 1]); }
        float u = swap16_add(swap32_add(t4[0], t4[2]), swap32_add(t4[1], t4[3]));
        u = dpp_add<DPP_XOR1>(u); u = dpp_add<DPP_XOR2>(u); u = dpp_add<DPP_ROR4>(u); u = dpp_add<DPP_ROR8>(u);
        const auto r16 = __builtin_amdgcn_permlane16_swap(__float_as_uint(u), __float_as_uint(u), false, false);
        const float mean = __uint_as_float(r16[0]) * (1.f / DM), m2 = mean * mean;
        float var = fmaf(__uint_as_float(r16[1]), 1.f / DM, -m2);
        if (__builtin_expect(__any(!(m2 <= var)), 0)) {
          const f32x2 mm = {__uint_as_float(__builtin_amdgcn_readlane(__float_as_uint(mean), 0)), __uint_as_float(__builtin_amdgcn_readlane(__float_as_uint(mean), 32))};
          f32x2 sq = {0.f, 0.f};
          #pragma unroll
          for (int e = 0; e < 8; ++e) {
            const f32x2 d0 = f32x2{lo16(xs[e >> 2][e & 3]), lo16(xs[2 + (e >> 2)][e & 3])} - mm, d1 = f32x2{hi16(xs[e >> 2][e & 3]), hi16(xs[2 + (e >> 2)][e & 3])} - mm;
            sq = __builtin_elementwise_fma(d1, d1, __builtin_elementwise_fma(d0, d0, sq));
          }
          const float v0 = wave_sum(sq[0]), v1 = wave_sum(sq[1]);
          var = (hi ? v1 : v0) * (1.f / DM);
        }
        sg = u32x4{cvtpk(0.f, mean) | tl, th, cvtpk(0.f, __builtin_amdgcn_sqrtf(rbh(rbh(var) + 1e-5f))) | tl, th};
      }
      {
        f32x2 ra, rc;
        asm volatile("s_waitcnt vmcnt(2) lgkmcnt(0)\n\ts_barrier\n\tds_read2st64_b32 %0, %2 offset1:1\n\tds_read2st64_b32 %1, %2 offset0:2 offset1:3\n\t"
                     "buffer_store_dwordx4 %3, %4, %5, 0 offen sc1\n\ts_waitcnt lgkmcnt(0)" : "=&v"(ra), "=&v"(rc) : "v"(vrr), "v"(sg), "v"(vst), "s"(srdz) : "memory");
        ra += rc;
        asm volatile("buffer_store_dwordx2 %0, %1, %2, 0 offen sc1" :: "v"(u32x2{((__float_as_uint(ra[0] + ra[1]) + 0x80u) & 0xffffff00u) | tl, th}), "v"(vmo), "s"(srdz) : "memory");
      }
      FQ(5);
      asm volatile("global_load_dwordx2 a[216:217], %0, %1" :: "v"(tid * 8u), "s"(p.c) : "memory");
      #pragma unroll
      for (int i = 0; i < 4; ++i) asm volatile("global_load_dwordx4 a[%2:%2 + 3], %0, %1" :: "v"(vx2 + i * 32768u), "s"(p.x), "n"(200 + 4 * i) : "memory");
      PAGE(5, "adaln_fr");
      unsigned lane2 = lane; asm volatile("" : "+v"(lane2));
#define lane lane2
    unsigned wo0 = (32u * wid + r8) * 2048u + m * 256u + 64u * hi + 16u * ((lane & 3u) ^ (2u * (r8 >> 2)));
    __amdgpu_buffer_rsrc_t srdt = __builtin_amdgcn_make_buffer_rsrc((void*)((const char*)p.wo + (size_t)kg * (DM * DM * 2)), (short)0, DM * DM * 2, SRD_WORD3);
    __amdgpu_buffer_rsrc_t srdp = __builtin_amdgcn_make_buffer_rsrc((void*)((const char*)(y < 4u ? p.kc : p.vc) + (size_t)hd * (SEQ * 256) + (y & 3u) * 4096u), (short)0, (int)(SEQ * 256 - (y & 3u) * 4096u), SRD_WORD3);
    unsigned vg0 = (256u * wid + (lane >> 1)) * (DFF * 2u) + sl * 32u + 16u * (lane & 1u);
    unsigned vdn = (16u * sl + hi) * 2048u + 512u * wid + 16u * (lane & 31u);
    const unsigned mc0 = 2u * DM + 64u * ((idx >> 2) * 8u + (b & 7u)) + 16u * (idx & 3u);
    const unsigned md0 = (256u * wid + (lane >> 1)) * (NMOD * 2u) + mc0 * 2u + 16u * (lane & 1u);
    sd0 = ldsb + 131072u + 8192u + 512u * wid + 16u * g;
    fl = md0;
    unsigned rd0 = ldsb + 131072u + 7168u + 8192u * ((lane >> 2) & 3u) + 256u * wid + 4u * (lane & 3u);
    unsigned ta0 = m0t + 256u * g + 32u * ((lane >> 2) & 3u) + 8u * (lane & 3u);
    const unsigned c8 = (b * 4u + wid) * 8192u, cd = moe ? sl * 32768u + wid * 8192u : 0u, cw = (idx * 4u + wid) * 16384u;
    const unsigned cm = moe ? ((sl & 3u) * 4u + wid) * 64u * (DFF * 2u) + (sl >> 2) * 128u : 0u;
    unsigned vxs = BOX * (l16 >> 1) + 64u * (l16 & 1u) + 16u * g;
    const unsigned sxs = (unsigned)WS_XIN + (y * 128u + wid * 32u) * BOX + (m >> 1) * 1024u + kg * 256u + (m & 1u) * 128u;
    unsigned vx0 = ((256u * y + 8u * hd + 2u * wid + ((lane >> 2) & 1u)) * DM + 256u * hi + 128u * (l16 >> 3) + 16u * (g & 1u) + 4u * (lane & 3u) + 32u * yp2) * 2u;
    unsigned tlo = 0, thi = 0;
#define tab(ln, lo, hi2) asm("v_writelane_b32 %0, %2, " #ln "\n\tv_writelane_b32 %1, %3, " #ln : "+v"(tlo), "+v"(thi) : "s"(lo), "s"(hi2))
#define tabp(ln, q, off) do { const unsigned long long a_ = (size_t)(q) + (off); tab(ln, (unsigned)a_, (unsigned)(a_ >> 32)); } while (0)
#define TORD 0
    tabp(1, p.x, ((y * 32u + hd) * 2u + (wid & 1u)) * 8192u); tabp(2, p.wu, moe ? c8 : 0u); tabp(3, p.wo, c8); tabp(4, p.wd, cd);
    tabp(5, p.wo, kg * 2097152u + cw); tabp(6, p.wo, kg * 2097152u + cw + 8192u); tabp(7, p.wg, cm);
    tabp(17, p.wmod, 0u); tab(18, NP_FILL == 8 ? sin + 20480u : (unsigned)WS_XIN + ((y * 32u + kg * 8u + (m >> 1)) * 4u + wid) * BOX + 20480u + (m & 1u) * 8192u, (unsigned)WS_OFL + y * 16384u + kg * 128u);
    tab(19, (unsigned)WS_OFL + (y * 32u + hd) * 512u + wid * 128u, (unsigned)WS_OFL + (y * 32u + kg * 8u) * 512u + wid * 128u);
    tabp(8, p.wg, 0u); tabp(9, p.wu, 0u); tabp(10, p.wd, 0u);
    tab(11, m, m * 4u * BOX); tab(12, (y & 1u) * 128u, 128u - (y & 1u) * 128u);
    tab(13, 0u, (unsigned)WS_PFL + (y * 32u + hd) * 512u + kg * 128u); tab(14, sxs, 0u);
    tab(15, (unsigned)WS_MODT + (mc0 - 2u * DM + 4u * wid) * 4u, (mc0 >> 10) == 4u ? 0x3f800000u : 0u);
    tab(16, (unsigned)WS_PFL + y * 16384u + (hd * 4u + wid) * 4u, (unsigned)WS_PFL + (y * 32u + hd) * 512u + wid * 128u);
    const char* psig = p.ws + WS_CNT + y * 8192u + hd * 8u;
    const char* pcnt = p.ws + WS_CNT + y * 8192u + hd * 256u;
    tabp(20, p.wr, 0u); tab(21, (unsigned)WS_PART + b * 8192u + wid * 2048u, 0u);
      asm volatile("" : "+v"(tlo), "+v"(thi), "+v"(wo0), "+v"(vg0), "+v"(vdn), "+v"(ta0), "+v"(vxs), "+v"(vx0), "+v"(rd0), "+v"(fl), "+v"(sd0), "+v"(kb0), "+v"(vvb), "+v"(vwb), "+v"(kd0), "+v"(vd0), "+v"(wd0), "+v"(hq0), "+v"(oq0));
      asm volatile("" : "+s"(srdk), "+s"(srdv), "+s"(srdt), "+s"(srdp), "+s"(psig), "+s"(pcnt), "+s"(flag), "+s"(wbytes));
#undef lane
      u32x2 mp[8]; u32x4 rs;
      unsigned vlm = ((wid >> 1) * 16u + 2u * (hd & 7u) + (wid & 1u)) * 4096u + lane * 8u, vlr = (unsigned)WS_RST + ((rb0 + wid) * 16u + l16) * 16u;
#define LOOK9 "buffer_load_dwordx2 %0, %[vlm], %[srdz], %[som] offen sc1\n\tbuffer_load_dwordx2 %1, %[vlm], %[srdz], %[som] offen offset:512 sc1\n\tbuffer_load_dwordx2 %2, %[vlm], %[srdz], %[som] offen offset:1024 sc1\n\t" \
              "buffer_load_dwordx2 %3, %[vlm], %[srdz], %[som] offen offset:1536 sc1\n\tbuffer_load_dwordx2 %4, %[vlm], %[srdz], %[som] offen offset:2048 sc1\n\tbuffer_load_dwordx2 %5, %[vlm], %[srdz], %[som] offen offset:2560 sc1\n\t" \
              "buffer_load_dwordx2 %6, %[vlm], %[srdz], %[som] offen offset:3072 sc1\n\tbuffer_load_dwordx2 %7, %[vlm], %[srdz], %[som] offen offset:3584 sc1\n\tbuffer_load_dwordx4 %8, %[vlr], %[srdz], 0 offen sc1\n\t"
#define LREG(c) c "{v[0:1]}"(mp[0]), c "{v[2:3]}"(mp[1]), c "{v[4:5]}"(mp[2]), c "{v[6:7]}"(mp[3]), c "{v[8:9]}"(mp[4]), c "{v[10:11]}"(mp[5]), c "{v[12:13]}"(mp[6]), c "{v[14:15]}"(mp[7]), c "{v[16:19]}"(rs)
      asm volatile(LOOK9 : LREG("=&") : [vlm] "v"(vlm), [vlr] "v"(vlr), [srdz] "s"(srdz), [som] "s"((unsigned)WS_MOD) : "memory");
      #pragma unroll
      for (int q = 0; q < 14; ++q)
        asm volatile("s_mov_b32 m0, %0\n\ts_nop 0\n\tbuffer_load_dwordx4 %1, %2, %3 offen lds" :: "s"(m0b + 65536u + 16384u * (q >> 2) + 1024u * (q & 3)), "v"(wd0), "s"(srdw),
                     "s"(0x80000u * (q >> 2) + 65536u * ((q >> 1) & 1) + 128u * (q & 1)) : "m0", "memory");
      asm volatile("s_waitcnt vmcnt(23)" ::: "memory");
      { const u32x4 cs = XV(4); *(u32x2*)(lds + 131072 + 8192 + tid * 8u) = u32x2{silu2(cs[0]), silu2(cs[1])}; }
      f32x2 xu[16];
      #pragma unroll
      for (int i = 0; i < 4; ++i) { const u32x4 o = XV(i);
        #pragma unroll
        for (int e = 0; e < 4; ++e) { xu[4 * i + e] = unpk(o[e]); asm volatile("" : "+v"(xu[4 * i + e])); } }
      unsigned vpw = ldsb + 131072u + 4u * tid, vpr = ldsb + 131072u + 1024u + 32u * l16 + 8u * wid, vpc = ldsb + 131072u + 32u * (tid >> 4), vpq = ldsb + 131072u + 1024u + 32u * l16;
      unsigned vh = (unsigned)WS_H + rb0 * 32768u + ch * 256u + 16u * l16; float one = wid >> 1 ? 1.f : 0.f;
      {
        unsigned bl, bh, cnt = SPIN_CAP;
#define XO(d, r, t) "v_bitop3_b32 %[" #d "], v" #r ", %[" #t "], %[" #d "] bitop3:0xbe\n\t"
        asm volatile(
          "s_waitcnt vmcnt(14)\n\t"
          "s_branch Lm1_%=\n"
          "Lm0_%=:\n\t"
          "s_sleep " STR(ASLEEP) "\n\t"
          LOOK9
          "s_waitcnt vmcnt(0)\n"
          "Lm1_%=:\n\t"
          "v_bitop3_b32 %[bl], v0, v2, %[tl] bitop3:0x7e\n\t"
          "v_bitop3_b32 %[bh], v1, v3, %[th] bitop3:0x7e\n\t"
          XO(bl, 4, tl) XO(bh, 5, th) XO(bl, 6, tl) XO(bh, 7, th) XO(bl, 8, tl) XO(bh, 9, th) XO(bl, 10, tl) XO(bh, 11, th) XO(bl, 12, tl) XO(bh, 13, th) XO(bl, 14, tl) XO(bh, 15, th)
          XO(bl, 16, tl) XO(bh, 17, th) XO(bl, 18, tl) XO(bh, 19, th)
          "v_and_or_b32 %[bl], %[bl], %[ff], %[bh]\n\t"
          "v_cmp_ne_u32 vcc, 0, %[bl]\n\t"
          "s_sub_u32 %[cnt], %[cnt], 1\n\t"
          "s_cbranch_scc1 Lm2_%=\n\t"
          "s_cbranch_vccnz Lm0_%=\n"
          "Lm2_%=:"
          : LREG("+"), [bl] "=&v"(bl), [bh] "=&v"(bh), [cnt] "+s"(cnt), "+v"(vpw), "+v"(vpr), "+v"(vpc), "+v"(vpq), "+v"(vh), "+v"(one)
          : [vlm] "v"(vlm), [vlr] "v"(vlr), [srdz] "s"(srdz), [som] "s"((unsigned)WS_MOD), [tl] "s"(tl), [th] "s"(th), [ff] "v"(0xffu) : "vcc", "scc", "memory");
      }
      f32x4 a0, a1, c0, c1, t0, t1;
      {
        auto val = [&](int k) { return __uint_as_float(mp[k][0] & 0xffffff00u); };
        const float v = rbh(one + rbh(((val(0) + val(1)) + (val(2) + val(3))) + ((val(4) + val(5)) + (val(6) + val(7)))));
        const float rd = __builtin_amdgcn_rcpf(hi16(rs[2]));
        asm volatile("ds_write_b32 %0, %1\n\tds_write_b64 %2, %3" :: "v"(vpw), "v"(v * QSCALE), "v"(vpr), "v"(f32x2{-hi16(rs[0]) * rd, rd}) : "memory");
      }
      PAGE(6, "adaln_fr");
      asm volatile("s_waitcnt lgkmcnt(0)\n\ts_barrier\n\tds_read_b128 %4, %7\n\tds_read_b128 %0, %6\n\tds_read_b128 %2, %6 offset:512\n\tds_read_b128 %1, %6 offset:16\n\tds_read_b128 %3, %6 offset:528\n\tds_read_b128 %5, %7 offset:16\n\t"
                   "buffer_load_dword a255, %8, %9, %10 offen\n\ts_waitcnt lgkmcnt(0)"
                   : "=&v"(a0), "=&v"(a1), "=&v"(c0), "=&v"(c1), "=&v"(t0), "=&v"(t1) : "v"(vpc), "v"(vpq), "v"(tq0), "s"(srdw), "s"(0x200000u) : "memory");
      FQ(0);
      #pragma unroll
      for (int i = 0; i < 4; ++i) {
        const float mn = i < 2 ? t0[2 * i] : t1[2 * i - 4], rd = i < 2 ? t0[2 * i + 1] : t1[2 * i - 3];
        u32x4 o;
        #pragma unroll
        for (int e = 0; e < 4; ++e) {
          const f32x2 sc = e < 2 ? f32x2{c0[2 * e], c0[2 * e + 1]} : f32x2{c1[2 * e - 4], c1[2 * e - 3]}, sh = e < 2 ? f32x2{a0[2 * e], a0[2 * e + 1]} : f32x2{a1[2 * e - 4], a1[2 * e - 3]};
          o[e] = cvtpk2(__builtin_elementwise_fma(__builtin_elementwise_fma(xu[4 * i + e], f32x2{rd, rd}, f32x2{mn, mn}), sc, sh));
        }
        asm volatile("buffer_store_dwordx4 %0, %1, %2, %3 offen sc1\n\ts_nop 1" :: "v"(o), "v"(vh), "s"(srdz), "s"(32768u * i) : "memory");
      }
      FQ(1);
    asm volatile(FA_BODY
      : FA_OUTS
      : [kb0] "v"(kb0), [vvb] "v"(vvb), [kd0] "v"(kd0), [vd0] "v"(vd0), [oq0] "v"(oq0),
        [m0b] "s"(m0b), [flag] "s"(flag), [srdz] "s"(srdz), [srdw] "s"(srdw), [srdk] "s"(srdk), [srdv] "s"(srdv), [m0t] "s"(m0t), [srdt] "s"(srdt), [srdp] "s"(srdp),
        [tag] "s"(tag), [pcnt] "s"(pcnt), [psig] "s"(psig), [wid] "s"(wid), [rd0] "v"(rd0), [wo0] "v"(wo0), [vg0] "v"(vg0), [vx0] "v"(vx0), [px] "s"(p.x), [vct] "v"(vct), [pct] "s"(pct), [sin] "s"(sin), [vdn] "v"(vdn), [ta0] "v"(ta0), [vxs] "v"(vxs),
        [tlo] "v"(tlo), [thi] "v"(thi), [wbytes] "s"(wbytes)
      : FA_CLOBBERS);
  }
#define RDELAYN 24
#define RDELAY2 8
#define RDELAY3 16
#define RCHKN 5
#define EARLYOUT 1
  unsigned lane; asm volatile("v_mbcnt_lo_u32_b32 %0, -1, 0\n\tv_mbcnt_hi_u32_b32 %0, -1, %0" : "=v"(lane));
  const unsigned tid = wid * 64u + lane, n = lane & 15u, g = lane >> 4;
  const unsigned r0 = 256u * y + 8u * hd + 2u * wid, row = r0 + ((n >> 2) & 1u);
  const unsigned colb = 256u * (g >> 1) + 128u * (n >> 3) + 16u * (g & 1u) + 4u * (n & 3u);
  char* par = lds;
  const unsigned cq0 = colb + 32u * yp2, cq1 = colb + 32u * (2u ^ yp2);
  auto parq = [&](int o, int k) { return *(const u32x2*)(par + o + (k >> 2 ? cq1 : cq0) * 2u + 64 * ((k >> 1) & 1) + 1024 * (k & 1)); };
  auto parf = [&](int o, int k) { return *(const f32x4*)(par + o + (k >> 2 ? cq1 : cq0) * 4u + 128 * ((k >> 1) & 1) + 2048 * (k & 1)); };
  if (__builtin_expect(__any(late != 0u), 0)) {
    u32x4 mt[4]; u32x2 wr;
    asm volatile("s_barrier" ::: "memory");
    for (unsigned spins = 0; spins <= SPIN_CAP; ++spins) {
      asm volatile("s_nop 4\n\tglobal_load_dwordx2 %0, %1, %2" : "=v"(wr) : "v"(tid * 8u), "s"(p.wr) : "memory");
      #pragma unroll
      for (int k = 0; k < 4; ++k) asm volatile("buffer_load_dwordx4 %0, %1, %2, %3 offen sc1" : "=v"(mt[k]) : "v"(tid * 16u), "s"(srdz), "s"((unsigned)WS_MODT + 4096u * k) : "memory");
      asm volatile("s_waitcnt vmcnt(0)" : "+v"(mt[0]), "+v"(mt[1]), "+v"(mt[2]), "+v"(mt[3]), "+v"(wr) :: "memory");
      const u32x4 t = mt[0] & mt[1] & mt[2] & mt[3];
      if (__all((t[0] & t[1] & t[2] & t[3] & 1u) != 0u)) break;
    }
    const f32x4 a = __builtin_bit_cast(f32x4, mt[2]) * f32x4{lo16(wr[0]), hi16(wr[0]), lo16(wr[1]), hi16(wr[1])};
    *(u32x4*)(par + tid * 16u) = mt[0]; *(f32x4*)(par + 4096 + tid * 16u) = a;
    #pragma unroll
    for (int k = 1; k < 4; ++k) *(u32x2*)(par + (k == 3 ? 14336 : 6144 + 2048 * k) + tid * 8u) = u32x2{__builtin_amdgcn_perm(mt[k][1], mt[k][0], 0x07060302u), __builtin_amdgcn_perm(mt[k][3], mt[k][2], 0x07060302u)};
    *(float*)(par + 12424 + 16u * wid) = wave_sum((a[0] + a[1]) + (a[2] + a[3]));
    asm volatile("s_waitcnt lgkmcnt(0)\n\ts_barrier" ::: "memory");
    #pragma unroll
    for (int k = 0; k < 8; ++k) { gaf[k] = parf(0, k); aw[k] = parf(4096, k); }
    arow = dpp_add<DPP_XOR2>(dpp_add<DPP_XOR1>(*(const float*)(par + 12424 + 16u * (lane & 3u))));
  }
  unsigned vin = lane * 16u, vin3 = lane * 16u + 12288u, vin2 = lane * 16u + 8192u, kp = (unsigned)WS_KEYS + (KEY_SEL & (KEY_REP - 1u)) * (16u * KSTR) + (lane >> 2) * KSTR + 32u * (lane & 3u);
  TQ(0);
  f32x2 x1[16], s2, d2, q2, sbf[16], gbf[16], c1f[16];
  unsigned vkey, vkb, vkr, nrow, vpub, ta, tu, tb; char* pk;
  {
#define SUM2(R) asm volatile("s_nop 1\n\tv_mfma_f32_16x16x32_bf16 %0, %2, a[" #R ":" #R "+3], 0\n\tv_mfma_f32_16x16x32_bf16 %1, %2, a[" #R "+8:" #R "+11], 0\n\t" \
                             "v_mfma_f32_16x16x32_bf16 %0, %3, a[" #R "+4:" #R "+7], %0\n\tv_mfma_f32_16x16x32_bf16 %1, %3, a[" #R "+12:" #R "+15], %1\n\ts_nop 7" : "=&v"(d[0]), "=&v"(d[1]) : "v"(alo), "v"(ahi))
    auto part_ = [&](int c2, f32x2& sa, f32x2& da, f32x2& qa) __attribute__((always_inline)) {
      f32x4 d[2];
      if (c2 == 0) SUM2(64); else if (c2 == 1) SUM2(80); else if (c2 == 2) SUM2(96); else SUM2(112);
      #pragma unroll
      for (int i = 4 * c2; i < 4 * c2 + 4; ++i) {
        const int e = 2 * (i & 1);
        const f32x2 v = rb2(__builtin_elementwise_fma(f32x2{d[(i >> 1) & 1][e], d[(i >> 1) & 1][e + 1]}, f32x2{gaf[i >> 1][e], gaf[i >> 1][e + 1]}, xf[i]));
        x1[i] = v;
        sa += v; da = __builtin_elementwise_fma(v, f32x2{aw[i >> 1][e], aw[i >> 1][e + 1]}, da); qa = __builtin_elementwise_fma(v, v, qa);
      }
    };
#define RCV4(o, a, b, c, d) \
      "buffer_load_dwordx4 " #a ", %[vin" #o "], %[srdz], %[sin] offen sc1\n\t" \
      "buffer_load_dwordx4 " #b ", %[vin" #o "], %[srdz], %[sin] offen offset:1024 sc1\n\t" \
      "buffer_load_dwordx4 " #c ", %[vin" #o "], %[srdz], %[sin] offen offset:2048 sc1\n\t" \
      "buffer_load_dwordx4 " #d ", %[vin" #o "], %[srdz], %[sin] offen offset:3072 sc1\n\t"
#define RWAIT(lv, w) \
      "Lrq" #lv "_%=:\n\t" \
      "v_mov_b32 " w ", 0\n\t" \
      "s_mov_b32 %[sc], " STR(RCHKN) "\n\t" \
      LOOK2 \
      "Lrs" #lv "_%=:\n\t" \
      "ds_read_b32 %[tmp], %[vsh]\n\t" \
      "s_waitcnt lgkmcnt(0)\n\t" \
      "v_and_or_b32 %[tmp], %[tmp], " #lv ", " w "\n\t" \
      "v_cmp_eq_u32 vcc, 0, %[tmp]\n\t" \
      "s_cbranch_vccz Lrd" #lv "_%=\n\t" \
      "s_sub_u32 %[sc], %[sc], 1\n\t" \
      "s_cbranch_scc0 Lrs" #lv "_%=\n\t" \
      "s_sub_u32 %[cnt], %[cnt], 1\n\t" \
      "s_cselect_b32 %[cnt], 0, %[cnt]\n\t" \
      "s_waitcnt vmcnt(0)\n\t" \
      "v_cmp_eq_u32 vcc, 0, " w "\n\t" \
      "s_cbranch_scc1 Lrl" #lv "_%=\n\t" \
      "s_cbranch_vccnz Lrq" #lv "_%=\n" \
      "Lrd" #lv "_%=:\n\t" \
      "v_mov_b32 %[tmp], " #lv "\n\t" \
      "s_mov_b64 exec, 1\n\t" \
      "ds_or_b32 %[vsh], %[tmp]\n\t" \
      "s_mov_b64 exec, -1\n" \
      "Lrl" #lv "_%=:\n\t"
#define RCV8A RCV4(0, a[64:67], a[68:71], a[72:75], a[76:79]) RCV4(1, a[80:83], a[84:87], a[88:91], a[92:95])
#define RCV4C RCV4(2, a[96:99], a[100:103], a[104:107], a[108:111])
#define RCV4D RCV4(3, a[112:115], a[116:119], a[120:123], a[124:127])
#define RCV8B RCV4C RCV4D
#define LOOK2 "buffer_load_dwordx2 v[228:229], %[vfl], %[srdz], %[sfl] offen offset:64 sc1\n\t"
    unsigned cnt, tmp, sc;
    u32x2 sbq[8], gbq[8], c1q[8];
    #pragma unroll
    for (int k = 0; k < 8; ++k) { sbq[k] = parq(8192, k); c1q[k] = parq(10240, k); gbq[k] = parq(14336, k); }
    #pragma unroll
    for (int i = 0; i < 16; ++i) { sbf[i] = unpk(sbq[i >> 1][i & 1]); c1f[i] = unpk(c1q[i >> 1][i & 1]); asm volatile("" : "+v"(sbf[i]), "+v"(c1f[i])); }
    unsigned vfl = (lane & 31u) * 512u, vin1 = lane * 16u + 4096u; const unsigned sfl = (unsigned)WS_OFL + y * 16384u + kg * 128u;
    PAGE(1, "adaln_pg2");
    asm volatile(
      "s_mov_b32 %[go], 0\n\t"
      "s_cmp_lg_u32 %[got], 0\n\t"
      "s_waitcnt vmcnt(4)\n\t"
      "s_cbranch_scc1 Lrf_%=\n\t"
      "v_cmp_eq_u32 vcc, 0, v229\n\t"
      "s_cbranch_vccnz Lrk_%=\n\t"
      "s_mov_b32 %[got], 2\n\t"
      RCV8A
      "Lrf_%=:\n\t"
      "v_cmp_eq_u32 vcc, 0, v228\n\t"
      "s_cbranch_vccnz Lrk_%=\n\t"
      "s_mov_b32 %[go], 1\n\t"
      RCV4C
      "s_branch Lrn_%=\n"
      "Lrk_%=:\n\t"
      LOOK2
      "Lrn_%=:"
      : "+{v228}"(sd0), "+{v229}"(fl), [got] "+s"(got), [go] "=&s"(got2), [vfl] "+v"(vfl), [vin1] "+v"(vin1), "+v"(gbq[0]), "+v"(gbq[1]), "+v"(gbq[2]), "+v"(gbq[3]), "+v"(gbq[4]), "+v"(gbq[5]), "+v"(gbq[6]), "+v"(gbq[7])
      : [srdz] "s"(srdz), [sfl] "s"(sfl), [sin] "s"(sin), [vin0] "v"(vin), [vin2] "v"(vin2) : "vcc", "scc", "memory");
#define NRE
    #pragma unroll
    for (int i = 0; i < 16; ++i) { gbf[i] = unpk(gbq[i >> 1][i & 1]); asm volatile("" : "+v"(gbf[i])); }
    vkey = lane * (16u * KSTR) + (b >> 4) * KSTR + (b & 15u) * 8u; vkb = ldsb + 12288u + 8u * wid + (lane & 4u); vkr = ldsb + 12288u + 4u * (lane & 7u); nrow = 2047u - row;
    vpub = (unsigned)WS_XIN + (y * 32u + hd) * 4u * BOX + 16384u + (g >> 1) * 512u + ((n >> 3) * 8u + (g & 1u) * 4u + (n & 3u)) * 16u;
    ta = m0b + 65536u + 8u * lane; tu = m0t + 8u * lane; tb = m0b + 98304u + 16384u * (g >> 1) + 2048u * (g & 1u) + 512u * ((lane >> 2) & 3u) + 8u * (lane & 3u);
    pk = p.ws + WS_KEYS;
    s2 = f32x2{0.f, 0.f}; d2 = s2; q2 = s2;
    f32x2 s3 = s2, d3 = s2, q3 = s2;
    asm volatile(
      "s_mov_b32 %[cnt], 0x10000\n\t"
      "s_cmp_lg_u32 %[go], 0\n\t"
      "s_cbranch_scc1 Lro_%=\n\t"
      "s_cmp_lg_u32 %[got], 0\n\t"
      "s_waitcnt vmcnt(0)\n\t"
      "s_cbranch_scc1 Lrf_%=\n\t"
      "v_cmp_eq_u32 vcc, 0, v229\n\t"
      "s_cbranch_vccz Lrl2_%=\n\t"
      RWAIT(2, "v229")
      RCV8A
      "Lrf_%=:\n\t"
      "v_cmp_eq_u32 vcc, 0, v228\n\t"
      "s_cbranch_vccz Lrm_%=\n\t"
      RWAIT(4, "v228")
      RCV4C
      "s_sleep " STR(RDELAYN) "\n\t"
      "s_branch Lrp_%=\n"
      "Lrm_%=:\n\t"
      RCV4C
      "s_sleep " STR(RDELAY3) "\n\t"
      "s_branch Lrp_%=\n"
      "Lro_%=:\n\t"
      "s_sleep " STR(RDELAY2) "\n"
      "Lrp_%=:\n\t"
      RCV4D
      "s_cmp_eq_u32 %[got], 1\n\t"
      "s_cbranch_scc1 Lrz_%=\n\t"
      "s_waitcnt vmcnt(8)\n"
      "Lrz_%=:"
      : "+{v228}"(sd0), "+{v229}"(fl), [cnt] "=&s"(cnt), [tmp] "=&v"(tmp), [sc] "=&s"(sc), "+v"(s2),
        "+v"(vkey), "+v"(vkb), "+v"(vkr), "+v"(nrow), "+v"(vpub), "+v"(ta), "+v"(tu), "+v"(tb), "+s"(pk)
      : [srdz] "s"(srdz), [got] "s"(got), [go] "s"(got2), [vfl] "v"(vfl), [sfl] "s"(sfl), [sin] "s"(sin), [vin0] "v"(vin), [vin1] "v"(vin1), [vin2] "v"(vin2), [vin3] "v"(vin3), [vsh] "v"(ldsb + 12420u)
      : "vcc", "scc", "memory");
    part_(0, s2, d2, q2); part_(1, s2, d2, q2);
    TQ(2);
    asm volatile("s_waitcnt vmcnt(4)" : "+{v228}"(sd0), "+{v229}"(fl), "+v"(s2), "+v"(d2), "+v"(q2), "+v"(s3) :: "memory");
    part_(2, s3, d3, q3);
    asm volatile("s_waitcnt vmcnt(0)" : "+v"(s3), "+v"(d3), "+v"(q3) :: "memory");
    TQ(3);
    part_(3, s3, d3, q3);
    if (__builtin_expect(__any(s3[0] + s3[1] != s3[0] + s3[1]), 0)) {
      unsigned spins = 0;
      do {
        asm volatile(RCV8B "s_waitcnt vmcnt(0)" :: [srdz] "s"(srdz), [sin] "s"(sin), [vin2] "v"(vin2), [vin3] "v"(vin3) : "memory"); NRE;
        s3 = f32x2{0.f, 0.f}; d3 = s3; q3 = s3;
        part_(2, s3, d3, q3); part_(3, s3, d3, q3);
      } while (__any(s3[0] + s3[1] != s3[0] + s3[1]) && ++spins < RETRY_CAP);
    }
    asm volatile("; all in");
    s2 += s3; d2 += d3; q2 += q3;
  }
  float mrow, rdrow, drow;
  {
    const float sv = s2[0] + s2[1], dv = d2[0] + d2[1], qv = q2[0] + q2[1];
    float u = swap16_add(swap32_add(sv, dv), swap32_add(qv, qv));
    u = dpp_add<DPP_XOR1>(u); u = dpp_add<DPP_XOR2>(u); u = dpp_add<DPP_ROR8>(u);
    const auto r16 = __builtin_amdgcn_permlane16_swap(__float_as_uint(u), __float_as_uint(u), false, false);
    const auto r32 = __builtin_amdgcn_permlane32_swap(r16[0], r16[0], false, false);
    const float mean = __uint_as_float(r32[0]) * (1.f / DM);
    drow = __uint_as_float(r32[1]);
    float var = __uint_as_float(r16[1]) * (1.f / DM) - mean * mean;
    if (__builtin_expect(__any(!(mean * mean <= var)), 0)) {
      f32x2 qq = {0.f, 0.f};
      #pragma unroll
      for (int e = 0; e < 16; ++e) { const f32x2 t = x1[e] - mean; qq = __builtin_elementwise_fma(t, t, qq); }
      var = row_sum(qq[0] + qq[1]) * (1.f / DM);
    }
    mrow = rbh(mean); rdrow = __builtin_amdgcn_rcpf(rbh(__builtin_amdgcn_sqrtf(rbh(rbh(var) + 1e-5f))));
  }
  unsigned* kb = (unsigned*)(lds + 12288);
  unsigned mykey, k1w;
  { const unsigned bb = __float_as_uint(fmaf(-mrow, arow, drow) * rdrow); mykey = max((bb ^ ((unsigned)((int)bb >> 31) | 0x80000000u)) & 0xfffff800u, 0x800u) | nrow; }
  asm volatile("ds_write_b32 %1, %2\n\ts_waitcnt lgkmcnt(0)\n\ts_barrier\n\tds_read_b32 %0, %3\n\ts_waitcnt lgkmcnt(0)" : "=&v"(k1w) : "v"(vkb), "v"(mykey), "v"(vkr) : "memory");
  bool cand;
  u32x4 ka, kc;
#define KSC " sc1"
  {
    unsigned k1 = k1w, k2 = 0;
    top2_dpp<DPP_XOR1>(k1, k2); top2_dpp<DPP_XOR2>(k1, k2); top2_dpp<0x141>(k1, k2);
    asm volatile("s_cmp_lg_u32 %[wid], 0\n\ts_cbranch_scc1 Lka_%=\n\ts_mov_b64 exec, %[mk]\n\tglobal_atomic_or_x2 %[va], %[kk], %[pk]" KSC "\n\ts_mov_b64 exec, -1\nLka_%=:"
                 :: [wid] "s"(wid), [mk] "n"((1u << KEY_REP) - 1u), [va] "v"(vkey), [kk] "v"(u32x2{k2, k1}), [pk] "s"(pk) : "scc", "memory");
    cand = mykey >= k2;
    vpub += mykey == k1 ? 0u : 2u * BOX;
  }
  TQ(4);
  PAGE(2, "adaln_pg3");
#define TRA(r, a, o) asm volatile("ds_read_b64_tr_b16 a[%0:%0 + 1], %1 offset:%2" :: "n"(r), "v"(a), "n"(o) : "memory")
#define FRAGS(k) do { \
      TRA(128 + 8 * (k), ta, 2048 * ((k) >> 2) + 512 * ((k) & 1) + 16384 * (((k) >> 1) & 1)); TRA(130 + 8 * (k), ta, 2048 * ((k) >> 2) + 512 * ((k) & 1) + 16384 * (((k) >> 1) & 1) + 1024); \
      TRA(132 + 8 * (k), tu, 2048 * ((k) >> 2) + 512 * ((k) & 1) + 4096 * (((k) >> 1) & 1)); TRA(134 + 8 * (k), tu, 2048 * ((k) >> 2) + 512 * ((k) & 1) + 4096 * (((k) >> 1) & 1) + 1024); \
      TRA(192 + 4 * (k), tb, 64 * (k)); TRA(194 + 4 * (k), tb, 64 * (k) + 32); } while (0)
  f32x2 h2[16];
  {
    asm volatile("" : "+v"(mrow));
    const float nm = -mrow * rdrow;
    #pragma unroll
    for (int k = 0; k < 8; ++k) {
      #pragma unroll
      for (int e = 0; e < 2; ++e) { const int i = 2 * k + e; h2[i] = __builtin_elementwise_fma(__builtin_elementwise_fma(x1[i], f32x2{rdrow, rdrow}, f32x2{nm, nm}), c1f[i], sbf[i]); }
      if (k & 1) { asm volatile("" : "+v"(h2[2 * k]), "+v"(h2[2 * k + 1])); FRAGS(k >> 1); }
    }
  }
  const unsigned cw = __builtin_amdgcn_readfirstlane((unsigned)__ballot(cand) & 0x11u);
  auto publish = [&](const f32x2* v, unsigned off) __attribute__((always_inline)) {
    #pragma unroll
    for (int k = 0; k < 4; ++k) { const int q = 4 * (k >> 1) + (k & 1);
      asm volatile("buffer_store_dwordx4 %0, %1, %2, %3 offen sc1\n\ts_nop 1" :: "v"(u32x4{cvtpk2(v[2 * q]), cvtpk2(v[2 * q + 1]), cvtpk2(v[2 * q + 4]), cvtpk2(v[2 * q + 5])}), "v"(vpub), "s"(srdz), "s"(off + (k & 1) * BOX + (((k >> 1) * 2u) ^ yp2) * 128u) : "memory"); }
  };
  if (cand) publish(h2, 0u);
  char* st = lds + 32768 + 4096u * wid;
  auto look1 = [&]() __attribute__((always_inline)) {
    asm volatile("s_cmp_lg_u32 %2, 0\n\ts_cbranch_scc1 Lkl_%=\n\tbuffer_load_dwordx4 %0, %3, %4, 0 offen sc1\n\tbuffer_load_dwordx4 %1, %3, %4, 0 offen offset:16 sc1\nLkl_%=:" : "=&v"(ka), "=&v"(kc) : "s"(wid), "v"(kp), "s"(srdz) : "scc", "memory");
  };
  if (KLOOK == 0) look1();
  #pragma unroll
  for (int k = 0; k < 8; ++k) {
    u32x2 yq;
    #pragma unroll
    for (int e = 0; e < 2; ++e) yq[e] = cvtpk2(__builtin_elementwise_fma(gbf[2 * k + e], h2[2 * k + e], x1[2 * k + e]));
    *(u32x2*)(st + (row & 1u) * 2048u + (colb + 32u * ((k >> 2) * 2u ^ yp2)) * 2u + 64 * ((k >> 1) & 1) + 1024 * (k & 1)) = yq;
    if (k & 1) FRAGS(4 + (k >> 1));
    if (k == KLOOK - 1) look1();
  }
  auto out_rows = [&](unsigned which) __attribute__((always_inline)) {
    #pragma unroll
    for (int r = 0; r < 2; ++r) {
      if ((which >> (4 * r)) & 1u) {
        const u32x4 a = *(const u32x4*)(st + 2048 * r + 16u * lane), c = *(const u32x4*)(st + 2048 * r + 1024 + 16u * lane);
        asm volatile("s_nop 4\n\tglobal_store_dwordx4 %0, %1, %3 offset:%4 sc1\n\tglobal_store_dwordx4 %0, %2, %3 offset:%5 sc1\n\ts_nop 1" :: "v"(r0 * 2048u + 16u * lane), "v"(a), "v"(c), "s"(pout), "n"(2048 * r), "n"(2048 * r + 1024) : "memory");
      }
    }
  };
  const unsigned un = tid & 127u, ca = 256u * (un >> 5) + 64u * ((un >> 4) & 1u) + 128u * ((un >> 3) & 1u) + 16u * ((un >> 2) & 1u) + 4u * (un & 3u);
  unsigned vrow = (unsigned)WS_XIN + 16384u + (moe ? (wid >> 1) * BOX + (wid & 1u) * 512u + 16u * g : (un >> 6) * BOX + (un & 63u) * 16u), vsel = ldsb + 12288u + 40u + 4u * (moe ? lane & 1u : wid >> 1);
  unsigned mk[8];
  #pragma unroll
  for (int t = 0; t < 8; ++t) { mk[t] = (unsigned)__builtin_amdgcn_sbfe((int)(1u << (n >> 1)), t, 1); asm volatile("" : "+v"(mk[t])); }
  unsigned vred = ldsb + 24576u + wid * 256u + (lane & 1u) * 64u + 16u * g, vrf = ldsb + 24576u + ((lane & 1u) * 4u + g) * 16u + ((lane >> 1) & 3u) * 4u, vpt = (unsigned)WS_PART + b * 8192u + wid * 2048u + lane * 16u, vkk = ldsb + 12288u + 32u;
  unsigned long long m2 = 0x0003000300030003ull;
  asm volatile("" : "+v"(vrow), "+v"(vsel), "+v"(vred), "+v"(vrf), "+v"(vpt), "+v"(vkk), "+s"(m2));
  const unsigned later = __builtin_amdgcn_readfirstlane(wid && EARLYOUT ? cw : 0x11u);
  out_rows(~later);
  TQ(1);
  PAGE(3, "adaln_pg8");
  if (wid == 0) {
    asm volatile("s_waitcnt vmcnt(0)\nLk0_%=:" : "+v"(ka), "+v"(kc) :: "memory");
    for (unsigned spins = 0;; ++spins) {
      const u32x4 t = __builtin_elementwise_min(ka, kc);
      if (__all(min(min(t[0], t[1]), min(t[2], t[3])) != 0u) || spins > SPIN_CAP) break;
      __builtin_amdgcn_s_sleep(KSLEEP);
      asm volatile("buffer_load_dwordx4 %0, %2, %3, 0 offen sc1\n\tbuffer_load_dwordx4 %1, %2, %3, 0 offen offset:16 sc1\n\ts_waitcnt vmcnt(0)" : "=&v"(ka), "=&v"(kc) : "v"(kp), "s"(srdz) : "memory");
    }
    unsigned k1 = ka[1], k2 = ka[0];
    unsigned l1 = kc[1], l2 = kc[0];
    top2_merge(k1, k2, ka[3], ka[2]); top2_merge(l1, l2, kc[3], kc[2]); top2_merge(k1, k2, l1, l2);
    top2_dpp<DPP_XOR1>(k1, k2); top2_dpp<DPP_XOR2>(k1, k2); top2_dpp<0x141>(k1, k2); top2_dpp<0x140>(k1, k2);
    { auto r1 = __builtin_amdgcn_permlane16_swap(k1, k1, false, false); auto r2 = __builtin_amdgcn_permlane16_swap(k2, k2, false, false); k1 = r1[0]; k2 = r2[0]; top2_merge(k1, k2, r1[1], r2[1]); }
    { auto r1 = __builtin_amdgcn_permlane32_swap(k1, k1, false, false); auto r2 = __builtin_amdgcn_permlane32_swap(k2, k2, false, false); k1 = r1[0]; k2 = r2[0]; top2_merge(k1, k2, r1[1], r2[1]); }
    k1 = ~k1 & 2047u; k2 = ~k2 & 2047u;
    *(u32x4*)(kb + 8) = u32x4{k1, k2, (k1 >> 3) * (4u * BOX), (k2 >> 3) * (4u * BOX) + ((k1 ^ k2) < 8u ? 2u * BOX : 0u)};
  }
  u32x2 kk; unsigned vo;
  asm volatile("s_waitcnt lgkmcnt(0)\n\ts_barrier\n\tds_read_b32 %0, %3\n\tds_read_b64 %1, %4\n\ts_waitcnt lgkmcnt(0)\n\tv_add_u32 %2, %2, %0" : "=&v"(vo), "=&v"(kk), "+v"(vrow) : "v"(vsel), "v"(vkk) : "memory");
  TQ(5);
  float* part = (float*)(p.ws + WS_PART);
  unsigned k1, k2, rest;
  auto routed = [&]() __attribute__((always_inline)) {
    asm volatile("" : "+v"(kk));
    k1 = __builtin_amdgcn_readfirstlane(kk[0]); k2 = __builtin_amdgcn_readfirstlane(kk[1]);
    rest = __builtin_amdgcn_readfirstlane(later & ((r0 != k1 && r0 != k2 ? 1u : 0u) | (r0 + 1u != k1 && r0 + 1u != k2 ? 16u : 0u)));
    if (__builtin_expect(rest != later, 0)) { if (row == k1 || row == k2) publish(x1, 20480u); }
  };
  if (__builtin_expect(!moe, 0)) {
    asm volatile("adaln_pg8:\n\ts_cbranch_execz adaln_pg4");
    PAGE(4, "adaln_kr");
    u32x4 ha; u32x2 w0, w1;
    asm volatile("s_nop 4\n\tglobal_load_dwordx2 %0, %2, %3\n\tglobal_load_dwordx2 %1, %2, %3 offset:64" : "=&v"(w0), "=&v"(w1) : "v"(ca * 2u), "s"(p.wr) : "memory");
    asm volatile("buffer_load_dwordx4 %0, %3, %4, 0 offen sc1\n\ts_waitcnt vmcnt(0)" : "=&v"(ha), "+v"(w0), "+v"(w1) : "v"(vrow), "s"(srdz) : "memory");
    for (unsigned spins = 0; __builtin_expect(__any(max(max(ha[0], ha[1]), max(ha[2], ha[3])) == 0xffffffffu), 0) && spins < RETRY_CAP; ++spins)
      asm volatile("buffer_load_dwordx4 %0, %1, %2, 0 offen sc1\n\ts_waitcnt vmcnt(0)" : "=&v"(ha) : "v"(vrow), "s"(srdz) : "memory");
    routed();
    if (b == 255u && tid == 0u) __hip_atomic_store((gu64*)(p.ws + WS_EP), tag, RLX_AGENT);
    char* rows = lds + 16384 + (tid >> 7) * 2048u + ca * 2u;
    *(u32x2*)rows = u32x2{ha[0], ha[1]}; *(u32x2*)(rows + 64) = u32x2{ha[2], ha[3]};
    {
      float dot = 0.f;
      #pragma unroll
      for (int e = 0; e < 4; ++e) { const unsigned w = e < 2 ? w0[e] : w1[e - 2]; dot = fmaf(hi16(ha[e]), hi16(w), fmaf(lo16(ha[e]), lo16(w), dot)); }
      dot = wave_sum(dot);
      if (lane == 0) kb[16u + wid] = __float_as_uint(dot);
    }
    asm volatile("s_waitcnt lgkmcnt(0)\n\ts_barrier" ::: "memory");
    TQ(6);
    const unsigned pp = (b - MOE_WGS) * 64u + lane, sq = pp & 1u, nn = 128u * (pp >> 8) + 16u * (2u * ((pp >> 6) & 3u) + ((pp >> 1) & 1u)) + ((pp >> 2) & 15u);
    float gbv = bf2f(*(const u16*)(par + 14336 + nn * 2u)), hv = bf2f(*(const u16*)(lds + 16384 + sq * 2048u + nn * 2u));
    float rsel = rbf(__builtin_amdgcn_rcpf(1.f + __builtin_amdgcn_exp2f(rbf(__uint_as_float(kb[16u + 2u * sq]) + __uint_as_float(kb[17u + 2u * sq])) * -1.44269504089f)));
    u16* vout = pout + (sq ? k2 : k1) * DM + nn;
    unsigned vp = (unsigned)WS_PART + (tid >> 4) * 8192u + (b - MOE_WGS) * 256u + (tid & 15u) * 16u;
    unsigned vw = ldsb + 24576u + wid * 256u + (lane & 15u) * 16u + 4u * g, vr = ldsb + 24576u + 4u * lane;
    unsigned vx = (unsigned)WS_XIN + 36864u + kb[10u + sq] + (nn >> 9) * BOX + (32u * ((nn >> 8) & 1u) + 16u * ((nn >> 6) & 1u) + 8u * ((nn >> 7) & 1u) + 4u * ((nn >> 4) & 1u) + ((nn >> 2) & 3u)) * 16u + ((nn >> 5) & 1u) * 8u + (nn & 3u) * 2u;
    asm volatile("" : "+v"(gbv), "+v"(hv), "+v"(rsel), "+v"(vout), "+v"(vp), "+v"(vw), "+v"(vr), "+v"(vx));
    out_rows(rest);
    TQ(7);
    __builtin_amdgcn_s_sleep(FSLEEP);
    float u; unsigned xw;
    for (unsigned spins = 0;; ++spins) {
      f32x4 v[14];
      asm volatile("buffer_load_ushort %0, %1, %2, 0 offen sc1" : "=v"(xw) : "v"(vx), "s"(srdz) : "memory");
      #pragma unroll
      for (int k = 0; k < 14; ++k) asm volatile("buffer_load_dwordx4 %0, %1, %2, %3 offen sc1" : "=v"(v[k]) : "v"(vp), "s"(srdz), "s"(131072u * k) : "memory");
      asm volatile("s_waitcnt vmcnt(12)" : "+v"(v[0]), "+v"(v[1]), "+v"(xw) :: "memory");
      f32x4 sum = v[0] + v[1];
#define NEXT2(k) asm volatile("s_waitcnt vmcnt(%3)" : "+v"(v[k]), "+v"(v[k + 1]), "+v"(sum) : "n"(12 - k) : "memory"); sum += v[k]; sum += v[k + 1]
      NEXT2(2); NEXT2(4); NEXT2(6); NEXT2(8); NEXT2(10); NEXT2(12);
      u = swap16_add(swap32_add(sum[0], sum[2]), swap32_add(sum[1], sum[3]));
      if (!__any(u != u || xw == 0xffffu) || spins > RETRY_CAP) break;
    }
    TQ(1);
    f32x2 ra, rc;
    asm volatile("ds_write_b32 %0, %1\n\ts_waitcnt lgkmcnt(0)\n\ts_barrier" :: "v"(vw), "v"(u) : "memory");
    if (wid == 0) {
      asm volatile("ds_read2st64_b32 %0, %2 offset1:1\n\tds_read2st64_b32 %1, %2 offset0:2 offset1:3\n\ts_waitcnt lgkmcnt(0)" : "=&v"(ra), "=&v"(rc) : "v"(vr) : "memory");
      ra += rc;
      const unsigned ov = cvtpk(fmaf(gbv, fmaf(ra[0] + ra[1], rsel, hv), lo16(xw)), 0.f);
      asm volatile("global_store_short %0, %1, off sc1" :: "v"(vout), "v"(ov) : "memory");
    }
  } else {
    f32x4 dg, du;
    bf16x8 hb[8];
    auto fetch = [&]() __attribute__((always_inline)) {
      asm volatile("s_mov_b64 exec, %8\n\t"
                   "buffer_load_dwordx4 %0, %9, %10, 0 offen sc1\n\tbuffer_load_dwordx4 %1, %9, %10, 0 offen offset:64 sc1\n\tbuffer_load_dwordx4 %2, %9, %10, 0 offen offset:128 sc1\n\tbuffer_load_dwordx4 %3, %9, %10, 0 offen offset:192 sc1\n\t"
                   "buffer_load_dwordx4 %4, %9, %10, 0 offen offset:256 sc1\n\tbuffer_load_dwordx4 %5, %9, %10, 0 offen offset:320 sc1\n\tbuffer_load_dwordx4 %6, %9, %10, 0 offen offset:384 sc1\n\tbuffer_load_dwordx4 %7, %9, %10, 0 offen offset:448 sc1\n\t"
                   "s_mov_b64 exec, -1"
                   : "=&v"(hb[0]), "=&v"(hb[1]), "=&v"(hb[2]), "=&v"(hb[3]), "=&v"(hb[4]), "=&v"(hb[5]), "=&v"(hb[6]), "=&v"(hb[7]) : "s"(m2), "v"(vrow), "s"(srdz) : "memory");
    };
#define MM2(a, h, c0, c1) "v_mfma_f32_16x16x32_bf16 %0, a[" #a ":" #a "+3], %" #h ", " c0 "\n\tv_mfma_f32_16x16x32_bf16 %1, a[" #a "+4:" #a "+7], %" #h ", " c1 "\n\t"
#define MM16 MM2(128, 2, "0", "0") MM2(136, 3, "%0", "%1") MM2(144, 4, "%0", "%1") MM2(152, 5, "%0", "%1") MM2(160, 6, "%0", "%1") MM2(168, 7, "%0", "%1") MM2(176, 8, "%0", "%1") MM2(184, 9, "%0", "%1") "s_nop 7"
#define HB "+v"(hb[0]), "+v"(hb[1]), "+v"(hb[2]), "+v"(hb[3]), "+v"(hb[4]), "+v"(hb[5]), "+v"(hb[6]), "+v"(hb[7])
    fetch();
    routed();
    asm volatile("s_cmp_lg_u32 %10, %11\n\ts_waitcnt vmcnt(4)\n\ts_cbranch_scc1 Lh_%=\n\ts_waitcnt vmcnt(0)\nLh_%=:\n\t" MM16 : "=&v"(dg), "=&v"(du), HB : "s"(rest), "s"(later) : "scc", "memory");
    TQ(6);
    for (unsigned spins = 0; __builtin_expect((__ballot(dg[0] != dg[0]) & m2) != 0ull, 0) && spins < RETRY_CAP; ++spins) {
      fetch();
      asm volatile("s_waitcnt vmcnt(0)\n\t" MM16 : "=&v"(dg), "=&v"(du), HB :: "memory");
    }
    f32x2 ra, rc, rd, re;
    asm volatile("s_mov_b64 exec, %7\n\tds_write_b128 %4, %5\n\tds_write_b128 %4, %6 offset:128\n\ts_mov_b64 exec, -1\n\ts_waitcnt lgkmcnt(0)\n\ts_barrier\n\t"
                 "ds_read2_b32 %0, %8 offset1:32\n\tds_read2_b32 %1, %8 offset0:64 offset1:96\n\tds_read2_b32 %2, %8 offset0:128 offset1:160\n\tds_read2_b32 %3, %8 offset0:192 offset1:224\n\ts_waitcnt lgkmcnt(0)"
                 : "=&v"(ra), "=&v"(rc), "=&v"(rd), "=&v"(re) : "v"(vred), "v"(dg), "v"(du), "s"(m2), "v"(vrf) : "memory");
    u32x2 actp;
    {
      const f32x2 gu = rb2((ra + rc) + (rd + re));
      const float sg = __uint_as_float(cvtpk(0.f, gu[0] * __builtin_amdgcn_rcpf(1.f + __builtin_amdgcn_exp2f(gu[0] * -1.44269504089f))));
      const unsigned a = cvtpk(0.f, sg * gu[1]);
      const unsigned o2 = __builtin_amdgcn_update_dpp(0u, a, DPP_XOR2, 0xf, 0xf, true);
      const unsigned pr = (lane & 2u) ? ((o2 >> 16) | a) : ((a >> 16) | o2);
      const unsigned o4 = __builtin_amdgcn_update_dpp(0u, pr, DPP_ROR4, 0xf, 0xf, true);
      actp = (lane & 4u) ? u32x2{o4, pr} : u32x2{pr, o4};
    }
    f32x4 yv[2];
#define ABD(t) "v"(u32x4{actp[0] & mk[t], actp[1] & mk[t], actp[0] & mk[t + 1], actp[1] & mk[t + 1]})
#define MM3(a, w, c0, c1) "v_mfma_f32_16x16x32_bf16 %0, %" #a ", a[" #w ":" #w "+3], " c0 "\n\tv_mfma_f32_16x16x32_bf16 %1, %" #a ", a[" #w "+16:" #w "+19], " c1 "\n\t"
    asm volatile("s_nop 1\n\t" MM3(2, 192, "0", "0") MM3(3, 196, "%0", "%1") MM3(4, 200, "%0", "%1") MM3(5, 204, "%0", "%1") "s_nop 6\n\t"
                 "buffer_store_dwordx4 %0, %6, %7, 0 offen sc1\n\tbuffer_store_dwordx4 %1, %6, %7, 0 offen offset:1024 sc1\n\ts_nop 1"
                 : "=&v"(yv[0]), "=&v"(yv[1]) : ABD(0), ABD(2), ABD(4), ABD(6), "v"(vpt), "s"(srdz) : "memory");
    TQ(7);
    out_rows(rest);
  }
  asm volatile("adaln_text_end:");
}
}

extern "C" void kernel_launch(void* const* d_in, const int* in_sizes, int n_in, void* d_out, int out_size, void* d_ws, size_t ws_size, hipStream_t stream) {
  (void)in_sizes; (void)n_in; (void)out_size;
  if (ws_size < WS_END) return;
  const u16* x    = (const u16*)d_in[0];
  const u16* kc   = (const u16*)d_in[1];
  const u16* vc   = (const u16*)d_in[2];
  const u16* wq   = (const u16*)d_in[3];
  const u16* wo   = (const u16*)d_in[4];
  const u16* wr   = (const u16*)d_in[6];
  const u16* wg   = (const u16*)d_in[7];
  const u16* wu   = (const u16*)d_in[8];
  const u16* wd   = (const u16*)d_in[9];
  const u16* c    = (const u16*)d_in[10];
  const u16* wmod = (const u16*)d_in[11];
  u16* out = (u16*)d_out;
  char* ws = (char*)d_ws;
  fa::Args fr;
  fr.x = x; fr.wmod = wmod; fr.wq = wq; fr.kc = kc; fr.vc = vc; fr.wo = wo; fr.wg = wg; fr.wu = wu; fr.wd = wd; fr.ws = ws; fr.out = out; fr.c = c; fr.wr = wr;
  hipFuncSetAttribute((const void*)fa::fattn_k, hipFuncAttributeMaxDynamicSharedMemorySize, (int)fa::LDS_BYTES);
  hipLaunchKernelGGL(fa::fattn_k, dim3(256), dim3(256), fa::LDS_BYTES, stream, fr);
}
```

```cpp
#include <hip/hip_runtime.h>
#include <hip/hip_bf16.h>
#include <utility>
#include <cstdint>

#pragma clang fp contract(off)
typedef float    f32x16 __attribute__((ext_vector_type(16)));
typedef float    f32x2  __attribute__((ext_vector_type(2)));
typedef float    f32x4  __attribute__((ext_vector_type(4)));
typedef __bf16   bf16x8 __attribute__((ext_vector_type(8)));
typedef __bf16   bf16x4 __attribute__((ext_vector_type(4)));
typedef __bf16   bf16x2 __attribute__((ext_vector_type(2)));
typedef unsigned u32x4  __attribute__((ext_vector_type(4)));
typedef unsigned u32x2  __attribute__((ext_vector_type(2)));
typedef short    s16x4  __attribute__((ext_vector_type(4)));
typedef unsigned short u16;
template <int... Is> using iseq = std::integer_sequence<int, Is...>;
template <int N> using make_iseq = std::make_integer_sequence<int, N>;
#define DI __device__ __forceinline__
#define TAILB
#define PACE2
#define SELB

constexpr int SEQ = 2048, DM = 1024, NHEAD = 32, DHEAD = 128, DQ = NHEAD * DHEAD, DFF = 3584, NMOD = 6 * DM;
constexpr size_t WS_EP    = 0;
constexpr size_t WS_RST   = 32768;
constexpr size_t WS_CNT   = 65536;
constexpr size_t WS_MOD   = 131072;
constexpr size_t WS_Z     = 262144;
constexpr size_t WS_MODT  = WS_Z;
#define KEY_REP 8u
#define KSTR 128u
constexpr size_t WS_KEYS  = WS_Z + 16384;
constexpr unsigned Z_SHARE = 1280;
static_assert(KEY_REP <= 8 && KSTR <= 384 && Z_SHARE * 256 == 327680, "zeroed area");
constexpr size_t WS_PFL   = WS_Z + 65536;
constexpr size_t WS_OFL   = WS_Z + 196608;
constexpr size_t WS_H     = 1048576;
constexpr size_t WS_PART  = WS_H  + (size_t)SEQ * DM * 2;
constexpr size_t WS_XIN   = WS_PART + (size_t)256 * 2 * DM * 4;
#define BOXV 38016
constexpr unsigned BOX    = BOXV;
constexpr size_t WS_END   = WS_XIN + (size_t)BOX * (SEQ / 2);

DI float bf2f(u16 b) { return __uint_as_float((unsigned)b << 16); }
DI float rbf(float x) { return (float)(__bf16)x; }
DI u16   f2bf(float x) { return __builtin_bit_cast(u16, (__bf16)x); }
DI float lo16(unsigned w) { return __uint_as_float(w << 16); }
DI float hi16(unsigned w) { return __uint_as_float(w & 0xffff0000u); }
DI unsigned cvtpk(float lo, float hi) {
  f32x2 v = {lo, hi}; bf16x2 b = __builtin_convertvector(v, bf16x2); return __builtin_bit_cast(unsigned, b);
}
template <int CTRL> DI float dpp_add(float v) { return v + __uint_as_float(__builtin_amdgcn_update_dpp(0u, __float_as_uint(v), CTRL, 0xf, 0xf, true)); }
DI float swap32_add(float a, float b) {
  auto r = __builtin_amdgcn_permlane32_swap(__float_as_uint(a), __float_as_uint(b), false, false); return __uint_as_float(r[0]) + __uint_as_float(r[1]);
}
DI float swap16_add(float a, float b) {
  auto r = __builtin_amdgcn_permlane16_swap(__float_as_uint(a), __float_as_uint(b), false, false); return __uint_as_float(r[0]) + __uint_as_float(r[1]);
}
constexpr int DPP_XOR1 = 0xB1, DPP_XOR2 = 0x4E, DPP_ROR4 = 0x124, DPP_ROR8 = 0x128;
DI float wave_sum(float v) {
  v = dpp_add<DPP_XOR1>(v); v = dpp_add<DPP_XOR2>(v); v = dpp_add<DPP_ROR4>(v); v = dpp_add<DPP_ROR8>(v);
  v = swap16_add(v, v); return swap32_add(v, v);
}

DI void st16_wt(void* p, u32x4 v) { asm volatile("global_store_dwordx4 %0, %1, off sc1\n\ts_nop 1" :: "v"(p), "v"(v) : "memory"); }

DI f32x2 unpk(unsigned w) { return f32x2{lo16(w), hi16(w)}; }
DI unsigned cvtpk2(f32x2 v) { return __builtin_bit_cast(unsigned, __builtin_convertvector(v, bf16x2)); }
DI float rbh(float v) { return __uint_as_float(cvtpk(0.f, v)); }
DI f32x2 rb2(f32x2 v) { return f32x2{__uint_as_float(cvtpk(0.f, v[0])), __uint_as_float(cvtpk(0.f, v[1]))}; }
DI unsigned ln_pair(float x0, float x1, float m, float rden, float c0, float c1, float s0, float s1) {
  const unsigned d = cvtpk2(f32x2{x0, x1} - m), l = cvtpk2(unpk(d) * rden), t = cvtpk2(unpk(l) * f32x2{c0, c1});
  return cvtpk2(unpk(t) + f32x2{s0, s1});
}
#define GAS __attribute__((address_space(1)))
#define LAS __attribute__((address_space(3)))
typedef GAS unsigned long long gu64;
typedef short v4i16_t __attribute__((ext_vector_type(4)));
DI bf16x4 lds_tr(unsigned a) { return __builtin_bit_cast(bf16x4, __builtin_amdgcn_ds_read_tr16_b64_v4i16((LAS v4i16_t*)a)); }
#define RLX_AGENT __ATOMIC_RELAXED, __HIP_MEMORY_SCOPE_AGENT
constexpr unsigned SPIN_CAP = 1u << 16;

static_assert(BOX == 38016, "box size");
#define NP_FILL 8
#define GOT_WAIT "8"
#define FA_CLOBBERS "v0", "v1", "v2", "v3", "v8", "v9", "v10", "v11", "v16", "v17", "v18", "v19", "v20", "v21", "v22", "v23", "v24", "v25", "v26", "v27", "v28", "v29", "v30", "v31", "v32", "v33", "v34", "v35", "v40", "v41", "v42", "v43", "v48", "v49", "v50", "v51", "v52", "v53", "v54", "v55", "v56", "v57", "v58", "v59", "v60", "v61", "v62", "v63", "v64", "v65", "v66", "v67", "v72", "v73", "v74", "v75", "v80", "v81", "v82", "v83", "v84", "v85", "v86", "v87", "v88", "v89", "v90", "v91", "v92", "v93", "v94", "v95", "v96", "v97", "v98", "v99", "v104", "v105", "v106", "v107", "v112", "v113", "v114", "v115", "v116", "v117", "v118", "v119", "v120", "v121", "v122", "v123", "v124", "v125", "v126", "v127", "v200", "v201", "v202", "v203", "v205", "v207", "v208", "v209", "v210", "v211", "v212", "v213", "v214", "v215", "v216", "v217", "v218", "v219", "v220", "v221", "v222", "v223", "v224", "v230", "v231", "v232", "v233", "v234", "v235", "v237", "a0", "a1", "a2", "a3", "a4", "a5", "a6", "a7", "a8", "a9", "a10", "a11", "a12", "a13", "a14", "a15", "a16", "a17", "a18", "a19", "a20", "a21", "a22", "a23", "a24", "a25", "a26", "a27", "a28", "a29", "a30", "a31", "a32", "a33", "a34", "a35", "a36", "a37", "a38", "a39", "a40", "a41", "a42", "a43", "a44", "a45", "a46", "a47", "a48", "a49", "a50", "a51", "a52", "a53", "a54", "a55", "a56", "a57", "a58", "a59", "a60", "a61", "a62", "a63", "a64", "a65", "a66", "a67", "a68", "a69", "a70", "a71", "a72", "a73", "a74", "a75", "a76", "a77", "a78", "a79", "a80", "a81", "a82", "a83", "a84", "a85", "a86", "a87", "a88", "a89", "a90", "a91", "a92", "a93", "a94", "a95", "a96", "a97", "a98", "a99", "a100", "a101", "a102", "a103", "a104", "a105", "a106", "a107", "a108", "a109", "a110", "a111", "a112", "a113", "a114", "a115", "a116", "a117", "a118", "a119", "a120", "a121", "a122", "a123", "a124", "a125", "a126", "a127", "a128", "a129", "a130", "a131", "a132", "a133", "a134", "a135", "a136", "a137", "a138", "a139", "a140", "a141", "a142", "a143", "a144", "a145", "a146", "a147", "a148", "a149", "a150", "a151", "a152", "a153", "a154", "a155", "a156", "a157", "a158", "a159", "a160", "a161", "a162", "a163", "a164", "a165", "a166", "a167", "a168", "a169", "a170", "a171", "a172", "a173", "a174", "a175", "a176", "a177", "a178", "a179", "a180", "a181", "a182", "a183", "a184", "a185", "a186", "a187", "a188", "a189", "a190", "a191", "a192", "a193", "a194", "a195", "a196", "a197", "a198", "a199", "a200", "a201", "a202", "a203", "a204", "a205", "a206", "a207", "a208", "a209", "a210", "a211", "a212", "a213", "a214", "a215", "a216", "a217", "a218", "a219", "a220", "a221", "a222", "a223", "a224", "a225", "a226", "a227", "a228", "a229", "a230", "a231", "a232", "a233", "a234", "a235", "a236", "a237", "a238", "a239", "a240", "a241", "a242", "a243", "a244", "a245", "a246", "a247", "a248", "a249", "a250", "a251", "a252", "a253", "a254", "a255", "s40", "s41", "s42", "s43", "s44", "s45", "s46", "s47", "s48", "s49", "s50", "s51", "s52", "s53", "s54", "s55", "s56", "s57", "s58", "s59", "s60", "s61", "s62", "s63", "s64", "s65", "s66", "s67", "s68", "s69", "vcc", "m0", "scc", "memory"
#define EARLY2 0
#define SHW 1
#define QROT 0
#define SKV 0
#define KVPAR 0
#define QT2 0
#define QSTAG 0
#define TLATE 0
#define NTV 5
#define BPERM 0
#define FA_OUTS [got] "=&s"(got), "=&{v[128:129]}"(xf[0]), "=&{v[130:131]}"(xf[1]), "=&{v[132:133]}"(xf[2]), "=&{v[134:135]}"(xf[3]), "=&{v[136:137]}"(xf[4]), "=&{v[138:139]}"(xf[5]), "=&{v[140:141]}"(xf[6]), "=&{v[142:143]}"(xf[7]), "=&{v[144:145]}"(xf[8]), "=&{v[146:147]}"(xf[9]), "=&{v[148:149]}"(xf[10]), "=&{v[150:151]}"(xf[11]), "=&{v[152:153]}"(xf[12]), "=&{v[154:155]}"(xf[13]), "=&{v[156:157]}"(xf[14]), "=&{v[158:159]}"(xf[15]), "=&{v[160:163]}"(gaf[0]), "=&{v[164:167]}"(gaf[1]), "=&{v[168:171]}"(gaf[2]), "=&{v[172:175]}"(gaf[3]), "=&{v[176:179]}"(gaf[4]), "=&{v[180:183]}"(gaf[5]), "=&{v[184:187]}"(gaf[6]), "=&{v[188:191]}"(gaf[7]), "=&{v[4:7]}"(aw[0]), "=&{v[12:15]}"(aw[1]), "=&{v[36:39]}"(aw[2]), "=&{v[44:47]}"(aw[3]), "=&{v[68:71]}"(aw[4]), "=&{v[76:79]}"(aw[5]), "=&{v[100:103]}"(aw[6]), "=&{v[108:111]}"(aw[7]), "=&{v[192:195]}"(alo), "=&{v[196:199]}"(ahi), "=&{v204}"(late), "=&{v206}"(arow), "+{v229}"(fl), "+{v225}"(vwb), "+{v226}"(wd0), "+{v227}"(hq0), "+{v228}"(sd0), "+{v236}"(tq0)
#define FA_BODY \
"s_branch adaln_body\n" \
".p2align 12\n" \
"adaln_pg7:\n" \
"adaln_body:\n" \
"s_cbranch_execz adaln_fr\n" \
"s_nop 0\n" \
"s_mov_b32 s55, 0x80000\n" \
"v_add_u32_e32 v219, 0x8000, v227\n" \
"v_add_u32_e32 v220, 0x10000, v227\n" \
"v_add_u32_e32 v221, 0x18000, v227\n" \
"s_mov_b32 s50, 0\n" \
"s_movk_i32 s53, 0x1000\n" \
"s_mov_b32 s52, 0\n" \
"s_nop 0\n" \
"s_mov_b32 s49, 1572864\n" \
"v_add_u32_e32 v216, 0xfffffc80, v226\n" \
"v_add_u32_e32 v217, 0xf800, v226\n" \
"v_add_u32_e32 v218, 0xf480, v226\n" \
"s_add_u32 s40, %[m0b], 0\n" \
"s_nop 0\n" \
"s_add_u32 s44, %[m0b], 65536\n" \
"s_add_u32 s41, %[m0b], 16384\n" \
"s_add_u32 s45, %[m0b], 81920\n" \
"s_add_u32 s42, %[m0b], 32768\n" \
"s_add_u32 s46, %[m0b], 98304\n" \
"s_add_u32 s43, %[m0b], 49152\n" \
"s_add_u32 s47, %[m0b], 114688\n" \
"v_xor_b32_e64 v208, 64, %[kb0]\n" \
"v_xor_b32_e32 v209, 0x80, %[kb0]\n" \
"v_xor_b32_e32 v210, 0xc0, %[kb0]\n" \
"v_xor_b32_e64 v212, 64, %[kd0]\n" \
"v_xor_b32_e32 v213, 0x80, %[kd0]\n" \
"v_xor_b32_e32 v214, 0xc0, %[kd0]\n" \
"v_xor_b32_e64 v211, 32, %[vvb]\n" \
"v_add_u32_e32 v215, 0xfffffc80, %[vd0]\n" \
"v_readlane_b32 s64, %[tlo], 17\n" \
"v_readlane_b32 s65, %[thi], 17\n" \
"s_mov_b32 s66, 12582912\n" \
"s_mov_b32 s67, 0x20000\n" \
"v_mov_b64_e32 v[0:1], 0\n" \
"v_mov_b64_e32 v[2:3], 0\n" \
"v_mov_b64_e32 v[4:5], 0\n" \
"v_mov_b64_e32 v[6:7], 0\n" \
"v_mov_b64_e32 v[8:9], 0\n" \
"v_mov_b64_e32 v[10:11], 0\n" \
"v_mov_b64_e32 v[12:13], 0\n" \
"v_mov_b64_e32 v[14:15], 0\n" \
"v_mov_b64_e32 v[16:17], 0\n" \
"v_mov_b64_e32 v[18:19], 0\n" \
"v_mov_b64_e32 v[20:21], 0\n" \
"v_mov_b64_e32 v[22:23], 0\n" \
"v_mov_b64_e32 v[24:25], 0\n" \
"v_mov_b64_e32 v[26:27], 0\n" \
"v_mov_b64_e32 v[28:29], 0\n" \
"v_mov_b64_e32 v[30:31], 0\n" \
"v_mov_b64_e32 v[32:33], 0\n" \
"v_mov_b64_e32 v[34:35], 0\n" \
"v_mov_b64_e32 v[36:37], 0\n" \
"v_mov_b64_e32 v[38:39], 0\n" \
"v_mov_b64_e32 v[40:41], 0\n" \
"v_mov_b64_e32 v[42:43], 0\n" \
"v_mov_b64_e32 v[44:45], 0\n" \
"v_mov_b64_e32 v[46:47], 0\n" \
"v_mov_b64_e32 v[48:49], 0\n" \
"v_mov_b64_e32 v[50:51], 0\n" \
"v_mov_b64_e32 v[52:53], 0\n" \
"v_mov_b64_e32 v[54:55], 0\n" \
"v_mov_b64_e32 v[56:57], 0\n" \
"v_mov_b64_e32 v[58:59], 0\n" \
"v_mov_b64_e32 v[60:61], 0\n" \
"v_mov_b64_e32 v[62:63], 0\n" \
"v_mov_b64_e32 v[64:65], 0\n" \
"v_mov_b64_e32 v[66:67], 0\n" \
"v_mov_b64_e32 v[68:69], 0\n" \
"v_mov_b64_e32 v[70:71], 0\n" \
"v_mov_b64_e32 v[72:73], 0\n" \
"v_mov_b64_e32 v[74:75], 0\n" \
"v_mov_b64_e32 v[76:77], 0\n" \
"v_mov_b64_e32 v[78:79], 0\n" \
"v_mov_b64_e32 v[80:81], 0\n" \
"v_mov_b64_e32 v[82:83], 0\n" \
"v_mov_b64_e32 v[84:85], 0\n" \
"v_mov_b64_e32 v[86:87], 0\n" \
"v_mov_b64_e32 v[88:89], 0\n" \
"v_mov_b64_e32 v[90:91], 0\n" \
"v_mov_b64_e32 v[92:93], 0\n" \
"v_mov_b64_e32 v[94:95], 0\n" \
"v_mov_b64_e32 v[96:97], 0\n" \
"v_mov_b64_e32 v[98:99], 0\n" \
"v_mov_b64_e32 v[100:101], 0\n" \
"v_mov_b64_e32 v[102:103], 0\n" \
"v_mov_b64_e32 v[104:105], 0\n" \
"v_mov_b64_e32 v[106:107], 0\n" \
"v_mov_b64_e32 v[108:109], 0\n" \
"v_mov_b64_e32 v[110:111], 0\n" \
"v_mov_b64_e32 v[112:113], 0\n" \
"v_mov_b64_e32 v[114:115], 0\n" \
"v_mov_b64_e32 v[116:117], 0\n" \
"v_mov_b64_e32 v[118:119], 0\n" \
"v_mov_b64_e32 v[120:121], 0\n" \
"v_mov_b64_e32 v[122:123], 0\n" \
"v_mov_b64_e32 v[124:125], 0\n" \
"v_mov_b64_e32 v[126:127], 0\n" \
"s_mov_b32 s51, 0x10000\n" \
"v_mbcnt_lo_u32_b32 v232, -1, 0\n" \
"v_mov_b64_e32 v[234:235], %[tag]\n" \
"v_lshlrev_b32_e32 v233, 3, v232\n" \
"v_lshlrev_b32_e32 v232, 8, v232\n" \
"s_waitcnt vmcnt(0)\n" \
"s_barrier\n" \
"s_cmp_lg_u32 %[wid], 0\n" \
"s_cbranch_scc1 Lwalk_%=\n" \
"s_mov_b32 exec_hi, 0\n" \
"global_store_dwordx2 v232, v[234:235], %[psig] sc1\n" \
"s_mov_b32 exec_hi, -1\n" \
"Lgo_%=:\n" \
"s_nop 0\n" \
"ds_read_b64_tr_b16 v[128:129], v225 offset:0\n" \
"ds_read_b64_tr_b16 v[130:131], v225 offset:256\n" \
"ds_read_b64_tr_b16 v[132:133], v225 offset:8192\n" \
"ds_read_b64_tr_b16 v[134:135], v225 offset:8448\n" \
"ds_read_b64_tr_b16 v[136:137], v225 offset:8\n" \
"ds_read_b64_tr_b16 v[138:139], v225 offset:264\n" \
"ds_read_b64_tr_b16 v[140:141], v225 offset:8200\n" \
"ds_read_b64_tr_b16 v[142:143], v225 offset:8456\n" \
"ds_read_b64_tr_b16 v[144:145], v225 offset:512\n" \
"ds_read_b64_tr_b16 v[146:147], v225 offset:768\n" \
"ds_read_b64_tr_b16 v[148:149], v225 offset:8704\n" \
"ds_read_b64_tr_b16 v[150:151], v225 offset:8960\n" \
"ds_read_b64_tr_b16 v[152:153], v225 offset:520\n" \
"ds_read_b64_tr_b16 v[154:155], v225 offset:776\n" \
"ds_read_b64_tr_b16 v[156:157], v225 offset:8712\n" \
"ds_read_b64_tr_b16 v[158:159], v225 offset:8968\n" \
"s_mov_b32 s60, 4194304\n" \
"v_and_b32_e32 v233, 56, v233\n" \
"s_lshl_b32 s61, %[wid], 6\n" \
"v_add_u32_e64 v233, s61, v233\n" \
"Lpoll_%=:\n" \
"global_load_dwordx2 v[230:231], v233, %[pcnt] sc1\n" \
"s_waitcnt vmcnt(0)\n" \
"v_cmp_ne_u64_e32 vcc, v[230:231], v[234:235]\n" \
"s_sub_u32 s51, s51, 1\n" \
"s_cbranch_scc1 Lpd_%=\n" \
"s_nop 3\n" \
"s_cbranch_vccnz Lpoll_%=\n" \
"Lpd_%=:\n" \
"buffer_load_dwordx4 a[128:131], v227, %[srdz], s50 offen offset:0 sc1\n" \
"buffer_load_dwordx4 a[132:135], v219, %[srdz], s50 offen offset:0 sc1\n" \
"buffer_load_dwordx4 a[136:139], v220, %[srdz], s50 offen offset:0 sc1\n" \
"buffer_load_dwordx4 a[140:143], v221, %[srdz], s50 offen offset:0 sc1\n" \
"buffer_load_dwordx4 a[144:147], v227, %[srdz], s50 offen offset:1024 sc1\n" \
"buffer_load_dwordx4 a[148:151], v219, %[srdz], s50 offen offset:1024 sc1\n" \
"buffer_load_dwordx4 a[152:155], v220, %[srdz], s50 offen offset:1024 sc1\n" \
"buffer_load_dwordx4 a[156:159], v221, %[srdz], s50 offen offset:1024 sc1\n" \
"buffer_load_dwordx4 a[160:163], v227, %[srdz], s50 offen offset:2048 sc1\n" \
"buffer_load_dwordx4 a[164:167], v219, %[srdz], s50 offen offset:2048 sc1\n" \
"buffer_load_dwordx4 a[168:171], v220, %[srdz], s50 offen offset:2048 sc1\n" \
"buffer_load_dwordx4 a[172:175], v221, %[srdz], s50 offen offset:2048 sc1\n" \
"buffer_load_dwordx4 a[176:179], v227, %[srdz], s50 offen offset:3072 sc1\n" \
"buffer_load_dwordx4 a[180:183], v219, %[srdz], s50 offen offset:3072 sc1\n" \
"buffer_load_dwordx4 a[184:187], v220, %[srdz], s50 offen offset:3072 sc1\n" \
"buffer_load_dwordx4 a[188:191], v221, %[srdz], s50 offen offset:3072 sc1\n" \
"s_add_u32 s50, s50, s53\n" \
"s_nop 0\n" \
"buffer_load_dwordx4 a[192:195], v227, %[srdz], s50 offen offset:0 sc1\n" \
"buffer_load_dwordx4 a[196:199], v219, %[srdz], s50 offen offset:0 sc1\n" \
"buffer_load_dwordx4 a[200:203], v220, %[srdz], s50 offen offset:0 sc1\n" \
"buffer_load_dwordx4 a[204:207], v221, %[srdz], s50 offen offset:0 sc1\n" \
"buffer_load_dwordx4 a[208:211], v227, %[srdz], s50 offen offset:1024 sc1\n" \
"buffer_load_dwordx4 a[212:215], v219, %[srdz], s50 offen offset:1024 sc1\n" \
"buffer_load_dwordx4 a[216:219], v220, %[srdz], s50 offen offset:1024 sc1\n" \
"buffer_load_dwordx4 a[220:223], v221, %[srdz], s50 offen offset:1024 sc1\n" \
"s_mov_b32 s51, 3\n" \
"s_waitcnt vmcnt(16) lgkmcnt(0)\n" \
"Lq_%=:\n" \
"v_mfma_f32_16x16x32_bf16 v[0:3], v[128:131], a[128:131], v[0:3]\n" \
"ds_read_b64_tr_b16 v[160:161], v225 offset:1024\n" \
"v_mfma_f32_16x16x32_bf16 v[0:3], v[132:135], a[144:147], v[0:3]\n" \
"ds_read_b64_tr_b16 v[162:163], v225 offset:1280\n" \
"v_mfma_f32_16x16x32_bf16 v[32:35], v[128:131], a[132:135], v[32:35]\n" \
"s_mov_b32 m0, s47\n" \
"s_nop 0\n" \
"buffer_load_dwordx4 v217, %[srdw], s49 offen offset:2048 lds\n" \
"v_mfma_f32_16x16x32_bf16 v[32:35], v[132:135], a[148:151], v[32:35]\n" \
"ds_read_b64_tr_b16 v[164:165], v225 offset:9216\n" \
"v_mfma_f32_16x16x32_bf16 v[64:67], v[128:131], a[136:139], v[64:67]\n" \
"ds_read_b64_tr_b16 v[166:167], v225 offset:9472\n" \
"v_mfma_f32_16x16x32_bf16 v[64:67], v[132:135], a[152:155], v[64:67]\n" \
"v_mfma_f32_16x16x32_bf16 v[96:99], v[128:131], a[140:143], v[96:99]\n" \
"ds_read_b64_tr_b16 v[168:169], v225 offset:1032\n" \
"v_mfma_f32_16x16x32_bf16 v[96:99], v[132:135], a[156:159], v[96:99]\n" \
"ds_read_b64_tr_b16 v[170:171], v225 offset:1288\n" \
"v_mfma_f32_16x16x32_bf16 v[4:7], v[136:139], a[128:131], v[4:7]\n" \
"buffer_load_dwordx4 v218, %[srdw], s49 offen offset:3072 lds\n" \
"v_mfma_f32_16x16x32_bf16 v[4:7], v[140:143], a[144:147], v[4:7]\n" \
"ds_read_b64_tr_b16 v[172:173], v225 offset:9224\n" \
"v_mfma_f32_16x16x32_bf16 v[36:39], v[136:139], a[132:135], v[36:39]\n" \
"ds_read_b64_tr_b16 v[174:175], v225 offset:9480\n" \
"v_mfma_f32_16x16x32_bf16 v[36:39], v[140:143], a[148:151], v[36:39]\n" \
"v_mfma_f32_16x16x32_bf16 v[68:71], v[136:139], a[136:139], v[68:71]\n" \
"ds_read_b64_tr_b16 v[176:177], v225 offset:1536\n" \
"v_mfma_f32_16x16x32_bf16 v[68:71], v[140:143], a[152:155], v[68:71]\n" \
"ds_read_b64_tr_b16 v[178:179], v225 offset:1792\n" \
"v_mfma_f32_16x16x32_bf16 v[100:103], v[136:139], a[140:143], v[100:103]\n" \
"buffer_load_dwordx4 a[224:227], v227, %[srdz], s50 offen offset:2048 sc1\n" \
"v_mfma_f32_16x16x32_bf16 v[100:103], v[140:143], a[156:159], v[100:103]\n" \
"ds_read_b64_tr_b16 v[180:181], v225 offset:9728\n" \
"v_mfma_f32_16x16x32_bf16 v[8:11], v[144:147], a[128:131], v[8:11]\n" \
"ds_read_b64_tr_b16 v[182:183], v225 offset:9984\n" \
"v_mfma_f32_16x16x32_bf16 v[8:11], v[148:151], a[144:147], v[8:11]\n" \
"v_mfma_f32_16x16x32_bf16 v[40:43], v[144:147], a[132:135], v[40:43]\n" \
"ds_read_b64_tr_b16 v[184:185], v225 offset:1544\n" \
"v_mfma_f32_16x16x32_bf16 v[40:43], v[148:151], a[148:151], v[40:43]\n" \
"ds_read_b64_tr_b16 v[186:187], v225 offset:1800\n" \
"v_mfma_f32_16x16x32_bf16 v[72:75], v[144:147], a[136:139], v[72:75]\n" \
"buffer_load_dwordx4 a[228:231], v219, %[srdz], s50 offen offset:2048 sc1\n" \
"v_mfma_f32_16x16x32_bf16 v[72:75], v[148:151], a[152:155], v[72:75]\n" \
"ds_read_b64_tr_b16 v[188:189], v225 offset:9736\n" \
"v_mfma_f32_16x16x32_bf16 v[104:107], v[144:147], a[140:143], v[104:107]\n" \
"ds_read_b64_tr_b16 v[190:191], v225 offset:9992\n" \
"v_mfma_f32_16x16x32_bf16 v[104:107], v[148:151], a[156:159], v[104:107]\n" \
"v_mfma_f32_16x16x32_bf16 v[12:15], v[152:155], a[128:131], v[12:15]\n" \
"buffer_load_dwordx4 a[232:235], v220, %[srdz], s50 offen offset:2048 sc1\n" \
"v_mfma_f32_16x16x32_bf16 v[12:15], v[156:159], a[144:147], v[12:15]\n" \
"v_mfma_f32_16x16x32_bf16 v[44:47], v[152:155], a[132:135], v[44:47]\n" \
"v_mfma_f32_16x16x32_bf16 v[44:47], v[156:159], a[148:151], v[44:47]\n" \
"v_mfma_f32_16x16x32_bf16 v[76:79], v[152:155], a[136:139], v[76:79]\n" \
"buffer_load_dwordx4 a[236:239], v221, %[srdz], s50 offen offset:2048 sc1\n" \
"v_mfma_f32_16x16x32_bf16 v[76:79], v[156:159], a[152:155], v[76:79]\n" \
"v_mfma_f32_16x16x32_bf16 v[108:111], v[152:155], a[140:143], v[108:111]\n" \
"v_mfma_f32_16x16x32_bf16 v[108:111], v[156:159], a[156:159], v[108:111]\n" \
"s_waitcnt vmcnt(29) lgkmcnt(0)\n" \
"s_barrier\n" \
"v_mfma_f32_16x16x32_bf16 v[16:19], v[160:163], a[128:131], v[16:19]\n" \
"ds_read_b64_tr_b16 v[128:129], v225 offset:16384\n" \
"v_mfma_f32_16x16x32_bf16 v[16:19], v[164:167], a[144:147], v[16:19]\n" \
"ds_read_b64_tr_b16 v[130:131], v225 offset:16640\n" \
"v_mfma_f32_16x16x32_bf16 v[48:51], v[160:163], a[132:135], v[48:51]\n" \
"s_add_u32 s49, s49, s55\n" \
"s_mov_b32 m0, s44\n" \
"s_nop 0\n" \
"buffer_load_dwordx4 v226, %[srdw], s49 offen offset:0 lds\n" \
"v_mfma_f32_16x16x32_bf16 v[48:51], v[164:167], a[148:151], v[48:51]\n" \
"ds_read_b64_tr_b16 v[132:133], v225 offset:24576\n" \
"v_mfma_f32_16x16x32_bf16 v[80:83], v[160:163], a[136:139], v[80:83]\n" \
"ds_read_b64_tr_b16 v[134:135], v225 offset:24832\n" \
"v_mfma_f32_16x16x32_bf16 v[80:83], v[164:167], a[152:155], v[80:83]\n" \
"v_mfma_f32_16x16x32_bf16 v[112:115], v[160:163], a[140:143], v[112:115]\n" \
"ds_read_b64_tr_b16 v[136:137], v225 offset:16392\n" \
"v_mfma_f32_16x16x32_bf16 v[112:115], v[164:167], a[156:159], v[112:115]\n" \
"ds_read_b64_tr_b16 v[138:139], v225 offset:16648\n" \
"v_mfma_f32_16x16x32_bf16 v[20:23], v[168:171], a[128:131], v[20:23]\n" \
"buffer_load_dwordx4 v216, %[srdw], s49 offen offset:1024 lds\n" \
"v_mfma_f32_16x16x32_bf16 v[20:23], v[172:175], a[144:147], v[20:23]\n" \
"ds_read_b64_tr_b16 v[140:141], v225 offset:24584\n" \
"v_mfma_f32_16x16x32_bf16 v[52:55], v[168:171], a[132:135], v[52:55]\n" \
"ds_read_b64_tr_b16 v[142:143], v225 offset:24840\n" \
"v_mfma_f32_16x16x32_bf16 v[52:55], v[172:175], a[148:151], v[52:55]\n" \
"v_mfma_f32_16x16x32_bf16 v[84:87], v[168:171], a[136:139], v[84:87]\n" \
"ds_read_b64_tr_b16 v[144:145], v225 offset:16896\n" \
"v_mfma_f32_16x16x32_bf16 v[84:87], v[172:175], a[152:155], v[84:87]\n" \
"ds_read_b64_tr_b16 v[146:147], v225 offset:17152\n" \
"v_mfma_f32_16x16x32_bf16 v[116:119], v[168:171], a[140:143], v[116:119]\n" \
"buffer_load_dwordx4 a[240:243], v227, %[srdz], s50 offen offset:3072 sc1\n" \
"v_mfma_f32_16x16x32_bf16 v[116:119], v[172:175], a[156:159], v[116:119]\n" \
"ds_read_b64_tr_b16 v[148:149], v225 offset:25088\n" \
"v_mfma_f32_16x16x32_bf16 v[24:27], v[176:179], a[128:131], v[24:27]\n" \
"ds_read_b64_tr_b16 v[150:151], v225 offset:25344\n" \
"v_mfma_f32_16x16x32_bf16 v[24:27], v[180:183], a[144:147], v[24:27]\n" \
"v_mfma_f32_16x16x32_bf16 v[56:59], v[176:179], a[132:135], v[56:59]\n" \
"ds_read_b64_tr_b16 v[152:153], v225 offset:16904\n" \
"v_mfma_f32_16x16x32_bf16 v[56:59], v[180:183], a[148:151], v[56:59]\n" \
"ds_read_b64_tr_b16 v[154:155], v225 offset:17160\n" \
"v_mfma_f32_16x16x32_bf16 v[88:91], v[176:179], a[136:139], v[88:91]\n" \
"buffer_load_dwordx4 a[244:247], v219, %[srdz], s50 offen offset:3072 sc1\n" \
"v_mfma_f32_16x16x32_bf16 v[88:91], v[180:183], a[152:155], v[88:91]\n" \
"ds_read_b64_tr_b16 v[156:157], v225 offset:25096\n" \
"v_mfma_f32_16x16x32_bf16 v[120:123], v[176:179], a[140:143], v[120:123]\n" \
"ds_read_b64_tr_b16 v[158:159], v225 offset:25352\n" \
"v_mfma_f32_16x16x32_bf16 v[120:123], v[180:183], a[156:159], v[120:123]\n" \
"v_mfma_f32_16x16x32_bf16 v[28:31], v[184:187], a[128:131], v[28:31]\n" \
"buffer_load_dwordx4 a[248:251], v220, %[srdz], s50 offen offset:3072 sc1\n" \
"v_mfma_f32_16x16x32_bf16 v[28:31], v[188:191], a[144:147], v[28:31]\n" \
"v_mfma_f32_16x16x32_bf16 v[60:63], v[184:187], a[132:135], v[60:63]\n" \
"v_mfma_f32_16x16x32_bf16 v[60:63], v[188:191], a[148:151], v[60:63]\n" \
"v_mfma_f32_16x16x32_bf16 v[92:95], v[184:187], a[136:139], v[92:95]\n" \
"buffer_load_dwordx4 a[252:255], v221, %[srdz], s50 offen offset:3072 sc1\n" \
"v_mfma_f32_16x16x32_bf16 v[92:95], v[188:191], a[152:155], v[92:95]\n" \
"v_mfma_f32_16x16x32_bf16 v[124:127], v[184:187], a[140:143], v[124:127]\n" \
"v_mfma_f32_16x16x32_bf16 v[124:127], v[188:191], a[156:159], v[124:127]\n" \
"s_waitcnt vmcnt(20) lgkmcnt(0)\n" \
"v_mfma_f32_16x16x32_bf16 v[0:3], v[128:131], a[160:163], v[0:3]\n" \
"ds_read_b64_tr_b16 v[160:161], v225 offset:17408\n" \
"v_mfma_f32_16x16x32_bf16 v[0:3], v[132:135], a[176:179], v[0:3]\n" \
"ds_read_b64_tr_b16 v[162:163], v225 offset:17664\n" \
"v_mfma_f32_16x16x32_bf16 v[32:35], v[128:131], a[164:167], v[32:35]\n" \
"s_mov_b32 m0, s44\n" \
"s_nop 0\n" \
"buffer_load_dwordx4 v217, %[srdw], s49 offen offset:2048 lds\n" \
"v_mfma_f32_16x16x32_bf16 v[32:35], v[132:135], a[180:183], v[32:35]\n" \
"ds_read_b64_tr_b16 v[164:165], v225 offset:25600\n" \
"v_mfma_f32_16x16x32_bf16 v[64:67], v[128:131], a[168:171], v[64:67]\n" \
"ds_read_b64_tr_b16 v[166:167], v225 offset:25856\n" \
"v_mfma_f32_16x16x32_bf16 v[64:67], v[132:135], a[184:187], v[64:67]\n" \
"v_mfma_f32_16x16x32_bf16 v[96:99], v[128:131], a[172:175], v[96:99]\n" \
"ds_read_b64_tr_b16 v[168:169], v225 offset:17416\n" \
"v_mfma_f32_16x16x32_bf16 v[96:99], v[132:135], a[188:191], v[96:99]\n" \
"ds_read_b64_tr_b16 v[170:171], v225 offset:17672\n" \
"v_mfma_f32_16x16x32_bf16 v[4:7], v[136:139], a[160:163], v[4:7]\n" \
"buffer_load_dwordx4 v218, %[srdw], s49 offen offset:3072 lds\n" \
"v_mfma_f32_16x16x32_bf16 v[4:7], v[140:143], a[176:179], v[4:7]\n" \
"ds_read_b64_tr_b16 v[172:173], v225 offset:25608\n" \
"v_mfma_f32_16x16x32_bf16 v[36:39], v[136:139], a[164:167], v[36:39]\n" \
"ds_read_b64_tr_b16 v[174:175], v225 offset:25864\n" \
"v_mfma_f32_16x16x32_bf16 v[36:39], v[140:143], a[180:183], v[36:39]\n" \
"v_mfma_f32_16x16x32_bf16 v[68:71], v[136:139], a[168:171], v[68:71]\n" \
"ds_read_b64_tr_b16 v[176:177], v225 offset:17920\n" \
"v_mfma_f32_16x16x32_bf16 v[68:71], v[140:143], a[184:187], v[68:71]\n" \
"ds_read_b64_tr_b16 v[178:179], v225 offset:18176\n" \
"v_mfma_f32_16x16x32_bf16 v[100:103], v[136:139], a[172:175], v[100:103]\n" \
"s_add_u32 s50, s50, s53\n" \
"buffer_load_dwordx4 a[128:131], v227, %[srdz], s50 offen offset:0 sc1\n" \
"v_mfma_f32_16x16x32_bf16 v[100:103], v[140:143], a[188:191], v[100:103]\n" \
"ds_read_b64_tr_b16 v[180:181], v225 offset:26112\n" \
"v_mfma_f32_16x16x32_bf16 v[8:11], v[144:147], a[160:163], v[8:11]\n" \
"ds_read_b64_tr_b16 v[182:183], v225 offset:26368\n" \
"v_mfma_f32_16x16x32_bf16 v[8:11], v[148:151], a[176:179], v[8:11]\n" \
"v_mfma_f32_16x16x32_bf16 v[40:43], v[144:147], a[164:167], v[40:43]\n" \
"ds_read_b64_tr_b16 v[184:185], v225 offset:17928\n" \
"v_mfma_f32_16x16x32_bf16 v[40:43], v[148:151], a[180:183], v[40:43]\n" \
"ds_read_b64_tr_b16 v[186:187], v225 offset:18184\n" \
"v_mfma_f32_16x16x32_bf16 v[72:75], v[144:147], a[168:171], v[72:75]\n" \
"buffer_load_dwordx4 a[132:135], v219, %[srdz], s50 offen offset:0 sc1\n" \
"v_mfma_f32_16x16x32_bf16 v[72:75], v[148:151], a[184:187], v[72:75]\n" \
"ds_read_b64_tr_b16 v[188:189], v225 offset:26120\n" \
"v_mfma_f32_16x16x32_bf16 v[104:107], v[144:147], a[172:175], v[104:107]\n" \
"ds_read_b64_tr_b16 v[190:191], v225 offset:26376\n" \
"v_mfma_f32_16x16x32_bf16 v[104:107], v[148:151], a[188:191], v[104:107]\n" \
"v_mfma_f32_16x16x32_bf16 v[12:15], v[152:155], a[160:163], v[12:15]\n" \
"buffer_load_dwordx4 a[136:139], v220, %[srdz], s50 offen offset:0 sc1\n" \
"v_mfma_f32_16x16x32_bf16 v[12:15], v[156:159], a[176:179], v[12:15]\n" \
"v_mfma_f32_16x16x32_bf16 v[44:47], v[152:155], a[164:167], v[44:47]\n" \
"v_mfma_f32_16x16x32_bf16 v[44:47], v[156:159], a[180:183], v[44:47]\n" \
"v_mfma_f32_16x16x32_bf16 v[76:79], v[152:155], a[168:171], v[76:79]\n" \
"buffer_load_dwordx4 a[140:143], v221, %[srdz], s50 offen offset:0 sc1\n" \
"v_mfma_f32_16x16x32_bf16 v[76:79], v[156:159], a[184:187], v[76:79]\n" \
"v_mfma_f32_16x16x32_bf16 v[108:111], v[152:155], a[172:175], v[108:111]\n" \
"v_mfma_f32_16x16x32_bf16 v[108:111], v[156:159], a[188:191], v[108:111]\n" \
"s_waitcnt vmcnt(29) lgkmcnt(0)\n" \
"s_barrier\n" \
"v_mfma_f32_16x16x32_bf16 v[16:19], v[160:163], a[160:163], v[16:19]\n" \
"ds_read_b64_tr_b16 v[128:129], v225 offset:32768\n" \
"v_mfma_f32_16x16x32_bf16 v[16:19], v[164:167], a[176:179], v[16:19]\n" \
"ds_read_b64_tr_b16 v[130:131], v225 offset:33024\n" \
"v_mfma_f32_16x16x32_bf16 v[48:51], v[160:163], a[164:167], v[48:51]\n" \
"s_add_u32 s49, s49, s55\n" \
"s_mov_b32 m0, s45\n" \
"s_nop 0\n" \
"buffer_load_dwordx4 v226, %[srdw], s49 offen offset:0 lds\n" \
"v_mfma_f32_16x16x32_bf16 v[48:51], v[164:167], a[180:183], v[48:51]\n" \
"ds_read_b64_tr_b16 v[132:133], v225 offset:40960\n" \
"v_mfma_f32_16x16x32_bf16 v[80:83], v[160:163], a[168:171], v[80:83]\n" \
"ds_read_b64_tr_b16 v[134:135], v225 offset:41216\n" \
"v_mfma_f32_16x16x32_bf16 v[80:83], v[164:167], a[184:187], v[80:83]\n" \
"v_mfma_f32_16x16x32_bf16 v[112:115], v[160:163], a[172:175], v[112:115]\n" \
"ds_read_b64_tr_b16 v[136:137], v225 offset:32776\n" \
"v_mfma_f32_16x16x32_bf16 v[112:115], v[164:167], a[188:191], v[112:115]\n" \
"ds_read_b64_tr_b16 v[138:139], v225 offset:33032\n" \
"v_mfma_f32_16x16x32_bf16 v[20:23], v[168:171], a[160:163], v[20:23]\n" \
"buffer_load_dwordx4 v216, %[srdw], s49 offen offset:1024 lds\n" \
"v_mfma_f32_16x16x32_bf16 v[20:23], v[172:175], a[176:179], v[20:23]\n" \
"ds_read_b64_tr_b16 v[140:141], v225 offset:40968\n" \
"v_mfma_f32_16x16x32_bf16 v[52:55], v[168:171], a[164:167], v[52:55]\n" \
"ds_read_b64_tr_b16 v[142:143], v225 offset:41224\n" \
"v_mfma_f32_16x16x32_bf16 v[52:55], v[172:175], a[180:183], v[52:55]\n" \
"v_mfma_f32_16x16x32_bf16 v[84:87], v[168:171], a[168:171], v[84:87]\n" \
"ds_read_b64_tr_b16 v[144:145], v225 offset:33280\n" \
"v_mfma_f32_16x16x32_bf16 v[84:87], v[172:175], a[184:187], v[84:87]\n" \
"ds_read_b64_tr_b16 v[146:147], v225 offset:33536\n" \
"v_mfma_f32_16x16x32_bf16 v[116:119], v[168:171], a[172:175], v[116:119]\n" \
"buffer_load_dwordx4 a[144:147], v227, %[srdz], s50 offen offset:1024 sc1\n" \
"v_mfma_f32_16x16x32_bf16 v[116:119], v[172:175], a[188:191], v[116:119]\n" \
"ds_read_b64_tr_b16 v[148:149], v225 offset:41472\n" \
"v_mfma_f32_16x16x32_bf16 v[24:27], v[176:179], a[160:163], v[24:27]\n" \
"ds_read_b64_tr_b16 v[150:151], v225 offset:41728\n" \
"v_mfma_f32_16x16x32_bf16 v[24:27], v[180:183], a[176:179], v[24:27]\n" \
"v_mfma_f32_16x16x32_bf16 v[56:59], v[176:179], a[164:167], v[56:59]\n" \
"ds_read_b64_tr_b16 v[152:153], v225 offset:33288\n" \
"v_mfma_f32_16x16x32_bf16 v[56:59], v[180:183], a[180:183], v[56:59]\n" \
"ds_read_b64_tr_b16 v[154:155], v225 offset:33544\n" \
"v_mfma_f32_16x16x32_bf16 v[88:91], v[176:179], a[168:171], v[88:91]\n" \
"buffer_load_dwordx4 a[148:151], v219, %[srdz], s50 offen offset:1024 sc1\n" \
"v_mfma_f32_16x16x32_bf16 v[88:91], v[180:183], a[184:187], v[88:91]\n" \
"ds_read_b64_tr_b16 v[156:157], v225 offset:41480\n" \
"v_mfma_f32_16x16x32_bf16 v[120:123], v[176:179], a[172:175], v[120:123]\n" \
"ds_read_b64_tr_b16 v[158:159], v225 offset:41736\n" \
"v_mfma_f32_16x16x32_bf16 v[120:123], v[180:183], a[188:191], v[120:123]\n" \
"v_mfma_f32_16x16x32_bf16 v[28:31], v[184:187], a[160:163], v[28:31]\n" \
"buffer_load_dwordx4 a[152:155], v220, %[srdz], s50 offen offset:1024 sc1\n" \
"v_mfma_f32_16x16x32_bf16 v[28:31], v[188:191], a[176:179], v[28:31]\n" \
"v_mfma_f32_16x16x32_bf16 v[60:63], v[184:187], a[164:167], v[60:63]\n" \
"v_mfma_f32_16x16x32_bf16 v[60:63], v[188:191], a[180:183], v[60:63]\n" \
"v_mfma_f32_16x16x32_bf16 v[92:95], v[184:187], a[168:171], v[92:95]\n" \
"buffer_load_dwordx4 a[156:159], v221, %[srdz], s50 offen offset:1024 sc1\n" \
"v_mfma_f32_16x16x32_bf16 v[92:95], v[188:191], a[184:187], v[92:95]\n" \
"v_mfma_f32_16x16x32_bf16 v[124:127], v[184:187], a[172:175], v[124:127]\n" \
"v_mfma_f32_16x16x32_bf16 v[124:127], v[188:191], a[188:191], v[124:127]\n" \
"s_waitcnt vmcnt(24) lgkmcnt(0)\n" \
"v_mfma_f32_16x16x32_bf16 v[0:3], v[128:131], a[192:195], v[0:3]\n" \
"ds_read_b64_tr_b16 v[160:161], v225 offset:33792\n" \
"v_mfma_f32_16x16x32_bf16 v[0:3], v[132:135], a[208:211], v[0:3]\n" \
"ds_read_b64_tr_b16 v[162:163], v225 offset:34048\n" \
"v_mfma_f32_16x16x32_bf16 v[32:35], v[128:131], a[196:199], v[32:35]\n" \
"s_mov_b32 m0, s45\n" \
"s_nop 0\n" \
"buffer_load_dwordx4 v217, %[srdw], s49 offen offset:2048 lds\n" \
"v_mfma_f32_16x16x32_bf16 v[32:35], v[132:135], a[212:215], v[32:35]\n" \
"ds_read_b64_tr_b16 v[164:165], v225 offset:41984\n" \
"v_mfma_f32_16x16x32_bf16 v[64:67], v[128:131], a[200:203], v[64:67]\n" \
"ds_read_b64_tr_b16 v[166:167], v225 offset:42240\n" \
"v_mfma_f32_16x16x32_bf16 v[64:67], v[132:135], a[216:219], v[64:67]\n" \
"v_mfma_f32_16x16x32_bf16 v[96:99], v[128:131], a[204:207], v[96:99]\n" \
"ds_read_b64_tr_b16 v[168:169], v225 offset:33800\n" \
"v_mfma_f32_16x16x32_bf16 v[96:99], v[132:135], a[220:223], v[96:99]\n" \
"ds_read_b64_tr_b16 v[170:171], v225 offset:34056\n" \
"v_mfma_f32_16x16x32_bf16 v[4:7], v[136:139], a[192:195], v[4:7]\n" \
"buffer_load_dwordx4 v218, %[srdw], s49 offen offset:3072 lds\n" \
"v_mfma_f32_16x16x32_bf16 v[4:7], v[140:143], a[208:211], v[4:7]\n" \
"ds_read_b64_tr_b16 v[172:173], v225 offset:41992\n" \
"v_mfma_f32_16x16x32_bf16 v[36:39], v[136:139], a[196:199], v[36:39]\n" \
"ds_read_b64_tr_b16 v[174:175], v225 offset:42248\n" \
"v_mfma_f32_16x16x32_bf16 v[36:39], v[140:143], a[212:215], v[36:39]\n" \
"v_mfma_f32_16x16x32_bf16 v[68:71], v[136:139], a[200:203], v[68:71]\n" \
"ds_read_b64_tr_b16 v[176:177], v225 offset:34304\n" \
"v_mfma_f32_16x16x32_bf16 v[68:71], v[140:143], a[216:219], v[68:71]\n" \
"ds_read_b64_tr_b16 v[178:179], v225 offset:34560\n" \
"v_mfma_f32_16x16x32_bf16 v[100:103], v[136:139], a[204:207], v[100:103]\n" \
"buffer_load_dwordx4 a[160:163], v227, %[srdz], s50 offen offset:2048 sc1\n" \
"v_mfma_f32_16x16x32_bf16 v[100:103], v[140:143], a[220:223], v[100:103]\n" \
"ds_read_b64_tr_b16 v[180:181], v225 offset:42496\n" \
"v_mfma_f32_16x16x32_bf16 v[8:11], v[144:147], a[192:195], v[8:11]\n" \
"ds_read_b64_tr_b16 v[182:183], v225 offset:42752\n" \
"v_mfma_f32_16x16x32_bf16 v[8:11], v[148:151], a[208:211], v[8:11]\n" \
"v_mfma_f32_16x16x32_bf16 v[40:43], v[144:147], a[196:199], v[40:43]\n" \
"ds_read_b64_tr_b16 v[184:185], v225 offset:34312\n" \
"v_mfma_f32_16x16x32_bf16 v[40:43], v[148:151], a[212:215], v[40:43]\n" \
"ds_read_b64_tr_b16 v[186:187], v225 offset:34568\n" \
"v_mfma_f32_16x16x32_bf16 v[72:75], v[144:147], a[200:203], v[72:75]\n" \
"buffer_load_dwordx4 a[164:167], v219, %[srdz], s50 offen offset:2048 sc1\n" \
"v_mfma_f32_16x16x32_bf16 v[72:75], v[148:151], a[216:219], v[72:75]\n" \
"ds_read_b64_tr_b16 v[188:189], v225 offset:42504\n" \
"v_mfma_f32_16x16x32_bf16 v[104:107], v[144:147], a[204:207], v[104:107]\n" \
"ds_read_b64_tr_b16 v[190:191], v225 offset:42760\n" \
"v_mfma_f32_16x16x32_bf16 v[104:107], v[148:151], a[220:223], v[104:107]\n" \
"v_mfma_f32_16x16x32_bf16 v[12:15], v[152:155], a[192:195], v[12:15]\n" \
"buffer_load_dwordx4 a[168:171], v220, %[srdz], s50 offen offset:2048 sc1\n" \
"v_mfma_f32_16x16x32_bf16 v[12:15], v[156:159], a[208:211], v[12:15]\n" \
"v_mfma_f32_16x16x32_bf16 v[44:47], v[152:155], a[196:199], v[44:47]\n" \
"v_mfma_f32_16x16x32_bf16 v[44:47], v[156:159], a[212:215], v[44:47]\n" \
"v_mfma_f32_16x16x32_bf16 v[76:79], v[152:155], a[200:203], v[76:79]\n" \
"buffer_load_dwordx4 a[172:175], v221, %[srdz], s50 offen offset:2048 sc1\n" \
"v_mfma_f32_16x16x32_bf16 v[76:79], v[156:159], a[216:219], v[76:79]\n" \
"v_mfma_f32_16x16x32_bf16 v[108:111], v[152:155], a[204:207], v[108:111]\n" \
"v_mfma_f32_16x16x32_bf16 v[108:111], v[156:159], a[220:223], v[108:111]\n" \
"s_waitcnt vmcnt(28) lgkmcnt(0)\n" \
"s_barrier\n" \
"v_mfma_f32_16x16x32_bf16 v[16:19], v[160:163], a[192:195], v[16:19]\n" \
"ds_read_b64_tr_b16 v[128:129], v225 offset:49152\n" \
"v_mfma_f32_16x16x32_bf16 v[16:19], v[164:167], a[208:211], v[16:19]\n" \
"ds_read_b64_tr_b16 v[130:131], v225 offset:49408\n" \
"v_mfma_f32_16x16x32_bf16 v[48:51], v[160:163], a[196:199], v[48:51]\n" \
"s_add_u32 s49, s49, s55\n" \
"s_mov_b32 m0, s46\n" \
"s_nop 0\n" \
"buffer_load_dwordx4 v226, %[srdw], s49 offen offset:0 lds\n" \
"v_mfma_f32_16x16x32_bf16 v[48:51], v[164:167], a[212:215], v[48:51]\n" \
"ds_read_b64_tr_b16 v[132:133], v225 offset:57344\n" \
"v_mfma_f32_16x16x32_bf16 v[80:83], v[160:163], a[200:203], v[80:83]\n" \
"ds_read_b64_tr_b16 v[134:135], v225 offset:57600\n" \
"v_mfma_f32_16x16x32_bf16 v[80:83], v[164:167], a[216:219], v[80:83]\n" \
"v_mfma_f32_16x16x32_bf16 v[112:115], v[160:163], a[204:207], v[112:115]\n" \
"ds_read_b64_tr_b16 v[136:137], v225 offset:49160\n" \
"v_mfma_f32_16x16x32_bf16 v[112:115], v[164:167], a[220:223], v[112:115]\n" \
"ds_read_b64_tr_b16 v[138:139], v225 offset:49416\n" \
"v_mfma_f32_16x16x32_bf16 v[20:23], v[168:171], a[192:195], v[20:23]\n" \
"buffer_load_dwordx4 v216, %[srdw], s49 offen offset:1024 lds\n" \
"v_mfma_f32_16x16x32_bf16 v[20:23], v[172:175], a[208:211], v[20:23]\n" \
"ds_read_b64_tr_b16 v[140:141], v225 offset:57352\n" \
"v_mfma_f32_16x16x32_bf16 v[52:55], v[168:171], a[196:199], v[52:55]\n" \
"ds_read_b64_tr_b16 v[142:143], v225 offset:57608\n" \
"v_mfma_f32_16x16x32_bf16 v[52:55], v[172:175], a[212:215], v[52:55]\n" \
"v_mfma_f32_16x16x32_bf16 v[84:87], v[168:171], a[200:203], v[84:87]\n" \
"ds_read_b64_tr_b16 v[144:145], v225 offset:49664\n" \
"v_mfma_f32_16x16x32_bf16 v[84:87], v[172:175], a[216:219], v[84:87]\n" \
"ds_read_b64_tr_b16 v[146:147], v225 offset:49920\n" \
"v_mfma_f32_16x16x32_bf16 v[116:119], v[168:171], a[204:207], v[116:119]\n" \
"buffer_load_dwordx4 a[176:179], v227, %[srdz], s50 offen offset:3072 sc1\n" \
"v_mfma_f32_16x16x32_bf16 v[116:119], v[172:175], a[220:223], v[116:119]\n" \
"ds_read_b64_tr_b16 v[148:149], v225 offset:57856\n" \
"v_mfma_f32_16x16x32_bf16 v[24:27], v[176:179], a[192:195], v[24:27]\n" \
"ds_read_b64_tr_b16 v[150:151], v225 offset:58112\n" \
"v_mfma_f32_16x16x32_bf16 v[24:27], v[180:183], a[208:211], v[24:27]\n" \
"v_mfma_f32_16x16x32_bf16 v[56:59], v[176:179], a[196:199], v[56:59]\n" \
"ds_read_b64_tr_b16 v[152:153], v225 offset:49672\n" \
"v_mfma_f32_16x16x32_bf16 v[56:59], v[180:183], a[212:215], v[56:59]\n" \
"ds_read_b64_tr_b16 v[154:155], v225 offset:49928\n" \
"v_mfma_f32_16x16x32_bf16 v[88:91], v[176:179], a[200:203], v[88:91]\n" \
"buffer_load_dwordx4 a[180:183], v219, %[srdz], s50 offen offset:3072 sc1\n" \
"v_mfma_f32_16x16x32_bf16 v[88:91], v[180:183], a[216:219], v[88:91]\n" \
"ds_read_b64_tr_b16 v[156:157], v225 offset:57864\n" \
"v_mfma_f32_16x16x32_bf16 v[120:123], v[176:179], a[204:207], v[120:123]\n" \
"ds_read_b64_tr_b16 v[158:159], v225 offset:58120\n" \
"v_mfma_f32_16x16x32_bf16 v[120:123], v[180:183], a[220:223], v[120:123]\n" \
"v_mfma_f32_16x16x32_bf16 v[28:31], v[184:187], a[192:195], v[28:31]\n" \
"buffer_load_dwordx4 a[184:187], v220, %[srdz], s50 offen offset:3072 sc1\n" \
"v_mfma_f32_16x16x32_bf16 v[28:31], v[188:191], a[208:211], v[28:31]\n" \
"v_mfma_f32_16x16x32_bf16 v[60:63], v[184:187], a[196:199], v[60:63]\n" \
"v_mfma_f32_16x16x32_bf16 v[60:63], v[188:191], a[212:215], v[60:63]\n" \
"v_mfma_f32_16x16x32_bf16 v[92:95], v[184:187], a[200:203], v[92:95]\n" \
"buffer_load_dwordx4 a[188:191], v221, %[srdz], s50 offen offset:3072 sc1\n" \
"v_mfma_f32_16x16x32_bf16 v[92:95], v[188:191], a[216:219], v[92:95]\n" \
"v_mfma_f32_16x16x32_bf16 v[124:127], v[184:187], a[204:207], v[124:127]\n" \
"v_mfma_f32_16x16x32_bf16 v[124:127], v[188:191], a[220:223], v[124:127]\n" \
"s_waitcnt vmcnt(24) lgkmcnt(0)\n" \
"v_mfma_f32_16x16x32_bf16 v[0:3], v[128:131], a[224:227], v[0:3]\n" \
"ds_read_b64_tr_b16 v[160:161], v225 offset:50176\n" \
"v_mfma_f32_16x16x32_bf16 v[0:3], v[132:135], a[240:243], v[0:3]\n" \
"ds_read_b64_tr_b16 v[162:163], v225 offset:50432\n" \
"v_mfma_f32_16x16x32_bf16 v[32:35], v[128:131], a[228:231], v[32:35]\n" \
"s_mov_b32 m0, s46\n" \
"s_nop 0\n" \
"buffer_load_dwordx4 v217, %[srdw], s49 offen offset:2048 lds\n" \
"v_mfma_f32_16x16x32_bf16 v[32:35], v[132:135], a[244:247], v[32:35]\n" \
"ds_read_b64_tr_b16 v[164:165], v225 offset:58368\n" \
"v_mfma_f32_16x16x32_bf16 v[64:67], v[128:131], a[232:235], v[64:67]\n" \
"ds_read_b64_tr_b16 v[166:167], v225 offset:58624\n" \
"v_mfma_f32_16x16x32_bf16 v[64:67], v[132:135], a[248:251], v[64:67]\n" \
"v_mfma_f32_16x16x32_bf16 v[96:99], v[128:131], a[236:239], v[96:99]\n" \
"ds_read_b64_tr_b16 v[168:169], v225 offset:50184\n" \
"v_mfma_f32_16x16x32_bf16 v[96:99], v[132:135], a[252:255], v[96:99]\n" \
"ds_read_b64_tr_b16 v[170:171], v225 offset:50440\n" \
"v_mfma_f32_16x16x32_bf16 v[4:7], v[136:139], a[224:227], v[4:7]\n" \
"buffer_load_dwordx4 v218, %[srdw], s49 offen offset:3072 lds\n" \
"v_mfma_f32_16x16x32_bf16 v[4:7], v[140:143], a[240:243], v[4:7]\n" \
"ds_read_b64_tr_b16 v[172:173], v225 offset:58376\n" \
"v_mfma_f32_16x16x32_bf16 v[36:39], v[136:139], a[228:231], v[36:39]\n" \
"ds_read_b64_tr_b16 v[174:175], v225 offset:58632\n" \
"v_mfma_f32_16x16x32_bf16 v[36:39], v[140:143], a[244:247], v[36:39]\n" \
"v_mfma_f32_16x16x32_bf16 v[68:71], v[136:139], a[232:235], v[68:71]\n" \
"ds_read_b64_tr_b16 v[176:177], v225 offset:50688\n" \
"v_mfma_f32_16x16x32_bf16 v[68:71], v[140:143], a[248:251], v[68:71]\n" \
"ds_read_b64_tr_b16 v[178:179], v225 offset:50944\n" \
"v_mfma_f32_16x16x32_bf16 v[100:103], v[136:139], a[236:239], v[100:103]\n" \
"s_add_u32 s50, s50, s53\n" \
"buffer_load_dwordx4 a[192:195], v227, %[srdz], s50 offen offset:0 sc1\n" \
"v_mfma_f32_16x16x32_bf16 v[100:103], v[140:143], a[252:255], v[100:103]\n" \
"ds_read_b64_tr_b16 v[180:181], v225 offset:58880\n" \
"v_mfma_f32_16x16x32_bf16 v[8:11], v[144:147], a[224:227], v[8:11]\n" \
"ds_read_b64_tr_b16 v[182:183], v225 offset:59136\n" \
"v_mfma_f32_16x16x32_bf16 v[8:11], v[148:151], a[240:243], v[8:11]\n" \
"v_mfma_f32_16x16x32_bf16 v[40:43], v[144:147], a[228:231], v[40:43]\n" \
"ds_read_b64_tr_b16 v[184:185], v225 offset:50696\n" \
"v_mfma_f32_16x16x32_bf16 v[40:43], v[148:151], a[244:247], v[40:43]\n" \
"ds_read_b64_tr_b16 v[186:187], v225 offset:50952\n" \
"v_mfma_f32_16x16x32_bf16 v[72:75], v[144:147], a[232:235], v[72:75]\n" \
"buffer_load_dwordx4 a[196:199], v219, %[srdz], s50 offen offset:0 sc1\n" \
"v_mfma_f32_16x16x32_bf16 v[72:75], v[148:151], a[248:251], v[72:75]\n" \
"ds_read_b64_tr_b16 v[188:189], v225 offset:58888\n" \
"v_mfma_f32_16x16x32_bf16 v[104:107], v[144:147], a[236:239], v[104:107]\n" \
"ds_read_b64_tr_b16 v[190:191], v225 offset:59144\n" \
"v_mfma_f32_16x16x32_bf16 v[104:107], v[148:151], a[252:255], v[104:107]\n" \
"v_mfma_f32_16x16x32_bf16 v[12:15], v[152:155], a[224:227], v[12:15]\n" \
"buffer_load_dwordx4 a[200:203], v220, %[srdz], s50 offen offset:0 sc1\n" \
"v_mfma_f32_16x16x32_bf16 v[12:15], v[156:159], a[240:243], v[12:15]\n" \
"v_mfma_f32_16x16x32_bf16 v[44:47], v[152:155], a[228:231], v[44:47]\n" \
"buffer_load_dword v237, v236, %[srdw], s60 offen\n" \
"s_add_u32 s60, s60, 0x200000\n" \
"v_mfma_f32_16x16x32_bf16 v[44:47], v[156:159], a[244:247], v[44:47]\n" \
"v_mfma_f32_16x16x32_bf16 v[76:79], v[152:155], a[232:235], v[76:79]\n" \
"buffer_load_dwordx4 a[204:207], v221, %[srdz], s50 offen offset:0 sc1\n" \
"v_mfma_f32_16x16x32_bf16 v[76:79], v[156:159], a[248:251], v[76:79]\n" \
".if . - adaln_body != 4096\n" \
".error \"size model\"\n" \
".endif\n" \
"Lk0_%=:\n" \
"s_cbranch_execz Lk1_%=\n" \
"s_nop 0\n" \
"v_mfma_f32_16x16x32_bf16 v[108:111], v[152:155], a[236:239], v[108:111]\n" \
"v_mfma_f32_16x16x32_bf16 v[108:111], v[156:159], a[252:255], v[108:111]\n" \
"s_waitcnt vmcnt(29) lgkmcnt(0)\n" \
"s_barrier\n" \
"v_mfma_f32_16x16x32_bf16 v[16:19], v[160:163], a[224:227], v[16:19]\n" \
"ds_read_b64_tr_b16 v[128:129], v225 offset:0\n" \
"v_mfma_f32_16x16x32_bf16 v[16:19], v[164:167], a[240:243], v[16:19]\n" \
"ds_read_b64_tr_b16 v[130:131], v225 offset:256\n" \
"v_mfma_f32_16x16x32_bf16 v[48:51], v[160:163], a[228:231], v[48:51]\n" \
"s_add_u32 s49, s49, s55\n" \
"s_mov_b32 m0, s47\n" \
"s_nop 0\n" \
"buffer_load_dwordx4 v226, %[srdw], s49 offen offset:0 lds\n" \
"v_mfma_f32_16x16x32_bf16 v[48:51], v[164:167], a[244:247], v[48:51]\n" \
"ds_read_b64_tr_b16 v[132:133], v225 offset:8192\n" \
"v_mfma_f32_16x16x32_bf16 v[80:83], v[160:163], a[232:235], v[80:83]\n" \
"ds_read_b64_tr_b16 v[134:135], v225 offset:8448\n" \
"v_mfma_f32_16x16x32_bf16 v[80:83], v[164:167], a[248:251], v[80:83]\n" \
"v_mfma_f32_16x16x32_bf16 v[112:115], v[160:163], a[236:239], v[112:115]\n" \
"ds_read_b64_tr_b16 v[136:137], v225 offset:8\n" \
"v_mfma_f32_16x16x32_bf16 v[112:115], v[164:167], a[252:255], v[112:115]\n" \
"ds_read_b64_tr_b16 v[138:139], v225 offset:264\n" \
"v_mfma_f32_16x16x32_bf16 v[20:23], v[168:171], a[224:227], v[20:23]\n" \
"buffer_load_dwordx4 v216, %[srdw], s49 offen offset:1024 lds\n" \
"v_mfma_f32_16x16x32_bf16 v[20:23], v[172:175], a[240:243], v[20:23]\n" \
"ds_read_b64_tr_b16 v[140:141], v225 offset:8200\n" \
"v_mfma_f32_16x16x32_bf16 v[52:55], v[168:171], a[228:231], v[52:55]\n" \
"ds_read_b64_tr_b16 v[142:143], v225 offset:8456\n" \
"v_mfma_f32_16x16x32_bf16 v[52:55], v[172:175], a[244:247], v[52:55]\n" \
"v_mfma_f32_16x16x32_bf16 v[84:87], v[168:171], a[232:235], v[84:87]\n" \
"ds_read_b64_tr_b16 v[144:145], v225 offset:512\n" \
"v_mfma_f32_16x16x32_bf16 v[84:87], v[172:175], a[248:251], v[84:87]\n" \
"ds_read_b64_tr_b16 v[146:147], v225 offset:768\n" \
"v_mfma_f32_16x16x32_bf16 v[116:119], v[168:171], a[236:239], v[116:119]\n" \
"buffer_load_dwordx4 a[208:211], v227, %[srdz], s50 offen offset:1024 sc1\n" \
"v_mfma_f32_16x16x32_bf16 v[116:119], v[172:175], a[252:255], v[116:119]\n" \
"ds_read_b64_tr_b16 v[148:149], v225 offset:8704\n" \
"v_mfma_f32_16x16x32_bf16 v[24:27], v[176:179], a[224:227], v[24:27]\n" \
"ds_read_b64_tr_b16 v[150:151], v225 offset:8960\n" \
"v_mfma_f32_16x16x32_bf16 v[24:27], v[180:183], a[240:243], v[24:27]\n" \
"v_mfma_f32_16x16x32_bf16 v[56:59], v[176:179], a[228:231], v[56:59]\n" \
"ds_read_b64_tr_b16 v[152:153], v225 offset:520\n" \
"v_mfma_f32_16x16x32_bf16 v[56:59], v[180:183], a[244:247], v[56:59]\n" \
"ds_read_b64_tr_b16 v[154:155], v225 offset:776\n" \
"v_mfma_f32_16x16x32_bf16 v[88:91], v[176:179], a[232:235], v[88:91]\n" \
"buffer_load_dwordx4 a[212:215], v219, %[srdz], s50 offen offset:1024 sc1\n" \
"v_mfma_f32_16x16x32_bf16 v[88:91], v[180:183], a[248:251], v[88:91]\n" \
"ds_read_b64_tr_b16 v[156:157], v225 offset:8712\n" \
"v_mfma_f32_16x16x32_bf16 v[120:123], v[176:179], a[236:239], v[120:123]\n" \
"ds_read_b64_tr_b16 v[158:159], v225 offset:8968\n" \
"v_mfma_f32_16x16x32_bf16 v[120:123], v[180:183], a[252:255], v[120:123]\n" \
"v_mfma_f32_16x16x32_bf16 v[28:31], v[184:187], a[224:227], v[28:31]\n" \
"buffer_load_dwordx4 a[216:219], v220, %[srdz], s50 offen offset:1024 sc1\n" \
"v_mfma_f32_16x16x32_bf16 v[28:31], v[188:191], a[240:243], v[28:31]\n" \
"v_mfma_f32_16x16x32_bf16 v[60:63], v[184:187], a[228:231], v[60:63]\n" \
"v_mfma_f32_16x16x32_bf16 v[60:63], v[188:191], a[244:247], v[60:63]\n" \
"v_mfma_f32_16x16x32_bf16 v[92:95], v[184:187], a[232:235], v[92:95]\n" \
"buffer_load_dwordx4 a[220:223], v221, %[srdz], s50 offen offset:1024 sc1\n" \
"v_mfma_f32_16x16x32_bf16 v[92:95], v[188:191], a[248:251], v[92:95]\n" \
"v_mfma_f32_16x16x32_bf16 v[124:127], v[184:187], a[236:239], v[124:127]\n" \
"v_mfma_f32_16x16x32_bf16 v[124:127], v[188:191], a[252:255], v[124:127]\n" \
"s_waitcnt vmcnt(25) lgkmcnt(0)\n" \
"s_sub_u32 s51, s51, 1\n" \
"s_cmp_lg_u32 s51, 0\n" \
"s_cbranch_scc1 Lq_%=\n" \
"s_nop 0\n" \
"v_mfma_f32_16x16x32_bf16 v[0:3], v[128:131], a[128:131], v[0:3]\n" \
"ds_read_b64_tr_b16 v[160:161], v225 offset:1024\n" \
"v_mfma_f32_16x16x32_bf16 v[0:3], v[132:135], a[144:147], v[0:3]\n" \
"ds_read_b64_tr_b16 v[162:163], v225 offset:1280\n" \
"v_mfma_f32_16x16x32_bf16 v[32:35], v[128:131], a[132:135], v[32:35]\n" \
"s_mov_b32 m0, s47\n" \
"s_nop 0\n" \
"buffer_load_dwordx4 v217, %[srdw], s49 offen offset:2048 lds\n" \
"v_mfma_f32_16x16x32_bf16 v[32:35], v[132:135], a[148:151], v[32:35]\n" \
"ds_read_b64_tr_b16 v[164:165], v225 offset:9216\n" \
"v_mfma_f32_16x16x32_bf16 v[64:67], v[128:131], a[136:139], v[64:67]\n" \
"ds_read_b64_tr_b16 v[166:167], v225 offset:9472\n" \
"v_mfma_f32_16x16x32_bf16 v[64:67], v[132:135], a[152:155], v[64:67]\n" \
"v_mfma_f32_16x16x32_bf16 v[96:99], v[128:131], a[140:143], v[96:99]\n" \
"ds_read_b64_tr_b16 v[168:169], v225 offset:1032\n" \
"v_mfma_f32_16x16x32_bf16 v[96:99], v[132:135], a[156:159], v[96:99]\n" \
"ds_read_b64_tr_b16 v[170:171], v225 offset:1288\n" \
"v_mfma_f32_16x16x32_bf16 v[4:7], v[136:139], a[128:131], v[4:7]\n" \
"buffer_load_dwordx4 v218, %[srdw], s49 offen offset:3072 lds\n" \
"v_mfma_f32_16x16x32_bf16 v[4:7], v[140:143], a[144:147], v[4:7]\n" \
"ds_read_b64_tr_b16 v[172:173], v225 offset:9224\n" \
"v_mfma_f32_16x16x32_bf16 v[36:39], v[136:139], a[132:135], v[36:39]\n" \
"ds_read_b64_tr_b16 v[174:175], v225 offset:9480\n" \
"v_mfma_f32_16x16x32_bf16 v[36:39], v[140:143], a[148:151], v[36:39]\n" \
"v_mfma_f32_16x16x32_bf16 v[68:71], v[136:139], a[136:139], v[68:71]\n" \
"ds_read_b64_tr_b16 v[176:177], v225 offset:1536\n" \
"v_mfma_f32_16x16x32_bf16 v[68:71], v[140:143], a[152:155], v[68:71]\n" \
"ds_read_b64_tr_b16 v[178:179], v225 offset:1792\n" \
"v_mfma_f32_16x16x32_bf16 v[100:103], v[136:139], a[140:143], v[100:103]\n" \
"buffer_load_dwordx4 a[224:227], v227, %[srdz], s50 offen offset:2048 sc1\n" \
"v_mfma_f32_16x16x32_bf16 v[100:103], v[140:143], a[156:159], v[100:103]\n" \
"ds_read_b64_tr_b16 v[180:181], v225 offset:9728\n" \
"v_mfma_f32_16x16x32_bf16 v[8:11], v[144:147], a[128:131], v[8:11]\n" \
"ds_read_b64_tr_b16 v[182:183], v225 offset:9984\n" \
"v_mfma_f32_16x16x32_bf16 v[8:11], v[148:151], a[144:147], v[8:11]\n" \
"v_mfma_f32_16x16x32_bf16 v[40:43], v[144:147], a[132:135], v[40:43]\n" \
"ds_read_b64_tr_b16 v[184:185], v225 offset:1544\n" \
"v_mfma_f32_16x16x32_bf16 v[40:43], v[148:151], a[148:151], v[40:43]\n" \
"ds_read_b64_tr_b16 v[186:187], v225 offset:1800\n" \
"v_mfma_f32_16x16x32_bf16 v[72:75], v[144:147], a[136:139], v[72:75]\n" \
"buffer_load_dwordx4 a[228:231], v219, %[srdz], s50 offen offset:2048 sc1\n" \
"v_mfma_f32_16x16x32_bf16 v[72:75], v[148:151], a[152:155], v[72:75]\n" \
"ds_read_b64_tr_b16 v[188:189], v225 offset:9736\n" \
"v_mfma_f32_16x16x32_bf16 v[104:107], v[144:147], a[140:143], v[104:107]\n" \
"ds_read_b64_tr_b16 v[190:191], v225 offset:9992\n" \
"v_mfma_f32_16x16x32_bf16 v[104:107], v[148:151], a[156:159], v[104:107]\n" \
"v_mfma_f32_16x16x32_bf16 v[12:15], v[152:155], a[128:131], v[12:15]\n" \
"buffer_load_dwordx4 a[232:235], v220, %[srdz], s50 offen offset:2048 sc1\n" \
"v_mfma_f32_16x16x32_bf16 v[12:15], v[156:159], a[144:147], v[12:15]\n" \
"v_mfma_f32_16x16x32_bf16 v[44:47], v[152:155], a[132:135], v[44:47]\n" \
"v_mfma_f32_16x16x32_bf16 v[44:47], v[156:159], a[148:151], v[44:47]\n" \
"v_mfma_f32_16x16x32_bf16 v[76:79], v[152:155], a[136:139], v[76:79]\n" \
"buffer_load_dwordx4 a[236:239], v221, %[srdz], s50 offen offset:2048 sc1\n" \
"v_mfma_f32_16x16x32_bf16 v[76:79], v[156:159], a[152:155], v[76:79]\n" \
"v_mfma_f32_16x16x32_bf16 v[108:111], v[152:155], a[140:143], v[108:111]\n" \
"v_mfma_f32_16x16x32_bf16 v[108:111], v[156:159], a[156:159], v[108:111]\n" \
"s_waitcnt vmcnt(29) lgkmcnt(0)\n" \
"s_barrier\n" \
"v_mfma_f32_16x16x32_bf16 v[16:19], v[160:163], a[128:131], v[16:19]\n" \
"ds_read_b64_tr_b16 v[128:129], v225 offset:16384\n" \
"v_mfma_f32_16x16x32_bf16 v[16:19], v[164:167], a[144:147], v[16:19]\n" \
"ds_read_b64_tr_b16 v[130:131], v225 offset:16640\n" \
"v_mfma_f32_16x16x32_bf16 v[48:51], v[160:163], a[132:135], v[48:51]\n" \
"s_mov_b32 s48, 16384\n" \
"s_mov_b32 m0, s44\n" \
"s_nop 0\n" \
"buffer_load_dwordx4 %[vd0], %[srdv], s48 offen offset:0 lds\n" \
"v_mfma_f32_16x16x32_bf16 v[48:51], v[164:167], a[148:151], v[48:51]\n" \
"ds_read_b64_tr_b16 v[132:133], v225 offset:24576\n" \
"v_mfma_f32_16x16x32_bf16 v[80:83], v[160:163], a[136:139], v[80:83]\n" \
"ds_read_b64_tr_b16 v[134:135], v225 offset:24832\n" \
"v_mfma_f32_16x16x32_bf16 v[80:83], v[164:167], a[152:155], v[80:83]\n" \
"v_mfma_f32_16x16x32_bf16 v[112:115], v[160:163], a[140:143], v[112:115]\n" \
"ds_read_b64_tr_b16 v[136:137], v225 offset:16392\n" \
"v_mfma_f32_16x16x32_bf16 v[112:115], v[164:167], a[156:159], v[112:115]\n" \
"ds_read_b64_tr_b16 v[138:139], v225 offset:16648\n" \
"v_mfma_f32_16x16x32_bf16 v[20:23], v[168:171], a[128:131], v[20:23]\n" \
"buffer_load_dwordx4 v215, %[srdv], s48 offen offset:1024 lds\n" \
"v_mfma_f32_16x16x32_bf16 v[20:23], v[172:175], a[144:147], v[20:23]\n" \
"ds_read_b64_tr_b16 v[140:141], v225 offset:24584\n" \
"v_mfma_f32_16x16x32_bf16 v[52:55], v[168:171], a[132:135], v[52:55]\n" \
"ds_read_b64_tr_b16 v[142:143], v225 offset:24840\n" \
"v_mfma_f32_16x16x32_bf16 v[52:55], v[172:175], a[148:151], v[52:55]\n" \
"v_mfma_f32_16x16x32_bf16 v[84:87], v[168:171], a[136:139], v[84:87]\n" \
"ds_read_b64_tr_b16 v[144:145], v225 offset:16896\n" \
"v_mfma_f32_16x16x32_bf16 v[84:87], v[172:175], a[152:155], v[84:87]\n" \
"ds_read_b64_tr_b16 v[146:147], v225 offset:17152\n" \
"v_mfma_f32_16x16x32_bf16 v[116:119], v[168:171], a[140:143], v[116:119]\n" \
"buffer_load_dwordx4 a[240:243], v227, %[srdz], s50 offen offset:3072 sc1\n" \
"v_mfma_f32_16x16x32_bf16 v[116:119], v[172:175], a[156:159], v[116:119]\n" \
"ds_read_b64_tr_b16 v[148:149], v225 offset:25088\n" \
"v_mfma_f32_16x16x32_bf16 v[24:27], v[176:179], a[128:131], v[24:27]\n" \
"ds_read_b64_tr_b16 v[150:151], v225 offset:25344\n" \
"v_mfma_f32_16x16x32_bf16 v[24:27], v[180:183], a[144:147], v[24:27]\n" \
"v_mfma_f32_16x16x32_bf16 v[56:59], v[176:179], a[132:135], v[56:59]\n" \
"ds_read_b64_tr_b16 v[152:153], v225 offset:16904\n" \
"v_mfma_f32_16x16x32_bf16 v[56:59], v[180:183], a[148:151], v[56:59]\n" \
"ds_read_b64_tr_b16 v[154:155], v225 offset:17160\n" \
"v_mfma_f32_16x16x32_bf16 v[88:91], v[176:179], a[136:139], v[88:91]\n" \
"buffer_load_dwordx4 a[244:247], v219, %[srdz], s50 offen offset:3072 sc1\n" \
"v_mfma_f32_16x16x32_bf16 v[88:91], v[180:183], a[152:155], v[88:91]\n" \
"ds_read_b64_tr_b16 v[156:157], v225 offset:25096\n" \
"v_mfma_f32_16x16x32_bf16 v[120:123], v[176:179], a[140:143], v[120:123]\n" \
"ds_read_b64_tr_b16 v[158:159], v225 offset:25352\n" \
"v_mfma_f32_16x16x32_bf16 v[120:123], v[180:183], a[156:159], v[120:123]\n" \
"v_mfma_f32_16x16x32_bf16 v[28:31], v[184:187], a[128:131], v[28:31]\n" \
"buffer_load_dwordx4 a[248:251], v220, %[srdz], s50 offen offset:3072 sc1\n" \
"v_mfma_f32_16x16x32_bf16 v[28:31], v[188:191], a[144:147], v[28:31]\n" \
"v_mfma_f32_16x16x32_bf16 v[60:63], v[184:187], a[132:135], v[60:63]\n" \
"v_mfma_f32_16x16x32_bf16 v[60:63], v[188:191], a[148:151], v[60:63]\n" \
"v_mfma_f32_16x16x32_bf16 v[92:95], v[184:187], a[136:139], v[92:95]\n" \
"buffer_load_dwordx4 a[252:255], v221, %[srdz], s50 offen offset:3072 sc1\n" \
"v_mfma_f32_16x16x32_bf16 v[92:95], v[188:191], a[152:155], v[92:95]\n" \
"v_mfma_f32_16x16x32_bf16 v[124:127], v[184:187], a[140:143], v[124:127]\n" \
"v_mfma_f32_16x16x32_bf16 v[124:127], v[188:191], a[156:159], v[124:127]\n" \
"s_waitcnt vmcnt(25) lgkmcnt(0)\n" \
"v_mfma_f32_16x16x32_bf16 v[0:3], v[128:131], a[160:163], v[0:3]\n" \
"ds_read_b64_tr_b16 v[160:161], v225 offset:17408\n" \
"v_mfma_f32_16x16x32_bf16 v[0:3], v[132:135], a[176:179], v[0:3]\n" \
"ds_read_b64_tr_b16 v[162:163], v225 offset:17664\n" \
"v_mfma_f32_16x16x32_bf16 v[32:35], v[128:131], a[164:167], v[32:35]\n" \
"s_mov_b32 s48, 16384\n" \
"s_mov_b32 m0, s44\n" \
"s_nop 0\n" \
"buffer_load_dwordx4 %[vd0], %[srdv], s48 offen offset:2048 lds\n" \
"v_mfma_f32_16x16x32_bf16 v[32:35], v[132:135], a[180:183], v[32:35]\n" \
"ds_read_b64_tr_b16 v[164:165], v225 offset:25600\n" \
"v_mfma_f32_16x16x32_bf16 v[64:67], v[128:131], a[168:171], v[64:67]\n" \
"ds_read_b64_tr_b16 v[166:167], v225 offset:25856\n" \
"v_mfma_f32_16x16x32_bf16 v[64:67], v[132:135], a[184:187], v[64:67]\n" \
"v_mfma_f32_16x16x32_bf16 v[96:99], v[128:131], a[172:175], v[96:99]\n" \
"ds_read_b64_tr_b16 v[168:169], v225 offset:17416\n" \
"v_mfma_f32_16x16x32_bf16 v[96:99], v[132:135], a[188:191], v[96:99]\n" \
"ds_read_b64_tr_b16 v[170:171], v225 offset:17672\n" \
"v_mfma_f32_16x16x32_bf16 v[4:7], v[136:139], a[160:163], v[4:7]\n" \
"buffer_load_dwordx4 v215, %[srdv], s48 offen offset:3072 lds\n" \
"v_mfma_f32_16x16x32_bf16 v[4:7], v[140:143], a[176:179], v[4:7]\n" \
"ds_read_b64_tr_b16 v[172:173], v225 offset:25608\n" \
"v_mfma_f32_16x16x32_bf16 v[36:39], v[136:139], a[164:167], v[36:39]\n" \
"ds_read_b64_tr_b16 v[174:175], v225 offset:25864\n" \
"v_mfma_f32_16x16x32_bf16 v[36:39], v[140:143], a[180:183], v[36:39]\n" \
"v_mfma_f32_16x16x32_bf16 v[68:71], v[136:139], a[168:171], v[68:71]\n" \
"ds_read_b64_tr_b16 v[176:177], v225 offset:17920\n" \
"v_mfma_f32_16x16x32_bf16 v[68:71], v[140:143], a[184:187], v[68:71]\n" \
"ds_read_b64_tr_b16 v[178:179], v225 offset:18176\n" \
"v_mfma_f32_16x16x32_bf16 v[100:103], v[136:139], a[172:175], v[100:103]\n" \
"s_mov_b32 s48, 0\n" \
"s_mov_b32 m0, s40\n" \
"s_nop 0\n" \
"buffer_load_dwordx4 %[kd0], %[srdk], s48 offen offset:0 lds\n" \
"v_mfma_f32_16x16x32_bf16 v[100:103], v[140:143], a[188:191], v[100:103]\n" \
"ds_read_b64_tr_b16 v[180:181], v225 offset:26112\n" \
"v_mfma_f32_16x16x32_bf16 v[8:11], v[144:147], a[160:163], v[8:11]\n" \
"ds_read_b64_tr_b16 v[182:183], v225 offset:26368\n" \
"v_mfma_f32_16x16x32_bf16 v[8:11], v[148:151], a[176:179], v[8:11]\n" \
"v_mfma_f32_16x16x32_bf16 v[40:43], v[144:147], a[164:167], v[40:43]\n" \
"ds_read_b64_tr_b16 v[184:185], v225 offset:17928\n" \
"v_mfma_f32_16x16x32_bf16 v[40:43], v[148:151], a[180:183], v[40:43]\n" \
"ds_read_b64_tr_b16 v[186:187], v225 offset:18184\n" \
"v_mfma_f32_16x16x32_bf16 v[72:75], v[144:147], a[168:171], v[72:75]\n" \
"buffer_load_dwordx4 v212, %[srdk], s48 offen offset:1024 lds\n" \
"v_mfma_f32_16x16x32_bf16 v[72:75], v[148:151], a[184:187], v[72:75]\n" \
"ds_read_b64_tr_b16 v[188:189], v225 offset:26120\n" \
"v_mfma_f32_16x16x32_bf16 v[104:107], v[144:147], a[172:175], v[104:107]\n" \
"ds_read_b64_tr_b16 v[190:191], v225 offset:26376\n" \
"v_mfma_f32_16x16x32_bf16 v[104:107], v[148:151], a[188:191], v[104:107]\n" \
"v_mfma_f32_16x16x32_bf16 v[12:15], v[152:155], a[160:163], v[12:15]\n" \
"buffer_load_dwordx4 v213, %[srdk], s48 offen offset:2048 lds\n" \
"v_mfma_f32_16x16x32_bf16 v[12:15], v[156:159], a[176:179], v[12:15]\n" \
"v_mfma_f32_16x16x32_bf16 v[44:47], v[152:155], a[164:167], v[44:47]\n" \
"v_mfma_f32_16x16x32_bf16 v[44:47], v[156:159], a[180:183], v[44:47]\n" \
"v_mfma_f32_16x16x32_bf16 v[76:79], v[152:155], a[168:171], v[76:79]\n" \
"buffer_load_dwordx4 v214, %[srdk], s48 offen offset:3072 lds\n" \
"v_mfma_f32_16x16x32_bf16 v[76:79], v[156:159], a[184:187], v[76:79]\n" \
"v_mfma_f32_16x16x32_bf16 v[108:111], v[152:155], a[172:175], v[108:111]\n" \
"v_mfma_f32_16x16x32_bf16 v[108:111], v[156:159], a[188:191], v[108:111]\n" \
"s_waitcnt vmcnt(29) lgkmcnt(0)\n" \
"s_barrier\n" \
"v_mfma_f32_16x16x32_bf16 v[16:19], v[160:163], a[160:163], v[16:19]\n" \
"ds_read_b64_tr_b16 v[128:129], v225 offset:32768\n" \
"v_mfma_f32_16x16x32_bf16 v[16:19], v[164:167], a[176:179], v[16:19]\n" \
"ds_read_b64_tr_b16 v[130:131], v225 offset:33024\n" \
"v_mfma_f32_16x16x32_bf16 v[48:51], v[160:163], a[164:167], v[48:51]\n" \
"s_mov_b32 s48, 32768\n" \
"s_mov_b32 m0, s45\n" \
"s_nop 0\n" \
"buffer_load_dwordx4 %[vd0], %[srdv], s48 offen offset:0 lds\n" \
"v_mfma_f32_16x16x32_bf16 v[48:51], v[164:167], a[180:183], v[48:51]\n" \
"ds_read_b64_tr_b16 v[132:133], v225 offset:40960\n" \
"v_mfma_f32_16x16x32_bf16 v[80:83], v[160:163], a[168:171], v[80:83]\n" \
"ds_read_b64_tr_b16 v[134:135], v225 offset:41216\n" \
"v_mfma_f32_16x16x32_bf16 v[80:83], v[164:167], a[184:187], v[80:83]\n" \
"v_mfma_f32_16x16x32_bf16 v[112:115], v[160:163], a[172:175], v[112:115]\n" \
"ds_read_b64_tr_b16 v[136:137], v225 offset:32776\n" \
"v_mfma_f32_16x16x32_bf16 v[112:115], v[164:167], a[188:191], v[112:115]\n" \
"ds_read_b64_tr_b16 v[138:139], v225 offset:33032\n" \
"v_mfma_f32_16x16x32_bf16 v[20:23], v[168:171], a[160:163], v[20:23]\n" \
"buffer_load_dwordx4 v215, %[srdv], s48 offen offset:1024 lds\n" \
"v_mfma_f32_16x16x32_bf16 v[20:23], v[172:175], a[176:179], v[20:23]\n" \
"ds_read_b64_tr_b16 v[140:141], v225 offset:40968\n" \
"v_mfma_f32_16x16x32_bf16 v[52:55], v[168:171], a[164:167], v[52:55]\n" \
"ds_read_b64_tr_b16 v[142:143], v225 offset:41224\n" \
"v_mfma_f32_16x16x32_bf16 v[52:55], v[172:175], a[180:183], v[52:55]\n" \
"v_mfma_f32_16x16x32_bf16 v[84:87], v[168:171], a[168:171], v[84:87]\n" \
"ds_read_b64_tr_b16 v[144:145], v225 offset:33280\n" \
"v_mfma_f32_16x16x32_bf16 v[84:87], v[172:175], a[184:187], v[84:87]\n" \
"ds_read_b64_tr_b16 v[146:147], v225 offset:33536\n" \
"v_mfma_f32_16x16x32_bf16 v[116:119], v[168:171], a[172:175], v[116:119]\n" \
"s_mov_b32 s48, 16384\n" \
"s_mov_b32 m0, s41\n" \
"s_nop 0\n" \
"buffer_load_dwordx4 %[kd0], %[srdk], s48 offen offset:0 lds\n" \
"v_mfma_f32_16x16x32_bf16 v[116:119], v[172:175], a[188:191], v[116:119]\n" \
"ds_read_b64_tr_b16 v[148:149], v225 offset:41472\n" \
"v_mfma_f32_16x16x32_bf16 v[24:27], v[176:179], a[160:163], v[24:27]\n" \
"ds_read_b64_tr_b16 v[150:151], v225 offset:41728\n" \
"v_mfma_f32_16x16x32_bf16 v[24:27], v[180:183], a[176:179], v[24:27]\n" \
"v_mfma_f32_16x16x32_bf16 v[56:59], v[176:179], a[164:167], v[56:59]\n" \
"ds_read_b64_tr_b16 v[152:153], v225 offset:33288\n" \
"v_mfma_f32_16x16x32_bf16 v[56:59], v[180:183], a[180:183], v[56:59]\n" \
"ds_read_b64_tr_b16 v[154:155], v225 offset:33544\n" \
"v_mfma_f32_16x16x32_bf16 v[88:91], v[176:179], a[168:171], v[88:91]\n" \
"buffer_load_dwordx4 v212, %[srdk], s48 offen offset:1024 lds\n" \
"v_mfma_f32_16x16x32_bf16 v[88:91], v[180:183], a[184:187], v[88:91]\n" \
"ds_read_b64_tr_b16 v[156:157], v225 offset:41480\n" \
"v_mfma_f32_16x16x32_bf16 v[120:123], v[176:179], a[172:175], v[120:123]\n" \
"ds_read_b64_tr_b16 v[158:159], v225 offset:41736\n" \
"v_mfma_f32_16x16x32_bf16 v[120:123], v[180:183], a[188:191], v[120:123]\n" \
"v_mfma_f32_16x16x32_bf16 v[28:31], v[184:187], a[160:163], v[28:31]\n" \
"buffer_load_dwordx4 v213, %[srdk], s48 offen offset:2048 lds\n" \
"v_mfma_f32_16x16x32_bf16 v[28:31], v[188:191], a[176:179], v[28:31]\n" \
"v_mfma_f32_16x16x32_bf16 v[60:63], v[184:187], a[164:167], v[60:63]\n" \
"v_mfma_f32_16x16x32_bf16 v[60:63], v[188:191], a[180:183], v[60:63]\n" \
"v_mfma_f32_16x16x32_bf16 v[92:95], v[184:187], a[168:171], v[92:95]\n" \
"buffer_load_dwordx4 v214, %[srdk], s48 offen offset:3072 lds\n" \
"v_mfma_f32_16x16x32_bf16 v[92:95], v[188:191], a[184:187], v[92:95]\n" \
"v_mfma_f32_16x16x32_bf16 v[124:127], v[184:187], a[172:175], v[124:127]\n" \
"v_mfma_f32_16x16x32_bf16 v[124:127], v[188:191], a[188:191], v[124:127]\n" \
"s_waitcnt vmcnt(24) lgkmcnt(0)\n" \
"v_mfma_f32_16x16x32_bf16 v[0:3], v[128:131], a[192:195], v[0:3]\n" \
"ds_read_b64_tr_b16 v[160:161], v225 offset:33792\n" \
"v_mfma_f32_16x16x32_bf16 v[0:3], v[132:135], a[208:211], v[0:3]\n" \
"ds_read_b64_tr_b16 v[162:163], v225 offset:34048\n" \
"v_mfma_f32_16x16x32_bf16 v[32:35], v[128:131], a[196:199], v[32:35]\n" \
"s_mov_b32 s48, 32768\n" \
"s_mov_b32 m0, s45\n" \
"s_nop 0\n" \
"buffer_load_dwordx4 %[vd0], %[srdv], s48 offen offset:2048 lds\n" \
"v_mfma_f32_16x16x32_bf16 v[32:35], v[132:135], a[212:215], v[32:35]\n" \
"ds_read_b64_tr_b16 v[164:165], v225 offset:41984\n" \
"v_mfma_f32_16x16x32_bf16 v[64:67], v[128:131], a[200:203], v[64:67]\n" \
"ds_read_b64_tr_b16 v[166:167], v225 offset:42240\n" \
"v_mfma_f32_16x16x32_bf16 v[64:67], v[132:135], a[216:219], v[64:67]\n" \
"v_mfma_f32_16x16x32_bf16 v[96:99], v[128:131], a[204:207], v[96:99]\n" \
"ds_read_b64_tr_b16 v[168:169], v225 offset:33800\n" \
"v_mfma_f32_16x16x32_bf16 v[96:99], v[132:135], a[220:223], v[96:99]\n" \
"ds_read_b64_tr_b16 v[170:171], v225 offset:34056\n" \
"v_mfma_f32_16x16x32_bf16 v[4:7], v[136:139], a[192:195], v[4:7]\n" \
"buffer_load_dwordx4 v215, %[srdv], s48 offen offset:3072 lds\n" \
"v_mfma_f32_16x16x32_bf16 v[4:7], v[140:143], a[208:211], v[4:7]\n" \
"ds_read_b64_tr_b16 v[172:173], v225 offset:41992\n" \
"v_mfma_f32_16x16x32_bf16 v[36:39], v[136:139], a[196:199], v[36:39]\n" \
"ds_read_b64_tr_b16 v[174:175], v225 offset:42248\n" \
"v_mfma_f32_16x16x32_bf16 v[36:39], v[140:143], a[212:215], v[36:39]\n" \
"v_mfma_f32_16x16x32_bf16 v[68:71], v[136:139], a[200:203], v[68:71]\n" \
"ds_read_b64_tr_b16 v[176:177], v225 offset:34304\n" \
"v_mfma_f32_16x16x32_bf16 v[68:71], v[140:143], a[216:219], v[68:71]\n" \
"ds_read_b64_tr_b16 v[178:179], v225 offset:34560\n" \
"v_mfma_f32_16x16x32_bf16 v[100:103], v[136:139], a[204:207], v[100:103]\n" \
"s_mov_b32 s48, 32768\n" \
"s_mov_b32 m0, s42\n" \
"s_nop 0\n" \
"buffer_load_dwordx4 %[kd0], %[srdk], s48 offen offset:0 lds\n" \
"v_mfma_f32_16x16x32_bf16 v[100:103], v[140:143], a[220:223], v[100:103]\n" \
"ds_read_b64_tr_b16 v[180:181], v225 offset:42496\n" \
"v_mfma_f32_16x16x32_bf16 v[8:11], v[144:147], a[192:195], v[8:11]\n" \
"ds_read_b64_tr_b16 v[182:183], v225 offset:42752\n" \
"v_mfma_f32_16x16x32_bf16 v[8:11], v[148:151], a[208:211], v[8:11]\n" \
"v_mfma_f32_16x16x32_bf16 v[40:43], v[144:147], a[196:199], v[40:43]\n" \
"ds_read_b64_tr_b16 v[184:185], v225 offset:34312\n" \
"v_mfma_f32_16x16x32_bf16 v[40:43], v[148:151], a[212:215], v[40:43]\n" \
"ds_read_b64_tr_b16 v[186:187], v225 offset:34568\n" \
"v_mfma_f32_16x16x32_bf16 v[72:75], v[144:147], a[200:203], v[72:75]\n" \
"buffer_load_dwordx4 v212, %[srdk], s48 offen offset:1024 lds\n" \
"v_mfma_f32_16x16x32_bf16 v[72:75], v[148:151], a[216:219], v[72:75]\n" \
"ds_read_b64_tr_b16 v[188:189], v225 offset:42504\n" \
"v_mfma_f32_16x16x32_bf16 v[104:107], v[144:147], a[204:207], v[104:107]\n" \
"ds_read_b64_tr_b16 v[190:191], v225 offset:42760\n" \
"v_mfma_f32_16x16x32_bf16 v[104:107], v[148:151], a[220:223], v[104:107]\n" \
"v_mfma_f32_16x16x32_bf16 v[12:15], v[152:155], a[192:195], v[12:15]\n" \
"buffer_load_dwordx4 v213, %[srdk], s48 offen offset:2048 lds\n" \
"v_mfma_f32_16x16x32_bf16 v[12:15], v[156:159], a[208:211], v[12:15]\n" \
"v_mfma_f32_16x16x32_bf16 v[44:47], v[152:155], a[196:199], v[44:47]\n" \
"v_mfma_f32_16x16x32_bf16 v[44:47], v[156:159], a[212:215], v[44:47]\n" \
"v_mfma_f32_16x16x32_bf16 v[76:79], v[152:155], a[200:203], v[76:79]\n" \
"buffer_load_dwordx4 v214, %[srdk], s48 offen offset:3072 lds\n" \
"v_mfma_f32_16x16x32_bf16 v[76:79], v[156:159], a[216:219], v[76:79]\n" \
"v_mfma_f32_16x16x32_bf16 v[108:111], v[152:155], a[204:207], v[108:111]\n" \
"v_mfma_f32_16x16x32_bf16 v[108:111], v[156:159], a[220:223], v[108:111]\n" \
"s_waitcnt vmcnt(28) lgkmcnt(0)\n" \
"s_barrier\n" \
"v_mfma_f32_16x16x32_bf16 v[16:19], v[160:163], a[192:195], v[16:19]\n" \
"ds_read_b64_tr_b16 v[128:129], v225 offset:49152\n" \
"s_mov_b32 s54, 0x4000\n" \
"v_mfma_f32_16x16x32_bf16 v[16:19], v[164:167], a[208:211], v[16:19]\n" \
"ds_read_b64_tr_b16 v[130:131], v225 offset:49408\n" \
"v_mfma_f32_16x16x32_bf16 v[48:51], v[160:163], a[196:199], v[48:51]\n" \
"s_mov_b32 s48, 49152\n" \
"s_mov_b32 m0, s46\n" \
"s_nop 0\n" \
"buffer_load_dwordx4 %[vd0], %[srdv], s48 offen offset:0 lds\n" \
"v_mbcnt_lo_u32_b32 v231, -1, 0\n" \
"v_mfma_f32_16x16x32_bf16 v[48:51], v[164:167], a[212:215], v[48:51]\n" \
"ds_read_b64_tr_b16 v[132:133], v225 offset:57344\n" \
"v_mfma_f32_16x16x32_bf16 v[80:83], v[160:163], a[200:203], v[80:83]\n" \
"ds_read_b64_tr_b16 v[134:135], v225 offset:57600\n" \
"v_mfma_f32_16x16x32_bf16 v[80:83], v[164:167], a[216:219], v[80:83]\n" \
"v_mbcnt_hi_u32_b32 v231, -1, v231\n" \
"v_mfma_f32_16x16x32_bf16 v[112:115], v[160:163], a[204:207], v[112:115]\n" \
"ds_read_b64_tr_b16 v[136:137], v225 offset:49160\n" \
"v_mfma_f32_16x16x32_bf16 v[112:115], v[164:167], a[220:223], v[112:115]\n" \
"ds_read_b64_tr_b16 v[138:139], v225 offset:49416\n" \
"v_mfma_f32_16x16x32_bf16 v[20:23], v[168:171], a[192:195], v[20:23]\n" \
"buffer_load_dwordx4 v215, %[srdv], s48 offen offset:1024 lds\n" \
"v_lshrrev_b32_e32 v232, 4, v231\n" \
"v_mfma_f32_16x16x32_bf16 v[20:23], v[172:175], a[208:211], v[20:23]\n" \
"ds_read_b64_tr_b16 v[140:141], v225 offset:57352\n" \
"v_mfma_f32_16x16x32_bf16 v[52:55], v[168:171], a[196:199], v[52:55]\n" \
"ds_read_b64_tr_b16 v[142:143], v225 offset:57608\n" \
"v_xor_b32_e64 v232, v232, v231\n" \
"v_mfma_f32_16x16x32_bf16 v[52:55], v[172:175], a[212:215], v[52:55]\n" \
"v_mfma_f32_16x16x32_bf16 v[84:87], v[168:171], a[200:203], v[84:87]\n" \
"ds_read_b64_tr_b16 v[144:145], v225 offset:49664\n" \
"v_mfma_f32_16x16x32_bf16 v[84:87], v[172:175], a[216:219], v[84:87]\n" \
"ds_read_b64_tr_b16 v[146:147], v225 offset:49920\n" \
"v_bfe_i32 v232, v232, 1, 1\n" \
"v_mfma_f32_16x16x32_bf16 v[116:119], v[168:171], a[204:207], v[116:119]\n" \
"s_mov_b32 s48, 49152\n" \
"s_mov_b32 m0, s43\n" \
"s_nop 0\n" \
"buffer_load_dwordx4 %[kd0], %[srdk], s48 offen offset:0 lds\n" \
"v_mfma_f32_16x16x32_bf16 v[116:119], v[172:175], a[220:223], v[116:119]\n" \
"ds_read_b64_tr_b16 v[148:149], v225 offset:57856\n" \
"v_mfma_f32_16x16x32_bf16 v[24:27], v[176:179], a[192:195], v[24:27]\n" \
"ds_read_b64_tr_b16 v[150:151], v225 offset:58112\n" \
"v_not_b32_e64 v232, v232\n" \
"v_mfma_f32_16x16x32_bf16 v[24:27], v[180:183], a[208:211], v[24:27]\n" \
"v_mfma_f32_16x16x32_bf16 v[56:59], v[176:179], a[196:199], v[56:59]\n" \
"ds_read_b64_tr_b16 v[152:153], v225 offset:49672\n" \
"v_mfma_f32_16x16x32_bf16 v[56:59], v[180:183], a[212:215], v[56:59]\n" \
"ds_read_b64_tr_b16 v[154:155], v225 offset:49928\n" \
"v_and_b32_e32 v230, 0x3f803f80, v232\n" \
"v_mfma_f32_16x16x32_bf16 v[88:91], v[176:179], a[200:203], v[88:91]\n" \
"buffer_load_dwordx4 v212, %[srdk], s48 offen offset:1024 lds\n" \
"v_mfma_f32_16x16x32_bf16 v[88:91], v[180:183], a[216:219], v[88:91]\n" \
"ds_read_b64_tr_b16 v[156:157], v225 offset:57864\n" \
"v_readlane_b32 s62, %[tlo], 18\n" \
"v_mfma_f32_16x16x32_bf16 v[120:123], v[176:179], a[204:207], v[120:123]\n" \
"ds_read_b64_tr_b16 v[158:159], v225 offset:58120\n" \
"v_mfma_f32_16x16x32_bf16 v[120:123], v[180:183], a[220:223], v[120:123]\n" \
"v_mfma_f32_16x16x32_bf16 v[28:31], v[184:187], a[192:195], v[28:31]\n" \
"buffer_load_dwordx4 v213, %[srdk], s48 offen offset:2048 lds\n" \
"v_readlane_b32 s61, %[tlo], 21\n" \
"v_mfma_f32_16x16x32_bf16 v[28:31], v[188:191], a[208:211], v[28:31]\n" \
"v_mfma_f32_16x16x32_bf16 v[60:63], v[184:187], a[196:199], v[60:63]\n" \
"v_mfma_f32_16x16x32_bf16 v[60:63], v[188:191], a[212:215], v[60:63]\n" \
"v_lshlrev_b32_e64 v224, 7, v231\n" \
"v_mfma_f32_16x16x32_bf16 v[92:95], v[184:187], a[200:203], v[92:95]\n" \
"buffer_load_dwordx4 v214, %[srdk], s48 offen offset:3072 lds\n" \
"v_mfma_f32_16x16x32_bf16 v[92:95], v[188:191], a[216:219], v[92:95]\n" \
"v_mfma_f32_16x16x32_bf16 v[124:127], v[184:187], a[204:207], v[124:127]\n" \
"v_mfma_f32_16x16x32_bf16 v[124:127], v[188:191], a[220:223], v[124:127]\n" \
"s_waitcnt vmcnt(24) lgkmcnt(0)\n" \
"v_mfma_f32_16x16x32_bf16 v[0:3], v[128:131], a[224:227], v[0:3]\n" \
"ds_read_b64_tr_b16 v[160:161], v225 offset:50176\n" \
"v_accvgpr_write_b32 a192, v230\n" \
"v_mfma_f32_16x16x32_bf16 v[0:3], v[132:135], a[240:243], v[0:3]\n" \
"ds_read_b64_tr_b16 v[162:163], v225 offset:50432\n" \
"v_mfma_f32_16x16x32_bf16 v[32:35], v[128:131], a[228:231], v[32:35]\n" \
"s_mov_b32 s48, 49152\n" \
"s_mov_b32 m0, s46\n" \
"s_nop 0\n" \
"buffer_load_dwordx4 %[vd0], %[srdv], s48 offen offset:2048 lds\n" \
"v_accvgpr_write_b32 a193, v230\n" \
"v_mfma_f32_16x16x32_bf16 v[32:35], v[132:135], a[244:247], v[32:35]\n" \
"ds_read_b64_tr_b16 v[164:165], v225 offset:58368\n" \
"v_mfma_f32_16x16x32_bf16 v[64:67], v[128:131], a[232:235], v[64:67]\n" \
"ds_read_b64_tr_b16 v[166:167], v225 offset:58624\n" \
"v_accvgpr_write_b32 a194, v230\n" \
"v_mfma_f32_16x16x32_bf16 v[64:67], v[132:135], a[248:251], v[64:67]\n" \
"v_mfma_f32_16x16x32_bf16 v[96:99], v[128:131], a[236:239], v[96:99]\n" \
"ds_read_b64_tr_b16 v[168:169], v225 offset:50184\n" \
"v_accvgpr_write_b32 a195, v230\n" \
"v_mfma_f32_16x16x32_bf16 v[96:99], v[132:135], a[252:255], v[96:99]\n" \
"ds_read_b64_tr_b16 v[170:171], v225 offset:50440\n" \
"v_mfma_f32_16x16x32_bf16 v[4:7], v[136:139], a[224:227], v[4:7]\n" \
"buffer_load_dwordx4 v215, %[srdv], s48 offen offset:3072 lds\n" \
"v_accvgpr_write_b32 a196, -1\n" \
"v_mfma_f32_16x16x32_bf16 v[4:7], v[140:143], a[240:243], v[4:7]\n" \
"ds_read_b64_tr_b16 v[172:173], v225 offset:58376\n" \
"v_mfma_f32_16x16x32_bf16 v[36:39], v[136:139], a[228:231], v[36:39]\n" \
"ds_read_b64_tr_b16 v[174:175], v225 offset:58632\n" \
"v_accvgpr_write_b32 a197, -1\n" \
"v_mfma_f32_16x16x32_bf16 v[36:39], v[140:143], a[244:247], v[36:39]\n" \
"v_mfma_f32_16x16x32_bf16 v[68:71], v[136:139], a[232:235], v[68:71]\n" \
"ds_read_b64_tr_b16 v[176:177], v225 offset:50688\n" \
"v_accvgpr_write_b32 a198, -1\n" \
"v_mfma_f32_16x16x32_bf16 v[68:71], v[140:143], a[248:251], v[68:71]\n" \
"ds_read_b64_tr_b16 v[178:179], v225 offset:50944\n" \
"v_mfma_f32_16x16x32_bf16 v[100:103], v[136:139], a[236:239], v[100:103]\n" \
"v_accvgpr_write_b32 a199, -1\n" \
"v_mfma_f32_16x16x32_bf16 v[100:103], v[140:143], a[252:255], v[100:103]\n" \
"ds_read_b64_tr_b16 v[180:181], v225 offset:58880\n" \
"v_mfma_f32_16x16x32_bf16 v[8:11], v[144:147], a[224:227], v[8:11]\n" \
"ds_read_b64_tr_b16 v[182:183], v225 offset:59136\n" \
"v_lshrrev_b32_e32 v231, 3, v224\n" \
"v_mfma_f32_16x16x32_bf16 v[8:11], v[148:151], a[240:243], v[8:11]\n" \
"v_mfma_f32_16x16x32_bf16 v[40:43], v[144:147], a[228:231], v[40:43]\n" \
"ds_read_b64_tr_b16 v[184:185], v225 offset:50696\n" \
"v_mov_b32_e64 v232, -1\n" \
"v_mfma_f32_16x16x32_bf16 v[40:43], v[148:151], a[244:247], v[40:43]\n" \
"ds_read_b64_tr_b16 v[186:187], v225 offset:50952\n" \
"v_mfma_f32_16x16x32_bf16 v[72:75], v[144:147], a[232:235], v[72:75]\n" \
"v_mov_b32_e64 v233, -1\n" \
"v_mfma_f32_16x16x32_bf16 v[72:75], v[148:151], a[248:251], v[72:75]\n" \
"ds_read_b64_tr_b16 v[188:189], v225 offset:58888\n" \
"v_mfma_f32_16x16x32_bf16 v[104:107], v[144:147], a[236:239], v[104:107]\n" \
"ds_read_b64_tr_b16 v[190:191], v225 offset:59144\n" \
"v_mov_b32_e64 v234, -1\n" \
"v_mfma_f32_16x16x32_bf16 v[104:107], v[148:151], a[252:255], v[104:107]\n" \
"v_mfma_f32_16x16x32_bf16 v[12:15], v[152:155], a[224:227], v[12:15]\n" \
"v_mov_b32_e64 v235, -1\n" \
"v_mfma_f32_16x16x32_bf16 v[12:15], v[156:159], a[240:243], v[12:15]\n" \
"v_mfma_f32_16x16x32_bf16 v[44:47], v[152:155], a[228:231], v[44:47]\n" \
"buffer_store_dwordx4 v[232:235], v231, %[srdz], s61 offen offset:0 sc1\n" \
"v_mfma_f32_16x16x32_bf16 v[44:47], v[156:159], a[244:247], v[44:47]\n" \
"v_mfma_f32_16x16x32_bf16 v[76:79], v[152:155], a[232:235], v[76:79]\n" \
"buffer_store_dwordx4 v[232:235], v231, %[srdz], s61 offen offset:1024 sc1\n" \
"v_mfma_f32_16x16x32_bf16 v[76:79], v[156:159], a[248:251], v[76:79]\n" \
"v_mfma_f32_16x16x32_bf16 v[108:111], v[152:155], a[236:239], v[108:111]\n" \
"v_mfma_f32_16x16x32_bf16 v[108:111], v[156:159], a[252:255], v[108:111]\n" \
"s_waitcnt vmcnt(22) lgkmcnt(0)\n" \
"s_barrier\n" \
"v_mfma_f32_16x16x32_bf16 v[16:19], v[160:163], a[224:227], v[16:19]\n" \
"ds_read_b128 a[64:67], %[kb0] offset:0\n" \
"v_mfma_f32_16x16x32_bf16 v[16:19], v[164:167], a[240:243], v[16:19]\n" \
"v_mfma_f32_16x16x32_bf16 v[20:23], v[168:171], a[224:227], v[20:23]\n" \
"v_cvt_pk_bf16_f32 v230, v0, v1\n" \
"v_cvt_pk_bf16_f32 v231, v2, v3\n" \
"v_cvt_pk_bf16_f32 v232, v4, v5\n" \
"ds_read_b128 a[68:71], v208 offset:0\n" \
"v_mfma_f32_16x16x32_bf16 v[20:23], v[172:175], a[240:243], v[20:23]\n" \
"v_cvt_pk_bf16_f32 v233, v6, v7\n" \
"v_accvgpr_write_b32 a0, v230\n" \
"v_accvgpr_write_b32 a1, v231\n" \
"v_mfma_f32_16x16x32_bf16 v[24:27], v[176:179], a[224:227], v[24:27]\n" \
"v_accvgpr_write_b32 a2, v232\n" \
"v_accvgpr_write_b32 a3, v233\n" \
"v_cvt_pk_bf16_f32 v234, v8, v9\n" \
"ds_read_b128 a[72:75], v209 offset:0\n" \
"v_mfma_f32_16x16x32_bf16 v[24:27], v[180:183], a[240:243], v[24:27]\n" \
"v_cvt_pk_bf16_f32 v235, v10, v11\n" \
"v_cvt_pk_bf16_f32 v236, v12, v13\n" \
"v_cvt_pk_bf16_f32 v237, v14, v15\n" \
"v_mfma_f32_16x16x32_bf16 v[28:31], v[184:187], a[224:227], v[28:31]\n" \
"v_accvgpr_write_b32 a4, v234\n" \
"v_accvgpr_write_b32 a5, v235\n" \
"v_accvgpr_write_b32 a6, v236\n" \
".if . - adaln_body != 8192\n" \
".error \"size model\"\n" \
".endif\n" \
"Lk1_%=:\n" \
"s_cbranch_execz Lk2_%=\n" \
"s_nop 0\n" \
"ds_read_b128 a[76:79], v210 offset:0\n" \
"v_mfma_f32_16x16x32_bf16 v[28:31], v[188:191], a[240:243], v[28:31]\n" \
"v_accvgpr_write_b32 a7, v237\n" \
"v_cvt_pk_bf16_f32 v230, v32, v33\n" \
"v_cvt_pk_bf16_f32 v231, v34, v35\n" \
"v_mfma_f32_16x16x32_bf16 v[48:51], v[160:163], a[228:231], v[48:51]\n" \
"v_cvt_pk_bf16_f32 v232, v36, v37\n" \
"v_cvt_pk_bf16_f32 v233, v38, v39\n" \
"v_accvgpr_write_b32 a16, v230\n" \
"ds_read_b128 a[92:95], v210 offset:4096\n" \
"v_mfma_f32_16x16x32_bf16 v[48:51], v[164:167], a[244:247], v[48:51]\n" \
"v_accvgpr_write_b32 a17, v231\n" \
"v_accvgpr_write_b32 a18, v232\n" \
"v_accvgpr_write_b32 a19, v233\n" \
"v_mfma_f32_16x16x32_bf16 v[52:55], v[168:171], a[228:231], v[52:55]\n" \
"v_cvt_pk_bf16_f32 v234, v40, v41\n" \
"v_cvt_pk_bf16_f32 v235, v42, v43\n" \
"v_cvt_pk_bf16_f32 v236, v44, v45\n" \
"ds_read_b128 a[88:91], v209 offset:4096\n" \
"v_mfma_f32_16x16x32_bf16 v[52:55], v[172:175], a[244:247], v[52:55]\n" \
"v_cvt_pk_bf16_f32 v237, v46, v47\n" \
"v_accvgpr_write_b32 a20, v234\n" \
"v_accvgpr_write_b32 a21, v235\n" \
"v_mfma_f32_16x16x32_bf16 v[56:59], v[176:179], a[228:231], v[56:59]\n" \
"v_accvgpr_write_b32 a22, v236\n" \
"v_accvgpr_write_b32 a23, v237\n" \
"v_cvt_pk_bf16_f32 v230, v16, v17\n" \
"ds_read_b128 a[84:87], v208 offset:4096\n" \
"v_mfma_f32_16x16x32_bf16 v[56:59], v[180:183], a[244:247], v[56:59]\n" \
"v_cvt_pk_bf16_f32 v231, v18, v19\n" \
"v_cvt_pk_bf16_f32 v232, v20, v21\n" \
"v_cvt_pk_bf16_f32 v233, v22, v23\n" \
"v_mfma_f32_16x16x32_bf16 v[60:63], v[184:187], a[228:231], v[60:63]\n" \
"v_accvgpr_write_b32 a8, v230\n" \
"v_accvgpr_write_b32 a9, v231\n" \
"v_accvgpr_write_b32 a10, v232\n" \
"ds_read_b128 a[80:83], %[kb0] offset:4096\n" \
"v_mfma_f32_16x16x32_bf16 v[60:63], v[188:191], a[244:247], v[60:63]\n" \
"v_accvgpr_write_b32 a11, v233\n" \
"v_cvt_pk_bf16_f32 v234, v24, v25\n" \
"v_cvt_pk_bf16_f32 v235, v26, v27\n" \
"v_mfma_f32_16x16x32_bf16 v[80:83], v[160:163], a[232:235], v[80:83]\n" \
"v_cvt_pk_bf16_f32 v236, v28, v29\n" \
"v_cvt_pk_bf16_f32 v237, v30, v31\n" \
"v_accvgpr_write_b32 a12, v234\n" \
"v_mfma_f32_16x16x32_bf16 v[80:83], v[164:167], a[248:251], v[80:83]\n" \
"v_accvgpr_write_b32 a13, v235\n" \
"v_accvgpr_write_b32 a14, v236\n" \
"v_accvgpr_write_b32 a15, v237\n" \
"v_mfma_f32_16x16x32_bf16 v[84:87], v[168:171], a[232:235], v[84:87]\n" \
"v_cvt_pk_bf16_f32 v230, v64, v65\n" \
"v_cvt_pk_bf16_f32 v231, v66, v67\n" \
"v_cvt_pk_bf16_f32 v232, v68, v69\n" \
"v_mfma_f32_16x16x32_bf16 v[84:87], v[172:175], a[248:251], v[84:87]\n" \
"v_cvt_pk_bf16_f32 v233, v70, v71\n" \
"v_accvgpr_write_b32 a32, v230\n" \
"v_accvgpr_write_b32 a33, v231\n" \
"v_mfma_f32_16x16x32_bf16 v[88:91], v[176:179], a[232:235], v[88:91]\n" \
"v_accvgpr_write_b32 a34, v232\n" \
"v_accvgpr_write_b32 a35, v233\n" \
"v_cvt_pk_bf16_f32 v234, v72, v73\n" \
"v_mfma_f32_16x16x32_bf16 v[88:91], v[180:183], a[248:251], v[88:91]\n" \
"v_cvt_pk_bf16_f32 v235, v74, v75\n" \
"v_cvt_pk_bf16_f32 v236, v76, v77\n" \
"v_cvt_pk_bf16_f32 v237, v78, v79\n" \
"v_mfma_f32_16x16x32_bf16 v[92:95], v[184:187], a[232:235], v[92:95]\n" \
"v_accvgpr_write_b32 a36, v234\n" \
"v_accvgpr_write_b32 a37, v235\n" \
"v_accvgpr_write_b32 a38, v236\n" \
"v_mfma_f32_16x16x32_bf16 v[92:95], v[188:191], a[248:251], v[92:95]\n" \
"v_accvgpr_write_b32 a39, v237\n" \
"v_cvt_pk_bf16_f32 v230, v48, v49\n" \
"v_cvt_pk_bf16_f32 v231, v50, v51\n" \
"v_mfma_f32_16x16x32_bf16 v[112:115], v[160:163], a[236:239], v[112:115]\n" \
"v_cvt_pk_bf16_f32 v232, v52, v53\n" \
"v_cvt_pk_bf16_f32 v233, v54, v55\n" \
"v_accvgpr_write_b32 a24, v230\n" \
"v_mfma_f32_16x16x32_bf16 v[112:115], v[164:167], a[252:255], v[112:115]\n" \
"v_accvgpr_write_b32 a25, v231\n" \
"v_accvgpr_write_b32 a26, v232\n" \
"v_accvgpr_write_b32 a27, v233\n" \
"v_mfma_f32_16x16x32_bf16 v[116:119], v[168:171], a[236:239], v[116:119]\n" \
"v_cvt_pk_bf16_f32 v234, v56, v57\n" \
"v_cvt_pk_bf16_f32 v235, v58, v59\n" \
"v_cvt_pk_bf16_f32 v236, v60, v61\n" \
"v_mfma_f32_16x16x32_bf16 v[116:119], v[172:175], a[252:255], v[116:119]\n" \
"v_cvt_pk_bf16_f32 v237, v62, v63\n" \
"v_accvgpr_write_b32 a28, v234\n" \
"v_accvgpr_write_b32 a29, v235\n" \
"v_mfma_f32_16x16x32_bf16 v[120:123], v[176:179], a[236:239], v[120:123]\n" \
"v_accvgpr_write_b32 a30, v236\n" \
"v_accvgpr_write_b32 a31, v237\n" \
"v_cvt_pk_bf16_f32 v230, v96, v97\n" \
"v_mfma_f32_16x16x32_bf16 v[120:123], v[180:183], a[252:255], v[120:123]\n" \
"v_cvt_pk_bf16_f32 v231, v98, v99\n" \
"v_cvt_pk_bf16_f32 v232, v100, v101\n" \
"v_cvt_pk_bf16_f32 v233, v102, v103\n" \
"v_mfma_f32_16x16x32_bf16 v[124:127], v[184:187], a[236:239], v[124:127]\n" \
"v_accvgpr_write_b32 a48, v230\n" \
"v_accvgpr_write_b32 a49, v231\n" \
"v_accvgpr_write_b32 a50, v232\n" \
"v_mfma_f32_16x16x32_bf16 v[124:127], v[188:191], a[252:255], v[124:127]\n" \
"v_accvgpr_write_b32 a51, v233\n" \
"s_waitcnt vmcnt(63) lgkmcnt(0)\n" \
"s_nop 0\n" \
"s_mov_b32 s48, 65536\n" \
"s_mov_b32 s60, 0\n" \
"s_waitcnt lgkmcnt(0)\n" \
"v_mfma_f32_16x16x32_bf16 v[128:131], a[64:67], a[0:3], 0\n" \
"ds_read_b128 a[96:99], %[kb0] offset:8192\n" \
"v_cvt_pk_bf16_f32 v234, v80, v81\n" \
"v_cvt_pk_bf16_f32 v235, v82, v83\n" \
"v_mfma_f32_16x16x32_bf16 v[128:131], a[68:71], a[4:7], v[128:131]\n" \
"ds_read_b128 a[100:103], v208 offset:8192\n" \
"v_cvt_pk_bf16_f32 v236, v84, v85\n" \
"v_cvt_pk_bf16_f32 v237, v86, v87\n" \
"v_mfma_f32_16x16x32_bf16 v[128:131], a[72:75], a[8:11], v[128:131]\n" \
"ds_read_b128 a[104:107], v209 offset:8192\n" \
"v_accvgpr_write_b32 a40, v234\n" \
"v_accvgpr_write_b32 a41, v235\n" \
"v_mfma_f32_16x16x32_bf16 v[128:131], a[76:79], a[12:15], v[128:131]\n" \
"ds_read_b128 a[108:111], v210 offset:8192\n" \
"v_accvgpr_write_b32 a42, v236\n" \
"v_accvgpr_write_b32 a43, v237\n" \
"v_mfma_f32_16x16x32_bf16 v[132:135], a[92:95], a[12:15], 0\n" \
"ds_read_b128 a[124:127], v210 offset:12288\n" \
"v_cvt_pk_bf16_f32 v230, v88, v89\n" \
"v_cvt_pk_bf16_f32 v231, v90, v91\n" \
"v_mfma_f32_16x16x32_bf16 v[132:135], a[88:91], a[8:11], v[132:135]\n" \
"ds_read_b128 a[120:123], v209 offset:12288\n" \
"v_cvt_pk_bf16_f32 v232, v92, v93\n" \
"v_cvt_pk_bf16_f32 v233, v94, v95\n" \
"v_mfma_f32_16x16x32_bf16 v[132:135], a[84:87], a[4:7], v[132:135]\n" \
"ds_read_b128 a[116:119], v208 offset:12288\n" \
"v_accvgpr_write_b32 a44, v230\n" \
"v_accvgpr_write_b32 a45, v231\n" \
"v_mfma_f32_16x16x32_bf16 v[132:135], a[80:83], a[0:3], v[132:135]\n" \
"ds_read_b128 a[112:115], %[kb0] offset:12288\n" \
"v_accvgpr_write_b32 a46, v232\n" \
"v_accvgpr_write_b32 a47, v233\n" \
"v_mfma_f32_16x16x32_bf16 v[136:139], a[64:67], a[16:19], 0\n" \
"ds_read_b64_tr_b16 a[128:129], %[vvb] offset:0\n" \
"v_cvt_pk_bf16_f32 v234, v104, v105\n" \
"v_cvt_pk_bf16_f32 v235, v106, v107\n" \
"v_mfma_f32_16x16x32_bf16 v[136:139], a[68:71], a[20:23], v[136:139]\n" \
"ds_read_b64_tr_b16 a[130:131], %[vvb] offset:4096\n" \
"v_cvt_pk_bf16_f32 v236, v108, v109\n" \
"v_cvt_pk_bf16_f32 v237, v110, v111\n" \
"v_mfma_f32_16x16x32_bf16 v[136:139], a[72:75], a[24:27], v[136:139]\n" \
"ds_read_b64_tr_b16 a[132:133], v211 offset:0\n" \
"v_accvgpr_write_b32 a52, v234\n" \
"v_accvgpr_write_b32 a53, v235\n" \
"v_mfma_f32_16x16x32_bf16 v[136:139], a[76:79], a[28:31], v[136:139]\n" \
"ds_read_b64_tr_b16 a[134:135], v211 offset:4096\n" \
"v_accvgpr_write_b32 a54, v236\n" \
"v_accvgpr_write_b32 a55, v237\n" \
"v_mfma_f32_16x16x32_bf16 v[140:143], a[92:95], a[28:31], 0\n" \
"ds_read_b64_tr_b16 a[136:137], %[vvb] offset:512\n" \
"v_cvt_pk_bf16_f32 v230, v112, v113\n" \
"v_cvt_pk_bf16_f32 v231, v114, v115\n" \
"v_mfma_f32_16x16x32_bf16 v[140:143], a[88:91], a[24:27], v[140:143]\n" \
"ds_read_b64_tr_b16 a[138:139], %[vvb] offset:4608\n" \
"v_cvt_pk_bf16_f32 v232, v116, v117\n" \
"v_cvt_pk_bf16_f32 v233, v118, v119\n" \
"v_mfma_f32_16x16x32_bf16 v[140:143], a[84:87], a[20:23], v[140:143]\n" \
"ds_read_b64_tr_b16 a[140:141], v211 offset:512\n" \
"v_accvgpr_write_b32 a56, v230\n" \
"v_accvgpr_write_b32 a57, v231\n" \
"v_mfma_f32_16x16x32_bf16 v[140:143], a[80:83], a[16:19], v[140:143]\n" \
"ds_read_b64_tr_b16 a[142:143], v211 offset:4608\n" \
"v_accvgpr_write_b32 a58, v232\n" \
"v_accvgpr_write_b32 a59, v233\n" \
"v_mfma_f32_16x16x32_bf16 v[144:147], a[64:67], a[32:35], 0\n" \
"ds_read_b64_tr_b16 a[144:145], %[vvb] offset:1024\n" \
"v_cvt_pk_bf16_f32 v234, v120, v121\n" \
"v_cvt_pk_bf16_f32 v235, v122, v123\n" \
"v_mfma_f32_16x16x32_bf16 v[144:147], a[68:71], a[36:39], v[144:147]\n" \
"ds_read_b64_tr_b16 a[146:147], %[vvb] offset:5120\n" \
"v_cvt_pk_bf16_f32 v236, v124, v125\n" \
"v_cvt_pk_bf16_f32 v237, v126, v127\n" \
"v_mfma_f32_16x16x32_bf16 v[144:147], a[72:75], a[40:43], v[144:147]\n" \
"ds_read_b64_tr_b16 a[148:149], v211 offset:1024\n" \
"v_accvgpr_write_b32 a60, v234\n" \
"v_accvgpr_write_b32 a61, v235\n" \
"v_mfma_f32_16x16x32_bf16 v[144:147], a[76:79], a[44:47], v[144:147]\n" \
"ds_read_b64_tr_b16 a[150:151], v211 offset:5120\n" \
"v_accvgpr_write_b32 a62, v236\n" \
"v_accvgpr_write_b32 a63, v237\n" \
"v_mfma_f32_16x16x32_bf16 v[148:151], a[92:95], a[44:47], 0\n" \
"ds_read_b64_tr_b16 a[152:153], %[vvb] offset:1536\n" \
"v_max3_f32 v192, v128, v129, v130\n" \
"v_max3_f32 v193, v131, v132, v133\n" \
"v_mfma_f32_16x16x32_bf16 v[148:151], a[88:91], a[40:43], v[148:151]\n" \
"ds_read_b64_tr_b16 a[154:155], %[vvb] offset:5632\n" \
"v_max3_f32 v192, v192, v134, v135\n" \
"v_max_f32_e64 v225, v192, v193\n" \
"v_mfma_f32_16x16x32_bf16 v[148:151], a[84:87], a[36:39], v[148:151]\n" \
"ds_read_b64_tr_b16 a[156:157], v211 offset:1536\n" \
"v_max3_f32 v192, v136, v137, v138\n" \
"v_max3_f32 v193, v139, v140, v141\n" \
"v_mfma_f32_16x16x32_bf16 v[148:151], a[80:83], a[32:35], v[148:151]\n" \
"ds_read_b64_tr_b16 a[158:159], v211 offset:5632\n" \
"v_max3_f32 v192, v192, v142, v143\n" \
"v_max_f32_e64 v226, v192, v193\n" \
"v_mfma_f32_16x16x32_bf16 v[152:155], a[64:67], a[48:51], 0\n" \
"ds_read_b128 a[200:203], v228 offset:0\n" \
"s_mov_b32 s44, %[m0t]\n" \
"s_mov_b32 s45, 0\n" \
"v_mfma_f32_16x16x32_bf16 v[152:155], a[68:71], a[52:55], v[152:155]\n" \
"ds_read_b128 a[204:207], v228 offset:64\n" \
"v_mfma_f32_16x16x32_bf16 v[152:155], a[72:75], a[56:59], v[152:155]\n" \
"ds_read_b128 a[208:211], v228 offset:128\n" \
"v_mfma_f32_16x16x32_bf16 v[152:155], a[76:79], a[60:63], v[152:155]\n" \
"ds_read_b128 a[212:215], v228 offset:192\n" \
"v_mfma_f32_16x16x32_bf16 v[156:159], a[92:95], a[60:63], 0\n" \
"ds_read_b128 a[216:219], v228 offset:256\n" \
"v_mfma_f32_16x16x32_bf16 v[156:159], a[88:91], a[56:59], v[156:159]\n" \
"ds_read_b128 a[220:223], v228 offset:320\n" \
"v_mfma_f32_16x16x32_bf16 v[156:159], a[84:87], a[52:55], v[156:159]\n" \
"ds_read_b128 a[224:227], v228 offset:384\n" \
"v_max3_f32 v192, v144, v145, v146\n" \
"v_max3_f32 v193, v147, v148, v149\n" \
"v_mfma_f32_16x16x32_bf16 v[156:159], a[80:83], a[48:51], v[156:159]\n" \
"ds_read_b128 a[228:231], v228 offset:448\n" \
"v_max3_f32 v192, v192, v150, v151\n" \
"v_max_f32_e64 v227, v192, v193\n" \
"s_nop 3\n" \
"s_nop 0\n" \
"v_max3_f32 v230, v152, v153, v154\n" \
"v_max3_f32 v231, v155, v156, v157\n" \
"v_max3_f32 v230, v230, v158, v159\n" \
"v_max_f32_e32 v228, v230, v231\n" \
"v_mov_b32_e32 v232, v225\n" \
"v_mov_b32_e32 v233, v226\n" \
"v_mov_b32_e32 v234, v227\n" \
"v_mov_b32_e32 v235, v228\n" \
"s_nop 0\n" \
"v_permlane16_swap_b32_e32 v225, v232\n" \
"v_permlane16_swap_b32_e32 v226, v233\n" \
"v_permlane16_swap_b32_e32 v227, v234\n" \
"v_permlane16_swap_b32_e32 v228, v235\n" \
"v_max_f32_e32 v225, v225, v232\n" \
"v_max_f32_e32 v226, v226, v233\n" \
"v_max_f32_e32 v227, v227, v234\n" \
"v_max_f32_e32 v228, v228, v235\n" \
"v_mov_b32_e32 v232, v225\n" \
"v_mov_b32_e32 v233, v226\n" \
"v_mov_b32_e32 v234, v227\n" \
"v_mov_b32_e32 v235, v228\n" \
"s_nop 0\n" \
"v_permlane32_swap_b32_e32 v225, v232\n" \
"v_permlane32_swap_b32_e32 v226, v233\n" \
"v_permlane32_swap_b32_e32 v227, v234\n" \
"v_permlane32_swap_b32_e32 v228, v235\n" \
"v_max_f32_e32 v225, v225, v232\n" \
"v_max_f32_e32 v226, v226, v233\n" \
"v_max_f32_e32 v227, v227, v234\n" \
"v_max_f32_e64 v228, v228, v235\n" \
"v_add_f32_e32 v225, 0x41800000, v225\n" \
"v_add_f32_e32 v226, 0x41800000, v226\n" \
"v_add_f32_e32 v227, 0x41800000, v227\n" \
"v_add_f32_e32 v228, 0x41800000, v228\n" \
"Lhavem_%=:\n" \
"s_nop 3\n" \
"v_mov_b64_e32 v[176:177], 0\n" \
"v_mov_b64_e32 v[178:179], 0\n" \
"v_mov_b64_e32 v[180:181], 0\n" \
"v_mov_b64_e32 v[182:183], 0\n" \
"s_and_b32 s68, s52, s69\n" \
"s_cselect_b32 s68, 0, 0x39400000\n" \
"s_waitcnt lgkmcnt(14)\n" \
"v_mfma_f32_16x16x32_bf16 v[230:233], a[128:131], a[128:131], 0\n" \
"v_sub_f32_e32 v160, 0, v225\n" \
"v_sub_f32_e32 v161, 0, v225\n" \
"v_sub_f32_e32 v162, 0, v225\n" \
"v_sub_f32_e32 v163, 0, v225\n" \
"v_sub_f32_e32 v164, 0, v226\n" \
"v_sub_f32_e32 v165, 0, v226\n" \
"v_sub_f32_e32 v166, 0, v226\n" \
"v_sub_f32_e32 v167, 0, v226\n" \
"v_sub_f32_e32 v168, 0, v227\n" \
"v_sub_f32_e32 v169, 0, v227\n" \
"v_sub_f32_e32 v170, 0, v227\n" \
"v_sub_f32_e32 v171, 0, v227\n" \
"v_sub_f32_e32 v172, 0, v228\n" \
"v_sub_f32_e32 v173, 0, v228\n" \
"v_sub_f32_e32 v174, 0, v228\n" \
"v_sub_f32_e32 v175, 0, v228\n" \
"v_sub_f32_e32 v128, v128, v225\n" \
"v_sub_f32_e32 v129, v129, v225\n" \
"v_sub_f32_e32 v130, v130, v225\n" \
"v_sub_f32_e32 v131, v131, v225\n" \
"v_sub_f32_e32 v132, v132, v225\n" \
"v_sub_f32_e32 v133, v133, v225\n" \
"v_sub_f32_e32 v134, v134, v225\n" \
"v_sub_f32_e32 v135, v135, v225\n" \
"v_sub_f32_e32 v136, v136, v226\n" \
"v_sub_f32_e32 v137, v137, v226\n" \
"v_sub_f32_e32 v138, v138, v226\n" \
"v_sub_f32_e32 v139, v139, v226\n" \
"v_sub_f32_e32 v140, v140, v226\n" \
"v_sub_f32_e32 v141, v141, v226\n" \
"v_sub_f32_e32 v142, v142, v226\n" \
"v_sub_f32_e32 v143, v143, v226\n" \
"v_sub_f32_e32 v144, v144, v227\n" \
"v_sub_f32_e32 v145, v145, v227\n" \
"v_sub_f32_e32 v146, v146, v227\n" \
"v_sub_f32_e32 v147, v147, v227\n" \
"v_sub_f32_e32 v148, v148, v227\n" \
"v_sub_f32_e32 v149, v149, v227\n" \
"v_sub_f32_e32 v150, v150, v227\n" \
"v_sub_f32_e32 v151, v151, v227\n" \
"v_sub_f32_e32 v152, v152, v228\n" \
"v_sub_f32_e32 v153, v153, v228\n" \
"v_sub_f32_e32 v154, v154, v228\n" \
"v_sub_f32_e32 v155, v155, v228\n" \
"v_sub_f32_e32 v156, v156, v228\n" \
"v_sub_f32_e32 v157, v157, v228\n" \
"v_sub_f32_e32 v158, v158, v228\n" \
"v_sub_f32_e32 v159, v159, v228\n" \
"v_exp_f32_e32 v128, v128\n" \
"v_exp_f32_e32 v129, v129\n" \
"v_exp_f32_e32 v130, v130\n" \
"v_exp_f32_e32 v131, v131\n" \
"v_exp_f32_e32 v132, v132\n" \
"v_exp_f32_e32 v133, v133\n" \
"v_exp_f32_e32 v134, v134\n" \
"v_exp_f32_e32 v135, v135\n" \
"v_exp_f32_e32 v136, v136\n" \
"v_exp_f32_e32 v137, v137\n" \
"v_exp_f32_e32 v138, v138\n" \
"v_exp_f32_e32 v139, v139\n" \
"v_exp_f32_e32 v140, v140\n" \
"v_exp_f32_e32 v141, v141\n" \
"v_exp_f32_e32 v142, v142\n" \
"v_exp_f32_e32 v143, v143\n" \
"v_exp_f32_e32 v144, v144\n" \
"v_exp_f32_e32 v145, v145\n" \
"v_exp_f32_e32 v146, v146\n" \
"v_exp_f32_e32 v147, v147\n" \
"v_exp_f32_e32 v148, v148\n" \
"v_exp_f32_e32 v149, v149\n" \
"v_exp_f32_e32 v150, v150\n" \
"v_exp_f32_e32 v151, v151\n" \
"v_exp_f32_e32 v152, v152\n" \
"v_exp_f32_e32 v153, v153\n" \
"v_exp_f32_e32 v154, v154\n" \
"v_exp_f32_e64 v155, v155\n" \
"v_bfe_i32 v228, v224, 7, 1\n" \
"v_and_b32_e32 v228, 0xaaaa, v228\n" \
"v_or_b32_e32 v228, 0x4444, v228\n" \
"v_cvt_pk_bf16_f32 v184, v128, v129\n" \
"v_cvt_pk_bf16_f32 v185, v130, v131\n" \
"v_cvt_pk_bf16_f32 v186, v132, v133\n" \
"v_cvt_pk_bf16_f32 v187, v134, v135\n" \
"v_cvt_pk_bf16_f32 v188, v136, v137\n" \
"v_cvt_pk_bf16_f32 v189, v138, v139\n" \
"v_cvt_pk_bf16_f32 v190, v140, v141\n" \
"v_cvt_pk_bf16_f32 v191, v142, v143\n" \
"v_cvt_pk_bf16_f32 v216, v144, v145\n" \
"v_cvt_pk_bf16_f32 v217, v146, v147\n" \
"v_cvt_pk_bf16_f32 v218, v148, v149\n" \
"v_cvt_pk_bf16_f32 v219, v150, v151\n" \
"v_max3_f32 v230, |v230|, |v231|, |v232|\n" \
"v_max_f32_e64 v230, v230, |v233|\n" \
"v_sqrt_f32_e32 v230, v230\n" \
"s_nop 0\n" \
"v_mul_legacy_f32 v225, s68, v230\n" \
"v_mov_b32_e32 v230, v225\n" \
"v_mov_b32_e32 v231, v225\n" \
"v_mov_b32_e32 v232, v225\n" \
"v_mov_b32_e32 v233, v225\n" \
"v_lshrrev_b32_e64 v235, 3, v224\n" \
"s_waitcnt vmcnt(2) lgkmcnt(0)\n" \
"s_barrier\n" \
"v_mfma_f32_16x16x32_bf16 v[128:131], a[96:99], a[0:3], v[160:163]\n" \
"v_exp_f32_e64 v156, v156\n" \
"v_mfma_f32_16x16x32_bf16 v[128:131], a[100:103], a[4:7], v[128:131]\n" \
"s_add_u32 m0, %[m0b], 114688\n" \
"v_mfma_f32_16x16x32_bf16 v[128:131], a[104:107], a[8:11], v[128:131]\n" \
"v_exp_f32_e64 v157, v157\n" \
"v_mfma_f32_16x16x32_bf16 v[128:131], a[108:111], a[12:15], v[128:131]\n" \
"v_cvt_pk_bf16_f32 v220, v152, v153\n" \
"v_cvt_pk_bf16_f32 v221, v154, v155\n" \
"v_mfma_f32_16x16x32_bf16 v[132:135], a[124:127], a[12:15], v[160:163]\n" \
"v_exp_f32_e64 v158, v158\n" \
"v_mfma_f32_16x16x32_bf16 v[132:135], a[120:123], a[8:11], v[132:135]\n" \
"v_mfma_f32_16x16x32_bf16 v[132:135], a[116:119], a[4:7], v[132:135]\n" \
"buffer_load_dwordx4 %[vd0], %[srdv], s48 offen offset:0 lds\n" \
"v_mfma_f32_16x16x32_bf16 v[132:135], a[112:115], a[0:3], v[132:135]\n" \
"v_exp_f32_e64 v159, v159\n" \
"v_mfma_f32_16x16x32_bf16 v[0:3], a[128:131], v[184:187], v[230:233]\n" \
"ds_read_b128 a[64:67], %[kb0] offset:16384\n" \
"v_mfma_f32_16x16x32_bf16 v[32:35], a[128:131], v[188:191], v[230:233]\n" \
"v_exp_f32_e64 v128, v128\n" \
"v_mfma_f32_16x16x32_bf16 v[4:7], a[132:135], v[184:187], v[230:233]\n" \
"v_cvt_pk_bf16_f32 v222, v156, v157\n" \
"v_cvt_pk_bf16_f32 v223, v158, v159\n" \
"v_mfma_f32_16x16x32_bf16 v[36:39], a[132:135], v[188:191], v[230:233]\n" \
"v_exp_f32_e64 v129, v129\n" \
"v_mfma_f32_16x16x32_bf16 v[8:11], a[136:139], v[184:187], v[230:233]\n" \
"ds_read_b128 a[68:71], v208 offset:16384\n" \
"v_mfma_f32_16x16x32_bf16 v[40:43], a[136:139], v[188:191], v[230:233]\n" \
"v_exp_f32_e64 v130, v130\n" \
"v_mfma_f32_16x16x32_bf16 v[12:15], a[140:143], v[184:187], v[230:233]\n" \
"ds_read_b128 a[72:75], v209 offset:16384\n" \
"v_mfma_f32_16x16x32_bf16 v[44:47], a[140:143], v[188:191], v[230:233]\n" \
"v_exp_f32_e64 v131, v131\n" \
"v_smfmac_f32_16x16x64_bf16 v[176:179], a[192:195], v[184:191], v228\n" \
"v_mfma_f32_16x16x32_bf16 v[136:139], a[96:99], a[16:19], v[164:167]\n" \
"v_exp_f32_e64 v132, v132\n" \
"v_mfma_f32_16x16x32_bf16 v[136:139], a[100:103], a[20:23], v[136:139]\n" \
"ds_read_b128 a[76:79], v210 offset:16384\n" \
"v_mfma_f32_16x16x32_bf16 v[136:139], a[104:107], a[24:27], v[136:139]\n" \
"v_exp_f32_e64 v133, v133\n" \
"v_mfma_f32_16x16x32_bf16 v[136:139], a[108:111], a[28:31], v[136:139]\n" \
"v_cvt_pk_bf16_f32 v192, v128, v129\n" \
"v_cvt_pk_bf16_f32 v193, v130, v131\n" \
"v_mfma_f32_16x16x32_bf16 v[140:143], a[124:127], a[28:31], v[164:167]\n" \
"v_exp_f32_e64 v134, v134\n" \
"v_mfma_f32_16x16x32_bf16 v[140:143], a[120:123], a[24:27], v[140:143]\n" \
"ds_read_b128 a[92:95], v210 offset:20480\n" \
"v_mfma_f32_16x16x32_bf16 v[140:143], a[116:119], a[20:23], v[140:143]\n" \
"buffer_load_dwordx4 v215, %[srdv], s48 offen offset:1024 lds\n" \
"v_mfma_f32_16x16x32_bf16 v[140:143], a[112:115], a[16:19], v[140:143]\n" \
"v_exp_f32_e64 v135, v135\n" \
"v_mfma_f32_16x16x32_bf16 v[16:19], a[144:147], v[184:187], v[230:233]\n" \
"ds_read_b128 a[88:91], v209 offset:20480\n" \
"v_mfma_f32_16x16x32_bf16 v[48:51], a[144:147], v[188:191], v[230:233]\n" \
"v_exp_f32_e64 v136, v136\n" \
"v_mfma_f32_16x16x32_bf16 v[20:23], a[148:151], v[184:187], v[230:233]\n" \
"v_cvt_pk_bf16_f32 v194, v132, v133\n" \
"v_cvt_pk_bf16_f32 v195, v134, v135\n" \
"v_mfma_f32_16x16x32_bf16 v[52:55], a[148:151], v[188:191], v[230:233]\n" \
"v_exp_f32_e64 v137, v137\n" \
"v_mfma_f32_16x16x32_bf16 v[24:27], a[152:155], v[184:187], v[230:233]\n" \
"v_exp_f32_e64 v138, v138\n" \
"v_mfma_f32_16x16x32_bf16 v[56:59], a[152:155], v[188:191], v[230:233]\n" \
"ds_read_b128 a[84:87], v208 offset:20480\n" \
"v_mfma_f32_16x16x32_bf16 v[28:31], a[156:159], v[184:187], v[230:233]\n" \
"v_exp_f32_e64 v139, v139\n" \
"v_mfma_f32_16x16x32_bf16 v[60:63], a[156:159], v[188:191], v[230:233]\n" \
"v_mfma_f32_16x16x32_bf16 v[144:147], a[96:99], a[32:35], v[168:171]\n" \
"v_exp_f32_e64 v140, v140\n" \
"v_mfma_f32_16x16x32_bf16 v[144:147], a[100:103], a[36:39], v[144:147]\n" \
"ds_read_b128 a[80:83], %[kb0] offset:20480\n" \
"v_mfma_f32_16x16x32_bf16 v[144:147], a[104:107], a[40:43], v[144:147]\n" \
"v_exp_f32_e64 v141, v141\n" \
"v_mfma_f32_16x16x32_bf16 v[144:147], a[108:111], a[44:47], v[144:147]\n" \
"v_cvt_pk_bf16_f32 v196, v136, v137\n" \
"v_cvt_pk_bf16_f32 v197, v138, v139\n" \
"v_mfma_f32_16x16x32_bf16 v[148:151], a[124:127], a[44:47], v[168:171]\n" \
"v_exp_f32_e64 v142, v142\n" \
"v_mfma_f32_16x16x32_bf16 v[148:151], a[120:123], a[40:43], v[148:151]\n" \
"v_mfma_f32_16x16x32_bf16 v[148:151], a[116:119], a[36:39], v[148:151]\n" \
"buffer_load_dwordx4 %[vd0], %[srdv], s48 offen offset:2048 lds\n" \
"v_mfma_f32_16x16x32_bf16 v[148:151], a[112:115], a[32:35], v[148:151]\n" \
"v_exp_f32_e64 v143, v143\n" \
"v_mfma_f32_16x16x32_bf16 v[64:67], a[128:131], v[216:219], v[230:233]\n" \
"ds_read_b64_tr_b16 a[160:161], %[vvb] offset:8192\n" \
"v_mfma_f32_16x16x32_bf16 v[96:99], a[128:131], v[220:223], v[230:233]\n" \
"v_exp_f32_e64 v144, v144\n" \
"v_mfma_f32_16x16x32_bf16 v[68:71], a[132:135], v[216:219], v[230:233]\n" \
"v_cvt_pk_bf16_f32 v198, v140, v141\n" \
"v_cvt_pk_bf16_f32 v199, v142, v143\n" \
"v_mfma_f32_16x16x32_bf16 v[100:103], a[132:135], v[220:223], v[230:233]\n" \
"v_exp_f32_e64 v145, v145\n" \
"v_mfma_f32_16x16x32_bf16 v[72:75], a[136:139], v[216:219], v[230:233]\n" \
"ds_read_b64_tr_b16 a[162:163], %[vvb] offset:12288\n" \
"ds_read_b64_tr_b16 a[164:165], v211 offset:8192\n" \
"v_mfma_f32_16x16x32_bf16 v[104:107], a[136:139], v[220:223], v[230:233]\n" \
"v_exp_f32_e64 v146, v146\n" \
"v_mfma_f32_16x16x32_bf16 v[76:79], a[140:143], v[216:219], v[230:233]\n" \
"ds_read_b64_tr_b16 a[166:167], v211 offset:12288\n" \
"v_mfma_f32_16x16x32_bf16 v[108:111], a[140:143], v[220:223], v[230:233]\n" \
"v_exp_f32_e64 v147, v147\n" \
"v_smfmac_f32_16x16x64_bf16 v[180:183], a[192:195], v[216:223], v228\n" \
"ds_read_b64_tr_b16 a[168:169], %[vvb] offset:8704\n" \
"v_mfma_f32_16x16x32_bf16 v[152:155], a[96:99], a[48:51], v[172:175]\n" \
"v_exp_f32_e64 v148, v148\n" \
"v_mfma_f32_16x16x32_bf16 v[152:155], a[100:103], a[52:55], v[152:155]\n" \
"ds_read_b64_tr_b16 a[170:171], %[vvb] offset:12800\n" \
"v_mfma_f32_16x16x32_bf16 v[152:155], a[104:107], a[56:59], v[152:155]\n" \
"v_exp_f32_e64 v149, v149\n" \
"v_mfma_f32_16x16x32_bf16 v[152:155], a[108:111], a[60:63], v[152:155]\n" \
"v_cvt_pk_bf16_f32 v200, v144, v145\n" \
"v_cvt_pk_bf16_f32 v201, v146, v147\n" \
"v_mfma_f32_16x16x32_bf16 v[156:159], a[124:127], a[60:63], v[172:175]\n" \
"v_exp_f32_e64 v150, v150\n" \
"v_mfma_f32_16x16x32_bf16 v[156:159], a[120:123], a[56:59], v[156:159]\n" \
"ds_read_b64_tr_b16 a[172:173], v211 offset:8704\n" \
"v_mfma_f32_16x16x32_bf16 v[156:159], a[116:119], a[52:55], v[156:159]\n" \
"buffer_load_dwordx4 v215, %[srdv], s48 offen offset:3072 lds\n" \
"v_mfma_f32_16x16x32_bf16 v[156:159], a[112:115], a[48:51], v[156:159]\n" \
"v_exp_f32_e64 v151, v151\n" \
"v_mfma_f32_16x16x32_bf16 v[80:83], a[144:147], v[216:219], v[230:233]\n" \
"ds_read_b64_tr_b16 a[174:175], v211 offset:12800\n" \
"ds_read_b64_tr_b16 a[176:177], %[vvb] offset:9216\n" \
"v_mfma_f32_16x16x32_bf16 v[112:115], a[144:147], v[220:223], v[230:233]\n" \
"v_exp_f32_e64 v152, v152\n" \
"v_mfma_f32_16x16x32_bf16 v[84:87], a[148:151], v[216:219], v[230:233]\n" \
"v_cvt_pk_bf16_f32 v202, v148, v149\n" \
"v_cvt_pk_bf16_f32 v203, v150, v151\n" \
"v_mfma_f32_16x16x32_bf16 v[116:119], a[148:151], v[220:223], v[230:233]\n" \
"v_exp_f32_e64 v153, v153\n" \
"v_mfma_f32_16x16x32_bf16 v[88:91], a[152:155], v[216:219], v[230:233]\n" \
"v_exp_f32_e64 v154, v154\n" \
"v_mfma_f32_16x16x32_bf16 v[120:123], a[152:155], v[220:223], v[230:233]\n" \
"ds_read_b64_tr_b16 a[178:179], %[vvb] offset:13312\n" \
"v_mfma_f32_16x16x32_bf16 v[92:95], a[156:159], v[216:219], v[230:233]\n" \
"v_exp_f32_e64 v155, v155\n" \
"v_mov_b64_e32 v[216:217], 0\n" \
"v_mov_b64_e32 v[218:219], 0\n" \
"v_mfma_f32_16x16x32_bf16 v[124:127], a[156:159], v[220:223], v[230:233]\n" \
"ds_read_b64_tr_b16 a[180:181], v211 offset:9216\n" \
"s_waitcnt lgkmcnt(13)\n" \
"s_nop 0\n" \
"LmA_%=:\n" \
"v_mfma_f32_16x16x32_bf16 v[128:131], a[64:67], a[0:3], v[160:163]\n" \
"v_exp_f32_e64 v156, v156\n" \
"v_mfma_f32_16x16x32_bf16 v[128:131], a[68:71], a[4:7], v[128:131]\n" \
"ds_read_b64_tr_b16 a[182:183], v211 offset:13312\n" \
"s_add_u32 m0, %[m0b], 0\n" \
"v_mfma_f32_16x16x32_bf16 v[128:131], a[72:75], a[8:11], v[128:131]\n" \
"v_exp_f32_e32 v157, v157\n" \
"v_mfma_f32_16x16x32_bf16 v[128:131], a[76:79], a[12:15], v[128:131]\n" \
"v_cvt_pk_bf16_f32 v204, v152, v153\n" \
"v_cvt_pk_bf16_f32 v205, v154, v155\n" \
"v_mfma_f32_16x16x32_bf16 v[132:135], a[92:95], a[12:15], v[160:163]\n" \
"v_exp_f32_e64 v158, v158\n" \
"v_mfma_f32_16x16x32_bf16 v[132:135], a[88:91], a[8:11], v[132:135]\n" \
"ds_read_b64_tr_b16 a[184:185], %[vvb] offset:9728\n" \
"s_waitcnt lgkmcnt(5)\n" \
"v_mfma_f32_16x16x32_bf16 v[132:135], a[84:87], a[4:7], v[132:135]\n" \
"buffer_load_dwordx4 %[kd0], %[srdk], s48 offen offset:0 lds\n" \
"v_mfma_f32_16x16x32_bf16 v[132:135], a[80:83], a[0:3], v[132:135]\n" \
"v_exp_f32_e32 v159, v159\n" \
"v_mfma_f32_16x16x32_bf16 v[64:67], a[128:131], v[216:219], v[64:67]\n" \
"ds_read_b64_tr_b16 a[186:187], %[vvb] offset:13824\n" \
"ds_read_b64_tr_b16 a[188:189], v211 offset:9728\n" \
"v_mfma_f32_16x16x32_bf16 v[64:67], a[160:163], v[200:203], v[64:67]\n" \
"v_exp_f32_e64 v128, v128\n" \
"v_mfma_f32_16x16x32_bf16 v[68:71], a[132:135], v[216:219], v[68:71]\n" \
"v_cvt_pk_bf16_f32 v206, v156, v157\n" \
"v_cvt_pk_bf16_f32 v207, v158, v159\n" \
"v_mfma_f32_16x16x32_bf16 v[68:71], a[164:167], v[200:203], v[68:71]\n" \
"v_exp_f32_e64 v129, v129\n" \
"v_mfma_f32_16x16x32_bf16 v[72:75], a[136:139], v[216:219], v[72:75]\n" \
"ds_read_b64_tr_b16 a[190:191], v211 offset:13824\n" \
"ds_read_b64_tr_b16 a[128:129], %[vvb] offset:16384\n" \
"s_add_u32 s60, s60, 1\n" \
"s_nop 0\n" \
"v_readlane_b32 s58, %[tlo], s60\n" \
"v_mfma_f32_16x16x32_bf16 v[72:75], a[168:171], v[200:203], v[72:75]\n" \
"v_exp_f32_e64 v130, v130\n" \
"v_mfma_f32_16x16x32_bf16 v[76:79], a[140:143], v[216:219], v[76:79]\n" \
"ds_read_b64_tr_b16 a[130:131], %[vvb] offset:20480\n" \
"ds_read_b64_tr_b16 a[132:133], v211 offset:16384\n" \
"v_readlane_b32 s59, %[thi], s60\n" \
"v_mfma_f32_16x16x32_bf16 v[76:79], a[172:175], v[200:203], v[76:79]\n" \
"v_exp_f32_e64 v131, v131\n" \
"v_smfmac_f32_16x16x64_bf16 v[176:179], a[192:195], v[192:199], v228\n" \
"ds_read_b64_tr_b16 a[134:135], v211 offset:20480\n" \
"ds_read_b64_tr_b16 a[136:137], %[vvb] offset:16896\n" \
"v_mfma_f32_16x16x32_bf16 v[136:139], a[64:67], a[16:19], v[164:167]\n" \
"v_exp_f32_e64 v132, v132\n" \
"v_mfma_f32_16x16x32_bf16 v[136:139], a[68:71], a[20:23], v[136:139]\n" \
"ds_read_b64_tr_b16 a[138:139], %[vvb] offset:20992\n" \
"ds_read_b64_tr_b16 a[140:141], v211 offset:16896\n" \
"v_mfma_f32_16x16x32_bf16 v[136:139], a[72:75], a[24:27], v[136:139]\n" \
"v_exp_f32_e64 v133, v133\n" \
"v_mfma_f32_16x16x32_bf16 v[136:139], a[76:79], a[28:31], v[136:139]\n" \
"v_cvt_pk_bf16_f32 v184, v128, v129\n" \
"v_cvt_pk_bf16_f32 v185, v130, v131\n" \
"v_mfma_f32_16x16x32_bf16 v[140:143], a[92:95], a[28:31], v[164:167]\n" \
"v_exp_f32_e64 v134, v134\n" \
"v_mfma_f32_16x16x32_bf16 v[140:143], a[88:91], a[24:27], v[140:143]\n" \
"ds_read_b64_tr_b16 a[142:143], v211 offset:20992\n" \
"s_waitcnt lgkmcnt(8)\n" \
"v_mfma_f32_16x16x32_bf16 v[140:143], a[84:87], a[20:23], v[140:143]\n" \
"buffer_load_dwordx4 v212, %[srdk], s48 offen offset:1024 lds\n" \
"v_mfma_f32_16x16x32_bf16 v[140:143], a[80:83], a[16:19], v[140:143]\n" \
"v_exp_f32_e32 v135, v135\n" \
".if . - adaln_body != 12304\n" \
".error \"size model\"\n" \
".endif\n" \
"Lk2_%=:\n" \
"s_cbranch_execz Lk3_%=\n" \
"s_nop 0\n" \
"v_mfma_f32_16x16x32_bf16 v[80:83], a[144:147], v[216:219], v[80:83]\n" \
"ds_read_b64_tr_b16 a[144:145], %[vvb] offset:17408\n" \
"ds_read_b64_tr_b16 a[146:147], %[vvb] offset:21504\n" \
"v_mfma_f32_16x16x32_bf16 v[80:83], a[176:179], v[200:203], v[80:83]\n" \
"v_exp_f32_e64 v136, v136\n" \
"v_mfma_f32_16x16x32_bf16 v[84:87], a[148:151], v[216:219], v[84:87]\n" \
"v_cvt_pk_bf16_f32 v186, v132, v133\n" \
"v_cvt_pk_bf16_f32 v187, v134, v135\n" \
"v_mfma_f32_16x16x32_bf16 v[84:87], a[180:183], v[200:203], v[84:87]\n" \
"v_exp_f32_e64 v137, v137\n" \
"v_mfma_f32_16x16x32_bf16 v[88:91], a[152:155], v[216:219], v[88:91]\n" \
"v_exp_f32_e64 v138, v138\n" \
"v_mfma_f32_16x16x32_bf16 v[88:91], a[184:187], v[200:203], v[88:91]\n" \
"ds_read_b64_tr_b16 a[148:149], v211 offset:17408\n" \
"ds_read_b64_tr_b16 a[150:151], v211 offset:21504\n" \
"v_mfma_f32_16x16x32_bf16 v[92:95], a[156:159], v[216:219], v[92:95]\n" \
"v_exp_f32_e64 v139, v139\n" \
"v_mfma_f32_16x16x32_bf16 v[92:95], a[188:191], v[200:203], v[92:95]\n" \
"ds_read_b64_tr_b16 a[152:153], %[vvb] offset:17920\n" \
"v_mfma_f32_16x16x32_bf16 v[144:147], a[64:67], a[32:35], v[168:171]\n" \
"v_exp_f32_e64 v140, v140\n" \
"v_mfma_f32_16x16x32_bf16 v[144:147], a[68:71], a[36:39], v[144:147]\n" \
"ds_read_b64_tr_b16 a[154:155], %[vvb] offset:22016\n" \
"ds_read_b64_tr_b16 a[156:157], v211 offset:17920\n" \
"v_mfma_f32_16x16x32_bf16 v[144:147], a[72:75], a[40:43], v[144:147]\n" \
"v_exp_f32_e64 v141, v141\n" \
"v_mfma_f32_16x16x32_bf16 v[144:147], a[76:79], a[44:47], v[144:147]\n" \
"v_cvt_pk_bf16_f32 v188, v136, v137\n" \
"v_cvt_pk_bf16_f32 v189, v138, v139\n" \
"v_mfma_f32_16x16x32_bf16 v[148:151], a[92:95], a[44:47], v[168:171]\n" \
"v_exp_f32_e64 v142, v142\n" \
"v_mfma_f32_16x16x32_bf16 v[148:151], a[88:91], a[40:43], v[148:151]\n" \
"ds_read_b64_tr_b16 a[158:159], v211 offset:22016\n" \
"s_waitcnt lgkmcnt(8)\n" \
"v_mfma_f32_16x16x32_bf16 v[148:151], a[84:87], a[36:39], v[148:151]\n" \
"buffer_load_dwordx4 v213, %[srdk], s48 offen offset:2048 lds\n" \
"v_mfma_f32_16x16x32_bf16 v[148:151], a[80:83], a[32:35], v[148:151]\n" \
"v_exp_f32_e32 v143, v143\n" \
"v_mfma_f32_16x16x32_bf16 v[0:3], a[160:163], v[192:195], v[0:3]\n" \
"ds_read_b128 a[96:99], %[kb0] offset:24576\n" \
"v_mfma_f32_16x16x32_bf16 v[0:3], a[128:131], v[184:187], v[0:3]\n" \
"v_exp_f32_e64 v144, v144\n" \
"v_mfma_f32_16x16x32_bf16 v[4:7], a[164:167], v[192:195], v[4:7]\n" \
"v_cvt_pk_bf16_f32 v190, v140, v141\n" \
"v_cvt_pk_bf16_f32 v191, v142, v143\n" \
"v_mfma_f32_16x16x32_bf16 v[4:7], a[132:135], v[184:187], v[4:7]\n" \
"v_exp_f32_e64 v145, v145\n" \
"v_mfma_f32_16x16x32_bf16 v[8:11], a[168:171], v[192:195], v[8:11]\n" \
"ds_read_b128 a[100:103], v208 offset:24576\n" \
"v_mfma_f32_16x16x32_bf16 v[8:11], a[136:139], v[184:187], v[8:11]\n" \
"v_exp_f32_e64 v146, v146\n" \
"v_mfma_f32_16x16x32_bf16 v[12:15], a[172:175], v[192:195], v[12:15]\n" \
"ds_read_b128 a[104:107], v209 offset:24576\n" \
"v_mfma_f32_16x16x32_bf16 v[12:15], a[140:143], v[184:187], v[12:15]\n" \
"v_exp_f32_e64 v147, v147\n" \
"v_smfmac_f32_16x16x64_bf16 v[180:183], a[192:195], v[200:207], v228\n" \
"ds_read_b128 a[108:111], v210 offset:24576\n" \
"v_mfma_f32_16x16x32_bf16 v[152:155], a[64:67], a[48:51], v[172:175]\n" \
"v_exp_f32_e64 v148, v148\n" \
"v_mfma_f32_16x16x32_bf16 v[152:155], a[68:71], a[52:55], v[152:155]\n" \
"ds_read_b128 a[124:127], v210 offset:28672\n" \
"v_mfma_f32_16x16x32_bf16 v[152:155], a[72:75], a[56:59], v[152:155]\n" \
"v_exp_f32_e64 v149, v149\n" \
"v_mfma_f32_16x16x32_bf16 v[152:155], a[76:79], a[60:63], v[152:155]\n" \
"v_cvt_pk_bf16_f32 v216, v144, v145\n" \
"v_cvt_pk_bf16_f32 v217, v146, v147\n" \
"v_mfma_f32_16x16x32_bf16 v[156:159], a[92:95], a[60:63], v[172:175]\n" \
"v_exp_f32_e64 v150, v150\n" \
"v_mfma_f32_16x16x32_bf16 v[156:159], a[88:91], a[56:59], v[156:159]\n" \
"ds_read_b128 a[120:123], v209 offset:28672\n" \
"s_waitcnt lgkmcnt(6)\n" \
"v_mfma_f32_16x16x32_bf16 v[156:159], a[84:87], a[52:55], v[156:159]\n" \
"buffer_load_dwordx4 v214, %[srdk], s48 offen offset:3072 lds\n" \
"v_mfma_f32_16x16x32_bf16 v[156:159], a[80:83], a[48:51], v[156:159]\n" \
"v_exp_f32_e32 v151, v151\n" \
"v_mfma_f32_16x16x32_bf16 v[16:19], a[176:179], v[192:195], v[16:19]\n" \
"ds_read_b128 a[116:119], v208 offset:28672\n" \
"v_mfma_f32_16x16x32_bf16 v[16:19], a[144:147], v[184:187], v[16:19]\n" \
"v_exp_f32_e64 v152, v152\n" \
"v_mfma_f32_16x16x32_bf16 v[20:23], a[180:183], v[192:195], v[20:23]\n" \
"v_cvt_pk_bf16_f32 v218, v148, v149\n" \
"v_cvt_pk_bf16_f32 v219, v150, v151\n" \
"v_mfma_f32_16x16x32_bf16 v[20:23], a[148:151], v[184:187], v[20:23]\n" \
"v_exp_f32_e64 v153, v153\n" \
"v_mfma_f32_16x16x32_bf16 v[24:27], a[184:187], v[192:195], v[24:27]\n" \
"v_exp_f32_e64 v154, v154\n" \
"v_mfma_f32_16x16x32_bf16 v[24:27], a[152:155], v[184:187], v[24:27]\n" \
"ds_read_b128 a[112:115], %[kb0] offset:28672\n" \
"s_add_u32 s48, s48, 0x4000\n" \
"s_mov_b32 m0, s44\n" \
"s_addk_i32 s44, 0x400\n" \
"buffer_load_dwordx4 v229, s[64:67], s45 offen offset:0 nt lds\n" \
"s_add_u32 s45, s45, 393216\n" \
"v_mfma_f32_16x16x32_bf16 v[28:31], a[188:191], v[192:195], v[28:31]\n" \
"v_exp_f32_e64 v155, v155\n" \
"v_mfma_f32_16x16x32_bf16 v[28:31], a[156:159], v[184:187], v[28:31]\n" \
"s_waitcnt vmcnt(14) lgkmcnt(2)\n" \
"s_barrier\n" \
"v_mfma_f32_16x16x32_bf16 v[128:131], a[96:99], a[0:3], v[160:163]\n" \
"v_exp_f32_e64 v156, v156\n" \
"v_mfma_f32_16x16x32_bf16 v[128:131], a[100:103], a[4:7], v[128:131]\n" \
"s_add_u32 m0, %[m0b], 65536\n" \
"v_mfma_f32_16x16x32_bf16 v[128:131], a[104:107], a[8:11], v[128:131]\n" \
"v_exp_f32_e64 v157, v157\n" \
"v_mfma_f32_16x16x32_bf16 v[128:131], a[108:111], a[12:15], v[128:131]\n" \
"v_cvt_pk_bf16_f32 v220, v152, v153\n" \
"v_cvt_pk_bf16_f32 v221, v154, v155\n" \
"v_mfma_f32_16x16x32_bf16 v[132:135], a[124:127], a[12:15], v[160:163]\n" \
"v_exp_f32_e64 v158, v158\n" \
"v_mfma_f32_16x16x32_bf16 v[132:135], a[120:123], a[8:11], v[132:135]\n" \
"s_waitcnt lgkmcnt(0)\n" \
"v_mfma_f32_16x16x32_bf16 v[132:135], a[116:119], a[4:7], v[132:135]\n" \
"buffer_load_dwordx4 %[vd0], %[srdv], s48 offen offset:0 lds\n" \
"v_mfma_f32_16x16x32_bf16 v[132:135], a[112:115], a[0:3], v[132:135]\n" \
"v_exp_f32_e32 v159, v159\n" \
"v_mfma_f32_16x16x32_bf16 v[32:35], a[160:163], v[196:199], v[32:35]\n" \
"ds_read_b128 a[64:67], %[kb0] offset:32768\n" \
"v_mfma_f32_16x16x32_bf16 v[32:35], a[128:131], v[188:191], v[32:35]\n" \
"v_exp_f32_e64 v128, v128\n" \
"v_mfma_f32_16x16x32_bf16 v[36:39], a[164:167], v[196:199], v[36:39]\n" \
"v_cvt_pk_bf16_f32 v222, v156, v157\n" \
"v_cvt_pk_bf16_f32 v223, v158, v159\n" \
"v_mfma_f32_16x16x32_bf16 v[36:39], a[132:135], v[188:191], v[36:39]\n" \
"v_exp_f32_e64 v129, v129\n" \
"v_mfma_f32_16x16x32_bf16 v[40:43], a[168:171], v[196:199], v[40:43]\n" \
"ds_read_b128 a[68:71], v208 offset:32768\n" \
"v_mfma_f32_16x16x32_bf16 v[40:43], a[136:139], v[188:191], v[40:43]\n" \
"v_exp_f32_e64 v130, v130\n" \
"v_mfma_f32_16x16x32_bf16 v[44:47], a[172:175], v[196:199], v[44:47]\n" \
"ds_read_b128 a[72:75], v209 offset:32768\n" \
"v_mfma_f32_16x16x32_bf16 v[44:47], a[140:143], v[188:191], v[44:47]\n" \
"v_exp_f32_e64 v131, v131\n" \
"v_smfmac_f32_16x16x64_bf16 v[176:179], a[192:195], v[184:191], v228\n" \
"v_mfma_f32_16x16x32_bf16 v[136:139], a[96:99], a[16:19], v[164:167]\n" \
"v_exp_f32_e64 v132, v132\n" \
"v_mfma_f32_16x16x32_bf16 v[136:139], a[100:103], a[20:23], v[136:139]\n" \
"ds_read_b128 a[76:79], v210 offset:32768\n" \
"v_mfma_f32_16x16x32_bf16 v[136:139], a[104:107], a[24:27], v[136:139]\n" \
"v_exp_f32_e64 v133, v133\n" \
"v_mfma_f32_16x16x32_bf16 v[136:139], a[108:111], a[28:31], v[136:139]\n" \
"v_cvt_pk_bf16_f32 v192, v128, v129\n" \
"v_cvt_pk_bf16_f32 v193, v130, v131\n" \
"v_mfma_f32_16x16x32_bf16 v[140:143], a[124:127], a[28:31], v[164:167]\n" \
"v_exp_f32_e64 v134, v134\n" \
"v_mfma_f32_16x16x32_bf16 v[140:143], a[120:123], a[24:27], v[140:143]\n" \
"ds_read_b128 a[92:95], v210 offset:36864\n" \
"v_mfma_f32_16x16x32_bf16 v[140:143], a[116:119], a[20:23], v[140:143]\n" \
"buffer_load_dwordx4 v215, %[srdv], s48 offen offset:1024 lds\n" \
"v_mfma_f32_16x16x32_bf16 v[140:143], a[112:115], a[16:19], v[140:143]\n" \
"v_exp_f32_e64 v135, v135\n" \
"v_mfma_f32_16x16x32_bf16 v[48:51], a[176:179], v[196:199], v[48:51]\n" \
"ds_read_b128 a[88:91], v209 offset:36864\n" \
"v_mfma_f32_16x16x32_bf16 v[48:51], a[144:147], v[188:191], v[48:51]\n" \
"v_exp_f32_e64 v136, v136\n" \
"v_mfma_f32_16x16x32_bf16 v[52:55], a[180:183], v[196:199], v[52:55]\n" \
"v_cvt_pk_bf16_f32 v194, v132, v133\n" \
"v_cvt_pk_bf16_f32 v195, v134, v135\n" \
"v_mfma_f32_16x16x32_bf16 v[52:55], a[148:151], v[188:191], v[52:55]\n" \
"v_exp_f32_e64 v137, v137\n" \
"v_mfma_f32_16x16x32_bf16 v[56:59], a[184:187], v[196:199], v[56:59]\n" \
"v_exp_f32_e64 v138, v138\n" \
"v_mfma_f32_16x16x32_bf16 v[56:59], a[152:155], v[188:191], v[56:59]\n" \
"ds_read_b128 a[84:87], v208 offset:36864\n" \
"v_mfma_f32_16x16x32_bf16 v[60:63], a[188:191], v[196:199], v[60:63]\n" \
"v_exp_f32_e64 v139, v139\n" \
"v_mfma_f32_16x16x32_bf16 v[60:63], a[156:159], v[188:191], v[60:63]\n" \
"s_cmp_lg_u32 %[wid], 0\n" \
"s_cbranch_scc1 LtA3_%=\n" \
"global_load_dword v237, v224, s[58:59]\n" \
"s_add_u32 s61, s48, 49152\n" \
"s_mov_b32 exec_hi, 0\n" \
"s_nop 0\n" \
"buffer_load_dword v237, v224, %[srdp], s61 offen\n" \
"s_mov_b32 exec_hi, -1\n" \
"s_nop 0\n" \
"LtA3_%=:\n" \
"v_mfma_f32_16x16x32_bf16 v[144:147], a[96:99], a[32:35], v[168:171]\n" \
"v_exp_f32_e64 v140, v140\n" \
"v_mfma_f32_16x16x32_bf16 v[144:147], a[100:103], a[36:39], v[144:147]\n" \
"ds_read_b128 a[80:83], %[kb0] offset:36864\n" \
"v_mfma_f32_16x16x32_bf16 v[144:147], a[104:107], a[40:43], v[144:147]\n" \
"v_exp_f32_e64 v141, v141\n" \
"v_mfma_f32_16x16x32_bf16 v[144:147], a[108:111], a[44:47], v[144:147]\n" \
"v_cvt_pk_bf16_f32 v196, v136, v137\n" \
"v_cvt_pk_bf16_f32 v197, v138, v139\n" \
"v_mfma_f32_16x16x32_bf16 v[148:151], a[124:127], a[44:47], v[168:171]\n" \
"v_exp_f32_e64 v142, v142\n" \
"v_mfma_f32_16x16x32_bf16 v[148:151], a[120:123], a[40:43], v[148:151]\n" \
"v_mfma_f32_16x16x32_bf16 v[148:151], a[116:119], a[36:39], v[148:151]\n" \
"buffer_load_dwordx4 %[vd0], %[srdv], s48 offen offset:2048 lds\n" \
"v_mfma_f32_16x16x32_bf16 v[148:151], a[112:115], a[32:35], v[148:151]\n" \
"v_exp_f32_e64 v143, v143\n" \
"v_mfma_f32_16x16x32_bf16 v[96:99], a[160:163], v[204:207], v[96:99]\n" \
"ds_read_b64_tr_b16 a[160:161], %[vvb] offset:24576\n" \
"v_mfma_f32_16x16x32_bf16 v[96:99], a[128:131], v[220:223], v[96:99]\n" \
"v_exp_f32_e64 v144, v144\n" \
"v_mfma_f32_16x16x32_bf16 v[100:103], a[164:167], v[204:207], v[100:103]\n" \
"v_cvt_pk_bf16_f32 v198, v140, v141\n" \
"v_cvt_pk_bf16_f32 v199, v142, v143\n" \
"v_mfma_f32_16x16x32_bf16 v[100:103], a[132:135], v[220:223], v[100:103]\n" \
"v_exp_f32_e64 v145, v145\n" \
"v_mfma_f32_16x16x32_bf16 v[104:107], a[168:171], v[204:207], v[104:107]\n" \
"ds_read_b64_tr_b16 a[162:163], %[vvb] offset:28672\n" \
"ds_read_b64_tr_b16 a[164:165], v211 offset:24576\n" \
"v_mfma_f32_16x16x32_bf16 v[104:107], a[136:139], v[220:223], v[104:107]\n" \
"v_exp_f32_e64 v146, v146\n" \
"v_mfma_f32_16x16x32_bf16 v[108:111], a[172:175], v[204:207], v[108:111]\n" \
"ds_read_b64_tr_b16 a[166:167], v211 offset:28672\n" \
"v_mfma_f32_16x16x32_bf16 v[108:111], a[140:143], v[220:223], v[108:111]\n" \
"v_exp_f32_e64 v147, v147\n" \
"v_smfmac_f32_16x16x64_bf16 v[180:183], a[192:195], v[216:223], v228\n" \
"ds_read_b64_tr_b16 a[168:169], %[vvb] offset:25088\n" \
"v_mfma_f32_16x16x32_bf16 v[152:155], a[96:99], a[48:51], v[172:175]\n" \
"v_exp_f32_e64 v148, v148\n" \
"v_mfma_f32_16x16x32_bf16 v[152:155], a[100:103], a[52:55], v[152:155]\n" \
"ds_read_b64_tr_b16 a[170:171], %[vvb] offset:29184\n" \
"v_mfma_f32_16x16x32_bf16 v[152:155], a[104:107], a[56:59], v[152:155]\n" \
"v_exp_f32_e64 v149, v149\n" \
"v_mfma_f32_16x16x32_bf16 v[152:155], a[108:111], a[60:63], v[152:155]\n" \
"v_cvt_pk_bf16_f32 v200, v144, v145\n" \
"v_cvt_pk_bf16_f32 v201, v146, v147\n" \
"v_mfma_f32_16x16x32_bf16 v[156:159], a[124:127], a[60:63], v[172:175]\n" \
"v_exp_f32_e64 v150, v150\n" \
"v_mfma_f32_16x16x32_bf16 v[156:159], a[120:123], a[56:59], v[156:159]\n" \
"ds_read_b64_tr_b16 a[172:173], v211 offset:25088\n" \
"v_mfma_f32_16x16x32_bf16 v[156:159], a[116:119], a[52:55], v[156:159]\n" \
"buffer_load_dwordx4 v215, %[srdv], s48 offen offset:3072 lds\n" \
"v_mfma_f32_16x16x32_bf16 v[156:159], a[112:115], a[48:51], v[156:159]\n" \
"v_exp_f32_e64 v151, v151\n" \
"v_mfma_f32_16x16x32_bf16 v[112:115], a[176:179], v[204:207], v[112:115]\n" \
"ds_read_b64_tr_b16 a[174:175], v211 offset:29184\n" \
"ds_read_b64_tr_b16 a[176:177], %[vvb] offset:25600\n" \
"v_mfma_f32_16x16x32_bf16 v[112:115], a[144:147], v[220:223], v[112:115]\n" \
"v_exp_f32_e64 v152, v152\n" \
"v_mfma_f32_16x16x32_bf16 v[116:119], a[180:183], v[204:207], v[116:119]\n" \
"v_cvt_pk_bf16_f32 v202, v148, v149\n" \
"v_cvt_pk_bf16_f32 v203, v150, v151\n" \
"v_mfma_f32_16x16x32_bf16 v[116:119], a[148:151], v[220:223], v[116:119]\n" \
"v_exp_f32_e64 v153, v153\n" \
"v_mfma_f32_16x16x32_bf16 v[120:123], a[184:187], v[204:207], v[120:123]\n" \
"v_exp_f32_e64 v154, v154\n" \
"v_mfma_f32_16x16x32_bf16 v[120:123], a[152:155], v[220:223], v[120:123]\n" \
"ds_read_b64_tr_b16 a[178:179], %[vvb] offset:29696\n" \
"v_mfma_f32_16x16x32_bf16 v[124:127], a[188:191], v[204:207], v[124:127]\n" \
"v_exp_f32_e64 v155, v155\n" \
"v_mfma_f32_16x16x32_bf16 v[124:127], a[156:159], v[220:223], v[124:127]\n" \
"ds_read_b64_tr_b16 a[180:181], v211 offset:25600\n" \
"s_waitcnt lgkmcnt(13)\n" \
"v_mfma_f32_16x16x32_bf16 v[128:131], a[64:67], a[0:3], v[160:163]\n" \
"v_exp_f32_e32 v156, v156\n" \
"v_mfma_f32_16x16x32_bf16 v[128:131], a[68:71], a[4:7], v[128:131]\n" \
"ds_read_b64_tr_b16 a[182:183], v211 offset:29696\n" \
"s_add_u32 m0, %[m0b], 16384\n" \
"v_mfma_f32_16x16x32_bf16 v[128:131], a[72:75], a[8:11], v[128:131]\n" \
"v_exp_f32_e64 v157, v157\n" \
"v_mfma_f32_16x16x32_bf16 v[128:131], a[76:79], a[12:15], v[128:131]\n" \
"v_cvt_pk_bf16_f32 v204, v152, v153\n" \
"v_cvt_pk_bf16_f32 v205, v154, v155\n" \
"v_mfma_f32_16x16x32_bf16 v[132:135], a[92:95], a[12:15], v[160:163]\n" \
"v_exp_f32_e64 v158, v158\n" \
"v_mfma_f32_16x16x32_bf16 v[132:135], a[88:91], a[8:11], v[132:135]\n" \
"ds_read_b64_tr_b16 a[184:185], %[vvb] offset:26112\n" \
"s_waitcnt lgkmcnt(5)\n" \
"v_mfma_f32_16x16x32_bf16 v[132:135], a[84:87], a[4:7], v[132:135]\n" \
"buffer_load_dwordx4 %[kd0], %[srdk], s48 offen offset:0 lds\n" \
"v_mfma_f32_16x16x32_bf16 v[132:135], a[80:83], a[0:3], v[132:135]\n" \
"v_exp_f32_e32 v159, v159\n" \
"v_mfma_f32_16x16x32_bf16 v[64:67], a[128:131], v[216:219], v[64:67]\n" \
"ds_read_b64_tr_b16 a[186:187], %[vvb] offset:30208\n" \
"ds_read_b64_tr_b16 a[188:189], v211 offset:26112\n" \
"v_mfma_f32_16x16x32_bf16 v[64:67], a[160:163], v[200:203], v[64:67]\n" \
"v_exp_f32_e64 v128, v128\n" \
"v_mfma_f32_16x16x32_bf16 v[68:71], a[132:135], v[216:219], v[68:71]\n" \
"v_cvt_pk_bf16_f32 v206, v156, v157\n" \
"v_cvt_pk_bf16_f32 v207, v158, v159\n" \
"v_mfma_f32_16x16x32_bf16 v[68:71], a[164:167], v[200:203], v[68:71]\n" \
"v_exp_f32_e64 v129, v129\n" \
"v_mfma_f32_16x16x32_bf16 v[72:75], a[136:139], v[216:219], v[72:75]\n" \
"ds_read_b64_tr_b16 a[190:191], v211 offset:30208\n" \
"ds_read_b64_tr_b16 a[128:129], %[vvb] offset:32768\n" \
"v_mfma_f32_16x16x32_bf16 v[72:75], a[168:171], v[200:203], v[72:75]\n" \
"v_exp_f32_e64 v130, v130\n" \
"v_mfma_f32_16x16x32_bf16 v[76:79], a[140:143], v[216:219], v[76:79]\n" \
"ds_read_b64_tr_b16 a[130:131], %[vvb] offset:36864\n" \
"ds_read_b64_tr_b16 a[132:133], v211 offset:32768\n" \
"v_mfma_f32_16x16x32_bf16 v[76:79], a[172:175], v[200:203], v[76:79]\n" \
"v_exp_f32_e64 v131, v131\n" \
"v_smfmac_f32_16x16x64_bf16 v[176:179], a[192:195], v[192:199], v228\n" \
"ds_read_b64_tr_b16 a[134:135], v211 offset:36864\n" \
"ds_read_b64_tr_b16 a[136:137], %[vvb] offset:33280\n" \
"v_mfma_f32_16x16x32_bf16 v[136:139], a[64:67], a[16:19], v[164:167]\n" \
"v_exp_f32_e64 v132, v132\n" \
"v_mfma_f32_16x16x32_bf16 v[136:139], a[68:71], a[20:23], v[136:139]\n" \
"ds_read_b64_tr_b16 a[138:139], %[vvb] offset:37376\n" \
"ds_read_b64_tr_b16 a[140:141], v211 offset:33280\n" \
"v_mfma_f32_16x16x32_bf16 v[136:139], a[72:75], a[24:27], v[136:139]\n" \
"v_exp_f32_e64 v133, v133\n" \
"v_mfma_f32_16x16x32_bf16 v[136:139], a[76:79], a[28:31], v[136:139]\n" \
"v_cvt_pk_bf16_f32 v184, v128, v129\n" \
"v_cvt_pk_bf16_f32 v185, v130, v131\n" \
"v_mfma_f32_16x16x32_bf16 v[140:143], a[92:95], a[28:31], v[164:167]\n" \
"v_exp_f32_e64 v134, v134\n" \
"v_mfma_f32_16x16x32_bf16 v[140:143], a[88:91], a[24:27], v[140:143]\n" \
"ds_read_b64_tr_b16 a[142:143], v211 offset:37376\n" \
"s_waitcnt lgkmcnt(8)\n" \
"v_mfma_f32_16x16x32_bf16 v[140:143], a[84:87], a[20:23], v[140:143]\n" \
"buffer_load_dwordx4 v212, %[srdk], s48 offen offset:1024 lds\n" \
"v_mfma_f32_16x16x32_bf16 v[140:143], a[80:83], a[16:19], v[140:143]\n" \
"v_exp_f32_e32 v135, v135\n" \
"v_mfma_f32_16x16x32_bf16 v[80:83], a[144:147], v[216:219], v[80:83]\n" \
"ds_read_b64_tr_b16 a[144:145], %[vvb] offset:33792\n" \
"ds_read_b64_tr_b16 a[146:147], %[vvb] offset:37888\n" \
"v_mfma_f32_16x16x32_bf16 v[80:83], a[176:179], v[200:203], v[80:83]\n" \
"v_exp_f32_e64 v136, v136\n" \
"v_mfma_f32_16x16x32_bf16 v[84:87], a[148:151], v[216:219], v[84:87]\n" \
"v_cvt_pk_bf16_f32 v186, v132, v133\n" \
"v_cvt_pk_bf16_f32 v187, v134, v135\n" \
"v_mfma_f32_16x16x32_bf16 v[84:87], a[180:183], v[200:203], v[84:87]\n" \
"v_exp_f32_e64 v137, v137\n" \
"v_mfma_f32_16x16x32_bf16 v[88:91], a[152:155], v[216:219], v[88:91]\n" \
"v_exp_f32_e64 v138, v138\n" \
"v_mfma_f32_16x16x32_bf16 v[88:91], a[184:187], v[200:203], v[88:91]\n" \
"ds_read_b64_tr_b16 a[148:149], v211 offset:33792\n" \
"ds_read_b64_tr_b16 a[150:151], v211 offset:37888\n" \
"v_mfma_f32_16x16x32_bf16 v[92:95], a[156:159], v[216:219], v[92:95]\n" \
"v_exp_f32_e64 v139, v139\n" \
"v_mfma_f32_16x16x32_bf16 v[92:95], a[188:191], v[200:203], v[92:95]\n" \
"ds_read_b64_tr_b16 a[152:153], %[vvb] offset:34304\n" \
"buffer_store_dwordx4 a[196:199], v235, %[srdz], s62 offen offset:0 sc1\n" \
"v_mfma_f32_16x16x32_bf16 v[144:147], a[64:67], a[32:35], v[168:171]\n" \
"v_exp_f32_e64 v140, v140\n" \
"v_mfma_f32_16x16x32_bf16 v[144:147], a[68:71], a[36:39], v[144:147]\n" \
"ds_read_b64_tr_b16 a[154:155], %[vvb] offset:38400\n" \
"ds_read_b64_tr_b16 a[156:157], v211 offset:34304\n" \
"v_mfma_f32_16x16x32_bf16 v[144:147], a[72:75], a[40:43], v[144:147]\n" \
"v_exp_f32_e64 v141, v141\n" \
"v_mfma_f32_16x16x32_bf16 v[144:147], a[76:79], a[44:47], v[144:147]\n" \
"v_cvt_pk_bf16_f32 v188, v136, v137\n" \
"v_cvt_pk_bf16_f32 v189, v138, v139\n" \
"v_mfma_f32_16x16x32_bf16 v[148:151], a[92:95], a[44:47], v[168:171]\n" \
"v_exp_f32_e64 v142, v142\n" \
"v_mfma_f32_16x16x32_bf16 v[148:151], a[88:91], a[40:43], v[148:151]\n" \
"ds_read_b64_tr_b16 a[158:159], v211 offset:38400\n" \
"s_waitcnt lgkmcnt(8)\n" \
"v_mfma_f32_16x16x32_bf16 v[148:151], a[84:87], a[36:39], v[148:151]\n" \
"buffer_load_dwordx4 v213, %[srdk], s48 offen offset:2048 lds\n" \
"v_mfma_f32_16x16x32_bf16 v[148:151], a[80:83], a[32:35], v[148:151]\n" \
"v_exp_f32_e32 v143, v143\n" \
"v_mfma_f32_16x16x32_bf16 v[0:3], a[160:163], v[192:195], v[0:3]\n" \
"ds_read_b128 a[96:99], %[kb0] offset:40960\n" \
"v_mfma_f32_16x16x32_bf16 v[0:3], a[128:131], v[184:187], v[0:3]\n" \
"v_exp_f32_e64 v144, v144\n" \
"v_mfma_f32_16x16x32_bf16 v[4:7], a[164:167], v[192:195], v[4:7]\n" \
"v_cvt_pk_bf16_f32 v190, v140, v141\n" \
"v_cvt_pk_bf16_f32 v191, v142, v143\n" \
"v_mfma_f32_16x16x32_bf16 v[4:7], a[132:135], v[184:187], v[4:7]\n" \
"v_exp_f32_e64 v145, v145\n" \
"v_mfma_f32_16x16x32_bf16 v[8:11], a[168:171], v[192:195], v[8:11]\n" \
"ds_read_b128 a[100:103], v208 offset:40960\n" \
"v_mfma_f32_16x16x32_bf16 v[8:11], a[136:139], v[184:187], v[8:11]\n" \
"v_exp_f32_e64 v146, v146\n" \
"v_mfma_f32_16x16x32_bf16 v[12:15], a[172:175], v[192:195], v[12:15]\n" \
"ds_read_b128 a[104:107], v209 offset:40960\n" \
"v_mfma_f32_16x16x32_bf16 v[12:15], a[140:143], v[184:187], v[12:15]\n" \
"v_exp_f32_e64 v147, v147\n" \
"v_smfmac_f32_16x16x64_bf16 v[180:183], a[192:195], v[200:207], v228\n" \
"ds_read_b128 a[108:111], v210 offset:40960\n" \
"v_mfma_f32_16x16x32_bf16 v[152:155], a[64:67], a[48:51], v[172:175]\n" \
"v_exp_f32_e64 v148, v148\n" \
"v_mfma_f32_16x16x32_bf16 v[152:155], a[68:71], a[52:55], v[152:155]\n" \
"ds_read_b128 a[124:127], v210 offset:45056\n" \
"v_mfma_f32_16x16x32_bf16 v[152:155], a[72:75], a[56:59], v[152:155]\n" \
"v_exp_f32_e64 v149, v149\n" \
"v_mfma_f32_16x16x32_bf16 v[152:155], a[76:79], a[60:63], v[152:155]\n" \
"v_cvt_pk_bf16_f32 v216, v144, v145\n" \
"v_cvt_pk_bf16_f32 v217, v146, v147\n" \
"v_mfma_f32_16x16x32_bf16 v[156:159], a[92:95], a[60:63], v[172:175]\n" \
"v_exp_f32_e64 v150, v150\n" \
"v_mfma_f32_16x16x32_bf16 v[156:159], a[88:91], a[56:59], v[156:159]\n" \
"ds_read_b128 a[120:123], v209 offset:45056\n" \
"s_waitcnt lgkmcnt(6)\n" \
"v_mfma_f32_16x16x32_bf16 v[156:159], a[84:87], a[52:55], v[156:159]\n" \
"buffer_load_dwordx4 v214, %[srdk], s48 offen offset:3072 lds\n" \
"v_mfma_f32_16x16x32_bf16 v[156:159], a[80:83], a[48:51], v[156:159]\n" \
"v_exp_f32_e32 v151, v151\n" \
"v_mfma_f32_16x16x32_bf16 v[16:19], a[176:179], v[192:195], v[16:19]\n" \
"ds_read_b128 a[116:119], v208 offset:45056\n" \
"v_mfma_f32_16x16x32_bf16 v[16:19], a[144:147], v[184:187], v[16:19]\n" \
"v_exp_f32_e64 v152, v152\n" \
"v_mfma_f32_16x16x32_bf16 v[20:23], a[180:183], v[192:195], v[20:23]\n" \
"v_cvt_pk_bf16_f32 v218, v148, v149\n" \
"v_cvt_pk_bf16_f32 v219, v150, v151\n" \
"v_mfma_f32_16x16x32_bf16 v[20:23], a[148:151], v[184:187], v[20:23]\n" \
"v_exp_f32_e64 v153, v153\n" \
"v_mfma_f32_16x16x32_bf16 v[24:27], a[184:187], v[192:195], v[24:27]\n" \
"v_exp_f32_e64 v154, v154\n" \
"v_mfma_f32_16x16x32_bf16 v[24:27], a[152:155], v[184:187], v[24:27]\n" \
"ds_read_b128 a[112:115], %[kb0] offset:45056\n" \
"s_add_u32 s48, s48, 0x4000\n" \
"v_mfma_f32_16x16x32_bf16 v[28:31], a[188:191], v[192:195], v[28:31]\n" \
"v_exp_f32_e64 v155, v155\n" \
"v_mfma_f32_16x16x32_bf16 v[28:31], a[156:159], v[184:187], v[28:31]\n" \
"s_waitcnt vmcnt(14) lgkmcnt(2)\n" \
"s_barrier\n" \
"v_mfma_f32_16x16x32_bf16 v[128:131], a[96:99], a[0:3], v[160:163]\n" \
"v_exp_f32_e64 v156, v156\n" \
"v_mfma_f32_16x16x32_bf16 v[128:131], a[100:103], a[4:7], v[128:131]\n" \
"s_add_u32 m0, %[m0b], 81920\n" \
"v_mfma_f32_16x16x32_bf16 v[128:131], a[104:107], a[8:11], v[128:131]\n" \
"v_exp_f32_e64 v157, v157\n" \
"v_mfma_f32_16x16x32_bf16 v[128:131], a[108:111], a[12:15], v[128:131]\n" \
"v_cvt_pk_bf16_f32 v220, v152, v153\n" \
"v_cvt_pk_bf16_f32 v221, v154, v155\n" \
"v_mfma_f32_16x16x32_bf16 v[132:135], a[124:127], a[12:15], v[160:163]\n" \
"v_exp_f32_e64 v158, v158\n" \
"v_mfma_f32_16x16x32_bf16 v[132:135], a[120:123], a[8:11], v[132:135]\n" \
"s_waitcnt lgkmcnt(0)\n" \
"v_mfma_f32_16x16x32_bf16 v[132:135], a[116:119], a[4:7], v[132:135]\n" \
"buffer_load_dwordx4 %[vd0], %[srdv], s48 offen offset:0 lds\n" \
"v_mfma_f32_16x16x32_bf16 v[132:135], a[112:115], a[0:3], v[132:135]\n" \
"v_exp_f32_e32 v159, v159\n" \
"v_mfma_f32_16x16x32_bf16 v[32:35], a[160:163], v[196:199], v[32:35]\n" \
"ds_read_b128 a[64:67], %[kb0] offset:49152\n" \
"v_mfma_f32_16x16x32_bf16 v[32:35], a[128:131], v[188:191], v[32:35]\n" \
"v_exp_f32_e64 v128, v128\n" \
"v_mfma_f32_16x16x32_bf16 v[36:39], a[164:167], v[196:199], v[36:39]\n" \
"v_cvt_pk_bf16_f32 v222, v156, v157\n" \
"v_cvt_pk_bf16_f32 v223, v158, v159\n" \
"v_mfma_f32_16x16x32_bf16 v[36:39], a[132:135], v[188:191], v[36:39]\n" \
"v_exp_f32_e64 v129, v129\n" \
"v_mfma_f32_16x16x32_bf16 v[40:43], a[168:171], v[196:199], v[40:43]\n" \
"ds_read_b128 a[68:71], v208 offset:49152\n" \
"v_mfma_f32_16x16x32_bf16 v[40:43], a[136:139], v[188:191], v[40:43]\n" \
"v_exp_f32_e64 v130, v130\n" \
"v_mfma_f32_16x16x32_bf16 v[44:47], a[172:175], v[196:199], v[44:47]\n" \
"ds_read_b128 a[72:75], v209 offset:49152\n" \
"v_mfma_f32_16x16x32_bf16 v[44:47], a[140:143], v[188:191], v[44:47]\n" \
"v_exp_f32_e64 v131, v131\n" \
"v_smfmac_f32_16x16x64_bf16 v[176:179], a[192:195], v[184:191], v228\n" \
"v_mfma_f32_16x16x32_bf16 v[136:139], a[96:99], a[16:19], v[164:167]\n" \
"v_exp_f32_e64 v132, v132\n" \
"v_mfma_f32_16x16x32_bf16 v[136:139], a[100:103], a[20:23], v[136:139]\n" \
"ds_read_b128 a[76:79], v210 offset:49152\n" \
"v_mfma_f32_16x16x32_bf16 v[136:139], a[104:107], a[24:27], v[136:139]\n" \
"v_exp_f32_e64 v133, v133\n" \
"v_mfma_f32_16x16x32_bf16 v[136:139], a[108:111], a[28:31], v[136:139]\n" \
"v_cvt_pk_bf16_f32 v192, v128, v129\n" \
"v_cvt_pk_bf16_f32 v193, v130, v131\n" \
"v_mfma_f32_16x16x32_bf16 v[140:143], a[124:127], a[28:31], v[164:167]\n" \
"v_exp_f32_e64 v134, v134\n" \
"v_mfma_f32_16x16x32_bf16 v[140:143], a[120:123], a[24:27], v[140:143]\n" \
"ds_read_b128 a[92:95], v210 offset:53248\n" \
"v_mfma_f32_16x16x32_bf16 v[140:143], a[116:119], a[20:23], v[140:143]\n" \
"buffer_load_dwordx4 v215, %[srdv], s48 offen offset:1024 lds\n" \
"v_mfma_f32_16x16x32_bf16 v[140:143], a[112:115], a[16:19], v[140:143]\n" \
"v_exp_f32_e64 v135, v135\n" \
"v_mfma_f32_16x16x32_bf16 v[48:51], a[176:179], v[196:199], v[48:51]\n" \
"ds_read_b128 a[88:91], v209 offset:53248\n" \
"v_mfma_f32_16x16x32_bf16 v[48:51], a[144:147], v[188:191], v[48:51]\n" \
"v_exp_f32_e64 v136, v136\n" \
"v_mfma_f32_16x16x32_bf16 v[52:55], a[180:183], v[196:199], v[52:55]\n" \
"v_cvt_pk_bf16_f32 v194, v132, v133\n" \
"v_cvt_pk_bf16_f32 v195, v134, v135\n" \
"v_mfma_f32_16x16x32_bf16 v[52:55], a[148:151], v[188:191], v[52:55]\n" \
"v_exp_f32_e64 v137, v137\n" \
"v_mfma_f32_16x16x32_bf16 v[56:59], a[184:187], v[196:199], v[56:59]\n" \
"v_exp_f32_e64 v138, v138\n" \
"v_mfma_f32_16x16x32_bf16 v[56:59], a[152:155], v[188:191], v[56:59]\n" \
"ds_read_b128 a[84:87], v208 offset:53248\n" \
"v_mfma_f32_16x16x32_bf16 v[60:63], a[188:191], v[196:199], v[60:63]\n" \
"v_exp_f32_e64 v139, v139\n" \
"v_mfma_f32_16x16x32_bf16 v[60:63], a[156:159], v[188:191], v[60:63]\n" \
"s_cmp_lg_u32 %[wid], 1\n" \
"s_cbranch_scc1 LtA5_%=\n" \
"global_load_dword v237, v224, s[58:59]\n" \
"s_add_u32 s61, s48, 49152\n" \
"s_mov_b32 exec_hi, 0\n" \
"s_nop 0\n" \
"buffer_load_dword v237, v224, %[srdp], s61 offen\n" \
"s_mov_b32 exec_hi, -1\n" \
"s_nop 0\n" \
"LtA5_%=:\n" \
"v_mfma_f32_16x16x32_bf16 v[144:147], a[96:99], a[32:35], v[168:171]\n" \
"v_exp_f32_e64 v140, v140\n" \
"v_mfma_f32_16x16x32_bf16 v[144:147], a[100:103], a[36:39], v[144:147]\n" \
"ds_read_b128 a[80:83], %[kb0] offset:53248\n" \
"v_mfma_f32_16x16x32_bf16 v[144:147], a[104:107], a[40:43], v[144:147]\n" \
"v_exp_f32_e64 v141, v141\n" \
"v_mfma_f32_16x16x32_bf16 v[144:147], a[108:111], a[44:47], v[144:147]\n" \
"v_cvt_pk_bf16_f32 v196, v136, v137\n" \
"v_cvt_pk_bf16_f32 v197, v138, v139\n" \
"v_mfma_f32_16x16x32_bf16 v[148:151], a[124:127], a[44:47], v[168:171]\n" \
"v_exp_f32_e64 v142, v142\n" \
"v_mfma_f32_16x16x32_bf16 v[148:151], a[120:123], a[40:43], v[148:151]\n" \
"v_mfma_f32_16x16x32_bf16 v[148:151], a[116:119], a[36:39], v[148:151]\n" \
"buffer_load_dwordx4 %[vd0], %[srdv], s48 offen offset:2048 lds\n" \
"v_mfma_f32_16x16x32_bf16 v[148:151], a[112:115], a[32:35], v[148:151]\n" \
"v_exp_f32_e64 v143, v143\n" \
"v_mfma_f32_16x16x32_bf16 v[96:99], a[160:163], v[204:207], v[96:99]\n" \
"ds_read_b64_tr_b16 a[160:161], %[vvb] offset:40960\n" \
"v_mfma_f32_16x16x32_bf16 v[96:99], a[128:131], v[220:223], v[96:99]\n" \
"v_exp_f32_e64 v144, v144\n" \
"v_mfma_f32_16x16x32_bf16 v[100:103], a[164:167], v[204:207], v[100:103]\n" \
"v_cvt_pk_bf16_f32 v198, v140, v141\n" \
"v_cvt_pk_bf16_f32 v199, v142, v143\n" \
"v_mfma_f32_16x16x32_bf16 v[100:103], a[132:135], v[220:223], v[100:103]\n" \
"v_exp_f32_e64 v145, v145\n" \
"v_mfma_f32_16x16x32_bf16 v[104:107], a[168:171], v[204:207], v[104:107]\n" \
"ds_read_b64_tr_b16 a[162:163], %[vvb] offset:45056\n" \
"ds_read_b64_tr_b16 a[164:165], v211 offset:40960\n" \
"v_mfma_f32_16x16x32_bf16 v[104:107], a[136:139], v[220:223], v[104:107]\n" \
"v_exp_f32_e64 v146, v146\n" \
"v_mfma_f32_16x16x32_bf16 v[108:111], a[172:175], v[204:207], v[108:111]\n" \
"ds_read_b64_tr_b16 a[166:167], v211 offset:45056\n" \
"v_mfma_f32_16x16x32_bf16 v[108:111], a[140:143], v[220:223], v[108:111]\n" \
"v_exp_f32_e64 v147, v147\n" \
"v_smfmac_f32_16x16x64_bf16 v[180:183], a[192:195], v[216:223], v228\n" \
"ds_read_b64_tr_b16 a[168:169], %[vvb] offset:41472\n" \
"v_mfma_f32_16x16x32_bf16 v[152:155], a[96:99], a[48:51], v[172:175]\n" \
"v_exp_f32_e64 v148, v148\n" \
"v_mfma_f32_16x16x32_bf16 v[152:155], a[100:103], a[52:55], v[152:155]\n" \
"ds_read_b64_tr_b16 a[170:171], %[vvb] offset:45568\n" \
"v_mfma_f32_16x16x32_bf16 v[152:155], a[104:107], a[56:59], v[152:155]\n" \
"v_exp_f32_e64 v149, v149\n" \
"v_mfma_f32_16x16x32_bf16 v[152:155], a[108:111], a[60:63], v[152:155]\n" \
"v_cvt_pk_bf16_f32 v200, v144, v145\n" \
"v_cvt_pk_bf16_f32 v201, v146, v147\n" \
"v_mfma_f32_16x16x32_bf16 v[156:159], a[124:127], a[60:63], v[172:175]\n" \
"v_exp_f32_e64 v150, v150\n" \
".if . - adaln_body != 16384\n" \
".error \"size model\"\n" \
".endif\n" \
"Lk3_%=:\n" \
"s_cbranch_execz Lk4_%=\n" \
"s_nop 0\n" \
"v_mfma_f32_16x16x32_bf16 v[156:159], a[120:123], a[56:59], v[156:159]\n" \
"ds_read_b64_tr_b16 a[172:173], v211 offset:41472\n" \
"v_mfma_f32_16x16x32_bf16 v[156:159], a[116:119], a[52:55], v[156:159]\n" \
"buffer_load_dwordx4 v215, %[srdv], s48 offen offset:3072 lds\n" \
"v_mfma_f32_16x16x32_bf16 v[156:159], a[112:115], a[48:51], v[156:159]\n" \
"v_exp_f32_e64 v151, v151\n" \
"v_mfma_f32_16x16x32_bf16 v[112:115], a[176:179], v[204:207], v[112:115]\n" \
"ds_read_b64_tr_b16 a[174:175], v211 offset:45568\n" \
"ds_read_b64_tr_b16 a[176:177], %[vvb] offset:41984\n" \
"v_mfma_f32_16x16x32_bf16 v[112:115], a[144:147], v[220:223], v[112:115]\n" \
"v_exp_f32_e64 v152, v152\n" \
"v_mfma_f32_16x16x32_bf16 v[116:119], a[180:183], v[204:207], v[116:119]\n" \
"v_cvt_pk_bf16_f32 v202, v148, v149\n" \
"v_cvt_pk_bf16_f32 v203, v150, v151\n" \
"v_mfma_f32_16x16x32_bf16 v[116:119], a[148:151], v[220:223], v[116:119]\n" \
"v_exp_f32_e64 v153, v153\n" \
"v_mfma_f32_16x16x32_bf16 v[120:123], a[184:187], v[204:207], v[120:123]\n" \
"v_exp_f32_e64 v154, v154\n" \
"v_mfma_f32_16x16x32_bf16 v[120:123], a[152:155], v[220:223], v[120:123]\n" \
"ds_read_b64_tr_b16 a[178:179], %[vvb] offset:46080\n" \
"v_mfma_f32_16x16x32_bf16 v[124:127], a[188:191], v[204:207], v[124:127]\n" \
"v_exp_f32_e64 v155, v155\n" \
"v_mfma_f32_16x16x32_bf16 v[124:127], a[156:159], v[220:223], v[124:127]\n" \
"ds_read_b64_tr_b16 a[180:181], v211 offset:41984\n" \
"s_waitcnt lgkmcnt(13)\n" \
"v_mfma_f32_16x16x32_bf16 v[128:131], a[64:67], a[0:3], v[160:163]\n" \
"v_exp_f32_e32 v156, v156\n" \
"v_mfma_f32_16x16x32_bf16 v[128:131], a[68:71], a[4:7], v[128:131]\n" \
"ds_read_b64_tr_b16 a[182:183], v211 offset:46080\n" \
"s_add_u32 m0, %[m0b], 32768\n" \
"v_mfma_f32_16x16x32_bf16 v[128:131], a[72:75], a[8:11], v[128:131]\n" \
"v_exp_f32_e64 v157, v157\n" \
"v_mfma_f32_16x16x32_bf16 v[128:131], a[76:79], a[12:15], v[128:131]\n" \
"v_cvt_pk_bf16_f32 v204, v152, v153\n" \
"v_cvt_pk_bf16_f32 v205, v154, v155\n" \
"v_mfma_f32_16x16x32_bf16 v[132:135], a[92:95], a[12:15], v[160:163]\n" \
"v_exp_f32_e64 v158, v158\n" \
"v_mfma_f32_16x16x32_bf16 v[132:135], a[88:91], a[8:11], v[132:135]\n" \
"ds_read_b64_tr_b16 a[184:185], %[vvb] offset:42496\n" \
"s_waitcnt lgkmcnt(5)\n" \
"v_mfma_f32_16x16x32_bf16 v[132:135], a[84:87], a[4:7], v[132:135]\n" \
"buffer_load_dwordx4 %[kd0], %[srdk], s48 offen offset:0 lds\n" \
"v_mfma_f32_16x16x32_bf16 v[132:135], a[80:83], a[0:3], v[132:135]\n" \
"v_exp_f32_e32 v159, v159\n" \
"v_mfma_f32_16x16x32_bf16 v[64:67], a[128:131], v[216:219], v[64:67]\n" \
"ds_read_b64_tr_b16 a[186:187], %[vvb] offset:46592\n" \
"ds_read_b64_tr_b16 a[188:189], v211 offset:42496\n" \
"v_mfma_f32_16x16x32_bf16 v[64:67], a[160:163], v[200:203], v[64:67]\n" \
"v_exp_f32_e64 v128, v128\n" \
"v_mfma_f32_16x16x32_bf16 v[68:71], a[132:135], v[216:219], v[68:71]\n" \
"v_cvt_pk_bf16_f32 v206, v156, v157\n" \
"v_cvt_pk_bf16_f32 v207, v158, v159\n" \
"v_mfma_f32_16x16x32_bf16 v[68:71], a[164:167], v[200:203], v[68:71]\n" \
"v_exp_f32_e64 v129, v129\n" \
"v_mfma_f32_16x16x32_bf16 v[72:75], a[136:139], v[216:219], v[72:75]\n" \
"ds_read_b64_tr_b16 a[190:191], v211 offset:46592\n" \
"ds_read_b64_tr_b16 a[128:129], %[vvb] offset:49152\n" \
"v_mfma_f32_16x16x32_bf16 v[72:75], a[168:171], v[200:203], v[72:75]\n" \
"v_exp_f32_e64 v130, v130\n" \
"v_mfma_f32_16x16x32_bf16 v[76:79], a[140:143], v[216:219], v[76:79]\n" \
"ds_read_b64_tr_b16 a[130:131], %[vvb] offset:53248\n" \
"ds_read_b64_tr_b16 a[132:133], v211 offset:49152\n" \
"v_mfma_f32_16x16x32_bf16 v[76:79], a[172:175], v[200:203], v[76:79]\n" \
"v_exp_f32_e64 v131, v131\n" \
"v_smfmac_f32_16x16x64_bf16 v[176:179], a[192:195], v[192:199], v228\n" \
"ds_read_b64_tr_b16 a[134:135], v211 offset:53248\n" \
"ds_read_b64_tr_b16 a[136:137], %[vvb] offset:49664\n" \
"v_mfma_f32_16x16x32_bf16 v[136:139], a[64:67], a[16:19], v[164:167]\n" \
"v_exp_f32_e64 v132, v132\n" \
"v_mfma_f32_16x16x32_bf16 v[136:139], a[68:71], a[20:23], v[136:139]\n" \
"ds_read_b64_tr_b16 a[138:139], %[vvb] offset:53760\n" \
"ds_read_b64_tr_b16 a[140:141], v211 offset:49664\n" \
"v_mfma_f32_16x16x32_bf16 v[136:139], a[72:75], a[24:27], v[136:139]\n" \
"v_exp_f32_e64 v133, v133\n" \
"v_mfma_f32_16x16x32_bf16 v[136:139], a[76:79], a[28:31], v[136:139]\n" \
"v_cvt_pk_bf16_f32 v184, v128, v129\n" \
"v_cvt_pk_bf16_f32 v185, v130, v131\n" \
"v_mfma_f32_16x16x32_bf16 v[140:143], a[92:95], a[28:31], v[164:167]\n" \
"v_exp_f32_e64 v134, v134\n" \
"v_mfma_f32_16x16x32_bf16 v[140:143], a[88:91], a[24:27], v[140:143]\n" \
"ds_read_b64_tr_b16 a[142:143], v211 offset:53760\n" \
"s_waitcnt lgkmcnt(8)\n" \
"v_mfma_f32_16x16x32_bf16 v[140:143], a[84:87], a[20:23], v[140:143]\n" \
"buffer_load_dwordx4 v212, %[srdk], s48 offen offset:1024 lds\n" \
"v_mfma_f32_16x16x32_bf16 v[140:143], a[80:83], a[16:19], v[140:143]\n" \
"v_exp_f32_e32 v135, v135\n" \
"v_mfma_f32_16x16x32_bf16 v[80:83], a[144:147], v[216:219], v[80:83]\n" \
"ds_read_b64_tr_b16 a[144:145], %[vvb] offset:50176\n" \
"ds_read_b64_tr_b16 a[146:147], %[vvb] offset:54272\n" \
"v_mfma_f32_16x16x32_bf16 v[80:83], a[176:179], v[200:203], v[80:83]\n" \
"v_exp_f32_e64 v136, v136\n" \
"v_mfma_f32_16x16x32_bf16 v[84:87], a[148:151], v[216:219], v[84:87]\n" \
"v_cvt_pk_bf16_f32 v186, v132, v133\n" \
"v_cvt_pk_bf16_f32 v187, v134, v135\n" \
"v_mfma_f32_16x16x32_bf16 v[84:87], a[180:183], v[200:203], v[84:87]\n" \
"v_exp_f32_e64 v137, v137\n" \
"v_mfma_f32_16x16x32_bf16 v[88:91], a[152:155], v[216:219], v[88:91]\n" \
"v_exp_f32_e64 v138, v138\n" \
"v_mfma_f32_16x16x32_bf16 v[88:91], a[184:187], v[200:203], v[88:91]\n" \
"ds_read_b64_tr_b16 a[148:149], v211 offset:50176\n" \
"ds_read_b64_tr_b16 a[150:151], v211 offset:54272\n" \
"v_mfma_f32_16x16x32_bf16 v[92:95], a[156:159], v[216:219], v[92:95]\n" \
"v_exp_f32_e64 v139, v139\n" \
"v_mfma_f32_16x16x32_bf16 v[92:95], a[188:191], v[200:203], v[92:95]\n" \
"ds_read_b64_tr_b16 a[152:153], %[vvb] offset:50688\n" \
"global_load_dword v237, %[vct], %[pct]\n" \
"v_mfma_f32_16x16x32_bf16 v[144:147], a[64:67], a[32:35], v[168:171]\n" \
"v_exp_f32_e64 v140, v140\n" \
"v_mfma_f32_16x16x32_bf16 v[144:147], a[68:71], a[36:39], v[144:147]\n" \
"ds_read_b64_tr_b16 a[154:155], %[vvb] offset:54784\n" \
"ds_read_b64_tr_b16 a[156:157], v211 offset:50688\n" \
"v_mfma_f32_16x16x32_bf16 v[144:147], a[72:75], a[40:43], v[144:147]\n" \
"v_exp_f32_e64 v141, v141\n" \
"v_mfma_f32_16x16x32_bf16 v[144:147], a[76:79], a[44:47], v[144:147]\n" \
"v_cvt_pk_bf16_f32 v188, v136, v137\n" \
"v_cvt_pk_bf16_f32 v189, v138, v139\n" \
"v_mfma_f32_16x16x32_bf16 v[148:151], a[92:95], a[44:47], v[168:171]\n" \
"v_exp_f32_e64 v142, v142\n" \
"v_mfma_f32_16x16x32_bf16 v[148:151], a[88:91], a[40:43], v[148:151]\n" \
"ds_read_b64_tr_b16 a[158:159], v211 offset:54784\n" \
"s_waitcnt lgkmcnt(8)\n" \
"v_mfma_f32_16x16x32_bf16 v[148:151], a[84:87], a[36:39], v[148:151]\n" \
"buffer_load_dwordx4 v213, %[srdk], s48 offen offset:2048 lds\n" \
"v_mfma_f32_16x16x32_bf16 v[148:151], a[80:83], a[32:35], v[148:151]\n" \
"v_exp_f32_e32 v143, v143\n" \
"v_mfma_f32_16x16x32_bf16 v[0:3], a[160:163], v[192:195], v[0:3]\n" \
"ds_read_b128 a[96:99], %[kb0] offset:57344\n" \
"v_mfma_f32_16x16x32_bf16 v[0:3], a[128:131], v[184:187], v[0:3]\n" \
"v_exp_f32_e64 v144, v144\n" \
"v_mfma_f32_16x16x32_bf16 v[4:7], a[164:167], v[192:195], v[4:7]\n" \
"v_cvt_pk_bf16_f32 v190, v140, v141\n" \
"v_cvt_pk_bf16_f32 v191, v142, v143\n" \
"v_mfma_f32_16x16x32_bf16 v[4:7], a[132:135], v[184:187], v[4:7]\n" \
"v_exp_f32_e64 v145, v145\n" \
"v_mfma_f32_16x16x32_bf16 v[8:11], a[168:171], v[192:195], v[8:11]\n" \
"ds_read_b128 a[100:103], v208 offset:57344\n" \
"v_mfma_f32_16x16x32_bf16 v[8:11], a[136:139], v[184:187], v[8:11]\n" \
"v_exp_f32_e64 v146, v146\n" \
"v_mfma_f32_16x16x32_bf16 v[12:15], a[172:175], v[192:195], v[12:15]\n" \
"ds_read_b128 a[104:107], v209 offset:57344\n" \
"v_mfma_f32_16x16x32_bf16 v[12:15], a[140:143], v[184:187], v[12:15]\n" \
"v_exp_f32_e64 v147, v147\n" \
"v_smfmac_f32_16x16x64_bf16 v[180:183], a[192:195], v[200:207], v228\n" \
"ds_read_b128 a[108:111], v210 offset:57344\n" \
"v_mfma_f32_16x16x32_bf16 v[152:155], a[64:67], a[48:51], v[172:175]\n" \
"v_exp_f32_e64 v148, v148\n" \
"v_mfma_f32_16x16x32_bf16 v[152:155], a[68:71], a[52:55], v[152:155]\n" \
"ds_read_b128 a[124:127], v210 offset:61440\n" \
"v_mfma_f32_16x16x32_bf16 v[152:155], a[72:75], a[56:59], v[152:155]\n" \
"v_exp_f32_e64 v149, v149\n" \
"v_mfma_f32_16x16x32_bf16 v[152:155], a[76:79], a[60:63], v[152:155]\n" \
"v_cvt_pk_bf16_f32 v216, v144, v145\n" \
"v_cvt_pk_bf16_f32 v217, v146, v147\n" \
"v_mfma_f32_16x16x32_bf16 v[156:159], a[92:95], a[60:63], v[172:175]\n" \
"v_exp_f32_e64 v150, v150\n" \
"v_mfma_f32_16x16x32_bf16 v[156:159], a[88:91], a[56:59], v[156:159]\n" \
"ds_read_b128 a[120:123], v209 offset:61440\n" \
"s_waitcnt lgkmcnt(6)\n" \
"v_mfma_f32_16x16x32_bf16 v[156:159], a[84:87], a[52:55], v[156:159]\n" \
"buffer_load_dwordx4 v214, %[srdk], s48 offen offset:3072 lds\n" \
"v_mfma_f32_16x16x32_bf16 v[156:159], a[80:83], a[48:51], v[156:159]\n" \
"v_exp_f32_e32 v151, v151\n" \
"v_mfma_f32_16x16x32_bf16 v[16:19], a[176:179], v[192:195], v[16:19]\n" \
"ds_read_b128 a[116:119], v208 offset:61440\n" \
"v_mfma_f32_16x16x32_bf16 v[16:19], a[144:147], v[184:187], v[16:19]\n" \
"v_exp_f32_e64 v152, v152\n" \
"v_mfma_f32_16x16x32_bf16 v[20:23], a[180:183], v[192:195], v[20:23]\n" \
"v_cvt_pk_bf16_f32 v218, v148, v149\n" \
"v_cvt_pk_bf16_f32 v219, v150, v151\n" \
"v_mfma_f32_16x16x32_bf16 v[20:23], a[148:151], v[184:187], v[20:23]\n" \
"v_exp_f32_e64 v153, v153\n" \
"v_mfma_f32_16x16x32_bf16 v[24:27], a[184:187], v[192:195], v[24:27]\n" \
"v_exp_f32_e64 v154, v154\n" \
"v_mfma_f32_16x16x32_bf16 v[24:27], a[152:155], v[184:187], v[24:27]\n" \
"ds_read_b128 a[112:115], %[kb0] offset:61440\n" \
"s_add_u32 s48, s48, 0x4000\n" \
"s_mov_b32 m0, s44\n" \
"s_addk_i32 s44, 0x400\n" \
"buffer_load_dwordx4 v229, s[64:67], s45 offen offset:0 nt lds\n" \
"s_add_u32 s45, s45, 393216\n" \
"v_mfma_f32_16x16x32_bf16 v[28:31], a[188:191], v[192:195], v[28:31]\n" \
"v_exp_f32_e64 v155, v155\n" \
"v_mfma_f32_16x16x32_bf16 v[28:31], a[156:159], v[184:187], v[28:31]\n" \
"s_waitcnt vmcnt(15) lgkmcnt(2)\n" \
"s_barrier\n" \
"v_mfma_f32_16x16x32_bf16 v[128:131], a[96:99], a[0:3], v[160:163]\n" \
"v_exp_f32_e64 v156, v156\n" \
"v_mfma_f32_16x16x32_bf16 v[128:131], a[100:103], a[4:7], v[128:131]\n" \
"s_add_u32 m0, %[m0b], 98304\n" \
"v_mfma_f32_16x16x32_bf16 v[128:131], a[104:107], a[8:11], v[128:131]\n" \
"v_exp_f32_e64 v157, v157\n" \
"v_mfma_f32_16x16x32_bf16 v[128:131], a[108:111], a[12:15], v[128:131]\n" \
"v_cvt_pk_bf16_f32 v220, v152, v153\n" \
"v_cvt_pk_bf16_f32 v221, v154, v155\n" \
"v_mfma_f32_16x16x32_bf16 v[132:135], a[124:127], a[12:15], v[160:163]\n" \
"v_exp_f32_e64 v158, v158\n" \
"v_mfma_f32_16x16x32_bf16 v[132:135], a[120:123], a[8:11], v[132:135]\n" \
"s_waitcnt lgkmcnt(0)\n" \
"v_mfma_f32_16x16x32_bf16 v[132:135], a[116:119], a[4:7], v[132:135]\n" \
"buffer_load_dwordx4 %[vd0], %[srdv], s48 offen offset:0 lds\n" \
"v_mfma_f32_16x16x32_bf16 v[132:135], a[112:115], a[0:3], v[132:135]\n" \
"v_exp_f32_e32 v159, v159\n" \
"v_mfma_f32_16x16x32_bf16 v[32:35], a[160:163], v[196:199], v[32:35]\n" \
"ds_read_b128 a[64:67], %[kb0] offset:0\n" \
"v_mfma_f32_16x16x32_bf16 v[32:35], a[128:131], v[188:191], v[32:35]\n" \
"v_exp_f32_e64 v128, v128\n" \
"v_mfma_f32_16x16x32_bf16 v[36:39], a[164:167], v[196:199], v[36:39]\n" \
"v_cvt_pk_bf16_f32 v222, v156, v157\n" \
"v_cvt_pk_bf16_f32 v223, v158, v159\n" \
"v_mfma_f32_16x16x32_bf16 v[36:39], a[132:135], v[188:191], v[36:39]\n" \
"v_exp_f32_e64 v129, v129\n" \
"v_mfma_f32_16x16x32_bf16 v[40:43], a[168:171], v[196:199], v[40:43]\n" \
"ds_read_b128 a[68:71], v208 offset:0\n" \
"v_mfma_f32_16x16x32_bf16 v[40:43], a[136:139], v[188:191], v[40:43]\n" \
"v_exp_f32_e64 v130, v130\n" \
"v_mfma_f32_16x16x32_bf16 v[44:47], a[172:175], v[196:199], v[44:47]\n" \
"ds_read_b128 a[72:75], v209 offset:0\n" \
"v_mfma_f32_16x16x32_bf16 v[44:47], a[140:143], v[188:191], v[44:47]\n" \
"v_exp_f32_e64 v131, v131\n" \
"v_smfmac_f32_16x16x64_bf16 v[176:179], a[192:195], v[184:191], v228\n" \
"v_mfma_f32_16x16x32_bf16 v[136:139], a[96:99], a[16:19], v[164:167]\n" \
"v_exp_f32_e64 v132, v132\n" \
"v_mfma_f32_16x16x32_bf16 v[136:139], a[100:103], a[20:23], v[136:139]\n" \
"ds_read_b128 a[76:79], v210 offset:0\n" \
"v_mfma_f32_16x16x32_bf16 v[136:139], a[104:107], a[24:27], v[136:139]\n" \
"v_exp_f32_e64 v133, v133\n" \
"v_mfma_f32_16x16x32_bf16 v[136:139], a[108:111], a[28:31], v[136:139]\n" \
"v_cvt_pk_bf16_f32 v192, v128, v129\n" \
"v_cvt_pk_bf16_f32 v193, v130, v131\n" \
"v_mfma_f32_16x16x32_bf16 v[140:143], a[124:127], a[28:31], v[164:167]\n" \
"v_exp_f32_e64 v134, v134\n" \
"v_mfma_f32_16x16x32_bf16 v[140:143], a[120:123], a[24:27], v[140:143]\n" \
"ds_read_b128 a[92:95], v210 offset:4096\n" \
"v_mfma_f32_16x16x32_bf16 v[140:143], a[116:119], a[20:23], v[140:143]\n" \
"buffer_load_dwordx4 v215, %[srdv], s48 offen offset:1024 lds\n" \
"v_mfma_f32_16x16x32_bf16 v[140:143], a[112:115], a[16:19], v[140:143]\n" \
"v_exp_f32_e64 v135, v135\n" \
"v_mfma_f32_16x16x32_bf16 v[48:51], a[176:179], v[196:199], v[48:51]\n" \
"ds_read_b128 a[88:91], v209 offset:4096\n" \
"v_mfma_f32_16x16x32_bf16 v[48:51], a[144:147], v[188:191], v[48:51]\n" \
"v_exp_f32_e64 v136, v136\n" \
"v_mfma_f32_16x16x32_bf16 v[52:55], a[180:183], v[196:199], v[52:55]\n" \
"v_cvt_pk_bf16_f32 v194, v132, v133\n" \
"v_cvt_pk_bf16_f32 v195, v134, v135\n" \
"v_mfma_f32_16x16x32_bf16 v[52:55], a[148:151], v[188:191], v[52:55]\n" \
"v_exp_f32_e64 v137, v137\n" \
"v_mfma_f32_16x16x32_bf16 v[56:59], a[184:187], v[196:199], v[56:59]\n" \
"v_exp_f32_e64 v138, v138\n" \
"v_mfma_f32_16x16x32_bf16 v[56:59], a[152:155], v[188:191], v[56:59]\n" \
"ds_read_b128 a[84:87], v208 offset:4096\n" \
"v_mfma_f32_16x16x32_bf16 v[60:63], a[188:191], v[196:199], v[60:63]\n" \
"v_exp_f32_e64 v139, v139\n" \
"v_mfma_f32_16x16x32_bf16 v[60:63], a[156:159], v[188:191], v[60:63]\n" \
"s_cmp_lg_u32 %[wid], 2\n" \
"s_cbranch_scc1 LtA7_%=\n" \
"global_load_dword v237, v224, s[58:59]\n" \
"s_add_u32 s61, s48, 49152\n" \
"s_mov_b32 exec_hi, 0\n" \
"s_nop 0\n" \
"buffer_load_dword v237, v224, %[srdp], s61 offen\n" \
"s_mov_b32 exec_hi, -1\n" \
"s_nop 0\n" \
"LtA7_%=:\n" \
"v_mfma_f32_16x16x32_bf16 v[144:147], a[96:99], a[32:35], v[168:171]\n" \
"v_exp_f32_e64 v140, v140\n" \
"v_mfma_f32_16x16x32_bf16 v[144:147], a[100:103], a[36:39], v[144:147]\n" \
"ds_read_b128 a[80:83], %[kb0] offset:4096\n" \
"v_mfma_f32_16x16x32_bf16 v[144:147], a[104:107], a[40:43], v[144:147]\n" \
"v_exp_f32_e64 v141, v141\n" \
"v_mfma_f32_16x16x32_bf16 v[144:147], a[108:111], a[44:47], v[144:147]\n" \
"v_cvt_pk_bf16_f32 v196, v136, v137\n" \
"v_cvt_pk_bf16_f32 v197, v138, v139\n" \
"v_mfma_f32_16x16x32_bf16 v[148:151], a[124:127], a[44:47], v[168:171]\n" \
"v_exp_f32_e64 v142, v142\n" \
"v_mfma_f32_16x16x32_bf16 v[148:151], a[120:123], a[40:43], v[148:151]\n" \
"v_mfma_f32_16x16x32_bf16 v[148:151], a[116:119], a[36:39], v[148:151]\n" \
"buffer_load_dwordx4 %[vd0], %[srdv], s48 offen offset:2048 lds\n" \
"v_mfma_f32_16x16x32_bf16 v[148:151], a[112:115], a[32:35], v[148:151]\n" \
"v_exp_f32_e64 v143, v143\n" \
"v_mfma_f32_16x16x32_bf16 v[96:99], a[160:163], v[204:207], v[96:99]\n" \
"ds_read_b64_tr_b16 a[160:161], %[vvb] offset:57344\n" \
"v_mfma_f32_16x16x32_bf16 v[96:99], a[128:131], v[220:223], v[96:99]\n" \
"v_exp_f32_e64 v144, v144\n" \
"v_mfma_f32_16x16x32_bf16 v[100:103], a[164:167], v[204:207], v[100:103]\n" \
"v_cvt_pk_bf16_f32 v198, v140, v141\n" \
"v_cvt_pk_bf16_f32 v199, v142, v143\n" \
"v_mfma_f32_16x16x32_bf16 v[100:103], a[132:135], v[220:223], v[100:103]\n" \
"v_exp_f32_e64 v145, v145\n" \
"v_mfma_f32_16x16x32_bf16 v[104:107], a[168:171], v[204:207], v[104:107]\n" \
"ds_read_b64_tr_b16 a[162:163], %[vvb] offset:61440\n" \
"ds_read_b64_tr_b16 a[164:165], v211 offset:57344\n" \
"v_mfma_f32_16x16x32_bf16 v[104:107], a[136:139], v[220:223], v[104:107]\n" \
"v_exp_f32_e64 v146, v146\n" \
"v_mfma_f32_16x16x32_bf16 v[108:111], a[172:175], v[204:207], v[108:111]\n" \
"ds_read_b64_tr_b16 a[166:167], v211 offset:61440\n" \
"v_mfma_f32_16x16x32_bf16 v[108:111], a[140:143], v[220:223], v[108:111]\n" \
"v_exp_f32_e64 v147, v147\n" \
"v_smfmac_f32_16x16x64_bf16 v[180:183], a[192:195], v[216:223], v228\n" \
"ds_read_b64_tr_b16 a[168:169], %[vvb] offset:57856\n" \
"v_mfma_f32_16x16x32_bf16 v[152:155], a[96:99], a[48:51], v[172:175]\n" \
"v_exp_f32_e64 v148, v148\n" \
"v_mfma_f32_16x16x32_bf16 v[152:155], a[100:103], a[52:55], v[152:155]\n" \
"ds_read_b64_tr_b16 a[170:171], %[vvb] offset:61952\n" \
"v_mfma_f32_16x16x32_bf16 v[152:155], a[104:107], a[56:59], v[152:155]\n" \
"v_exp_f32_e64 v149, v149\n" \
"v_mfma_f32_16x16x32_bf16 v[152:155], a[108:111], a[60:63], v[152:155]\n" \
"v_cvt_pk_bf16_f32 v200, v144, v145\n" \
"v_cvt_pk_bf16_f32 v201, v146, v147\n" \
"v_mfma_f32_16x16x32_bf16 v[156:159], a[124:127], a[60:63], v[172:175]\n" \
"v_exp_f32_e64 v150, v150\n" \
"v_mfma_f32_16x16x32_bf16 v[156:159], a[120:123], a[56:59], v[156:159]\n" \
"ds_read_b64_tr_b16 a[172:173], v211 offset:57856\n" \
"v_mfma_f32_16x16x32_bf16 v[156:159], a[116:119], a[52:55], v[156:159]\n" \
"buffer_load_dwordx4 v215, %[srdv], s48 offen offset:3072 lds\n" \
"v_mfma_f32_16x16x32_bf16 v[156:159], a[112:115], a[48:51], v[156:159]\n" \
"v_exp_f32_e64 v151, v151\n" \
"v_mfma_f32_16x16x32_bf16 v[112:115], a[176:179], v[204:207], v[112:115]\n" \
"ds_read_b64_tr_b16 a[174:175], v211 offset:61952\n" \
"ds_read_b64_tr_b16 a[176:177], %[vvb] offset:58368\n" \
"v_mfma_f32_16x16x32_bf16 v[112:115], a[144:147], v[220:223], v[112:115]\n" \
"v_exp_f32_e64 v152, v152\n" \
"v_mfma_f32_16x16x32_bf16 v[116:119], a[180:183], v[204:207], v[116:119]\n" \
"v_cvt_pk_bf16_f32 v202, v148, v149\n" \
"v_cvt_pk_bf16_f32 v203, v150, v151\n" \
"v_mfma_f32_16x16x32_bf16 v[116:119], a[148:151], v[220:223], v[116:119]\n" \
"v_exp_f32_e64 v153, v153\n" \
"v_mfma_f32_16x16x32_bf16 v[120:123], a[184:187], v[204:207], v[120:123]\n" \
"v_exp_f32_e64 v154, v154\n" \
"v_mfma_f32_16x16x32_bf16 v[120:123], a[152:155], v[220:223], v[120:123]\n" \
"ds_read_b64_tr_b16 a[178:179], %[vvb] offset:62464\n" \
"v_mfma_f32_16x16x32_bf16 v[124:127], a[188:191], v[204:207], v[124:127]\n" \
"v_exp_f32_e64 v155, v155\n" \
"v_mfma_f32_16x16x32_bf16 v[124:127], a[156:159], v[220:223], v[124:127]\n" \
"ds_read_b64_tr_b16 a[180:181], v211 offset:58368\n" \
"s_waitcnt lgkmcnt(13)\n" \
"v_mfma_f32_16x16x32_bf16 v[128:131], a[64:67], a[0:3], v[160:163]\n" \
"v_exp_f32_e32 v156, v156\n" \
"v_mfma_f32_16x16x32_bf16 v[128:131], a[68:71], a[4:7], v[128:131]\n" \
"ds_read_b64_tr_b16 a[182:183], v211 offset:62464\n" \
"s_add_u32 m0, %[m0b], 49152\n" \
"v_mfma_f32_16x16x32_bf16 v[128:131], a[72:75], a[8:11], v[128:131]\n" \
"v_exp_f32_e64 v157, v157\n" \
"v_mfma_f32_16x16x32_bf16 v[128:131], a[76:79], a[12:15], v[128:131]\n" \
"v_cvt_pk_bf16_f32 v204, v152, v153\n" \
"v_cvt_pk_bf16_f32 v205, v154, v155\n" \
"v_mfma_f32_16x16x32_bf16 v[132:135], a[92:95], a[12:15], v[160:163]\n" \
"v_exp_f32_e64 v158, v158\n" \
"v_mfma_f32_16x16x32_bf16 v[132:135], a[88:91], a[8:11], v[132:135]\n" \
"ds_read_b64_tr_b16 a[184:185], %[vvb] offset:58880\n" \
"s_waitcnt lgkmcnt(5)\n" \
"v_mfma_f32_16x16x32_bf16 v[132:135], a[84:87], a[4:7], v[132:135]\n" \
"buffer_load_dwordx4 %[kd0], %[srdk], s48 offen offset:0 lds\n" \
"v_mfma_f32_16x16x32_bf16 v[132:135], a[80:83], a[0:3], v[132:135]\n" \
"v_exp_f32_e32 v159, v159\n" \
"v_mfma_f32_16x16x32_bf16 v[64:67], a[128:131], v[216:219], v[64:67]\n" \
"ds_read_b64_tr_b16 a[186:187], %[vvb] offset:62976\n" \
"ds_read_b64_tr_b16 a[188:189], v211 offset:58880\n" \
"v_mfma_f32_16x16x32_bf16 v[64:67], a[160:163], v[200:203], v[64:67]\n" \
"v_exp_f32_e64 v128, v128\n" \
"v_mfma_f32_16x16x32_bf16 v[68:71], a[132:135], v[216:219], v[68:71]\n" \
"v_cvt_pk_bf16_f32 v206, v156, v157\n" \
"v_cvt_pk_bf16_f32 v207, v158, v159\n" \
"v_mfma_f32_16x16x32_bf16 v[68:71], a[164:167], v[200:203], v[68:71]\n" \
"v_exp_f32_e64 v129, v129\n" \
"v_mfma_f32_16x16x32_bf16 v[72:75], a[136:139], v[216:219], v[72:75]\n" \
"ds_read_b64_tr_b16 a[190:191], v211 offset:62976\n" \
"ds_read_b64_tr_b16 a[128:129], %[vvb] offset:0\n" \
"v_mfma_f32_16x16x32_bf16 v[72:75], a[168:171], v[200:203], v[72:75]\n" \
"v_exp_f32_e64 v130, v130\n" \
"v_mfma_f32_16x16x32_bf16 v[76:79], a[140:143], v[216:219], v[76:79]\n" \
"ds_read_b64_tr_b16 a[130:131], %[vvb] offset:4096\n" \
"ds_read_b64_tr_b16 a[132:133], v211 offset:0\n" \
"v_mfma_f32_16x16x32_bf16 v[76:79], a[172:175], v[200:203], v[76:79]\n" \
"v_exp_f32_e64 v131, v131\n" \
"v_smfmac_f32_16x16x64_bf16 v[176:179], a[192:195], v[192:199], v228\n" \
"ds_read_b64_tr_b16 a[134:135], v211 offset:4096\n" \
"ds_read_b64_tr_b16 a[136:137], %[vvb] offset:512\n" \
"v_mfma_f32_16x16x32_bf16 v[136:139], a[64:67], a[16:19], v[164:167]\n" \
"v_exp_f32_e64 v132, v132\n" \
"v_mfma_f32_16x16x32_bf16 v[136:139], a[68:71], a[20:23], v[136:139]\n" \
"ds_read_b64_tr_b16 a[138:139], %[vvb] offset:4608\n" \
"ds_read_b64_tr_b16 a[140:141], v211 offset:512\n" \
"v_mfma_f32_16x16x32_bf16 v[136:139], a[72:75], a[24:27], v[136:139]\n" \
"v_exp_f32_e64 v133, v133\n" \
"v_mfma_f32_16x16x32_bf16 v[136:139], a[76:79], a[28:31], v[136:139]\n" \
"v_cvt_pk_bf16_f32 v184, v128, v129\n" \
"v_cvt_pk_bf16_f32 v185, v130, v131\n" \
"v_mfma_f32_16x16x32_bf16 v[140:143], a[92:95], a[28:31], v[164:167]\n" \
"v_exp_f32_e64 v134, v134\n" \
"v_mfma_f32_16x16x32_bf16 v[140:143], a[88:91], a[24:27], v[140:143]\n" \
"ds_read_b64_tr_b16 a[142:143], v211 offset:4608\n" \
"s_waitcnt lgkmcnt(8)\n" \
"v_mfma_f32_16x16x32_bf16 v[140:143], a[84:87], a[20:23], v[140:143]\n" \
"buffer_load_dwordx4 v212, %[srdk], s48 offen offset:1024 lds\n" \
"v_mfma_f32_16x16x32_bf16 v[140:143], a[80:83], a[16:19], v[140:143]\n" \
"v_exp_f32_e32 v135, v135\n" \
"v_mfma_f32_16x16x32_bf16 v[80:83], a[144:147], v[216:219], v[80:83]\n" \
"ds_read_b64_tr_b16 a[144:145], %[vvb] offset:1024\n" \
"ds_read_b64_tr_b16 a[146:147], %[vvb] offset:5120\n" \
"v_mfma_f32_16x16x32_bf16 v[80:83], a[176:179], v[200:203], v[80:83]\n" \
"v_exp_f32_e64 v136, v136\n" \
"v_mfma_f32_16x16x32_bf16 v[84:87], a[148:151], v[216:219], v[84:87]\n" \
"v_cvt_pk_bf16_f32 v186, v132, v133\n" \
"v_cvt_pk_bf16_f32 v187, v134, v135\n" \
"v_mfma_f32_16x16x32_bf16 v[84:87], a[180:183], v[200:203], v[84:87]\n" \
"v_exp_f32_e64 v137, v137\n" \
"v_mfma_f32_16x16x32_bf16 v[88:91], a[152:155], v[216:219], v[88:91]\n" \
"v_exp_f32_e64 v138, v138\n" \
"v_mfma_f32_16x16x32_bf16 v[88:91], a[184:187], v[200:203], v[88:91]\n" \
"ds_read_b64_tr_b16 a[148:149], v211 offset:1024\n" \
"ds_read_b64_tr_b16 a[150:151], v211 offset:5120\n" \
"v_mfma_f32_16x16x32_bf16 v[92:95], a[156:159], v[216:219], v[92:95]\n" \
"v_exp_f32_e64 v139, v139\n" \
"v_mfma_f32_16x16x32_bf16 v[92:95], a[188:191], v[200:203], v[92:95]\n" \
"ds_read_b64_tr_b16 a[152:153], %[vvb] offset:1536\n" \
"buffer_store_dwordx4 a[196:199], v235, %[srdz], s62 offen offset:1024 sc1\n" \
"s_add_u32 s62, s62, 2048\n" \
"v_mfma_f32_16x16x32_bf16 v[144:147], a[64:67], a[32:35], v[168:171]\n" \
"v_exp_f32_e64 v140, v140\n" \
"v_mfma_f32_16x16x32_bf16 v[144:147], a[68:71], a[36:39], v[144:147]\n" \
"ds_read_b64_tr_b16 a[154:155], %[vvb] offset:5632\n" \
"ds_read_b64_tr_b16 a[156:157], v211 offset:1536\n" \
"v_mfma_f32_16x16x32_bf16 v[144:147], a[72:75], a[40:43], v[144:147]\n" \
"v_exp_f32_e64 v141, v141\n" \
"v_mfma_f32_16x16x32_bf16 v[144:147], a[76:79], a[44:47], v[144:147]\n" \
"v_cvt_pk_bf16_f32 v188, v136, v137\n" \
"v_cvt_pk_bf16_f32 v189, v138, v139\n" \
"v_mfma_f32_16x16x32_bf16 v[148:151], a[92:95], a[44:47], v[168:171]\n" \
"v_exp_f32_e64 v142, v142\n" \
"v_mfma_f32_16x16x32_bf16 v[148:151], a[88:91], a[40:43], v[148:151]\n" \
"ds_read_b64_tr_b16 a[158:159], v211 offset:5632\n" \
"s_waitcnt lgkmcnt(8)\n" \
"v_mfma_f32_16x16x32_bf16 v[148:151], a[84:87], a[36:39], v[148:151]\n" \
"buffer_load_dwordx4 v213, %[srdk], s48 offen offset:2048 lds\n" \
"v_mfma_f32_16x16x32_bf16 v[148:151], a[80:83], a[32:35], v[148:151]\n" \
"v_exp_f32_e32 v143, v143\n" \
"v_mfma_f32_16x16x32_bf16 v[0:3], a[160:163], v[192:195], v[0:3]\n" \
"ds_read_b128 a[96:99], %[kb0] offset:8192\n" \
"v_mfma_f32_16x16x32_bf16 v[0:3], a[128:131], v[184:187], v[0:3]\n" \
"v_exp_f32_e64 v144, v144\n" \
"v_mfma_f32_16x16x32_bf16 v[4:7], a[164:167], v[192:195], v[4:7]\n" \
"v_cvt_pk_bf16_f32 v190, v140, v141\n" \
"v_cvt_pk_bf16_f32 v191, v142, v143\n" \
"v_mfma_f32_16x16x32_bf16 v[4:7], a[132:135], v[184:187], v[4:7]\n" \
"v_exp_f32_e64 v145, v145\n" \
"v_mfma_f32_16x16x32_bf16 v[8:11], a[168:171], v[192:195], v[8:11]\n" \
"ds_read_b128 a[100:103], v208 offset:8192\n" \
"v_mfma_f32_16x16x32_bf16 v[8:11], a[136:139], v[184:187], v[8:11]\n" \
"v_exp_f32_e64 v146, v146\n" \
"v_mfma_f32_16x16x32_bf16 v[12:15], a[172:175], v[192:195], v[12:15]\n" \
"ds_read_b128 a[104:107], v209 offset:8192\n" \
"v_mfma_f32_16x16x32_bf16 v[12:15], a[140:143], v[184:187], v[12:15]\n" \
"v_exp_f32_e64 v147, v147\n" \
"v_smfmac_f32_16x16x64_bf16 v[180:183], a[192:195], v[200:207], v228\n" \
"ds_read_b128 a[108:111], v210 offset:8192\n" \
"v_mfma_f32_16x16x32_bf16 v[152:155], a[64:67], a[48:51], v[172:175]\n" \
"v_exp_f32_e64 v148, v148\n" \
"v_mfma_f32_16x16x32_bf16 v[152:155], a[68:71], a[52:55], v[152:155]\n" \
"ds_read_b128 a[124:127], v210 offset:12288\n" \
"v_mfma_f32_16x16x32_bf16 v[152:155], a[72:75], a[56:59], v[152:155]\n" \
"v_exp_f32_e64 v149, v149\n" \
"v_mfma_f32_16x16x32_bf16 v[152:155], a[76:79], a[60:63], v[152:155]\n" \
"v_cvt_pk_bf16_f32 v216, v144, v145\n" \
"v_cvt_pk_bf16_f32 v217, v146, v147\n" \
"v_mfma_f32_16x16x32_bf16 v[156:159], a[92:95], a[60:63], v[172:175]\n" \
"v_exp_f32_e64 v150, v150\n" \
"v_mfma_f32_16x16x32_bf16 v[156:159], a[88:91], a[56:59], v[156:159]\n" \
"ds_read_b128 a[120:123], v209 offset:12288\n" \
"s_waitcnt lgkmcnt(6)\n" \
"v_mfma_f32_16x16x32_bf16 v[156:159], a[84:87], a[52:55], v[156:159]\n" \
"buffer_load_dwordx4 v214, %[srdk], s48 offen offset:3072 lds\n" \
"v_mfma_f32_16x16x32_bf16 v[156:159], a[80:83], a[48:51], v[156:159]\n" \
"v_exp_f32_e32 v151, v151\n" \
"v_mfma_f32_16x16x32_bf16 v[16:19], a[176:179], v[192:195], v[16:19]\n" \
"ds_read_b128 a[116:119], v208 offset:12288\n" \
"v_mfma_f32_16x16x32_bf16 v[16:19], a[144:147], v[184:187], v[16:19]\n" \
"v_exp_f32_e64 v152, v152\n" \
"v_mfma_f32_16x16x32_bf16 v[20:23], a[180:183], v[192:195], v[20:23]\n" \
"v_cvt_pk_bf16_f32 v218, v148, v149\n" \
"v_cvt_pk_bf16_f32 v219, v150, v151\n" \
"v_mfma_f32_16x16x32_bf16 v[20:23], a[148:151], v[184:187], v[20:23]\n" \
"v_exp_f32_e64 v153, v153\n" \
"v_mfma_f32_16x16x32_bf16 v[24:27], a[184:187], v[192:195], v[24:27]\n" \
"v_exp_f32_e64 v154, v154\n" \
"v_mfma_f32_16x16x32_bf16 v[24:27], a[152:155], v[184:187], v[24:27]\n" \
"ds_read_b128 a[112:115], %[kb0] offset:12288\n" \
"s_add_u32 s48, s48, 0x4000\n" \
"v_mfma_f32_16x16x32_bf16 v[28:31], a[188:191], v[192:195], v[28:31]\n" \
"v_exp_f32_e64 v155, v155\n" \
"v_mfma_f32_16x16x32_bf16 v[28:31], a[156:159], v[184:187], v[28:31]\n" \
"s_waitcnt vmcnt(15) lgkmcnt(2)\n" \
"s_barrier\n" \
"v_mfma_f32_16x16x32_bf16 v[128:131], a[96:99], a[0:3], v[160:163]\n" \
"v_exp_f32_e64 v156, v156\n" \
"v_mfma_f32_16x16x32_bf16 v[128:131], a[100:103], a[4:7], v[128:131]\n" \
"s_add_u32 m0, %[m0b], 114688\n" \
"v_mfma_f32_16x16x32_bf16 v[128:131], a[104:107], a[8:11], v[128:131]\n" \
"v_exp_f32_e64 v157, v157\n" \
"v_mfma_f32_16x16x32_bf16 v[128:131], a[108:111], a[12:15], v[128:131]\n" \
"v_cvt_pk_bf16_f32 v220, v152, v153\n" \
"v_cvt_pk_bf16_f32 v221, v154, v155\n" \
"v_mfma_f32_16x16x32_bf16 v[132:135], a[124:127], a[12:15], v[160:163]\n" \
"v_exp_f32_e64 v158, v158\n" \
"v_mfma_f32_16x16x32_bf16 v[132:135], a[120:123], a[8:11], v[132:135]\n" \
"s_waitcnt lgkmcnt(0)\n" \
"v_mfma_f32_16x16x32_bf16 v[132:135], a[116:119], a[4:7], v[132:135]\n" \
"buffer_load_dwordx4 %[vd0], %[srdv], s48 offen offset:0 lds\n" \
"v_mfma_f32_16x16x32_bf16 v[132:135], a[112:115], a[0:3], v[132:135]\n" \
"v_exp_f32_e32 v159, v159\n" \
"v_mfma_f32_16x16x32_bf16 v[32:35], a[160:163], v[196:199], v[32:35]\n" \
"ds_read_b128 a[64:67], %[kb0] offset:16384\n" \
"v_mfma_f32_16x16x32_bf16 v[32:35], a[128:131], v[188:191], v[32:35]\n" \
"v_exp_f32_e64 v128, v128\n" \
"v_mfma_f32_16x16x32_bf16 v[36:39], a[164:167], v[196:199], v[36:39]\n" \
"v_cvt_pk_bf16_f32 v222, v156, v157\n" \
"v_cvt_pk_bf16_f32 v223, v158, v159\n" \
"v_mfma_f32_16x16x32_bf16 v[36:39], a[132:135], v[188:191], v[36:39]\n" \
"v_exp_f32_e64 v129, v129\n" \
"v_mfma_f32_16x16x32_bf16 v[40:43], a[168:171], v[196:199], v[40:43]\n" \
"ds_read_b128 a[68:71], v208 offset:16384\n" \
"v_mfma_f32_16x16x32_bf16 v[40:43], a[136:139], v[188:191], v[40:43]\n" \
"v_exp_f32_e64 v130, v130\n" \
"v_mfma_f32_16x16x32_bf16 v[44:47], a[172:175], v[196:199], v[44:47]\n" \
"ds_read_b128 a[72:75], v209 offset:16384\n" \
"v_mfma_f32_16x16x32_bf16 v[44:47], a[140:143], v[188:191], v[44:47]\n" \
"v_exp_f32_e64 v131, v131\n" \
"v_smfmac_f32_16x16x64_bf16 v[176:179], a[192:195], v[184:191], v228\n" \
"v_mfma_f32_16x16x32_bf16 v[136:139], a[96:99], a[16:19], v[164:167]\n" \
"v_exp_f32_e64 v132, v132\n" \
"v_mfma_f32_16x16x32_bf16 v[136:139], a[100:103], a[20:23], v[136:139]\n" \
".if . - adaln_body != 20480\n" \
".error \"size model\"\n" \
".endif\n" \
"Lk4_%=:\n" \
"s_cbranch_execz Lk5_%=\n" \
"s_nop 0\n" \
"ds_read_b128 a[76:79], v210 offset:16384\n" \
"v_mfma_f32_16x16x32_bf16 v[136:139], a[104:107], a[24:27], v[136:139]\n" \
"v_exp_f32_e64 v133, v133\n" \
"v_mfma_f32_16x16x32_bf16 v[136:139], a[108:111], a[28:31], v[136:139]\n" \
"v_cvt_pk_bf16_f32 v192, v128, v129\n" \
"v_cvt_pk_bf16_f32 v193, v130, v131\n" \
"v_mfma_f32_16x16x32_bf16 v[140:143], a[124:127], a[28:31], v[164:167]\n" \
"v_exp_f32_e64 v134, v134\n" \
"v_mfma_f32_16x16x32_bf16 v[140:143], a[120:123], a[24:27], v[140:143]\n" \
"ds_read_b128 a[92:95], v210 offset:20480\n" \
"v_mfma_f32_16x16x32_bf16 v[140:143], a[116:119], a[20:23], v[140:143]\n" \
"buffer_load_dwordx4 v215, %[srdv], s48 offen offset:1024 lds\n" \
"v_mfma_f32_16x16x32_bf16 v[140:143], a[112:115], a[16:19], v[140:143]\n" \
"v_exp_f32_e64 v135, v135\n" \
"v_mfma_f32_16x16x32_bf16 v[48:51], a[176:179], v[196:199], v[48:51]\n" \
"ds_read_b128 a[88:91], v209 offset:20480\n" \
"v_mfma_f32_16x16x32_bf16 v[48:51], a[144:147], v[188:191], v[48:51]\n" \
"v_exp_f32_e64 v136, v136\n" \
"v_mfma_f32_16x16x32_bf16 v[52:55], a[180:183], v[196:199], v[52:55]\n" \
"v_cvt_pk_bf16_f32 v194, v132, v133\n" \
"v_cvt_pk_bf16_f32 v195, v134, v135\n" \
"v_mfma_f32_16x16x32_bf16 v[52:55], a[148:151], v[188:191], v[52:55]\n" \
"v_exp_f32_e64 v137, v137\n" \
"v_mfma_f32_16x16x32_bf16 v[56:59], a[184:187], v[196:199], v[56:59]\n" \
"v_exp_f32_e64 v138, v138\n" \
"v_mfma_f32_16x16x32_bf16 v[56:59], a[152:155], v[188:191], v[56:59]\n" \
"ds_read_b128 a[84:87], v208 offset:20480\n" \
"v_mfma_f32_16x16x32_bf16 v[60:63], a[188:191], v[196:199], v[60:63]\n" \
"v_exp_f32_e64 v139, v139\n" \
"v_mfma_f32_16x16x32_bf16 v[60:63], a[156:159], v[188:191], v[60:63]\n" \
"s_cmp_lg_u32 %[wid], 3\n" \
"s_cbranch_scc1 LtA1_%=\n" \
"global_load_dword v237, v224, s[58:59]\n" \
"s_add_u32 s61, s48, 49152\n" \
"s_mov_b32 exec_hi, 0\n" \
"s_nop 0\n" \
"buffer_load_dword v237, v224, %[srdp], s61 offen\n" \
"s_mov_b32 exec_hi, -1\n" \
"s_nop 0\n" \
"LtA1_%=:\n" \
"v_mfma_f32_16x16x32_bf16 v[144:147], a[96:99], a[32:35], v[168:171]\n" \
"v_exp_f32_e64 v140, v140\n" \
"v_mfma_f32_16x16x32_bf16 v[144:147], a[100:103], a[36:39], v[144:147]\n" \
"ds_read_b128 a[80:83], %[kb0] offset:20480\n" \
"v_mfma_f32_16x16x32_bf16 v[144:147], a[104:107], a[40:43], v[144:147]\n" \
"v_exp_f32_e64 v141, v141\n" \
"v_mfma_f32_16x16x32_bf16 v[144:147], a[108:111], a[44:47], v[144:147]\n" \
"v_cvt_pk_bf16_f32 v196, v136, v137\n" \
"v_cvt_pk_bf16_f32 v197, v138, v139\n" \
"v_mfma_f32_16x16x32_bf16 v[148:151], a[124:127], a[44:47], v[168:171]\n" \
"v_exp_f32_e64 v142, v142\n" \
"v_mfma_f32_16x16x32_bf16 v[148:151], a[120:123], a[40:43], v[148:151]\n" \
"v_mfma_f32_16x16x32_bf16 v[148:151], a[116:119], a[36:39], v[148:151]\n" \
"buffer_load_dwordx4 %[vd0], %[srdv], s48 offen offset:2048 lds\n" \
"v_mfma_f32_16x16x32_bf16 v[148:151], a[112:115], a[32:35], v[148:151]\n" \
"v_exp_f32_e64 v143, v143\n" \
"v_mfma_f32_16x16x32_bf16 v[96:99], a[160:163], v[204:207], v[96:99]\n" \
"ds_read_b64_tr_b16 a[160:161], %[vvb] offset:8192\n" \
"v_mfma_f32_16x16x32_bf16 v[96:99], a[128:131], v[220:223], v[96:99]\n" \
"v_exp_f32_e64 v144, v144\n" \
"v_mfma_f32_16x16x32_bf16 v[100:103], a[164:167], v[204:207], v[100:103]\n" \
"v_cvt_pk_bf16_f32 v198, v140, v141\n" \
"v_cvt_pk_bf16_f32 v199, v142, v143\n" \
"v_mfma_f32_16x16x32_bf16 v[100:103], a[132:135], v[220:223], v[100:103]\n" \
"v_exp_f32_e64 v145, v145\n" \
"v_mfma_f32_16x16x32_bf16 v[104:107], a[168:171], v[204:207], v[104:107]\n" \
"ds_read_b64_tr_b16 a[162:163], %[vvb] offset:12288\n" \
"ds_read_b64_tr_b16 a[164:165], v211 offset:8192\n" \
"v_mfma_f32_16x16x32_bf16 v[104:107], a[136:139], v[220:223], v[104:107]\n" \
"v_exp_f32_e64 v146, v146\n" \
"v_mfma_f32_16x16x32_bf16 v[108:111], a[172:175], v[204:207], v[108:111]\n" \
"ds_read_b64_tr_b16 a[166:167], v211 offset:12288\n" \
"v_mfma_f32_16x16x32_bf16 v[108:111], a[140:143], v[220:223], v[108:111]\n" \
"v_exp_f32_e64 v147, v147\n" \
"v_smfmac_f32_16x16x64_bf16 v[180:183], a[192:195], v[216:223], v228\n" \
"ds_read_b64_tr_b16 a[168:169], %[vvb] offset:8704\n" \
"v_mfma_f32_16x16x32_bf16 v[152:155], a[96:99], a[48:51], v[172:175]\n" \
"v_exp_f32_e64 v148, v148\n" \
"v_mfma_f32_16x16x32_bf16 v[152:155], a[100:103], a[52:55], v[152:155]\n" \
"ds_read_b64_tr_b16 a[170:171], %[vvb] offset:12800\n" \
"v_mfma_f32_16x16x32_bf16 v[152:155], a[104:107], a[56:59], v[152:155]\n" \
"v_exp_f32_e64 v149, v149\n" \
"v_mfma_f32_16x16x32_bf16 v[152:155], a[108:111], a[60:63], v[152:155]\n" \
"v_cvt_pk_bf16_f32 v200, v144, v145\n" \
"v_cvt_pk_bf16_f32 v201, v146, v147\n" \
"v_mfma_f32_16x16x32_bf16 v[156:159], a[124:127], a[60:63], v[172:175]\n" \
"v_exp_f32_e64 v150, v150\n" \
"v_mfma_f32_16x16x32_bf16 v[156:159], a[120:123], a[56:59], v[156:159]\n" \
"ds_read_b64_tr_b16 a[172:173], v211 offset:8704\n" \
"v_mfma_f32_16x16x32_bf16 v[156:159], a[116:119], a[52:55], v[156:159]\n" \
"buffer_load_dwordx4 v215, %[srdv], s48 offen offset:3072 lds\n" \
"v_mfma_f32_16x16x32_bf16 v[156:159], a[112:115], a[48:51], v[156:159]\n" \
"v_exp_f32_e64 v151, v151\n" \
"v_mfma_f32_16x16x32_bf16 v[112:115], a[176:179], v[204:207], v[112:115]\n" \
"ds_read_b64_tr_b16 a[174:175], v211 offset:12800\n" \
"ds_read_b64_tr_b16 a[176:177], %[vvb] offset:9216\n" \
"v_mfma_f32_16x16x32_bf16 v[112:115], a[144:147], v[220:223], v[112:115]\n" \
"v_exp_f32_e64 v152, v152\n" \
"v_mfma_f32_16x16x32_bf16 v[116:119], a[180:183], v[204:207], v[116:119]\n" \
"v_cvt_pk_bf16_f32 v202, v148, v149\n" \
"v_cvt_pk_bf16_f32 v203, v150, v151\n" \
"v_mfma_f32_16x16x32_bf16 v[116:119], a[148:151], v[220:223], v[116:119]\n" \
"v_exp_f32_e64 v153, v153\n" \
"v_mfma_f32_16x16x32_bf16 v[120:123], a[184:187], v[204:207], v[120:123]\n" \
"v_exp_f32_e64 v154, v154\n" \
"v_mfma_f32_16x16x32_bf16 v[120:123], a[152:155], v[220:223], v[120:123]\n" \
"ds_read_b64_tr_b16 a[178:179], %[vvb] offset:13312\n" \
"v_mfma_f32_16x16x32_bf16 v[124:127], a[188:191], v[204:207], v[124:127]\n" \
"v_exp_f32_e64 v155, v155\n" \
"v_mfma_f32_16x16x32_bf16 v[124:127], a[156:159], v[220:223], v[124:127]\n" \
"ds_read_b64_tr_b16 a[180:181], v211 offset:9216\n" \
"s_waitcnt lgkmcnt(13)\n" \
"s_cmp_lg_u32 s60, 4\n" \
"s_cbranch_scc1 LmA_%=\n" \
"s_nop 0\n" \
"v_readlane_b32 s64, %[tlo], 9\n" \
"v_readlane_b32 s65, %[thi], 9\n" \
"s_mov_b32 s66, %[wbytes]\n" \
"v_mov_b32_e32 v229, %[vg0]\n" \
"s_mov_b32 s44, %[m0t]\n" \
"s_mov_b32 s45, 0\n" \
"s_cmp_lg_u32 s52, 0\n" \
"s_cbranch_scc1 LmB_%=\n" \
"s_add_u32 s62, %[sin], 8192\n" \
"s_add_u32 s63, %[sin], 28672\n" \
"s_waitcnt vmcnt(13)\n" \
"s_nop 0\n" \
"ds_read_b64_tr_b16 a[232:233], %[ta0] offset:0\n" \
"ds_read_b64_tr_b16 a[234:235], %[ta0] offset:128\n" \
"ds_read_b64_tr_b16 a[236:237], %[ta0] offset:1024\n" \
"ds_read_b64_tr_b16 a[238:239], %[ta0] offset:1152\n" \
"ds_read_b64_tr_b16 a[240:241], %[ta0] offset:2048\n" \
"ds_read_b64_tr_b16 a[242:243], %[ta0] offset:2176\n" \
"ds_read_b64_tr_b16 a[244:245], %[ta0] offset:3072\n" \
"ds_read_b64_tr_b16 a[246:247], %[ta0] offset:3200\n" \
"ds_read_b64_tr_b16 a[248:249], %[ta0] offset:4096\n" \
"ds_read_b64_tr_b16 a[250:251], %[ta0] offset:4224\n" \
"ds_read_b64_tr_b16 a[252:253], %[ta0] offset:5120\n" \
"ds_read_b64_tr_b16 a[254:255], %[ta0] offset:5248\n" \
"v_add_u32_e32 v234, %[m0t], v235\n" \
"s_waitcnt lgkmcnt(8)\n" \
"v_mfma_f32_16x16x32_bf16 v[230:233], a[232:235], a[200:203], 0\n" \
"v_mfma_f32_16x16x32_bf16 v[230:233], a[236:239], a[204:207], v[230:233]\n" \
"ds_read_b64_tr_b16 a[232:233], %[ta0] offset:6144\n" \
"ds_read_b64_tr_b16 a[234:235], %[ta0] offset:6272\n" \
"ds_read_b64_tr_b16 a[236:237], %[ta0] offset:7168\n" \
"ds_read_b64_tr_b16 a[238:239], %[ta0] offset:7296\n" \
"s_waitcnt lgkmcnt(4)\n" \
"s_nop 0\n" \
"v_mfma_f32_16x16x32_bf16 v[230:233], a[240:243], a[208:211], v[230:233]\n" \
"v_mfma_f32_16x16x32_bf16 v[230:233], a[244:247], a[212:215], v[230:233]\n" \
"v_mfma_f32_16x16x32_bf16 v[230:233], a[248:251], a[216:219], v[230:233]\n" \
"v_mfma_f32_16x16x32_bf16 v[230:233], a[252:255], a[220:223], v[230:233]\n" \
"s_waitcnt lgkmcnt(0)\n" \
"s_nop 0\n" \
"v_mfma_f32_16x16x32_bf16 v[230:233], a[232:235], a[224:227], v[230:233]\n" \
"v_mfma_f32_16x16x32_bf16 v[230:233], a[236:239], a[228:231], v[230:233]\n" \
"s_nop 7\n" \
"s_nop 0\n" \
"ds_write_b128 v234, v[230:233] offset:7168\n" \
"LmB_%=:\n" \
"v_mfma_f32_16x16x32_bf16 v[128:131], a[64:67], a[0:3], v[160:163]\n" \
"v_exp_f32_e64 v156, v156\n" \
"v_mfma_f32_16x16x32_bf16 v[128:131], a[68:71], a[4:7], v[128:131]\n" \
"ds_read_b64_tr_b16 a[182:183], v211 offset:13312\n" \
"s_add_u32 m0, %[m0b], 0\n" \
"v_mfma_f32_16x16x32_bf16 v[128:131], a[72:75], a[8:11], v[128:131]\n" \
"v_exp_f32_e32 v157, v157\n" \
"v_mfma_f32_16x16x32_bf16 v[128:131], a[76:79], a[12:15], v[128:131]\n" \
"v_cvt_pk_bf16_f32 v204, v152, v153\n" \
"v_cvt_pk_bf16_f32 v205, v154, v155\n" \
"v_mfma_f32_16x16x32_bf16 v[132:135], a[92:95], a[12:15], v[160:163]\n" \
"v_exp_f32_e64 v158, v158\n" \
"v_mfma_f32_16x16x32_bf16 v[132:135], a[88:91], a[8:11], v[132:135]\n" \
"ds_read_b64_tr_b16 a[184:185], %[vvb] offset:9728\n" \
"s_waitcnt lgkmcnt(5)\n" \
"v_mfma_f32_16x16x32_bf16 v[132:135], a[84:87], a[4:7], v[132:135]\n" \
"buffer_load_dwordx4 %[kd0], %[srdk], s48 offen offset:0 lds\n" \
"v_mfma_f32_16x16x32_bf16 v[132:135], a[80:83], a[0:3], v[132:135]\n" \
"v_exp_f32_e32 v159, v159\n" \
"v_mfma_f32_16x16x32_bf16 v[64:67], a[128:131], v[216:219], v[64:67]\n" \
"ds_read_b64_tr_b16 a[186:187], %[vvb] offset:13824\n" \
"ds_read_b64_tr_b16 a[188:189], v211 offset:9728\n" \
"v_mfma_f32_16x16x32_bf16 v[64:67], a[160:163], v[200:203], v[64:67]\n" \
"v_exp_f32_e64 v128, v128\n" \
"v_mfma_f32_16x16x32_bf16 v[68:71], a[132:135], v[216:219], v[68:71]\n" \
"v_cvt_pk_bf16_f32 v206, v156, v157\n" \
"v_cvt_pk_bf16_f32 v207, v158, v159\n" \
"v_mfma_f32_16x16x32_bf16 v[68:71], a[164:167], v[200:203], v[68:71]\n" \
"v_exp_f32_e64 v129, v129\n" \
"v_mfma_f32_16x16x32_bf16 v[72:75], a[136:139], v[216:219], v[72:75]\n" \
"ds_read_b64_tr_b16 a[190:191], v211 offset:13824\n" \
"ds_read_b64_tr_b16 a[128:129], %[vvb] offset:16384\n" \
"s_add_u32 s60, s60, 1\n" \
"s_nop 0\n" \
"v_readlane_b32 s58, %[tlo], s60\n" \
"v_mfma_f32_16x16x32_bf16 v[72:75], a[168:171], v[200:203], v[72:75]\n" \
"v_exp_f32_e64 v130, v130\n" \
"v_mfma_f32_16x16x32_bf16 v[76:79], a[140:143], v[216:219], v[76:79]\n" \
"ds_read_b64_tr_b16 a[130:131], %[vvb] offset:20480\n" \
"ds_read_b64_tr_b16 a[132:133], v211 offset:16384\n" \
"v_readlane_b32 s59, %[thi], s60\n" \
"v_mfma_f32_16x16x32_bf16 v[76:79], a[172:175], v[200:203], v[76:79]\n" \
"v_exp_f32_e64 v131, v131\n" \
"v_smfmac_f32_16x16x64_bf16 v[176:179], a[192:195], v[192:199], v228\n" \
"ds_read_b64_tr_b16 a[134:135], v211 offset:20480\n" \
"ds_read_b64_tr_b16 a[136:137], %[vvb] offset:16896\n" \
"s_cmp_eq_u32 s60, 7\n" \
"s_cselect_b32 s61, 56, 1\n" \
"v_mul_u32_u24_e64 v236, s61, v224\n" \
"v_mfma_f32_16x16x32_bf16 v[136:139], a[64:67], a[16:19], v[164:167]\n" \
"v_exp_f32_e64 v132, v132\n" \
"v_mfma_f32_16x16x32_bf16 v[136:139], a[68:71], a[20:23], v[136:139]\n" \
"ds_read_b64_tr_b16 a[138:139], %[vvb] offset:20992\n" \
"ds_read_b64_tr_b16 a[140:141], v211 offset:16896\n" \
"v_mfma_f32_16x16x32_bf16 v[136:139], a[72:75], a[24:27], v[136:139]\n" \
"v_exp_f32_e64 v133, v133\n" \
"v_mfma_f32_16x16x32_bf16 v[136:139], a[76:79], a[28:31], v[136:139]\n" \
"v_cvt_pk_bf16_f32 v184, v128, v129\n" \
"v_cvt_pk_bf16_f32 v185, v130, v131\n" \
"v_mfma_f32_16x16x32_bf16 v[140:143], a[92:95], a[28:31], v[164:167]\n" \
"v_exp_f32_e64 v134, v134\n" \
"v_mfma_f32_16x16x32_bf16 v[140:143], a[88:91], a[24:27], v[140:143]\n" \
"ds_read_b64_tr_b16 a[142:143], v211 offset:20992\n" \
"s_waitcnt lgkmcnt(8)\n" \
"v_mfma_f32_16x16x32_bf16 v[140:143], a[84:87], a[20:23], v[140:143]\n" \
"buffer_load_dwordx4 v212, %[srdk], s48 offen offset:1024 lds\n" \
"v_mfma_f32_16x16x32_bf16 v[140:143], a[80:83], a[16:19], v[140:143]\n" \
"v_exp_f32_e32 v135, v135\n" \
"v_mfma_f32_16x16x32_bf16 v[80:83], a[144:147], v[216:219], v[80:83]\n" \
"ds_read_b64_tr_b16 a[144:145], %[vvb] offset:17408\n" \
"ds_read_b64_tr_b16 a[146:147], %[vvb] offset:21504\n" \
"v_mfma_f32_16x16x32_bf16 v[80:83], a[176:179], v[200:203], v[80:83]\n" \
"v_exp_f32_e64 v136, v136\n" \
"v_mfma_f32_16x16x32_bf16 v[84:87], a[148:151], v[216:219], v[84:87]\n" \
"v_cvt_pk_bf16_f32 v186, v132, v133\n" \
"v_cvt_pk_bf16_f32 v187, v134, v135\n" \
"v_mfma_f32_16x16x32_bf16 v[84:87], a[180:183], v[200:203], v[84:87]\n" \
"v_exp_f32_e64 v137, v137\n" \
"v_mfma_f32_16x16x32_bf16 v[88:91], a[152:155], v[216:219], v[88:91]\n" \
"v_exp_f32_e64 v138, v138\n" \
"v_mfma_f32_16x16x32_bf16 v[88:91], a[184:187], v[200:203], v[88:91]\n" \
"ds_read_b64_tr_b16 a[148:149], v211 offset:17408\n" \
"ds_read_b64_tr_b16 a[150:151], v211 offset:21504\n" \
"v_mfma_f32_16x16x32_bf16 v[92:95], a[156:159], v[216:219], v[92:95]\n" \
"v_exp_f32_e64 v139, v139\n" \
"v_mfma_f32_16x16x32_bf16 v[92:95], a[188:191], v[200:203], v[92:95]\n" \
"ds_read_b64_tr_b16 a[152:153], %[vvb] offset:17920\n" \
"buffer_store_dwordx4 a[196:199], v235, %[srdz], s63 offen offset:0 sc1\n" \
"v_mfma_f32_16x16x32_bf16 v[144:147], a[64:67], a[32:35], v[168:171]\n" \
"v_exp_f32_e64 v140, v140\n" \
"v_mfma_f32_16x16x32_bf16 v[144:147], a[68:71], a[36:39], v[144:147]\n" \
"ds_read_b64_tr_b16 a[154:155], %[vvb] offset:22016\n" \
"ds_read_b64_tr_b16 a[156:157], v211 offset:17920\n" \
"v_mfma_f32_16x16x32_bf16 v[144:147], a[72:75], a[40:43], v[144:147]\n" \
"v_exp_f32_e64 v141, v141\n" \
"v_mfma_f32_16x16x32_bf16 v[144:147], a[76:79], a[44:47], v[144:147]\n" \
"v_cvt_pk_bf16_f32 v188, v136, v137\n" \
"v_cvt_pk_bf16_f32 v189, v138, v139\n" \
"v_mfma_f32_16x16x32_bf16 v[148:151], a[92:95], a[44:47], v[168:171]\n" \
"v_exp_f32_e64 v142, v142\n" \
"v_mfma_f32_16x16x32_bf16 v[148:151], a[88:91], a[40:43], v[148:151]\n" \
"ds_read_b64_tr_b16 a[158:159], v211 offset:22016\n" \
"s_waitcnt lgkmcnt(8)\n" \
"v_mfma_f32_16x16x32_bf16 v[148:151], a[84:87], a[36:39], v[148:151]\n" \
"buffer_load_dwordx4 v213, %[srdk], s48 offen offset:2048 lds\n" \
"v_mfma_f32_16x16x32_bf16 v[148:151], a[80:83], a[32:35], v[148:151]\n" \
"v_exp_f32_e32 v143, v143\n" \
"v_mfma_f32_16x16x32_bf16 v[0:3], a[160:163], v[192:195], v[0:3]\n" \
"ds_read_b128 a[96:99], %[kb0] offset:24576\n" \
"v_mfma_f32_16x16x32_bf16 v[0:3], a[128:131], v[184:187], v[0:3]\n" \
"v_exp_f32_e64 v144, v144\n" \
"v_mfma_f32_16x16x32_bf16 v[4:7], a[164:167], v[192:195], v[4:7]\n" \
"v_cvt_pk_bf16_f32 v190, v140, v141\n" \
"v_cvt_pk_bf16_f32 v191, v142, v143\n" \
"v_mfma_f32_16x16x32_bf16 v[4:7], a[132:135], v[184:187], v[4:7]\n" \
"v_exp_f32_e64 v145, v145\n" \
"v_mfma_f32_16x16x32_bf16 v[8:11], a[168:171], v[192:195], v[8:11]\n" \
"ds_read_b128 a[100:103], v208 offset:24576\n" \
"v_mfma_f32_16x16x32_bf16 v[8:11], a[136:139], v[184:187], v[8:11]\n" \
"v_exp_f32_e64 v146, v146\n" \
"v_mfma_f32_16x16x32_bf16 v[12:15], a[172:175], v[192:195], v[12:15]\n" \
"ds_read_b128 a[104:107], v209 offset:24576\n" \
"v_mfma_f32_16x16x32_bf16 v[12:15], a[140:143], v[184:187], v[12:15]\n" \
"v_exp_f32_e64 v147, v147\n" \
"v_smfmac_f32_16x16x64_bf16 v[180:183], a[192:195], v[200:207], v228\n" \
"ds_read_b128 a[108:111], v210 offset:24576\n" \
"v_mfma_f32_16x16x32_bf16 v[152:155], a[64:67], a[48:51], v[172:175]\n" \
"v_exp_f32_e64 v148, v148\n" \
"v_mfma_f32_16x16x32_bf16 v[152:155], a[68:71], a[52:55], v[152:155]\n" \
"ds_read_b128 a[124:127], v210 offset:28672\n" \
"v_mfma_f32_16x16x32_bf16 v[152:155], a[72:75], a[56:59], v[152:155]\n" \
"v_exp_f32_e64 v149, v149\n" \
"v_mfma_f32_16x16x32_bf16 v[152:155], a[76:79], a[60:63], v[152:155]\n" \
"v_cvt_pk_bf16_f32 v216, v144, v145\n" \
"v_cvt_pk_bf16_f32 v217, v146, v147\n" \
"v_mfma_f32_16x16x32_bf16 v[156:159], a[92:95], a[60:63], v[172:175]\n" \
"v_exp_f32_e64 v150, v150\n" \
"v_mfma_f32_16x16x32_bf16 v[156:159], a[88:91], a[56:59], v[156:159]\n" \
"ds_read_b128 a[120:123], v209 offset:28672\n" \
"s_waitcnt lgkmcnt(6)\n" \
"v_mfma_f32_16x16x32_bf16 v[156:159], a[84:87], a[52:55], v[156:159]\n" \
"buffer_load_dwordx4 v214, %[srdk], s48 offen offset:3072 lds\n" \
"v_mfma_f32_16x16x32_bf16 v[156:159], a[80:83], a[48:51], v[156:159]\n" \
"v_exp_f32_e32 v151, v151\n" \
"v_mfma_f32_16x16x32_bf16 v[16:19], a[176:179], v[192:195], v[16:19]\n" \
"ds_read_b128 a[116:119], v208 offset:28672\n" \
"v_mfma_f32_16x16x32_bf16 v[16:19], a[144:147], v[184:187], v[16:19]\n" \
"v_exp_f32_e64 v152, v152\n" \
"v_mfma_f32_16x16x32_bf16 v[20:23], a[180:183], v[192:195], v[20:23]\n" \
"v_cvt_pk_bf16_f32 v218, v148, v149\n" \
"v_cvt_pk_bf16_f32 v219, v150, v151\n" \
"v_mfma_f32_16x16x32_bf16 v[20:23], a[148:151], v[184:187], v[20:23]\n" \
"v_exp_f32_e64 v153, v153\n" \
"v_mfma_f32_16x16x32_bf16 v[24:27], a[184:187], v[192:195], v[24:27]\n" \
"v_exp_f32_e64 v154, v154\n" \
"v_mfma_f32_16x16x32_bf16 v[24:27], a[152:155], v[184:187], v[24:27]\n" \
"ds_read_b128 a[112:115], %[kb0] offset:28672\n" \
"s_add_u32 s48, s48, 0x4000\n" \
"s_mov_b32 m0, s44\n" \
"s_addk_i32 s44, 0x400\n" \
"buffer_load_dwordx4 v229, s[64:67], s45 offen offset:0 lds\n" \
"s_add_u32 s45, s45, 229376\n" \
"v_mfma_f32_16x16x32_bf16 v[28:31], a[188:191], v[192:195], v[28:31]\n" \
"v_exp_f32_e64 v155, v155\n" \
"v_mfma_f32_16x16x32_bf16 v[28:31], a[156:159], v[184:187], v[28:31]\n" \
"s_waitcnt vmcnt(15) lgkmcnt(2)\n" \
"s_barrier\n" \
"v_mfma_f32_16x16x32_bf16 v[128:131], a[96:99], a[0:3], v[160:163]\n" \
"v_exp_f32_e64 v156, v156\n" \
"v_mfma_f32_16x16x32_bf16 v[128:131], a[100:103], a[4:7], v[128:131]\n" \
"s_add_u32 m0, %[m0b], 65536\n" \
"v_mfma_f32_16x16x32_bf16 v[128:131], a[104:107], a[8:11], v[128:131]\n" \
"v_exp_f32_e64 v157, v157\n" \
"v_mfma_f32_16x16x32_bf16 v[128:131], a[108:111], a[12:15], v[128:131]\n" \
"v_cvt_pk_bf16_f32 v220, v152, v153\n" \
"v_cvt_pk_bf16_f32 v221, v154, v155\n" \
"v_mfma_f32_16x16x32_bf16 v[132:135], a[124:127], a[12:15], v[160:163]\n" \
"v_exp_f32_e64 v158, v158\n" \
"v_mfma_f32_16x16x32_bf16 v[132:135], a[120:123], a[8:11], v[132:135]\n" \
"s_waitcnt lgkmcnt(0)\n" \
"v_mfma_f32_16x16x32_bf16 v[132:135], a[116:119], a[4:7], v[132:135]\n" \
"buffer_load_dwordx4 %[vd0], %[srdv], s48 offen offset:0 lds\n" \
"v_mfma_f32_16x16x32_bf16 v[132:135], a[112:115], a[0:3], v[132:135]\n" \
"v_exp_f32_e32 v159, v159\n" \
"v_mfma_f32_16x16x32_bf16 v[32:35], a[160:163], v[196:199], v[32:35]\n" \
"ds_read_b128 a[64:67], %[kb0] offset:32768\n" \
"v_mfma_f32_16x16x32_bf16 v[32:35], a[128:131], v[188:191], v[32:35]\n" \
"v_exp_f32_e64 v128, v128\n" \
"v_mfma_f32_16x16x32_bf16 v[36:39], a[164:167], v[196:199], v[36:39]\n" \
"v_cvt_pk_bf16_f32 v222, v156, v157\n" \
"v_cvt_pk_bf16_f32 v223, v158, v159\n" \
"v_mfma_f32_16x16x32_bf16 v[36:39], a[132:135], v[188:191], v[36:39]\n" \
"v_exp_f32_e64 v129, v129\n" \
"v_mfma_f32_16x16x32_bf16 v[40:43], a[168:171], v[196:199], v[40:43]\n" \
"ds_read_b128 a[68:71], v208 offset:32768\n" \
"v_mfma_f32_16x16x32_bf16 v[40:43], a[136:139], v[188:191], v[40:43]\n" \
"v_exp_f32_e64 v130, v130\n" \
"v_mfma_f32_16x16x32_bf16 v[44:47], a[172:175], v[196:199], v[44:47]\n" \
"ds_read_b128 a[72:75], v209 offset:32768\n" \
"v_mfma_f32_16x16x32_bf16 v[44:47], a[140:143], v[188:191], v[44:47]\n" \
"v_exp_f32_e64 v131, v131\n" \
"v_smfmac_f32_16x16x64_bf16 v[176:179], a[192:195], v[184:191], v228\n" \
"v_mfma_f32_16x16x32_bf16 v[136:139], a[96:99], a[16:19], v[164:167]\n" \
"v_exp_f32_e64 v132, v132\n" \
"v_mfma_f32_16x16x32_bf16 v[136:139], a[100:103], a[20:23], v[136:139]\n" \
"ds_read_b128 a[76:79], v210 offset:32768\n" \
"v_mfma_f32_16x16x32_bf16 v[136:139], a[104:107], a[24:27], v[136:139]\n" \
"v_exp_f32_e64 v133, v133\n" \
"v_mfma_f32_16x16x32_bf16 v[136:139], a[108:111], a[28:31], v[136:139]\n" \
"v_cvt_pk_bf16_f32 v192, v128, v129\n" \
"v_cvt_pk_bf16_f32 v193, v130, v131\n" \
"v_mfma_f32_16x16x32_bf16 v[140:143], a[124:127], a[28:31], v[164:167]\n" \
"v_exp_f32_e64 v134, v134\n" \
"v_mfma_f32_16x16x32_bf16 v[140:143], a[120:123], a[24:27], v[140:143]\n" \
"ds_read_b128 a[92:95], v210 offset:36864\n" \
"v_mfma_f32_16x16x32_bf16 v[140:143], a[116:119], a[20:23], v[140:143]\n" \
"buffer_load_dwordx4 v215, %[srdv], s48 offen offset:1024 lds\n" \
"v_mfma_f32_16x16x32_bf16 v[140:143], a[112:115], a[16:19], v[140:143]\n" \
"v_exp_f32_e64 v135, v135\n" \
"v_mfma_f32_16x16x32_bf16 v[48:51], a[176:179], v[196:199], v[48:51]\n" \
"ds_read_b128 a[88:91], v209 offset:36864\n" \
"v_mfma_f32_16x16x32_bf16 v[48:51], a[144:147], v[188:191], v[48:51]\n" \
"v_exp_f32_e64 v136, v136\n" \
"v_mfma_f32_16x16x32_bf16 v[52:55], a[180:183], v[196:199], v[52:55]\n" \
"v_cvt_pk_bf16_f32 v194, v132, v133\n" \
"v_cvt_pk_bf16_f32 v195, v134, v135\n" \
"v_mfma_f32_16x16x32_bf16 v[52:55], a[148:151], v[188:191], v[52:55]\n" \
"v_exp_f32_e64 v137, v137\n" \
"v_mfma_f32_16x16x32_bf16 v[56:59], a[184:187], v[196:199], v[56:59]\n" \
"v_exp_f32_e64 v138, v138\n" \
"v_mfma_f32_16x16x32_bf16 v[56:59], a[152:155], v[188:191], v[56:59]\n" \
"ds_read_b128 a[84:87], v208 offset:36864\n" \
"v_mfma_f32_16x16x32_bf16 v[60:63], a[188:191], v[196:199], v[60:63]\n" \
"v_exp_f32_e64 v139, v139\n" \
"v_mfma_f32_16x16x32_bf16 v[60:63], a[156:159], v[188:191], v[60:63]\n" \
"s_cmp_lg_u32 %[wid], 0\n" \
"s_cbranch_scc1 LtB3_%=\n" \
"global_load_dword v237, v236, s[58:59]\n" \
"s_add_u32 s61, s48, 49152\n" \
"s_mov_b32 exec_hi, 0\n" \
"s_nop 0\n" \
"buffer_load_dword v237, v224, %[srdp], s61 offen\n" \
"s_mov_b32 exec_hi, -1\n" \
"s_nop 0\n" \
"LtB3_%=:\n" \
"v_mfma_f32_16x16x32_bf16 v[144:147], a[96:99], a[32:35], v[168:171]\n" \
"v_exp_f32_e64 v140, v140\n" \
"v_mfma_f32_16x16x32_bf16 v[144:147], a[100:103], a[36:39], v[144:147]\n" \
"ds_read_b128 a[80:83], %[kb0] offset:36864\n" \
"v_mfma_f32_16x16x32_bf16 v[144:147], a[104:107], a[40:43], v[144:147]\n" \
"v_exp_f32_e64 v141, v141\n" \
"v_mfma_f32_16x16x32_bf16 v[144:147], a[108:111], a[44:47], v[144:147]\n" \
"v_cvt_pk_bf16_f32 v196, v136, v137\n" \
"v_cvt_pk_bf16_f32 v197, v138, v139\n" \
"v_mfma_f32_16x16x32_bf16 v[148:151], a[124:127], a[44:47], v[168:171]\n" \
"v_exp_f32_e64 v142, v142\n" \
"v_mfma_f32_16x16x32_bf16 v[148:151], a[120:123], a[40:43], v[148:151]\n" \
"v_mfma_f32_16x16x32_bf16 v[148:151], a[116:119], a[36:39], v[148:151]\n" \
"buffer_load_dwordx4 %[vd0], %[srdv], s48 offen offset:2048 lds\n" \
"v_mfma_f32_16x16x32_bf16 v[148:151], a[112:115], a[32:35], v[148:151]\n" \
"v_exp_f32_e64 v143, v143\n" \
"v_mfma_f32_16x16x32_bf16 v[96:99], a[160:163], v[204:207], v[96:99]\n" \
"ds_read_b64_tr_b16 a[160:161], %[vvb] offset:24576\n" \
"v_mfma_f32_16x16x32_bf16 v[96:99], a[128:131], v[220:223], v[96:99]\n" \
"v_exp_f32_e64 v144, v144\n" \
"v_mfma_f32_16x16x32_bf16 v[100:103], a[164:167], v[204:207], v[100:103]\n" \
"v_cvt_pk_bf16_f32 v198, v140, v141\n" \
"v_cvt_pk_bf16_f32 v199, v142, v143\n" \
"v_mfma_f32_16x16x32_bf16 v[100:103], a[132:135], v[220:223], v[100:103]\n" \
"v_exp_f32_e64 v145, v145\n" \
"v_mfma_f32_16x16x32_bf16 v[104:107], a[168:171], v[204:207], v[104:107]\n" \
"ds_read_b64_tr_b16 a[162:163], %[vvb] offset:28672\n" \
"ds_read_b64_tr_b16 a[164:165], v211 offset:24576\n" \
"v_mfma_f32_16x16x32_bf16 v[104:107], a[136:139], v[220:223], v[104:107]\n" \
"v_exp_f32_e64 v146, v146\n" \
"v_mfma_f32_16x16x32_bf16 v[108:111], a[172:175], v[204:207], v[108:111]\n" \
"ds_read_b64_tr_b16 a[166:167], v211 offset:28672\n" \
"v_mfma_f32_16x16x32_bf16 v[108:111], a[140:143], v[220:223], v[108:111]\n" \
"v_exp_f32_e64 v147, v147\n" \
"v_smfmac_f32_16x16x64_bf16 v[180:183], a[192:195], v[216:223], v228\n" \
"ds_read_b64_tr_b16 a[168:169], %[vvb] offset:25088\n" \
"buffer_store_dwordx4 a[196:199], v235, %[srdz], s62 offen offset:0 sc1\n" \
"v_mfma_f32_16x16x32_bf16 v[152:155], a[96:99], a[48:51], v[172:175]\n" \
"v_exp_f32_e64 v148, v148\n" \
"v_mfma_f32_16x16x32_bf16 v[152:155], a[100:103], a[52:55], v[152:155]\n" \
"ds_read_b64_tr_b16 a[170:171], %[vvb] offset:29184\n" \
"v_mfma_f32_16x16x32_bf16 v[152:155], a[104:107], a[56:59], v[152:155]\n" \
"v_exp_f32_e64 v149, v149\n" \
"v_mfma_f32_16x16x32_bf16 v[152:155], a[108:111], a[60:63], v[152:155]\n" \
"v_cvt_pk_bf16_f32 v200, v144, v145\n" \
"v_cvt_pk_bf16_f32 v201, v146, v147\n" \
"v_mfma_f32_16x16x32_bf16 v[156:159], a[124:127], a[60:63], v[172:175]\n" \
"v_exp_f32_e64 v150, v150\n" \
"v_mfma_f32_16x16x32_bf16 v[156:159], a[120:123], a[56:59], v[156:159]\n" \
"ds_read_b64_tr_b16 a[172:173], v211 offset:25088\n" \
"v_mfma_f32_16x16x32_bf16 v[156:159], a[116:119], a[52:55], v[156:159]\n" \
"buffer_load_dwordx4 v215, %[srdv], s48 offen offset:3072 lds\n" \
"v_mfma_f32_16x16x32_bf16 v[156:159], a[112:115], a[48:51], v[156:159]\n" \
"v_exp_f32_e64 v151, v151\n" \
"v_mfma_f32_16x16x32_bf16 v[112:115], a[176:179], v[204:207], v[112:115]\n" \
"ds_read_b64_tr_b16 a[174:175], v211 offset:29184\n" \
"ds_read_b64_tr_b16 a[176:177], %[vvb] offset:25600\n" \
"v_mfma_f32_16x16x32_bf16 v[112:115], a[144:147], v[220:223], v[112:115]\n" \
"v_exp_f32_e64 v152, v152\n" \
"v_mfma_f32_16x16x32_bf16 v[116:119], a[180:183], v[204:207], v[116:119]\n" \
"v_cvt_pk_bf16_f32 v202, v148, v149\n" \
"v_cvt_pk_bf16_f32 v203, v150, v151\n" \
"v_mfma_f32_16x16x32_bf16 v[116:119], a[148:151], v[220:223], v[116:119]\n" \
"v_exp_f32_e64 v153, v153\n" \
"v_mfma_f32_16x16x32_bf16 v[120:123], a[184:187], v[204:207], v[120:123]\n" \
"v_exp_f32_e64 v154, v154\n" \
"v_mfma_f32_16x16x32_bf16 v[120:123], a[152:155], v[220:223], v[120:123]\n" \
"ds_read_b64_tr_b16 a[178:179], %[vvb] offset:29696\n" \
"v_mfma_f32_16x16x32_bf16 v[124:127], a[188:191], v[204:207], v[124:127]\n" \
"v_exp_f32_e64 v155, v155\n" \
"v_mfma_f32_16x16x32_bf16 v[124:127], a[156:159], v[220:223], v[124:127]\n" \
"ds_read_b64_tr_b16 a[180:181], v211 offset:25600\n" \
"s_waitcnt lgkmcnt(13)\n" \
"v_mfma_f32_16x16x32_bf16 v[128:131], a[64:67], a[0:3], v[160:163]\n" \
"v_exp_f32_e32 v156, v156\n" \
"v_mfma_f32_16x16x32_bf16 v[128:131], a[68:71], a[4:7], v[128:131]\n" \
"ds_read_b64_tr_b16 a[182:183], v211 offset:29696\n" \
"s_add_u32 m0, %[m0b], 16384\n" \
"v_mfma_f32_16x16x32_bf16 v[128:131], a[72:75], a[8:11], v[128:131]\n" \
"v_exp_f32_e64 v157, v157\n" \
"v_mfma_f32_16x16x32_bf16 v[128:131], a[76:79], a[12:15], v[128:131]\n" \
"v_cvt_pk_bf16_f32 v204, v152, v153\n" \
"v_cvt_pk_bf16_f32 v205, v154, v155\n" \
"v_mfma_f32_16x16x32_bf16 v[132:135], a[92:95], a[12:15], v[160:163]\n" \
"v_exp_f32_e64 v158, v158\n" \
"v_mfma_f32_16x16x32_bf16 v[132:135], a[88:91], a[8:11], v[132:135]\n" \
"ds_read_b64_tr_b16 a[184:185], %[vvb] offset:26112\n" \
"s_waitcnt lgkmcnt(5)\n" \
"v_mfma_f32_16x16x32_bf16 v[132:135], a[84:87], a[4:7], v[132:135]\n" \
"buffer_load_dwordx4 %[kd0], %[srdk], s48 offen offset:0 lds\n" \
"v_mfma_f32_16x16x32_bf16 v[132:135], a[80:83], a[0:3], v[132:135]\n" \
"v_exp_f32_e32 v159, v159\n" \
"v_mfma_f32_16x16x32_bf16 v[64:67], a[128:131], v[216:219], v[64:67]\n" \
"ds_read_b64_tr_b16 a[186:187], %[vvb] offset:30208\n" \
"ds_read_b64_tr_b16 a[188:189], v211 offset:26112\n" \
"v_mfma_f32_16x16x32_bf16 v[64:67], a[160:163], v[200:203], v[64:67]\n" \
"v_exp_f32_e64 v128, v128\n" \
"v_mfma_f32_16x16x32_bf16 v[68:71], a[132:135], v[216:219], v[68:71]\n" \
"v_cvt_pk_bf16_f32 v206, v156, v157\n" \
"v_cvt_pk_bf16_f32 v207, v158, v159\n" \
"v_mfma_f32_16x16x32_bf16 v[68:71], a[164:167], v[200:203], v[68:71]\n" \
"v_exp_f32_e64 v129, v129\n" \
"v_mfma_f32_16x16x32_bf16 v[72:75], a[136:139], v[216:219], v[72:75]\n" \
"ds_read_b64_tr_b16 a[190:191], v211 offset:30208\n" \
"ds_read_b64_tr_b16 a[128:129], %[vvb] offset:32768\n" \
"v_mfma_f32_16x16x32_bf16 v[72:75], a[168:171], v[200:203], v[72:75]\n" \
"v_exp_f32_e64 v130, v130\n" \
"v_mfma_f32_16x16x32_bf16 v[76:79], a[140:143], v[216:219], v[76:79]\n" \
"ds_read_b64_tr_b16 a[130:131], %[vvb] offset:36864\n" \
"ds_read_b64_tr_b16 a[132:133], v211 offset:32768\n" \
"v_mfma_f32_16x16x32_bf16 v[76:79], a[172:175], v[200:203], v[76:79]\n" \
"v_exp_f32_e64 v131, v131\n" \
"v_smfmac_f32_16x16x64_bf16 v[176:179], a[192:195], v[192:199], v228\n" \
"ds_read_b64_tr_b16 a[134:135], v211 offset:36864\n" \
"ds_read_b64_tr_b16 a[136:137], %[vvb] offset:33280\n" \
"v_mfma_f32_16x16x32_bf16 v[136:139], a[64:67], a[16:19], v[164:167]\n" \
"v_exp_f32_e64 v132, v132\n" \
"v_mfma_f32_16x16x32_bf16 v[136:139], a[68:71], a[20:23], v[136:139]\n" \
"ds_read_b64_tr_b16 a[138:139], %[vvb] offset:37376\n" \
"ds_read_b64_tr_b16 a[140:141], v211 offset:33280\n" \
"v_mfma_f32_16x16x32_bf16 v[136:139], a[72:75], a[24:27], v[136:139]\n" \
"v_exp_f32_e64 v133, v133\n" \
"v_mfma_f32_16x16x32_bf16 v[136:139], a[76:79], a[28:31], v[136:139]\n" \
"v_cvt_pk_bf16_f32 v184, v128, v129\n" \
"v_cvt_pk_bf16_f32 v185, v130, v131\n" \
"v_mfma_f32_16x16x32_bf16 v[140:143], a[92:95], a[28:31], v[164:167]\n" \
"v_exp_f32_e64 v134, v134\n" \
"v_mfma_f32_16x16x32_bf16 v[140:143], a[88:91], a[24:27], v[140:143]\n" \
"ds_read_b64_tr_b16 a[142:143], v211 offset:37376\n" \
"s_waitcnt lgkmcnt(8)\n" \
"v_mfma_f32_16x16x32_bf16 v[140:143], a[84:87], a[20:23], v[140:143]\n" \
"buffer_load_dwordx4 v212, %[srdk], s48 offen offset:1024 lds\n" \
"v_mfma_f32_16x16x32_bf16 v[140:143], a[80:83], a[16:19], v[140:143]\n" \
"v_exp_f32_e32 v135, v135\n" \
"v_mfma_f32_16x16x32_bf16 v[80:83], a[144:147], v[216:219], v[80:83]\n" \
"ds_read_b64_tr_b16 a[144:145], %[vvb] offset:33792\n" \
"ds_read_b64_tr_b16 a[146:147], %[vvb] offset:37888\n" \
".if . - adaln_body != 24576\n" \
".error \"size model\"\n" \
".endif\n" \
"Lk5_%=:\n" \
"s_cbranch_execz adaln_kr\n" \
"s_nop 0\n" \
"v_mfma_f32_16x16x32_bf16 v[80:83], a[176:179], v[200:203], v[80:83]\n" \
"v_exp_f32_e64 v136, v136\n" \
"v_mfma_f32_16x16x32_bf16 v[84:87], a[148:151], v[216:219], v[84:87]\n" \
"v_cvt_pk_bf16_f32 v186, v132, v133\n" \
"v_cvt_pk_bf16_f32 v187, v134, v135\n" \
"v_mfma_f32_16x16x32_bf16 v[84:87], a[180:183], v[200:203], v[84:87]\n" \
"v_exp_f32_e64 v137, v137\n" \
"v_mfma_f32_16x16x32_bf16 v[88:91], a[152:155], v[216:219], v[88:91]\n" \
"v_exp_f32_e64 v138, v138\n" \
"v_mfma_f32_16x16x32_bf16 v[88:91], a[184:187], v[200:203], v[88:91]\n" \
"ds_read_b64_tr_b16 a[148:149], v211 offset:33792\n" \
"ds_read_b64_tr_b16 a[150:151], v211 offset:37888\n" \
"v_mfma_f32_16x16x32_bf16 v[92:95], a[156:159], v[216:219], v[92:95]\n" \
"v_exp_f32_e64 v139, v139\n" \
"v_mfma_f32_16x16x32_bf16 v[92:95], a[188:191], v[200:203], v[92:95]\n" \
"ds_read_b64_tr_b16 a[152:153], %[vvb] offset:34304\n" \
"buffer_store_dwordx4 a[196:199], v235, %[srdz], s63 offen offset:1024 sc1\n" \
"v_mfma_f32_16x16x32_bf16 v[144:147], a[64:67], a[32:35], v[168:171]\n" \
"v_exp_f32_e64 v140, v140\n" \
"v_mfma_f32_16x16x32_bf16 v[144:147], a[68:71], a[36:39], v[144:147]\n" \
"ds_read_b64_tr_b16 a[154:155], %[vvb] offset:38400\n" \
"ds_read_b64_tr_b16 a[156:157], v211 offset:34304\n" \
"v_mfma_f32_16x16x32_bf16 v[144:147], a[72:75], a[40:43], v[144:147]\n" \
"v_exp_f32_e64 v141, v141\n" \
"v_mfma_f32_16x16x32_bf16 v[144:147], a[76:79], a[44:47], v[144:147]\n" \
"v_cvt_pk_bf16_f32 v188, v136, v137\n" \
"v_cvt_pk_bf16_f32 v189, v138, v139\n" \
"v_mfma_f32_16x16x32_bf16 v[148:151], a[92:95], a[44:47], v[168:171]\n" \
"v_exp_f32_e64 v142, v142\n" \
"v_mfma_f32_16x16x32_bf16 v[148:151], a[88:91], a[40:43], v[148:151]\n" \
"ds_read_b64_tr_b16 a[158:159], v211 offset:38400\n" \
"s_waitcnt lgkmcnt(8)\n" \
"v_mfma_f32_16x16x32_bf16 v[148:151], a[84:87], a[36:39], v[148:151]\n" \
"buffer_load_dwordx4 v213, %[srdk], s48 offen offset:2048 lds\n" \
"v_mfma_f32_16x16x32_bf16 v[148:151], a[80:83], a[32:35], v[148:151]\n" \
"v_exp_f32_e32 v143, v143\n" \
"v_mfma_f32_16x16x32_bf16 v[0:3], a[160:163], v[192:195], v[0:3]\n" \
"ds_read_b128 a[96:99], %[kb0] offset:40960\n" \
"v_mfma_f32_16x16x32_bf16 v[0:3], a[128:131], v[184:187], v[0:3]\n" \
"v_exp_f32_e64 v144, v144\n" \
"v_mfma_f32_16x16x32_bf16 v[4:7], a[164:167], v[192:195], v[4:7]\n" \
"v_cvt_pk_bf16_f32 v190, v140, v141\n" \
"v_cvt_pk_bf16_f32 v191, v142, v143\n" \
"v_mfma_f32_16x16x32_bf16 v[4:7], a[132:135], v[184:187], v[4:7]\n" \
"v_exp_f32_e64 v145, v145\n" \
"v_mfma_f32_16x16x32_bf16 v[8:11], a[168:171], v[192:195], v[8:11]\n" \
"ds_read_b128 a[100:103], v208 offset:40960\n" \
"v_mfma_f32_16x16x32_bf16 v[8:11], a[136:139], v[184:187], v[8:11]\n" \
"v_exp_f32_e64 v146, v146\n" \
"v_mfma_f32_16x16x32_bf16 v[12:15], a[172:175], v[192:195], v[12:15]\n" \
"ds_read_b128 a[104:107], v209 offset:40960\n" \
"v_mfma_f32_16x16x32_bf16 v[12:15], a[140:143], v[184:187], v[12:15]\n" \
"v_exp_f32_e64 v147, v147\n" \
"v_smfmac_f32_16x16x64_bf16 v[180:183], a[192:195], v[200:207], v228\n" \
"ds_read_b128 a[108:111], v210 offset:40960\n" \
"v_mfma_f32_16x16x32_bf16 v[152:155], a[64:67], a[48:51], v[172:175]\n" \
"v_exp_f32_e64 v148, v148\n" \
"v_mfma_f32_16x16x32_bf16 v[152:155], a[68:71], a[52:55], v[152:155]\n" \
"ds_read_b128 a[124:127], v210 offset:45056\n" \
"v_mfma_f32_16x16x32_bf16 v[152:155], a[72:75], a[56:59], v[152:155]\n" \
"v_exp_f32_e64 v149, v149\n" \
"v_mfma_f32_16x16x32_bf16 v[152:155], a[76:79], a[60:63], v[152:155]\n" \
"v_cvt_pk_bf16_f32 v216, v144, v145\n" \
"v_cvt_pk_bf16_f32 v217, v146, v147\n" \
"v_mfma_f32_16x16x32_bf16 v[156:159], a[92:95], a[60:63], v[172:175]\n" \
"v_exp_f32_e64 v150, v150\n" \
"v_mfma_f32_16x16x32_bf16 v[156:159], a[88:91], a[56:59], v[156:159]\n" \
"ds_read_b128 a[120:123], v209 offset:45056\n" \
"s_waitcnt lgkmcnt(6)\n" \
"v_mfma_f32_16x16x32_bf16 v[156:159], a[84:87], a[52:55], v[156:159]\n" \
"buffer_load_dwordx4 v214, %[srdk], s48 offen offset:3072 lds\n" \
"v_mfma_f32_16x16x32_bf16 v[156:159], a[80:83], a[48:51], v[156:159]\n" \
"v_exp_f32_e32 v151, v151\n" \
"v_mfma_f32_16x16x32_bf16 v[16:19], a[176:179], v[192:195], v[16:19]\n" \
"ds_read_b128 a[116:119], v208 offset:45056\n" \
"v_mfma_f32_16x16x32_bf16 v[16:19], a[144:147], v[184:187], v[16:19]\n" \
"v_exp_f32_e64 v152, v152\n" \
"v_mfma_f32_16x16x32_bf16 v[20:23], a[180:183], v[192:195], v[20:23]\n" \
"v_cvt_pk_bf16_f32 v218, v148, v149\n" \
"v_cvt_pk_bf16_f32 v219, v150, v151\n" \
"v_mfma_f32_16x16x32_bf16 v[20:23], a[148:151], v[184:187], v[20:23]\n" \
"v_exp_f32_e64 v153, v153\n" \
"v_mfma_f32_16x16x32_bf16 v[24:27], a[184:187], v[192:195], v[24:27]\n" \
"v_exp_f32_e64 v154, v154\n" \
"v_mfma_f32_16x16x32_bf16 v[24:27], a[152:155], v[184:187], v[24:27]\n" \
"ds_read_b128 a[112:115], %[kb0] offset:45056\n" \
"s_add_u32 s48, s48, 0x4000\n" \
"v_mfma_f32_16x16x32_bf16 v[28:31], a[188:191], v[192:195], v[28:31]\n" \
"v_exp_f32_e64 v155, v155\n" \
"v_mfma_f32_16x16x32_bf16 v[28:31], a[156:159], v[184:187], v[28:31]\n" \
"s_waitcnt vmcnt(16) lgkmcnt(2)\n" \
"s_barrier\n" \
"v_mfma_f32_16x16x32_bf16 v[128:131], a[96:99], a[0:3], v[160:163]\n" \
"v_exp_f32_e64 v156, v156\n" \
"v_mfma_f32_16x16x32_bf16 v[128:131], a[100:103], a[4:7], v[128:131]\n" \
"s_add_u32 m0, %[m0b], 81920\n" \
"v_mfma_f32_16x16x32_bf16 v[128:131], a[104:107], a[8:11], v[128:131]\n" \
"v_exp_f32_e64 v157, v157\n" \
"v_mfma_f32_16x16x32_bf16 v[128:131], a[108:111], a[12:15], v[128:131]\n" \
"v_cvt_pk_bf16_f32 v220, v152, v153\n" \
"v_cvt_pk_bf16_f32 v221, v154, v155\n" \
"v_mfma_f32_16x16x32_bf16 v[132:135], a[124:127], a[12:15], v[160:163]\n" \
"v_exp_f32_e64 v158, v158\n" \
"v_mfma_f32_16x16x32_bf16 v[132:135], a[120:123], a[8:11], v[132:135]\n" \
"s_waitcnt lgkmcnt(0)\n" \
"v_mfma_f32_16x16x32_bf16 v[132:135], a[116:119], a[4:7], v[132:135]\n" \
"buffer_load_dwordx4 %[vd0], %[srdv], s48 offen offset:0 lds\n" \
"v_mfma_f32_16x16x32_bf16 v[132:135], a[112:115], a[0:3], v[132:135]\n" \
"v_exp_f32_e32 v159, v159\n" \
"v_mfma_f32_16x16x32_bf16 v[32:35], a[160:163], v[196:199], v[32:35]\n" \
"ds_read_b128 a[64:67], %[kb0] offset:49152\n" \
"v_mfma_f32_16x16x32_bf16 v[32:35], a[128:131], v[188:191], v[32:35]\n" \
"v_exp_f32_e64 v128, v128\n" \
"v_mfma_f32_16x16x32_bf16 v[36:39], a[164:167], v[196:199], v[36:39]\n" \
"v_cvt_pk_bf16_f32 v222, v156, v157\n" \
"v_cvt_pk_bf16_f32 v223, v158, v159\n" \
"v_mfma_f32_16x16x32_bf16 v[36:39], a[132:135], v[188:191], v[36:39]\n" \
"v_exp_f32_e64 v129, v129\n" \
"v_mfma_f32_16x16x32_bf16 v[40:43], a[168:171], v[196:199], v[40:43]\n" \
"ds_read_b128 a[68:71], v208 offset:49152\n" \
"v_mfma_f32_16x16x32_bf16 v[40:43], a[136:139], v[188:191], v[40:43]\n" \
"v_exp_f32_e64 v130, v130\n" \
"v_mfma_f32_16x16x32_bf16 v[44:47], a[172:175], v[196:199], v[44:47]\n" \
"ds_read_b128 a[72:75], v209 offset:49152\n" \
"v_mfma_f32_16x16x32_bf16 v[44:47], a[140:143], v[188:191], v[44:47]\n" \
"v_exp_f32_e64 v131, v131\n" \
"v_smfmac_f32_16x16x64_bf16 v[176:179], a[192:195], v[184:191], v228\n" \
"v_mfma_f32_16x16x32_bf16 v[136:139], a[96:99], a[16:19], v[164:167]\n" \
"v_exp_f32_e64 v132, v132\n" \
"v_mfma_f32_16x16x32_bf16 v[136:139], a[100:103], a[20:23], v[136:139]\n" \
"ds_read_b128 a[76:79], v210 offset:49152\n" \
"v_mfma_f32_16x16x32_bf16 v[136:139], a[104:107], a[24:27], v[136:139]\n" \
"v_exp_f32_e64 v133, v133\n" \
"v_mfma_f32_16x16x32_bf16 v[136:139], a[108:111], a[28:31], v[136:139]\n" \
"v_cvt_pk_bf16_f32 v192, v128, v129\n" \
"v_cvt_pk_bf16_f32 v193, v130, v131\n" \
"v_mfma_f32_16x16x32_bf16 v[140:143], a[124:127], a[28:31], v[164:167]\n" \
"v_exp_f32_e64 v134, v134\n" \
"v_mfma_f32_16x16x32_bf16 v[140:143], a[120:123], a[24:27], v[140:143]\n" \
"ds_read_b128 a[92:95], v210 offset:53248\n" \
"v_mfma_f32_16x16x32_bf16 v[140:143], a[116:119], a[20:23], v[140:143]\n" \
"buffer_load_dwordx4 v215, %[srdv], s48 offen offset:1024 lds\n" \
"v_mfma_f32_16x16x32_bf16 v[140:143], a[112:115], a[16:19], v[140:143]\n" \
"v_exp_f32_e64 v135, v135\n" \
"v_mfma_f32_16x16x32_bf16 v[48:51], a[176:179], v[196:199], v[48:51]\n" \
"ds_read_b128 a[88:91], v209 offset:53248\n" \
"v_mfma_f32_16x16x32_bf16 v[48:51], a[144:147], v[188:191], v[48:51]\n" \
"v_exp_f32_e64 v136, v136\n" \
"v_mfma_f32_16x16x32_bf16 v[52:55], a[180:183], v[196:199], v[52:55]\n" \
"v_cvt_pk_bf16_f32 v194, v132, v133\n" \
"v_cvt_pk_bf16_f32 v195, v134, v135\n" \
"v_mfma_f32_16x16x32_bf16 v[52:55], a[148:151], v[188:191], v[52:55]\n" \
"v_exp_f32_e64 v137, v137\n" \
"v_mfma_f32_16x16x32_bf16 v[56:59], a[184:187], v[196:199], v[56:59]\n" \
"v_exp_f32_e64 v138, v138\n" \
"v_mfma_f32_16x16x32_bf16 v[56:59], a[152:155], v[188:191], v[56:59]\n" \
"ds_read_b128 a[84:87], v208 offset:53248\n" \
"v_mfma_f32_16x16x32_bf16 v[60:63], a[188:191], v[196:199], v[60:63]\n" \
"v_exp_f32_e64 v139, v139\n" \
"v_mfma_f32_16x16x32_bf16 v[60:63], a[156:159], v[188:191], v[60:63]\n" \
"s_cmp_lg_u32 %[wid], 1\n" \
"s_cbranch_scc1 LtB5_%=\n" \
"global_load_dword v237, v236, s[58:59]\n" \
"s_add_u32 s61, s48, 49152\n" \
"s_mov_b32 exec_hi, 0\n" \
"s_nop 0\n" \
"buffer_load_dword v237, v224, %[srdp], s61 offen\n" \
"s_mov_b32 exec_hi, -1\n" \
"s_nop 0\n" \
"LtB5_%=:\n" \
"v_mfma_f32_16x16x32_bf16 v[144:147], a[96:99], a[32:35], v[168:171]\n" \
"v_exp_f32_e64 v140, v140\n" \
"v_mfma_f32_16x16x32_bf16 v[144:147], a[100:103], a[36:39], v[144:147]\n" \
"ds_read_b128 a[80:83], %[kb0] offset:53248\n" \
"v_mfma_f32_16x16x32_bf16 v[144:147], a[104:107], a[40:43], v[144:147]\n" \
"v_exp_f32_e64 v141, v141\n" \
"v_mfma_f32_16x16x32_bf16 v[144:147], a[108:111], a[44:47], v[144:147]\n" \
"v_cvt_pk_bf16_f32 v196, v136, v137\n" \
"v_cvt_pk_bf16_f32 v197, v138, v139\n" \
"v_mfma_f32_16x16x32_bf16 v[148:151], a[124:127], a[44:47], v[168:171]\n" \
"v_exp_f32_e64 v142, v142\n" \
"v_mfma_f32_16x16x32_bf16 v[148:151], a[120:123], a[40:43], v[148:151]\n" \
"v_mfma_f32_16x16x32_bf16 v[148:151], a[116:119], a[36:39], v[148:151]\n" \
"buffer_load_dwordx4 %[vd0], %[srdv], s48 offen offset:2048 lds\n" \
"v_mfma_f32_16x16x32_bf16 v[148:151], a[112:115], a[32:35], v[148:151]\n" \
"v_exp_f32_e64 v143, v143\n" \
"v_mfma_f32_16x16x32_bf16 v[96:99], a[160:163], v[204:207], v[96:99]\n" \
"ds_read_b64_tr_b16 a[160:161], %[vvb] offset:40960\n" \
"v_mfma_f32_16x16x32_bf16 v[96:99], a[128:131], v[220:223], v[96:99]\n" \
"v_exp_f32_e64 v144, v144\n" \
"v_mfma_f32_16x16x32_bf16 v[100:103], a[164:167], v[204:207], v[100:103]\n" \
"v_cvt_pk_bf16_f32 v198, v140, v141\n" \
"v_cvt_pk_bf16_f32 v199, v142, v143\n" \
"v_mfma_f32_16x16x32_bf16 v[100:103], a[132:135], v[220:223], v[100:103]\n" \
"v_exp_f32_e64 v145, v145\n" \
"v_mfma_f32_16x16x32_bf16 v[104:107], a[168:171], v[204:207], v[104:107]\n" \
"ds_read_b64_tr_b16 a[162:163], %[vvb] offset:45056\n" \
"ds_read_b64_tr_b16 a[164:165], v211 offset:40960\n" \
"v_mfma_f32_16x16x32_bf16 v[104:107], a[136:139], v[220:223], v[104:107]\n" \
"v_exp_f32_e64 v146, v146\n" \
"v_mfma_f32_16x16x32_bf16 v[108:111], a[172:175], v[204:207], v[108:111]\n" \
"ds_read_b64_tr_b16 a[166:167], v211 offset:45056\n" \
"v_mfma_f32_16x16x32_bf16 v[108:111], a[140:143], v[220:223], v[108:111]\n" \
"v_exp_f32_e64 v147, v147\n" \
"v_smfmac_f32_16x16x64_bf16 v[180:183], a[192:195], v[216:223], v228\n" \
"ds_read_b64_tr_b16 a[168:169], %[vvb] offset:41472\n" \
"v_mfma_f32_16x16x32_bf16 v[152:155], a[96:99], a[48:51], v[172:175]\n" \
"v_exp_f32_e64 v148, v148\n" \
"v_mfma_f32_16x16x32_bf16 v[152:155], a[100:103], a[52:55], v[152:155]\n" \
"ds_read_b64_tr_b16 a[170:171], %[vvb] offset:45568\n" \
"v_mfma_f32_16x16x32_bf16 v[152:155], a[104:107], a[56:59], v[152:155]\n" \
"v_exp_f32_e64 v149, v149\n" \
"v_mfma_f32_16x16x32_bf16 v[152:155], a[108:111], a[60:63], v[152:155]\n" \
"v_cvt_pk_bf16_f32 v200, v144, v145\n" \
"v_cvt_pk_bf16_f32 v201, v146, v147\n" \
"v_mfma_f32_16x16x32_bf16 v[156:159], a[124:127], a[60:63], v[172:175]\n" \
"v_exp_f32_e64 v150, v150\n" \
"v_mfma_f32_16x16x32_bf16 v[156:159], a[120:123], a[56:59], v[156:159]\n" \
"ds_read_b64_tr_b16 a[172:173], v211 offset:41472\n" \
"v_mfma_f32_16x16x32_bf16 v[156:159], a[116:119], a[52:55], v[156:159]\n" \
"buffer_load_dwordx4 v215, %[srdv], s48 offen offset:3072 lds\n" \
"v_mfma_f32_16x16x32_bf16 v[156:159], a[112:115], a[48:51], v[156:159]\n" \
"v_exp_f32_e64 v151, v151\n" \
"v_mfma_f32_16x16x32_bf16 v[112:115], a[176:179], v[204:207], v[112:115]\n" \
"ds_read_b64_tr_b16 a[174:175], v211 offset:45568\n" \
"ds_read_b64_tr_b16 a[176:177], %[vvb] offset:41984\n" \
"v_mfma_f32_16x16x32_bf16 v[112:115], a[144:147], v[220:223], v[112:115]\n" \
"v_exp_f32_e64 v152, v152\n" \
"v_mfma_f32_16x16x32_bf16 v[116:119], a[180:183], v[204:207], v[116:119]\n" \
"v_cvt_pk_bf16_f32 v202, v148, v149\n" \
"v_cvt_pk_bf16_f32 v203, v150, v151\n" \
"v_mfma_f32_16x16x32_bf16 v[116:119], a[148:151], v[220:223], v[116:119]\n" \
"v_exp_f32_e64 v153, v153\n" \
"v_mfma_f32_16x16x32_bf16 v[120:123], a[184:187], v[204:207], v[120:123]\n" \
"v_exp_f32_e64 v154, v154\n" \
"v_mfma_f32_16x16x32_bf16 v[120:123], a[152:155], v[220:223], v[120:123]\n" \
"ds_read_b64_tr_b16 a[178:179], %[vvb] offset:46080\n" \
"v_mfma_f32_16x16x32_bf16 v[124:127], a[188:191], v[204:207], v[124:127]\n" \
"v_exp_f32_e64 v155, v155\n" \
"v_mfma_f32_16x16x32_bf16 v[124:127], a[156:159], v[220:223], v[124:127]\n" \
"ds_read_b64_tr_b16 a[180:181], v211 offset:41984\n" \
"s_waitcnt lgkmcnt(13)\n" \
"v_mfma_f32_16x16x32_bf16 v[128:131], a[64:67], a[0:3], v[160:163]\n" \
"v_exp_f32_e32 v156, v156\n" \
"v_mfma_f32_16x16x32_bf16 v[128:131], a[68:71], a[4:7], v[128:131]\n" \
"ds_read_b64_tr_b16 a[182:183], v211 offset:46080\n" \
"s_add_u32 m0, %[m0b], 32768\n" \
"v_mfma_f32_16x16x32_bf16 v[128:131], a[72:75], a[8:11], v[128:131]\n" \
"v_exp_f32_e64 v157, v157\n" \
"v_mfma_f32_16x16x32_bf16 v[128:131], a[76:79], a[12:15], v[128:131]\n" \
"v_cvt_pk_bf16_f32 v204, v152, v153\n" \
"v_cvt_pk_bf16_f32 v205, v154, v155\n" \
"v_mfma_f32_16x16x32_bf16 v[132:135], a[92:95], a[12:15], v[160:163]\n" \
"v_exp_f32_e64 v158, v158\n" \
"v_mfma_f32_16x16x32_bf16 v[132:135], a[88:91], a[8:11], v[132:135]\n" \
"ds_read_b64_tr_b16 a[184:185], %[vvb] offset:42496\n" \
"s_waitcnt lgkmcnt(5)\n" \
"v_mfma_f32_16x16x32_bf16 v[132:135], a[84:87], a[4:7], v[132:135]\n" \
"buffer_load_dwordx4 %[kd0], %[srdk], s48 offen offset:0 lds\n" \
"v_mfma_f32_16x16x32_bf16 v[132:135], a[80:83], a[0:3], v[132:135]\n" \
"v_exp_f32_e32 v159, v159\n" \
"v_mfma_f32_16x16x32_bf16 v[64:67], a[128:131], v[216:219], v[64:67]\n" \
"ds_read_b64_tr_b16 a[186:187], %[vvb] offset:46592\n" \
"ds_read_b64_tr_b16 a[188:189], v211 offset:42496\n" \
"v_mfma_f32_16x16x32_bf16 v[64:67], a[160:163], v[200:203], v[64:67]\n" \
"v_exp_f32_e64 v128, v128\n" \
"v_mfma_f32_16x16x32_bf16 v[68:71], a[132:135], v[216:219], v[68:71]\n" \
"v_cvt_pk_bf16_f32 v206, v156, v157\n" \
"v_cvt_pk_bf16_f32 v207, v158, v159\n" \
"v_mfma_f32_16x16x32_bf16 v[68:71], a[164:167], v[200:203], v[68:71]\n" \
"v_exp_f32_e64 v129, v129\n" \
"v_mfma_f32_16x16x32_bf16 v[72:75], a[136:139], v[216:219], v[72:75]\n" \
"ds_read_b64_tr_b16 a[190:191], v211 offset:46592\n" \
"ds_read_b64_tr_b16 a[128:129], %[vvb] offset:49152\n" \
"v_mfma_f32_16x16x32_bf16 v[72:75], a[168:171], v[200:203], v[72:75]\n" \
"v_exp_f32_e64 v130, v130\n" \
"v_mfma_f32_16x16x32_bf16 v[76:79], a[140:143], v[216:219], v[76:79]\n" \
"ds_read_b64_tr_b16 a[130:131], %[vvb] offset:53248\n" \
"ds_read_b64_tr_b16 a[132:133], v211 offset:49152\n" \
"v_mfma_f32_16x16x32_bf16 v[76:79], a[172:175], v[200:203], v[76:79]\n" \
"v_exp_f32_e64 v131, v131\n" \
"v_smfmac_f32_16x16x64_bf16 v[176:179], a[192:195], v[192:199], v228\n" \
"ds_read_b64_tr_b16 a[134:135], v211 offset:53248\n" \
"ds_read_b64_tr_b16 a[136:137], %[vvb] offset:49664\n" \
"buffer_store_dwordx4 a[196:199], v235, %[srdz], s62 offen offset:1024 sc1\n" \
"v_mfma_f32_16x16x32_bf16 v[136:139], a[64:67], a[16:19], v[164:167]\n" \
"v_exp_f32_e64 v132, v132\n" \
"v_mfma_f32_16x16x32_bf16 v[136:139], a[68:71], a[20:23], v[136:139]\n" \
"ds_read_b64_tr_b16 a[138:139], %[vvb] offset:53760\n" \
"ds_read_b64_tr_b16 a[140:141], v211 offset:49664\n" \
"v_mfma_f32_16x16x32_bf16 v[136:139], a[72:75], a[24:27], v[136:139]\n" \
"v_exp_f32_e64 v133, v133\n" \
"v_mfma_f32_16x16x32_bf16 v[136:139], a[76:79], a[28:31], v[136:139]\n" \
"v_cvt_pk_bf16_f32 v184, v128, v129\n" \
"v_cvt_pk_bf16_f32 v185, v130, v131\n" \
"v_mfma_f32_16x16x32_bf16 v[140:143], a[92:95], a[28:31], v[164:167]\n" \
"v_exp_f32_e64 v134, v134\n" \
"v_mfma_f32_16x16x32_bf16 v[140:143], a[88:91], a[24:27], v[140:143]\n" \
"ds_read_b64_tr_b16 a[142:143], v211 offset:53760\n" \
"s_waitcnt lgkmcnt(8)\n" \
"v_mfma_f32_16x16x32_bf16 v[140:143], a[84:87], a[20:23], v[140:143]\n" \
"buffer_load_dwordx4 v212, %[srdk], s48 offen offset:1024 lds\n" \
"v_mfma_f32_16x16x32_bf16 v[140:143], a[80:83], a[16:19], v[140:143]\n" \
"v_exp_f32_e32 v135, v135\n" \
"v_mfma_f32_16x16x32_bf16 v[80:83], a[144:147], v[216:219], v[80:83]\n" \
"ds_read_b64_tr_b16 a[144:145], %[vvb] offset:50176\n" \
"ds_read_b64_tr_b16 a[146:147], %[vvb] offset:54272\n" \
"v_mfma_f32_16x16x32_bf16 v[80:83], a[176:179], v[200:203], v[80:83]\n" \
"v_exp_f32_e64 v136, v136\n" \
"v_mfma_f32_16x16x32_bf16 v[84:87], a[148:151], v[216:219], v[84:87]\n" \
"v_cvt_pk_bf16_f32 v186, v132, v133\n" \
"v_cvt_pk_bf16_f32 v187, v134, v135\n" \
"v_mfma_f32_16x16x32_bf16 v[84:87], a[180:183], v[200:203], v[84:87]\n" \
"v_exp_f32_e64 v137, v137\n" \
"v_mfma_f32_16x16x32_bf16 v[88:91], a[152:155], v[216:219], v[88:91]\n" \
"v_exp_f32_e64 v138, v138\n" \
"v_mfma_f32_16x16x32_bf16 v[88:91], a[184:187], v[200:203], v[88:91]\n" \
"ds_read_b64_tr_b16 a[148:149], v211 offset:50176\n" \
"ds_read_b64_tr_b16 a[150:151], v211 offset:54272\n" \
"v_mfma_f32_16x16x32_bf16 v[92:95], a[156:159], v[216:219], v[92:95]\n" \
"v_exp_f32_e64 v139, v139\n" \
"v_mfma_f32_16x16x32_bf16 v[92:95], a[188:191], v[200:203], v[92:95]\n" \
"ds_read_b64_tr_b16 a[152:153], %[vvb] offset:50688\n" \
"global_load_dword v237, %[vct], %[pct]\n" \
"v_mfma_f32_16x16x32_bf16 v[144:147], a[64:67], a[32:35], v[168:171]\n" \
"v_exp_f32_e64 v140, v140\n" \
"v_mfma_f32_16x16x32_bf16 v[144:147], a[68:71], a[36:39], v[144:147]\n" \
"ds_read_b64_tr_b16 a[154:155], %[vvb] offset:54784\n" \
"ds_read_b64_tr_b16 a[156:157], v211 offset:50688\n" \
"v_mfma_f32_16x16x32_bf16 v[144:147], a[72:75], a[40:43], v[144:147]\n" \
"v_exp_f32_e64 v141, v141\n" \
"v_mfma_f32_16x16x32_bf16 v[144:147], a[76:79], a[44:47], v[144:147]\n" \
"v_cvt_pk_bf16_f32 v188, v136, v137\n" \
"v_cvt_pk_bf16_f32 v189, v138, v139\n" \
"v_mfma_f32_16x16x32_bf16 v[148:151], a[92:95], a[44:47], v[168:171]\n" \
"v_exp_f32_e64 v142, v142\n" \
"v_mfma_f32_16x16x32_bf16 v[148:151], a[88:91], a[40:43], v[148:151]\n" \
"ds_read_b64_tr_b16 a[158:159], v211 offset:54784\n" \
"s_waitcnt lgkmcnt(8)\n" \
"v_mfma_f32_16x16x32_bf16 v[148:151], a[84:87], a[36:39], v[148:151]\n" \
"buffer_load_dwordx4 v213, %[srdk], s48 offen offset:2048 lds\n" \
"v_mfma_f32_16x16x32_bf16 v[148:151], a[80:83], a[32:35], v[148:151]\n" \
"v_exp_f32_e32 v143, v143\n" \
"v_mfma_f32_16x16x32_bf16 v[0:3], a[160:163], v[192:195], v[0:3]\n" \
"ds_read_b128 a[96:99], %[kb0] offset:57344\n" \
"v_mfma_f32_16x16x32_bf16 v[0:3], a[128:131], v[184:187], v[0:3]\n" \
"v_exp_f32_e64 v144, v144\n" \
"v_mfma_f32_16x16x32_bf16 v[4:7], a[164:167], v[192:195], v[4:7]\n" \
"v_cvt_pk_bf16_f32 v190, v140, v141\n" \
"v_cvt_pk_bf16_f32 v191, v142, v143\n" \
"v_mfma_f32_16x16x32_bf16 v[4:7], a[132:135], v[184:187], v[4:7]\n" \
"v_exp_f32_e64 v145, v145\n" \
"v_mfma_f32_16x16x32_bf16 v[8:11], a[168:171], v[192:195], v[8:11]\n" \
"ds_read_b128 a[100:103], v208 offset:57344\n" \
"v_mfma_f32_16x16x32_bf16 v[8:11], a[136:139], v[184:187], v[8:11]\n" \
"v_exp_f32_e64 v146, v146\n" \
"v_mfma_f32_16x16x32_bf16 v[12:15], a[172:175], v[192:195], v[12:15]\n" \
"ds_read_b128 a[104:107], v209 offset:57344\n" \
"v_mfma_f32_16x16x32_bf16 v[12:15], a[140:143], v[184:187], v[12:15]\n" \
"v_exp_f32_e64 v147, v147\n" \
"v_smfmac_f32_16x16x64_bf16 v[180:183], a[192:195], v[200:207], v228\n" \
"ds_read_b128 a[108:111], v210 offset:57344\n" \
"v_mfma_f32_16x16x32_bf16 v[152:155], a[64:67], a[48:51], v[172:175]\n" \
"v_exp_f32_e64 v148, v148\n" \
"v_mfma_f32_16x16x32_bf16 v[152:155], a[68:71], a[52:55], v[152:155]\n" \
"ds_read_b128 a[124:127], v210 offset:61440\n" \
"v_mfma_f32_16x16x32_bf16 v[152:155], a[72:75], a[56:59], v[152:155]\n" \
"v_exp_f32_e64 v149, v149\n" \
"v_mfma_f32_16x16x32_bf16 v[152:155], a[76:79], a[60:63], v[152:155]\n" \
"v_cvt_pk_bf16_f32 v216, v144, v145\n" \
"v_cvt_pk_bf16_f32 v217, v146, v147\n" \
"v_mfma_f32_16x16x32_bf16 v[156:159], a[92:95], a[60:63], v[172:175]\n" \
"v_exp_f32_e64 v150, v150\n" \
"v_mfma_f32_16x16x32_bf16 v[156:159], a[88:91], a[56:59], v[156:159]\n" \
"ds_read_b128 a[120:123], v209 offset:61440\n" \
"s_waitcnt lgkmcnt(6)\n" \
"v_mfma_f32_16x16x32_bf16 v[156:159], a[84:87], a[52:55], v[156:159]\n" \
"buffer_load_dwordx4 v214, %[srdk], s48 offen offset:3072 lds\n" \
"v_mfma_f32_16x16x32_bf16 v[156:159], a[80:83], a[48:51], v[156:159]\n" \
"v_exp_f32_e32 v151, v151\n" \
"v_mfma_f32_16x16x32_bf16 v[16:19], a[176:179], v[192:195], v[16:19]\n" \
"ds_read_b128 a[116:119], v208 offset:61440\n" \
"v_mfma_f32_16x16x32_bf16 v[16:19], a[144:147], v[184:187], v[16:19]\n" \
"v_exp_f32_e64 v152, v152\n" \
"v_mfma_f32_16x16x32_bf16 v[20:23], a[180:183], v[192:195], v[20:23]\n" \
"v_cvt_pk_bf16_f32 v218, v148, v149\n" \
"v_cvt_pk_bf16_f32 v219, v150, v151\n" \
"v_mfma_f32_16x16x32_bf16 v[20:23], a[148:151], v[184:187], v[20:23]\n" \
"v_exp_f32_e64 v153, v153\n" \
"v_mfma_f32_16x16x32_bf16 v[24:27], a[184:187], v[192:195], v[24:27]\n" \
"v_exp_f32_e64 v154, v154\n" \
"v_mfma_f32_16x16x32_bf16 v[24:27], a[152:155], v[184:187], v[24:27]\n" \
"ds_read_b128 a[112:115], %[kb0] offset:61440\n" \
"s_add_u32 s48, s48, 0x4000\n" \
"s_mov_b32 m0, s44\n" \
"s_addk_i32 s44, 0x400\n" \
"buffer_load_dwordx4 v229, s[64:67], s45 offen offset:0 lds\n" \
"s_add_u32 s45, s45, 229376\n" \
"v_mfma_f32_16x16x32_bf16 v[28:31], a[188:191], v[192:195], v[28:31]\n" \
"v_exp_f32_e64 v155, v155\n" \
"v_mfma_f32_16x16x32_bf16 v[28:31], a[156:159], v[184:187], v[28:31]\n" \
"s_waitcnt vmcnt(16) lgkmcnt(2)\n" \
"s_barrier\n" \
"v_mfma_f32_16x16x32_bf16 v[128:131], a[96:99], a[0:3], v[160:163]\n" \
"v_exp_f32_e64 v156, v156\n" \
"v_mfma_f32_16x16x32_bf16 v[128:131], a[100:103], a[4:7], v[128:131]\n" \
"s_add_u32 m0, %[m0b], 98304\n" \
"v_mfma_f32_16x16x32_bf16 v[128:131], a[104:107], a[8:11], v[128:131]\n" \
"v_exp_f32_e64 v157, v157\n" \
"v_mfma_f32_16x16x32_bf16 v[128:131], a[108:111], a[12:15], v[128:131]\n" \
"v_cvt_pk_bf16_f32 v220, v152, v153\n" \
"v_cvt_pk_bf16_f32 v221, v154, v155\n" \
"v_mfma_f32_16x16x32_bf16 v[132:135], a[124:127], a[12:15], v[160:163]\n" \
"v_exp_f32_e64 v158, v158\n" \
"v_mfma_f32_16x16x32_bf16 v[132:135], a[120:123], a[8:11], v[132:135]\n" \
"s_waitcnt lgkmcnt(0)\n" \
"v_mfma_f32_16x16x32_bf16 v[132:135], a[116:119], a[4:7], v[132:135]\n" \
"buffer_load_dwordx4 %[vd0], %[srdv], s48 offen offset:0 lds\n" \
"v_mfma_f32_16x16x32_bf16 v[132:135], a[112:115], a[0:3], v[132:135]\n" \
"v_exp_f32_e32 v159, v159\n" \
"v_mfma_f32_16x16x32_bf16 v[32:35], a[160:163], v[196:199], v[32:35]\n" \
"ds_read_b128 a[64:67], %[kb0] offset:0\n" \
"v_mfma_f32_16x16x32_bf16 v[32:35], a[128:131], v[188:191], v[32:35]\n" \
"v_exp_f32_e64 v128, v128\n" \
"v_mfma_f32_16x16x32_bf16 v[36:39], a[164:167], v[196:199], v[36:39]\n" \
"v_cvt_pk_bf16_f32 v222, v156, v157\n" \
"v_cvt_pk_bf16_f32 v223, v158, v159\n" \
"v_mfma_f32_16x16x32_bf16 v[36:39], a[132:135], v[188:191], v[36:39]\n" \
"v_exp_f32_e64 v129, v129\n" \
"v_mfma_f32_16x16x32_bf16 v[40:43], a[168:171], v[196:199], v[40:43]\n" \
"ds_read_b128 a[68:71], v208 offset:0\n" \
"v_mfma_f32_16x16x32_bf16 v[40:43], a[136:139], v[188:191], v[40:43]\n" \
"v_exp_f32_e64 v130, v130\n" \
"v_mfma_f32_16x16x32_bf16 v[44:47], a[172:175], v[196:199], v[44:47]\n" \
"ds_read_b128 a[72:75], v209 offset:0\n" \
"v_mfma_f32_16x16x32_bf16 v[44:47], a[140:143], v[188:191], v[44:47]\n" \
"v_exp_f32_e64 v131, v131\n" \
"v_smfmac_f32_16x16x64_bf16 v[176:179], a[192:195], v[184:191], v228\n" \
"buffer_store_dwordx4 a[196:199], v235, %[srdz], s63 offen offset:2048 sc1\n" \
"s_add_u32 s63, s63, 3072\n" \
"v_mfma_f32_16x16x32_bf16 v[136:139], a[96:99], a[16:19], v[164:167]\n" \
"v_exp_f32_e64 v132, v132\n" \
"v_mfma_f32_16x16x32_bf16 v[136:139], a[100:103], a[20:23], v[136:139]\n" \
"ds_read_b128 a[76:79], v210 offset:0\n" \
"v_mfma_f32_16x16x32_bf16 v[136:139], a[104:107], a[24:27], v[136:139]\n" \
"v_exp_f32_e64 v133, v133\n" \
"v_mfma_f32_16x16x32_bf16 v[136:139], a[108:111], a[28:31], v[136:139]\n" \
"v_cvt_pk_bf16_f32 v192, v128, v129\n" \
"v_cvt_pk_bf16_f32 v193, v130, v131\n" \
"v_mfma_f32_16x16x32_bf16 v[140:143], a[124:127], a[28:31], v[164:167]\n" \
"v_exp_f32_e64 v134, v134\n" \
"v_mfma_f32_16x16x32_bf16 v[140:143], a[120:123], a[24:27], v[140:143]\n" \
"ds_read_b128 a[92:95], v210 offset:4096\n" \
"v_mfma_f32_16x16x32_bf16 v[140:143], a[116:119], a[20:23], v[140:143]\n" \
"buffer_load_dwordx4 v215, %[srdv], s48 offen offset:1024 lds\n" \
"v_mfma_f32_16x16x32_bf16 v[140:143], a[112:115], a[16:19], v[140:143]\n" \
"v_exp_f32_e64 v135, v135\n" \
"v_mfma_f32_16x16x32_bf16 v[48:51], a[176:179], v[196:199], v[48:51]\n" \
"ds_read_b128 a[88:91], v209 offset:4096\n" \
"v_mfma_f32_16x16x32_bf16 v[48:51], a[144:147], v[188:191], v[48:51]\n" \
"v_exp_f32_e64 v136, v136\n" \
"v_mfma_f32_16x16x32_bf16 v[52:55], a[180:183], v[196:199], v[52:55]\n" \
"v_cvt_pk_bf16_f32 v194, v132, v133\n" \
"v_cvt_pk_bf16_f32 v195, v134, v135\n" \
"v_mfma_f32_16x16x32_bf16 v[52:55], a[148:151], v[188:191], v[52:55]\n" \
"v_exp_f32_e64 v137, v137\n" \
"v_mfma_f32_16x16x32_bf16 v[56:59], a[184:187], v[196:199], v[56:59]\n" \
"v_exp_f32_e64 v138, v138\n" \
"v_mfma_f32_16x16x32_bf16 v[56:59], a[152:155], v[188:191], v[56:59]\n" \
"ds_read_b128 a[84:87], v208 offset:4096\n" \
"v_mfma_f32_16x16x32_bf16 v[60:63], a[188:191], v[196:199], v[60:63]\n" \
"v_exp_f32_e64 v139, v139\n" \
"v_mfma_f32_16x16x32_bf16 v[60:63], a[156:159], v[188:191], v[60:63]\n" \
"s_cmp_lg_u32 %[wid], 2\n" \
"s_cbranch_scc1 LtB7_%=\n" \
"global_load_dword v237, v236, s[58:59]\n" \
"s_add_u32 s61, s48, 49152\n" \
"s_mov_b32 exec_hi, 0\n" \
"s_nop 0\n" \
"buffer_load_dword v237, v224, %[srdp], s61 offen\n" \
"s_mov_b32 exec_hi, -1\n" \
"s_nop 0\n" \
"LtB7_%=:\n" \
"v_mfma_f32_16x16x32_bf16 v[144:147], a[96:99], a[32:35], v[168:171]\n" \
"v_exp_f32_e64 v140, v140\n" \
"v_mfma_f32_16x16x32_bf16 v[144:147], a[100:103], a[36:39], v[144:147]\n" \
"ds_read_b128 a[80:83], %[kb0] offset:4096\n" \
"v_mfma_f32_16x16x32_bf16 v[144:147], a[104:107], a[40:43], v[144:147]\n" \
"v_exp_f32_e64 v141, v141\n" \
"v_mfma_f32_16x16x32_bf16 v[144:147], a[108:111], a[44:47], v[144:147]\n" \
"v_cvt_pk_bf16_f32 v196, v136, v137\n" \
"v_cvt_pk_bf16_f32 v197, v138, v139\n" \
"v_mfma_f32_16x16x32_bf16 v[148:151], a[124:127], a[44:47], v[168:171]\n" \
"v_exp_f32_e64 v142, v142\n" \
"v_mfma_f32_16x16x32_bf16 v[148:151], a[120:123], a[40:43], v[148:151]\n" \
"v_mfma_f32_16x16x32_bf16 v[148:151], a[116:119], a[36:39], v[148:151]\n" \
"buffer_load_dwordx4 %[vd0], %[srdv], s48 offen offset:2048 lds\n" \
"v_mfma_f32_16x16x32_bf16 v[148:151], a[112:115], a[32:35], v[148:151]\n" \
"v_exp_f32_e64 v143, v143\n" \
"v_mfma_f32_16x16x32_bf16 v[96:99], a[160:163], v[204:207], v[96:99]\n" \
"ds_read_b64_tr_b16 a[160:161], %[vvb] offset:57344\n" \
"v_mfma_f32_16x16x32_bf16 v[96:99], a[128:131], v[220:223], v[96:99]\n" \
"v_exp_f32_e64 v144, v144\n" \
"v_mfma_f32_16x16x32_bf16 v[100:103], a[164:167], v[204:207], v[100:103]\n" \
"v_cvt_pk_bf16_f32 v198, v140, v141\n" \
"v_cvt_pk_bf16_f32 v199, v142, v143\n" \
"v_mfma_f32_16x16x32_bf16 v[100:103], a[132:135], v[220:223], v[100:103]\n" \
"v_exp_f32_e64 v145, v145\n" \
"v_mfma_f32_16x16x32_bf16 v[104:107], a[168:171], v[204:207], v[104:107]\n" \
"ds_read_b64_tr_b16 a[162:163], %[vvb] offset:61440\n" \
"ds_read_b64_tr_b16 a[164:165], v211 offset:57344\n" \
"v_mfma_f32_16x16x32_bf16 v[104:107], a[136:139], v[220:223], v[104:107]\n" \
"v_exp_f32_e64 v146, v146\n" \
"v_mfma_f32_16x16x32_bf16 v[108:111], a[172:175], v[204:207], v[108:111]\n" \
"ds_read_b64_tr_b16 a[166:167], v211 offset:61440\n" \
"v_mfma_f32_16x16x32_bf16 v[108:111], a[140:143], v[220:223], v[108:111]\n" \
"v_exp_f32_e64 v147, v147\n" \
"v_smfmac_f32_16x16x64_bf16 v[180:183], a[192:195], v[216:223], v228\n" \
"ds_read_b64_tr_b16 a[168:169], %[vvb] offset:57856\n" \
"v_mfma_f32_16x16x32_bf16 v[152:155], a[96:99], a[48:51], v[172:175]\n" \
"v_exp_f32_e64 v148, v148\n" \
"v_mfma_f32_16x16x32_bf16 v[152:155], a[100:103], a[52:55], v[152:155]\n" \
"ds_read_b64_tr_b16 a[170:171], %[vvb] offset:61952\n" \
"v_mfma_f32_16x16x32_bf16 v[152:155], a[104:107], a[56:59], v[152:155]\n" \
"v_exp_f32_e64 v149, v149\n" \
"v_mfma_f32_16x16x32_bf16 v[152:155], a[108:111], a[60:63], v[152:155]\n" \
"v_cvt_pk_bf16_f32 v200, v144, v145\n" \
"v_cvt_pk_bf16_f32 v201, v146, v147\n" \
"v_mfma_f32_16x16x32_bf16 v[156:159], a[124:127], a[60:63], v[172:175]\n" \
"v_exp_f32_e64 v150, v150\n" \
"v_mfma_f32_16x16x32_bf16 v[156:159], a[120:123], a[56:59], v[156:159]\n" \
".if . - adaln_body != 28672\n" \
".error \"size model\"\n" \
".endif\n" \
"Lk6_%=:\n" \
"s_cbranch_execz Lk7_%=\n" \
"s_nop 0\n" \
"ds_read_b64_tr_b16 a[172:173], v211 offset:57856\n" \
"v_mfma_f32_16x16x32_bf16 v[156:159], a[116:119], a[52:55], v[156:159]\n" \
"buffer_load_dwordx4 v215, %[srdv], s48 offen offset:3072 lds\n" \
"v_mfma_f32_16x16x32_bf16 v[156:159], a[112:115], a[48:51], v[156:159]\n" \
"v_exp_f32_e64 v151, v151\n" \
"v_mfma_f32_16x16x32_bf16 v[112:115], a[176:179], v[204:207], v[112:115]\n" \
"ds_read_b64_tr_b16 a[174:175], v211 offset:61952\n" \
"ds_read_b64_tr_b16 a[176:177], %[vvb] offset:58368\n" \
"v_mfma_f32_16x16x32_bf16 v[112:115], a[144:147], v[220:223], v[112:115]\n" \
"v_exp_f32_e64 v152, v152\n" \
"v_mfma_f32_16x16x32_bf16 v[116:119], a[180:183], v[204:207], v[116:119]\n" \
"v_cvt_pk_bf16_f32 v202, v148, v149\n" \
"v_cvt_pk_bf16_f32 v203, v150, v151\n" \
"v_mfma_f32_16x16x32_bf16 v[116:119], a[148:151], v[220:223], v[116:119]\n" \
"v_exp_f32_e64 v153, v153\n" \
"v_mfma_f32_16x16x32_bf16 v[120:123], a[184:187], v[204:207], v[120:123]\n" \
"v_exp_f32_e64 v154, v154\n" \
"v_mfma_f32_16x16x32_bf16 v[120:123], a[152:155], v[220:223], v[120:123]\n" \
"ds_read_b64_tr_b16 a[178:179], %[vvb] offset:62464\n" \
"v_mfma_f32_16x16x32_bf16 v[124:127], a[188:191], v[204:207], v[124:127]\n" \
"v_exp_f32_e64 v155, v155\n" \
"v_mfma_f32_16x16x32_bf16 v[124:127], a[156:159], v[220:223], v[124:127]\n" \
"ds_read_b64_tr_b16 a[180:181], v211 offset:58368\n" \
"s_waitcnt lgkmcnt(13)\n" \
"v_mfma_f32_16x16x32_bf16 v[128:131], a[64:67], a[0:3], v[160:163]\n" \
"v_exp_f32_e32 v156, v156\n" \
"v_mfma_f32_16x16x32_bf16 v[128:131], a[68:71], a[4:7], v[128:131]\n" \
"ds_read_b64_tr_b16 a[182:183], v211 offset:62464\n" \
"s_add_u32 m0, %[m0b], 49152\n" \
"v_mfma_f32_16x16x32_bf16 v[128:131], a[72:75], a[8:11], v[128:131]\n" \
"v_exp_f32_e64 v157, v157\n" \
"v_mfma_f32_16x16x32_bf16 v[128:131], a[76:79], a[12:15], v[128:131]\n" \
"v_cvt_pk_bf16_f32 v204, v152, v153\n" \
"v_cvt_pk_bf16_f32 v205, v154, v155\n" \
"v_mfma_f32_16x16x32_bf16 v[132:135], a[92:95], a[12:15], v[160:163]\n" \
"v_exp_f32_e64 v158, v158\n" \
"v_mfma_f32_16x16x32_bf16 v[132:135], a[88:91], a[8:11], v[132:135]\n" \
"ds_read_b64_tr_b16 a[184:185], %[vvb] offset:58880\n" \
"s_waitcnt lgkmcnt(5)\n" \
"v_mfma_f32_16x16x32_bf16 v[132:135], a[84:87], a[4:7], v[132:135]\n" \
"buffer_load_dwordx4 %[kd0], %[srdk], s48 offen offset:0 lds\n" \
"v_mfma_f32_16x16x32_bf16 v[132:135], a[80:83], a[0:3], v[132:135]\n" \
"v_exp_f32_e32 v159, v159\n" \
"v_mfma_f32_16x16x32_bf16 v[64:67], a[128:131], v[216:219], v[64:67]\n" \
"ds_read_b64_tr_b16 a[186:187], %[vvb] offset:62976\n" \
"ds_read_b64_tr_b16 a[188:189], v211 offset:58880\n" \
"v_mfma_f32_16x16x32_bf16 v[64:67], a[160:163], v[200:203], v[64:67]\n" \
"v_exp_f32_e64 v128, v128\n" \
"v_mfma_f32_16x16x32_bf16 v[68:71], a[132:135], v[216:219], v[68:71]\n" \
"v_cvt_pk_bf16_f32 v206, v156, v157\n" \
"v_cvt_pk_bf16_f32 v207, v158, v159\n" \
"v_mfma_f32_16x16x32_bf16 v[68:71], a[164:167], v[200:203], v[68:71]\n" \
"v_exp_f32_e64 v129, v129\n" \
"v_mfma_f32_16x16x32_bf16 v[72:75], a[136:139], v[216:219], v[72:75]\n" \
"ds_read_b64_tr_b16 a[190:191], v211 offset:62976\n" \
"ds_read_b64_tr_b16 a[128:129], %[vvb] offset:0\n" \
"v_mfma_f32_16x16x32_bf16 v[72:75], a[168:171], v[200:203], v[72:75]\n" \
"v_exp_f32_e64 v130, v130\n" \
"v_mfma_f32_16x16x32_bf16 v[76:79], a[140:143], v[216:219], v[76:79]\n" \
"ds_read_b64_tr_b16 a[130:131], %[vvb] offset:4096\n" \
"ds_read_b64_tr_b16 a[132:133], v211 offset:0\n" \
"v_mfma_f32_16x16x32_bf16 v[76:79], a[172:175], v[200:203], v[76:79]\n" \
"v_exp_f32_e64 v131, v131\n" \
"v_smfmac_f32_16x16x64_bf16 v[176:179], a[192:195], v[192:199], v228\n" \
"ds_read_b64_tr_b16 a[134:135], v211 offset:4096\n" \
"ds_read_b64_tr_b16 a[136:137], %[vvb] offset:512\n" \
"v_mfma_f32_16x16x32_bf16 v[136:139], a[64:67], a[16:19], v[164:167]\n" \
"v_exp_f32_e64 v132, v132\n" \
"v_mfma_f32_16x16x32_bf16 v[136:139], a[68:71], a[20:23], v[136:139]\n" \
"ds_read_b64_tr_b16 a[138:139], %[vvb] offset:4608\n" \
"ds_read_b64_tr_b16 a[140:141], v211 offset:512\n" \
"v_mfma_f32_16x16x32_bf16 v[136:139], a[72:75], a[24:27], v[136:139]\n" \
"v_exp_f32_e64 v133, v133\n" \
"v_mfma_f32_16x16x32_bf16 v[136:139], a[76:79], a[28:31], v[136:139]\n" \
"v_cvt_pk_bf16_f32 v184, v128, v129\n" \
"v_cvt_pk_bf16_f32 v185, v130, v131\n" \
"v_mfma_f32_16x16x32_bf16 v[140:143], a[92:95], a[28:31], v[164:167]\n" \
"v_exp_f32_e64 v134, v134\n" \
"v_mfma_f32_16x16x32_bf16 v[140:143], a[88:91], a[24:27], v[140:143]\n" \
"ds_read_b64_tr_b16 a[142:143], v211 offset:4608\n" \
"s_waitcnt lgkmcnt(8)\n" \
"v_mfma_f32_16x16x32_bf16 v[140:143], a[84:87], a[20:23], v[140:143]\n" \
"buffer_load_dwordx4 v212, %[srdk], s48 offen offset:1024 lds\n" \
"v_mfma_f32_16x16x32_bf16 v[140:143], a[80:83], a[16:19], v[140:143]\n" \
"v_exp_f32_e32 v135, v135\n" \
"v_mfma_f32_16x16x32_bf16 v[80:83], a[144:147], v[216:219], v[80:83]\n" \
"ds_read_b64_tr_b16 a[144:145], %[vvb] offset:1024\n" \
"ds_read_b64_tr_b16 a[146:147], %[vvb] offset:5120\n" \
"v_mfma_f32_16x16x32_bf16 v[80:83], a[176:179], v[200:203], v[80:83]\n" \
"v_exp_f32_e64 v136, v136\n" \
"v_mfma_f32_16x16x32_bf16 v[84:87], a[148:151], v[216:219], v[84:87]\n" \
"v_cvt_pk_bf16_f32 v186, v132, v133\n" \
"v_cvt_pk_bf16_f32 v187, v134, v135\n" \
"v_mfma_f32_16x16x32_bf16 v[84:87], a[180:183], v[200:203], v[84:87]\n" \
"v_exp_f32_e64 v137, v137\n" \
"v_mfma_f32_16x16x32_bf16 v[88:91], a[152:155], v[216:219], v[88:91]\n" \
"v_exp_f32_e64 v138, v138\n" \
"v_mfma_f32_16x16x32_bf16 v[88:91], a[184:187], v[200:203], v[88:91]\n" \
"ds_read_b64_tr_b16 a[148:149], v211 offset:1024\n" \
"ds_read_b64_tr_b16 a[150:151], v211 offset:5120\n" \
"v_mfma_f32_16x16x32_bf16 v[92:95], a[156:159], v[216:219], v[92:95]\n" \
"v_exp_f32_e64 v139, v139\n" \
"v_mfma_f32_16x16x32_bf16 v[92:95], a[188:191], v[200:203], v[92:95]\n" \
"ds_read_b64_tr_b16 a[152:153], %[vvb] offset:1536\n" \
"buffer_store_dwordx4 a[196:199], v235, %[srdz], s62 offen offset:2048 sc1\n" \
"s_add_u32 s62, s62, 3072\n" \
"v_mfma_f32_16x16x32_bf16 v[144:147], a[64:67], a[32:35], v[168:171]\n" \
"v_exp_f32_e64 v140, v140\n" \
"v_mfma_f32_16x16x32_bf16 v[144:147], a[68:71], a[36:39], v[144:147]\n" \
"ds_read_b64_tr_b16 a[154:155], %[vvb] offset:5632\n" \
"ds_read_b64_tr_b16 a[156:157], v211 offset:1536\n" \
"v_mfma_f32_16x16x32_bf16 v[144:147], a[72:75], a[40:43], v[144:147]\n" \
"v_exp_f32_e64 v141, v141\n" \
"v_mfma_f32_16x16x32_bf16 v[144:147], a[76:79], a[44:47], v[144:147]\n" \
"v_cvt_pk_bf16_f32 v188, v136, v137\n" \
"v_cvt_pk_bf16_f32 v189, v138, v139\n" \
"v_mfma_f32_16x16x32_bf16 v[148:151], a[92:95], a[44:47], v[168:171]\n" \
"v_exp_f32_e64 v142, v142\n" \
"v_mfma_f32_16x16x32_bf16 v[148:151], a[88:91], a[40:43], v[148:151]\n" \
"ds_read_b64_tr_b16 a[158:159], v211 offset:5632\n" \
"s_waitcnt lgkmcnt(8)\n" \
"v_mfma_f32_16x16x32_bf16 v[148:151], a[84:87], a[36:39], v[148:151]\n" \
"buffer_load_dwordx4 v213, %[srdk], s48 offen offset:2048 lds\n" \
"v_mfma_f32_16x16x32_bf16 v[148:151], a[80:83], a[32:35], v[148:151]\n" \
"v_exp_f32_e32 v143, v143\n" \
"v_mfma_f32_16x16x32_bf16 v[0:3], a[160:163], v[192:195], v[0:3]\n" \
"ds_read_b128 a[96:99], %[kb0] offset:8192\n" \
"v_mfma_f32_16x16x32_bf16 v[0:3], a[128:131], v[184:187], v[0:3]\n" \
"v_exp_f32_e64 v144, v144\n" \
"v_mfma_f32_16x16x32_bf16 v[4:7], a[164:167], v[192:195], v[4:7]\n" \
"v_cvt_pk_bf16_f32 v190, v140, v141\n" \
"v_cvt_pk_bf16_f32 v191, v142, v143\n" \
"v_mfma_f32_16x16x32_bf16 v[4:7], a[132:135], v[184:187], v[4:7]\n" \
"v_exp_f32_e64 v145, v145\n" \
"v_mfma_f32_16x16x32_bf16 v[8:11], a[168:171], v[192:195], v[8:11]\n" \
"ds_read_b128 a[100:103], v208 offset:8192\n" \
"v_mfma_f32_16x16x32_bf16 v[8:11], a[136:139], v[184:187], v[8:11]\n" \
"v_exp_f32_e64 v146, v146\n" \
"v_mfma_f32_16x16x32_bf16 v[12:15], a[172:175], v[192:195], v[12:15]\n" \
"ds_read_b128 a[104:107], v209 offset:8192\n" \
"v_mfma_f32_16x16x32_bf16 v[12:15], a[140:143], v[184:187], v[12:15]\n" \
"v_exp_f32_e64 v147, v147\n" \
"v_smfmac_f32_16x16x64_bf16 v[180:183], a[192:195], v[200:207], v228\n" \
"ds_read_b128 a[108:111], v210 offset:8192\n" \
"v_mfma_f32_16x16x32_bf16 v[152:155], a[64:67], a[48:51], v[172:175]\n" \
"v_exp_f32_e64 v148, v148\n" \
"v_mfma_f32_16x16x32_bf16 v[152:155], a[68:71], a[52:55], v[152:155]\n" \
"ds_read_b128 a[124:127], v210 offset:12288\n" \
"v_mfma_f32_16x16x32_bf16 v[152:155], a[72:75], a[56:59], v[152:155]\n" \
"v_exp_f32_e64 v149, v149\n" \
"v_mfma_f32_16x16x32_bf16 v[152:155], a[76:79], a[60:63], v[152:155]\n" \
"v_cvt_pk_bf16_f32 v216, v144, v145\n" \
"v_cvt_pk_bf16_f32 v217, v146, v147\n" \
"v_mfma_f32_16x16x32_bf16 v[156:159], a[92:95], a[60:63], v[172:175]\n" \
"v_exp_f32_e64 v150, v150\n" \
"v_mfma_f32_16x16x32_bf16 v[156:159], a[88:91], a[56:59], v[156:159]\n" \
"ds_read_b128 a[120:123], v209 offset:12288\n" \
"s_waitcnt lgkmcnt(6)\n" \
"v_mfma_f32_16x16x32_bf16 v[156:159], a[84:87], a[52:55], v[156:159]\n" \
"buffer_load_dwordx4 v214, %[srdk], s48 offen offset:3072 lds\n" \
"v_mfma_f32_16x16x32_bf16 v[156:159], a[80:83], a[48:51], v[156:159]\n" \
"v_exp_f32_e32 v151, v151\n" \
"v_mfma_f32_16x16x32_bf16 v[16:19], a[176:179], v[192:195], v[16:19]\n" \
"ds_read_b128 a[116:119], v208 offset:12288\n" \
"v_mfma_f32_16x16x32_bf16 v[16:19], a[144:147], v[184:187], v[16:19]\n" \
"v_exp_f32_e64 v152, v152\n" \
"v_mfma_f32_16x16x32_bf16 v[20:23], a[180:183], v[192:195], v[20:23]\n" \
"v_cvt_pk_bf16_f32 v218, v148, v149\n" \
"v_cvt_pk_bf16_f32 v219, v150, v151\n" \
"v_mfma_f32_16x16x32_bf16 v[20:23], a[148:151], v[184:187], v[20:23]\n" \
"v_exp_f32_e64 v153, v153\n" \
"v_mfma_f32_16x16x32_bf16 v[24:27], a[184:187], v[192:195], v[24:27]\n" \
"v_exp_f32_e64 v154, v154\n" \
"v_mfma_f32_16x16x32_bf16 v[24:27], a[152:155], v[184:187], v[24:27]\n" \
"ds_read_b128 a[112:115], %[kb0] offset:12288\n" \
"s_add_u32 s48, s48, 0x4000\n" \
"v_mfma_f32_16x16x32_bf16 v[28:31], a[188:191], v[192:195], v[28:31]\n" \
"v_exp_f32_e64 v155, v155\n" \
"v_mfma_f32_16x16x32_bf16 v[28:31], a[156:159], v[184:187], v[28:31]\n" \
"s_waitcnt vmcnt(17) lgkmcnt(2)\n" \
"s_barrier\n" \
"v_mfma_f32_16x16x32_bf16 v[128:131], a[96:99], a[0:3], v[160:163]\n" \
"v_exp_f32_e64 v156, v156\n" \
"v_mfma_f32_16x16x32_bf16 v[128:131], a[100:103], a[4:7], v[128:131]\n" \
"s_add_u32 m0, %[m0b], 114688\n" \
"v_mfma_f32_16x16x32_bf16 v[128:131], a[104:107], a[8:11], v[128:131]\n" \
"v_exp_f32_e64 v157, v157\n" \
"v_mfma_f32_16x16x32_bf16 v[128:131], a[108:111], a[12:15], v[128:131]\n" \
"v_cvt_pk_bf16_f32 v220, v152, v153\n" \
"v_cvt_pk_bf16_f32 v221, v154, v155\n" \
"v_mfma_f32_16x16x32_bf16 v[132:135], a[124:127], a[12:15], v[160:163]\n" \
"v_exp_f32_e64 v158, v158\n" \
"v_mfma_f32_16x16x32_bf16 v[132:135], a[120:123], a[8:11], v[132:135]\n" \
"s_waitcnt lgkmcnt(0)\n" \
"v_mfma_f32_16x16x32_bf16 v[132:135], a[116:119], a[4:7], v[132:135]\n" \
"buffer_load_dwordx4 %[vd0], %[srdv], s48 offen offset:0 lds\n" \
"v_mfma_f32_16x16x32_bf16 v[132:135], a[112:115], a[0:3], v[132:135]\n" \
"v_exp_f32_e32 v159, v159\n" \
"v_mfma_f32_16x16x32_bf16 v[32:35], a[160:163], v[196:199], v[32:35]\n" \
"ds_read_b128 a[64:67], %[kb0] offset:16384\n" \
"v_mfma_f32_16x16x32_bf16 v[32:35], a[128:131], v[188:191], v[32:35]\n" \
"v_exp_f32_e64 v128, v128\n" \
"v_mfma_f32_16x16x32_bf16 v[36:39], a[164:167], v[196:199], v[36:39]\n" \
"v_cvt_pk_bf16_f32 v222, v156, v157\n" \
"v_cvt_pk_bf16_f32 v223, v158, v159\n" \
"v_mfma_f32_16x16x32_bf16 v[36:39], a[132:135], v[188:191], v[36:39]\n" \
"v_exp_f32_e64 v129, v129\n" \
"v_mfma_f32_16x16x32_bf16 v[40:43], a[168:171], v[196:199], v[40:43]\n" \
"ds_read_b128 a[68:71], v208 offset:16384\n" \
"v_mfma_f32_16x16x32_bf16 v[40:43], a[136:139], v[188:191], v[40:43]\n" \
"v_exp_f32_e64 v130, v130\n" \
"v_mfma_f32_16x16x32_bf16 v[44:47], a[172:175], v[196:199], v[44:47]\n" \
"ds_read_b128 a[72:75], v209 offset:16384\n" \
"v_mfma_f32_16x16x32_bf16 v[44:47], a[140:143], v[188:191], v[44:47]\n" \
"v_exp_f32_e64 v131, v131\n" \
"v_smfmac_f32_16x16x64_bf16 v[176:179], a[192:195], v[184:191], v228\n" \
"v_mfma_f32_16x16x32_bf16 v[136:139], a[96:99], a[16:19], v[164:167]\n" \
"v_exp_f32_e64 v132, v132\n" \
"v_mfma_f32_16x16x32_bf16 v[136:139], a[100:103], a[20:23], v[136:139]\n" \
"ds_read_b128 a[76:79], v210 offset:16384\n" \
"v_mfma_f32_16x16x32_bf16 v[136:139], a[104:107], a[24:27], v[136:139]\n" \
"v_exp_f32_e64 v133, v133\n" \
"v_mfma_f32_16x16x32_bf16 v[136:139], a[108:111], a[28:31], v[136:139]\n" \
"v_cvt_pk_bf16_f32 v192, v128, v129\n" \
"v_cvt_pk_bf16_f32 v193, v130, v131\n" \
"v_mfma_f32_16x16x32_bf16 v[140:143], a[124:127], a[28:31], v[164:167]\n" \
"v_exp_f32_e64 v134, v134\n" \
"v_mfma_f32_16x16x32_bf16 v[140:143], a[120:123], a[24:27], v[140:143]\n" \
"ds_read_b128 a[92:95], v210 offset:20480\n" \
"v_mfma_f32_16x16x32_bf16 v[140:143], a[116:119], a[20:23], v[140:143]\n" \
"buffer_load_dwordx4 v215, %[srdv], s48 offen offset:1024 lds\n" \
"v_mfma_f32_16x16x32_bf16 v[140:143], a[112:115], a[16:19], v[140:143]\n" \
"v_exp_f32_e64 v135, v135\n" \
"v_mfma_f32_16x16x32_bf16 v[48:51], a[176:179], v[196:199], v[48:51]\n" \
"ds_read_b128 a[88:91], v209 offset:20480\n" \
"v_mfma_f32_16x16x32_bf16 v[48:51], a[144:147], v[188:191], v[48:51]\n" \
"v_exp_f32_e64 v136, v136\n" \
"v_mfma_f32_16x16x32_bf16 v[52:55], a[180:183], v[196:199], v[52:55]\n" \
"v_cvt_pk_bf16_f32 v194, v132, v133\n" \
"v_cvt_pk_bf16_f32 v195, v134, v135\n" \
"v_mfma_f32_16x16x32_bf16 v[52:55], a[148:151], v[188:191], v[52:55]\n" \
"v_exp_f32_e64 v137, v137\n" \
"v_mfma_f32_16x16x32_bf16 v[56:59], a[184:187], v[196:199], v[56:59]\n" \
"v_exp_f32_e64 v138, v138\n" \
"v_mfma_f32_16x16x32_bf16 v[56:59], a[152:155], v[188:191], v[56:59]\n" \
"ds_read_b128 a[84:87], v208 offset:20480\n" \
"v_mfma_f32_16x16x32_bf16 v[60:63], a[188:191], v[196:199], v[60:63]\n" \
"v_exp_f32_e64 v139, v139\n" \
"v_mfma_f32_16x16x32_bf16 v[60:63], a[156:159], v[188:191], v[60:63]\n" \
"s_cmp_lg_u32 %[wid], 3\n" \
"s_cbranch_scc1 LtB1_%=\n" \
"global_load_dword v237, v236, s[58:59]\n" \
"s_add_u32 s61, s48, 49152\n" \
"s_mov_b32 exec_hi, 0\n" \
"s_nop 0\n" \
"buffer_load_dword v237, v224, %[srdp], s61 offen\n" \
"s_mov_b32 exec_hi, -1\n" \
"s_nop 0\n" \
"LtB1_%=:\n" \
"v_mfma_f32_16x16x32_bf16 v[144:147], a[96:99], a[32:35], v[168:171]\n" \
"v_exp_f32_e64 v140, v140\n" \
"v_mfma_f32_16x16x32_bf16 v[144:147], a[100:103], a[36:39], v[144:147]\n" \
"ds_read_b128 a[80:83], %[kb0] offset:20480\n" \
"v_mfma_f32_16x16x32_bf16 v[144:147], a[104:107], a[40:43], v[144:147]\n" \
"v_exp_f32_e64 v141, v141\n" \
"v_mfma_f32_16x16x32_bf16 v[144:147], a[108:111], a[44:47], v[144:147]\n" \
"v_cvt_pk_bf16_f32 v196, v136, v137\n" \
"v_cvt_pk_bf16_f32 v197, v138, v139\n" \
"v_mfma_f32_16x16x32_bf16 v[148:151], a[124:127], a[44:47], v[168:171]\n" \
"v_exp_f32_e64 v142, v142\n" \
"v_mfma_f32_16x16x32_bf16 v[148:151], a[120:123], a[40:43], v[148:151]\n" \
"v_mfma_f32_16x16x32_bf16 v[148:151], a[116:119], a[36:39], v[148:151]\n" \
"buffer_load_dwordx4 %[vd0], %[srdv], s48 offen offset:2048 lds\n" \
"v_mfma_f32_16x16x32_bf16 v[148:151], a[112:115], a[32:35], v[148:151]\n" \
"v_exp_f32_e64 v143, v143\n" \
"v_mfma_f32_16x16x32_bf16 v[96:99], a[160:163], v[204:207], v[96:99]\n" \
"ds_read_b64_tr_b16 a[160:161], %[vvb] offset:8192\n" \
"v_mfma_f32_16x16x32_bf16 v[96:99], a[128:131], v[220:223], v[96:99]\n" \
"v_exp_f32_e64 v144, v144\n" \
"v_mfma_f32_16x16x32_bf16 v[100:103], a[164:167], v[204:207], v[100:103]\n" \
"v_cvt_pk_bf16_f32 v198, v140, v141\n" \
"v_cvt_pk_bf16_f32 v199, v142, v143\n" \
"v_mfma_f32_16x16x32_bf16 v[100:103], a[132:135], v[220:223], v[100:103]\n" \
"v_exp_f32_e64 v145, v145\n" \
"v_mfma_f32_16x16x32_bf16 v[104:107], a[168:171], v[204:207], v[104:107]\n" \
"ds_read_b64_tr_b16 a[162:163], %[vvb] offset:12288\n" \
"ds_read_b64_tr_b16 a[164:165], v211 offset:8192\n" \
"v_mfma_f32_16x16x32_bf16 v[104:107], a[136:139], v[220:223], v[104:107]\n" \
"v_exp_f32_e64 v146, v146\n" \
"v_mfma_f32_16x16x32_bf16 v[108:111], a[172:175], v[204:207], v[108:111]\n" \
"ds_read_b64_tr_b16 a[166:167], v211 offset:12288\n" \
"v_mfma_f32_16x16x32_bf16 v[108:111], a[140:143], v[220:223], v[108:111]\n" \
"v_exp_f32_e64 v147, v147\n" \
"v_smfmac_f32_16x16x64_bf16 v[180:183], a[192:195], v[216:223], v228\n" \
"ds_read_b64_tr_b16 a[168:169], %[vvb] offset:8704\n" \
"v_mfma_f32_16x16x32_bf16 v[152:155], a[96:99], a[48:51], v[172:175]\n" \
"v_exp_f32_e64 v148, v148\n" \
"v_mfma_f32_16x16x32_bf16 v[152:155], a[100:103], a[52:55], v[152:155]\n" \
"ds_read_b64_tr_b16 a[170:171], %[vvb] offset:12800\n" \
"v_mfma_f32_16x16x32_bf16 v[152:155], a[104:107], a[56:59], v[152:155]\n" \
"v_exp_f32_e64 v149, v149\n" \
"v_mfma_f32_16x16x32_bf16 v[152:155], a[108:111], a[60:63], v[152:155]\n" \
"v_cvt_pk_bf16_f32 v200, v144, v145\n" \
"v_cvt_pk_bf16_f32 v201, v146, v147\n" \
"v_mfma_f32_16x16x32_bf16 v[156:159], a[124:127], a[60:63], v[172:175]\n" \
"v_exp_f32_e64 v150, v150\n" \
"v_mfma_f32_16x16x32_bf16 v[156:159], a[120:123], a[56:59], v[156:159]\n" \
"ds_read_b64_tr_b16 a[172:173], v211 offset:8704\n" \
"v_mfma_f32_16x16x32_bf16 v[156:159], a[116:119], a[52:55], v[156:159]\n" \
"buffer_load_dwordx4 v215, %[srdv], s48 offen offset:3072 lds\n" \
"v_mfma_f32_16x16x32_bf16 v[156:159], a[112:115], a[48:51], v[156:159]\n" \
"v_exp_f32_e64 v151, v151\n" \
"v_mfma_f32_16x16x32_bf16 v[112:115], a[176:179], v[204:207], v[112:115]\n" \
"ds_read_b64_tr_b16 a[174:175], v211 offset:12800\n" \
"ds_read_b64_tr_b16 a[176:177], %[vvb] offset:9216\n" \
"v_mfma_f32_16x16x32_bf16 v[112:115], a[144:147], v[220:223], v[112:115]\n" \
"v_exp_f32_e64 v152, v152\n" \
"v_mfma_f32_16x16x32_bf16 v[116:119], a[180:183], v[204:207], v[116:119]\n" \
"v_cvt_pk_bf16_f32 v202, v148, v149\n" \
"v_cvt_pk_bf16_f32 v203, v150, v151\n" \
"v_mfma_f32_16x16x32_bf16 v[116:119], a[148:151], v[220:223], v[116:119]\n" \
"v_exp_f32_e64 v153, v153\n" \
"v_mfma_f32_16x16x32_bf16 v[120:123], a[184:187], v[204:207], v[120:123]\n" \
"v_exp_f32_e64 v154, v154\n" \
"v_mfma_f32_16x16x32_bf16 v[120:123], a[152:155], v[220:223], v[120:123]\n" \
"ds_read_b64_tr_b16 a[178:179], %[vvb] offset:13312\n" \
"v_mfma_f32_16x16x32_bf16 v[124:127], a[188:191], v[204:207], v[124:127]\n" \
"v_exp_f32_e64 v155, v155\n" \
"v_mfma_f32_16x16x32_bf16 v[124:127], a[156:159], v[220:223], v[124:127]\n" \
"ds_read_b64_tr_b16 a[180:181], v211 offset:9216\n" \
"s_waitcnt lgkmcnt(13)\n" \
"s_cmp_lg_u32 s60, 7\n" \
"s_cbranch_scc1 LmB_%=\n" \
"s_nop 0\n" \
"v_readlane_b32 s48, %[tlo], 11\n" \
"v_readlane_b32 s58, %[tlo], 12\n" \
"v_mfma_f32_16x16x32_bf16 v[128:131], a[64:67], a[0:3], v[160:163]\n" \
"v_exp_f32_e64 v156, v156\n" \
"v_mfma_f32_16x16x32_bf16 v[128:131], a[68:71], a[4:7], v[128:131]\n" \
"ds_read_b64_tr_b16 a[182:183], v211 offset:13312\n" \
"s_add_u32 m0, %[m0b], 0\n" \
"s_mov_b32 s49, s48\n" \
"v_mfma_f32_16x16x32_bf16 v[128:131], a[72:75], a[8:11], v[128:131]\n" \
"v_exp_f32_e64 v157, v157\n" \
"v_mfma_f32_16x16x32_bf16 v[128:131], a[76:79], a[12:15], v[128:131]\n" \
"v_cvt_pk_bf16_f32 v204, v152, v153\n" \
"v_cvt_pk_bf16_f32 v205, v154, v155\n" \
"v_mfma_f32_16x16x32_bf16 v[132:135], a[92:95], a[12:15], v[160:163]\n" \
"v_exp_f32_e64 v158, v158\n" \
"v_mfma_f32_16x16x32_bf16 v[132:135], a[88:91], a[8:11], v[132:135]\n" \
"ds_read_b64_tr_b16 a[184:185], %[vvb] offset:9728\n" \
"s_lshl_b32 s49, s49, 18\n" \
"s_or_b32 s49, s49, s58\n" \
"v_accvgpr_write_b32 a228, v225\n" \
"s_waitcnt lgkmcnt(5)\n" \
"v_mfma_f32_16x16x32_bf16 v[132:135], a[84:87], a[4:7], v[132:135]\n" \
"buffer_load_dwordx4 %[wo0], %[srdt], s49 offen offset:0 lds\n" \
"v_mfma_f32_16x16x32_bf16 v[132:135], a[80:83], a[0:3], v[132:135]\n" \
"v_exp_f32_e32 v159, v159\n" \
"v_mfma_f32_16x16x32_bf16 v[64:67], a[128:131], v[216:219], v[64:67]\n" \
"ds_read_b64_tr_b16 a[186:187], %[vvb] offset:13824\n" \
"ds_read_b64_tr_b16 a[188:189], v211 offset:9728\n" \
"v_readlane_b32 s60, %[thi], 11\n" \
"v_mfma_f32_16x16x32_bf16 v[64:67], a[160:163], v[200:203], v[64:67]\n" \
"v_exp_f32_e64 v128, v128\n" \
"v_mfma_f32_16x16x32_bf16 v[68:71], a[132:135], v[216:219], v[68:71]\n" \
"v_cvt_pk_bf16_f32 v206, v156, v157\n" \
"v_cvt_pk_bf16_f32 v207, v158, v159\n" \
"v_mfma_f32_16x16x32_bf16 v[68:71], a[164:167], v[200:203], v[68:71]\n" \
"v_exp_f32_e64 v129, v129\n" \
"v_mfma_f32_16x16x32_bf16 v[72:75], a[136:139], v[216:219], v[72:75]\n" \
"ds_read_b64_tr_b16 a[190:191], v211 offset:13824\n" \
"ds_read_b64_tr_b16 a[128:129], %[vvb] offset:16384\n" \
"v_readlane_b32 s59, %[thi], 12\n" \
"v_mfma_f32_16x16x32_bf16 v[72:75], a[168:171], v[200:203], v[72:75]\n" \
"v_exp_f32_e64 v130, v130\n" \
"v_mfma_f32_16x16x32_bf16 v[76:79], a[140:143], v[216:219], v[76:79]\n" \
"ds_read_b64_tr_b16 a[130:131], %[vvb] offset:20480\n" \
"ds_read_b64_tr_b16 a[132:133], v211 offset:16384\n" \
"v_readlane_b32 s53, %[tlo], 13\n" \
"v_mfma_f32_16x16x32_bf16 v[76:79], a[172:175], v[200:203], v[76:79]\n" \
"v_exp_f32_e64 v131, v131\n" \
"v_smfmac_f32_16x16x64_bf16 v[176:179], a[192:195], v[192:199], v228\n" \
"ds_read_b64_tr_b16 a[134:135], v211 offset:20480\n" \
"ds_read_b64_tr_b16 a[136:137], %[vvb] offset:16896\n" \
"v_readlane_b32 s50, %[thi], 13\n" \
"v_mfma_f32_16x16x32_bf16 v[136:139], a[64:67], a[16:19], v[164:167]\n" \
"v_exp_f32_e64 v132, v132\n" \
"v_mfma_f32_16x16x32_bf16 v[136:139], a[68:71], a[20:23], v[136:139]\n" \
"ds_read_b64_tr_b16 a[138:139], %[vvb] offset:20992\n" \
"ds_read_b64_tr_b16 a[140:141], v211 offset:16896\n" \
"v_readlane_b32 s46, %[tlo], 14\n" \
"v_add_u32_e32 v212, 0x3c00, %[wo0]\n" \
"v_mfma_f32_16x16x32_bf16 v[136:139], a[72:75], a[24:27], v[136:139]\n" \
"v_exp_f32_e64 v133, v133\n" \
"v_mfma_f32_16x16x32_bf16 v[136:139], a[76:79], a[28:31], v[136:139]\n" \
"v_cvt_pk_bf16_f32 v184, v128, v129\n" \
"v_cvt_pk_bf16_f32 v185, v130, v131\n" \
"v_mfma_f32_16x16x32_bf16 v[140:143], a[92:95], a[28:31], v[164:167]\n" \
"v_exp_f32_e64 v134, v134\n" \
"v_mfma_f32_16x16x32_bf16 v[140:143], a[88:91], a[24:27], v[140:143]\n" \
"ds_read_b64_tr_b16 a[142:143], v211 offset:20992\n" \
"v_readlane_b32 s47, %[thi], 14\n" \
"v_add_u32_e32 v213, 0x7800, %[wo0]\n" \
"s_waitcnt lgkmcnt(8)\n" \
"v_mfma_f32_16x16x32_bf16 v[140:143], a[84:87], a[20:23], v[140:143]\n" \
"buffer_load_dwordx4 v212, %[srdt], s49 offen offset:1024 lds\n" \
"v_mfma_f32_16x16x32_bf16 v[140:143], a[80:83], a[16:19], v[140:143]\n" \
"v_exp_f32_e32 v135, v135\n" \
"v_mfma_f32_16x16x32_bf16 v[80:83], a[144:147], v[216:219], v[80:83]\n" \
"ds_read_b64_tr_b16 a[144:145], %[vvb] offset:17408\n" \
"ds_read_b64_tr_b16 a[146:147], %[vvb] offset:21504\n" \
"ds_read_b32 v230, %[rd0]\n" \
"v_mfma_f32_16x16x32_bf16 v[80:83], a[176:179], v[200:203], v[80:83]\n" \
"v_exp_f32_e64 v136, v136\n" \
"v_mfma_f32_16x16x32_bf16 v[84:87], a[148:151], v[216:219], v[84:87]\n" \
"v_cvt_pk_bf16_f32 v186, v132, v133\n" \
"v_cvt_pk_bf16_f32 v187, v134, v135\n" \
"v_mfma_f32_16x16x32_bf16 v[84:87], a[180:183], v[200:203], v[84:87]\n" \
"v_exp_f32_e64 v137, v137\n" \
"v_mfma_f32_16x16x32_bf16 v[88:91], a[152:155], v[216:219], v[88:91]\n" \
"v_exp_f32_e64 v138, v138\n" \
"v_mfma_f32_16x16x32_bf16 v[88:91], a[184:187], v[200:203], v[88:91]\n" \
"ds_read_b64_tr_b16 a[148:149], v211 offset:17408\n" \
"v_readlane_b32 s55, %[thi], 15\n" \
"ds_read_b64_tr_b16 a[150:151], v211 offset:21504\n" \
"v_lshrrev_b32_e64 v234, 5, v224\n" \
"v_mfma_f32_16x16x32_bf16 v[92:95], a[156:159], v[216:219], v[92:95]\n" \
"v_exp_f32_e64 v139, v139\n" \
"v_mfma_f32_16x16x32_bf16 v[92:95], a[188:191], v[200:203], v[92:95]\n" \
"ds_read_b64_tr_b16 a[152:153], %[vvb] offset:17920\n" \
"v_mfma_f32_16x16x32_bf16 v[144:147], a[64:67], a[32:35], v[168:171]\n" \
"v_exp_f32_e64 v140, v140\n" \
"v_mfma_f32_16x16x32_bf16 v[144:147], a[68:71], a[36:39], v[144:147]\n" \
"ds_read_b64_tr_b16 a[154:155], %[vvb] offset:22016\n" \
"ds_read_b64_tr_b16 a[156:157], v211 offset:17920\n" \
"s_waitcnt lgkmcnt(5)\n" \
"v_add_f32_dpp v230, v230, v230 row_ror:4 row_mask:0xf bank_mask:0xf\n" \
"v_mfma_f32_16x16x32_bf16 v[144:147], a[72:75], a[40:43], v[144:147]\n" \
"v_exp_f32_e32 v141, v141\n" \
"v_mfma_f32_16x16x32_bf16 v[144:147], a[76:79], a[44:47], v[144:147]\n" \
"v_cvt_pk_bf16_f32 v188, v136, v137\n" \
"v_cvt_pk_bf16_f32 v189, v138, v139\n" \
"v_mfma_f32_16x16x32_bf16 v[148:151], a[92:95], a[44:47], v[168:171]\n" \
"v_exp_f32_e64 v142, v142\n" \
"v_mfma_f32_16x16x32_bf16 v[148:151], a[88:91], a[40:43], v[148:151]\n" \
"ds_read_b64_tr_b16 a[158:159], v211 offset:22016\n" \
"v_add_f32_dpp v230, v230, v230 row_ror:8 row_mask:0xf bank_mask:0xf\n" \
"s_waitcnt lgkmcnt(9)\n" \
"v_mfma_f32_16x16x32_bf16 v[148:151], a[84:87], a[36:39], v[148:151]\n" \
"buffer_load_dwordx4 v213, %[srdt], s49 offen offset:2048 lds\n" \
"v_mfma_f32_16x16x32_bf16 v[148:151], a[80:83], a[32:35], v[148:151]\n" \
"v_exp_f32_e32 v143, v143\n" \
"v_mfma_f32_16x16x32_bf16 v[0:3], a[160:163], v[192:195], v[0:3]\n" \
"ds_read_b128 a[96:99], %[kb0] offset:24576\n" \
"v_cvt_pk_bf16_f32 v230, v230, v230\n" \
"v_mfma_f32_16x16x32_bf16 v[0:3], a[128:131], v[184:187], v[0:3]\n" \
"v_exp_f32_e64 v144, v144\n" \
"v_mfma_f32_16x16x32_bf16 v[4:7], a[164:167], v[192:195], v[4:7]\n" \
"v_cvt_pk_bf16_f32 v190, v140, v141\n" \
"v_cvt_pk_bf16_f32 v191, v142, v143\n" \
"v_mfma_f32_16x16x32_bf16 v[4:7], a[132:135], v[184:187], v[4:7]\n" \
"v_exp_f32_e64 v145, v145\n" \
"v_mfma_f32_16x16x32_bf16 v[8:11], a[168:171], v[192:195], v[8:11]\n" \
"ds_read_b128 a[100:103], v208 offset:24576\n" \
"v_lshlrev_b32_e64 v230, 16, v230\n" \
"v_mfma_f32_16x16x32_bf16 v[8:11], a[136:139], v[184:187], v[8:11]\n" \
"v_exp_f32_e64 v146, v146\n" \
"v_mfma_f32_16x16x32_bf16 v[12:15], a[172:175], v[192:195], v[12:15]\n" \
"ds_read_b128 a[104:107], v209 offset:24576\n" \
"v_add_f32_e64 v230, s55, v230\n" \
"v_add_u32_e32 v214, 0xb400, %[wo0]\n" \
"v_mfma_f32_16x16x32_bf16 v[12:15], a[140:143], v[184:187], v[12:15]\n" \
"v_exp_f32_e64 v147, v147\n" \
"v_smfmac_f32_16x16x64_bf16 v[180:183], a[192:195], v[200:207], v228\n" \
"ds_read_b128 a[108:111], v210 offset:24576\n" \
"v_cvt_pk_bf16_f32 v230, v230, v230\n" \
"v_readlane_b32 s55, %[tlo], 15\n" \
"v_mfma_f32_16x16x32_bf16 v[152:155], a[64:67], a[48:51], v[172:175]\n" \
"v_exp_f32_e64 v148, v148\n" \
"v_mfma_f32_16x16x32_bf16 v[152:155], a[68:71], a[52:55], v[152:155]\n" \
"ds_read_b128 a[124:127], v210 offset:28672\n" \
"v_lshl_or_b32 v230, v230, 16, 1\n" \
"v_mfma_f32_16x16x32_bf16 v[152:155], a[72:75], a[56:59], v[152:155]\n" \
"v_exp_f32_e64 v149, v149\n" \
"v_mfma_f32_16x16x32_bf16 v[152:155], a[76:79], a[60:63], v[152:155]\n" \
"v_cvt_pk_bf16_f32 v216, v144, v145\n" \
"v_cvt_pk_bf16_f32 v217, v146, v147\n" \
"v_mfma_f32_16x16x32_bf16 v[156:159], a[92:95], a[60:63], v[172:175]\n" \
"v_exp_f32_e64 v150, v150\n" \
"v_mfma_f32_16x16x32_bf16 v[156:159], a[88:91], a[56:59], v[156:159]\n" \
"ds_read_b128 a[120:123], v209 offset:28672\n" \
"s_cmp_lg_u32 s52, 0\n" \
"s_cselect_b32 s55, 0x80000000, s55\n" \
"s_mov_b64 exec, 15\n" \
"buffer_store_dword v230, v234, %[srdz], s55 offen sc1\n" \
"s_mov_b64 exec, -1\n" \
"s_waitcnt lgkmcnt(6)\n" \
"v_mfma_f32_16x16x32_bf16 v[156:159], a[84:87], a[52:55], v[156:159]\n" \
"buffer_load_dwordx4 v214, %[srdt], s49 offen offset:3072 lds\n" \
"v_mfma_f32_16x16x32_bf16 v[156:159], a[80:83], a[48:51], v[156:159]\n" \
"v_exp_f32_e64 v151, v151\n" \
"v_mfma_f32_16x16x32_bf16 v[16:19], a[176:179], v[192:195], v[16:19]\n" \
"ds_read_b128 a[116:119], v208 offset:28672\n" \
"v_mfma_f32_16x16x32_bf16 v[16:19], a[144:147], v[184:187], v[16:19]\n" \
"v_exp_f32_e64 v152, v152\n" \
"v_mfma_f32_16x16x32_bf16 v[20:23], a[180:183], v[192:195], v[20:23]\n" \
"v_cvt_pk_bf16_f32 v218, v148, v149\n" \
"v_cvt_pk_bf16_f32 v219, v150, v151\n" \
"v_mfma_f32_16x16x32_bf16 v[20:23], a[148:151], v[184:187], v[20:23]\n" \
"v_exp_f32_e64 v153, v153\n" \
"v_mfma_f32_16x16x32_bf16 v[24:27], a[184:187], v[192:195], v[24:27]\n" \
"v_exp_f32_e64 v154, v154\n" \
"v_mfma_f32_16x16x32_bf16 v[24:27], a[152:155], v[184:187], v[24:27]\n" \
"ds_read_b128 a[112:115], %[kb0] offset:28672\n" \
"s_mov_b32 m0, s44\n" \
"s_addk_i32 s44, 0x400\n" \
"buffer_load_dwordx4 v229, s[64:67], s45 offen offset:0 lds\n" \
"s_add_u32 s45, s45, 229376\n" \
"v_mfma_f32_16x16x32_bf16 v[28:31], a[188:191], v[192:195], v[28:31]\n" \
"v_exp_f32_e64 v155, v155\n" \
"v_mfma_f32_16x16x32_bf16 v[28:31], a[156:159], v[184:187], v[28:31]\n" \
"s_waitcnt vmcnt(6) lgkmcnt(2)\n" \
"s_barrier\n" \
"v_mfma_f32_16x16x32_bf16 v[128:131], a[96:99], a[0:3], v[160:163]\n" \
"v_exp_f32_e64 v156, v156\n" \
"s_mov_b32 m0, s44\n" \
"s_addk_i32 s44, 0x400\n" \
"buffer_load_dwordx4 v229, s[64:67], s45 offen offset:0 lds\n" \
"s_add_u32 s45, s45, 229376\n" \
"v_readlane_b32 s64, %[tlo], 8\n" \
"v_readlane_b32 s65, %[thi], 8\n" \
"v_mfma_f32_16x16x32_bf16 v[128:131], a[100:103], a[4:7], v[128:131]\n" \
"v_mbcnt_lo_u32_b32 v231, -1, 0\n" \
"v_mfma_f32_16x16x32_bf16 v[128:131], a[104:107], a[8:11], v[128:131]\n" \
".if . - adaln_body != 32768\n" \
".error \"size model\"\n" \
".endif\n" \
"Lk7_%=:\n" \
"s_cbranch_execz Lk8_%=\n" \
"s_nop 0\n" \
"v_exp_f32_e64 v157, v157\n" \
"v_mfma_f32_16x16x32_bf16 v[128:131], a[108:111], a[12:15], v[128:131]\n" \
"v_cvt_pk_bf16_f32 v220, v152, v153\n" \
"v_cvt_pk_bf16_f32 v221, v154, v155\n" \
"v_mfma_f32_16x16x32_bf16 v[132:135], a[124:127], a[12:15], v[160:163]\n" \
"v_exp_f32_e64 v158, v158\n" \
"v_mfma_f32_16x16x32_bf16 v[132:135], a[120:123], a[8:11], v[132:135]\n" \
"v_mbcnt_hi_u32_b32 v231, -1, v231\n" \
"s_waitcnt lgkmcnt(0)\n" \
"v_mfma_f32_16x16x32_bf16 v[132:135], a[116:119], a[4:7], v[132:135]\n" \
"s_add_u32 m0, %[m0b], 65536\n" \
"s_mov_b32 s61, 0\n" \
"buffer_load_dwordx4 %[vg0], s[64:67], s61 offen offset:0 lds\n" \
"v_mfma_f32_16x16x32_bf16 v[132:135], a[112:115], a[0:3], v[132:135]\n" \
"v_exp_f32_e64 v159, v159\n" \
"v_mfma_f32_16x16x32_bf16 v[32:35], a[160:163], v[196:199], v[32:35]\n" \
"ds_read_b128 a[64:67], %[kb0] offset:32768\n" \
"v_and_b32_e64 v226, 31, v231\n" \
"v_mfma_f32_16x16x32_bf16 v[32:35], a[128:131], v[188:191], v[32:35]\n" \
"v_exp_f32_e64 v128, v128\n" \
"v_mfma_f32_16x16x32_bf16 v[36:39], a[164:167], v[196:199], v[36:39]\n" \
"v_cvt_pk_bf16_f32 v222, v156, v157\n" \
"v_cvt_pk_bf16_f32 v223, v158, v159\n" \
"v_mfma_f32_16x16x32_bf16 v[36:39], a[132:135], v[188:191], v[36:39]\n" \
"v_exp_f32_e64 v129, v129\n" \
"v_mfma_f32_16x16x32_bf16 v[40:43], a[168:171], v[196:199], v[40:43]\n" \
"ds_read_b128 a[68:71], v208 offset:32768\n" \
"v_lshlrev_b32_e64 v226, 2, v226\n" \
"v_mfma_f32_16x16x32_bf16 v[40:43], a[136:139], v[188:191], v[40:43]\n" \
"v_exp_f32_e64 v130, v130\n" \
"v_mfma_f32_16x16x32_bf16 v[44:47], a[172:175], v[196:199], v[44:47]\n" \
"ds_read_b128 a[72:75], v209 offset:32768\n" \
"v_cmp_gt_u32_e32 vcc, 1, v231\n" \
"v_mfma_f32_16x16x32_bf16 v[44:47], a[140:143], v[188:191], v[44:47]\n" \
"v_exp_f32_e32 v131, v131\n" \
"v_smfmac_f32_16x16x64_bf16 v[176:179], a[192:195], v[184:191], v228\n" \
"v_mov_b32_e32 v225, 0x80000000\n" \
"v_mfma_f32_16x16x32_bf16 v[136:139], a[96:99], a[16:19], v[164:167]\n" \
"v_exp_f32_e64 v132, v132\n" \
"v_mfma_f32_16x16x32_bf16 v[136:139], a[100:103], a[20:23], v[136:139]\n" \
"ds_read_b128 a[76:79], v210 offset:32768\n" \
"v_mfma_f32_16x16x32_bf16 v[136:139], a[104:107], a[24:27], v[136:139]\n" \
"v_exp_f32_e64 v133, v133\n" \
"v_mfma_f32_16x16x32_bf16 v[136:139], a[108:111], a[28:31], v[136:139]\n" \
"v_cvt_pk_bf16_f32 v192, v128, v129\n" \
"v_cvt_pk_bf16_f32 v193, v130, v131\n" \
"v_mfma_f32_16x16x32_bf16 v[140:143], a[124:127], a[28:31], v[164:167]\n" \
"v_exp_f32_e64 v134, v134\n" \
"v_mfma_f32_16x16x32_bf16 v[140:143], a[120:123], a[24:27], v[140:143]\n" \
"ds_read_b128 a[92:95], v210 offset:36864\n" \
"v_cndmask_b32_e32 v225, v225, v224, vcc\n" \
"v_mfma_f32_16x16x32_bf16 v[140:143], a[116:119], a[20:23], v[140:143]\n" \
"s_add_u32 m0, %[m0b], 66560\n" \
"s_mov_b32 s61, 229376\n" \
"buffer_load_dwordx4 %[vg0], s[64:67], s61 offen offset:0 lds\n" \
"v_mfma_f32_16x16x32_bf16 v[140:143], a[112:115], a[16:19], v[140:143]\n" \
"v_exp_f32_e32 v135, v135\n" \
"v_mfma_f32_16x16x32_bf16 v[48:51], a[176:179], v[196:199], v[48:51]\n" \
"ds_read_b128 a[88:91], v209 offset:36864\n" \
"v_mfma_f32_16x16x32_bf16 v[48:51], a[144:147], v[188:191], v[48:51]\n" \
"v_exp_f32_e64 v136, v136\n" \
"v_mfma_f32_16x16x32_bf16 v[52:55], a[180:183], v[196:199], v[52:55]\n" \
"v_cvt_pk_bf16_f32 v194, v132, v133\n" \
"v_cvt_pk_bf16_f32 v195, v134, v135\n" \
"v_mfma_f32_16x16x32_bf16 v[52:55], a[148:151], v[188:191], v[52:55]\n" \
"v_exp_f32_e64 v137, v137\n" \
"v_mfma_f32_16x16x32_bf16 v[56:59], a[184:187], v[196:199], v[56:59]\n" \
"v_exp_f32_e64 v138, v138\n" \
"v_mfma_f32_16x16x32_bf16 v[56:59], a[152:155], v[188:191], v[56:59]\n" \
"ds_read_b128 a[84:87], v208 offset:36864\n" \
"v_mfma_f32_16x16x32_bf16 v[60:63], a[188:191], v[196:199], v[60:63]\n" \
"v_exp_f32_e64 v139, v139\n" \
"v_mfma_f32_16x16x32_bf16 v[60:63], a[156:159], v[188:191], v[60:63]\n" \
"v_mfma_f32_16x16x32_bf16 v[144:147], a[96:99], a[32:35], v[168:171]\n" \
"v_exp_f32_e64 v140, v140\n" \
"v_mfma_f32_16x16x32_bf16 v[144:147], a[100:103], a[36:39], v[144:147]\n" \
"ds_read_b128 a[80:83], %[kb0] offset:36864\n" \
"s_sub_u32 s61, s48, 0\n" \
"s_cmp_lt_u32 s61, 4\n" \
"s_cselect_b64 vcc, -1, 0\n" \
"v_mfma_f32_16x16x32_bf16 v[144:147], a[104:107], a[40:43], v[144:147]\n" \
"v_exp_f32_e32 v141, v141\n" \
"v_mfma_f32_16x16x32_bf16 v[144:147], a[108:111], a[44:47], v[144:147]\n" \
"v_cvt_pk_bf16_f32 v196, v136, v137\n" \
"v_cvt_pk_bf16_f32 v197, v138, v139\n" \
"v_mfma_f32_16x16x32_bf16 v[148:151], a[124:127], a[44:47], v[168:171]\n" \
"v_exp_f32_e64 v142, v142\n" \
"v_mfma_f32_16x16x32_bf16 v[148:151], a[120:123], a[40:43], v[148:151]\n" \
"v_readlane_b32 s55, %[tlo], 16\n" \
"v_mfma_f32_16x16x32_bf16 v[148:151], a[116:119], a[36:39], v[148:151]\n" \
"s_add_u32 m0, %[m0b], 67584\n" \
"s_mov_b32 s61, 458752\n" \
"buffer_load_dwordx4 %[vg0], s[64:67], s61 offen offset:0 lds\n" \
"v_mfma_f32_16x16x32_bf16 v[148:151], a[112:115], a[32:35], v[148:151]\n" \
"v_exp_f32_e64 v143, v143\n" \
"v_mfma_f32_16x16x32_bf16 v[96:99], a[160:163], v[204:207], v[96:99]\n" \
"ds_read_b64_tr_b16 a[160:161], %[vvb] offset:24576\n" \
"v_cmp_gt_u32_e64 s[56:57], 32, v231\n" \
"v_mfma_f32_16x16x32_bf16 v[96:99], a[128:131], v[220:223], v[96:99]\n" \
"v_exp_f32_e64 v144, v144\n" \
"v_mfma_f32_16x16x32_bf16 v[100:103], a[164:167], v[204:207], v[100:103]\n" \
"v_cvt_pk_bf16_f32 v198, v140, v141\n" \
"v_cvt_pk_bf16_f32 v199, v142, v143\n" \
"v_mfma_f32_16x16x32_bf16 v[100:103], a[132:135], v[220:223], v[100:103]\n" \
"v_exp_f32_e64 v145, v145\n" \
"v_mfma_f32_16x16x32_bf16 v[104:107], a[168:171], v[204:207], v[104:107]\n" \
"ds_read_b64_tr_b16 a[162:163], %[vvb] offset:28672\n" \
"ds_read_b64_tr_b16 a[164:165], v211 offset:24576\n" \
"v_lshlrev_b32_e64 v232, 2, v224\n" \
"v_mfma_f32_16x16x32_bf16 v[104:107], a[136:139], v[220:223], v[104:107]\n" \
"v_exp_f32_e64 v146, v146\n" \
"v_mfma_f32_16x16x32_bf16 v[108:111], a[172:175], v[204:207], v[108:111]\n" \
"ds_read_b64_tr_b16 a[166:167], v211 offset:28672\n" \
"v_mov_b32_e32 v233, 0x80000000\n" \
"v_mfma_f32_16x16x32_bf16 v[108:111], a[140:143], v[220:223], v[108:111]\n" \
"v_exp_f32_e64 v147, v147\n" \
"v_smfmac_f32_16x16x64_bf16 v[180:183], a[192:195], v[216:223], v228\n" \
"ds_read_b64_tr_b16 a[168:169], %[vvb] offset:25088\n" \
"v_mfma_f32_16x16x32_bf16 v[152:155], a[96:99], a[48:51], v[172:175]\n" \
"v_exp_f32_e64 v148, v148\n" \
"v_mfma_f32_16x16x32_bf16 v[152:155], a[100:103], a[52:55], v[152:155]\n" \
"ds_read_b64_tr_b16 a[170:171], %[vvb] offset:29184\n" \
"v_cndmask_b32_e64 v232, v233, v232, s[56:57]\n" \
"v_mfma_f32_16x16x32_bf16 v[152:155], a[104:107], a[56:59], v[152:155]\n" \
"v_exp_f32_e64 v149, v149\n" \
"v_mfma_f32_16x16x32_bf16 v[152:155], a[108:111], a[60:63], v[152:155]\n" \
"v_cvt_pk_bf16_f32 v200, v144, v145\n" \
"v_cvt_pk_bf16_f32 v201, v146, v147\n" \
"v_mfma_f32_16x16x32_bf16 v[156:159], a[124:127], a[60:63], v[172:175]\n" \
"v_exp_f32_e64 v150, v150\n" \
"v_mfma_f32_16x16x32_bf16 v[156:159], a[120:123], a[56:59], v[156:159]\n" \
"ds_read_b64_tr_b16 a[172:173], v211 offset:25088\n" \
"v_mov_b32_e64 v230, 1\n" \
"v_mfma_f32_16x16x32_bf16 v[156:159], a[116:119], a[52:55], v[156:159]\n" \
"s_add_u32 m0, %[m0b], 68608\n" \
"s_mov_b32 s61, 688128\n" \
"buffer_load_dwordx4 %[vg0], s[64:67], s61 offen offset:0 lds\n" \
"v_mfma_f32_16x16x32_bf16 v[156:159], a[112:115], a[48:51], v[156:159]\n" \
"v_exp_f32_e64 v151, v151\n" \
"v_mfma_f32_16x16x32_bf16 v[112:115], a[176:179], v[204:207], v[112:115]\n" \
"ds_read_b64_tr_b16 a[174:175], v211 offset:29184\n" \
"ds_read_b64_tr_b16 a[176:177], %[vvb] offset:25600\n" \
"v_mfma_f32_16x16x32_bf16 v[112:115], a[144:147], v[220:223], v[112:115]\n" \
"v_exp_f32_e64 v152, v152\n" \
"v_mfma_f32_16x16x32_bf16 v[116:119], a[180:183], v[204:207], v[116:119]\n" \
"v_cvt_pk_bf16_f32 v202, v148, v149\n" \
"v_cvt_pk_bf16_f32 v203, v150, v151\n" \
"v_mfma_f32_16x16x32_bf16 v[116:119], a[148:151], v[220:223], v[116:119]\n" \
"v_exp_f32_e64 v153, v153\n" \
"v_mfma_f32_16x16x32_bf16 v[120:123], a[184:187], v[204:207], v[120:123]\n" \
"v_exp_f32_e64 v154, v154\n" \
"v_mfma_f32_16x16x32_bf16 v[120:123], a[152:155], v[220:223], v[120:123]\n" \
"ds_read_b64_tr_b16 a[178:179], %[vvb] offset:29696\n" \
"buffer_store_dword v230, v232, %[srdz], s55 offen sc1\n" \
"v_mfma_f32_16x16x32_bf16 v[124:127], a[188:191], v[204:207], v[124:127]\n" \
"v_exp_f32_e64 v155, v155\n" \
"v_mfma_f32_16x16x32_bf16 v[124:127], a[156:159], v[220:223], v[124:127]\n" \
"ds_read_b64_tr_b16 a[180:181], v211 offset:25600\n" \
"s_waitcnt lgkmcnt(13)\n" \
"v_mfma_f32_16x16x32_bf16 v[128:131], a[64:67], a[0:3], v[160:163]\n" \
"v_exp_f32_e32 v156, v156\n" \
"v_mfma_f32_16x16x32_bf16 v[128:131], a[68:71], a[4:7], v[128:131]\n" \
"ds_read_b64_tr_b16 a[182:183], v211 offset:29696\n" \
"s_add_u32 m0, %[m0b], 16384\n" \
"s_mov_b32 s49, s48\n" \
"v_mfma_f32_16x16x32_bf16 v[128:131], a[72:75], a[8:11], v[128:131]\n" \
"v_exp_f32_e32 v157, v157\n" \
"v_mfma_f32_16x16x32_bf16 v[128:131], a[76:79], a[12:15], v[128:131]\n" \
"v_cvt_pk_bf16_f32 v204, v152, v153\n" \
"v_cvt_pk_bf16_f32 v205, v154, v155\n" \
"v_mfma_f32_16x16x32_bf16 v[132:135], a[92:95], a[12:15], v[160:163]\n" \
"v_exp_f32_e64 v158, v158\n" \
"v_mfma_f32_16x16x32_bf16 v[132:135], a[88:91], a[8:11], v[132:135]\n" \
"ds_read_b64_tr_b16 a[184:185], %[vvb] offset:26112\n" \
"s_lshl_b32 s49, s49, 18\n" \
"s_or_b32 s49, s49, s59\n" \
"s_waitcnt lgkmcnt(5)\n" \
"v_mfma_f32_16x16x32_bf16 v[132:135], a[84:87], a[4:7], v[132:135]\n" \
"buffer_load_dwordx4 %[wo0], %[srdt], s49 offen offset:0 lds\n" \
"v_mfma_f32_16x16x32_bf16 v[132:135], a[80:83], a[0:3], v[132:135]\n" \
"v_exp_f32_e32 v159, v159\n" \
"v_mfma_f32_16x16x32_bf16 v[64:67], a[128:131], v[216:219], v[64:67]\n" \
"ds_read_b64_tr_b16 a[186:187], %[vvb] offset:30208\n" \
"ds_read_b64_tr_b16 a[188:189], v211 offset:26112\n" \
"v_xor_b32_e32 v232, 0x80, %[vx0]\n" \
"v_mfma_f32_16x16x32_bf16 v[64:67], a[160:163], v[200:203], v[64:67]\n" \
"v_exp_f32_e64 v128, v128\n" \
"v_mfma_f32_16x16x32_bf16 v[68:71], a[132:135], v[216:219], v[68:71]\n" \
"v_cvt_pk_bf16_f32 v206, v156, v157\n" \
"v_cvt_pk_bf16_f32 v207, v158, v159\n" \
"v_mfma_f32_16x16x32_bf16 v[68:71], a[164:167], v[200:203], v[68:71]\n" \
"v_exp_f32_e64 v129, v129\n" \
"v_mfma_f32_16x16x32_bf16 v[72:75], a[136:139], v[216:219], v[72:75]\n" \
"ds_read_b64_tr_b16 a[190:191], v211 offset:30208\n" \
"ds_read_b64_tr_b16 a[128:129], %[vvb] offset:32768\n" \
"v_mfma_f32_16x16x32_bf16 v[72:75], a[168:171], v[200:203], v[72:75]\n" \
"v_exp_f32_e64 v130, v130\n" \
"v_mfma_f32_16x16x32_bf16 v[76:79], a[140:143], v[216:219], v[76:79]\n" \
"ds_read_b64_tr_b16 a[130:131], %[vvb] offset:36864\n" \
"ds_read_b64_tr_b16 a[132:133], v211 offset:32768\n" \
"v_mfma_f32_16x16x32_bf16 v[76:79], a[172:175], v[200:203], v[76:79]\n" \
"v_exp_f32_e64 v131, v131\n" \
"v_smfmac_f32_16x16x64_bf16 v[176:179], a[192:195], v[192:199], v228\n" \
"ds_read_b64_tr_b16 a[134:135], v211 offset:36864\n" \
"ds_read_b64_tr_b16 a[136:137], %[vvb] offset:33280\n" \
"global_load_dwordx2 a[240:241], %[vx0], %[px] offset:0\n" \
"v_mfma_f32_16x16x32_bf16 v[136:139], a[64:67], a[16:19], v[164:167]\n" \
"v_exp_f32_e64 v132, v132\n" \
"v_mfma_f32_16x16x32_bf16 v[136:139], a[68:71], a[20:23], v[136:139]\n" \
"ds_read_b64_tr_b16 a[138:139], %[vvb] offset:37376\n" \
"ds_read_b64_tr_b16 a[140:141], v211 offset:33280\n" \
"v_mfma_f32_16x16x32_bf16 v[136:139], a[72:75], a[24:27], v[136:139]\n" \
"v_exp_f32_e64 v133, v133\n" \
"v_mfma_f32_16x16x32_bf16 v[136:139], a[76:79], a[28:31], v[136:139]\n" \
"v_cvt_pk_bf16_f32 v184, v128, v129\n" \
"v_cvt_pk_bf16_f32 v185, v130, v131\n" \
"v_mfma_f32_16x16x32_bf16 v[140:143], a[92:95], a[28:31], v[164:167]\n" \
"v_exp_f32_e64 v134, v134\n" \
"v_mfma_f32_16x16x32_bf16 v[140:143], a[88:91], a[24:27], v[140:143]\n" \
"ds_read_b64_tr_b16 a[142:143], v211 offset:37376\n" \
"s_waitcnt lgkmcnt(8)\n" \
"v_mfma_f32_16x16x32_bf16 v[140:143], a[84:87], a[20:23], v[140:143]\n" \
"buffer_load_dwordx4 v212, %[srdt], s49 offen offset:1024 lds\n" \
"v_mfma_f32_16x16x32_bf16 v[140:143], a[80:83], a[16:19], v[140:143]\n" \
"v_exp_f32_e32 v135, v135\n" \
"v_mfma_f32_16x16x32_bf16 v[80:83], a[144:147], v[216:219], v[80:83]\n" \
"ds_read_b64_tr_b16 a[144:145], %[vvb] offset:33792\n" \
"ds_read_b64_tr_b16 a[146:147], %[vvb] offset:37888\n" \
"v_mfma_f32_16x16x32_bf16 v[80:83], a[176:179], v[200:203], v[80:83]\n" \
"v_exp_f32_e64 v136, v136\n" \
"v_mfma_f32_16x16x32_bf16 v[84:87], a[148:151], v[216:219], v[84:87]\n" \
"v_cvt_pk_bf16_f32 v186, v132, v133\n" \
"v_cvt_pk_bf16_f32 v187, v134, v135\n" \
"v_mfma_f32_16x16x32_bf16 v[84:87], a[180:183], v[200:203], v[84:87]\n" \
"v_exp_f32_e64 v137, v137\n" \
"v_mfma_f32_16x16x32_bf16 v[88:91], a[152:155], v[216:219], v[88:91]\n" \
"v_exp_f32_e64 v138, v138\n" \
"v_mfma_f32_16x16x32_bf16 v[88:91], a[184:187], v[200:203], v[88:91]\n" \
"ds_read_b64_tr_b16 a[148:149], v211 offset:33792\n" \
"ds_read_b64_tr_b16 a[150:151], v211 offset:37888\n" \
"v_mfma_f32_16x16x32_bf16 v[92:95], a[156:159], v[216:219], v[92:95]\n" \
"v_exp_f32_e64 v139, v139\n" \
"v_mfma_f32_16x16x32_bf16 v[92:95], a[188:191], v[200:203], v[92:95]\n" \
"ds_read_b64_tr_b16 a[152:153], %[vvb] offset:34304\n" \
"global_load_dwordx2 a[242:243], %[vx0], %[px] offset:1024\n" \
"v_mfma_f32_16x16x32_bf16 v[144:147], a[64:67], a[32:35], v[168:171]\n" \
"v_exp_f32_e64 v140, v140\n" \
"v_mfma_f32_16x16x32_bf16 v[144:147], a[68:71], a[36:39], v[144:147]\n" \
"ds_read_b64_tr_b16 a[154:155], %[vvb] offset:38400\n" \
"ds_read_b64_tr_b16 a[156:157], v211 offset:34304\n" \
"v_mfma_f32_16x16x32_bf16 v[144:147], a[72:75], a[40:43], v[144:147]\n" \
"v_exp_f32_e64 v141, v141\n" \
"v_mfma_f32_16x16x32_bf16 v[144:147], a[76:79], a[44:47], v[144:147]\n" \
"v_cvt_pk_bf16_f32 v188, v136, v137\n" \
"v_cvt_pk_bf16_f32 v189, v138, v139\n" \
"v_mfma_f32_16x16x32_bf16 v[148:151], a[92:95], a[44:47], v[168:171]\n" \
"v_exp_f32_e64 v142, v142\n" \
"v_mfma_f32_16x16x32_bf16 v[148:151], a[88:91], a[40:43], v[148:151]\n" \
"ds_read_b64_tr_b16 a[158:159], v211 offset:38400\n" \
"s_waitcnt lgkmcnt(8)\n" \
"v_mfma_f32_16x16x32_bf16 v[148:151], a[84:87], a[36:39], v[148:151]\n" \
"buffer_load_dwordx4 v213, %[srdt], s49 offen offset:2048 lds\n" \
"v_mfma_f32_16x16x32_bf16 v[148:151], a[80:83], a[32:35], v[148:151]\n" \
"v_exp_f32_e32 v143, v143\n" \
"v_mfma_f32_16x16x32_bf16 v[0:3], a[160:163], v[192:195], v[0:3]\n" \
"ds_read_b128 a[96:99], %[kb0] offset:40960\n" \
"v_mfma_f32_16x16x32_bf16 v[0:3], a[128:131], v[184:187], v[0:3]\n" \
"v_exp_f32_e64 v144, v144\n" \
"v_mfma_f32_16x16x32_bf16 v[4:7], a[164:167], v[192:195], v[4:7]\n" \
"v_cvt_pk_bf16_f32 v190, v140, v141\n" \
"v_cvt_pk_bf16_f32 v191, v142, v143\n" \
"v_mfma_f32_16x16x32_bf16 v[4:7], a[132:135], v[184:187], v[4:7]\n" \
"v_exp_f32_e64 v145, v145\n" \
"v_mfma_f32_16x16x32_bf16 v[8:11], a[168:171], v[192:195], v[8:11]\n" \
"ds_read_b128 a[100:103], v208 offset:40960\n" \
"v_mfma_f32_16x16x32_bf16 v[8:11], a[136:139], v[184:187], v[8:11]\n" \
"v_exp_f32_e64 v146, v146\n" \
"v_mfma_f32_16x16x32_bf16 v[12:15], a[172:175], v[192:195], v[12:15]\n" \
"ds_read_b128 a[104:107], v209 offset:40960\n" \
"v_mfma_f32_16x16x32_bf16 v[12:15], a[140:143], v[184:187], v[12:15]\n" \
"v_exp_f32_e64 v147, v147\n" \
"v_smfmac_f32_16x16x64_bf16 v[180:183], a[192:195], v[200:207], v228\n" \
"ds_read_b128 a[108:111], v210 offset:40960\n" \
"global_load_dwordx2 a[248:249], v232, %[px] offset:0\n" \
"v_mfma_f32_16x16x32_bf16 v[152:155], a[64:67], a[48:51], v[172:175]\n" \
"v_exp_f32_e64 v148, v148\n" \
"v_mfma_f32_16x16x32_bf16 v[152:155], a[68:71], a[52:55], v[152:155]\n" \
"ds_read_b128 a[124:127], v210 offset:45056\n" \
"v_mfma_f32_16x16x32_bf16 v[152:155], a[72:75], a[56:59], v[152:155]\n" \
"v_exp_f32_e64 v149, v149\n" \
"v_mfma_f32_16x16x32_bf16 v[152:155], a[76:79], a[60:63], v[152:155]\n" \
"v_cvt_pk_bf16_f32 v216, v144, v145\n" \
"v_cvt_pk_bf16_f32 v217, v146, v147\n" \
"v_mfma_f32_16x16x32_bf16 v[156:159], a[92:95], a[60:63], v[172:175]\n" \
"v_exp_f32_e64 v150, v150\n" \
"v_mfma_f32_16x16x32_bf16 v[156:159], a[88:91], a[56:59], v[156:159]\n" \
"ds_read_b128 a[120:123], v209 offset:45056\n" \
"s_waitcnt lgkmcnt(6)\n" \
"v_mfma_f32_16x16x32_bf16 v[156:159], a[84:87], a[52:55], v[156:159]\n" \
"buffer_load_dwordx4 v214, %[srdt], s49 offen offset:3072 lds\n" \
"v_mfma_f32_16x16x32_bf16 v[156:159], a[80:83], a[48:51], v[156:159]\n" \
"v_exp_f32_e32 v151, v151\n" \
"v_mfma_f32_16x16x32_bf16 v[16:19], a[176:179], v[192:195], v[16:19]\n" \
"ds_read_b128 a[116:119], v208 offset:45056\n" \
"v_mfma_f32_16x16x32_bf16 v[16:19], a[144:147], v[184:187], v[16:19]\n" \
"v_exp_f32_e64 v152, v152\n" \
"v_mfma_f32_16x16x32_bf16 v[20:23], a[180:183], v[192:195], v[20:23]\n" \
"v_cvt_pk_bf16_f32 v218, v148, v149\n" \
"v_cvt_pk_bf16_f32 v219, v150, v151\n" \
"v_mfma_f32_16x16x32_bf16 v[20:23], a[148:151], v[184:187], v[20:23]\n" \
"v_exp_f32_e64 v153, v153\n" \
"v_mfma_f32_16x16x32_bf16 v[24:27], a[184:187], v[192:195], v[24:27]\n" \
"v_exp_f32_e64 v154, v154\n" \
"v_mfma_f32_16x16x32_bf16 v[24:27], a[152:155], v[184:187], v[24:27]\n" \
"ds_read_b128 a[112:115], %[kb0] offset:45056\n" \
"global_load_dwordx2 a[250:251], v232, %[px] offset:1024\n" \
"v_mfma_f32_16x16x32_bf16 v[28:31], a[188:191], v[192:195], v[28:31]\n" \
"v_exp_f32_e64 v155, v155\n" \
"v_mfma_f32_16x16x32_bf16 v[28:31], a[156:159], v[184:187], v[28:31]\n" \
"s_waitcnt vmcnt(63) lgkmcnt(2)\n" \
"s_barrier\n" \
"v_mfma_f32_16x16x32_bf16 v[128:131], a[96:99], a[0:3], v[160:163]\n" \
"v_exp_f32_e64 v156, v156\n" \
"v_mfma_f32_16x16x32_bf16 v[128:131], a[100:103], a[4:7], v[128:131]\n" \
"v_lshrrev_b32_e64 v227, 5, v231\n" \
"v_mfma_f32_16x16x32_bf16 v[128:131], a[104:107], a[8:11], v[128:131]\n" \
"v_exp_f32_e64 v157, v157\n" \
"v_mfma_f32_16x16x32_bf16 v[128:131], a[108:111], a[12:15], v[128:131]\n" \
"v_cvt_pk_bf16_f32 v220, v152, v153\n" \
"v_cvt_pk_bf16_f32 v221, v154, v155\n" \
"v_mfma_f32_16x16x32_bf16 v[132:135], a[124:127], a[12:15], v[160:163]\n" \
"v_exp_f32_e64 v158, v158\n" \
"v_mfma_f32_16x16x32_bf16 v[132:135], a[120:123], a[8:11], v[132:135]\n" \
"v_lshlrev_b32_e32 v227, 10, v227\n" \
"s_waitcnt lgkmcnt(0)\n" \
"v_mfma_f32_16x16x32_bf16 v[132:135], a[116:119], a[4:7], v[132:135]\n" \
"s_add_u32 m0, %[m0b], 81920\n" \
"s_mov_b32 s61, 917504\n" \
"buffer_load_dwordx4 %[vg0], s[64:67], s61 offen offset:0 lds\n" \
"v_mfma_f32_16x16x32_bf16 v[132:135], a[112:115], a[0:3], v[132:135]\n" \
"v_exp_f32_e64 v159, v159\n" \
"v_mfma_f32_16x16x32_bf16 v[32:35], a[160:163], v[196:199], v[32:35]\n" \
"ds_read_b128 a[64:67], %[kb0] offset:49152\n" \
"v_add_u32_e32 v227, 0x10000, v227\n" \
"v_mfma_f32_16x16x32_bf16 v[32:35], a[128:131], v[188:191], v[32:35]\n" \
"v_exp_f32_e64 v128, v128\n" \
"v_mfma_f32_16x16x32_bf16 v[36:39], a[164:167], v[196:199], v[36:39]\n" \
"v_cvt_pk_bf16_f32 v222, v156, v157\n" \
"v_cvt_pk_bf16_f32 v223, v158, v159\n" \
"v_mfma_f32_16x16x32_bf16 v[36:39], a[132:135], v[188:191], v[36:39]\n" \
"v_exp_f32_e64 v129, v129\n" \
"v_mfma_f32_16x16x32_bf16 v[40:43], a[168:171], v[196:199], v[40:43]\n" \
"ds_read_b128 a[68:71], v208 offset:49152\n" \
"v_sub_u32_e64 v215, %[vvb], v227\n" \
"v_mfma_f32_16x16x32_bf16 v[40:43], a[136:139], v[188:191], v[40:43]\n" \
"v_exp_f32_e64 v130, v130\n" \
"v_mfma_f32_16x16x32_bf16 v[44:47], a[172:175], v[196:199], v[44:47]\n" \
"ds_read_b128 a[72:75], v209 offset:49152\n" \
"v_xor_b32_e64 v227, 32, v215\n" \
"v_mfma_f32_16x16x32_bf16 v[44:47], a[140:143], v[188:191], v[44:47]\n" \
"v_exp_f32_e64 v131, v131\n" \
"v_smfmac_f32_16x16x64_bf16 v[176:179], a[192:195], v[184:191], v228\n" \
"global_load_dwordx2 a[244:245], %[vx0], %[px] offset:64\n" \
"v_mfma_f32_16x16x32_bf16 v[136:139], a[96:99], a[16:19], v[164:167]\n" \
"v_exp_f32_e64 v132, v132\n" \
"v_mfma_f32_16x16x32_bf16 v[136:139], a[100:103], a[20:23], v[136:139]\n" \
"ds_read_b128 a[76:79], v210 offset:49152\n" \
"v_mfma_f32_16x16x32_bf16 v[136:139], a[104:107], a[24:27], v[136:139]\n" \
"v_exp_f32_e64 v133, v133\n" \
"v_mfma_f32_16x16x32_bf16 v[136:139], a[108:111], a[28:31], v[136:139]\n" \
"v_cvt_pk_bf16_f32 v192, v128, v129\n" \
"v_cvt_pk_bf16_f32 v193, v130, v131\n" \
"v_mfma_f32_16x16x32_bf16 v[140:143], a[124:127], a[28:31], v[164:167]\n" \
"v_exp_f32_e64 v134, v134\n" \
"v_mfma_f32_16x16x32_bf16 v[140:143], a[120:123], a[24:27], v[140:143]\n" \
"ds_read_b128 a[92:95], v210 offset:53248\n" \
"v_mfma_f32_16x16x32_bf16 v[140:143], a[116:119], a[20:23], v[140:143]\n" \
"s_add_u32 m0, %[m0b], 82944\n" \
"s_mov_b32 s61, 1146880\n" \
"buffer_load_dwordx4 %[vg0], s[64:67], s61 offen offset:0 lds\n" \
"v_mfma_f32_16x16x32_bf16 v[140:143], a[112:115], a[16:19], v[140:143]\n" \
"v_exp_f32_e64 v135, v135\n" \
"v_mfma_f32_16x16x32_bf16 v[48:51], a[176:179], v[196:199], v[48:51]\n" \
"ds_read_b128 a[88:91], v209 offset:53248\n" \
"v_mfma_f32_16x16x32_bf16 v[48:51], a[144:147], v[188:191], v[48:51]\n" \
"v_exp_f32_e64 v136, v136\n" \
"v_mfma_f32_16x16x32_bf16 v[52:55], a[180:183], v[196:199], v[52:55]\n" \
"v_cvt_pk_bf16_f32 v194, v132, v133\n" \
"v_cvt_pk_bf16_f32 v195, v134, v135\n" \
"v_mfma_f32_16x16x32_bf16 v[52:55], a[148:151], v[188:191], v[52:55]\n" \
"v_exp_f32_e64 v137, v137\n" \
"v_mfma_f32_16x16x32_bf16 v[56:59], a[184:187], v[196:199], v[56:59]\n" \
"v_exp_f32_e64 v138, v138\n" \
"v_mfma_f32_16x16x32_bf16 v[56:59], a[152:155], v[188:191], v[56:59]\n" \
"ds_read_b128 a[84:87], v208 offset:53248\n" \
"v_mfma_f32_16x16x32_bf16 v[60:63], a[188:191], v[196:199], v[60:63]\n" \
"v_exp_f32_e64 v139, v139\n" \
"v_mfma_f32_16x16x32_bf16 v[60:63], a[156:159], v[188:191], v[60:63]\n" \
"global_load_dwordx2 a[246:247], %[vx0], %[px] offset:1088\n" \
"v_mfma_f32_16x16x32_bf16 v[144:147], a[96:99], a[32:35], v[168:171]\n" \
"v_exp_f32_e64 v140, v140\n" \
"v_mfma_f32_16x16x32_bf16 v[144:147], a[100:103], a[36:39], v[144:147]\n" \
"ds_read_b128 a[80:83], %[kb0] offset:53248\n" \
"v_mfma_f32_16x16x32_bf16 v[144:147], a[104:107], a[40:43], v[144:147]\n" \
"v_exp_f32_e64 v141, v141\n" \
"v_mfma_f32_16x16x32_bf16 v[144:147], a[108:111], a[44:47], v[144:147]\n" \
"v_cvt_pk_bf16_f32 v196, v136, v137\n" \
"v_cvt_pk_bf16_f32 v197, v138, v139\n" \
"v_mfma_f32_16x16x32_bf16 v[148:151], a[124:127], a[44:47], v[168:171]\n" \
"v_exp_f32_e64 v142, v142\n" \
"v_mfma_f32_16x16x32_bf16 v[148:151], a[120:123], a[40:43], v[148:151]\n" \
"v_mfma_f32_16x16x32_bf16 v[148:151], a[116:119], a[36:39], v[148:151]\n" \
"s_add_u32 m0, %[m0b], 83968\n" \
"s_mov_b32 s61, 1376256\n" \
"buffer_load_dwordx4 %[vg0], s[64:67], s61 offen offset:0 lds\n" \
"v_mfma_f32_16x16x32_bf16 v[148:151], a[112:115], a[32:35], v[148:151]\n" \
"v_exp_f32_e64 v143, v143\n" \
"v_mfma_f32_16x16x32_bf16 v[96:99], a[160:163], v[204:207], v[96:99]\n" \
"ds_read_b64_tr_b16 a[160:161], %[vvb] offset:40960\n" \
"v_mfma_f32_16x16x32_bf16 v[96:99], a[128:131], v[220:223], v[96:99]\n" \
"v_exp_f32_e64 v144, v144\n" \
"v_mfma_f32_16x16x32_bf16 v[100:103], a[164:167], v[204:207], v[100:103]\n" \
"v_cvt_pk_bf16_f32 v198, v140, v141\n" \
"v_cvt_pk_bf16_f32 v199, v142, v143\n" \
"v_mfma_f32_16x16x32_bf16 v[100:103], a[132:135], v[220:223], v[100:103]\n" \
"v_exp_f32_e64 v145, v145\n" \
"v_mfma_f32_16x16x32_bf16 v[104:107], a[168:171], v[204:207], v[104:107]\n" \
"ds_read_b64_tr_b16 a[162:163], %[vvb] offset:45056\n" \
"ds_read_b64_tr_b16 a[164:165], v211 offset:40960\n" \
"v_mfma_f32_16x16x32_bf16 v[104:107], a[136:139], v[220:223], v[104:107]\n" \
"v_exp_f32_e64 v146, v146\n" \
"v_mfma_f32_16x16x32_bf16 v[108:111], a[172:175], v[204:207], v[108:111]\n" \
"ds_read_b64_tr_b16 a[166:167], v211 offset:45056\n" \
"v_mfma_f32_16x16x32_bf16 v[108:111], a[140:143], v[220:223], v[108:111]\n" \
"v_exp_f32_e64 v147, v147\n" \
"v_smfmac_f32_16x16x64_bf16 v[180:183], a[192:195], v[216:223], v228\n" \
"ds_read_b64_tr_b16 a[168:169], %[vvb] offset:41472\n" \
"global_load_dwordx2 a[252:253], v232, %[px] offset:64\n" \
"v_mfma_f32_16x16x32_bf16 v[152:155], a[96:99], a[48:51], v[172:175]\n" \
"v_exp_f32_e64 v148, v148\n" \
"v_mfma_f32_16x16x32_bf16 v[152:155], a[100:103], a[52:55], v[152:155]\n" \
"ds_read_b64_tr_b16 a[170:171], %[vvb] offset:45568\n" \
"v_mfma_f32_16x16x32_bf16 v[152:155], a[104:107], a[56:59], v[152:155]\n" \
"v_exp_f32_e64 v149, v149\n" \
"v_mfma_f32_16x16x32_bf16 v[152:155], a[108:111], a[60:63], v[152:155]\n" \
"v_cvt_pk_bf16_f32 v200, v144, v145\n" \
"v_cvt_pk_bf16_f32 v201, v146, v147\n" \
"v_mfma_f32_16x16x32_bf16 v[156:159], a[124:127], a[60:63], v[172:175]\n" \
"v_exp_f32_e64 v150, v150\n" \
"v_mfma_f32_16x16x32_bf16 v[156:159], a[120:123], a[56:59], v[156:159]\n" \
"ds_read_b64_tr_b16 a[172:173], v211 offset:41472\n" \
"v_mfma_f32_16x16x32_bf16 v[156:159], a[116:119], a[52:55], v[156:159]\n" \
"s_add_u32 m0, %[m0b], 84992\n" \
"s_mov_b32 s61, 1605632\n" \
"buffer_load_dwordx4 %[vg0], s[64:67], s61 offen offset:0 lds\n" \
"v_mfma_f32_16x16x32_bf16 v[156:159], a[112:115], a[48:51], v[156:159]\n" \
"v_exp_f32_e64 v151, v151\n" \
"v_mfma_f32_16x16x32_bf16 v[112:115], a[176:179], v[204:207], v[112:115]\n" \
"ds_read_b64_tr_b16 a[174:175], v211 offset:45568\n" \
"ds_read_b64_tr_b16 a[176:177], %[vvb] offset:41984\n" \
"v_mfma_f32_16x16x32_bf16 v[112:115], a[144:147], v[220:223], v[112:115]\n" \
"v_exp_f32_e64 v152, v152\n" \
"v_mfma_f32_16x16x32_bf16 v[116:119], a[180:183], v[204:207], v[116:119]\n" \
"v_cvt_pk_bf16_f32 v202, v148, v149\n" \
"v_cvt_pk_bf16_f32 v203, v150, v151\n" \
"v_mfma_f32_16x16x32_bf16 v[116:119], a[148:151], v[220:223], v[116:119]\n" \
"v_exp_f32_e64 v153, v153\n" \
"v_mfma_f32_16x16x32_bf16 v[120:123], a[184:187], v[204:207], v[120:123]\n" \
"v_exp_f32_e64 v154, v154\n" \
"v_mfma_f32_16x16x32_bf16 v[120:123], a[152:155], v[220:223], v[120:123]\n" \
"ds_read_b64_tr_b16 a[178:179], %[vvb] offset:46080\n" \
"global_load_dwordx2 a[254:255], v232, %[px] offset:1088\n" \
"v_mfma_f32_16x16x32_bf16 v[124:127], a[188:191], v[204:207], v[124:127]\n" \
"v_exp_f32_e64 v155, v155\n" \
"v_mfma_f32_16x16x32_bf16 v[124:127], a[156:159], v[220:223], v[124:127]\n" \
"ds_read_b64_tr_b16 a[180:181], v211 offset:41984\n" \
"s_waitcnt lgkmcnt(13)\n" \
"v_mfma_f32_16x16x32_bf16 v[128:131], a[64:67], a[0:3], v[160:163]\n" \
"v_exp_f32_e32 v156, v156\n" \
"v_mfma_f32_16x16x32_bf16 v[128:131], a[68:71], a[4:7], v[128:131]\n" \
"ds_read_b64_tr_b16 a[182:183], v211 offset:46080\n" \
"s_add_u32 m0, %[m0b], 32768\n" \
"s_cmp_gt_u32 s48, 0\n" \
"v_mfma_f32_16x16x32_bf16 v[128:131], a[72:75], a[8:11], v[128:131]\n" \
"v_exp_f32_e32 v157, v157\n" \
"v_mfma_f32_16x16x32_bf16 v[128:131], a[76:79], a[12:15], v[128:131]\n" \
"v_cvt_pk_bf16_f32 v204, v152, v153\n" \
"v_cvt_pk_bf16_f32 v205, v154, v155\n" \
"v_mfma_f32_16x16x32_bf16 v[132:135], a[92:95], a[12:15], v[160:163]\n" \
"v_exp_f32_e64 v158, v158\n" \
"v_mfma_f32_16x16x32_bf16 v[132:135], a[88:91], a[8:11], v[132:135]\n" \
"ds_read_b64_tr_b16 a[184:185], %[vvb] offset:42496\n" \
"s_cselect_b32 s49, 0, 1\n" \
"s_lshl_b32 s49, s49, 18\n" \
"s_or_b32 s49, s49, s58\n" \
"s_waitcnt lgkmcnt(5)\n" \
"v_mfma_f32_16x16x32_bf16 v[132:135], a[84:87], a[4:7], v[132:135]\n" \
"buffer_load_dwordx4 %[wo0], %[srdt], s49 offen offset:0 lds\n" \
"v_mfma_f32_16x16x32_bf16 v[132:135], a[80:83], a[0:3], v[132:135]\n" \
"v_exp_f32_e64 v159, v159\n" \
"v_mfma_f32_16x16x32_bf16 v[64:67], a[128:131], v[216:219], v[64:67]\n" \
"ds_read_b64_tr_b16 a[186:187], %[vvb] offset:46592\n" \
"ds_read_b64_tr_b16 a[188:189], v211 offset:42496\n" \
"v_mfma_f32_16x16x32_bf16 v[64:67], a[160:163], v[200:203], v[64:67]\n" \
"v_exp_f32_e64 v128, v128\n" \
"v_mfma_f32_16x16x32_bf16 v[68:71], a[132:135], v[216:219], v[68:71]\n" \
"v_cvt_pk_bf16_f32 v206, v156, v157\n" \
"v_cvt_pk_bf16_f32 v207, v158, v159\n" \
"v_mfma_f32_16x16x32_bf16 v[68:71], a[164:167], v[200:203], v[68:71]\n" \
"v_exp_f32_e64 v129, v129\n" \
"v_mfma_f32_16x16x32_bf16 v[72:75], a[136:139], v[216:219], v[72:75]\n" \
"ds_read_b64_tr_b16 a[190:191], v211 offset:46592\n" \
"ds_read_b64_tr_b16 a[128:129], %[vvb] offset:49152\n" \
"v_mfma_f32_16x16x32_bf16 v[72:75], a[168:171], v[200:203], v[72:75]\n" \
"v_exp_f32_e64 v130, v130\n" \
"v_mfma_f32_16x16x32_bf16 v[76:79], a[140:143], v[216:219], v[76:79]\n" \
"ds_read_b64_tr_b16 a[130:131], %[vvb] offset:53248\n" \
"ds_read_b64_tr_b16 a[132:133], v211 offset:49152\n" \
"v_mfma_f32_16x16x32_bf16 v[76:79], a[172:175], v[200:203], v[76:79]\n" \
"v_exp_f32_e64 v131, v131\n" \
"v_smfmac_f32_16x16x64_bf16 v[176:179], a[192:195], v[192:199], v228\n" \
"ds_read_b64_tr_b16 a[134:135], v211 offset:53248\n" \
"ds_read_b64_tr_b16 a[136:137], %[vvb] offset:49664\n" \
"v_mfma_f32_16x16x32_bf16 v[136:139], a[64:67], a[16:19], v[164:167]\n" \
"v_exp_f32_e64 v132, v132\n" \
"v_mfma_f32_16x16x32_bf16 v[136:139], a[68:71], a[20:23], v[136:139]\n" \
"ds_read_b64_tr_b16 a[138:139], %[vvb] offset:53760\n" \
"ds_read_b64_tr_b16 a[140:141], v211 offset:49664\n" \
"v_mfma_f32_16x16x32_bf16 v[136:139], a[72:75], a[24:27], v[136:139]\n" \
".if . - adaln_body != 36864\n" \
".error \"size model\"\n" \
".endif\n" \
"Lk8_%=:\n" \
"s_cbranch_execz Lk9_%=\n" \
"s_nop 0\n" \
"v_exp_f32_e64 v133, v133\n" \
"v_mfma_f32_16x16x32_bf16 v[136:139], a[76:79], a[28:31], v[136:139]\n" \
"v_cvt_pk_bf16_f32 v184, v128, v129\n" \
"v_cvt_pk_bf16_f32 v185, v130, v131\n" \
"v_mfma_f32_16x16x32_bf16 v[140:143], a[92:95], a[28:31], v[164:167]\n" \
"v_exp_f32_e64 v134, v134\n" \
"v_mfma_f32_16x16x32_bf16 v[140:143], a[88:91], a[24:27], v[140:143]\n" \
"ds_read_b64_tr_b16 a[142:143], v211 offset:53760\n" \
"s_waitcnt lgkmcnt(8)\n" \
"v_mfma_f32_16x16x32_bf16 v[140:143], a[84:87], a[20:23], v[140:143]\n" \
"buffer_load_dwordx4 v212, %[srdt], s49 offen offset:1024 lds\n" \
"v_mfma_f32_16x16x32_bf16 v[140:143], a[80:83], a[16:19], v[140:143]\n" \
"v_exp_f32_e32 v135, v135\n" \
"v_mfma_f32_16x16x32_bf16 v[80:83], a[144:147], v[216:219], v[80:83]\n" \
"ds_read_b64_tr_b16 a[144:145], %[vvb] offset:50176\n" \
"ds_read_b64_tr_b16 a[146:147], %[vvb] offset:54272\n" \
"v_mfma_f32_16x16x32_bf16 v[80:83], a[176:179], v[200:203], v[80:83]\n" \
"v_exp_f32_e64 v136, v136\n" \
"v_mfma_f32_16x16x32_bf16 v[84:87], a[148:151], v[216:219], v[84:87]\n" \
"v_cvt_pk_bf16_f32 v186, v132, v133\n" \
"v_cvt_pk_bf16_f32 v187, v134, v135\n" \
"v_mfma_f32_16x16x32_bf16 v[84:87], a[180:183], v[200:203], v[84:87]\n" \
"v_exp_f32_e64 v137, v137\n" \
"v_mfma_f32_16x16x32_bf16 v[88:91], a[152:155], v[216:219], v[88:91]\n" \
"v_exp_f32_e64 v138, v138\n" \
"v_mfma_f32_16x16x32_bf16 v[88:91], a[184:187], v[200:203], v[88:91]\n" \
"ds_read_b64_tr_b16 a[148:149], v211 offset:50176\n" \
"ds_read_b64_tr_b16 a[150:151], v211 offset:54272\n" \
"v_readlane_b32 s64, %[tlo], 10\n" \
"v_mfma_f32_16x16x32_bf16 v[92:95], a[156:159], v[216:219], v[92:95]\n" \
"v_exp_f32_e64 v139, v139\n" \
"v_mfma_f32_16x16x32_bf16 v[92:95], a[188:191], v[200:203], v[92:95]\n" \
"ds_read_b64_tr_b16 a[152:153], %[vvb] offset:50688\n" \
"v_readlane_b32 s65, %[thi], 10\n" \
"v_mfma_f32_16x16x32_bf16 v[144:147], a[64:67], a[32:35], v[168:171]\n" \
"v_exp_f32_e64 v140, v140\n" \
"v_mfma_f32_16x16x32_bf16 v[144:147], a[68:71], a[36:39], v[144:147]\n" \
"ds_read_b64_tr_b16 a[154:155], %[vvb] offset:54784\n" \
"ds_read_b64_tr_b16 a[156:157], v211 offset:50688\n" \
"v_mfma_f32_16x16x32_bf16 v[144:147], a[72:75], a[40:43], v[144:147]\n" \
"v_exp_f32_e64 v141, v141\n" \
"v_mfma_f32_16x16x32_bf16 v[144:147], a[76:79], a[44:47], v[144:147]\n" \
"v_cvt_pk_bf16_f32 v188, v136, v137\n" \
"v_cvt_pk_bf16_f32 v189, v138, v139\n" \
"v_mfma_f32_16x16x32_bf16 v[148:151], a[92:95], a[44:47], v[168:171]\n" \
"v_exp_f32_e64 v142, v142\n" \
"v_mfma_f32_16x16x32_bf16 v[148:151], a[88:91], a[40:43], v[148:151]\n" \
"ds_read_b64_tr_b16 a[158:159], v211 offset:54784\n" \
"s_waitcnt lgkmcnt(8)\n" \
"v_mfma_f32_16x16x32_bf16 v[148:151], a[84:87], a[36:39], v[148:151]\n" \
"buffer_load_dwordx4 v213, %[srdt], s49 offen offset:2048 lds\n" \
"v_max3_f32 v232, v33, v38, v43\n" \
"v_mfma_f32_16x16x32_bf16 v[148:151], a[80:83], a[32:35], v[148:151]\n" \
"v_exp_f32_e32 v143, v143\n" \
"v_mfma_f32_16x16x32_bf16 v[0:3], a[160:163], v[192:195], v[0:3]\n" \
"ds_read_b128 a[96:99], %[kb0] offset:57344\n" \
"v_min3_f32 v233, v33, v38, v43\n" \
"v_mfma_f32_16x16x32_bf16 v[0:3], a[128:131], v[184:187], v[0:3]\n" \
"v_exp_f32_e64 v144, v144\n" \
"v_mfma_f32_16x16x32_bf16 v[4:7], a[164:167], v[192:195], v[4:7]\n" \
"v_cvt_pk_bf16_f32 v190, v140, v141\n" \
"v_cvt_pk_bf16_f32 v191, v142, v143\n" \
"v_mfma_f32_16x16x32_bf16 v[4:7], a[132:135], v[184:187], v[4:7]\n" \
"v_exp_f32_e64 v145, v145\n" \
"v_mfma_f32_16x16x32_bf16 v[8:11], a[168:171], v[192:195], v[8:11]\n" \
"ds_read_b128 a[100:103], v208 offset:57344\n" \
"v_max3_f32 v232, v232, v48, v53\n" \
"v_mfma_f32_16x16x32_bf16 v[8:11], a[136:139], v[184:187], v[8:11]\n" \
"v_exp_f32_e64 v146, v146\n" \
"v_mfma_f32_16x16x32_bf16 v[12:15], a[172:175], v[192:195], v[12:15]\n" \
"ds_read_b128 a[104:107], v209 offset:57344\n" \
"v_min3_f32 v233, v233, v48, v53\n" \
"v_mfma_f32_16x16x32_bf16 v[12:15], a[140:143], v[184:187], v[12:15]\n" \
"v_exp_f32_e64 v147, v147\n" \
"v_smfmac_f32_16x16x64_bf16 v[180:183], a[192:195], v[200:207], v228\n" \
"ds_read_b128 a[108:111], v210 offset:57344\n" \
"buffer_load_dword v229, v226, %[srdz], s50 offen sc1\n" \
"v_mfma_f32_16x16x32_bf16 v[152:155], a[64:67], a[48:51], v[172:175]\n" \
"v_exp_f32_e64 v148, v148\n" \
"v_mfma_f32_16x16x32_bf16 v[152:155], a[68:71], a[52:55], v[152:155]\n" \
"ds_read_b128 a[124:127], v210 offset:61440\n" \
"v_sub_f32_e64 v234, v232, v233\n" \
"v_mfma_f32_16x16x32_bf16 v[152:155], a[72:75], a[56:59], v[152:155]\n" \
"v_exp_f32_e64 v149, v149\n" \
"v_mfma_f32_16x16x32_bf16 v[152:155], a[76:79], a[60:63], v[152:155]\n" \
"v_cvt_pk_bf16_f32 v216, v144, v145\n" \
"v_cvt_pk_bf16_f32 v217, v146, v147\n" \
"v_mfma_f32_16x16x32_bf16 v[156:159], a[92:95], a[60:63], v[172:175]\n" \
"v_exp_f32_e64 v150, v150\n" \
"v_mfma_f32_16x16x32_bf16 v[156:159], a[88:91], a[56:59], v[156:159]\n" \
"ds_read_b128 a[120:123], v209 offset:61440\n" \
"s_waitcnt lgkmcnt(6)\n" \
"v_mfma_f32_16x16x32_bf16 v[156:159], a[84:87], a[52:55], v[156:159]\n" \
"buffer_load_dwordx4 v214, %[srdt], s49 offen offset:3072 lds\n" \
"v_mfma_f32_16x16x32_bf16 v[156:159], a[80:83], a[48:51], v[156:159]\n" \
"v_exp_f32_e32 v151, v151\n" \
"v_mfma_f32_16x16x32_bf16 v[16:19], a[176:179], v[192:195], v[16:19]\n" \
"ds_read_b128 a[116:119], v208 offset:61440\n" \
"v_mfma_f32_16x16x32_bf16 v[16:19], a[144:147], v[184:187], v[16:19]\n" \
"v_exp_f32_e64 v152, v152\n" \
"v_mfma_f32_16x16x32_bf16 v[20:23], a[180:183], v[192:195], v[20:23]\n" \
"v_cvt_pk_bf16_f32 v218, v148, v149\n" \
"v_cvt_pk_bf16_f32 v219, v150, v151\n" \
"v_mfma_f32_16x16x32_bf16 v[20:23], a[148:151], v[184:187], v[20:23]\n" \
"v_exp_f32_e64 v153, v153\n" \
"v_mfma_f32_16x16x32_bf16 v[24:27], a[184:187], v[192:195], v[24:27]\n" \
"v_exp_f32_e64 v154, v154\n" \
"v_mfma_f32_16x16x32_bf16 v[24:27], a[152:155], v[184:187], v[24:27]\n" \
"ds_read_b128 a[112:115], %[kb0] offset:61440\n" \
"v_mfma_f32_16x16x32_bf16 v[28:31], a[188:191], v[192:195], v[28:31]\n" \
"v_exp_f32_e64 v155, v155\n" \
"v_mfma_f32_16x16x32_bf16 v[28:31], a[156:159], v[184:187], v[28:31]\n" \
"s_waitcnt vmcnt(28) lgkmcnt(2)\n" \
"s_barrier\n" \
"v_mfma_f32_16x16x32_bf16 v[128:131], a[96:99], a[0:3], v[160:163]\n" \
"v_exp_f32_e64 v156, v156\n" \
"v_mfma_f32_16x16x32_bf16 v[128:131], a[100:103], a[4:7], v[128:131]\n" \
"s_add_u32 m0, %[m0b], 98304\n" \
"s_cmp_gt_u32 s48, 0\n" \
"v_mfma_f32_16x16x32_bf16 v[128:131], a[104:107], a[8:11], v[128:131]\n" \
"v_exp_f32_e32 v157, v157\n" \
"v_mfma_f32_16x16x32_bf16 v[128:131], a[108:111], a[12:15], v[128:131]\n" \
"v_cvt_pk_bf16_f32 v220, v152, v153\n" \
"v_cvt_pk_bf16_f32 v221, v154, v155\n" \
"v_mfma_f32_16x16x32_bf16 v[132:135], a[124:127], a[12:15], v[160:163]\n" \
"v_exp_f32_e64 v158, v158\n" \
"v_mfma_f32_16x16x32_bf16 v[132:135], a[120:123], a[8:11], v[132:135]\n" \
"s_cselect_b32 s49, 0, 1\n" \
"s_lshl_b32 s49, s49, 18\n" \
"s_or_b32 s49, s49, s59\n" \
"v_max3_f32 v232, v97, v102, v107\n" \
"s_waitcnt lgkmcnt(0)\n" \
"v_mfma_f32_16x16x32_bf16 v[132:135], a[116:119], a[4:7], v[132:135]\n" \
"buffer_load_dwordx4 %[wo0], %[srdt], s49 offen offset:0 lds\n" \
"v_mfma_f32_16x16x32_bf16 v[132:135], a[112:115], a[0:3], v[132:135]\n" \
"v_exp_f32_e64 v159, v159\n" \
"v_mfma_f32_16x16x32_bf16 v[32:35], a[160:163], v[196:199], v[32:35]\n" \
"ds_read_b64_tr_b16 a[196:197], v215 offset:0\n" \
"v_min3_f32 v233, v97, v102, v107\n" \
"v_mfma_f32_16x16x32_bf16 v[32:35], a[128:131], v[188:191], v[32:35]\n" \
"v_exp_f32_e64 v128, v128\n" \
"v_mfma_f32_16x16x32_bf16 v[36:39], a[164:167], v[196:199], v[36:39]\n" \
"v_cvt_pk_bf16_f32 v222, v156, v157\n" \
"v_cvt_pk_bf16_f32 v223, v158, v159\n" \
"v_mfma_f32_16x16x32_bf16 v[36:39], a[132:135], v[188:191], v[36:39]\n" \
"v_exp_f32_e64 v129, v129\n" \
"v_mfma_f32_16x16x32_bf16 v[40:43], a[168:171], v[196:199], v[40:43]\n" \
"ds_read_b64_tr_b16 a[198:199], v215 offset:2048\n" \
"v_max3_f32 v232, v232, v112, v117\n" \
"v_mfma_f32_16x16x32_bf16 v[40:43], a[136:139], v[188:191], v[40:43]\n" \
"v_exp_f32_e64 v130, v130\n" \
"v_mfma_f32_16x16x32_bf16 v[44:47], a[172:175], v[196:199], v[44:47]\n" \
"ds_read_b64_tr_b16 a[200:201], v215 offset:4096\n" \
"v_min3_f32 v233, v233, v112, v117\n" \
"v_mfma_f32_16x16x32_bf16 v[44:47], a[140:143], v[188:191], v[44:47]\n" \
"v_exp_f32_e64 v131, v131\n" \
"v_smfmac_f32_16x16x64_bf16 v[176:179], a[192:195], v[184:191], v228\n" \
"v_sub_f32_e32 v232, v232, v233\n" \
"v_min_f32_e32 v234, v234, v232\n" \
"v_mfma_f32_16x16x32_bf16 v[136:139], a[96:99], a[16:19], v[164:167]\n" \
"v_exp_f32_e64 v132, v132\n" \
"v_mfma_f32_16x16x32_bf16 v[136:139], a[100:103], a[20:23], v[136:139]\n" \
"ds_read_b64_tr_b16 a[202:203], v215 offset:6144\n" \
"v_max3_f32 v232, v1, v6, v11\n" \
"v_mfma_f32_16x16x32_bf16 v[136:139], a[104:107], a[24:27], v[136:139]\n" \
"v_exp_f32_e64 v133, v133\n" \
"v_mfma_f32_16x16x32_bf16 v[136:139], a[108:111], a[28:31], v[136:139]\n" \
"v_cvt_pk_bf16_f32 v192, v128, v129\n" \
"v_cvt_pk_bf16_f32 v193, v130, v131\n" \
"v_mfma_f32_16x16x32_bf16 v[140:143], a[124:127], a[28:31], v[164:167]\n" \
"v_exp_f32_e64 v134, v134\n" \
"v_mfma_f32_16x16x32_bf16 v[140:143], a[120:123], a[24:27], v[140:143]\n" \
"ds_read_b64_tr_b16 a[204:205], v215 offset:8192\n" \
"v_min3_f32 v233, v1, v6, v11\n" \
"v_mfma_f32_16x16x32_bf16 v[140:143], a[116:119], a[20:23], v[140:143]\n" \
"buffer_load_dwordx4 v212, %[srdt], s49 offen offset:1024 lds\n" \
"v_max3_f32 v232, v232, v16, v21\n" \
"v_mfma_f32_16x16x32_bf16 v[140:143], a[112:115], a[16:19], v[140:143]\n" \
"v_exp_f32_e64 v135, v135\n" \
"v_mfma_f32_16x16x32_bf16 v[48:51], a[176:179], v[196:199], v[48:51]\n" \
"ds_read_b64_tr_b16 a[206:207], v215 offset:10240\n" \
"v_add_u32_e32 v208, 0x1000, %[oq0]\n" \
"v_mfma_f32_16x16x32_bf16 v[48:51], a[144:147], v[188:191], v[48:51]\n" \
"v_exp_f32_e64 v136, v136\n" \
"v_mfma_f32_16x16x32_bf16 v[52:55], a[180:183], v[196:199], v[52:55]\n" \
"v_cvt_pk_bf16_f32 v194, v132, v133\n" \
"v_cvt_pk_bf16_f32 v195, v134, v135\n" \
"v_mfma_f32_16x16x32_bf16 v[52:55], a[148:151], v[188:191], v[52:55]\n" \
"v_exp_f32_e64 v137, v137\n" \
"v_mfma_f32_16x16x32_bf16 v[56:59], a[184:187], v[196:199], v[56:59]\n" \
"v_exp_f32_e64 v138, v138\n" \
"v_mfma_f32_16x16x32_bf16 v[56:59], a[152:155], v[188:191], v[56:59]\n" \
"ds_read_b64_tr_b16 a[208:209], v215 offset:12288\n" \
"v_add_u32_e32 v209, 0x2000, %[oq0]\n" \
"v_mfma_f32_16x16x32_bf16 v[60:63], a[188:191], v[196:199], v[60:63]\n" \
"v_exp_f32_e64 v139, v139\n" \
"v_mfma_f32_16x16x32_bf16 v[60:63], a[156:159], v[188:191], v[60:63]\n" \
"v_add_u32_e32 v210, 0x3000, %[oq0]\n" \
"v_mfma_f32_16x16x32_bf16 v[144:147], a[96:99], a[32:35], v[168:171]\n" \
"v_exp_f32_e64 v140, v140\n" \
"v_mfma_f32_16x16x32_bf16 v[144:147], a[100:103], a[36:39], v[144:147]\n" \
"ds_read_b64_tr_b16 a[210:211], v215 offset:14336\n" \
"v_min3_f32 v233, v233, v16, v21\n" \
"v_mfma_f32_16x16x32_bf16 v[144:147], a[104:107], a[40:43], v[144:147]\n" \
"v_exp_f32_e64 v141, v141\n" \
"v_mfma_f32_16x16x32_bf16 v[144:147], a[108:111], a[44:47], v[144:147]\n" \
"v_cvt_pk_bf16_f32 v196, v136, v137\n" \
"v_cvt_pk_bf16_f32 v197, v138, v139\n" \
"v_mfma_f32_16x16x32_bf16 v[148:151], a[124:127], a[44:47], v[168:171]\n" \
"v_exp_f32_e64 v142, v142\n" \
"v_mfma_f32_16x16x32_bf16 v[148:151], a[120:123], a[40:43], v[148:151]\n" \
"v_sub_f32_e32 v232, v232, v233\n" \
"v_min_f32_e32 v234, v234, v232\n" \
"v_mfma_f32_16x16x32_bf16 v[148:151], a[116:119], a[36:39], v[148:151]\n" \
"buffer_load_dwordx4 v213, %[srdt], s49 offen offset:2048 lds\n" \
"v_max3_f32 v232, v65, v70, v75\n" \
"v_mfma_f32_16x16x32_bf16 v[148:151], a[112:115], a[32:35], v[148:151]\n" \
"v_exp_f32_e64 v143, v143\n" \
"v_mfma_f32_16x16x32_bf16 v[96:99], a[160:163], v[204:207], v[96:99]\n" \
"ds_read_b64_tr_b16 a[160:161], %[vvb] offset:57344\n" \
"v_min3_f32 v233, v65, v70, v75\n" \
"v_mfma_f32_16x16x32_bf16 v[96:99], a[128:131], v[220:223], v[96:99]\n" \
"v_exp_f32_e64 v144, v144\n" \
"v_mfma_f32_16x16x32_bf16 v[100:103], a[164:167], v[204:207], v[100:103]\n" \
"v_cvt_pk_bf16_f32 v198, v140, v141\n" \
"v_cvt_pk_bf16_f32 v199, v142, v143\n" \
"v_mfma_f32_16x16x32_bf16 v[100:103], a[132:135], v[220:223], v[100:103]\n" \
"v_exp_f32_e64 v145, v145\n" \
"v_mfma_f32_16x16x32_bf16 v[104:107], a[168:171], v[204:207], v[104:107]\n" \
"ds_read_b64_tr_b16 a[162:163], %[vvb] offset:61440\n" \
"ds_read_b64_tr_b16 a[164:165], v211 offset:57344\n" \
"v_mfma_f32_16x16x32_bf16 v[104:107], a[136:139], v[220:223], v[104:107]\n" \
"v_exp_f32_e64 v146, v146\n" \
"v_mfma_f32_16x16x32_bf16 v[108:111], a[172:175], v[204:207], v[108:111]\n" \
"ds_read_b64_tr_b16 a[166:167], v211 offset:61440\n" \
"v_max3_f32 v232, v232, v80, v85\n" \
"v_mfma_f32_16x16x32_bf16 v[108:111], a[140:143], v[220:223], v[108:111]\n" \
"v_exp_f32_e64 v147, v147\n" \
"v_smfmac_f32_16x16x64_bf16 v[180:183], a[192:195], v[216:223], v228\n" \
"ds_read_b64_tr_b16 a[168:169], %[vvb] offset:57856\n" \
"v_min3_f32 v233, v233, v80, v85\n" \
"v_mfma_f32_16x16x32_bf16 v[152:155], a[96:99], a[48:51], v[172:175]\n" \
"v_exp_f32_e64 v148, v148\n" \
"v_mfma_f32_16x16x32_bf16 v[152:155], a[100:103], a[52:55], v[152:155]\n" \
"ds_read_b64_tr_b16 a[170:171], %[vvb] offset:61952\n" \
"v_sub_f32_e64 v232, v232, v233\n" \
"v_mfma_f32_16x16x32_bf16 v[152:155], a[104:107], a[56:59], v[152:155]\n" \
"v_exp_f32_e64 v149, v149\n" \
"v_mfma_f32_16x16x32_bf16 v[152:155], a[108:111], a[60:63], v[152:155]\n" \
"v_cvt_pk_bf16_f32 v200, v144, v145\n" \
"v_cvt_pk_bf16_f32 v201, v146, v147\n" \
"v_mfma_f32_16x16x32_bf16 v[156:159], a[124:127], a[60:63], v[172:175]\n" \
"v_exp_f32_e64 v150, v150\n" \
"v_mfma_f32_16x16x32_bf16 v[156:159], a[120:123], a[56:59], v[156:159]\n" \
"ds_read_b64_tr_b16 a[172:173], v211 offset:57856\n" \
"v_min_f32_e64 v234, v234, v232\n" \
"v_mfma_f32_16x16x32_bf16 v[156:159], a[116:119], a[52:55], v[156:159]\n" \
"buffer_load_dwordx4 v214, %[srdt], s49 offen offset:3072 lds\n" \
"v_accvgpr_read_b32 v233, a228\n" \
"v_mul_f32_e32 v233, 0x3a000000, v233\n" \
"v_mfma_f32_16x16x32_bf16 v[156:159], a[112:115], a[48:51], v[156:159]\n" \
"v_exp_f32_e64 v151, v151\n" \
"v_mfma_f32_16x16x32_bf16 v[112:115], a[176:179], v[204:207], v[112:115]\n" \
"ds_read_b64_tr_b16 a[174:175], v211 offset:61952\n" \
"ds_read_b64_tr_b16 a[176:177], %[vvb] offset:58368\n" \
"v_mfma_f32_16x16x32_bf16 v[112:115], a[144:147], v[220:223], v[112:115]\n" \
"v_exp_f32_e64 v152, v152\n" \
"v_mfma_f32_16x16x32_bf16 v[116:119], a[180:183], v[204:207], v[116:119]\n" \
"v_cvt_pk_bf16_f32 v202, v148, v149\n" \
"v_cvt_pk_bf16_f32 v203, v150, v151\n" \
"v_mfma_f32_16x16x32_bf16 v[116:119], a[148:151], v[220:223], v[116:119]\n" \
"v_exp_f32_e64 v153, v153\n" \
"v_mfma_f32_16x16x32_bf16 v[120:123], a[184:187], v[204:207], v[120:123]\n" \
"v_exp_f32_e64 v154, v154\n" \
"v_mfma_f32_16x16x32_bf16 v[120:123], a[152:155], v[220:223], v[120:123]\n" \
"ds_read_b64_tr_b16 a[178:179], %[vvb] offset:62464\n" \
"s_waitcnt vmcnt(5)\n" \
"v_cmp_eq_u32_e64 s[56:57], 0, v229\n" \
"s_cmp_lg_u64 s[56:57], 0\n" \
"s_cbranch_scc1 Lspk_%=\n" \
"Lbkk_%=:\n" \
"v_mfma_f32_16x16x32_bf16 v[124:127], a[188:191], v[204:207], v[124:127]\n" \
"v_exp_f32_e32 v155, v155\n" \
"v_mfma_f32_16x16x32_bf16 v[124:127], a[156:159], v[220:223], v[124:127]\n" \
"ds_read_b64_tr_b16 a[180:181], v211 offset:58368\n" \
"ds_read_b64_tr_b16 a[182:183], v211 offset:62464\n" \
"ds_read_b64_tr_b16 a[184:185], %[vvb] offset:58880\n" \
"ds_read_b64_tr_b16 a[186:187], %[vvb] offset:62976\n" \
"ds_read_b64_tr_b16 a[188:189], v211 offset:58880\n" \
"ds_read_b64_tr_b16 a[190:191], v211 offset:62976\n" \
"s_waitcnt lgkmcnt(8)\n" \
"s_nop 0\n" \
"v_smfmac_f32_16x16x64_bf16 v[176:179], a[192:195], v[192:199], v228\n" \
"v_exp_f32_e64 v156, v156\n" \
"ds_read_b64_tr_b16 a[212:213], v227 offset:0\n" \
"v_mfma_f32_16x16x32_bf16 v[0:3], a[160:163], v[192:195], v[0:3]\n" \
"v_exp_f32_e64 v157, v157\n" \
"ds_read_b64_tr_b16 a[214:215], v227 offset:2048\n" \
"v_mfma_f32_16x16x32_bf16 v[4:7], a[164:167], v[192:195], v[4:7]\n" \
"v_cvt_pk_bf16_f32 v204, v152, v153\n" \
"ds_read_b64_tr_b16 a[216:217], v227 offset:4096\n" \
"v_mfma_f32_16x16x32_bf16 v[8:11], a[168:171], v[192:195], v[8:11]\n" \
"v_cvt_pk_bf16_f32 v205, v154, v155\n" \
"ds_read_b64_tr_b16 a[218:219], v227 offset:6144\n" \
"v_mfma_f32_16x16x32_bf16 v[12:15], a[172:175], v[192:195], v[12:15]\n" \
"v_exp_f32_e64 v158, v158\n" \
"ds_read_b64_tr_b16 a[220:221], v227 offset:8192\n" \
"s_waitcnt lgkmcnt(5)\n" \
"s_nop 0\n" \
"v_mfma_f32_16x16x32_bf16 v[16:19], a[176:179], v[192:195], v[16:19]\n" \
"v_exp_f32_e64 v159, v159\n" \
"ds_read_b64_tr_b16 a[222:223], v227 offset:10240\n" \
"v_mfma_f32_16x16x32_bf16 v[20:23], a[180:183], v[192:195], v[20:23]\n" \
"v_cvt_pk_bf16_f32 v206, v156, v157\n" \
"ds_read_b64_tr_b16 a[224:225], v227 offset:12288\n" \
"v_accvgpr_read_b32 v229, a228\n" \
"v_mfma_f32_16x16x32_bf16 v[24:27], a[184:187], v[192:195], v[24:27]\n" \
"v_cvt_pk_bf16_f32 v207, v158, v159\n" \
"ds_read_b64_tr_b16 a[226:227], v227 offset:14336\n" \
"v_mfma_f32_16x16x32_bf16 v[28:31], a[188:191], v[192:195], v[28:31]\n" \
"v_add_f32_e64 v176, v176, v177\n" \
"v_smfmac_f32_16x16x64_bf16 v[180:183], a[192:195], v[200:207], v228\n" \
"v_add_f32_e32 v178, v178, v179\n" \
"v_add_f32_e32 v231, v176, v178\n" \
"v_rcp_f32_e32 v176, v176\n" \
"v_rcp_f32_e32 v178, v178\n" \
"v_mul_f32_e64 v177, -v229, v176\n" \
"v_mul_f32_e64 v179, -v229, v178\n" \
"v_mfma_f32_16x16x32_bf16 v[32:35], a[160:163], v[196:199], v[32:35]\n" \
"v_pk_fma_f32 v[0:1], v[0:1], v[176:177], v[176:177] op_sel:[0,0,1] op_sel_hi:[1,0,1]\n" \
"v_pk_fma_f32 v[2:3], v[2:3], v[176:177], v[176:177] op_sel:[0,0,1] op_sel_hi:[1,0,1]\n" \
"v_pk_fma_f32 v[4:5], v[4:5], v[176:177], v[176:177] op_sel:[0,0,1] op_sel_hi:[1,0,1]\n" \
"v_pk_fma_f32 v[6:7], v[6:7], v[176:177], v[176:177] op_sel:[0,0,1] op_sel_hi:[1,0,1]\n" \
"v_cvt_pk_bf16_f32 v0, v0, v1\n" \
"v_cvt_pk_bf16_f32 v1, v2, v3\n" \
"v_cvt_pk_bf16_f32 v2, v4, v5\n" \
"v_cvt_pk_bf16_f32 v3, v6, v7\n" \
"v_mfma_f32_16x16x32_bf16 v[36:39], a[164:167], v[196:199], v[36:39]\n" \
"s_mov_b32 s55, 0x4e800000\n" \
"v_mov_b32_e32 v236, %[flag]\n" \
"v_add_f32_e32 v180, v180, v181\n" \
"v_add_f32_e32 v182, v182, v183\n" \
"v_add_f32_e32 v231, v231, v180\n" \
"v_add_f32_e32 v231, v231, v182\n" \
"v_rcp_f32_e32 v180, v180\n" \
"v_cmp_nle_f32_e64 s[56:57], v231, s55\n" \
"v_mfma_f32_16x16x32_bf16 v[40:43], a[168:171], v[196:199], v[40:43]\n" \
"v_rcp_f32_e64 v182, v182\n" \
"v_cmp_nle_f32_e64 s[44:45], v233, v234\n" \
"v_mul_f32_e64 v181, -v229, v180\n" \
"v_mul_f32_e64 v183, -v229, v182\n" \
"s_nop 1\n" \
"s_or_b64 s[56:57], s[56:57], s[44:45]\n" \
"s_cselect_b32 s55, 1, 0\n" \
"v_mov_b32_e32 v230, s55\n" \
"v_mfma_f32_16x16x32_bf16 v[44:47], a[172:175], v[196:199], v[44:47]\n" \
"s_mov_b32 s69, s55\n" \
"s_andn2_b32 s55, s55, s52\n" \
"s_cselect_b64 vcc, 0, vcc\n" \
"s_nop 0\n" \
"ds_write_b32 v236, v230\n" \
"v_and_b32_e32 v236, -16, v236\n" \
"s_cbranch_vccz Lg37_%=\n" \
"buffer_store_dwordx4 v[0:3], %[oq0], %[srdz], s60 offen offset:0 sc1\n" \
"Lg37_%=:\n" \
"v_pk_fma_f32 v[8:9], v[8:9], v[176:177], v[176:177] op_sel:[0,0,1] op_sel_hi:[1,0,1]\n" \
"v_pk_fma_f32 v[10:11], v[10:11], v[176:177], v[176:177] op_sel:[0,0,1] op_sel_hi:[1,0,1]\n" \
"v_mfma_f32_16x16x32_bf16 v[48:51], a[176:179], v[196:199], v[48:51]\n" \
"v_pk_fma_f32 v[12:13], v[12:13], v[176:177], v[176:177] op_sel:[0,0,1] op_sel_hi:[1,0,1]\n" \
"v_pk_fma_f32 v[14:15], v[14:15], v[176:177], v[176:177] op_sel:[0,0,1] op_sel_hi:[1,0,1]\n" \
"v_cvt_pk_bf16_f32 v8, v8, v9\n" \
"v_cvt_pk_bf16_f32 v9, v10, v11\n" \
"v_cvt_pk_bf16_f32 v10, v12, v13\n" \
"v_cvt_pk_bf16_f32 v11, v14, v15\n" \
"s_cbranch_vccz Lg38_%=\n" \
"s_nop 0\n" \
"buffer_store_dwordx4 v[8:11], %[oq0], %[srdz], s60 offen offset:1024 sc1\n" \
"Lg38_%=:\n" \
"v_pk_fma_f32 v[16:17], v[16:17], v[176:177], v[176:177] op_sel:[0,0,1] op_sel_hi:[1,0,1]\n" \
"v_mfma_f32_16x16x32_bf16 v[52:55], a[180:183], v[196:199], v[52:55]\n" \
"v_pk_fma_f32 v[18:19], v[18:19], v[176:177], v[176:177] op_sel:[0,0,1] op_sel_hi:[1,0,1]\n" \
"v_pk_fma_f32 v[20:21], v[20:21], v[176:177], v[176:177] op_sel:[0,0,1] op_sel_hi:[1,0,1]\n" \
"v_pk_fma_f32 v[22:23], v[22:23], v[176:177], v[176:177] op_sel:[0,0,1] op_sel_hi:[1,0,1]\n" \
"v_cvt_pk_bf16_f32 v16, v16, v17\n" \
"v_cvt_pk_bf16_f32 v17, v18, v19\n" \
"v_cvt_pk_bf16_f32 v18, v20, v21\n" \
"v_cvt_pk_bf16_f32 v19, v22, v23\n" \
"s_cbranch_vccz Lg39_%=\n" \
"s_nop 0\n" \
"buffer_store_dwordx4 v[16:19], %[oq0], %[srdz], s60 offen offset:2048 sc1\n" \
"Lg39_%=:\n" \
"v_mfma_f32_16x16x32_bf16 v[56:59], a[184:187], v[196:199], v[56:59]\n" \
"v_pk_fma_f32 v[24:25], v[24:25], v[176:177], v[176:177] op_sel:[0,0,1] op_sel_hi:[1,0,1]\n" \
"v_pk_fma_f32 v[26:27], v[26:27], v[176:177], v[176:177] op_sel:[0,0,1] op_sel_hi:[1,0,1]\n" \
"v_pk_fma_f32 v[28:29], v[28:29], v[176:177], v[176:177] op_sel:[0,0,1] op_sel_hi:[1,0,1]\n" \
"v_pk_fma_f32 v[30:31], v[30:31], v[176:177], v[176:177] op_sel:[0,0,1] op_sel_hi:[1,0,1]\n" \
"v_cvt_pk_bf16_f32 v24, v24, v25\n" \
"v_cvt_pk_bf16_f32 v25, v26, v27\n" \
"v_cvt_pk_bf16_f32 v26, v28, v29\n" \
"v_cvt_pk_bf16_f32 v27, v30, v31\n" \
"v_mfma_f32_16x16x32_bf16 v[60:63], a[188:191], v[196:199], v[60:63]\n" \
"s_cbranch_vccz Lg40_%=\n" \
"s_nop 0\n" \
"buffer_store_dwordx4 v[24:27], %[oq0], %[srdz], s60 offen offset:3072 sc1\n" \
"Lg40_%=:\n" \
"v_mfma_f32_16x16x32_bf16 v[64:67], a[128:131], v[216:219], v[64:67]\n" \
"v_fma_f32 v32, v32, v178, v179\n" \
"v_fma_f32 v33, v33, v178, v179\n" \
"v_fma_f32 v34, v34, v178, v179\n" \
"v_fma_f32 v35, v35, v178, v179\n" \
"v_fma_f32 v36, v36, v178, v179\n" \
"v_fma_f32 v37, v37, v178, v179\n" \
"v_mfma_f32_16x16x32_bf16 v[64:67], a[160:163], v[200:203], v[64:67]\n" \
"v_fma_f32 v38, v38, v178, v179\n" \
"v_fma_f32 v39, v39, v178, v179\n" \
"v_cvt_pk_bf16_f32 v32, v32, v33\n" \
"v_cvt_pk_bf16_f32 v33, v34, v35\n" \
"v_mfma_f32_16x16x32_bf16 v[68:71], a[132:135], v[216:219], v[68:71]\n" \
"v_cvt_pk_bf16_f32 v34, v36, v37\n" \
"v_cvt_pk_bf16_f32 v35, v38, v39\n" \
"s_cbranch_vccz Lg41_%=\n" \
"s_nop 0\n" \
"buffer_store_dwordx4 v[32:35], v208, %[srdz], s60 offen offset:0 sc1\n" \
"Lg41_%=:\n" \
"v_mfma_f32_16x16x32_bf16 v[68:71], a[164:167], v[200:203], v[68:71]\n" \
"v_fma_f32 v40, v40, v178, v179\n" \
"v_fma_f32 v41, v41, v178, v179\n" \
"v_fma_f32 v42, v42, v178, v179\n" \
"v_fma_f32 v43, v43, v178, v179\n" \
"v_fma_f32 v44, v44, v178, v179\n" \
"v_fma_f32 v45, v45, v178, v179\n" \
"v_mfma_f32_16x16x32_bf16 v[72:75], a[136:139], v[216:219], v[72:75]\n" \
"v_fma_f32 v46, v46, v178, v179\n" \
"v_fma_f32 v47, v47, v178, v179\n" \
"v_cvt_pk_bf16_f32 v40, v40, v41\n" \
"v_cvt_pk_bf16_f32 v41, v42, v43\n" \
"v_mfma_f32_16x16x32_bf16 v[72:75], a[168:171], v[200:203], v[72:75]\n" \
"v_cvt_pk_bf16_f32 v42, v44, v45\n" \
"v_cvt_pk_bf16_f32 v43, v46, v47\n" \
"s_cbranch_vccz Lg42_%=\n" \
"s_nop 0\n" \
"buffer_store_dwordx4 v[40:43], v208, %[srdz], s60 offen offset:1024 sc1\n" \
"Lg42_%=:\n" \
"v_mfma_f32_16x16x32_bf16 v[76:79], a[140:143], v[216:219], v[76:79]\n" \
"v_fma_f32 v48, v48, v178, v179\n" \
"v_fma_f32 v49, v49, v178, v179\n" \
"v_fma_f32 v50, v50, v178, v179\n" \
"v_fma_f32 v51, v51, v178, v179\n" \
"v_fma_f32 v52, v52, v178, v179\n" \
"v_fma_f32 v53, v53, v178, v179\n" \
"v_mfma_f32_16x16x32_bf16 v[76:79], a[172:175], v[200:203], v[76:79]\n" \
"v_fma_f32 v54, v54, v178, v179\n" \
"v_fma_f32 v55, v55, v178, v179\n" \
"v_cvt_pk_bf16_f32 v48, v48, v49\n" \
"v_cvt_pk_bf16_f32 v49, v50, v51\n" \
"v_mfma_f32_16x16x32_bf16 v[80:83], a[144:147], v[216:219], v[80:83]\n" \
"v_cvt_pk_bf16_f32 v50, v52, v53\n" \
"v_cvt_pk_bf16_f32 v51, v54, v55\n" \
"s_cbranch_vccz Lg43_%=\n" \
"s_nop 0\n" \
"buffer_store_dwordx4 v[48:51], v208, %[srdz], s60 offen offset:2048 sc1\n" \
"Lg43_%=:\n" \
"v_mfma_f32_16x16x32_bf16 v[80:83], a[176:179], v[200:203], v[80:83]\n" \
"v_fma_f32 v56, v56, v178, v179\n" \
"v_fma_f32 v57, v57, v178, v179\n" \
"v_fma_f32 v58, v58, v178, v179\n" \
"v_fma_f32 v59, v59, v178, v179\n" \
"v_fma_f32 v60, v60, v178, v179\n" \
"v_fma_f32 v61, v61, v178, v179\n" \
"v_mfma_f32_16x16x32_bf16 v[84:87], a[148:151], v[216:219], v[84:87]\n" \
"v_fma_f32 v62, v62, v178, v179\n" \
"v_fma_f32 v63, v63, v178, v179\n" \
"v_cvt_pk_bf16_f32 v56, v56, v57\n" \
"v_cvt_pk_bf16_f32 v57, v58, v59\n" \
"v_mfma_f32_16x16x32_bf16 v[84:87], a[180:183], v[200:203], v[84:87]\n" \
"v_cvt_pk_bf16_f32 v58, v60, v61\n" \
"v_cvt_pk_bf16_f32 v59, v62, v63\n" \
"s_cbranch_vccz Lg44_%=\n" \
"s_nop 0\n" \
"buffer_store_dwordx4 v[56:59], v208, %[srdz], s60 offen offset:3072 sc1\n" \
"Lg44_%=:\n" \
"v_mfma_f32_16x16x32_bf16 v[88:91], a[152:155], v[216:219], v[88:91]\n" \
"v_mfma_f32_16x16x32_bf16 v[88:91], a[184:187], v[200:203], v[88:91]\n" \
"v_mfma_f32_16x16x32_bf16 v[92:95], a[156:159], v[216:219], v[92:95]\n" \
"v_mfma_f32_16x16x32_bf16 v[92:95], a[188:191], v[200:203], v[92:95]\n" \
"s_waitcnt vmcnt(12) lgkmcnt(0)\n" \
"s_bitcmp1_b32 0xf, s48\n" \
"s_cbranch_scc1 Lcw1_%=\n" \
"s_waitcnt vmcnt(4)\n" \
"Lcw1_%=:\n" \
"s_barrier\n" \
"s_nop 0\n" \
"ds_read_b128 v[232:235], v236\n" \
"v_mfma_f32_16x16x32_bf16 v[96:99], a[160:163], v[204:207], v[96:99]\n" \
"v_pk_fma_f32 v[64:65], v[64:65], v[180:181], v[180:181] op_sel:[0,0,1] op_sel_hi:[1,0,1]\n" \
"v_pk_fma_f32 v[66:67], v[66:67], v[180:181], v[180:181] op_sel:[0,0,1] op_sel_hi:[1,0,1]\n" \
"v_pk_fma_f32 v[68:69], v[68:69], v[180:181], v[180:181] op_sel:[0,0,1] op_sel_hi:[1,0,1]\n" \
"v_pk_fma_f32 v[70:71], v[70:71], v[180:181], v[180:181] op_sel:[0,0,1] op_sel_hi:[1,0,1]\n" \
"v_cvt_pk_bf16_f32 v64, v64, v65\n" \
"v_mfma_f32_16x16x32_bf16 v[100:103], a[164:167], v[204:207], v[100:103]\n" \
"v_cvt_pk_bf16_f32 v65, v66, v67\n" \
"v_cvt_pk_bf16_f32 v66, v68, v69\n" \
"v_cvt_pk_bf16_f32 v67, v70, v71\n" \
"s_cbranch_vccz Lg45_%=\n" \
"s_nop 0\n" \
"buffer_store_dwordx4 v[64:67], v209, %[srdz], s60 offen offset:0 sc1\n" \
"Lg45_%=:\n" \
"v_pk_fma_f32 v[72:73], v[72:73], v[180:181], v[180:181] op_sel:[0,0,1] op_sel_hi:[1,0,1]\n" \
"v_mfma_f32_16x16x32_bf16 v[104:107], a[168:171], v[204:207], v[104:107]\n" \
"v_pk_fma_f32 v[74:75], v[74:75], v[180:181], v[180:181] op_sel:[0,0,1] op_sel_hi:[1,0,1]\n" \
"v_pk_fma_f32 v[76:77], v[76:77], v[180:181], v[180:181] op_sel:[0,0,1] op_sel_hi:[1,0,1]\n" \
"v_pk_fma_f32 v[78:79], v[78:79], v[180:181], v[180:181] op_sel:[0,0,1] op_sel_hi:[1,0,1]\n" \
"v_cvt_pk_bf16_f32 v72, v72, v73\n" \
"v_cvt_pk_bf16_f32 v73, v74, v75\n" \
"v_mfma_f32_16x16x32_bf16 v[108:111], a[172:175], v[204:207], v[108:111]\n" \
"v_cvt_pk_bf16_f32 v74, v76, v77\n" \
"v_cvt_pk_bf16_f32 v75, v78, v79\n" \
"s_cbranch_vccz Lg46_%=\n" \
"s_nop 0\n" \
"buffer_store_dwordx4 v[72:75], v209, %[srdz], s60 offen offset:1024 sc1\n" \
"Lg46_%=:\n" \
"v_pk_fma_f32 v[80:81], v[80:81], v[180:181], v[180:181] op_sel:[0,0,1] op_sel_hi:[1,0,1]\n" \
"v_pk_fma_f32 v[82:83], v[82:83], v[180:181], v[180:181] op_sel:[0,0,1] op_sel_hi:[1,0,1]\n" \
"v_mfma_f32_16x16x32_bf16 v[112:115], a[176:179], v[204:207], v[112:115]\n" \
"v_pk_fma_f32 v[84:85], v[84:85], v[180:181], v[180:181] op_sel:[0,0,1] op_sel_hi:[1,0,1]\n" \
"v_pk_fma_f32 v[86:87], v[86:87], v[180:181], v[180:181] op_sel:[0,0,1] op_sel_hi:[1,0,1]\n" \
"v_cvt_pk_bf16_f32 v80, v80, v81\n" \
"v_cvt_pk_bf16_f32 v81, v82, v83\n" \
"v_cvt_pk_bf16_f32 v82, v84, v85\n" \
"v_mfma_f32_16x16x32_bf16 v[116:119], a[180:183], v[204:207], v[116:119]\n" \
"v_cvt_pk_bf16_f32 v83, v86, v87\n" \
"s_cbranch_vccz Lg47_%=\n" \
"s_nop 0\n" \
"buffer_store_dwordx4 v[80:83], v209, %[srdz], s60 offen offset:2048 sc1\n" \
"Lg47_%=:\n" \
"v_pk_fma_f32 v[88:89], v[88:89], v[180:181], v[180:181] op_sel:[0,0,1] op_sel_hi:[1,0,1]\n" \
"v_pk_fma_f32 v[90:91], v[90:91], v[180:181], v[180:181] op_sel:[0,0,1] op_sel_hi:[1,0,1]\n" \
"v_pk_fma_f32 v[92:93], v[92:93], v[180:181], v[180:181] op_sel:[0,0,1] op_sel_hi:[1,0,1]\n" \
"v_mfma_f32_16x16x32_bf16 v[120:123], a[184:187], v[204:207], v[120:123]\n" \
"v_pk_fma_f32 v[94:95], v[94:95], v[180:181], v[180:181] op_sel:[0,0,1] op_sel_hi:[1,0,1]\n" \
"v_cvt_pk_bf16_f32 v88, v88, v89\n" \
"v_cvt_pk_bf16_f32 v89, v90, v91\n" \
"v_cvt_pk_bf16_f32 v90, v92, v93\n" \
"v_cvt_pk_bf16_f32 v91, v94, v95\n" \
"v_mfma_f32_16x16x32_bf16 v[124:127], a[188:191], v[204:207], v[124:127]\n" \
"s_cbranch_vccz Lg48_%=\n" \
"s_nop 0\n" \
"buffer_store_dwordx4 v[88:91], v209, %[srdz], s60 offen offset:3072 sc1\n" \
"Lg48_%=:\n" \
"s_waitcnt lgkmcnt(0)\n" \
"s_nop 0\n" \
"v_or3_b32 v232, v232, v233, v234\n" \
"v_or_b32_e32 v232, v232, v235\n" \
"s_nop 0\n" \
"v_readfirstlane_b32 s55, v232\n" \
"s_nop 3\n" \
"s_andn2_b32 s55, s55, s52\n" \
"s_cbranch_scc1 Lslow_%=\n" \
"v_mfma_f32_16x16x32_bf16 v[192:195], a[196:199], v[0:3], 0\n" \
"v_mfma_f32_16x16x32_bf16 v[192:195], a[200:203], v[8:11], v[192:195]\n" \
"v_fma_f32 v96, v96, v182, v183\n" \
"v_fma_f32 v97, v97, v182, v183\n" \
"v_fma_f32 v98, v98, v182, v183\n" \
"v_fma_f32 v99, v99, v182, v183\n" \
"v_mfma_f32_16x16x32_bf16 v[192:195], a[204:207], v[16:19], v[192:195]\n" \
"v_fma_f32 v100, v100, v182, v183\n" \
".if . - adaln_body != 40960\n" \
".error \"size model\"\n" \
".endif\n" \
"Lk9_%=:\n" \
"s_cbranch_execz Lk10_%=\n" \
"s_nop 0\n" \
"v_fma_f32 v101, v101, v182, v183\n" \
"v_fma_f32 v102, v102, v182, v183\n" \
"v_fma_f32 v103, v103, v182, v183\n" \
"s_add_u32 m0, %[m0b], 114688\n" \
"s_cmp_gt_u32 s48, 1\n" \
"s_cselect_b32 s49, 1, 2\n" \
"s_lshl_b32 s49, s49, 18\n" \
"s_or_b32 s49, s49, s58\n" \
"buffer_load_dwordx4 %[wo0], %[srdt], s49 offen offset:0 lds\n" \
"v_mfma_f32_16x16x32_bf16 v[192:195], a[208:211], v[24:27], v[192:195]\n" \
"v_cvt_pk_bf16_f32 v96, v96, v97\n" \
"v_cvt_pk_bf16_f32 v97, v98, v99\n" \
"v_mfma_f32_16x16x32_bf16 v[144:147], a[212:215], v[0:3], 0\n" \
"v_cvt_pk_bf16_f32 v98, v100, v101\n" \
"v_mfma_f32_16x16x32_bf16 v[144:147], a[216:219], v[8:11], v[144:147]\n" \
"v_cvt_pk_bf16_f32 v99, v102, v103\n" \
"s_cbranch_vccz Lg1_%=\n" \
"s_nop 0\n" \
"buffer_store_dwordx4 v[96:99], v210, %[srdz], s60 offen offset:0 sc1\n" \
"Lg1_%=:\n" \
"v_mfma_f32_16x16x32_bf16 v[144:147], a[220:223], v[16:19], v[144:147]\n" \
"ds_read_b64_tr_b16 a[228:229], v215 offset:512\n" \
"v_fma_f32 v104, v104, v182, v183\n" \
"v_fma_f32 v105, v105, v182, v183\n" \
"v_fma_f32 v106, v106, v182, v183\n" \
"v_fma_f32 v107, v107, v182, v183\n" \
"v_mfma_f32_16x16x32_bf16 v[144:147], a[224:227], v[24:27], v[144:147]\n" \
"ds_read_b64_tr_b16 a[230:231], v215 offset:2560\n" \
"v_fma_f32 v108, v108, v182, v183\n" \
"v_fma_f32 v109, v109, v182, v183\n" \
"v_mfma_f32_16x16x32_bf16 v[196:199], a[196:199], v[32:35], 0\n" \
"ds_read_b64_tr_b16 a[232:233], v215 offset:4608\n" \
"v_fma_f32 v110, v110, v182, v183\n" \
"v_fma_f32 v111, v111, v182, v183\n" \
"v_cvt_pk_bf16_f32 v104, v104, v105\n" \
"v_mfma_f32_16x16x32_bf16 v[196:199], a[200:203], v[40:43], v[196:199]\n" \
"ds_read_b64_tr_b16 a[234:235], v215 offset:6656\n" \
"v_cvt_pk_bf16_f32 v105, v106, v107\n" \
"v_cvt_pk_bf16_f32 v106, v108, v109\n" \
"v_mfma_f32_16x16x32_bf16 v[196:199], a[204:207], v[48:51], v[196:199]\n" \
"v_cvt_pk_bf16_f32 v107, v110, v111\n" \
"s_cbranch_vccz Lg2_%=\n" \
"s_nop 0\n" \
"buffer_store_dwordx4 v[104:107], v210, %[srdz], s60 offen offset:1024 sc1\n" \
"Lg2_%=:\n" \
"v_mfma_f32_16x16x32_bf16 v[196:199], a[208:211], v[56:59], v[196:199]\n" \
"v_fma_f32 v112, v112, v182, v183\n" \
"v_fma_f32 v113, v113, v182, v183\n" \
"v_mfma_f32_16x16x32_bf16 v[148:151], a[212:215], v[32:35], 0\n" \
"v_fma_f32 v114, v114, v182, v183\n" \
"v_fma_f32 v115, v115, v182, v183\n" \
"v_fma_f32 v116, v116, v182, v183\n" \
"v_fma_f32 v117, v117, v182, v183\n" \
"buffer_load_dwordx4 v212, %[srdt], s49 offen offset:1024 lds\n" \
"v_mfma_f32_16x16x32_bf16 v[148:151], a[216:219], v[40:43], v[148:151]\n" \
"v_fma_f32 v118, v118, v182, v183\n" \
"v_fma_f32 v119, v119, v182, v183\n" \
"v_cvt_pk_bf16_f32 v112, v112, v113\n" \
"v_mfma_f32_16x16x32_bf16 v[148:151], a[220:223], v[48:51], v[148:151]\n" \
"ds_read_b64_tr_b16 a[236:237], v215 offset:8704\n" \
"v_cvt_pk_bf16_f32 v113, v114, v115\n" \
"v_mfma_f32_16x16x32_bf16 v[148:151], a[224:227], v[56:59], v[148:151]\n" \
"v_cvt_pk_bf16_f32 v114, v116, v117\n" \
"v_cvt_pk_bf16_f32 v115, v118, v119\n" \
"v_mfma_f32_16x16x32_bf16 v[200:203], a[196:199], v[64:67], 0\n" \
"ds_read_b64_tr_b16 a[238:239], v215 offset:10752\n" \
"s_cbranch_vccz Lg3_%=\n" \
"s_nop 0\n" \
"buffer_store_dwordx4 v[112:115], v210, %[srdz], s60 offen offset:2048 sc1\n" \
"Lg3_%=:\n" \
"v_fma_f32 v120, v120, v182, v183\n" \
"v_fma_f32 v121, v121, v182, v183\n" \
"v_mfma_f32_16x16x32_bf16 v[200:203], a[200:203], v[72:75], v[200:203]\n" \
"v_fma_f32 v122, v122, v182, v183\n" \
"v_fma_f32 v123, v123, v182, v183\n" \
"v_fma_f32 v124, v124, v182, v183\n" \
"v_fma_f32 v125, v125, v182, v183\n" \
"v_mfma_f32_16x16x32_bf16 v[200:203], a[204:207], v[80:83], v[200:203]\n" \
"v_fma_f32 v126, v126, v182, v183\n" \
"v_fma_f32 v127, v127, v182, v183\n" \
"v_mfma_f32_16x16x32_bf16 v[200:203], a[208:211], v[88:91], v[200:203]\n" \
"v_cvt_pk_bf16_f32 v120, v120, v121\n" \
"v_cvt_pk_bf16_f32 v121, v122, v123\n" \
"v_mfma_f32_16x16x32_bf16 v[152:155], a[212:215], v[64:67], 0\n" \
"v_cvt_pk_bf16_f32 v122, v124, v125\n" \
"v_cvt_pk_bf16_f32 v123, v126, v127\n" \
"v_mfma_f32_16x16x32_bf16 v[152:155], a[216:219], v[72:75], v[152:155]\n" \
"s_cbranch_vccz Lg4_%=\n" \
"s_nop 0\n" \
"buffer_store_dwordx4 v[120:123], v210, %[srdz], s60 offen offset:3072 sc1\n" \
"Lg4_%=:\n" \
"v_mfma_f32_16x16x32_bf16 v[152:155], a[220:223], v[80:83], v[152:155]\n" \
"v_add_u32_e32 v220, 0x10000, v215\n" \
"v_mfma_f32_16x16x32_bf16 v[152:155], a[224:227], v[88:91], v[152:155]\n" \
"v_mov_b32_e64 v230, 1\n" \
"v_readlane_b32 s62, %[tlo], 19\n" \
"v_readlane_b32 s63, %[thi], 19\n" \
"v_and_b32_e32 v233, 0x380, v224\n" \
"v_lshlrev_b32_e64 v233, 2, v233\n" \
"buffer_load_dwordx4 v213, %[srdt], s49 offen offset:2048 lds\n" \
"v_mfma_f32_16x16x32_bf16 v[204:207], a[196:199], v[96:99], 0\n" \
"v_add_u32_e32 v221, 0x10000, v227\n" \
"v_mfma_f32_16x16x32_bf16 v[204:207], a[200:203], v[104:107], v[204:207]\n" \
"ds_read_b64_tr_b16 a[196:197], v215 offset:12800\n" \
"v_mfma_f32_16x16x32_bf16 v[204:207], a[204:207], v[112:115], v[204:207]\n" \
"ds_read_b64_tr_b16 a[198:199], v215 offset:14848\n" \
"v_mfma_f32_16x16x32_bf16 v[204:207], a[208:211], v[120:123], v[204:207]\n" \
"ds_read_b64_tr_b16 a[200:201], v227 offset:512\n" \
"v_mfma_f32_16x16x32_bf16 v[156:159], a[212:215], v[96:99], 0\n" \
"ds_read_b64_tr_b16 a[202:203], v227 offset:2560\n" \
"v_mfma_f32_16x16x32_bf16 v[156:159], a[216:219], v[104:107], v[156:159]\n" \
"ds_read_b64_tr_b16 a[204:205], v227 offset:4608\n" \
"v_mfma_f32_16x16x32_bf16 v[156:159], a[220:223], v[112:115], v[156:159]\n" \
"ds_read_b64_tr_b16 a[206:207], v227 offset:6656\n" \
"v_mfma_f32_16x16x32_bf16 v[156:159], a[224:227], v[120:123], v[156:159]\n" \
"s_waitcnt vmcnt(63) lgkmcnt(4)\n" \
"s_nop 0\n" \
"v_mfma_f32_16x16x32_bf16 v[160:163], a[228:231], v[0:3], 0\n" \
"ds_read_b64_tr_b16 a[208:209], v227 offset:8704\n" \
"v_mfma_f32_16x16x32_bf16 v[160:163], a[232:235], v[8:11], v[160:163]\n" \
"ds_read_b64_tr_b16 a[210:211], v227 offset:10752\n" \
"buffer_load_dwordx4 v214, %[srdt], s49 offen offset:3072 lds\n" \
"v_mfma_f32_16x16x32_bf16 v[164:167], a[228:231], v[32:35], 0\n" \
"ds_read_b64_tr_b16 a[212:213], v227 offset:12800\n" \
"v_mfma_f32_16x16x32_bf16 v[164:167], a[232:235], v[40:43], v[164:167]\n" \
"ds_read_b64_tr_b16 a[214:215], v227 offset:14848\n" \
"v_mfma_f32_16x16x32_bf16 v[168:171], a[228:231], v[64:67], 0\n" \
"v_mfma_f32_16x16x32_bf16 v[168:171], a[232:235], v[72:75], v[168:171]\n" \
"v_mfma_f32_16x16x32_bf16 v[172:175], a[228:231], v[96:99], 0\n" \
"ds_read_b64_tr_b16 a[216:217], v215 offset:16384\n" \
"v_mfma_f32_16x16x32_bf16 v[172:175], a[232:235], v[104:107], v[172:175]\n" \
"ds_read_b64_tr_b16 a[218:219], v215 offset:18432\n" \
"v_mfma_f32_16x16x32_bf16 v[160:163], a[236:239], v[16:19], v[160:163]\n" \
"ds_read_b64_tr_b16 a[220:221], v215 offset:20480\n" \
"v_mfma_f32_16x16x32_bf16 v[160:163], a[196:199], v[24:27], v[160:163]\n" \
"ds_read_b64_tr_b16 a[222:223], v215 offset:22528\n" \
"v_mfma_f32_16x16x32_bf16 v[164:167], a[236:239], v[48:51], v[164:167]\n" \
"v_mfma_f32_16x16x32_bf16 v[164:167], a[196:199], v[56:59], v[164:167]\n" \
"v_mfma_f32_16x16x32_bf16 v[168:171], a[236:239], v[80:83], v[168:171]\n" \
"s_waitcnt vmcnt(63) lgkmcnt(0)\n" \
"s_barrier\n" \
"v_mfma_f32_16x16x32_bf16 v[168:171], a[196:199], v[88:91], v[168:171]\n" \
"v_mfma_f32_16x16x32_bf16 v[172:175], a[236:239], v[112:115], v[172:175]\n" \
"ds_read_b64_tr_b16 a[224:225], v215 offset:24576\n" \
"v_mfma_f32_16x16x32_bf16 v[172:175], a[196:199], v[120:123], v[172:175]\n" \
"s_waitcnt vmcnt(63) lgkmcnt(5)\n" \
"s_nop 0\n" \
"v_mfma_f32_16x16x32_bf16 v[176:179], a[200:203], v[0:3], 0\n" \
"ds_read_b64_tr_b16 a[226:227], v215 offset:26624\n" \
"v_mfma_f32_16x16x32_bf16 v[176:179], a[204:207], v[8:11], v[176:179]\n" \
"ds_read_b64_tr_b16 a[228:229], v215 offset:28672\n" \
"s_add_u32 m0, %[m0b], 49152\n" \
"s_cmp_gt_u32 s48, 1\n" \
"s_cselect_b32 s49, 1, 2\n" \
"s_lshl_b32 s49, s49, 18\n" \
"s_or_b32 s49, s49, s59\n" \
"buffer_load_dwordx4 %[wo0], %[srdt], s49 offen offset:0 lds\n" \
"v_mfma_f32_16x16x32_bf16 v[180:183], a[200:203], v[32:35], 0\n" \
"ds_read_b64_tr_b16 a[230:231], v215 offset:30720\n" \
"v_mfma_f32_16x16x32_bf16 v[180:183], a[204:207], v[40:43], v[180:183]\n" \
"v_mfma_f32_16x16x32_bf16 v[184:187], a[200:203], v[64:67], 0\n" \
"v_mfma_f32_16x16x32_bf16 v[184:187], a[204:207], v[72:75], v[184:187]\n" \
"buffer_load_dwordx4 v212, %[srdt], s49 offen offset:1024 lds\n" \
"v_mfma_f32_16x16x32_bf16 v[188:191], a[200:203], v[96:99], 0\n" \
"ds_read_b64_tr_b16 a[232:233], v227 offset:16384\n" \
"v_mfma_f32_16x16x32_bf16 v[188:191], a[204:207], v[104:107], v[188:191]\n" \
"ds_read_b64_tr_b16 a[234:235], v227 offset:18432\n" \
"v_mfma_f32_16x16x32_bf16 v[176:179], a[208:211], v[16:19], v[176:179]\n" \
"ds_read_b64_tr_b16 a[236:237], v227 offset:20480\n" \
"v_mfma_f32_16x16x32_bf16 v[176:179], a[212:215], v[24:27], v[176:179]\n" \
"ds_read_b64_tr_b16 a[238:239], v227 offset:22528\n" \
"v_mfma_f32_16x16x32_bf16 v[180:183], a[208:211], v[48:51], v[180:183]\n" \
"v_mfma_f32_16x16x32_bf16 v[180:183], a[212:215], v[56:59], v[180:183]\n" \
"v_mfma_f32_16x16x32_bf16 v[184:187], a[208:211], v[80:83], v[184:187]\n" \
"v_mfma_f32_16x16x32_bf16 v[184:187], a[212:215], v[88:91], v[184:187]\n" \
"buffer_load_dwordx4 v213, %[srdt], s49 offen offset:2048 lds\n" \
"v_mfma_f32_16x16x32_bf16 v[188:191], a[208:211], v[112:115], v[188:191]\n" \
"ds_read_b64_tr_b16 a[196:197], v227 offset:24576\n" \
"v_mfma_f32_16x16x32_bf16 v[188:191], a[212:215], v[120:123], v[188:191]\n" \
"s_waitcnt vmcnt(63) lgkmcnt(5)\n" \
"s_nop 0\n" \
"v_mfma_f32_16x16x32_bf16 v[4:7], a[216:219], v[0:3], 0\n" \
"ds_read_b64_tr_b16 a[198:199], v227 offset:26624\n" \
"v_mfma_f32_16x16x32_bf16 v[4:7], a[220:223], v[8:11], v[4:7]\n" \
"ds_read_b64_tr_b16 a[200:201], v227 offset:28672\n" \
"v_mfma_f32_16x16x32_bf16 v[36:39], a[216:219], v[32:35], 0\n" \
"ds_read_b64_tr_b16 a[202:203], v227 offset:30720\n" \
"v_mfma_f32_16x16x32_bf16 v[36:39], a[220:223], v[40:43], v[36:39]\n" \
"v_mfma_f32_16x16x32_bf16 v[68:71], a[216:219], v[64:67], 0\n" \
"buffer_load_dwordx4 v214, %[srdt], s49 offen offset:3072 lds\n" \
"v_mfma_f32_16x16x32_bf16 v[68:71], a[220:223], v[72:75], v[68:71]\n" \
"v_mfma_f32_16x16x32_bf16 v[100:103], a[216:219], v[96:99], 0\n" \
"ds_read_b64_tr_b16 a[204:205], v215 offset:16896\n" \
"v_mfma_f32_16x16x32_bf16 v[100:103], a[220:223], v[104:107], v[100:103]\n" \
"ds_read_b64_tr_b16 a[206:207], v215 offset:18944\n" \
"v_mfma_f32_16x16x32_bf16 v[4:7], a[224:227], v[16:19], v[4:7]\n" \
"ds_read_b64_tr_b16 a[208:209], v215 offset:20992\n" \
"v_mfma_f32_16x16x32_bf16 v[4:7], a[228:231], v[24:27], v[4:7]\n" \
"ds_read_b64_tr_b16 a[210:211], v215 offset:23040\n" \
"v_mfma_f32_16x16x32_bf16 v[36:39], a[224:227], v[48:51], v[36:39]\n" \
"v_mfma_f32_16x16x32_bf16 v[36:39], a[228:231], v[56:59], v[36:39]\n" \
"v_mfma_f32_16x16x32_bf16 v[68:71], a[224:227], v[80:83], v[68:71]\n" \
"v_mfma_f32_16x16x32_bf16 v[68:71], a[228:231], v[88:91], v[68:71]\n" \
"v_mfma_f32_16x16x32_bf16 v[100:103], a[224:227], v[112:115], v[100:103]\n" \
"ds_read_b64_tr_b16 a[212:213], v215 offset:25088\n" \
"v_mfma_f32_16x16x32_bf16 v[100:103], a[228:231], v[120:123], v[100:103]\n" \
"s_waitcnt vmcnt(63) lgkmcnt(5)\n" \
"s_nop 0\n" \
"v_mfma_f32_16x16x32_bf16 v[12:15], a[232:235], v[0:3], 0\n" \
"ds_read_b64_tr_b16 a[214:215], v215 offset:27136\n" \
"s_cmp_gt_u32 s48, 0\n" \
"s_cselect_b32 s50, 0, 1\n" \
"s_mul_i32 s50, s50, 152064\n" \
"buffer_load_dwordx4 a[64:67], %[oq0], %[srdz], s50 offen offset:0 sc1\n" \
"v_mfma_f32_16x16x32_bf16 v[12:15], a[236:239], v[8:11], v[12:15]\n" \
"ds_read_b64_tr_b16 a[216:217], v215 offset:29184\n" \
"v_mfma_f32_16x16x32_bf16 v[44:47], a[232:235], v[32:35], 0\n" \
"ds_read_b64_tr_b16 a[218:219], v215 offset:31232\n" \
"buffer_load_dwordx4 a[68:71], %[oq0], %[srdz], s50 offen offset:1024 sc1\n" \
"v_mfma_f32_16x16x32_bf16 v[44:47], a[236:239], v[40:43], v[44:47]\n" \
"v_mfma_f32_16x16x32_bf16 v[76:79], a[232:235], v[64:67], 0\n" \
"buffer_load_dwordx4 a[80:83], v208, %[srdz], s50 offen offset:0 sc1\n" \
"v_mfma_f32_16x16x32_bf16 v[76:79], a[236:239], v[72:75], v[76:79]\n" \
"v_mfma_f32_16x16x32_bf16 v[108:111], a[232:235], v[96:99], 0\n" \
"ds_read_b64_tr_b16 a[220:221], v227 offset:16896\n" \
"buffer_load_dwordx4 a[84:87], v208, %[srdz], s50 offen offset:1024 sc1\n" \
"v_mfma_f32_16x16x32_bf16 v[108:111], a[236:239], v[104:107], v[108:111]\n" \
"ds_read_b64_tr_b16 a[222:223], v227 offset:18944\n" \
"v_mfma_f32_16x16x32_bf16 v[12:15], a[196:199], v[16:19], v[12:15]\n" \
"ds_read_b64_tr_b16 a[224:225], v227 offset:25088\n" \
"buffer_load_dwordx4 a[96:99], v209, %[srdz], s50 offen offset:0 sc1\n" \
"v_mfma_f32_16x16x32_bf16 v[12:15], a[200:203], v[24:27], v[12:15]\n" \
"ds_read_b64_tr_b16 a[226:227], v227 offset:27136\n" \
"v_mfma_f32_16x16x32_bf16 v[44:47], a[196:199], v[48:51], v[44:47]\n" \
"ds_read_b64_tr_b16 a[228:229], v227 offset:20992\n" \
"buffer_load_dwordx4 a[100:103], v209, %[srdz], s50 offen offset:1024 sc1\n" \
"v_mfma_f32_16x16x32_bf16 v[44:47], a[200:203], v[56:59], v[44:47]\n" \
"ds_read_b64_tr_b16 a[230:231], v227 offset:23040\n" \
"v_mfma_f32_16x16x32_bf16 v[76:79], a[196:199], v[80:83], v[76:79]\n" \
"ds_read_b64_tr_b16 a[232:233], v227 offset:29184\n" \
"buffer_load_dwordx4 a[112:115], v210, %[srdz], s50 offen offset:0 sc1\n" \
"v_mfma_f32_16x16x32_bf16 v[76:79], a[200:203], v[88:91], v[76:79]\n" \
"ds_read_b64_tr_b16 a[234:235], v227 offset:31232\n" \
"v_mfma_f32_16x16x32_bf16 v[108:111], a[196:199], v[112:115], v[108:111]\n" \
"ds_read_b64_tr_b16 a[236:237], v215 offset:32768\n" \
"buffer_load_dwordx4 a[116:119], v210, %[srdz], s50 offen offset:1024 sc1\n" \
"v_mfma_f32_16x16x32_bf16 v[108:111], a[200:203], v[120:123], v[108:111]\n" \
"s_waitcnt vmcnt(63) lgkmcnt(13)\n" \
"s_nop 0\n" \
"v_mfma_f32_16x16x32_bf16 v[20:23], a[204:207], v[0:3], 0\n" \
"ds_read_b64_tr_b16 a[238:239], v215 offset:34816\n" \
"buffer_load_dwordx4 a[72:75], %[oq0], %[srdz], s50 offen offset:2048 sc1\n" \
"v_mfma_f32_16x16x32_bf16 v[20:23], a[208:211], v[8:11], v[20:23]\n" \
"ds_read_b64_tr_b16 a[196:197], v215 offset:36864\n" \
"v_mfma_f32_16x16x32_bf16 v[52:55], a[204:207], v[32:35], 0\n" \
"ds_read_b64_tr_b16 a[198:199], v215 offset:38912\n" \
"buffer_load_dwordx4 a[76:79], %[oq0], %[srdz], s50 offen offset:3072 sc1\n" \
"v_mfma_f32_16x16x32_bf16 v[52:55], a[208:211], v[40:43], v[52:55]\n" \
"v_mfma_f32_16x16x32_bf16 v[84:87], a[204:207], v[64:67], 0\n" \
"s_waitcnt vmcnt(63) lgkmcnt(4)\n" \
"s_barrier\n" \
"v_mfma_f32_16x16x32_bf16 v[84:87], a[208:211], v[72:75], v[84:87]\n" \
"buffer_load_dwordx4 a[88:91], v208, %[srdz], s50 offen offset:2048 sc1\n" \
"v_mfma_f32_16x16x32_bf16 v[116:119], a[204:207], v[96:99], 0\n" \
"ds_read_b64_tr_b16 a[200:201], v215 offset:40960\n" \
"v_mfma_f32_16x16x32_bf16 v[116:119], a[208:211], v[104:107], v[116:119]\n" \
"ds_read_b64_tr_b16 a[202:203], v215 offset:43008\n" \
"buffer_load_dwordx4 a[92:95], v208, %[srdz], s50 offen offset:3072 sc1\n" \
"v_mfma_f32_16x16x32_bf16 v[20:23], a[212:215], v[16:19], v[20:23]\n" \
"ds_read_b64_tr_b16 a[204:205], v215 offset:45056\n" \
"v_mfma_f32_16x16x32_bf16 v[20:23], a[216:219], v[24:27], v[20:23]\n" \
"ds_read_b64_tr_b16 a[206:207], v215 offset:47104\n" \
"buffer_load_dwordx4 a[104:107], v209, %[srdz], s50 offen offset:2048 sc1\n" \
"v_mfma_f32_16x16x32_bf16 v[52:55], a[212:215], v[48:51], v[52:55]\n" \
"v_mfma_f32_16x16x32_bf16 v[52:55], a[216:219], v[56:59], v[52:55]\n" \
"buffer_load_dwordx4 a[108:111], v209, %[srdz], s50 offen offset:3072 sc1\n" \
"v_mfma_f32_16x16x32_bf16 v[84:87], a[212:215], v[80:83], v[84:87]\n" \
"v_mfma_f32_16x16x32_bf16 v[84:87], a[216:219], v[88:91], v[84:87]\n" \
"buffer_load_dwordx4 a[120:123], v210, %[srdz], s50 offen offset:2048 sc1\n" \
"v_mfma_f32_16x16x32_bf16 v[116:119], a[212:215], v[112:115], v[116:119]\n" \
"v_mfma_f32_16x16x32_bf16 v[116:119], a[216:219], v[120:123], v[116:119]\n" \
"s_waitcnt vmcnt(63) lgkmcnt(10)\n" \
"s_nop 0\n" \
"v_mfma_f32_16x16x32_bf16 v[28:31], a[220:223], v[0:3], 0\n" \
"buffer_load_dwordx4 a[124:127], v210, %[srdz], s50 offen offset:3072 sc1\n" \
"v_mfma_f32_16x16x32_bf16 v[28:31], a[228:231], v[8:11], v[28:31]\n" \
"s_sub_u32 s61, s48, 4\n" \
"s_cmp_lt_u32 s61, 2\n" \
"s_cselect_b64 vcc, -1, 0\n" \
"s_nop 0\n" \
"v_mfma_f32_16x16x32_bf16 v[60:63], a[220:223], v[32:35], 0\n" \
"s_cbranch_vccz Lg5_%=\n" \
"s_nop 0\n" \
"buffer_store_dwordx4 v[0:3], %[oq0], %[srdz], s60 offen offset:0 sc1\n" \
"Lg5_%=:\n" \
"v_mfma_f32_16x16x32_bf16 v[60:63], a[228:231], v[40:43], v[60:63]\n" \
"v_mfma_f32_16x16x32_bf16 v[92:95], a[220:223], v[64:67], 0\n" \
"s_cmp_gt_u32 s48, 1\n" \
"s_cselect_b32 s50, 1, 2\n" \
"s_mul_i32 s50, s50, 152064\n" \
"buffer_load_dwordx4 a[0:3], %[oq0], %[srdz], s50 offen offset:0 sc1\n" \
"v_mfma_f32_16x16x32_bf16 v[92:95], a[228:231], v[72:75], v[92:95]\n" \
"v_mfma_f32_16x16x32_bf16 v[124:127], a[220:223], v[96:99], 0\n" \
"ds_read_b64_tr_b16 a[208:209], v227 offset:32768\n" \
"v_mfma_f32_16x16x32_bf16 v[124:127], a[228:231], v[104:107], v[124:127]\n" \
"ds_read_b64_tr_b16 a[210:211], v227 offset:34816\n" \
"s_waitcnt vmcnt(15) lgkmcnt(6)\n" \
"s_nop 0\n" \
"v_mfma_f32_16x16x32_bf16 v[128:131], a[236:239], a[64:67], v[192:195]\n" \
"ds_read_b64_tr_b16 a[212:213], v227 offset:36864\n" \
"v_mfma_f32_16x16x32_bf16 v[128:131], a[196:199], a[68:71], v[128:131]\n" \
"ds_read_b64_tr_b16 a[214:215], v227 offset:38912\n" \
"s_waitcnt vmcnt(13) lgkmcnt(15)\n" \
"s_nop 0\n" \
"v_mfma_f32_16x16x32_bf16 v[132:135], a[236:239], a[80:83], v[196:199]\n" \
"s_cbranch_vccz Lg6_%=\n" \
"s_nop 0\n" \
"buffer_store_dwordx4 v[8:11], %[oq0], %[srdz], s60 offen offset:1024 sc1\n" \
"Lg6_%=:\n" \
"v_mfma_f32_16x16x32_bf16 v[132:135], a[196:199], a[84:87], v[132:135]\n" \
"s_waitcnt vmcnt(13) lgkmcnt(15)\n" \
"s_bitcmp1_b32 0x30, s48\n" \
"s_cbranch_scc1 Lcw2_%=\n" \
"s_waitcnt vmcnt(11)\n" \
"Lcw2_%=:\n" \
"v_mfma_f32_16x16x32_bf16 v[136:139], a[236:239], a[96:99], v[200:203]\n" \
"buffer_load_dwordx4 a[4:7], %[oq0], %[srdz], s50 offen offset:1024 sc1\n" \
"v_mfma_f32_16x16x32_bf16 v[136:139], a[196:199], a[100:103], v[136:139]\n" \
"s_waitcnt vmcnt(12) lgkmcnt(15)\n" \
"s_bitcmp1_b32 0x30, s48\n" \
"s_cbranch_scc1 Lcw3_%=\n" \
"s_waitcnt vmcnt(10)\n" \
"Lcw3_%=:\n" \
"v_mfma_f32_16x16x32_bf16 v[140:143], a[236:239], a[112:115], v[204:207]\n" \
"ds_read_b64_tr_b16 a[216:217], v227 offset:40960\n" \
"v_mfma_f32_16x16x32_bf16 v[140:143], a[196:199], a[116:119], v[140:143]\n" \
"s_waitcnt vmcnt(10) lgkmcnt(5)\n" \
"s_bitcmp1_b32 0x30, s48\n" \
"s_cbranch_scc1 Lcw4_%=\n" \
"s_waitcnt vmcnt(8)\n" \
"Lcw4_%=:\n" \
"v_mfma_f32_16x16x32_bf16 v[128:131], a[200:203], a[72:75], v[128:131]\n" \
"ds_read_b64_tr_b16 a[218:219], v227 offset:43008\n" \
"buffer_load_dwordx4 a[8:11], %[oq0], %[srdz], s50 offen offset:2048 sc1\n" \
"v_mfma_f32_16x16x32_bf16 v[128:131], a[204:207], a[76:79], v[128:131]\n" \
"ds_read_b64_tr_b16 a[220:221], v227 offset:45056\n" \
"s_waitcnt vmcnt(9) lgkmcnt(15)\n" \
"s_bitcmp1_b32 0x30, s48\n" \
"s_cbranch_scc1 Lcw5_%=\n" \
"s_waitcnt vmcnt(7)\n" \
"Lcw5_%=:\n" \
"v_mfma_f32_16x16x32_bf16 v[132:135], a[200:203], a[88:91], v[132:135]\n" \
"ds_read_b64_tr_b16 a[222:223], v227 offset:47104\n" \
"v_mfma_f32_16x16x32_bf16 v[132:135], a[204:207], a[92:95], v[132:135]\n" \
"s_cbranch_vccz Lg7_%=\n" \
"s_nop 0\n" \
"buffer_store_dwordx4 v[16:19], %[oq0], %[srdz], s60 offen offset:2048 sc1\n" \
"Lg7_%=:\n" \
"s_waitcnt vmcnt(6) lgkmcnt(14)\n" \
"s_bitcmp1_b32 0x30, s48\n" \
"s_cbranch_scc1 Lcw6_%=\n" \
"s_waitcnt vmcnt(3)\n" \
"Lcw6_%=:\n" \
"v_mfma_f32_16x16x32_bf16 v[136:139], a[200:203], a[104:107], v[136:139]\n" \
"v_mfma_f32_16x16x32_bf16 v[136:139], a[204:207], a[108:111], v[136:139]\n" \
"buffer_load_dwordx4 a[12:15], %[oq0], %[srdz], s50 offen offset:3072 sc1\n" \
"v_mfma_f32_16x16x32_bf16 v[140:143], a[200:203], a[120:123], v[140:143]\n" \
"ds_read_b64_tr_b16 a[228:229], v215 offset:33280\n" \
"v_mfma_f32_16x16x32_bf16 v[140:143], a[204:207], a[124:127], v[140:143]\n" \
"ds_read_b64_tr_b16 a[230:231], v215 offset:35328\n" \
"v_mfma_f32_16x16x32_bf16 v[28:31], a[224:227], v[16:19], v[28:31]\n" \
"buffer_load_dwordx4 a[16:19], v208, %[srdz], s50 offen offset:0 sc1\n" \
"v_mfma_f32_16x16x32_bf16 v[28:31], a[232:235], v[24:27], v[28:31]\n" \
"v_mfma_f32_16x16x32_bf16 v[60:63], a[224:227], v[48:51], v[60:63]\n" \
"v_mfma_f32_16x16x32_bf16 v[60:63], a[232:235], v[56:59], v[60:63]\n" \
"s_cbranch_vccz Lg8_%=\n" \
"s_nop 0\n" \
"buffer_store_dwordx4 v[24:27], %[oq0], %[srdz], s60 offen offset:3072 sc1\n" \
"Lg8_%=:\n" \
"v_mfma_f32_16x16x32_bf16 v[92:95], a[224:227], v[80:83], v[92:95]\n" \
"v_mfma_f32_16x16x32_bf16 v[92:95], a[232:235], v[88:91], v[92:95]\n" \
"v_cmp_u_f32_e64 s[44:45], v128, v132\n" \
"buffer_load_dwordx4 a[20:23], v208, %[srdz], s50 offen offset:1024 sc1\n" \
"v_mfma_f32_16x16x32_bf16 v[124:127], a[224:227], v[112:115], v[124:127]\n" \
"v_mfma_f32_16x16x32_bf16 v[124:127], a[232:235], v[120:123], v[124:127]\n" \
"v_cmp_u_f32_e64 s[56:57], v136, v140\n" \
"s_or_b64 s[56:57], s[56:57], s[44:45]\n" \
"s_cbranch_scc1 Lsp1_%=\n" \
"Lbk1_%=:\n" \
"s_waitcnt vmcnt(63) lgkmcnt(2)\n" \
"s_nop 0\n" \
"v_mfma_f32_16x16x32_bf16 v[144:147], a[208:211], a[64:67], v[144:147]\n" \
"ds_read_b64_tr_b16 a[224:225], v215 offset:37376\n" \
"v_mfma_f32_16x16x32_bf16 v[144:147], a[212:215], a[68:71], v[144:147]\n" \
"ds_read_b64_tr_b16 a[226:227], v215 offset:39424\n" \
"buffer_load_dwordx4 a[24:27], v208, %[srdz], s50 offen offset:2048 sc1\n" \
"v_mfma_f32_16x16x32_bf16 v[148:151], a[208:211], a[80:83], v[148:151]\n" \
"ds_read_b64_tr_b16 a[196:197], v215 offset:41472\n" \
"v_mfma_f32_16x16x32_bf16 v[148:151], a[212:215], a[84:87], v[148:151]\n" \
"ds_read_b64_tr_b16 a[198:199], v215 offset:43520\n" \
"v_mfma_f32_16x16x32_bf16 v[152:155], a[208:211], a[96:99], v[152:155]\n" \
"ds_read_b64_tr_b16 a[200:201], v215 offset:45568\n" \
"s_cbranch_vccz Lg9_%=\n" \
"s_nop 0\n" \
"buffer_store_dwordx4 v[32:35], v208, %[srdz], s60 offen offset:0 sc1\n" \
"Lg9_%=:\n" \
"v_mfma_f32_16x16x32_bf16 v[152:155], a[212:215], a[100:103], v[152:155]\n" \
"ds_read_b64_tr_b16 a[202:203], v215 offset:47616\n" \
"v_mfma_f32_16x16x32_bf16 v[156:159], a[208:211], a[112:115], v[156:159]\n" \
"ds_read_b64_tr_b16 a[204:205], v227 offset:33280\n" \
"buffer_load_dwordx4 a[28:31], v208, %[srdz], s50 offen offset:3072 sc1\n" \
"v_mfma_f32_16x16x32_bf16 v[156:159], a[212:215], a[116:119], v[156:159]\n" \
"ds_read_b64_tr_b16 a[206:207], v227 offset:35328\n" \
"v_mfma_f32_16x16x32_bf16 v[144:147], a[216:219], a[72:75], v[144:147]\n" \
"ds_read_b64_tr_b16 a[232:233], v227 offset:37376\n" \
"v_mfma_f32_16x16x32_bf16 v[144:147], a[220:223], a[76:79], v[144:147]\n" \
"ds_read_b64_tr_b16 a[234:235], v227 offset:39424\n" \
"v_mfma_f32_16x16x32_bf16 v[148:151], a[216:219], a[88:91], v[148:151]\n" \
"buffer_load_dwordx4 a[32:35], v209, %[srdz], s50 offen offset:0 sc1\n" \
"v_mfma_f32_16x16x32_bf16 v[148:151], a[220:223], a[92:95], v[148:151]\n" \
"v_mfma_f32_16x16x32_bf16 v[152:155], a[216:219], a[104:107], v[152:155]\n" \
"s_cbranch_vccz Lg10_%=\n" \
"s_nop 0\n" \
"buffer_store_dwordx4 v[40:43], v208, %[srdz], s60 offen offset:1024 sc1\n" \
"Lg10_%=:\n" \
"v_mfma_f32_16x16x32_bf16 v[152:155], a[220:223], a[108:111], v[152:155]\n" \
"v_mfma_f32_16x16x32_bf16 v[156:159], a[216:219], a[120:123], v[156:159]\n" \
"ds_read_b64_tr_b16 a[236:237], v227 offset:41472\n" \
"buffer_load_dwordx4 a[36:39], v209, %[srdz], s50 offen offset:1024 sc1\n" \
"v_mfma_f32_16x16x32_bf16 v[156:159], a[220:223], a[124:127], v[156:159]\n" \
"s_waitcnt vmcnt(63) lgkmcnt(5)\n" \
"s_nop 0\n" \
"v_mfma_f32_16x16x32_bf16 v[160:163], a[228:231], a[64:67], v[160:163]\n" \
"ds_read_b64_tr_b16 a[238:239], v227 offset:43520\n" \
"v_mfma_f32_16x16x32_bf16 v[160:163], a[224:227], a[68:71], v[160:163]\n" \
"ds_read_b64_tr_b16 a[208:209], v227 offset:45568\n" \
"v_mfma_f32_16x16x32_bf16 v[164:167], a[228:231], a[80:83], v[164:167]\n" \
"ds_read_b64_tr_b16 a[210:211], v227 offset:47616\n" \
"buffer_load_dwordx4 a[40:43], v209, %[srdz], s50 offen offset:2048 sc1\n" \
"v_mfma_f32_16x16x32_bf16 v[164:167], a[224:227], a[84:87], v[164:167]\n" \
"v_mfma_f32_16x16x32_bf16 v[168:171], a[228:231], a[96:99], v[168:171]\n" \
"v_mfma_f32_16x16x32_bf16 v[168:171], a[224:227], a[100:103], v[168:171]\n" \
"s_cbranch_vccz Lg11_%=\n" \
"s_nop 0\n" \
"buffer_store_dwordx4 v[48:51], v208, %[srdz], s60 offen offset:2048 sc1\n" \
"Lg11_%=:\n" \
"v_mfma_f32_16x16x32_bf16 v[172:175], a[228:231], a[112:115], v[172:175]\n" \
"v_mfma_f32_16x16x32_bf16 v[172:175], a[224:227], a[116:119], v[172:175]\n" \
"buffer_load_dwordx4 a[44:47], v209, %[srdz], s50 offen offset:3072 sc1\n" \
"v_mfma_f32_16x16x32_bf16 v[160:163], a[196:199], a[72:75], v[160:163]\n" \
"v_mfma_f32_16x16x32_bf16 v[160:163], a[200:203], a[76:79], v[160:163]\n" \
"buffer_load_dwordx4 a[48:51], v210, %[srdz], s50 offen offset:0 sc1\n" \
"v_mfma_f32_16x16x32_bf16 v[164:167], a[196:199], a[88:91], v[164:167]\n" \
"v_mfma_f32_16x16x32_bf16 v[164:167], a[200:203], a[92:95], v[164:167]\n" \
"buffer_load_dwordx4 a[52:55], v210, %[srdz], s50 offen offset:1024 sc1\n" \
"v_mfma_f32_16x16x32_bf16 v[168:171], a[196:199], a[104:107], v[168:171]\n" \
"s_waitcnt vmcnt(54) lgkmcnt(0)\n" \
"s_bitcmp1_b32 0xf, s48\n" \
"s_cbranch_scc1 Lcw7_%=\n" \
"s_waitcnt vmcnt(45)\n" \
"s_bitcmp1_b32 0x30, s48\n" \
"s_cbranch_scc1 Lcw7_%=\n" \
"s_waitcnt vmcnt(38)\n" \
"Lcw7_%=:\n" \
"s_barrier\n" \
"v_mfma_f32_16x16x32_bf16 v[168:171], a[200:203], a[108:111], v[168:171]\n" \
"ds_read_b64_tr_b16 a[212:213], v220 offset:32768\n" \
"s_cbranch_vccz Lg12_%=\n" \
"s_nop 0\n" \
"buffer_store_dwordx4 v[56:59], v208, %[srdz], s60 offen offset:3072 sc1\n" \
"Lg12_%=:\n" \
"v_mfma_f32_16x16x32_bf16 v[172:175], a[196:199], a[120:123], v[172:175]\n" \
"ds_read_b64_tr_b16 a[214:215], v220 offset:34816\n" \
"v_mfma_f32_16x16x32_bf16 v[172:175], a[200:203], a[124:127], v[172:175]\n" \
"s_waitcnt vmcnt(63) lgkmcnt(2)\n" \
"s_nop 0\n" \
"v_mfma_f32_16x16x32_bf16 v[176:179], a[204:207], a[64:67], v[176:179]\n" \
"ds_read_b64_tr_b16 a[216:217], v220 offset:36864\n" \
"buffer_load_dwordx4 a[56:59], v210, %[srdz], s50 offen offset:2048 sc1\n" \
"v_mfma_f32_16x16x32_bf16 v[176:179], a[232:235], a[68:71], v[176:179]\n" \
"ds_read_b64_tr_b16 a[218:219], v220 offset:38912\n" \
"v_mfma_f32_16x16x32_bf16 v[180:183], a[204:207], a[80:83], v[180:183]\n" \
"ds_read_b64_tr_b16 a[220:221], v220 offset:40960\n" \
"v_mfma_f32_16x16x32_bf16 v[180:183], a[232:235], a[84:87], v[180:183]\n" \
"ds_read_b64_tr_b16 a[222:223], v220 offset:43008\n" \
"v_mfma_f32_16x16x32_bf16 v[184:187], a[204:207], a[96:99], v[184:187]\n" \
"ds_read_b64_tr_b16 a[228:229], v220 offset:45056\n" \
"buffer_load_dwordx4 a[60:63], v210, %[srdz], s50 offen offset:3072 sc1\n" \
"v_mfma_f32_16x16x32_bf16 v[184:187], a[232:235], a[100:103], v[184:187]\n" \
"ds_read_b64_tr_b16 a[230:231], v220 offset:47104\n" \
"v_mfma_f32_16x16x32_bf16 v[188:191], a[204:207], a[112:115], v[188:191]\n" \
"ds_read_b64_tr_b16 a[224:225], v221 offset:32768\n" \
"s_cbranch_vccz Lg13_%=\n" \
"s_nop 0\n" \
"buffer_store_dwordx4 v[64:67], v209, %[srdz], s60 offen offset:0 sc1\n" \
"Lg13_%=:\n" \
"v_mfma_f32_16x16x32_bf16 v[188:191], a[232:235], a[116:119], v[188:191]\n" \
"ds_read_b64_tr_b16 a[226:227], v221 offset:34816\n" \
"v_mfma_f32_16x16x32_bf16 v[176:179], a[236:239], a[72:75], v[176:179]\n" \
"ds_read_b64_tr_b16 a[196:197], v221 offset:36864\n" \
"v_mfma_f32_16x16x32_bf16 v[176:179], a[208:211], a[76:79], v[176:179]\n" \
"ds_read_b64_tr_b16 a[198:199], v221 offset:38912\n" \
"v_mfma_f32_16x16x32_bf16 v[180:183], a[236:239], a[88:91], v[180:183]\n" \
"s_cmp_gt_u32 s48, 2\n" \
"s_cselect_b32 s50, 2, 3\n" \
"s_mul_i32 s50, s50, 152064\n" \
"buffer_load_dwordx4 a[128:131], %[oq0], %[srdz], s50 offen offset:0 sc1\n" \
"v_mfma_f32_16x16x32_bf16 v[180:183], a[208:211], a[92:95], v[180:183]\n" \
"v_mfma_f32_16x16x32_bf16 v[184:187], a[236:239], a[104:107], v[184:187]\n" \
"v_mfma_f32_16x16x32_bf16 v[184:187], a[208:211], a[108:111], v[184:187]\n" \
"v_mfma_f32_16x16x32_bf16 v[188:191], a[236:239], a[120:123], v[188:191]\n" \
"ds_read_b64_tr_b16 a[200:201], v221 offset:40960\n" \
"s_cbranch_vccz Lg14_%=\n" \
"s_nop 0\n" \
"buffer_store_dwordx4 v[72:75], v209, %[srdz], s60 offen offset:1024 sc1\n" \
"Lg14_%=:\n" \
"v_mfma_f32_16x16x32_bf16 v[188:191], a[208:211], a[124:127], v[188:191]\n" \
"s_waitcnt vmcnt(63) lgkmcnt(5)\n" \
"s_nop 0\n" \
"v_mfma_f32_16x16x32_bf16 v[4:7], a[212:215], a[64:67], v[4:7]\n" \
"ds_read_b64_tr_b16 a[202:203], v221 offset:43008\n" \
"v_mfma_f32_16x16x32_bf16 v[4:7], a[216:219], a[68:71], v[4:7]\n" \
"ds_read_b64_tr_b16 a[204:205], v221 offset:45056\n" \
"s_add_u32 m0, %[m0b], 0\n" \
"s_cmp_gt_u32 s48, 2\n" \
"s_cselect_b32 s49, 2, 3\n" \
"s_lshl_b32 s49, s49, 18\n" \
"s_or_b32 s49, s49, s58\n" \
"s_nop 0\n" \
"buffer_load_dwordx4 %[wo0], %[srdt], s49 offen offset:0 lds\n" \
"v_mfma_f32_16x16x32_bf16 v[36:39], a[212:215], a[80:83], v[36:39]\n" \
"ds_read_b64_tr_b16 a[206:207], v221 offset:47104\n" \
"v_mfma_f32_16x16x32_bf16 v[36:39], a[216:219], a[84:87], v[36:39]\n" \
"buffer_load_dwordx4 a[132:135], %[oq0], %[srdz], s50 offen offset:1024 sc1\n" \
"v_mfma_f32_16x16x32_bf16 v[68:71], a[212:215], a[96:99], v[68:71]\n" \
"v_mfma_f32_16x16x32_bf16 v[68:71], a[216:219], a[100:103], v[68:71]\n" \
"v_mfma_f32_16x16x32_bf16 v[100:103], a[212:215], a[112:115], v[100:103]\n" \
"ds_read_b64_tr_b16 a[232:233], v220 offset:33280\n" \
"v_mfma_f32_16x16x32_bf16 v[100:103], a[216:219], a[116:119], v[100:103]\n" \
"ds_read_b64_tr_b16 a[234:235], v220 offset:35328\n" \
"s_cbranch_vccz Lg15_%=\n" \
"s_nop 0\n" \
"buffer_store_dwordx4 v[80:83], v209, %[srdz], s60 offen offset:2048 sc1\n" \
"Lg15_%=:\n" \
"v_mfma_f32_16x16x32_bf16 v[4:7], a[220:223], a[72:75], v[4:7]\n" \
"ds_read_b64_tr_b16 a[236:237], v220 offset:37376\n" \
"v_mfma_f32_16x16x32_bf16 v[4:7], a[228:231], a[76:79], v[4:7]\n" \
"ds_read_b64_tr_b16 a[238:239], v220 offset:39424\n" \
"buffer_load_dwordx4 a[136:139], %[oq0], %[srdz], s50 offen offset:2048 sc1\n" \
"v_mfma_f32_16x16x32_bf16 v[36:39], a[220:223], a[88:91], v[36:39]\n" \
"v_mfma_f32_16x16x32_bf16 v[36:39], a[228:231], a[92:95], v[36:39]\n" \
"v_mfma_f32_16x16x32_bf16 v[68:71], a[220:223], a[104:107], v[68:71]\n" \
"v_mfma_f32_16x16x32_bf16 v[68:71], a[228:231], a[108:111], v[68:71]\n" \
"buffer_load_dwordx4 a[140:143], %[oq0], %[srdz], s50 offen offset:3072 sc1\n" \
"v_mfma_f32_16x16x32_bf16 v[100:103], a[220:223], a[120:123], v[100:103]\n" \
"ds_read_b64_tr_b16 a[208:209], v220 offset:41472\n" \
"v_mfma_f32_16x16x32_bf16 v[100:103], a[228:231], a[124:127], v[100:103]\n" \
"s_waitcnt vmcnt(63) lgkmcnt(5)\n" \
"s_nop 0\n" \
"v_mfma_f32_16x16x32_bf16 v[12:15], a[224:227], a[64:67], v[12:15]\n" \
"ds_read_b64_tr_b16 a[210:211], v220 offset:43520\n" \
"s_cbranch_vccz Lg16_%=\n" \
"s_nop 0\n" \
"buffer_store_dwordx4 v[88:91], v209, %[srdz], s60 offen offset:3072 sc1\n" \
"Lg16_%=:\n" \
"v_mfma_f32_16x16x32_bf16 v[12:15], a[196:199], a[68:71], v[12:15]\n" \
"ds_read_b64_tr_b16 a[212:213], v220 offset:45568\n" \
"v_mfma_f32_16x16x32_bf16 v[44:47], a[224:227], a[80:83], v[44:47]\n" \
"ds_read_b64_tr_b16 a[214:215], v220 offset:47616\n" \
"buffer_load_dwordx4 v212, %[srdt], s49 offen offset:1024 lds\n" \
"v_mfma_f32_16x16x32_bf16 v[44:47], a[196:199], a[84:87], v[44:47]\n" \
"v_mfma_f32_16x16x32_bf16 v[76:79], a[224:227], a[96:99], v[76:79]\n" \
"v_mfma_f32_16x16x32_bf16 v[76:79], a[196:199], a[100:103], v[76:79]\n" \
"buffer_load_dwordx4 a[144:147], v208, %[srdz], s50 offen offset:0 sc1\n" \
"v_mfma_f32_16x16x32_bf16 v[108:111], a[224:227], a[112:115], v[108:111]\n" \
"ds_read_b64_tr_b16 a[216:217], v221 offset:33280\n" \
"v_mfma_f32_16x16x32_bf16 v[108:111], a[196:199], a[116:119], v[108:111]\n" \
"ds_read_b64_tr_b16 a[218:219], v221 offset:35328\n" \
"v_mfma_f32_16x16x32_bf16 v[12:15], a[200:203], a[72:75], v[12:15]\n" \
"ds_read_b64_tr_b16 a[220:221], v221 offset:41472\n" \
"s_cbranch_vccz Lg17_%=\n" \
"s_nop 0\n" \
"buffer_store_dwordx4 v[96:99], v210, %[srdz], s60 offen offset:0 sc1\n" \
"Lg17_%=:\n" \
"v_mfma_f32_16x16x32_bf16 v[12:15], a[204:207], a[76:79], v[12:15]\n" \
"ds_read_b64_tr_b16 a[222:223], v221 offset:43520\n" \
"v_mfma_f32_16x16x32_bf16 v[44:47], a[200:203], a[88:91], v[44:47]\n" \
"ds_read_b64_tr_b16 a[228:229], v221 offset:37376\n" \
"v_mfma_f32_16x16x32_bf16 v[44:47], a[204:207], a[92:95], v[44:47]\n" \
"ds_read_b64_tr_b16 a[230:231], v221 offset:39424\n" \
"v_mfma_f32_16x16x32_bf16 v[76:79], a[200:203], a[104:107], v[76:79]\n" \
"ds_read_b64_tr_b16 a[224:225], v221 offset:45568\n" \
"v_mfma_f32_16x16x32_bf16 v[76:79], a[204:207], a[108:111], v[76:79]\n" \
"ds_read_b64_tr_b16 a[226:227], v221 offset:47616\n" \
".if . - adaln_body != 45056\n" \
".error \"size model\"\n" \
".endif\n" \
"Lk10_%=:\n" \
"s_cbranch_execz Lk11_%=\n" \
"s_nop 0\n" \
"v_mfma_f32_16x16x32_bf16 v[108:111], a[200:203], a[120:123], v[108:111]\n" \
"buffer_load_dwordx4 a[148:151], v208, %[srdz], s50 offen offset:1024 sc1\n" \
"v_mfma_f32_16x16x32_bf16 v[108:111], a[204:207], a[124:127], v[108:111]\n" \
"s_waitcnt vmcnt(63) lgkmcnt(12)\n" \
"s_nop 0\n" \
"v_mfma_f32_16x16x32_bf16 v[20:23], a[232:235], a[64:67], v[20:23]\n" \
"s_cbranch_vccz Lg18_%=\n" \
"s_nop 0\n" \
"buffer_store_dwordx4 v[104:107], v210, %[srdz], s60 offen offset:1024 sc1\n" \
"Lg18_%=:\n" \
"v_mfma_f32_16x16x32_bf16 v[20:23], a[236:239], a[68:71], v[20:23]\n" \
"v_mfma_f32_16x16x32_bf16 v[52:55], a[232:235], a[80:83], v[52:55]\n" \
"buffer_load_dwordx4 v213, %[srdt], s49 offen offset:2048 lds\n" \
"v_mfma_f32_16x16x32_bf16 v[52:55], a[236:239], a[84:87], v[52:55]\n" \
"v_mfma_f32_16x16x32_bf16 v[84:87], a[232:235], a[96:99], v[84:87]\n" \
"s_waitcnt vmcnt(59) lgkmcnt(0)\n" \
"s_bitcmp1_b32 0x30, s48\n" \
"s_cbranch_scc1 Lcw8_%=\n" \
"s_waitcnt vmcnt(45)\n" \
"Lcw8_%=:\n" \
"s_barrier\n" \
"s_nop 0\n" \
"v_mfma_f32_16x16x32_bf16 v[84:87], a[236:239], a[100:103], v[84:87]\n" \
"ds_read_b64_tr_b16 a[196:197], v220 offset:49152\n" \
"v_mfma_f32_16x16x32_bf16 v[116:119], a[232:235], a[112:115], v[116:119]\n" \
"ds_read_b64_tr_b16 a[198:199], v220 offset:51200\n" \
"buffer_load_dwordx4 a[152:155], v208, %[srdz], s50 offen offset:2048 sc1\n" \
"v_mfma_f32_16x16x32_bf16 v[116:119], a[236:239], a[116:119], v[116:119]\n" \
"ds_read_b64_tr_b16 a[200:201], v220 offset:53248\n" \
"v_mfma_f32_16x16x32_bf16 v[20:23], a[208:211], a[72:75], v[20:23]\n" \
"ds_read_b64_tr_b16 a[202:203], v220 offset:55296\n" \
"v_mfma_f32_16x16x32_bf16 v[20:23], a[212:215], a[76:79], v[20:23]\n" \
"ds_read_b64_tr_b16 a[204:205], v220 offset:57344\n" \
"s_cbranch_vccz Lg19_%=\n" \
"s_nop 0\n" \
"buffer_store_dwordx4 v[112:115], v210, %[srdz], s60 offen offset:2048 sc1\n" \
"Lg19_%=:\n" \
"v_mfma_f32_16x16x32_bf16 v[52:55], a[208:211], a[88:91], v[52:55]\n" \
"ds_read_b64_tr_b16 a[206:207], v220 offset:59392\n" \
"v_mfma_f32_16x16x32_bf16 v[52:55], a[212:215], a[92:95], v[52:55]\n" \
"ds_read_b64_tr_b16 a[232:233], v220 offset:61440\n" \
"v_mfma_f32_16x16x32_bf16 v[84:87], a[208:211], a[104:107], v[84:87]\n" \
"ds_read_b64_tr_b16 a[234:235], v220 offset:63488\n" \
"v_mfma_f32_16x16x32_bf16 v[84:87], a[212:215], a[108:111], v[84:87]\n" \
"buffer_load_dwordx4 a[156:159], v208, %[srdz], s50 offen offset:3072 sc1\n" \
"v_mfma_f32_16x16x32_bf16 v[116:119], a[208:211], a[120:123], v[116:119]\n" \
"v_mfma_f32_16x16x32_bf16 v[116:119], a[212:215], a[124:127], v[116:119]\n" \
"s_waitcnt vmcnt(21) lgkmcnt(4)\n" \
"s_bitcmp1_b32 0x30, s48\n" \
"s_cbranch_scc1 Lcw9_%=\n" \
"s_waitcnt vmcnt(13)\n" \
"Lcw9_%=:\n" \
"v_mfma_f32_16x16x32_bf16 v[28:31], a[216:219], a[64:67], v[28:31]\n" \
"v_mfma_f32_16x16x32_bf16 v[28:31], a[228:231], a[68:71], v[28:31]\n" \
"s_cbranch_vccz Lg20_%=\n" \
"s_nop 0\n" \
"buffer_store_dwordx4 v[120:123], v210, %[srdz], s60 offen offset:3072 sc1\n" \
"Lg20_%=:\n" \
"v_mfma_f32_16x16x32_bf16 v[60:63], a[216:219], a[80:83], v[60:63]\n" \
"v_mfma_f32_16x16x32_bf16 v[60:63], a[228:231], a[84:87], v[60:63]\n" \
"buffer_load_dwordx4 v214, %[srdt], s49 offen offset:3072 lds\n" \
"v_mfma_f32_16x16x32_bf16 v[92:95], a[216:219], a[96:99], v[92:95]\n" \
"v_mfma_f32_16x16x32_bf16 v[92:95], a[228:231], a[100:103], v[92:95]\n" \
"buffer_load_dwordx4 a[160:163], v209, %[srdz], s50 offen offset:0 sc1\n" \
"v_mfma_f32_16x16x32_bf16 v[124:127], a[216:219], a[112:115], v[124:127]\n" \
"ds_read_b64_tr_b16 a[236:237], v221 offset:49152\n" \
"v_mfma_f32_16x16x32_bf16 v[124:127], a[228:231], a[116:119], v[124:127]\n" \
"ds_read_b64_tr_b16 a[238:239], v221 offset:51200\n" \
"v_mfma_f32_16x16x32_bf16 v[192:195], a[196:199], a[0:3], v[128:131]\n" \
"ds_read_b64_tr_b16 a[208:209], v221 offset:53248\n" \
"v_mfma_f32_16x16x32_bf16 v[192:195], a[200:203], a[4:7], v[192:195]\n" \
"ds_read_b64_tr_b16 a[210:211], v221 offset:55296\n" \
"v_mfma_f32_16x16x32_bf16 v[196:199], a[196:199], a[16:19], v[132:135]\n" \
"buffer_load_dwordx4 a[164:167], v209, %[srdz], s50 offen offset:1024 sc1\n" \
"v_mfma_f32_16x16x32_bf16 v[196:199], a[200:203], a[20:23], v[196:199]\n" \
"v_mfma_f32_16x16x32_bf16 v[200:203], a[196:199], a[32:35], v[136:139]\n" \
"v_mfma_f32_16x16x32_bf16 v[200:203], a[200:203], a[36:39], v[200:203]\n" \
"v_mfma_f32_16x16x32_bf16 v[204:207], a[196:199], a[48:51], v[140:143]\n" \
"ds_read_b64_tr_b16 a[212:213], v221 offset:57344\n" \
"s_sub_u32 s61, s48, 6\n" \
"s_cmp_lt_u32 s61, 2\n" \
"s_cselect_b64 vcc, -1, 0\n" \
"s_nop 0\n" \
"v_mfma_f32_16x16x32_bf16 v[204:207], a[200:203], a[52:55], v[204:207]\n" \
"s_waitcnt vmcnt(22) lgkmcnt(5)\n" \
"s_bitcmp1_b32 0x30, s48\n" \
"s_cbranch_scc1 Lcw10_%=\n" \
"s_waitcnt vmcnt(14)\n" \
"Lcw10_%=:\n" \
"v_mfma_f32_16x16x32_bf16 v[192:195], a[204:207], a[8:11], v[192:195]\n" \
"ds_read_b64_tr_b16 a[214:215], v221 offset:59392\n" \
"s_cbranch_vccz Lg21_%=\n" \
"s_nop 0\n" \
"buffer_store_dwordx4 v[0:3], %[oq0], %[srdz], s60 offen offset:0 sc1\n" \
"Lg21_%=:\n" \
"v_mfma_f32_16x16x32_bf16 v[192:195], a[232:235], a[12:15], v[192:195]\n" \
"ds_read_b64_tr_b16 a[216:217], v221 offset:61440\n" \
"v_mfma_f32_16x16x32_bf16 v[196:199], a[204:207], a[24:27], v[196:199]\n" \
"ds_read_b64_tr_b16 a[218:219], v221 offset:63488\n" \
"v_mfma_f32_16x16x32_bf16 v[196:199], a[232:235], a[28:31], v[196:199]\n" \
"buffer_load_dwordx4 a[168:171], v209, %[srdz], s50 offen offset:2048 sc1\n" \
"v_mfma_f32_16x16x32_bf16 v[200:203], a[204:207], a[40:43], v[200:203]\n" \
"v_mfma_f32_16x16x32_bf16 v[200:203], a[232:235], a[44:47], v[200:203]\n" \
"v_mfma_f32_16x16x32_bf16 v[204:207], a[204:207], a[56:59], v[204:207]\n" \
"ds_read_b64_tr_b16 a[228:229], v220 offset:49664\n" \
"v_mfma_f32_16x16x32_bf16 v[204:207], a[232:235], a[60:63], v[204:207]\n" \
"ds_read_b64_tr_b16 a[230:231], v220 offset:51712\n" \
"v_mfma_f32_16x16x32_bf16 v[28:31], a[220:223], a[72:75], v[28:31]\n" \
"s_cbranch_vccz Lg22_%=\n" \
"s_nop 0\n" \
"buffer_store_dwordx4 v[8:11], %[oq0], %[srdz], s60 offen offset:1024 sc1\n" \
"Lg22_%=:\n" \
"v_mfma_f32_16x16x32_bf16 v[28:31], a[224:227], a[76:79], v[28:31]\n" \
"v_mfma_f32_16x16x32_bf16 v[60:63], a[220:223], a[88:91], v[60:63]\n" \
"buffer_load_dwordx4 a[172:175], v209, %[srdz], s50 offen offset:3072 sc1\n" \
"v_mfma_f32_16x16x32_bf16 v[60:63], a[224:227], a[92:95], v[60:63]\n" \
"v_mfma_f32_16x16x32_bf16 v[92:95], a[220:223], a[104:107], v[92:95]\n" \
"v_mfma_f32_16x16x32_bf16 v[92:95], a[224:227], a[108:111], v[92:95]\n" \
"v_cmp_u_f32_e64 s[44:45], v192, v196\n" \
"v_mfma_f32_16x16x32_bf16 v[124:127], a[220:223], a[120:123], v[124:127]\n" \
"s_add_u32 m0, %[m0b], 16384\n" \
"s_cmp_gt_u32 s48, 2\n" \
"s_cselect_b32 s49, 2, 3\n" \
"s_lshl_b32 s49, s49, 18\n" \
"s_or_b32 s49, s49, s59\n" \
"buffer_load_dwordx4 %[wo0], %[srdt], s49 offen offset:0 lds\n" \
"v_mfma_f32_16x16x32_bf16 v[124:127], a[224:227], a[124:127], v[124:127]\n" \
"v_cmp_u_f32_e64 s[56:57], v200, v204\n" \
"s_or_b64 s[56:57], s[56:57], s[44:45]\n" \
"s_cbranch_scc1 Lsp2_%=\n" \
"Lbk2_%=:\n" \
"s_waitcnt vmcnt(63) lgkmcnt(2)\n" \
"s_nop 0\n" \
"v_mfma_f32_16x16x32_bf16 v[144:147], a[236:239], a[0:3], v[144:147]\n" \
"ds_read_b64_tr_b16 a[220:221], v220 offset:53760\n" \
"v_mfma_f32_16x16x32_bf16 v[144:147], a[208:211], a[4:7], v[144:147]\n" \
"ds_read_b64_tr_b16 a[222:223], v220 offset:55808\n" \
"s_cbranch_vccz Lg23_%=\n" \
"s_nop 0\n" \
"buffer_store_dwordx4 v[16:19], %[oq0], %[srdz], s60 offen offset:2048 sc1\n" \
"Lg23_%=:\n" \
"v_mfma_f32_16x16x32_bf16 v[148:151], a[236:239], a[16:19], v[148:151]\n" \
"ds_read_b64_tr_b16 a[196:197], v220 offset:57856\n" \
"v_mfma_f32_16x16x32_bf16 v[148:151], a[208:211], a[20:23], v[148:151]\n" \
"ds_read_b64_tr_b16 a[198:199], v220 offset:59904\n" \
"buffer_load_dwordx4 a[176:179], v210, %[srdz], s50 offen offset:0 sc1\n" \
"v_mfma_f32_16x16x32_bf16 v[152:155], a[236:239], a[32:35], v[152:155]\n" \
"ds_read_b64_tr_b16 a[200:201], v220 offset:61952\n" \
"v_mfma_f32_16x16x32_bf16 v[152:155], a[208:211], a[36:39], v[152:155]\n" \
"ds_read_b64_tr_b16 a[202:203], v220 offset:64000\n" \
"v_mfma_f32_16x16x32_bf16 v[156:159], a[236:239], a[48:51], v[156:159]\n" \
"ds_read_b64_tr_b16 a[204:205], v221 offset:49664\n" \
"v_mfma_f32_16x16x32_bf16 v[156:159], a[208:211], a[52:55], v[156:159]\n" \
"ds_read_b64_tr_b16 a[206:207], v221 offset:51712\n" \
"v_mfma_f32_16x16x32_bf16 v[144:147], a[212:215], a[8:11], v[144:147]\n" \
"ds_read_b64_tr_b16 a[224:225], v221 offset:53760\n" \
"v_mfma_f32_16x16x32_bf16 v[144:147], a[216:219], a[12:15], v[144:147]\n" \
"ds_read_b64_tr_b16 a[226:227], v221 offset:55808\n" \
"v_mfma_f32_16x16x32_bf16 v[148:151], a[212:215], a[24:27], v[148:151]\n" \
"s_cbranch_vccz Lg24_%=\n" \
"s_nop 0\n" \
"buffer_store_dwordx4 v[24:27], %[oq0], %[srdz], s60 offen offset:3072 sc1\n" \
"Lg24_%=:\n" \
"v_mfma_f32_16x16x32_bf16 v[148:151], a[216:219], a[28:31], v[148:151]\n" \
"v_mfma_f32_16x16x32_bf16 v[152:155], a[212:215], a[40:43], v[152:155]\n" \
"buffer_load_dwordx4 a[180:183], v210, %[srdz], s50 offen offset:1024 sc1\n" \
"v_mfma_f32_16x16x32_bf16 v[152:155], a[216:219], a[44:47], v[152:155]\n" \
"v_mfma_f32_16x16x32_bf16 v[156:159], a[212:215], a[56:59], v[156:159]\n" \
"ds_read_b64_tr_b16 a[232:233], v221 offset:57856\n" \
"v_mfma_f32_16x16x32_bf16 v[156:159], a[216:219], a[60:63], v[156:159]\n" \
"s_waitcnt vmcnt(63) lgkmcnt(5)\n" \
"s_nop 0\n" \
"v_mfma_f32_16x16x32_bf16 v[160:163], a[228:231], a[0:3], v[160:163]\n" \
"ds_read_b64_tr_b16 a[234:235], v221 offset:59904\n" \
"buffer_load_dwordx4 v212, %[srdt], s49 offen offset:1024 lds\n" \
"v_mfma_f32_16x16x32_bf16 v[160:163], a[220:223], a[4:7], v[160:163]\n" \
"ds_read_b64_tr_b16 a[236:237], v221 offset:61952\n" \
"v_mfma_f32_16x16x32_bf16 v[164:167], a[228:231], a[16:19], v[164:167]\n" \
"ds_read_b64_tr_b16 a[238:239], v221 offset:64000\n" \
"v_mfma_f32_16x16x32_bf16 v[164:167], a[220:223], a[20:23], v[164:167]\n" \
"s_cbranch_vccz Lg25_%=\n" \
"s_nop 0\n" \
"buffer_store_dwordx4 v[32:35], v208, %[srdz], s60 offen offset:0 sc1\n" \
"Lg25_%=:\n" \
"v_mfma_f32_16x16x32_bf16 v[168:171], a[228:231], a[32:35], v[168:171]\n" \
"v_mfma_f32_16x16x32_bf16 v[168:171], a[220:223], a[36:39], v[168:171]\n" \
"buffer_load_dwordx4 a[184:187], v210, %[srdz], s50 offen offset:2048 sc1\n" \
"v_mfma_f32_16x16x32_bf16 v[172:175], a[228:231], a[48:51], v[172:175]\n" \
"v_mfma_f32_16x16x32_bf16 v[172:175], a[220:223], a[52:55], v[172:175]\n" \
"buffer_load_dwordx4 a[188:191], v210, %[srdz], s50 offen offset:3072 sc1\n" \
"v_mfma_f32_16x16x32_bf16 v[160:163], a[196:199], a[8:11], v[160:163]\n" \
"v_mfma_f32_16x16x32_bf16 v[160:163], a[200:203], a[12:15], v[160:163]\n" \
"v_mfma_f32_16x16x32_bf16 v[164:167], a[196:199], a[24:27], v[164:167]\n" \
"v_mfma_f32_16x16x32_bf16 v[164:167], a[200:203], a[28:31], v[164:167]\n" \
"v_mfma_f32_16x16x32_bf16 v[168:171], a[196:199], a[40:43], v[168:171]\n" \
"s_waitcnt vmcnt(63) lgkmcnt(0)\n" \
"s_bitcmp1_b32 0x30, s48\n" \
"s_cbranch_scc1 Lcw11_%=\n" \
"s_waitcnt vmcnt(59)\n" \
"s_bitcmp1_b32 0xc0, s48\n" \
"s_cbranch_scc1 Lcw11_%=\n" \
"s_waitcnt vmcnt(54)\n" \
"Lcw11_%=:\n" \
"s_barrier\n" \
"s_nop 0\n" \
"v_mfma_f32_16x16x32_bf16 v[168:171], a[200:203], a[44:47], v[168:171]\n" \
"ds_read_b64_tr_b16 a[208:209], v215 offset:49152\n" \
"s_cbranch_vccz Lg26_%=\n" \
"s_nop 0\n" \
"buffer_store_dwordx4 v[40:43], v208, %[srdz], s60 offen offset:1024 sc1\n" \
"Lg26_%=:\n" \
"v_mfma_f32_16x16x32_bf16 v[172:175], a[196:199], a[56:59], v[172:175]\n" \
"ds_read_b64_tr_b16 a[210:211], v215 offset:51200\n" \
"v_mfma_f32_16x16x32_bf16 v[172:175], a[200:203], a[60:63], v[172:175]\n" \
"s_waitcnt vmcnt(63) lgkmcnt(2)\n" \
"s_nop 0\n" \
"v_mfma_f32_16x16x32_bf16 v[176:179], a[204:207], a[0:3], v[176:179]\n" \
"ds_read_b64_tr_b16 a[212:213], v215 offset:53248\n" \
"global_load_dword v237, %[vct], %[pct]\n" \
"v_mfma_f32_16x16x32_bf16 v[176:179], a[224:227], a[4:7], v[176:179]\n" \
"ds_read_b64_tr_b16 a[214:215], v215 offset:55296\n" \
"v_mfma_f32_16x16x32_bf16 v[180:183], a[204:207], a[16:19], v[180:183]\n" \
"ds_read_b64_tr_b16 a[216:217], v215 offset:57344\n" \
"buffer_load_dwordx4 v213, %[srdt], s49 offen offset:2048 lds\n" \
"v_mfma_f32_16x16x32_bf16 v[180:183], a[224:227], a[20:23], v[180:183]\n" \
"ds_read_b64_tr_b16 a[218:219], v215 offset:59392\n" \
"v_mfma_f32_16x16x32_bf16 v[184:187], a[204:207], a[32:35], v[184:187]\n" \
"ds_read_b64_tr_b16 a[228:229], v215 offset:61440\n" \
"v_mfma_f32_16x16x32_bf16 v[184:187], a[224:227], a[36:39], v[184:187]\n" \
"ds_read_b64_tr_b16 a[230:231], v215 offset:63488\n" \
"s_cbranch_vccz Lg27_%=\n" \
"s_nop 0\n" \
"buffer_store_dwordx4 v[48:51], v208, %[srdz], s60 offen offset:2048 sc1\n" \
"Lg27_%=:\n" \
"v_mfma_f32_16x16x32_bf16 v[188:191], a[204:207], a[48:51], v[188:191]\n" \
"ds_read_b64_tr_b16 a[220:221], v227 offset:49152\n" \
"v_mfma_f32_16x16x32_bf16 v[188:191], a[224:227], a[52:55], v[188:191]\n" \
"ds_read_b64_tr_b16 a[222:223], v227 offset:51200\n" \
"v_mfma_f32_16x16x32_bf16 v[176:179], a[232:235], a[8:11], v[176:179]\n" \
"ds_read_b64_tr_b16 a[196:197], v227 offset:53248\n" \
"v_mfma_f32_16x16x32_bf16 v[176:179], a[236:239], a[12:15], v[176:179]\n" \
"ds_read_b64_tr_b16 a[198:199], v227 offset:55296\n" \
"s_cmp_gt_u32 s48, 3\n" \
"s_cselect_b32 s50, 3, 4\n" \
"s_mul_i32 s50, s50, 152064\n" \
"buffer_load_dwordx4 a[64:67], %[oq0], %[srdz], s50 offen offset:0 sc1\n" \
"v_mfma_f32_16x16x32_bf16 v[180:183], a[232:235], a[24:27], v[180:183]\n" \
"v_mfma_f32_16x16x32_bf16 v[180:183], a[236:239], a[28:31], v[180:183]\n" \
"buffer_load_dwordx4 v214, %[srdt], s49 offen offset:3072 lds\n" \
"v_mfma_f32_16x16x32_bf16 v[184:187], a[232:235], a[40:43], v[184:187]\n" \
"v_mfma_f32_16x16x32_bf16 v[184:187], a[236:239], a[44:47], v[184:187]\n" \
"v_mfma_f32_16x16x32_bf16 v[188:191], a[232:235], a[56:59], v[188:191]\n" \
"ds_read_b64_tr_b16 a[200:201], v227 offset:57344\n" \
"s_cbranch_vccz Lg28_%=\n" \
"s_nop 0\n" \
"buffer_store_dwordx4 v[56:59], v208, %[srdz], s60 offen offset:3072 sc1\n" \
"Lg28_%=:\n" \
"v_mfma_f32_16x16x32_bf16 v[188:191], a[236:239], a[60:63], v[188:191]\n" \
"s_waitcnt vmcnt(63) lgkmcnt(5)\n" \
"s_nop 0\n" \
"v_mfma_f32_16x16x32_bf16 v[4:7], a[208:211], a[0:3], v[4:7]\n" \
"ds_read_b64_tr_b16 a[202:203], v227 offset:59392\n" \
"buffer_load_dwordx4 a[68:71], %[oq0], %[srdz], s50 offen offset:1024 sc1\n" \
"v_mfma_f32_16x16x32_bf16 v[4:7], a[212:215], a[4:7], v[4:7]\n" \
"ds_read_b64_tr_b16 a[204:205], v227 offset:61440\n" \
"v_mfma_f32_16x16x32_bf16 v[36:39], a[208:211], a[16:19], v[36:39]\n" \
"ds_read_b64_tr_b16 a[206:207], v227 offset:63488\n" \
"v_mfma_f32_16x16x32_bf16 v[36:39], a[212:215], a[20:23], v[36:39]\n" \
"v_mfma_f32_16x16x32_bf16 v[68:71], a[208:211], a[32:35], v[68:71]\n" \
"buffer_load_dwordx4 a[72:75], %[oq0], %[srdz], s50 offen offset:2048 sc1\n" \
"v_mfma_f32_16x16x32_bf16 v[68:71], a[212:215], a[36:39], v[68:71]\n" \
"v_mfma_f32_16x16x32_bf16 v[100:103], a[208:211], a[48:51], v[100:103]\n" \
"ds_read_b64_tr_b16 a[224:225], v215 offset:49664\n" \
"v_mfma_f32_16x16x32_bf16 v[100:103], a[212:215], a[52:55], v[100:103]\n" \
"ds_read_b64_tr_b16 a[226:227], v215 offset:51712\n" \
"s_cbranch_vccz Lg29_%=\n" \
"s_nop 0\n" \
"buffer_store_dwordx4 v[64:67], v209, %[srdz], s60 offen offset:0 sc1\n" \
"Lg29_%=:\n" \
"v_mfma_f32_16x16x32_bf16 v[4:7], a[216:219], a[8:11], v[4:7]\n" \
"ds_read_b64_tr_b16 a[232:233], v215 offset:53760\n" \
"v_mfma_f32_16x16x32_bf16 v[4:7], a[228:231], a[12:15], v[4:7]\n" \
"ds_read_b64_tr_b16 a[234:235], v215 offset:55808\n" \
"v_mfma_f32_16x16x32_bf16 v[36:39], a[216:219], a[24:27], v[36:39]\n" \
"v_mfma_f32_16x16x32_bf16 v[36:39], a[228:231], a[28:31], v[36:39]\n" \
"buffer_load_dwordx4 a[76:79], %[oq0], %[srdz], s50 offen offset:3072 sc1\n" \
"v_mfma_f32_16x16x32_bf16 v[68:71], a[216:219], a[40:43], v[68:71]\n" \
"v_mfma_f32_16x16x32_bf16 v[68:71], a[228:231], a[44:47], v[68:71]\n" \
"v_mfma_f32_16x16x32_bf16 v[100:103], a[216:219], a[56:59], v[100:103]\n" \
"ds_read_b64_tr_b16 a[236:237], v215 offset:57856\n" \
"v_mfma_f32_16x16x32_bf16 v[100:103], a[228:231], a[60:63], v[100:103]\n" \
"s_waitcnt vmcnt(63) lgkmcnt(5)\n" \
"s_nop 0\n" \
"v_mfma_f32_16x16x32_bf16 v[12:15], a[220:223], a[0:3], v[12:15]\n" \
"ds_read_b64_tr_b16 a[238:239], v215 offset:59904\n" \
"s_cbranch_vccz Lg30_%=\n" \
"s_nop 0\n" \
"buffer_store_dwordx4 v[72:75], v209, %[srdz], s60 offen offset:1024 sc1\n" \
"Lg30_%=:\n" \
"v_mfma_f32_16x16x32_bf16 v[12:15], a[196:199], a[4:7], v[12:15]\n" \
"ds_read_b64_tr_b16 a[208:209], v215 offset:61952\n" \
"v_mfma_f32_16x16x32_bf16 v[44:47], a[220:223], a[16:19], v[44:47]\n" \
"ds_read_b64_tr_b16 a[210:211], v215 offset:64000\n" \
"v_mfma_f32_16x16x32_bf16 v[44:47], a[196:199], a[20:23], v[44:47]\n" \
"buffer_load_dwordx4 a[80:83], v208, %[srdz], s50 offen offset:0 sc1\n" \
"v_mfma_f32_16x16x32_bf16 v[76:79], a[220:223], a[32:35], v[76:79]\n" \
"v_mfma_f32_16x16x32_bf16 v[76:79], a[196:199], a[36:39], v[76:79]\n" \
"s_add_u32 m0, %[m0b], 32768\n" \
"s_cmp_gt_u32 s48, 3\n" \
"s_cselect_b32 s49, 3, 4\n" \
"s_lshl_b32 s49, s49, 18\n" \
"s_or_b32 s49, s49, s58\n" \
"buffer_load_dwordx4 %[wo0], %[srdt], s49 offen offset:0 lds\n" \
"v_mfma_f32_16x16x32_bf16 v[108:111], a[220:223], a[48:51], v[108:111]\n" \
"ds_read_b64_tr_b16 a[212:213], v227 offset:49664\n" \
"v_mfma_f32_16x16x32_bf16 v[108:111], a[196:199], a[52:55], v[108:111]\n" \
"ds_read_b64_tr_b16 a[214:215], v227 offset:51712\n" \
"v_mfma_f32_16x16x32_bf16 v[12:15], a[200:203], a[8:11], v[12:15]\n" \
"ds_read_b64_tr_b16 a[216:217], v227 offset:57856\n" \
"v_mfma_f32_16x16x32_bf16 v[12:15], a[204:207], a[12:15], v[12:15]\n" \
"ds_read_b64_tr_b16 a[218:219], v227 offset:59904\n" \
"s_cbranch_vccz Lg31_%=\n" \
"s_nop 0\n" \
"buffer_store_dwordx4 v[80:83], v209, %[srdz], s60 offen offset:2048 sc1\n" \
"Lg31_%=:\n" \
"v_mfma_f32_16x16x32_bf16 v[44:47], a[200:203], a[24:27], v[44:47]\n" \
"ds_read_b64_tr_b16 a[228:229], v227 offset:53760\n" \
"v_mfma_f32_16x16x32_bf16 v[44:47], a[204:207], a[28:31], v[44:47]\n" \
"ds_read_b64_tr_b16 a[230:231], v227 offset:55808\n" \
"v_mfma_f32_16x16x32_bf16 v[76:79], a[200:203], a[40:43], v[76:79]\n" \
"ds_read_b64_tr_b16 a[220:221], v227 offset:61952\n" \
"buffer_load_dwordx4 a[84:87], v208, %[srdz], s50 offen offset:1024 sc1\n" \
"v_mfma_f32_16x16x32_bf16 v[76:79], a[204:207], a[44:47], v[76:79]\n" \
"ds_read_b64_tr_b16 a[222:223], v227 offset:64000\n" \
"v_mfma_f32_16x16x32_bf16 v[108:111], a[200:203], a[56:59], v[108:111]\n" \
"v_mfma_f32_16x16x32_bf16 v[108:111], a[204:207], a[60:63], v[108:111]\n" \
"s_waitcnt vmcnt(63) lgkmcnt(12)\n" \
"s_nop 0\n" \
"v_mfma_f32_16x16x32_bf16 v[20:23], a[224:227], a[0:3], v[20:23]\n" \
"v_mfma_f32_16x16x32_bf16 v[20:23], a[232:235], a[4:7], v[20:23]\n" \
"v_mfma_f32_16x16x32_bf16 v[52:55], a[224:227], a[16:19], v[52:55]\n" \
"s_cbranch_vccz Lg32_%=\n" \
"s_nop 0\n" \
"buffer_store_dwordx4 v[88:91], v209, %[srdz], s60 offen offset:3072 sc1\n" \
"Lg32_%=:\n" \
"v_mfma_f32_16x16x32_bf16 v[52:55], a[232:235], a[20:23], v[52:55]\n" \
"v_mfma_f32_16x16x32_bf16 v[84:87], a[224:227], a[32:35], v[84:87]\n" \
"s_waitcnt vmcnt(32) lgkmcnt(0)\n" \
"s_nop 0\n" \
"s_bitcmp1_b32 0xc0, s48\n" \
"s_cbranch_scc1 Lcw12_%=\n" \
"s_waitcnt vmcnt(20)\n" \
"Lcw12_%=:\n" \
"s_barrier\n" \
"s_nop 0\n" \
"v_mfma_f32_16x16x32_bf16 v[84:87], a[232:235], a[36:39], v[84:87]\n" \
"ds_read_b64_tr_b16 a[196:197], v215 offset:0\n" \
"buffer_load_dwordx4 a[88:91], v208, %[srdz], s50 offen offset:2048 sc1\n" \
"v_mfma_f32_16x16x32_bf16 v[116:119], a[224:227], a[48:51], v[116:119]\n" \
"ds_read_b64_tr_b16 a[198:199], v215 offset:2048\n" \
"v_mfma_f32_16x16x32_bf16 v[116:119], a[232:235], a[52:55], v[116:119]\n" \
"ds_read_b64_tr_b16 a[200:201], v215 offset:4096\n" \
"buffer_load_dwordx4 v212, %[srdt], s49 offen offset:1024 lds\n" \
"v_mfma_f32_16x16x32_bf16 v[20:23], a[236:239], a[8:11], v[20:23]\n" \
"ds_read_b64_tr_b16 a[202:203], v215 offset:6144\n" \
"v_mfma_f32_16x16x32_bf16 v[20:23], a[208:211], a[12:15], v[20:23]\n" \
"ds_read_b64_tr_b16 a[204:205], v215 offset:8192\n" \
"v_mfma_f32_16x16x32_bf16 v[52:55], a[236:239], a[24:27], v[52:55]\n" \
"ds_read_b64_tr_b16 a[206:207], v215 offset:10240\n" \
"v_mfma_f32_16x16x32_bf16 v[52:55], a[208:211], a[28:31], v[52:55]\n" \
"ds_read_b64_tr_b16 a[224:225], v215 offset:12288\n" \
"s_cbranch_vccz Lg33_%=\n" \
"s_nop 0\n" \
"buffer_store_dwordx4 v[96:99], v210, %[srdz], s60 offen offset:0 sc1\n" \
"Lg33_%=:\n" \
"v_mfma_f32_16x16x32_bf16 v[84:87], a[236:239], a[40:43], v[84:87]\n" \
"ds_read_b64_tr_b16 a[226:227], v215 offset:14336\n" \
"v_mfma_f32_16x16x32_bf16 v[84:87], a[208:211], a[44:47], v[84:87]\n" \
"buffer_load_dwordx4 a[92:95], v208, %[srdz], s50 offen offset:3072 sc1\n" \
"v_mfma_f32_16x16x32_bf16 v[116:119], a[236:239], a[56:59], v[116:119]\n" \
"v_mfma_f32_16x16x32_bf16 v[116:119], a[208:211], a[60:63], v[116:119]\n" \
"s_waitcnt vmcnt(25) lgkmcnt(4)\n" \
"s_nop 0\n" \
"s_bitcmp1_b32 0xc0, s48\n" \
"s_cbranch_scc1 Lcw13_%=\n" \
"s_waitcnt vmcnt(16)\n" \
"Lcw13_%=:\n" \
"v_mfma_f32_16x16x32_bf16 v[28:31], a[212:215], a[0:3], v[28:31]\n" \
"buffer_load_dwordx4 a[96:99], v209, %[srdz], s50 offen offset:0 sc1\n" \
"v_mfma_f32_16x16x32_bf16 v[28:31], a[228:231], a[4:7], v[28:31]\n" \
"v_mfma_f32_16x16x32_bf16 v[60:63], a[212:215], a[16:19], v[60:63]\n" \
"buffer_load_dwordx4 v213, %[srdt], s49 offen offset:2048 lds\n" \
"v_mfma_f32_16x16x32_bf16 v[60:63], a[228:231], a[20:23], v[60:63]\n" \
"v_mfma_f32_16x16x32_bf16 v[92:95], a[212:215], a[32:35], v[92:95]\n" \
"s_cbranch_vccz Lg34_%=\n" \
"s_nop 0\n" \
"buffer_store_dwordx4 v[104:107], v210, %[srdz], s60 offen offset:1024 sc1\n" \
"Lg34_%=:\n" \
"v_mfma_f32_16x16x32_bf16 v[92:95], a[228:231], a[36:39], v[92:95]\n" \
"v_mfma_f32_16x16x32_bf16 v[124:127], a[212:215], a[48:51], v[124:127]\n" \
"ds_read_b64_tr_b16 a[232:233], v227 offset:0\n" \
"v_mfma_f32_16x16x32_bf16 v[124:127], a[228:231], a[52:55], v[124:127]\n" \
"ds_read_b64_tr_b16 a[234:235], v227 offset:2048\n" \
"v_mfma_f32_16x16x32_bf16 v[128:131], a[196:199], a[128:131], v[192:195]\n" \
"ds_read_b64_tr_b16 a[236:237], v227 offset:4096\n" \
"v_mfma_f32_16x16x32_bf16 v[128:131], a[200:203], a[132:135], v[128:131]\n" \
"ds_read_b64_tr_b16 a[238:239], v227 offset:6144\n" \
"buffer_load_dwordx4 a[100:103], v209, %[srdz], s50 offen offset:1024 sc1\n" \
"v_mfma_f32_16x16x32_bf16 v[132:135], a[196:199], a[144:147], v[196:199]\n" \
"v_mfma_f32_16x16x32_bf16 v[132:135], a[200:203], a[148:151], v[132:135]\n" \
"v_mfma_f32_16x16x32_bf16 v[136:139], a[196:199], a[160:163], v[200:203]\n" \
"v_mfma_f32_16x16x32_bf16 v[136:139], a[200:203], a[164:167], v[136:139]\n" \
"s_cbranch_vccz Lg35_%=\n" \
"s_nop 0\n" \
"buffer_store_dwordx4 v[112:115], v210, %[srdz], s60 offen offset:2048 sc1\n" \
"Lg35_%=:\n" \
"v_mfma_f32_16x16x32_bf16 v[140:143], a[196:199], a[176:179], v[204:207]\n" \
"ds_read_b64_tr_b16 a[208:209], v227 offset:8192\n" \
"v_mfma_f32_16x16x32_bf16 v[140:143], a[200:203], a[180:183], v[140:143]\n" \
"s_waitcnt vmcnt(26) lgkmcnt(5)\n" \
"s_nop 0\n" \
"s_bitcmp1_b32 0xc0, s48\n" \
"s_cbranch_scc1 Lcw14_%=\n" \
"s_waitcnt vmcnt(16)\n" \
"Lcw14_%=:\n" \
"v_mfma_f32_16x16x32_bf16 v[128:131], a[204:207], a[136:139], v[128:131]\n" \
"ds_read_b64_tr_b16 a[210:211], v227 offset:10240\n" \
"buffer_load_dwordx4 a[104:107], v209, %[srdz], s50 offen offset:2048 sc1\n" \
"v_mfma_f32_16x16x32_bf16 v[128:131], a[224:227], a[140:143], v[128:131]\n" \
"ds_read_b64_tr_b16 a[212:213], v227 offset:12288\n" \
"v_mfma_f32_16x16x32_bf16 v[132:135], a[204:207], a[152:155], v[132:135]\n" \
"ds_read_b64_tr_b16 a[214:215], v227 offset:14336\n" \
"v_mfma_f32_16x16x32_bf16 v[132:135], a[224:227], a[156:159], v[132:135]\n" \
"buffer_load_dwordx4 v214, %[srdt], s49 offen offset:3072 lds\n" \
"v_mfma_f32_16x16x32_bf16 v[136:139], a[204:207], a[168:171], v[136:139]\n" \
"v_mfma_f32_16x16x32_bf16 v[136:139], a[224:227], a[172:175], v[136:139]\n" \
"v_mfma_f32_16x16x32_bf16 v[140:143], a[204:207], a[184:187], v[140:143]\n" \
"ds_read_b64_tr_b16 a[228:229], v215 offset:512\n" \
"s_cbranch_vccz Lg36_%=\n" \
"s_nop 0\n" \
"buffer_store_dwordx4 v[120:123], v210, %[srdz], s60 offen offset:3072 sc1\n" \
"Lg36_%=:\n" \
"v_mfma_f32_16x16x32_bf16 v[140:143], a[224:227], a[188:191], v[140:143]\n" \
"ds_read_b64_tr_b16 a[230:231], v215 offset:2560\n" \
"v_mfma_f32_16x16x32_bf16 v[28:31], a[216:219], a[8:11], v[28:31]\n" \
"buffer_load_dwordx4 a[108:111], v209, %[srdz], s50 offen offset:3072 sc1\n" \
"v_mfma_f32_16x16x32_bf16 v[28:31], a[220:223], a[12:15], v[28:31]\n" \
"v_mfma_f32_16x16x32_bf16 v[60:63], a[216:219], a[24:27], v[60:63]\n" \
"v_mfma_f32_16x16x32_bf16 v[60:63], a[220:223], a[28:31], v[60:63]\n" \
"v_mfma_f32_16x16x32_bf16 v[92:95], a[216:219], a[40:43], v[92:95]\n" \
"buffer_load_dwordx4 a[112:115], v210, %[srdz], s50 offen offset:0 sc1\n" \
"v_mfma_f32_16x16x32_bf16 v[92:95], a[220:223], a[44:47], v[92:95]\n" \
"v_cmp_u_f32_e64 s[44:45], v128, v132\n" \
"v_mfma_f32_16x16x32_bf16 v[124:127], a[216:219], a[56:59], v[124:127]\n" \
"v_mfma_f32_16x16x32_bf16 v[124:127], a[220:223], a[60:63], v[124:127]\n" \
"v_cmp_u_f32_e64 s[56:57], v136, v140\n" \
"s_or_b64 s[56:57], s[56:57], s[44:45]\n" \
"s_cbranch_scc1 Lsp3_%=\n" \
"Lbk3_%=:\n" \
"s_waitcnt vmcnt(63) lgkmcnt(2)\n" \
"s_nop 0\n" \
"v_mfma_f32_16x16x32_bf16 v[144:147], a[232:235], a[128:131], v[144:147]\n" \
"ds_read_b64_tr_b16 a[216:217], v215 offset:4608\n" \
"v_mfma_f32_16x16x32_bf16 v[144:147], a[236:239], a[132:135], v[144:147]\n" \
"ds_read_b64_tr_b16 a[218:219], v215 offset:6656\n" \
"v_mfma_f32_16x16x32_bf16 v[148:151], a[232:235], a[144:147], v[148:151]\n" \
"ds_read_b64_tr_b16 a[196:197], v215 offset:8704\n" \
"v_mfma_f32_16x16x32_bf16 v[148:151], a[236:239], a[148:151], v[148:151]\n" \
"ds_read_b64_tr_b16 a[198:199], v215 offset:10752\n" \
"v_mfma_f32_16x16x32_bf16 v[152:155], a[232:235], a[160:163], v[152:155]\n" \
"ds_read_b64_tr_b16 a[200:201], v215 offset:12800\n" \
"v_mfma_f32_16x16x32_bf16 v[152:155], a[236:239], a[164:167], v[152:155]\n" \
"ds_read_b64_tr_b16 a[202:203], v215 offset:14848\n" \
"v_mfma_f32_16x16x32_bf16 v[156:159], a[232:235], a[176:179], v[156:159]\n" \
"ds_read_b64_tr_b16 a[204:205], v227 offset:512\n" \
"buffer_load_dwordx4 a[116:119], v210, %[srdz], s50 offen offset:1024 sc1\n" \
"v_mfma_f32_16x16x32_bf16 v[156:159], a[236:239], a[180:183], v[156:159]\n" \
"ds_read_b64_tr_b16 a[206:207], v227 offset:2560\n" \
"v_mfma_f32_16x16x32_bf16 v[144:147], a[208:211], a[136:139], v[144:147]\n" \
"ds_read_b64_tr_b16 a[220:221], v227 offset:4608\n" \
"v_mfma_f32_16x16x32_bf16 v[144:147], a[212:215], a[140:143], v[144:147]\n" \
"ds_read_b64_tr_b16 a[222:223], v227 offset:6656\n" \
"v_mfma_f32_16x16x32_bf16 v[148:151], a[208:211], a[152:155], v[148:151]\n" \
"s_add_u32 m0, %[m0b], 98304\n" \
"s_cmp_gt_u32 s48, 4\n" \
"s_cselect_b32 s49, 4, 5\n" \
"s_lshl_b32 s49, s49, 18\n" \
"s_or_b32 s49, s49, s58\n" \
"buffer_load_dwordx4 %[wo0], %[srdt], s49 offen offset:0 lds\n" \
"v_mfma_f32_16x16x32_bf16 v[148:151], a[212:215], a[156:159], v[148:151]\n" \
"v_mfma_f32_16x16x32_bf16 v[152:155], a[208:211], a[168:171], v[152:155]\n" \
"buffer_load_dwordx4 a[120:123], v210, %[srdz], s50 offen offset:2048 sc1\n" \
"v_mfma_f32_16x16x32_bf16 v[152:155], a[212:215], a[172:175], v[152:155]\n" \
"v_mfma_f32_16x16x32_bf16 v[156:159], a[208:211], a[184:187], v[156:159]\n" \
"ds_read_b64_tr_b16 a[224:225], v227 offset:8704\n" \
"v_mfma_f32_16x16x32_bf16 v[156:159], a[212:215], a[188:191], v[156:159]\n" \
"s_waitcnt vmcnt(63) lgkmcnt(5)\n" \
"s_nop 0\n" \
"v_mfma_f32_16x16x32_bf16 v[160:163], a[228:231], a[128:131], v[160:163]\n" \
"ds_read_b64_tr_b16 a[226:227], v227 offset:10752\n" \
"v_mfma_f32_16x16x32_bf16 v[160:163], a[216:219], a[132:135], v[160:163]\n" \
"ds_read_b64_tr_b16 a[232:233], v227 offset:12800\n" \
"v_mfma_f32_16x16x32_bf16 v[164:167], a[228:231], a[144:147], v[164:167]\n" \
"ds_read_b64_tr_b16 a[234:235], v227 offset:14848\n" \
"v_mfma_f32_16x16x32_bf16 v[164:167], a[216:219], a[148:151], v[164:167]\n" \
"buffer_load_dwordx4 a[124:127], v210, %[srdz], s50 offen offset:3072 sc1\n" \
"v_mfma_f32_16x16x32_bf16 v[168:171], a[228:231], a[160:163], v[168:171]\n" \
"v_mfma_f32_16x16x32_bf16 v[168:171], a[216:219], a[164:167], v[168:171]\n" \
"v_mfma_f32_16x16x32_bf16 v[172:175], a[228:231], a[176:179], v[172:175]\n" \
"v_mfma_f32_16x16x32_bf16 v[172:175], a[216:219], a[180:183], v[172:175]\n" \
"v_mfma_f32_16x16x32_bf16 v[160:163], a[196:199], a[136:139], v[160:163]\n" \
"v_mfma_f32_16x16x32_bf16 v[160:163], a[200:203], a[140:143], v[160:163]\n" \
"buffer_load_dwordx4 v212, %[srdt], s49 offen offset:1024 lds\n" \
"v_mfma_f32_16x16x32_bf16 v[164:167], a[196:199], a[152:155], v[164:167]\n" \
"v_mfma_f32_16x16x32_bf16 v[164:167], a[200:203], a[156:159], v[164:167]\n" \
"v_mfma_f32_16x16x32_bf16 v[168:171], a[196:199], a[168:171], v[168:171]\n" \
"s_waitcnt vmcnt(30) lgkmcnt(0)\n" \
"s_nop 0\n" \
"s_bitcmp1_b32 0xc0, s48\n" \
"s_cbranch_scc1 Lcw15_%=\n" \
"s_waitcnt vmcnt(21)\n" \
"Lcw15_%=:\n" \
"s_barrier\n" \
"s_nop 0\n" \
"v_mfma_f32_16x16x32_bf16 v[168:171], a[200:203], a[172:175], v[168:171]\n" \
"ds_read_b64_tr_b16 a[236:237], v215 offset:16384\n" \
"v_mfma_f32_16x16x32_bf16 v[172:175], a[196:199], a[184:187], v[172:175]\n" \
"ds_read_b64_tr_b16 a[238:239], v215 offset:18432\n" \
"v_mfma_f32_16x16x32_bf16 v[172:175], a[200:203], a[188:191], v[172:175]\n" \
"s_waitcnt vmcnt(63) lgkmcnt(2)\n" \
"s_nop 0\n" \
"v_mfma_f32_16x16x32_bf16 v[176:179], a[204:207], a[128:131], v[176:179]\n" \
"ds_read_b64_tr_b16 a[208:209], v215 offset:20480\n" \
"s_cmp_gt_u32 s48, 4\n" \
"s_cselect_b32 s50, 4, 5\n" \
"s_mul_i32 s50, s50, 152064\n" \
"buffer_load_dwordx4 v[0:3], %[oq0], %[srdz], s50 offen offset:0 sc1\n" \
"v_mfma_f32_16x16x32_bf16 v[176:179], a[220:223], a[132:135], v[176:179]\n" \
"ds_read_b64_tr_b16 a[210:211], v215 offset:22528\n" \
"v_mfma_f32_16x16x32_bf16 v[180:183], a[204:207], a[144:147], v[180:183]\n" \
"ds_read_b64_tr_b16 a[212:213], v215 offset:24576\n" \
"v_mfma_f32_16x16x32_bf16 v[180:183], a[220:223], a[148:151], v[180:183]\n" \
"ds_read_b64_tr_b16 a[214:215], v215 offset:26624\n" \
"v_mfma_f32_16x16x32_bf16 v[184:187], a[204:207], a[160:163], v[184:187]\n" \
"ds_read_b64_tr_b16 a[228:229], v215 offset:28672\n" \
"buffer_load_dwordx4 v[8:11], %[oq0], %[srdz], s50 offen offset:1024 sc1\n" \
"v_mfma_f32_16x16x32_bf16 v[184:187], a[220:223], a[164:167], v[184:187]\n" \
"ds_read_b64_tr_b16 a[230:231], v215 offset:30720\n" \
"v_mfma_f32_16x16x32_bf16 v[188:191], a[204:207], a[176:179], v[188:191]\n" \
"ds_read_b64_tr_b16 a[216:217], v227 offset:16384\n" \
"v_mfma_f32_16x16x32_bf16 v[188:191], a[220:223], a[180:183], v[188:191]\n" \
"ds_read_b64_tr_b16 a[218:219], v227 offset:18432\n" \
"v_mfma_f32_16x16x32_bf16 v[176:179], a[224:227], a[136:139], v[176:179]\n" \
"ds_read_b64_tr_b16 a[196:197], v227 offset:20480\n" \
"buffer_load_dwordx4 v[16:19], %[oq0], %[srdz], s50 offen offset:2048 sc1\n" \
"v_mfma_f32_16x16x32_bf16 v[176:179], a[232:235], a[140:143], v[176:179]\n" \
"ds_read_b64_tr_b16 a[198:199], v227 offset:22528\n" \
"v_mfma_f32_16x16x32_bf16 v[180:183], a[224:227], a[152:155], v[180:183]\n" \
"buffer_load_dwordx4 v213, %[srdt], s49 offen offset:2048 lds\n" \
"v_mfma_f32_16x16x32_bf16 v[180:183], a[232:235], a[156:159], v[180:183]\n" \
"v_mfma_f32_16x16x32_bf16 v[184:187], a[224:227], a[168:171], v[184:187]\n" \
"buffer_load_dwordx4 v[24:27], %[oq0], %[srdz], s50 offen offset:3072 sc1\n" \
"v_mfma_f32_16x16x32_bf16 v[184:187], a[232:235], a[172:175], v[184:187]\n" \
"v_mfma_f32_16x16x32_bf16 v[188:191], a[224:227], a[184:187], v[188:191]\n" \
"ds_read_b64_tr_b16 a[200:201], v227 offset:24576\n" \
"v_mfma_f32_16x16x32_bf16 v[188:191], a[232:235], a[188:191], v[188:191]\n" \
"s_waitcnt vmcnt(63) lgkmcnt(5)\n" \
"s_nop 0\n" \
"v_mfma_f32_16x16x32_bf16 v[4:7], a[236:239], a[128:131], v[4:7]\n" \
"ds_read_b64_tr_b16 a[202:203], v227 offset:26624\n" \
"buffer_load_dwordx4 v[32:35], v208, %[srdz], s50 offen offset:0 sc1\n" \
"v_mfma_f32_16x16x32_bf16 v[4:7], a[208:211], a[132:135], v[4:7]\n" \
"ds_read_b64_tr_b16 a[204:205], v227 offset:28672\n" \
"v_mfma_f32_16x16x32_bf16 v[36:39], a[236:239], a[144:147], v[36:39]\n" \
"ds_read_b64_tr_b16 a[206:207], v227 offset:30720\n" \
"v_mfma_f32_16x16x32_bf16 v[36:39], a[208:211], a[148:151], v[36:39]\n" \
"buffer_load_dwordx4 v[40:43], v208, %[srdz], s50 offen offset:1024 sc1\n" \
"v_mfma_f32_16x16x32_bf16 v[68:71], a[236:239], a[160:163], v[68:71]\n" \
"v_mfma_f32_16x16x32_bf16 v[68:71], a[208:211], a[164:167], v[68:71]\n" \
"buffer_load_dwordx4 v[48:51], v208, %[srdz], s50 offen offset:2048 sc1\n" \
"v_mfma_f32_16x16x32_bf16 v[100:103], a[236:239], a[176:179], v[100:103]\n" \
"ds_read_b64_tr_b16 a[220:221], v215 offset:16896\n" \
"v_mfma_f32_16x16x32_bf16 v[100:103], a[208:211], a[180:183], v[100:103]\n" \
"ds_read_b64_tr_b16 a[222:223], v215 offset:18944\n" \
"v_mfma_f32_16x16x32_bf16 v[4:7], a[212:215], a[136:139], v[4:7]\n" \
"ds_read_b64_tr_b16 a[224:225], v215 offset:20992\n" \
"v_mfma_f32_16x16x32_bf16 v[4:7], a[228:231], a[140:143], v[4:7]\n" \
"ds_read_b64_tr_b16 a[226:227], v215 offset:23040\n" \
"v_mfma_f32_16x16x32_bf16 v[36:39], a[212:215], a[152:155], v[36:39]\n" \
"buffer_load_dwordx4 v214, %[srdt], s49 offen offset:3072 lds\n" \
"v_mfma_f32_16x16x32_bf16 v[36:39], a[228:231], a[156:159], v[36:39]\n" \
"v_mfma_f32_16x16x32_bf16 v[68:71], a[212:215], a[168:171], v[68:71]\n" \
"buffer_load_dwordx4 v[56:59], v208, %[srdz], s50 offen offset:3072 sc1\n" \
"v_mfma_f32_16x16x32_bf16 v[68:71], a[228:231], a[172:175], v[68:71]\n" \
"v_mfma_f32_16x16x32_bf16 v[100:103], a[212:215], a[184:187], v[100:103]\n" \
"ds_read_b64_tr_b16 a[232:233], v215 offset:25088\n" \
"v_mfma_f32_16x16x32_bf16 v[100:103], a[228:231], a[188:191], v[100:103]\n" \
"s_waitcnt vmcnt(63) lgkmcnt(5)\n" \
"s_nop 0\n" \
"v_mfma_f32_16x16x32_bf16 v[12:15], a[216:219], a[128:131], v[12:15]\n" \
"ds_read_b64_tr_b16 a[234:235], v215 offset:27136\n" \
"buffer_load_dwordx4 v[64:67], v209, %[srdz], s50 offen offset:0 sc1\n" \
"v_mfma_f32_16x16x32_bf16 v[12:15], a[196:199], a[132:135], v[12:15]\n" \
"ds_read_b64_tr_b16 a[236:237], v215 offset:29184\n" \
".if . - adaln_body != 49152\n" \
".error \"size model\"\n" \
".endif\n" \
"Lk11_%=:\n" \
"s_cbranch_execz adaln_kr\n" \
"s_nop 0\n" \
"v_mfma_f32_16x16x32_bf16 v[44:47], a[216:219], a[144:147], v[44:47]\n" \
"ds_read_b64_tr_b16 a[238:239], v215 offset:31232\n" \
"v_mfma_f32_16x16x32_bf16 v[44:47], a[196:199], a[148:151], v[44:47]\n" \
"buffer_load_dwordx4 v[72:75], v209, %[srdz], s50 offen offset:1024 sc1\n" \
"v_mfma_f32_16x16x32_bf16 v[76:79], a[216:219], a[160:163], v[76:79]\n" \
"v_mfma_f32_16x16x32_bf16 v[76:79], a[196:199], a[164:167], v[76:79]\n" \
"buffer_load_dwordx4 v[80:83], v209, %[srdz], s50 offen offset:2048 sc1\n" \
"v_mfma_f32_16x16x32_bf16 v[108:111], a[216:219], a[176:179], v[108:111]\n" \
"ds_read_b64_tr_b16 a[208:209], v227 offset:16896\n" \
"v_mfma_f32_16x16x32_bf16 v[108:111], a[196:199], a[180:183], v[108:111]\n" \
"ds_read_b64_tr_b16 a[210:211], v227 offset:18944\n" \
"v_mfma_f32_16x16x32_bf16 v[12:15], a[200:203], a[136:139], v[12:15]\n" \
"ds_read_b64_tr_b16 a[212:213], v227 offset:25088\n" \
"v_mfma_f32_16x16x32_bf16 v[12:15], a[204:207], a[140:143], v[12:15]\n" \
"ds_read_b64_tr_b16 a[214:215], v227 offset:27136\n" \
"v_mfma_f32_16x16x32_bf16 v[44:47], a[200:203], a[152:155], v[44:47]\n" \
"ds_read_b64_tr_b16 a[228:229], v227 offset:20992\n" \
"v_mfma_f32_16x16x32_bf16 v[44:47], a[204:207], a[156:159], v[44:47]\n" \
"ds_read_b64_tr_b16 a[230:231], v227 offset:23040\n" \
"buffer_load_dwordx4 v[88:91], v209, %[srdz], s50 offen offset:3072 sc1\n" \
"v_mfma_f32_16x16x32_bf16 v[76:79], a[200:203], a[168:171], v[76:79]\n" \
"ds_read_b64_tr_b16 a[216:217], v227 offset:29184\n" \
"v_mfma_f32_16x16x32_bf16 v[76:79], a[204:207], a[172:175], v[76:79]\n" \
"ds_read_b64_tr_b16 a[218:219], v227 offset:31232\n" \
"v_mfma_f32_16x16x32_bf16 v[108:111], a[200:203], a[184:187], v[108:111]\n" \
"buffer_load_dwordx4 v[96:99], v210, %[srdz], s50 offen offset:0 sc1\n" \
"v_mfma_f32_16x16x32_bf16 v[108:111], a[204:207], a[188:191], v[108:111]\n" \
"s_waitcnt vmcnt(63) lgkmcnt(12)\n" \
"s_nop 0\n" \
"v_mfma_f32_16x16x32_bf16 v[20:23], a[220:223], a[128:131], v[20:23]\n" \
"s_add_u32 m0, %[m0b], 114688\n" \
"s_cmp_gt_u32 s48, 5\n" \
"s_cselect_b32 s49, 5, 6\n" \
"s_lshl_b32 s49, s49, 18\n" \
"s_or_b32 s49, s49, s58\n" \
"buffer_load_dwordx4 %[wo0], %[srdt], s49 offen offset:0 lds\n" \
"v_mfma_f32_16x16x32_bf16 v[20:23], a[224:227], a[132:135], v[20:23]\n" \
"v_mfma_f32_16x16x32_bf16 v[52:55], a[220:223], a[144:147], v[52:55]\n" \
"buffer_load_dwordx4 v[104:107], v210, %[srdz], s50 offen offset:1024 sc1\n" \
"v_mfma_f32_16x16x32_bf16 v[52:55], a[224:227], a[148:151], v[52:55]\n" \
"v_mfma_f32_16x16x32_bf16 v[84:87], a[220:223], a[160:163], v[84:87]\n" \
"s_waitcnt vmcnt(24) lgkmcnt(0)\n" \
"s_barrier\n" \
"v_mfma_f32_16x16x32_bf16 v[84:87], a[224:227], a[164:167], v[84:87]\n" \
"ds_read_b64_tr_b16 a[196:197], v215 offset:32768\n" \
"v_mfma_f32_16x16x32_bf16 v[116:119], a[220:223], a[176:179], v[116:119]\n" \
"ds_read_b64_tr_b16 a[198:199], v215 offset:34816\n" \
"v_mfma_f32_16x16x32_bf16 v[116:119], a[224:227], a[180:183], v[116:119]\n" \
"ds_read_b64_tr_b16 a[200:201], v215 offset:36864\n" \
"buffer_load_dwordx4 v[112:115], v210, %[srdz], s50 offen offset:2048 sc1\n" \
"v_mfma_f32_16x16x32_bf16 v[20:23], a[232:235], a[136:139], v[20:23]\n" \
"ds_read_b64_tr_b16 a[202:203], v215 offset:38912\n" \
"v_mfma_f32_16x16x32_bf16 v[20:23], a[236:239], a[140:143], v[20:23]\n" \
"ds_read_b64_tr_b16 a[204:205], v215 offset:40960\n" \
"v_mfma_f32_16x16x32_bf16 v[52:55], a[232:235], a[152:155], v[52:55]\n" \
"ds_read_b64_tr_b16 a[206:207], v215 offset:43008\n" \
"v_mfma_f32_16x16x32_bf16 v[52:55], a[236:239], a[156:159], v[52:55]\n" \
"ds_read_b64_tr_b16 a[220:221], v215 offset:45056\n" \
"buffer_load_dwordx4 v[120:123], v210, %[srdz], s50 offen offset:3072 sc1\n" \
"v_mfma_f32_16x16x32_bf16 v[84:87], a[232:235], a[168:171], v[84:87]\n" \
"ds_read_b64_tr_b16 a[222:223], v215 offset:47104\n" \
"v_readlane_b32 s54, %[thi], 16\n" \
"v_mfma_f32_16x16x32_bf16 v[84:87], a[236:239], a[172:175], v[84:87]\n" \
"v_mfma_f32_16x16x32_bf16 v[116:119], a[232:235], a[184:187], v[116:119]\n" \
"v_mfma_f32_16x16x32_bf16 v[116:119], a[236:239], a[188:191], v[116:119]\n" \
"s_waitcnt vmcnt(23) lgkmcnt(4)\n" \
"s_nop 0\n" \
"v_mfma_f32_16x16x32_bf16 v[28:31], a[208:211], a[128:131], v[28:31]\n" \
"buffer_load_dwordx4 v212, %[srdt], s49 offen offset:1024 lds\n" \
"v_mfma_f32_16x16x32_bf16 v[28:31], a[228:231], a[132:135], v[28:31]\n" \
"v_mfma_f32_16x16x32_bf16 v[60:63], a[208:211], a[144:147], v[60:63]\n" \
"s_cmp_gt_u32 s48, 5\n" \
"s_cselect_b32 s50, 5, 6\n" \
"s_mul_i32 s50, s50, 152064\n" \
"buffer_load_dwordx4 a[0:3], %[oq0], %[srdz], s50 offen offset:0 sc1\n" \
"v_mfma_f32_16x16x32_bf16 v[60:63], a[228:231], a[148:151], v[60:63]\n" \
"v_mfma_f32_16x16x32_bf16 v[92:95], a[208:211], a[160:163], v[92:95]\n" \
"buffer_load_dword v232, v226, %[srdz], s54 offen sc1\n" \
"v_mfma_f32_16x16x32_bf16 v[92:95], a[228:231], a[164:167], v[92:95]\n" \
"v_mfma_f32_16x16x32_bf16 v[124:127], a[208:211], a[176:179], v[124:127]\n" \
"ds_read_b64_tr_b16 a[224:225], v227 offset:32768\n" \
"buffer_load_dwordx4 a[4:7], %[oq0], %[srdz], s50 offen offset:1024 sc1\n" \
"v_mfma_f32_16x16x32_bf16 v[124:127], a[228:231], a[180:183], v[124:127]\n" \
"ds_read_b64_tr_b16 a[226:227], v227 offset:34816\n" \
"v_mfma_f32_16x16x32_bf16 v[192:195], a[196:199], a[64:67], v[128:131]\n" \
"ds_read_b64_tr_b16 a[232:233], v227 offset:36864\n" \
"v_mfma_f32_16x16x32_bf16 v[192:195], a[200:203], a[68:71], v[192:195]\n" \
"ds_read_b64_tr_b16 a[234:235], v227 offset:38912\n" \
"v_mfma_f32_16x16x32_bf16 v[196:199], a[196:199], a[80:83], v[132:135]\n" \
"buffer_load_dwordx4 a[8:11], %[oq0], %[srdz], s50 offen offset:2048 sc1\n" \
"v_mfma_f32_16x16x32_bf16 v[196:199], a[200:203], a[84:87], v[196:199]\n" \
"v_mfma_f32_16x16x32_bf16 v[200:203], a[196:199], a[96:99], v[136:139]\n" \
"buffer_load_dwordx4 a[12:15], %[oq0], %[srdz], s50 offen offset:3072 sc1\n" \
"v_mfma_f32_16x16x32_bf16 v[200:203], a[200:203], a[100:103], v[200:203]\n" \
"v_mfma_f32_16x16x32_bf16 v[204:207], a[196:199], a[112:115], v[140:143]\n" \
"ds_read_b64_tr_b16 a[236:237], v227 offset:40960\n" \
"v_mfma_f32_16x16x32_bf16 v[204:207], a[200:203], a[116:119], v[204:207]\n" \
"s_waitcnt vmcnt(26) lgkmcnt(5)\n" \
"s_nop 0\n" \
"v_mfma_f32_16x16x32_bf16 v[192:195], a[204:207], a[72:75], v[192:195]\n" \
"ds_read_b64_tr_b16 a[238:239], v227 offset:43008\n" \
"v_mfma_f32_16x16x32_bf16 v[192:195], a[220:223], a[76:79], v[192:195]\n" \
"ds_read_b64_tr_b16 a[208:209], v227 offset:45056\n" \
"buffer_load_dwordx4 v213, %[srdt], s49 offen offset:2048 lds\n" \
"v_mfma_f32_16x16x32_bf16 v[196:199], a[204:207], a[88:91], v[196:199]\n" \
"ds_read_b64_tr_b16 a[210:211], v227 offset:47104\n" \
"v_mfma_f32_16x16x32_bf16 v[196:199], a[220:223], a[92:95], v[196:199]\n" \
"buffer_load_dwordx4 a[16:19], v208, %[srdz], s50 offen offset:0 sc1\n" \
"v_mfma_f32_16x16x32_bf16 v[200:203], a[204:207], a[104:107], v[200:203]\n" \
"v_mfma_f32_16x16x32_bf16 v[200:203], a[220:223], a[108:111], v[200:203]\n" \
"buffer_load_dwordx4 a[20:23], v208, %[srdz], s50 offen offset:1024 sc1\n" \
"v_mfma_f32_16x16x32_bf16 v[204:207], a[204:207], a[120:123], v[204:207]\n" \
"ds_read_b64_tr_b16 a[228:229], v215 offset:33280\n" \
"v_mfma_f32_16x16x32_bf16 v[204:207], a[220:223], a[124:127], v[204:207]\n" \
"ds_read_b64_tr_b16 a[230:231], v215 offset:35328\n" \
"v_mfma_f32_16x16x32_bf16 v[28:31], a[212:215], a[136:139], v[28:31]\n" \
"buffer_load_dwordx4 a[24:27], v208, %[srdz], s50 offen offset:2048 sc1\n" \
"v_mfma_f32_16x16x32_bf16 v[28:31], a[216:219], a[140:143], v[28:31]\n" \
"v_mfma_f32_16x16x32_bf16 v[60:63], a[212:215], a[152:155], v[60:63]\n" \
"buffer_load_dwordx4 a[28:31], v208, %[srdz], s50 offen offset:3072 sc1\n" \
"v_mfma_f32_16x16x32_bf16 v[60:63], a[216:219], a[156:159], v[60:63]\n" \
"v_mfma_f32_16x16x32_bf16 v[92:95], a[212:215], a[168:171], v[92:95]\n" \
"v_mfma_f32_16x16x32_bf16 v[92:95], a[216:219], a[172:175], v[92:95]\n" \
"v_cmp_u_f32_e64 s[44:45], v192, v196\n" \
"buffer_load_dwordx4 v214, %[srdt], s49 offen offset:3072 lds\n" \
"v_mfma_f32_16x16x32_bf16 v[124:127], a[212:215], a[184:187], v[124:127]\n" \
"v_mfma_f32_16x16x32_bf16 v[124:127], a[216:219], a[188:191], v[124:127]\n" \
"v_cmp_u_f32_e64 s[56:57], v200, v204\n" \
"s_or_b64 s[56:57], s[56:57], s[44:45]\n" \
"s_cbranch_scc1 Lsp4_%=\n" \
"Lbk4_%=:\n" \
"s_waitcnt vmcnt(63) lgkmcnt(2)\n" \
"s_nop 0\n" \
"v_mfma_f32_16x16x32_bf16 v[144:147], a[224:227], a[64:67], v[144:147]\n" \
"ds_read_b64_tr_b16 a[212:213], v215 offset:37376\n" \
"v_mfma_f32_16x16x32_bf16 v[144:147], a[232:235], a[68:71], v[144:147]\n" \
"ds_read_b64_tr_b16 a[214:215], v215 offset:39424\n" \
"buffer_load_dwordx4 a[32:35], v209, %[srdz], s50 offen offset:0 sc1\n" \
"v_mfma_f32_16x16x32_bf16 v[148:151], a[224:227], a[80:83], v[148:151]\n" \
"ds_read_b64_tr_b16 a[196:197], v215 offset:41472\n" \
"v_mfma_f32_16x16x32_bf16 v[148:151], a[232:235], a[84:87], v[148:151]\n" \
"ds_read_b64_tr_b16 a[198:199], v215 offset:43520\n" \
"v_mfma_f32_16x16x32_bf16 v[152:155], a[224:227], a[96:99], v[152:155]\n" \
"ds_read_b64_tr_b16 a[200:201], v215 offset:45568\n" \
"v_mfma_f32_16x16x32_bf16 v[152:155], a[232:235], a[100:103], v[152:155]\n" \
"ds_read_b64_tr_b16 a[202:203], v215 offset:47616\n" \
"buffer_load_dwordx4 a[36:39], v209, %[srdz], s50 offen offset:1024 sc1\n" \
"v_mfma_f32_16x16x32_bf16 v[156:159], a[224:227], a[112:115], v[156:159]\n" \
"ds_read_b64_tr_b16 a[204:205], v227 offset:33280\n" \
"v_mfma_f32_16x16x32_bf16 v[156:159], a[232:235], a[116:119], v[156:159]\n" \
"ds_read_b64_tr_b16 a[206:207], v227 offset:35328\n" \
"v_mfma_f32_16x16x32_bf16 v[144:147], a[236:239], a[72:75], v[144:147]\n" \
"ds_read_b64_tr_b16 a[216:217], v227 offset:41472\n" \
"v_mfma_f32_16x16x32_bf16 v[144:147], a[208:211], a[76:79], v[144:147]\n" \
"ds_read_b64_tr_b16 a[218:219], v227 offset:43520\n" \
"buffer_load_dwordx4 a[40:43], v209, %[srdz], s50 offen offset:2048 sc1\n" \
"v_mfma_f32_16x16x32_bf16 v[148:151], a[236:239], a[88:91], v[148:151]\n" \
"ds_read_b64_tr_b16 a[220:221], v227 offset:37376\n" \
"v_mfma_f32_16x16x32_bf16 v[148:151], a[208:211], a[92:95], v[148:151]\n" \
"ds_read_b64_tr_b16 a[222:223], v227 offset:39424\n" \
"v_mfma_f32_16x16x32_bf16 v[152:155], a[236:239], a[104:107], v[152:155]\n" \
"ds_read_b64_tr_b16 a[224:225], v227 offset:45568\n" \
"v_mfma_f32_16x16x32_bf16 v[152:155], a[208:211], a[108:111], v[152:155]\n" \
"ds_read_b64_tr_b16 a[226:227], v227 offset:47616\n" \
"buffer_load_dwordx4 a[44:47], v209, %[srdz], s50 offen offset:3072 sc1\n" \
"v_mfma_f32_16x16x32_bf16 v[156:159], a[236:239], a[120:123], v[156:159]\n" \
"v_mfma_f32_16x16x32_bf16 v[156:159], a[208:211], a[124:127], v[156:159]\n" \
"s_waitcnt vmcnt(63) lgkmcnt(12)\n" \
"s_nop 0\n" \
"v_mfma_f32_16x16x32_bf16 v[160:163], a[228:231], a[64:67], v[160:163]\n" \
"buffer_load_dwordx4 a[48:51], v210, %[srdz], s50 offen offset:0 sc1\n" \
"v_mfma_f32_16x16x32_bf16 v[160:163], a[212:215], a[68:71], v[160:163]\n" \
"v_mfma_f32_16x16x32_bf16 v[164:167], a[228:231], a[80:83], v[164:167]\n" \
"buffer_load_dwordx4 a[52:55], v210, %[srdz], s50 offen offset:1024 sc1\n" \
"v_mfma_f32_16x16x32_bf16 v[164:167], a[212:215], a[84:87], v[164:167]\n" \
"v_mfma_f32_16x16x32_bf16 v[168:171], a[228:231], a[96:99], v[168:171]\n" \
"s_waitcnt vmcnt(28) lgkmcnt(0)\n" \
"s_barrier\n" \
"v_mfma_f32_16x16x32_bf16 v[168:171], a[212:215], a[100:103], v[168:171]\n" \
"ds_read_b64_tr_b16 a[232:233], v220 offset:32768\n" \
"v_mfma_f32_16x16x32_bf16 v[172:175], a[228:231], a[112:115], v[172:175]\n" \
"ds_read_b64_tr_b16 a[234:235], v220 offset:34816\n" \
"v_mfma_f32_16x16x32_bf16 v[172:175], a[212:215], a[116:119], v[172:175]\n" \
"ds_read_b64_tr_b16 a[236:237], v220 offset:36864\n" \
"v_mfma_f32_16x16x32_bf16 v[160:163], a[196:199], a[72:75], v[160:163]\n" \
"ds_read_b64_tr_b16 a[238:239], v220 offset:38912\n" \
"v_mfma_f32_16x16x32_bf16 v[160:163], a[200:203], a[76:79], v[160:163]\n" \
"ds_read_b64_tr_b16 a[208:209], v220 offset:40960\n" \
"s_add_u32 m0, %[m0b], 49152\n" \
"s_cmp_gt_u32 s48, 6\n" \
"s_cselect_b32 s49, 6, 7\n" \
"s_lshl_b32 s49, s49, 18\n" \
"s_or_b32 s49, s49, s58\n" \
"buffer_load_dwordx4 %[wo0], %[srdt], s49 offen offset:0 lds\n" \
"v_mfma_f32_16x16x32_bf16 v[164:167], a[196:199], a[88:91], v[164:167]\n" \
"ds_read_b64_tr_b16 a[210:211], v220 offset:43008\n" \
"v_mfma_f32_16x16x32_bf16 v[164:167], a[200:203], a[92:95], v[164:167]\n" \
"ds_read_b64_tr_b16 a[228:229], v220 offset:45056\n" \
"buffer_load_dwordx4 a[56:59], v210, %[srdz], s50 offen offset:2048 sc1\n" \
"v_mfma_f32_16x16x32_bf16 v[168:171], a[196:199], a[104:107], v[168:171]\n" \
"ds_read_b64_tr_b16 a[230:231], v220 offset:47104\n" \
"v_mfma_f32_16x16x32_bf16 v[168:171], a[200:203], a[108:111], v[168:171]\n" \
"buffer_load_dwordx4 a[60:63], v210, %[srdz], s50 offen offset:3072 sc1\n" \
"v_mfma_f32_16x16x32_bf16 v[172:175], a[196:199], a[120:123], v[172:175]\n" \
"v_mfma_f32_16x16x32_bf16 v[172:175], a[200:203], a[124:127], v[172:175]\n" \
"s_waitcnt vmcnt(23) lgkmcnt(4)\n" \
"s_nop 0\n" \
"v_mfma_f32_16x16x32_bf16 v[176:179], a[204:207], a[64:67], v[176:179]\n" \
"v_mfma_f32_16x16x32_bf16 v[176:179], a[220:223], a[68:71], v[176:179]\n" \
"s_cmp_gt_u32 s48, 6\n" \
"s_cselect_b32 s50, 6, 7\n" \
"s_mul_i32 s50, s50, 152064\n" \
"buffer_load_dwordx4 a[128:131], %[oq0], %[srdz], s50 offen offset:0 sc1\n" \
"v_mfma_f32_16x16x32_bf16 v[180:183], a[204:207], a[80:83], v[180:183]\n" \
"v_mfma_f32_16x16x32_bf16 v[180:183], a[220:223], a[84:87], v[180:183]\n" \
"v_mfma_f32_16x16x32_bf16 v[184:187], a[204:207], a[96:99], v[184:187]\n" \
"v_mfma_f32_16x16x32_bf16 v[184:187], a[220:223], a[100:103], v[184:187]\n" \
"buffer_load_dwordx4 v212, %[srdt], s49 offen offset:1024 lds\n" \
"v_mfma_f32_16x16x32_bf16 v[188:191], a[204:207], a[112:115], v[188:191]\n" \
"ds_read_b64_tr_b16 a[212:213], v221 offset:32768\n" \
"v_mfma_f32_16x16x32_bf16 v[188:191], a[220:223], a[116:119], v[188:191]\n" \
"ds_read_b64_tr_b16 a[214:215], v221 offset:34816\n" \
"v_mfma_f32_16x16x32_bf16 v[128:131], a[232:235], v[0:3], v[192:195]\n" \
"ds_read_b64_tr_b16 a[196:197], v221 offset:36864\n" \
"buffer_load_dwordx4 a[132:135], %[oq0], %[srdz], s50 offen offset:1024 sc1\n" \
"v_mfma_f32_16x16x32_bf16 v[128:131], a[236:239], v[8:11], v[128:131]\n" \
"ds_read_b64_tr_b16 a[198:199], v221 offset:38912\n" \
"v_mfma_f32_16x16x32_bf16 v[132:135], a[232:235], v[32:35], v[196:199]\n" \
"buffer_load_dwordx4 a[136:139], %[oq0], %[srdz], s50 offen offset:2048 sc1\n" \
"v_mfma_f32_16x16x32_bf16 v[132:135], a[236:239], v[40:43], v[132:135]\n" \
"v_mfma_f32_16x16x32_bf16 v[136:139], a[232:235], v[64:67], v[200:203]\n" \
"v_mfma_f32_16x16x32_bf16 v[136:139], a[236:239], v[72:75], v[136:139]\n" \
"buffer_load_dwordx4 a[140:143], %[oq0], %[srdz], s50 offen offset:3072 sc1\n" \
"v_mfma_f32_16x16x32_bf16 v[140:143], a[232:235], v[96:99], v[204:207]\n" \
"ds_read_b64_tr_b16 a[200:201], v221 offset:40960\n" \
"v_mfma_f32_16x16x32_bf16 v[140:143], a[236:239], v[104:107], v[140:143]\n" \
"s_waitcnt vmcnt(26) lgkmcnt(5)\n" \
"s_nop 0\n" \
"v_mfma_f32_16x16x32_bf16 v[128:131], a[208:211], v[16:19], v[128:131]\n" \
"ds_read_b64_tr_b16 a[202:203], v221 offset:43008\n" \
"v_mfma_f32_16x16x32_bf16 v[128:131], a[228:231], v[24:27], v[128:131]\n" \
"ds_read_b64_tr_b16 a[204:205], v221 offset:45056\n" \
"v_mfma_f32_16x16x32_bf16 v[132:135], a[208:211], v[48:51], v[132:135]\n" \
"ds_read_b64_tr_b16 a[206:207], v221 offset:47104\n" \
"v_mfma_f32_16x16x32_bf16 v[132:135], a[228:231], v[56:59], v[132:135]\n" \
"buffer_load_dwordx4 a[144:147], v208, %[srdz], s50 offen offset:0 sc1\n" \
"v_mfma_f32_16x16x32_bf16 v[136:139], a[208:211], v[80:83], v[136:139]\n" \
"v_mfma_f32_16x16x32_bf16 v[136:139], a[228:231], v[88:91], v[136:139]\n" \
"buffer_load_dwordx4 v213, %[srdt], s49 offen offset:2048 lds\n" \
"v_mfma_f32_16x16x32_bf16 v[140:143], a[208:211], v[112:115], v[140:143]\n" \
"ds_read_b64_tr_b16 a[220:221], v220 offset:33280\n" \
"v_mfma_f32_16x16x32_bf16 v[140:143], a[228:231], v[120:123], v[140:143]\n" \
"ds_read_b64_tr_b16 a[222:223], v220 offset:35328\n" \
"v_mfma_f32_16x16x32_bf16 v[176:179], a[216:219], a[72:75], v[176:179]\n" \
"buffer_load_dwordx4 a[148:151], v208, %[srdz], s50 offen offset:1024 sc1\n" \
"v_mfma_f32_16x16x32_bf16 v[176:179], a[224:227], a[76:79], v[176:179]\n" \
"v_mfma_f32_16x16x32_bf16 v[180:183], a[216:219], a[88:91], v[180:183]\n" \
"buffer_load_dwordx4 a[152:155], v208, %[srdz], s50 offen offset:2048 sc1\n" \
"v_mfma_f32_16x16x32_bf16 v[180:183], a[224:227], a[92:95], v[180:183]\n" \
"v_mfma_f32_16x16x32_bf16 v[184:187], a[216:219], a[104:107], v[184:187]\n" \
"v_mfma_f32_16x16x32_bf16 v[184:187], a[224:227], a[108:111], v[184:187]\n" \
"v_cmp_u_f32_e64 s[44:45], v128, v132\n" \
"buffer_load_dwordx4 a[156:159], v208, %[srdz], s50 offen offset:3072 sc1\n" \
"v_mfma_f32_16x16x32_bf16 v[188:191], a[216:219], a[120:123], v[188:191]\n" \
"v_mfma_f32_16x16x32_bf16 v[188:191], a[224:227], a[124:127], v[188:191]\n" \
"v_cmp_u_f32_e64 s[56:57], v136, v140\n" \
"s_or_b64 s[56:57], s[56:57], s[44:45]\n" \
"s_cbranch_scc1 Lsp5_%=\n" \
"Lbk5_%=:\n" \
"s_waitcnt vmcnt(63) lgkmcnt(2)\n" \
"s_nop 0\n" \
"v_mfma_f32_16x16x32_bf16 v[144:147], a[212:215], v[0:3], v[144:147]\n" \
"ds_read_b64_tr_b16 a[216:217], v220 offset:37376\n" \
"v_mfma_f32_16x16x32_bf16 v[144:147], a[196:199], v[8:11], v[144:147]\n" \
"ds_read_b64_tr_b16 a[218:219], v220 offset:39424\n" \
"v_mfma_f32_16x16x32_bf16 v[148:151], a[212:215], v[32:35], v[148:151]\n" \
"ds_read_b64_tr_b16 a[208:209], v220 offset:41472\n" \
"v_mfma_f32_16x16x32_bf16 v[148:151], a[196:199], v[40:43], v[148:151]\n" \
"ds_read_b64_tr_b16 a[210:211], v220 offset:43520\n" \
"v_mfma_f32_16x16x32_bf16 v[152:155], a[212:215], v[64:67], v[152:155]\n" \
"ds_read_b64_tr_b16 a[224:225], v220 offset:45568\n" \
"buffer_load_dwordx4 a[160:163], v209, %[srdz], s50 offen offset:0 sc1\n" \
"v_mfma_f32_16x16x32_bf16 v[152:155], a[196:199], v[72:75], v[152:155]\n" \
"ds_read_b64_tr_b16 a[226:227], v220 offset:47616\n" \
"v_mfma_f32_16x16x32_bf16 v[156:159], a[212:215], v[96:99], v[156:159]\n" \
"ds_read_b64_tr_b16 a[228:229], v221 offset:33280\n" \
"buffer_load_dwordx4 v214, %[srdt], s49 offen offset:3072 lds\n" \
"v_mfma_f32_16x16x32_bf16 v[156:159], a[196:199], v[104:107], v[156:159]\n" \
"ds_read_b64_tr_b16 a[230:231], v221 offset:35328\n" \
"v_mfma_f32_16x16x32_bf16 v[144:147], a[200:203], v[16:19], v[144:147]\n" \
"ds_read_b64_tr_b16 a[232:233], v221 offset:41472\n" \
"buffer_load_dwordx4 a[164:167], v209, %[srdz], s50 offen offset:1024 sc1\n" \
"v_mfma_f32_16x16x32_bf16 v[144:147], a[204:207], v[24:27], v[144:147]\n" \
"ds_read_b64_tr_b16 a[234:235], v221 offset:43520\n" \
"v_mfma_f32_16x16x32_bf16 v[148:151], a[200:203], v[48:51], v[148:151]\n" \
"ds_read_b64_tr_b16 a[236:237], v221 offset:37376\n" \
"v_mfma_f32_16x16x32_bf16 v[148:151], a[204:207], v[56:59], v[148:151]\n" \
"ds_read_b64_tr_b16 a[238:239], v221 offset:39424\n" \
"v_mfma_f32_16x16x32_bf16 v[152:155], a[200:203], v[80:83], v[152:155]\n" \
"ds_read_b64_tr_b16 a[212:213], v221 offset:45568\n" \
"buffer_load_dwordx4 a[168:171], v209, %[srdz], s50 offen offset:2048 sc1\n" \
"v_mfma_f32_16x16x32_bf16 v[152:155], a[204:207], v[88:91], v[152:155]\n" \
"ds_read_b64_tr_b16 a[214:215], v221 offset:47616\n" \
"v_mfma_f32_16x16x32_bf16 v[156:159], a[200:203], v[112:115], v[156:159]\n" \
"buffer_load_dwordx4 a[172:175], v209, %[srdz], s50 offen offset:3072 sc1\n" \
"v_mfma_f32_16x16x32_bf16 v[156:159], a[204:207], v[120:123], v[156:159]\n" \
"s_waitcnt vmcnt(63) lgkmcnt(12)\n" \
"s_nop 0\n" \
"v_mfma_f32_16x16x32_bf16 v[160:163], a[220:223], v[0:3], v[160:163]\n" \
"v_mfma_f32_16x16x32_bf16 v[160:163], a[216:219], v[8:11], v[160:163]\n" \
"buffer_load_dwordx4 a[176:179], v210, %[srdz], s50 offen offset:0 sc1\n" \
"v_mfma_f32_16x16x32_bf16 v[164:167], a[220:223], v[32:35], v[164:167]\n" \
"v_mfma_f32_16x16x32_bf16 v[164:167], a[216:219], v[40:43], v[164:167]\n" \
"v_mfma_f32_16x16x32_bf16 v[168:171], a[220:223], v[64:67], v[168:171]\n" \
"s_waitcnt vmcnt(25) lgkmcnt(0)\n" \
"s_barrier\n" \
"v_mfma_f32_16x16x32_bf16 v[168:171], a[216:219], v[72:75], v[168:171]\n" \
"ds_read_b64_tr_b16 a[196:197], v220 offset:49152\n" \
"v_mfma_f32_16x16x32_bf16 v[172:175], a[220:223], v[96:99], v[172:175]\n" \
"ds_read_b64_tr_b16 a[198:199], v220 offset:51200\n" \
"v_mfma_f32_16x16x32_bf16 v[172:175], a[216:219], v[104:107], v[172:175]\n" \
"ds_read_b64_tr_b16 a[200:201], v220 offset:53248\n" \
"v_mfma_f32_16x16x32_bf16 v[160:163], a[208:211], v[16:19], v[160:163]\n" \
"ds_read_b64_tr_b16 a[202:203], v220 offset:55296\n" \
"buffer_load_dwordx4 a[180:183], v210, %[srdz], s50 offen offset:1024 sc1\n" \
"v_mfma_f32_16x16x32_bf16 v[160:163], a[224:227], v[24:27], v[160:163]\n" \
"ds_read_b64_tr_b16 a[204:205], v220 offset:57344\n" \
"v_mfma_f32_16x16x32_bf16 v[164:167], a[208:211], v[48:51], v[164:167]\n" \
"ds_read_b64_tr_b16 a[206:207], v220 offset:59392\n" \
"v_mfma_f32_16x16x32_bf16 v[164:167], a[224:227], v[56:59], v[164:167]\n" \
"ds_read_b64_tr_b16 a[220:221], v220 offset:61440\n" \
"v_mfma_f32_16x16x32_bf16 v[168:171], a[208:211], v[80:83], v[168:171]\n" \
"ds_read_b64_tr_b16 a[222:223], v220 offset:63488\n" \
"buffer_load_dwordx4 a[184:187], v210, %[srdz], s50 offen offset:2048 sc1\n" \
"v_mfma_f32_16x16x32_bf16 v[168:171], a[224:227], v[88:91], v[168:171]\n" \
"v_mfma_f32_16x16x32_bf16 v[172:175], a[208:211], v[112:115], v[172:175]\n" \
"buffer_load_dwordx4 a[188:191], v210, %[srdz], s50 offen offset:3072 sc1\n" \
"v_mfma_f32_16x16x32_bf16 v[172:175], a[224:227], v[120:123], v[172:175]\n" \
"s_waitcnt vmcnt(63) lgkmcnt(4)\n" \
"s_nop 0\n" \
"v_mfma_f32_16x16x32_bf16 v[176:179], a[228:231], v[0:3], v[176:179]\n" \
"v_mfma_f32_16x16x32_bf16 v[176:179], a[236:239], v[8:11], v[176:179]\n" \
"v_mfma_f32_16x16x32_bf16 v[180:183], a[228:231], v[32:35], v[180:183]\n" \
"v_mfma_f32_16x16x32_bf16 v[180:183], a[236:239], v[40:43], v[180:183]\n" \
"v_mfma_f32_16x16x32_bf16 v[184:187], a[228:231], v[64:67], v[184:187]\n" \
"v_mfma_f32_16x16x32_bf16 v[184:187], a[236:239], v[72:75], v[184:187]\n" \
"v_mfma_f32_16x16x32_bf16 v[188:191], a[228:231], v[96:99], v[188:191]\n" \
"ds_read_b64_tr_b16 a[216:217], v221 offset:49152\n" \
"s_waitcnt vmcnt(22)\n" \
"v_cmp_eq_u32_e32 vcc, 0, v232\n" \
"s_cbranch_vccnz Lspp_%=\n" \
"Lbkp_%=:\n" \
"s_nop 0\n" \
"v_mfma_f32_16x16x32_bf16 v[188:191], a[236:239], v[104:107], v[188:191]\n" \
"ds_read_b64_tr_b16 a[218:219], v221 offset:51200\n" \
"v_mfma_f32_16x16x32_bf16 v[192:195], a[196:199], a[0:3], v[128:131]\n" \
"ds_read_b64_tr_b16 a[208:209], v221 offset:53248\n" \
"v_mfma_f32_16x16x32_bf16 v[192:195], a[200:203], a[4:7], v[192:195]\n" \
"ds_read_b64_tr_b16 a[210:211], v221 offset:55296\n" \
"s_add_u32 m0, %[m0b], 0\n" \
"s_cmp_gt_u32 s48, 3\n" \
"s_cselect_b32 s49, 3, 4\n" \
"s_lshl_b32 s49, s49, 18\n" \
"s_or_b32 s49, s49, s59\n" \
"s_nop 0\n" \
"buffer_load_dwordx4 %[wo0], %[srdt], s49 offen offset:0 lds\n" \
"v_mfma_f32_16x16x32_bf16 v[196:199], a[196:199], a[16:19], v[132:135]\n" \
"v_mfma_f32_16x16x32_bf16 v[196:199], a[200:203], a[20:23], v[196:199]\n" \
"v_mfma_f32_16x16x32_bf16 v[200:203], a[196:199], a[32:35], v[136:139]\n" \
"v_mfma_f32_16x16x32_bf16 v[200:203], a[200:203], a[36:39], v[200:203]\n" \
"v_mfma_f32_16x16x32_bf16 v[204:207], a[196:199], a[48:51], v[140:143]\n" \
"ds_read_b64_tr_b16 a[224:225], v221 offset:57344\n" \
"v_mfma_f32_16x16x32_bf16 v[204:207], a[200:203], a[52:55], v[204:207]\n" \
"s_waitcnt vmcnt(20) lgkmcnt(5)\n" \
"s_nop 0\n" \
"v_mfma_f32_16x16x32_bf16 v[192:195], a[204:207], a[8:11], v[192:195]\n" \
"ds_read_b64_tr_b16 a[226:227], v221 offset:59392\n" \
"v_mfma_f32_16x16x32_bf16 v[192:195], a[220:223], a[12:15], v[192:195]\n" \
"ds_read_b64_tr_b16 a[228:229], v221 offset:61440\n" \
"v_mfma_f32_16x16x32_bf16 v[196:199], a[204:207], a[24:27], v[196:199]\n" \
"ds_read_b64_tr_b16 a[230:231], v221 offset:63488\n" \
"v_mfma_f32_16x16x32_bf16 v[196:199], a[220:223], a[28:31], v[196:199]\n" \
"buffer_load_dwordx4 v212, %[srdt], s49 offen offset:1024 lds\n" \
"v_mfma_f32_16x16x32_bf16 v[200:203], a[204:207], a[40:43], v[200:203]\n" \
"v_mfma_f32_16x16x32_bf16 v[200:203], a[220:223], a[44:47], v[200:203]\n" \
"v_mfma_f32_16x16x32_bf16 v[204:207], a[204:207], a[56:59], v[204:207]\n" \
"ds_read_b64_tr_b16 a[236:237], v220 offset:49664\n" \
"v_mfma_f32_16x16x32_bf16 v[204:207], a[220:223], a[60:63], v[204:207]\n" \
"ds_read_b64_tr_b16 a[238:239], v220 offset:51712\n" \
"v_mfma_f32_16x16x32_bf16 v[176:179], a[232:235], v[16:19], v[176:179]\n" \
"v_mfma_f32_16x16x32_bf16 v[176:179], a[212:215], v[24:27], v[176:179]\n" \
"v_mfma_f32_16x16x32_bf16 v[180:183], a[232:235], v[48:51], v[180:183]\n" \
"buffer_load_dwordx4 v213, %[srdt], s49 offen offset:2048 lds\n" \
"v_mfma_f32_16x16x32_bf16 v[180:183], a[212:215], v[56:59], v[180:183]\n" \
"v_mfma_f32_16x16x32_bf16 v[184:187], a[232:235], v[80:83], v[184:187]\n" \
"s_add_u32 m0, %[m0b], 98304\n" \
"s_mov_b32 s61, 0\n" \
"s_nop 0\n" \
"buffer_load_dwordx4 %[vdn], s[64:67], s61 offen offset:0 lds\n" \
"v_mfma_f32_16x16x32_bf16 v[184:187], a[212:215], v[88:91], v[184:187]\n" \
"v_cmp_u_f32_e64 s[44:45], v192, v196\n" \
"v_mfma_f32_16x16x32_bf16 v[188:191], a[232:235], v[112:115], v[188:191]\n" \
"v_mfma_f32_16x16x32_bf16 v[188:191], a[212:215], v[120:123], v[188:191]\n" \
"v_cmp_u_f32_e64 s[56:57], v200, v204\n" \
"s_or_b64 s[56:57], s[56:57], s[44:45]\n" \
"s_cbranch_scc1 Lsp6_%=\n" \
"Lbk6_%=:\n" \
"s_waitcnt vmcnt(63) lgkmcnt(2)\n" \
"s_nop 0\n" \
"v_mfma_f32_16x16x32_bf16 v[144:147], a[216:219], a[0:3], v[144:147]\n" \
"ds_read_b64_tr_b16 a[232:233], v220 offset:53760\n" \
"v_mfma_f32_16x16x32_bf16 v[144:147], a[208:211], a[4:7], v[144:147]\n" \
"ds_read_b64_tr_b16 a[234:235], v220 offset:55808\n" \
"v_mfma_f32_16x16x32_bf16 v[148:151], a[216:219], a[16:19], v[148:151]\n" \
"ds_read_b64_tr_b16 a[196:197], v220 offset:57856\n" \
"v_mfma_f32_16x16x32_bf16 v[148:151], a[208:211], a[20:23], v[148:151]\n" \
"ds_read_b64_tr_b16 a[198:199], v220 offset:59904\n" \
"v_mfma_f32_16x16x32_bf16 v[152:155], a[216:219], a[32:35], v[152:155]\n" \
"ds_read_b64_tr_b16 a[200:201], v220 offset:61952\n" \
"v_mfma_f32_16x16x32_bf16 v[152:155], a[208:211], a[36:39], v[152:155]\n" \
"ds_read_b64_tr_b16 a[202:203], v220 offset:64000\n" \
"v_mfma_f32_16x16x32_bf16 v[156:159], a[216:219], a[48:51], v[156:159]\n" \
"ds_read_b64_tr_b16 a[204:205], v221 offset:49664\n" \
"v_mfma_f32_16x16x32_bf16 v[156:159], a[208:211], a[52:55], v[156:159]\n" \
"ds_read_b64_tr_b16 a[206:207], v221 offset:51712\n" \
"s_add_u32 m0, %[m0b], 0\n" \
"s_nop 0\n" \
"buffer_load_dwordx4 v214, %[srdt], s49 offen offset:3072 lds\n" \
"v_mfma_f32_16x16x32_bf16 v[144:147], a[224:227], a[8:11], v[144:147]\n" \
"ds_read_b64_tr_b16 a[212:213], v221 offset:57856\n" \
"v_mfma_f32_16x16x32_bf16 v[144:147], a[228:231], a[12:15], v[144:147]\n" \
"ds_read_b64_tr_b16 a[214:215], v221 offset:59904\n" \
"v_mfma_f32_16x16x32_bf16 v[148:151], a[224:227], a[24:27], v[148:151]\n" \
"ds_read_b64_tr_b16 a[220:221], v221 offset:53760\n" \
"v_mfma_f32_16x16x32_bf16 v[148:151], a[228:231], a[28:31], v[148:151]\n" \
"ds_read_b64_tr_b16 a[222:223], v221 offset:55808\n" \
"v_mfma_f32_16x16x32_bf16 v[152:155], a[224:227], a[40:43], v[152:155]\n" \
"ds_read_b64_tr_b16 a[216:217], v221 offset:61952\n" \
"v_mfma_f32_16x16x32_bf16 v[152:155], a[228:231], a[44:47], v[152:155]\n" \
"ds_read_b64_tr_b16 a[218:219], v221 offset:64000\n" \
"v_mfma_f32_16x16x32_bf16 v[156:159], a[224:227], a[56:59], v[156:159]\n" \
"s_add_u32 m0, %[m0b], 99328\n" \
"s_mov_b32 s61, 4096\n" \
"buffer_load_dwordx4 %[vdn], s[64:67], s61 offen offset:0 lds\n" \
"v_mfma_f32_16x16x32_bf16 v[156:159], a[228:231], a[60:63], v[156:159]\n" \
"s_waitcnt vmcnt(63) lgkmcnt(12)\n" \
"s_nop 0\n" \
"v_mfma_f32_16x16x32_bf16 v[160:163], a[236:239], a[0:3], v[160:163]\n" \
"v_mfma_f32_16x16x32_bf16 v[160:163], a[232:235], a[4:7], v[160:163]\n" \
"v_mfma_f32_16x16x32_bf16 v[164:167], a[236:239], a[16:19], v[164:167]\n" \
"v_mfma_f32_16x16x32_bf16 v[164:167], a[232:235], a[20:23], v[164:167]\n" \
"s_add_u32 m0, %[m0b], 16384\n" \
"s_cmp_gt_u32 s48, 4\n" \
"s_cselect_b32 s49, 4, 5\n" \
"s_lshl_b32 s49, s49, 18\n" \
"s_or_b32 s49, s49, s59\n" \
"buffer_load_dwordx4 %[wo0], %[srdt], s49 offen offset:0 lds\n" \
"v_mfma_f32_16x16x32_bf16 v[168:171], a[236:239], a[32:35], v[168:171]\n" \
"s_waitcnt vmcnt(14) lgkmcnt(0)\n" \
"s_barrier\n" \
"v_mfma_f32_16x16x32_bf16 v[168:171], a[232:235], a[36:39], v[168:171]\n" \
"ds_read_b64_tr_b16 a[208:209], v215 offset:49152\n" \
"v_mfma_f32_16x16x32_bf16 v[172:175], a[236:239], a[48:51], v[172:175]\n" \
"ds_read_b64_tr_b16 a[210:211], v215 offset:51200\n" \
"v_mfma_f32_16x16x32_bf16 v[172:175], a[232:235], a[52:55], v[172:175]\n" \
"ds_read_b64_tr_b16 a[224:225], v215 offset:53248\n" \
"v_mfma_f32_16x16x32_bf16 v[160:163], a[196:199], a[8:11], v[160:163]\n" \
"ds_read_b64_tr_b16 a[226:227], v215 offset:55296\n" \
"v_mfma_f32_16x16x32_bf16 v[160:163], a[200:203], a[12:15], v[160:163]\n" \
"ds_read_b64_tr_b16 a[228:229], v215 offset:57344\n" \
"v_mfma_f32_16x16x32_bf16 v[164:167], a[196:199], a[24:27], v[164:167]\n" \
"ds_read_b64_tr_b16 a[230:231], v215 offset:59392\n" \
"v_mfma_f32_16x16x32_bf16 v[164:167], a[200:203], a[28:31], v[164:167]\n" \
"ds_read_b64_tr_b16 a[236:237], v215 offset:61440\n" \
"v_mfma_f32_16x16x32_bf16 v[168:171], a[196:199], a[40:43], v[168:171]\n" \
"ds_read_b64_tr_b16 a[238:239], v215 offset:63488\n" \
"v_mfma_f32_16x16x32_bf16 v[168:171], a[200:203], a[44:47], v[168:171]\n" \
"buffer_load_dwordx4 v212, %[srdt], s49 offen offset:1024 lds\n" \
"v_mfma_f32_16x16x32_bf16 v[172:175], a[196:199], a[56:59], v[172:175]\n" \
"v_mfma_f32_16x16x32_bf16 v[172:175], a[200:203], a[60:63], v[172:175]\n" \
"s_waitcnt vmcnt(10) lgkmcnt(4)\n" \
"s_nop 0\n" \
"v_mfma_f32_16x16x32_bf16 v[176:179], a[204:207], a[0:3], v[176:179]\n" \
"s_add_u32 m0, %[m0b], 100352\n" \
"s_mov_b32 s61, 8192\n" \
"buffer_load_dwordx4 %[vdn], s[64:67], s61 offen offset:0 lds\n" \
"v_mfma_f32_16x16x32_bf16 v[176:179], a[220:223], a[4:7], v[176:179]\n" \
"v_mfma_f32_16x16x32_bf16 v[180:183], a[204:207], a[16:19], v[180:183]\n" \
"v_mfma_f32_16x16x32_bf16 v[180:183], a[220:223], a[20:23], v[180:183]\n" \
"v_mfma_f32_16x16x32_bf16 v[184:187], a[204:207], a[32:35], v[184:187]\n" \
"v_mfma_f32_16x16x32_bf16 v[184:187], a[220:223], a[36:39], v[184:187]\n" \
"v_mfma_f32_16x16x32_bf16 v[188:191], a[204:207], a[48:51], v[188:191]\n" \
"ds_read_b64_tr_b16 a[232:233], v227 offset:49152\n" \
"v_mfma_f32_16x16x32_bf16 v[188:191], a[220:223], a[52:55], v[188:191]\n" \
"ds_read_b64_tr_b16 a[234:235], v227 offset:51200\n" \
"v_mfma_f32_16x16x32_bf16 v[128:131], a[208:211], a[128:131], v[192:195]\n" \
"ds_read_b64_tr_b16 a[196:197], v227 offset:53248\n" \
"v_mfma_f32_16x16x32_bf16 v[128:131], a[224:227], a[132:135], v[128:131]\n" \
"ds_read_b64_tr_b16 a[198:199], v227 offset:55296\n" \
"v_mfma_f32_16x16x32_bf16 v[132:135], a[208:211], a[144:147], v[196:199]\n" \
"s_add_u32 m0, %[m0b], 16384\n" \
"s_nop 0\n" \
"s_nop 0\n" \
"buffer_load_dwordx4 v213, %[srdt], s49 offen offset:2048 lds\n" \
"v_mfma_f32_16x16x32_bf16 v[132:135], a[224:227], a[148:151], v[132:135]\n" \
"v_mfma_f32_16x16x32_bf16 v[136:139], a[208:211], a[160:163], v[200:203]\n" \
"v_mfma_f32_16x16x32_bf16 v[136:139], a[224:227], a[164:167], v[136:139]\n" \
"v_mfma_f32_16x16x32_bf16 v[140:143], a[208:211], a[176:179], v[204:207]\n" \
"ds_read_b64_tr_b16 a[200:201], v227 offset:57344\n" \
"v_mfma_f32_16x16x32_bf16 v[140:143], a[224:227], a[180:183], v[140:143]\n" \
"s_waitcnt vmcnt(10) lgkmcnt(5)\n" \
"s_nop 0\n" \
"v_mfma_f32_16x16x32_bf16 v[128:131], a[228:231], a[136:139], v[128:131]\n" \
"ds_read_b64_tr_b16 a[202:203], v227 offset:59392\n" \
"v_mfma_f32_16x16x32_bf16 v[128:131], a[236:239], a[140:143], v[128:131]\n" \
"ds_read_b64_tr_b16 a[204:205], v227 offset:61440\n" \
"s_add_u32 m0, %[m0b], 101376\n" \
"s_mov_b32 s61, 12288\n" \
"buffer_load_dwordx4 %[vdn], s[64:67], s61 offen offset:0 lds\n" \
"v_mfma_f32_16x16x32_bf16 v[132:135], a[228:231], a[152:155], v[132:135]\n" \
"ds_read_b64_tr_b16 a[206:207], v227 offset:63488\n" \
"v_mfma_f32_16x16x32_bf16 v[132:135], a[236:239], a[156:159], v[132:135]\n" \
"s_add_u32 m0, %[m0b], 16384\n" \
"s_nop 0\n" \
"s_nop 0\n" \
"buffer_load_dwordx4 v214, %[srdt], s49 offen offset:3072 lds\n" \
"v_mfma_f32_16x16x32_bf16 v[136:139], a[228:231], a[168:171], v[136:139]\n" \
"v_mfma_f32_16x16x32_bf16 v[136:139], a[236:239], a[172:175], v[136:139]\n" \
"v_mfma_f32_16x16x32_bf16 v[140:143], a[228:231], a[184:187], v[140:143]\n" \
"ds_read_b64_tr_b16 a[220:221], v215 offset:49664\n" \
"v_mfma_f32_16x16x32_bf16 v[140:143], a[236:239], a[188:191], v[140:143]\n" \
"ds_read_b64_tr_b16 a[222:223], v215 offset:51712\n" \
"v_mfma_f32_16x16x32_bf16 v[176:179], a[212:215], a[8:11], v[176:179]\n" \
"v_mfma_f32_16x16x32_bf16 v[176:179], a[216:219], a[12:15], v[176:179]\n" \
"v_mfma_f32_16x16x32_bf16 v[180:183], a[212:215], a[24:27], v[180:183]\n" \
"v_mfma_f32_16x16x32_bf16 v[180:183], a[216:219], a[28:31], v[180:183]\n" \
"v_mfma_f32_16x16x32_bf16 v[184:187], a[212:215], a[40:43], v[184:187]\n" \
"v_mfma_f32_16x16x32_bf16 v[184:187], a[216:219], a[44:47], v[184:187]\n" \
"v_cmp_u_f32_e64 s[44:45], v128, v132\n" \
"v_mfma_f32_16x16x32_bf16 v[188:191], a[212:215], a[56:59], v[188:191]\n" \
"v_mfma_f32_16x16x32_bf16 v[188:191], a[216:219], a[60:63], v[188:191]\n" \
"v_cmp_u_f32_e64 s[56:57], v136, v140\n" \
"s_or_b64 s[56:57], s[56:57], s[44:45]\n" \
"s_cbranch_scc1 Lsp7_%=\n" \
"Lbk7_%=:\n" \
"s_waitcnt vmcnt(63) lgkmcnt(2)\n" \
"s_nop 0\n" \
"v_mfma_f32_16x16x32_bf16 v[144:147], a[232:235], a[128:131], v[144:147]\n" \
"ds_read_b64_tr_b16 a[212:213], v215 offset:53760\n" \
"v_mfma_f32_16x16x32_bf16 v[144:147], a[196:199], a[132:135], v[144:147]\n" \
"ds_read_b64_tr_b16 a[214:215], v215 offset:55808\n" \
"v_mfma_f32_16x16x32_bf16 v[148:151], a[232:235], a[144:147], v[148:151]\n" \
"ds_read_b64_tr_b16 a[208:209], v215 offset:57856\n" \
"v_mfma_f32_16x16x32_bf16 v[148:151], a[196:199], a[148:151], v[148:151]\n" \
"ds_read_b64_tr_b16 a[210:211], v215 offset:59904\n" \
"s_lshl_b32 s61, %[wid], 10\n" \
"v_lshrrev_b32_e32 v231, 3, v224\n" \
"v_add_u32_e64 v231, s61, v231\n" \
".if . - adaln_body != 53248\n" \
".error \"size model\"\n" \
".endif\n" \
"Lk12_%=:\n" \
"s_cbranch_execz Lk13_%=\n" \
"s_nop 0\n" \
"s_add_u32 m0, %[m0b], 32768\n" \
"s_cmp_gt_u32 s48, 5\n" \
"s_cselect_b32 s49, 5, 6\n" \
"s_lshl_b32 s49, s49, 18\n" \
"s_or_b32 s49, s49, s59\n" \
"buffer_load_dwordx4 %[wo0], %[srdt], s49 offen offset:0 lds\n" \
"v_mfma_f32_16x16x32_bf16 v[152:155], a[232:235], a[160:163], v[152:155]\n" \
"ds_read_b64_tr_b16 a[216:217], v215 offset:61952\n" \
"v_readlane_b32 s44, %[tlo], 20\n" \
"v_readlane_b32 s45, %[thi], 20\n" \
"v_lshrrev_b32_e64 v236, 1, v231\n" \
"v_mfma_f32_16x16x32_bf16 v[152:155], a[196:199], a[164:167], v[152:155]\n" \
"ds_read_b64_tr_b16 a[218:219], v215 offset:64000\n" \
"v_mfma_f32_16x16x32_bf16 v[156:159], a[232:235], a[176:179], v[156:159]\n" \
"ds_read_b64_tr_b16 a[224:225], v227 offset:49664\n" \
"v_mfma_f32_16x16x32_bf16 v[156:159], a[196:199], a[180:183], v[156:159]\n" \
"ds_read_b64_tr_b16 a[226:227], v227 offset:51712\n" \
"v_mfma_f32_16x16x32_bf16 v[144:147], a[200:203], a[136:139], v[144:147]\n" \
"ds_read_b64_tr_b16 a[228:229], v227 offset:53760\n" \
"v_mfma_f32_16x16x32_bf16 v[144:147], a[204:207], a[140:143], v[144:147]\n" \
"ds_read_b64_tr_b16 a[230:231], v227 offset:55808\n" \
"v_mfma_f32_16x16x32_bf16 v[148:151], a[200:203], a[152:155], v[148:151]\n" \
"s_mov_b32 s61, 262144\n" \
"buffer_load_dwordx4 v[192:195], v231, %[srdz], s61 offen offset:0 sc1\n" \
"v_mfma_f32_16x16x32_bf16 v[148:151], a[204:207], a[156:159], v[148:151]\n" \
"v_mfma_f32_16x16x32_bf16 v[152:155], a[200:203], a[168:171], v[152:155]\n" \
"buffer_load_dwordx4 v212, %[srdt], s49 offen offset:1024 lds\n" \
"v_mfma_f32_16x16x32_bf16 v[152:155], a[204:207], a[172:175], v[152:155]\n" \
"v_mfma_f32_16x16x32_bf16 v[156:159], a[200:203], a[184:187], v[156:159]\n" \
"ds_read_b64_tr_b16 a[236:237], v227 offset:57856\n" \
"v_mfma_f32_16x16x32_bf16 v[156:159], a[204:207], a[188:191], v[156:159]\n" \
"s_waitcnt vmcnt(63) lgkmcnt(5)\n" \
"s_nop 0\n" \
"v_mfma_f32_16x16x32_bf16 v[160:163], a[220:223], a[128:131], v[160:163]\n" \
"ds_read_b64_tr_b16 a[238:239], v227 offset:59904\n" \
"v_mfma_f32_16x16x32_bf16 v[160:163], a[212:215], a[132:135], v[160:163]\n" \
"ds_read_b64_tr_b16 a[232:233], v227 offset:61952\n" \
"v_mfma_f32_16x16x32_bf16 v[164:167], a[220:223], a[144:147], v[164:167]\n" \
"ds_read_b64_tr_b16 a[234:235], v227 offset:64000\n" \
"s_mov_b32 s61, 266240\n" \
"buffer_load_dwordx4 v[196:199], v231, %[srdz], s61 offen offset:0 sc1\n" \
"v_mfma_f32_16x16x32_bf16 v[164:167], a[212:215], a[148:151], v[164:167]\n" \
"v_mfma_f32_16x16x32_bf16 v[168:171], a[220:223], a[160:163], v[168:171]\n" \
"v_cvt_pk_bf16_f32 v128, v128, v129\n" \
"buffer_load_dwordx4 v213, %[srdt], s49 offen offset:2048 lds\n" \
"v_mfma_f32_16x16x32_bf16 v[168:171], a[212:215], a[164:167], v[168:171]\n" \
"v_cvt_pk_bf16_f32 v129, v130, v131\n" \
"v_mfma_f32_16x16x32_bf16 v[172:175], a[220:223], a[176:179], v[172:175]\n" \
"v_cvt_pk_bf16_f32 v130, v144, v145\n" \
"v_mfma_f32_16x16x32_bf16 v[172:175], a[212:215], a[180:183], v[172:175]\n" \
"v_cvt_pk_bf16_f32 v131, v146, v147\n" \
"v_mfma_f32_16x16x32_bf16 v[160:163], a[208:211], a[136:139], v[160:163]\n" \
"s_add_u32 s55, s46, 0\n" \
"s_nop 0\n" \
"buffer_store_dwordx4 v[128:131], %[vxs], %[srdz], s55 offen offset:0 sc1\n" \
"v_mfma_f32_16x16x32_bf16 v[160:163], a[216:219], a[140:143], v[160:163]\n" \
"v_mfma_f32_16x16x32_bf16 v[164:167], a[208:211], a[152:155], v[164:167]\n" \
"v_cvt_pk_bf16_f32 v132, v132, v133\n" \
"s_mov_b32 s61, 270336\n" \
"buffer_load_dwordx4 v[200:203], v231, %[srdz], s61 offen offset:0 sc1\n" \
"v_mfma_f32_16x16x32_bf16 v[164:167], a[216:219], a[156:159], v[164:167]\n" \
"v_cvt_pk_bf16_f32 v133, v134, v135\n" \
"v_mfma_f32_16x16x32_bf16 v[168:171], a[208:211], a[168:171], v[168:171]\n" \
"s_waitcnt vmcnt(14) lgkmcnt(0)\n" \
"s_barrier\n" \
"v_mfma_f32_16x16x32_bf16 v[168:171], a[216:219], a[172:175], v[168:171]\n" \
"ds_read_b64_tr_b16 a[196:197], v215 offset:0\n" \
"v_cvt_pk_bf16_f32 v134, v148, v149\n" \
"v_mfma_f32_16x16x32_bf16 v[172:175], a[208:211], a[184:187], v[172:175]\n" \
"ds_read_b64_tr_b16 a[198:199], v215 offset:2048\n" \
"v_cvt_pk_bf16_f32 v135, v150, v151\n" \
"v_mfma_f32_16x16x32_bf16 v[172:175], a[216:219], a[188:191], v[172:175]\n" \
"s_waitcnt vmcnt(63) lgkmcnt(2)\n" \
"s_nop 0\n" \
"v_mfma_f32_16x16x32_bf16 v[176:179], a[224:227], a[128:131], v[176:179]\n" \
"ds_read_b64_tr_b16 a[200:201], v215 offset:4096\n" \
"v_cvt_pk_bf16_f32 v136, v136, v137\n" \
"v_mfma_f32_16x16x32_bf16 v[176:179], a[228:231], a[132:135], v[176:179]\n" \
"ds_read_b64_tr_b16 a[202:203], v215 offset:6144\n" \
"v_cvt_pk_bf16_f32 v137, v138, v139\n" \
"v_mfma_f32_16x16x32_bf16 v[180:183], a[224:227], a[144:147], v[180:183]\n" \
"ds_read_b64_tr_b16 a[204:205], v215 offset:8192\n" \
"s_add_u32 s55, s46, 304128\n" \
"buffer_store_dwordx4 v[132:135], %[vxs], %[srdz], s55 offen offset:0 sc1\n" \
"v_mfma_f32_16x16x32_bf16 v[180:183], a[228:231], a[148:151], v[180:183]\n" \
"ds_read_b64_tr_b16 a[206:207], v215 offset:10240\n" \
"v_cvt_pk_bf16_f32 v138, v152, v153\n" \
"v_mfma_f32_16x16x32_bf16 v[184:187], a[224:227], a[160:163], v[184:187]\n" \
"ds_read_b64_tr_b16 a[220:221], v215 offset:12288\n" \
"v_cvt_pk_bf16_f32 v139, v154, v155\n" \
"s_mov_b32 s61, 274432\n" \
"buffer_load_dwordx4 v[204:207], v231, %[srdz], s61 offen offset:0 sc1\n" \
"v_mfma_f32_16x16x32_bf16 v[184:187], a[228:231], a[164:167], v[184:187]\n" \
"ds_read_b64_tr_b16 a[222:223], v215 offset:14336\n" \
"v_mfma_f32_16x16x32_bf16 v[188:191], a[224:227], a[176:179], v[188:191]\n" \
"ds_read_b64_tr_b16 a[212:213], v227 offset:0\n" \
"v_cvt_pk_bf16_f32 v140, v140, v141\n" \
"buffer_load_dwordx4 v214, %[srdt], s49 offen offset:3072 lds\n" \
"v_mfma_f32_16x16x32_bf16 v[188:191], a[228:231], a[180:183], v[188:191]\n" \
"ds_read_b64_tr_b16 a[214:215], v227 offset:2048\n" \
"v_cvt_pk_bf16_f32 v141, v142, v143\n" \
"v_mfma_f32_16x16x32_bf16 v[176:179], a[236:239], a[136:139], v[176:179]\n" \
"ds_read_b64_tr_b16 a[208:209], v227 offset:4096\n" \
"s_add_u32 s55, s46, 608256\n" \
"buffer_store_dwordx4 v[136:139], %[vxs], %[srdz], s55 offen offset:0 sc1\n" \
"v_mfma_f32_16x16x32_bf16 v[176:179], a[232:235], a[140:143], v[176:179]\n" \
"ds_read_b64_tr_b16 a[210:211], v227 offset:6144\n" \
"v_cvt_pk_bf16_f32 v142, v156, v157\n" \
"v_mfma_f32_16x16x32_bf16 v[180:183], a[236:239], a[152:155], v[180:183]\n" \
"v_cvt_pk_bf16_f32 v143, v158, v159\n" \
"v_mfma_f32_16x16x32_bf16 v[180:183], a[232:235], a[156:159], v[180:183]\n" \
"s_add_u32 s55, s46, 912384\n" \
"buffer_store_dwordx4 v[140:143], %[vxs], %[srdz], s55 offen offset:0 sc1\n" \
"v_mfma_f32_16x16x32_bf16 v[184:187], a[236:239], a[168:171], v[184:187]\n" \
"v_mfma_f32_16x16x32_bf16 v[184:187], a[232:235], a[172:175], v[184:187]\n" \
"global_load_dwordx2 v[232:233], v236, s[44:45]\n" \
"v_mfma_f32_16x16x32_bf16 v[188:191], a[236:239], a[184:187], v[188:191]\n" \
"ds_read_b64_tr_b16 a[216:217], v227 offset:8192\n" \
"v_mfma_f32_16x16x32_bf16 v[188:191], a[232:235], a[188:191], v[188:191]\n" \
"s_waitcnt vmcnt(63) lgkmcnt(5)\n" \
"s_nop 0\n" \
"v_mfma_f32_16x16x32_bf16 v[4:7], a[196:199], a[64:67], v[4:7]\n" \
"ds_read_b64_tr_b16 a[218:219], v227 offset:10240\n" \
"v_mfma_f32_16x16x32_bf16 v[4:7], a[200:203], a[68:71], v[4:7]\n" \
"ds_read_b64_tr_b16 a[224:225], v227 offset:12288\n" \
"v_mfma_f32_16x16x32_bf16 v[36:39], a[196:199], a[80:83], v[36:39]\n" \
"ds_read_b64_tr_b16 a[226:227], v227 offset:14336\n" \
"v_mfma_f32_16x16x32_bf16 v[36:39], a[200:203], a[84:87], v[36:39]\n" \
"v_mfma_f32_16x16x32_bf16 v[68:71], a[196:199], a[96:99], v[68:71]\n" \
"v_cvt_pk_bf16_f32 v160, v160, v161\n" \
"v_mfma_f32_16x16x32_bf16 v[68:71], a[200:203], a[100:103], v[68:71]\n" \
"v_cvt_pk_bf16_f32 v161, v162, v163\n" \
"v_mfma_f32_16x16x32_bf16 v[100:103], a[196:199], a[112:115], v[100:103]\n" \
"ds_read_b64_tr_b16 a[228:229], v215 offset:512\n" \
"v_cvt_pk_bf16_f32 v162, v176, v177\n" \
"v_mfma_f32_16x16x32_bf16 v[100:103], a[200:203], a[116:119], v[100:103]\n" \
"ds_read_b64_tr_b16 a[230:231], v215 offset:2560\n" \
"v_cvt_pk_bf16_f32 v163, v178, v179\n" \
"v_mfma_f32_16x16x32_bf16 v[4:7], a[204:207], a[72:75], v[4:7]\n" \
"ds_read_b64_tr_b16 a[236:237], v215 offset:4608\n" \
"v_mfma_f32_16x16x32_bf16 v[4:7], a[220:223], a[76:79], v[4:7]\n" \
"ds_read_b64_tr_b16 a[238:239], v215 offset:6656\n" \
"v_mfma_f32_16x16x32_bf16 v[36:39], a[204:207], a[88:91], v[36:39]\n" \
"s_add_u32 s55, s46, 4096\n" \
"buffer_store_dwordx4 v[160:163], %[vxs], %[srdz], s55 offen offset:0 sc1\n" \
"v_mfma_f32_16x16x32_bf16 v[36:39], a[220:223], a[92:95], v[36:39]\n" \
"v_cvt_pk_bf16_f32 v164, v164, v165\n" \
"v_cvt_pk_bf16_f32 v165, v166, v167\n" \
"v_mfma_f32_16x16x32_bf16 v[68:71], a[204:207], a[104:107], v[68:71]\n" \
"v_cvt_pk_bf16_f32 v166, v180, v181\n" \
"s_add_u32 m0, %[m0b], 49152\n" \
"s_cmp_gt_u32 s48, 6\n" \
"s_cselect_b32 s49, 6, 7\n" \
"s_lshl_b32 s49, s49, 18\n" \
"s_or_b32 s49, s49, s59\n" \
"buffer_load_dwordx4 %[wo0], %[srdt], s49 offen offset:0 lds\n" \
"v_mfma_f32_16x16x32_bf16 v[68:71], a[220:223], a[108:111], v[68:71]\n" \
"v_cvt_pk_bf16_f32 v167, v182, v183\n" \
"v_mfma_f32_16x16x32_bf16 v[100:103], a[204:207], a[120:123], v[100:103]\n" \
"ds_read_b64_tr_b16 a[232:233], v215 offset:8704\n" \
"v_mfma_f32_16x16x32_bf16 v[100:103], a[220:223], a[124:127], v[100:103]\n" \
"s_waitcnt vmcnt(63) lgkmcnt(5)\n" \
"s_nop 0\n" \
"v_mfma_f32_16x16x32_bf16 v[12:15], a[212:215], a[64:67], v[12:15]\n" \
"ds_read_b64_tr_b16 a[234:235], v215 offset:10752\n" \
"v_cvt_pk_bf16_f32 v168, v168, v169\n" \
"v_mfma_f32_16x16x32_bf16 v[12:15], a[208:211], a[68:71], v[12:15]\n" \
"ds_read_b64_tr_b16 a[196:197], v215 offset:12800\n" \
"s_add_u32 s55, s46, 308224\n" \
"buffer_store_dwordx4 v[164:167], %[vxs], %[srdz], s55 offen offset:0 sc1\n" \
"v_mfma_f32_16x16x32_bf16 v[44:47], a[212:215], a[80:83], v[44:47]\n" \
"ds_read_b64_tr_b16 a[198:199], v215 offset:14848\n" \
"v_cvt_pk_bf16_f32 v169, v170, v171\n" \
"v_mfma_f32_16x16x32_bf16 v[44:47], a[208:211], a[84:87], v[44:47]\n" \
"v_cvt_pk_bf16_f32 v170, v184, v185\n" \
"v_cvt_pk_bf16_f32 v171, v186, v187\n" \
"v_mfma_f32_16x16x32_bf16 v[76:79], a[212:215], a[96:99], v[76:79]\n" \
"s_add_u32 s55, s46, 612352\n" \
"buffer_store_dwordx4 v[168:171], %[vxs], %[srdz], s55 offen offset:0 sc1\n" \
"v_mfma_f32_16x16x32_bf16 v[76:79], a[208:211], a[100:103], v[76:79]\n" \
"v_mfma_f32_16x16x32_bf16 v[108:111], a[212:215], a[112:115], v[108:111]\n" \
"ds_read_b64_tr_b16 a[200:201], v227 offset:512\n" \
"v_cvt_pk_bf16_f32 v172, v172, v173\n" \
"buffer_load_dwordx4 v212, %[srdt], s49 offen offset:1024 lds\n" \
"v_mfma_f32_16x16x32_bf16 v[108:111], a[208:211], a[116:119], v[108:111]\n" \
"ds_read_b64_tr_b16 a[202:203], v227 offset:2560\n" \
"v_cvt_pk_bf16_f32 v173, v174, v175\n" \
"v_mfma_f32_16x16x32_bf16 v[12:15], a[216:219], a[72:75], v[12:15]\n" \
"ds_read_b64_tr_b16 a[204:205], v227 offset:4608\n" \
"v_cvt_pk_bf16_f32 v174, v188, v189\n" \
"v_mfma_f32_16x16x32_bf16 v[12:15], a[224:227], a[76:79], v[12:15]\n" \
"ds_read_b64_tr_b16 a[206:207], v227 offset:6656\n" \
"v_cvt_pk_bf16_f32 v175, v190, v191\n" \
"v_mfma_f32_16x16x32_bf16 v[44:47], a[216:219], a[88:91], v[44:47]\n" \
"s_add_u32 s55, s46, 916480\n" \
"buffer_store_dwordx4 v[172:175], %[vxs], %[srdz], s55 offen offset:0 sc1\n" \
"v_mfma_f32_16x16x32_bf16 v[44:47], a[224:227], a[92:95], v[44:47]\n" \
"v_mfma_f32_16x16x32_bf16 v[76:79], a[216:219], a[104:107], v[76:79]\n" \
"buffer_load_dwordx4 v213, %[srdt], s49 offen offset:2048 lds\n" \
"v_mfma_f32_16x16x32_bf16 v[76:79], a[224:227], a[108:111], v[76:79]\n" \
"v_mfma_f32_16x16x32_bf16 v[108:111], a[216:219], a[120:123], v[108:111]\n" \
"ds_read_b64_tr_b16 a[220:221], v227 offset:8704\n" \
"v_accvgpr_read_b32 v129, a240\n" \
"v_mfma_f32_16x16x32_bf16 v[108:111], a[224:227], a[124:127], v[108:111]\n" \
"s_waitcnt vmcnt(63) lgkmcnt(5)\n" \
"s_nop 0\n" \
"v_mfma_f32_16x16x32_bf16 v[20:23], a[228:231], a[64:67], v[20:23]\n" \
"ds_read_b64_tr_b16 a[222:223], v227 offset:10752\n" \
"v_lshlrev_b32_e64 v128, 16, v129\n" \
"v_mfma_f32_16x16x32_bf16 v[20:23], a[236:239], a[68:71], v[20:23]\n" \
"ds_read_b64_tr_b16 a[212:213], v227 offset:12800\n" \
"v_and_b32_e32 v129, 0xffff0000, v129\n" \
"v_mfma_f32_16x16x32_bf16 v[52:55], a[228:231], a[80:83], v[52:55]\n" \
"ds_read_b64_tr_b16 a[214:215], v227 offset:14848\n" \
"v_accvgpr_read_b32 v131, a241\n" \
"v_mfma_f32_16x16x32_bf16 v[52:55], a[236:239], a[84:87], v[52:55]\n" \
"v_lshlrev_b32_e64 v130, 16, v131\n" \
"v_and_b32_e32 v131, 0xffff0000, v131\n" \
"global_load_dword v237, %[vct], %[pct]\n" \
"v_mfma_f32_16x16x32_bf16 v[84:87], a[228:231], a[96:99], v[84:87]\n" \
"v_accvgpr_read_b32 v133, a242\n" \
"v_mfma_f32_16x16x32_bf16 v[84:87], a[236:239], a[100:103], v[84:87]\n" \
"v_lshlrev_b32_e64 v132, 16, v133\n" \
"s_add_u32 m0, %[m0b], 114688\n" \
"s_mov_b32 s61, 16384\n" \
"buffer_load_dwordx4 %[vdn], s[64:67], s61 offen offset:0 lds\n" \
"v_mfma_f32_16x16x32_bf16 v[116:119], a[228:231], a[112:115], v[116:119]\n" \
"v_and_b32_e32 v133, 0xffff0000, v133\n" \
"v_mfma_f32_16x16x32_bf16 v[116:119], a[236:239], a[116:119], v[116:119]\n" \
"v_accvgpr_read_b32 v135, a243\n" \
"s_add_u32 m0, %[m0b], 49152\n" \
"s_nop 0\n" \
"s_nop 0\n" \
"buffer_load_dwordx4 v214, %[srdt], s49 offen offset:3072 lds\n" \
"v_mfma_f32_16x16x32_bf16 v[20:23], a[232:235], a[72:75], v[20:23]\n" \
"v_lshlrev_b32_e64 v134, 16, v135\n" \
"v_mfma_f32_16x16x32_bf16 v[20:23], a[196:199], a[76:79], v[20:23]\n" \
"v_and_b32_e32 v135, 0xffff0000, v135\n" \
"v_mfma_f32_16x16x32_bf16 v[52:55], a[232:235], a[88:91], v[52:55]\n" \
"v_accvgpr_read_b32 v137, a244\n" \
"v_mfma_f32_16x16x32_bf16 v[52:55], a[196:199], a[92:95], v[52:55]\n" \
"v_lshlrev_b32_e64 v136, 16, v137\n" \
"v_mfma_f32_16x16x32_bf16 v[84:87], a[232:235], a[104:107], v[84:87]\n" \
"s_waitcnt vmcnt(23) lgkmcnt(0)\n" \
"s_barrier\n" \
"v_mfma_f32_16x16x32_bf16 v[84:87], a[196:199], a[108:111], v[84:87]\n" \
"ds_read_b64_tr_b16 a[208:209], v215 offset:16384\n" \
"v_and_b32_e32 v137, 0xffff0000, v137\n" \
"v_mfma_f32_16x16x32_bf16 v[116:119], a[232:235], a[120:123], v[116:119]\n" \
"ds_read_b64_tr_b16 a[210:211], v215 offset:18432\n" \
"v_accvgpr_read_b32 v139, a245\n" \
"v_mfma_f32_16x16x32_bf16 v[116:119], a[196:199], a[124:127], v[116:119]\n" \
"s_waitcnt vmcnt(63) lgkmcnt(2)\n" \
"s_nop 0\n" \
"v_mfma_f32_16x16x32_bf16 v[28:31], a[200:203], a[64:67], v[28:31]\n" \
"ds_read_b64_tr_b16 a[216:217], v215 offset:20480\n" \
"v_lshlrev_b32_e64 v138, 16, v139\n" \
"v_mfma_f32_16x16x32_bf16 v[28:31], a[204:207], a[68:71], v[28:31]\n" \
"ds_read_b64_tr_b16 a[218:219], v215 offset:22528\n" \
"v_and_b32_e32 v139, 0xffff0000, v139\n" \
"v_mfma_f32_16x16x32_bf16 v[60:63], a[200:203], a[80:83], v[60:63]\n" \
"ds_read_b64_tr_b16 a[224:225], v215 offset:24576\n" \
"v_accvgpr_read_b32 v141, a246\n" \
"v_mfma_f32_16x16x32_bf16 v[60:63], a[204:207], a[84:87], v[60:63]\n" \
"ds_read_b64_tr_b16 a[226:227], v215 offset:26624\n" \
"v_lshlrev_b32_e64 v140, 16, v141\n" \
"v_mfma_f32_16x16x32_bf16 v[92:95], a[200:203], a[96:99], v[92:95]\n" \
"ds_read_b64_tr_b16 a[228:229], v215 offset:28672\n" \
"v_and_b32_e32 v141, 0xffff0000, v141\n" \
"s_add_u32 m0, %[m0b], 115712\n" \
"s_mov_b32 s61, 20480\n" \
"buffer_load_dwordx4 %[vdn], s[64:67], s61 offen offset:0 lds\n" \
"v_mfma_f32_16x16x32_bf16 v[92:95], a[204:207], a[100:103], v[92:95]\n" \
"ds_read_b64_tr_b16 a[230:231], v215 offset:30720\n" \
"v_accvgpr_read_b32 v143, a247\n" \
"v_mfma_f32_16x16x32_bf16 v[124:127], a[200:203], a[112:115], v[124:127]\n" \
"ds_read_b64_tr_b16 a[236:237], v227 offset:16384\n" \
"v_lshlrev_b32_e64 v142, 16, v143\n" \
"v_mfma_f32_16x16x32_bf16 v[124:127], a[204:207], a[116:119], v[124:127]\n" \
"ds_read_b64_tr_b16 a[238:239], v227 offset:18432\n" \
"v_and_b32_e32 v143, 0xffff0000, v143\n" \
"v_mfma_f32_16x16x32_bf16 v[28:31], a[220:223], a[72:75], v[28:31]\n" \
"ds_read_b64_tr_b16 a[232:233], v227 offset:20480\n" \
"v_accvgpr_read_b32 v145, a248\n" \
"v_mfma_f32_16x16x32_bf16 v[28:31], a[212:215], a[76:79], v[28:31]\n" \
"ds_read_b64_tr_b16 a[234:235], v227 offset:22528\n" \
"v_lshlrev_b32_e64 v144, 16, v145\n" \
"v_mfma_f32_16x16x32_bf16 v[60:63], a[220:223], a[88:91], v[60:63]\n" \
"v_and_b32_e32 v145, 0xffff0000, v145\n" \
"v_accvgpr_read_b32 v147, a249\n" \
"v_mfma_f32_16x16x32_bf16 v[60:63], a[212:215], a[92:95], v[60:63]\n" \
"v_lshlrev_b32_e64 v146, 16, v147\n" \
"v_and_b32_e32 v147, 0xffff0000, v147\n" \
"v_mfma_f32_16x16x32_bf16 v[92:95], a[220:223], a[104:107], v[92:95]\n" \
"v_accvgpr_read_b32 v149, a250\n" \
"v_mfma_f32_16x16x32_bf16 v[92:95], a[212:215], a[108:111], v[92:95]\n" \
"v_lshlrev_b32_e64 v148, 16, v149\n" \
"v_mfma_f32_16x16x32_bf16 v[124:127], a[220:223], a[120:123], v[124:127]\n" \
"ds_read_b64_tr_b16 a[196:197], v227 offset:24576\n" \
"v_and_b32_e32 v149, 0xffff0000, v149\n" \
"v_mfma_f32_16x16x32_bf16 v[124:127], a[212:215], a[124:127], v[124:127]\n" \
"s_waitcnt vmcnt(63) lgkmcnt(5)\n" \
"s_nop 0\n" \
"v_mfma_f32_16x16x32_bf16 v[4:7], a[208:211], v[0:3], v[4:7]\n" \
"ds_read_b64_tr_b16 a[198:199], v227 offset:26624\n" \
"v_accvgpr_read_b32 v151, a251\n" \
"v_mfma_f32_16x16x32_bf16 v[4:7], a[216:219], v[8:11], v[4:7]\n" \
"ds_read_b64_tr_b16 a[200:201], v227 offset:28672\n" \
"v_lshlrev_b32_e64 v150, 16, v151\n" \
"v_mfma_f32_16x16x32_bf16 v[36:39], a[208:211], v[32:35], v[36:39]\n" \
"ds_read_b64_tr_b16 a[202:203], v227 offset:30720\n" \
"v_and_b32_e32 v151, 0xffff0000, v151\n" \
"v_mfma_f32_16x16x32_bf16 v[36:39], a[216:219], v[40:43], v[36:39]\n" \
"v_accvgpr_read_b32 v153, a252\n" \
"v_lshlrev_b32_e64 v152, 16, v153\n" \
"s_add_u32 m0, %[m0b], 116736\n" \
"s_mov_b32 s61, 24576\n" \
"buffer_load_dwordx4 %[vdn], s[64:67], s61 offen offset:0 lds\n" \
"v_mfma_f32_16x16x32_bf16 v[68:71], a[208:211], v[64:67], v[68:71]\n" \
"v_and_b32_e32 v153, 0xffff0000, v153\n" \
"v_mfma_f32_16x16x32_bf16 v[68:71], a[216:219], v[72:75], v[68:71]\n" \
"v_accvgpr_read_b32 v155, a253\n" \
"v_mfma_f32_16x16x32_bf16 v[100:103], a[208:211], v[96:99], v[100:103]\n" \
"v_lshlrev_b32_e64 v154, 16, v155\n" \
"v_mfma_f32_16x16x32_bf16 v[100:103], a[216:219], v[104:107], v[100:103]\n" \
"v_and_b32_e32 v155, 0xffff0000, v155\n" \
"v_mfma_f32_16x16x32_bf16 v[4:7], a[224:227], v[16:19], v[4:7]\n" \
"v_accvgpr_read_b32 v157, a254\n" \
"v_mfma_f32_16x16x32_bf16 v[4:7], a[228:231], v[24:27], v[4:7]\n" \
"v_lshlrev_b32_e64 v156, 16, v157\n" \
"v_mfma_f32_16x16x32_bf16 v[36:39], a[224:227], v[48:51], v[36:39]\n" \
"v_and_b32_e32 v157, 0xffff0000, v157\n" \
"v_mfma_f32_16x16x32_bf16 v[36:39], a[228:231], v[56:59], v[36:39]\n" \
"v_accvgpr_read_b32 v159, a255\n" \
"v_mfma_f32_16x16x32_bf16 v[68:71], a[224:227], v[80:83], v[68:71]\n" \
"s_waitcnt vmcnt(15) lgkmcnt(15)\n" \
"s_barrier\n" \
"v_mfma_f32_16x16x32_bf16 v[68:71], a[228:231], v[88:91], v[68:71]\n" \
"ds_read_b64_tr_b16 a[204:205], v215 offset:32768\n" \
"v_lshlrev_b32_e64 v158, 16, v159\n" \
"v_mfma_f32_16x16x32_bf16 v[100:103], a[224:227], v[112:115], v[100:103]\n" \
"ds_read_b64_tr_b16 a[206:207], v215 offset:34816\n" \
"v_and_b32_e32 v159, 0xffff0000, v159\n" \
"v_mfma_f32_16x16x32_bf16 v[100:103], a[228:231], v[120:123], v[100:103]\n" \
"s_waitcnt vmcnt(63) lgkmcnt(2)\n" \
"s_nop 0\n" \
"v_mfma_f32_16x16x32_bf16 v[12:15], a[236:239], v[0:3], v[12:15]\n" \
"ds_read_b64_tr_b16 a[220:221], v215 offset:36864\n" \
"v_and_b32_e32 v208, 0x7ff, %[vx0]\n" \
"v_mfma_f32_16x16x32_bf16 v[12:15], a[232:235], v[8:11], v[12:15]\n" \
"ds_read_b64_tr_b16 a[222:223], v215 offset:38912\n" \
"v_lshlrev_b32_e64 v208, 1, v208\n" \
"v_mfma_f32_16x16x32_bf16 v[44:47], a[236:239], v[32:35], v[44:47]\n" \
"ds_read_b64_tr_b16 a[212:213], v215 offset:40960\n" \
"v_xor_b32_e32 v209, 0x100, v208\n" \
"v_mfma_f32_16x16x32_bf16 v[44:47], a[232:235], v[40:43], v[44:47]\n" \
"ds_read_b64_tr_b16 a[214:215], v215 offset:43008\n" \
"v_lshrrev_b32_e64 v210, 3, v224\n" \
"v_mfma_f32_16x16x32_bf16 v[76:79], a[236:239], v[64:67], v[76:79]\n" \
"ds_read_b64_tr_b16 a[208:209], v215 offset:45056\n" \
"v_and_b32_e64 v210, 48, v210\n" \
"v_mfma_f32_16x16x32_bf16 v[76:79], a[232:235], v[72:75], v[76:79]\n" \
"ds_read_b64_tr_b16 a[210:211], v215 offset:47104\n" \
"s_lshl_b32 s61, %[wid], 12\n" \
"s_sub_u32 s61, %[m0b], s61\n" \
"v_add_u32_e32 v208, s61, v208\n" \
"v_add_u32_e32 v209, s61, v209\n" \
"v_add_u32_e64 v210, s61, v210\n" \
"v_mfma_f32_16x16x32_bf16 v[108:111], a[236:239], v[96:99], v[108:111]\n" \
"ds_read_b64_tr_b16 a[216:217], v227 offset:32768\n" \
"v_mfma_f32_16x16x32_bf16 v[108:111], a[232:235], v[104:107], v[108:111]\n" \
"ds_read_b64_tr_b16 a[218:219], v227 offset:34816\n" \
"s_waitcnt vmcnt(12)\n" \
"s_nop 0\n" \
"v_and_or_b32 v236, v192, v193, 0\n" \
"s_add_u32 m0, %[m0b], 117760\n" \
"s_mov_b32 s61, 28672\n" \
"buffer_load_dwordx4 %[vdn], s[64:67], s61 offen offset:0 lds\n" \
"v_mfma_f32_16x16x32_bf16 v[12:15], a[196:199], v[16:19], v[12:15]\n" \
"ds_read_b64_tr_b16 a[224:225], v227 offset:36864\n" \
"v_bitop3_b32 v236, v236, v194, v195 bitop3:0x80\n" \
"v_mfma_f32_16x16x32_bf16 v[12:15], a[200:203], v[24:27], v[12:15]\n" \
"ds_read_b64_tr_b16 a[226:227], v227 offset:38912\n" \
"v_bitop3_b32 v236, v236, v196, v197 bitop3:0x80\n" \
"v_mfma_f32_16x16x32_bf16 v[44:47], a[196:199], v[48:51], v[44:47]\n" \
"v_bitop3_b32 v236, v236, v198, v199 bitop3:0x80\n" \
"v_mfma_f32_16x16x32_bf16 v[44:47], a[200:203], v[56:59], v[44:47]\n" \
"v_bitop3_b32 v236, v236, v200, v201 bitop3:0x80\n" \
"v_mfma_f32_16x16x32_bf16 v[76:79], a[196:199], v[80:83], v[76:79]\n" \
"v_bitop3_b32 v236, v236, v202, v203 bitop3:0x80\n" \
"v_mfma_f32_16x16x32_bf16 v[76:79], a[200:203], v[88:91], v[76:79]\n" \
"v_bitop3_b32 v236, v236, v204, v205 bitop3:0x80\n" \
"v_mfma_f32_16x16x32_bf16 v[108:111], a[196:199], v[112:115], v[108:111]\n" \
"ds_read_b64_tr_b16 a[228:229], v227 offset:40960\n" \
"v_bitop3_b32 v236, v236, v206, v207 bitop3:0x80\n" \
"v_mfma_f32_16x16x32_bf16 v[108:111], a[200:203], v[120:123], v[108:111]\n" \
"s_waitcnt vmcnt(63) lgkmcnt(5)\n" \
"s_nop 0\n" \
"v_mfma_f32_16x16x32_bf16 v[4:7], a[204:207], a[0:3], v[4:7]\n" \
"ds_read_b64_tr_b16 a[230:231], v227 offset:43008\n" \
"s_mov_b32 s54, 0x07060302\n" \
"v_and_b32_e64 v236, 1, v236\n" \
"v_mfma_f32_16x16x32_bf16 v[4:7], a[220:223], a[4:7], v[4:7]\n" \
"ds_read_b64_tr_b16 a[236:237], v227 offset:45056\n" \
"v_lshrrev_b32_e64 v218, 1, v231\n" \
"v_mfma_f32_16x16x32_bf16 v[36:39], a[204:207], a[16:19], v[36:39]\n" \
"ds_read_b64_tr_b16 a[238:239], v227 offset:47104\n" \
"v_cmp_eq_u32_e64 s[56:57], 0, v236\n" \
"v_mfma_f32_16x16x32_bf16 v[36:39], a[220:223], a[20:23], v[36:39]\n" \
"s_waitcnt vmcnt(7)\n" \
"v_mov_b32_e32 v230, 1\n" \
"buffer_store_dword v230, v225, %[srdz], s62 offen offset:68 sc1\n" \
"v_mfma_f32_16x16x32_bf16 v[68:71], a[204:207], a[32:35], v[68:71]\n" \
"s_lshl_b32 s61, %[wid], 12\n" \
"s_sub_u32 s61, %[m0b], s61\n" \
"v_add_u32_e32 v231, s61, v231\n" \
"v_add_u32_e32 v218, s61, v218\n" \
"v_mov_b32_e64 v219, s61\n" \
"ds_write_b128 v231, v[192:195] offset:0\n" \
"v_mfma_f32_16x16x32_bf16 v[68:71], a[220:223], a[36:39], v[68:71]\n" \
"v_lshlrev_b32_e64 v234, 16, v233\n" \
"v_and_b32_e32 v235, 0xffff0000, v233\n" \
"v_mfma_f32_16x16x32_bf16 v[100:103], a[204:207], a[48:51], v[100:103]\n" \
"v_and_b32_e32 v233, 0xffff0000, v232\n" \
"v_mfma_f32_16x16x32_bf16 v[100:103], a[220:223], a[52:55], v[100:103]\n" \
"v_lshlrev_b32_e64 v232, 16, v232\n" \
"v_mfma_f32_16x16x32_bf16 v[4:7], a[212:215], a[8:11], v[4:7]\n" \
"v_mul_f32_e64 v234, v202, v234\n" \
"v_mfma_f32_16x16x32_bf16 v[4:7], a[208:211], a[12:15], v[4:7]\n" \
"v_mul_f32_e64 v235, v203, v235\n" \
"v_mfma_f32_16x16x32_bf16 v[36:39], a[212:215], a[24:27], v[36:39]\n" \
"v_mul_f32_e64 v233, v201, v233\n" \
"v_mfma_f32_16x16x32_bf16 v[36:39], a[208:211], a[28:31], v[36:39]\n" \
"v_mul_f32_e64 v232, v200, v232\n" \
"v_mfma_f32_16x16x32_bf16 v[68:71], a[212:215], a[40:43], v[68:71]\n" \
"s_waitcnt vmcnt(4) lgkmcnt(15)\n" \
"s_barrier\n" \
"v_mfma_f32_16x16x32_bf16 v[68:71], a[208:211], a[44:47], v[68:71]\n" \
"ds_read_b64_tr_b16 a[232:233], v215 offset:49152\n" \
"v_add_f32_e64 v223, v234, v235\n" \
"v_mfma_f32_16x16x32_bf16 v[100:103], a[212:215], a[56:59], v[100:103]\n" \
"ds_read_b64_tr_b16 a[234:235], v215 offset:51200\n" \
"v_add_f32_e64 v222, v232, v233\n" \
"v_mfma_f32_16x16x32_bf16 v[100:103], a[208:211], a[60:63], v[100:103]\n" \
"s_waitcnt vmcnt(63) lgkmcnt(3)\n" \
"s_nop 0\n" \
"v_mfma_f32_16x16x32_bf16 v[12:15], a[216:219], a[0:3], v[12:15]\n" \
"ds_read_b64_tr_b16 a[196:197], v215 offset:53248\n" \
"ds_write_b128 v231, v[232:235] offset:4096\n" \
"v_mfma_f32_16x16x32_bf16 v[12:15], a[224:227], a[4:7], v[12:15]\n" \
"ds_read_b64_tr_b16 a[198:199], v215 offset:55296\n" \
"v_add_f32_e64 v222, v222, v223\n" \
"v_mfma_f32_16x16x32_bf16 v[44:47], a[216:219], a[16:19], v[44:47]\n" \
"ds_read_b64_tr_b16 a[200:201], v215 offset:57344\n" \
"v_perm_b32 v196, v197, v196, s54\n" \
"v_mfma_f32_16x16x32_bf16 v[44:47], a[224:227], a[20:23], v[44:47]\n" \
"ds_read_b64_tr_b16 a[202:203], v215 offset:59392\n" \
"v_add_f32_dpp v222, v222, v222 quad_perm:[1,0,3,2] row_mask:0xf bank_mask:0xf\n" \
"v_mfma_f32_16x16x32_bf16 v[76:79], a[216:219], a[32:35], v[76:79]\n" \
"ds_read_b64_tr_b16 a[204:205], v215 offset:61440\n" \
"v_perm_b32 v197, v199, v198, s54\n" \
"v_mfma_f32_16x16x32_bf16 v[76:79], a[224:227], a[36:39], v[76:79]\n" \
"ds_read_b64_tr_b16 a[206:207], v215 offset:63488\n" \
"v_add_f32_dpp v222, v222, v222 quad_perm:[2,3,0,1] row_mask:0xf bank_mask:0xf\n" \
"v_mfma_f32_16x16x32_bf16 v[108:111], a[216:219], a[48:51], v[108:111]\n" \
"ds_read_b64_tr_b16 a[220:221], v227 offset:49152\n" \
"ds_write_b64 v218, v[196:197] offset:8192\n" \
"v_mfma_f32_16x16x32_bf16 v[108:111], a[224:227], a[52:55], v[108:111]\n" \
"ds_read_b64_tr_b16 a[222:223], v227 offset:51200\n" \
"v_add_f32_dpp v222, v222, v222 row_ror:4 row_mask:0xf bank_mask:0xf\n" \
"v_mfma_f32_16x16x32_bf16 v[12:15], a[228:231], a[8:11], v[12:15]\n" \
"ds_read_b64_tr_b16 a[212:213], v227 offset:53248\n" \
"v_perm_b32 v200, v201, v200, s54\n" \
"v_mfma_f32_16x16x32_bf16 v[12:15], a[236:239], a[12:15], v[12:15]\n" \
"ds_read_b64_tr_b16 a[214:215], v227 offset:55296\n" \
"v_add_f32_dpp v222, v222, v222 row_ror:8 row_mask:0xf bank_mask:0xf\n" \
"v_mfma_f32_16x16x32_bf16 v[44:47], a[228:231], a[24:27], v[44:47]\n" \
"v_perm_b32 v201, v203, v202, s54\n" \
"v_mov_b32_e64 v223, v222\n" \
"v_mfma_f32_16x16x32_bf16 v[44:47], a[236:239], a[28:31], v[44:47]\n" \
"ds_write_b64 v218, v[200:201] offset:10240\n" \
"s_nop 0\n" \
"v_permlane16_swap_b32_e32 v222, v223\n" \
"v_mfma_f32_16x16x32_bf16 v[76:79], a[228:231], a[40:43], v[76:79]\n" \
"v_perm_b32 v204, v205, v204, s54\n" \
"v_mfma_f32_16x16x32_bf16 v[76:79], a[236:239], a[44:47], v[76:79]\n" \
"v_add_f32_e64 v222, v222, v223\n" \
"v_mfma_f32_16x16x32_bf16 v[108:111], a[228:231], a[56:59], v[108:111]\n" \
"ds_read_b64_tr_b16 a[208:209], v227 offset:57344\n" \
"v_perm_b32 v205, v207, v206, s54\n" \
"v_mfma_f32_16x16x32_bf16 v[108:111], a[236:239], a[60:63], v[108:111]\n" \
"s_waitcnt vmcnt(63) lgkmcnt(7)\n" \
"s_nop 0\n" \
"v_mfma_f32_16x16x32_bf16 v[4:7], a[232:235], a[128:131], v[4:7]\n" \
"ds_read_b64_tr_b16 a[210:211], v227 offset:59392\n" \
"v_mov_b32_e64 v223, v222\n" \
"v_mfma_f32_16x16x32_bf16 v[4:7], a[196:199], a[132:135], v[4:7]\n" \
"ds_read_b64_tr_b16 a[216:217], v227 offset:61440\n" \
"ds_write_b64 v218, v[204:205] offset:14336\n" \
"v_mfma_f32_16x16x32_bf16 v[36:39], a[232:235], a[144:147], v[36:39]\n" \
"ds_read_b64_tr_b16 a[218:219], v227 offset:63488\n" \
"s_nop 0\n" \
"v_permlane32_swap_b32_e32 v222, v223\n" \
"v_mfma_f32_16x16x32_bf16 v[36:39], a[196:199], a[148:151], v[36:39]\n" \
"v_lshl_add_u32 v219, %[wid], 4, v219\n" \
"v_add_f32_e64 v222, v222, v223\n" \
"v_mfma_f32_16x16x32_bf16 v[68:71], a[232:235], a[160:163], v[68:71]\n" \
"s_cmp_lg_u64 s[56:57], 0\n" \
"s_cselect_b32 s61, 1, 0\n" \
"v_mov_b32_e64 v192, s61\n" \
"v_mfma_f32_16x16x32_bf16 v[68:71], a[196:199], a[164:167], v[68:71]\n" \
"v_mov_b32_e64 v193, v192\n" \
"v_mfma_f32_16x16x32_bf16 v[100:103], a[232:235], a[176:179], v[100:103]\n" \
"ds_read_b64_tr_b16 a[224:225], v215 offset:16896\n" \
"ds_write_b32 v219, v222 offset:12424\n" \
"v_mfma_f32_16x16x32_bf16 v[100:103], a[196:199], a[180:183], v[100:103]\n" \
"ds_read_b64_tr_b16 a[226:227], v215 offset:18944\n" \
"ds_write_b64 v219, v[192:193] offset:12416\n" \
"v_mfma_f32_16x16x32_bf16 v[4:7], a[200:203], a[136:139], v[4:7]\n" \
"ds_read_b64_tr_b16 a[228:229], v215 offset:20992\n" \
"v_mfma_f32_16x16x32_bf16 v[4:7], a[204:207], a[140:143], v[4:7]\n" \
"ds_read_b64_tr_b16 a[230:231], v215 offset:23040\n" \
"v_mfma_f32_16x16x32_bf16 v[36:39], a[200:203], a[152:155], v[36:39]\n" \
"v_mfma_f32_16x16x32_bf16 v[36:39], a[204:207], a[156:159], v[36:39]\n" \
"v_mfma_f32_16x16x32_bf16 v[68:71], a[200:203], a[168:171], v[68:71]\n" \
"v_mfma_f32_16x16x32_bf16 v[68:71], a[204:207], a[172:175], v[68:71]\n" \
"v_mfma_f32_16x16x32_bf16 v[100:103], a[200:203], a[184:187], v[100:103]\n" \
"ds_read_b64_tr_b16 a[236:237], v215 offset:25088\n" \
"v_mfma_f32_16x16x32_bf16 v[100:103], a[204:207], a[188:191], v[100:103]\n" \
"s_waitcnt vmcnt(63) lgkmcnt(3)\n" \
"s_barrier\n" \
"v_mfma_f32_16x16x32_bf16 v[12:15], a[220:223], a[128:131], v[12:15]\n" \
"ds_read_b64_tr_b16 a[238:239], v215 offset:27136\n" \
"ds_read_b128 v[204:207], v210 offset:12416\n" \
"v_mfma_f32_16x16x32_bf16 v[12:15], a[212:215], a[132:135], v[12:15]\n" \
"ds_read_b64_tr_b16 a[232:233], v215 offset:29184\n" \
"v_lshrrev_b32_e64 v200, 3, v224\n" \
"v_mfma_f32_16x16x32_bf16 v[44:47], a[220:223], a[144:147], v[44:47]\n" \
"ds_read_b64_tr_b16 a[234:235], v215 offset:31232\n" \
"ds_read_b128 v[160:163], v208 offset:0\n" \
"v_mfma_f32_16x16x32_bf16 v[44:47], a[212:215], a[148:151], v[44:47]\n" \
"v_bfe_u32 v201, v224, 8, 3\n" \
".if . - adaln_body != 57344\n" \
".error \"size model\"\n" \
".endif\n" \
"Lk13_%=:\n" \
"s_cbranch_execz adaln_pg0\n" \
"s_nop 0\n" \
"v_and_b32_e64 v200, 16, v200\n" \
"v_mfma_f32_16x16x32_bf16 v[76:79], a[220:223], a[160:163], v[76:79]\n" \
"ds_read_b128 v[164:167], v208 offset:2048\n" \
"v_lshlrev_b32_e64 v201, v201, 1\n" \
"v_mfma_f32_16x16x32_bf16 v[76:79], a[212:215], a[164:167], v[76:79]\n" \
"s_movk_i32 s61, 0x3f80\n" \
"s_nop 0\n" \
"v_lshlrev_b32_e64 v200, v200, s61\n" \
"v_readlane_b32 s68, %[thi], 18\n" \
"v_lshlrev_b32_e64 v226, 7, v226\n" \
"v_mfma_f32_16x16x32_bf16 v[108:111], a[220:223], a[176:179], v[108:111]\n" \
"ds_read_b64_tr_b16 a[196:197], v227 offset:16896\n" \
"ds_read_b128 v[168:171], v208 offset:128\n" \
"v_mfma_f32_16x16x32_bf16 v[108:111], a[212:215], a[180:183], v[108:111]\n" \
"ds_read_b64_tr_b16 a[198:199], v227 offset:18944\n" \
"v_bfe_i32 v192, v201, 0, 1\n" \
"v_mfma_f32_16x16x32_bf16 v[12:15], a[208:211], a[136:139], v[12:15]\n" \
"ds_read_b64_tr_b16 a[200:201], v227 offset:20992\n" \
"ds_read_b128 v[172:175], v208 offset:2176\n" \
"v_mfma_f32_16x16x32_bf16 v[12:15], a[216:219], a[140:143], v[12:15]\n" \
"ds_read_b64_tr_b16 a[202:203], v227 offset:23040\n" \
"v_and_b32_e64 v192, v200, v192\n" \
"v_mfma_f32_16x16x32_bf16 v[44:47], a[208:211], a[152:155], v[44:47]\n" \
"ds_read_b128 v[176:179], v209 offset:0\n" \
"v_bfe_i32 v193, v201, 1, 1\n" \
"v_mfma_f32_16x16x32_bf16 v[44:47], a[216:219], a[156:159], v[44:47]\n" \
"v_and_b32_e64 v193, v200, v193\n" \
"v_bfe_i32 v194, v201, 2, 1\n" \
"v_mfma_f32_16x16x32_bf16 v[76:79], a[208:211], a[168:171], v[76:79]\n" \
"ds_read_b128 v[180:183], v209 offset:2048\n" \
"v_and_b32_e64 v194, v200, v194\n" \
"v_mfma_f32_16x16x32_bf16 v[76:79], a[216:219], a[172:175], v[76:79]\n" \
"v_bfe_i32 v195, v201, 3, 1\n" \
"v_and_b32_e64 v195, v200, v195\n" \
"buffer_load_dword v216, v226, %[srdz], s68 offen offset:68 sc1\n" \
"v_mfma_f32_16x16x32_bf16 v[108:111], a[208:211], a[184:187], v[108:111]\n" \
"ds_read_b64_tr_b16 a[204:205], v227 offset:25088\n" \
"ds_read_b128 v[184:187], v209 offset:128\n" \
"v_lshrrev_b32_e64 v217, 3, v224\n" \
"s_add_u32 s51, %[sin], 4096\n" \
"v_mfma_f32_16x16x32_bf16 v[108:111], a[216:219], a[188:191], v[108:111]\n" \
"s_waitcnt vmcnt(63) lgkmcnt(12)\n" \
"s_nop 0\n" \
"v_mfma_f32_16x16x32_bf16 v[20:23], a[224:227], v[0:3], v[20:23]\n" \
"ds_read_b64_tr_b16 a[206:207], v227 offset:27136\n" \
"ds_read_b128 v[188:191], v209 offset:2176\n" \
"v_mfma_f32_16x16x32_bf16 v[20:23], a[228:231], v[8:11], v[20:23]\n" \
"ds_read_b64_tr_b16 a[220:221], v227 offset:29184\n" \
"v_bfe_i32 v196, v201, 4, 1\n" \
"v_mfma_f32_16x16x32_bf16 v[52:55], a[224:227], v[32:35], v[52:55]\n" \
"ds_read_b64_tr_b16 a[222:223], v227 offset:31232\n" \
"v_and_b32_e64 v196, v200, v196\n" \
"v_mfma_f32_16x16x32_bf16 v[52:55], a[228:231], v[40:43], v[52:55]\n" \
"v_bfe_i32 v197, v201, 5, 1\n" \
"v_and_b32_e64 v197, v200, v197\n" \
"v_mfma_f32_16x16x32_bf16 v[84:87], a[224:227], v[64:67], v[84:87]\n" \
"v_cvt_pk_bf16_f32 v4, v4, v5\n" \
"v_bfe_i32 v198, v201, 6, 1\n" \
"v_mfma_f32_16x16x32_bf16 v[84:87], a[228:231], v[72:75], v[84:87]\n" \
"v_cvt_pk_bf16_f32 v5, v6, v7\n" \
"v_and_b32_e64 v198, v200, v198\n" \
"v_mfma_f32_16x16x32_bf16 v[116:119], a[224:227], v[96:99], v[116:119]\n" \
"ds_read_b64_tr_b16 a[212:213], v215 offset:33280\n" \
"v_cvt_pk_bf16_f32 v6, v12, v13\n" \
"v_mfma_f32_16x16x32_bf16 v[116:119], a[228:231], v[104:107], v[116:119]\n" \
"ds_read_b64_tr_b16 a[214:215], v215 offset:35328\n" \
"v_cvt_pk_bf16_f32 v7, v14, v15\n" \
"v_mfma_f32_16x16x32_bf16 v[20:23], a[236:239], v[16:19], v[20:23]\n" \
"ds_read_b64_tr_b16 a[208:209], v215 offset:37376\n" \
"v_bfe_i32 v199, v201, 7, 1\n" \
"v_mfma_f32_16x16x32_bf16 v[20:23], a[232:235], v[24:27], v[20:23]\n" \
"ds_read_b64_tr_b16 a[210:211], v215 offset:39424\n" \
"v_and_b32_e64 v199, v200, v199\n" \
"v_mfma_f32_16x16x32_bf16 v[52:55], a[236:239], v[48:51], v[52:55]\n" \
"s_add_u32 s55, s46, 8192\n" \
"buffer_store_dwordx4 v[4:7], %[vxs], %[srdz], s55 offen offset:0 sc1\n" \
"v_mfma_f32_16x16x32_bf16 v[52:55], a[232:235], v[56:59], v[52:55]\n" \
"v_cvt_pk_bf16_f32 v36, v36, v37\n" \
"v_cvt_pk_bf16_f32 v37, v38, v39\n" \
"v_mfma_f32_16x16x32_bf16 v[84:87], a[236:239], v[80:83], v[84:87]\n" \
"v_cvt_pk_bf16_f32 v38, v44, v45\n" \
"s_waitcnt lgkmcnt(14)\n" \
"s_nop 0\n" \
"v_add_f32_dpp v206, v206, v206 quad_perm:[1,0,3,2] row_mask:0xf bank_mask:0xf\n" \
"v_mfma_f32_16x16x32_bf16 v[84:87], a[232:235], v[88:91], v[84:87]\n" \
"v_cvt_pk_bf16_f32 v39, v46, v47\n" \
"v_mfma_f32_16x16x32_bf16 v[116:119], a[236:239], v[112:115], v[116:119]\n" \
"ds_read_b64_tr_b16 a[216:217], v215 offset:41472\n" \
"v_add_f32_dpp v206, v206, v206 quad_perm:[2,3,0,1] row_mask:0xf bank_mask:0xf\n" \
"v_mfma_f32_16x16x32_bf16 v[116:119], a[232:235], v[120:123], v[116:119]\n" \
"s_waitcnt vmcnt(63) lgkmcnt(5)\n" \
"s_nop 0\n" \
"v_mfma_f32_16x16x32_bf16 v[28:31], a[196:199], v[0:3], v[28:31]\n" \
"ds_read_b64_tr_b16 a[218:219], v215 offset:43520\n" \
"v_cvt_pk_bf16_f32 v68, v68, v69\n" \
"v_mfma_f32_16x16x32_bf16 v[28:31], a[200:203], v[8:11], v[28:31]\n" \
"ds_read_b64_tr_b16 a[224:225], v215 offset:45568\n" \
"s_add_u32 s55, s46, 312320\n" \
"buffer_store_dwordx4 v[36:39], %[vxs], %[srdz], s55 offen offset:0 sc1\n" \
"v_mfma_f32_16x16x32_bf16 v[60:63], a[196:199], v[32:35], v[60:63]\n" \
"ds_read_b64_tr_b16 a[226:227], v215 offset:47616\n" \
"v_cvt_pk_bf16_f32 v69, v70, v71\n" \
"v_mfma_f32_16x16x32_bf16 v[60:63], a[200:203], v[40:43], v[60:63]\n" \
"v_cvt_pk_bf16_f32 v70, v76, v77\n" \
"v_cvt_pk_bf16_f32 v71, v78, v79\n" \
"v_mfma_f32_16x16x32_bf16 v[92:95], a[196:199], v[64:67], v[92:95]\n" \
"s_add_u32 s55, s46, 616448\n" \
"buffer_store_dwordx4 v[68:71], %[vxs], %[srdz], s55 offen offset:0 sc1\n" \
"v_mfma_f32_16x16x32_bf16 v[92:95], a[200:203], v[72:75], v[92:95]\n" \
"v_mfma_f32_16x16x32_bf16 v[124:127], a[196:199], v[96:99], v[124:127]\n" \
"ds_read_b64_tr_b16 a[228:229], v227 offset:33280\n" \
"v_cvt_pk_bf16_f32 v100, v100, v101\n" \
"v_mfma_f32_16x16x32_bf16 v[124:127], a[200:203], v[104:107], v[124:127]\n" \
"ds_read_b64_tr_b16 a[230:231], v227 offset:35328\n" \
"v_cvt_pk_bf16_f32 v101, v102, v103\n" \
"v_mfma_f32_16x16x32_bf16 v[28:31], a[204:207], v[16:19], v[28:31]\n" \
"ds_read_b64_tr_b16 a[236:237], v227 offset:37376\n" \
"v_cvt_pk_bf16_f32 v102, v108, v109\n" \
"v_mfma_f32_16x16x32_bf16 v[28:31], a[220:223], v[24:27], v[28:31]\n" \
"ds_read_b64_tr_b16 a[238:239], v227 offset:39424\n" \
"v_cvt_pk_bf16_f32 v103, v110, v111\n" \
"v_mfma_f32_16x16x32_bf16 v[60:63], a[204:207], v[48:51], v[60:63]\n" \
"s_add_u32 s55, s46, 920576\n" \
"buffer_store_dwordx4 v[100:103], %[vxs], %[srdz], s55 offen offset:0 sc1\n" \
"v_mfma_f32_16x16x32_bf16 v[60:63], a[220:223], v[56:59], v[60:63]\n" \
"v_mfma_f32_16x16x32_bf16 v[92:95], a[204:207], v[80:83], v[92:95]\n" \
"v_mov_b32_e64 v230, 1\n" \
"buffer_store_dword v230, v225, %[srdz], s62 offen offset:64 sc1\n" \
"v_mfma_f32_16x16x32_bf16 v[92:95], a[220:223], v[88:91], v[92:95]\n" \
"ds_read_b128 v[4:7], v208 offset:4096\n" \
"v_mfma_f32_16x16x32_bf16 v[124:127], a[204:207], v[112:115], v[124:127]\n" \
"ds_read_b64_tr_b16 a[232:233], v227 offset:41472\n" \
"v_mfma_f32_16x16x32_bf16 v[124:127], a[220:223], v[120:123], v[124:127]\n" \
"s_waitcnt vmcnt(63) lgkmcnt(6)\n" \
"s_nop 0\n" \
"v_mfma_f32_16x16x32_bf16 v[20:23], a[212:215], a[0:3], v[20:23]\n" \
"ds_read_b64_tr_b16 a[234:235], v227 offset:43520\n" \
"ds_read_b128 v[12:15], v208 offset:6144\n" \
"v_mfma_f32_16x16x32_bf16 v[20:23], a[208:211], a[4:7], v[20:23]\n" \
"ds_read_b64_tr_b16 a[196:197], v227 offset:45568\n" \
"v_mfma_f32_16x16x32_bf16 v[52:55], a[212:215], a[16:19], v[52:55]\n" \
"ds_read_b64_tr_b16 a[198:199], v227 offset:47616\n" \
"v_mfma_f32_16x16x32_bf16 v[52:55], a[208:211], a[20:23], v[52:55]\n" \
"ds_read_b128 v[36:39], v208 offset:4224\n" \
"v_mfma_f32_16x16x32_bf16 v[84:87], a[212:215], a[32:35], v[84:87]\n" \
"v_mfma_f32_16x16x32_bf16 v[84:87], a[208:211], a[36:39], v[84:87]\n" \
"v_mfma_f32_16x16x32_bf16 v[116:119], a[212:215], a[48:51], v[116:119]\n" \
"ds_read_b64_tr_b16 a[200:201], v215 offset:49664\n" \
"ds_read_b128 v[44:47], v208 offset:6272\n" \
"v_mfma_f32_16x16x32_bf16 v[116:119], a[208:211], a[52:55], v[116:119]\n" \
"ds_read_b64_tr_b16 a[202:203], v215 offset:51712\n" \
"v_mfma_f32_16x16x32_bf16 v[20:23], a[216:219], a[8:11], v[20:23]\n" \
"ds_read_b64_tr_b16 a[204:205], v215 offset:53760\n" \
"v_mfma_f32_16x16x32_bf16 v[20:23], a[224:227], a[12:15], v[20:23]\n" \
"ds_read_b64_tr_b16 a[206:207], v215 offset:55808\n" \
"ds_read_b128 v[68:71], v209 offset:4096\n" \
"v_mfma_f32_16x16x32_bf16 v[52:55], a[216:219], a[24:27], v[52:55]\n" \
"ds_read_b64_tr_b16 a[220:221], v215 offset:57856\n" \
"v_mfma_f32_16x16x32_bf16 v[52:55], a[224:227], a[28:31], v[52:55]\n" \
"ds_read_b64_tr_b16 a[222:223], v215 offset:59904\n" \
"v_mfma_f32_16x16x32_bf16 v[84:87], a[216:219], a[40:43], v[84:87]\n" \
"ds_read_b64_tr_b16 a[212:213], v215 offset:61952\n" \
"ds_read_b128 v[76:79], v209 offset:6144\n" \
"v_mfma_f32_16x16x32_bf16 v[84:87], a[224:227], a[44:47], v[84:87]\n" \
"ds_read_b64_tr_b16 a[214:215], v215 offset:64000\n" \
"v_mfma_f32_16x16x32_bf16 v[116:119], a[216:219], a[56:59], v[116:119]\n" \
"ds_read_b64_tr_b16 a[208:209], v227 offset:49664\n" \
"v_mfma_f32_16x16x32_bf16 v[116:119], a[224:227], a[60:63], v[116:119]\n" \
"s_waitcnt vmcnt(63) lgkmcnt(13)\n" \
"s_nop 0\n" \
"v_mfma_f32_16x16x32_bf16 v[28:31], a[228:231], a[0:3], v[28:31]\n" \
"ds_read_b64_tr_b16 a[210:211], v227 offset:51712\n" \
"ds_read_b128 v[100:103], v209 offset:4224\n" \
"v_mfma_f32_16x16x32_bf16 v[28:31], a[236:239], a[4:7], v[28:31]\n" \
"ds_read_b64_tr_b16 a[216:217], v227 offset:53760\n" \
"v_mfma_f32_16x16x32_bf16 v[60:63], a[228:231], a[16:19], v[60:63]\n" \
"ds_read_b64_tr_b16 a[218:219], v227 offset:55808\n" \
"ds_read_b128 v[108:111], v209 offset:6272\n" \
"v_mfma_f32_16x16x32_bf16 v[60:63], a[236:239], a[20:23], v[60:63]\n" \
"ds_read_b64_tr_b16 a[224:225], v227 offset:57856\n" \
"v_mfma_f32_16x16x32_bf16 v[92:95], a[228:231], a[32:35], v[92:95]\n" \
"ds_read_b64_tr_b16 a[226:227], v227 offset:59904\n" \
"v_mfma_f32_16x16x32_bf16 v[92:95], a[236:239], a[36:39], v[92:95]\n" \
"v_mfma_f32_16x16x32_bf16 v[124:127], a[228:231], a[48:51], v[124:127]\n" \
"v_mfma_f32_16x16x32_bf16 v[124:127], a[236:239], a[52:55], v[124:127]\n" \
"ds_read_b64_tr_b16 a[228:229], v227 offset:61952\n" \
"v_mfma_f32_16x16x32_bf16 v[28:31], a[232:235], a[8:11], v[28:31]\n" \
"ds_read_b64_tr_b16 a[230:231], v227 offset:64000\n" \
"s_waitcnt vmcnt(5)\n" \
"v_cmp_eq_u32_e32 vcc, 0, v216\n" \
"s_nop 0\n" \
"s_cmp_eq_u64 vcc, 0\n" \
"s_cselect_b32 %[got], 1, 0\n" \
"s_nop 0\n" \
"s_cselect_b32 s54, %[sin], 0x80000000\n" \
"s_cselect_b32 s51, s51, 0x80000000\n" \
"v_mfma_f32_16x16x32_bf16 v[28:31], a[196:199], a[12:15], v[28:31]\n" \
"v_mfma_f32_16x16x32_bf16 v[60:63], a[232:235], a[24:27], v[60:63]\n" \
"buffer_load_dwordx4 a[64:67], v217, %[srdz], s54 offen offset:0 sc1\n" \
"v_mfma_f32_16x16x32_bf16 v[60:63], a[196:199], a[28:31], v[60:63]\n" \
"v_mfma_f32_16x16x32_bf16 v[92:95], a[232:235], a[40:43], v[92:95]\n" \
"buffer_load_dwordx4 a[68:71], v217, %[srdz], s54 offen offset:1024 sc1\n" \
"v_mfma_f32_16x16x32_bf16 v[92:95], a[196:199], a[44:47], v[92:95]\n" \
"v_mfma_f32_16x16x32_bf16 v[124:127], a[232:235], a[56:59], v[124:127]\n" \
"buffer_load_dwordx4 a[72:75], v217, %[srdz], s54 offen offset:2048 sc1\n" \
"v_mfma_f32_16x16x32_bf16 v[124:127], a[196:199], a[60:63], v[124:127]\n" \
"s_waitcnt vmcnt(63) lgkmcnt(0)\n" \
"s_nop 0\n" \
"v_mfma_f32_16x16x32_bf16 v[20:23], a[200:203], a[128:131], v[20:23]\n" \
"buffer_load_dwordx4 a[76:79], v217, %[srdz], s54 offen offset:3072 sc1\n" \
"v_mfma_f32_16x16x32_bf16 v[20:23], a[204:207], a[132:135], v[20:23]\n" \
"v_mfma_f32_16x16x32_bf16 v[20:23], a[220:223], a[136:139], v[20:23]\n" \
"buffer_load_dwordx4 a[80:83], v217, %[srdz], s51 offen offset:0 sc1\n" \
"v_mfma_f32_16x16x32_bf16 v[20:23], a[212:215], a[140:143], v[20:23]\n" \
"v_mfma_f32_16x16x32_bf16 v[28:31], a[208:211], a[128:131], v[28:31]\n" \
"buffer_load_dwordx4 a[84:87], v217, %[srdz], s51 offen offset:1024 sc1\n" \
"v_mfma_f32_16x16x32_bf16 v[28:31], a[216:219], a[132:135], v[28:31]\n" \
"v_mfma_f32_16x16x32_bf16 v[28:31], a[224:227], a[136:139], v[28:31]\n" \
"buffer_load_dwordx4 a[88:91], v217, %[srdz], s51 offen offset:2048 sc1\n" \
"v_mfma_f32_16x16x32_bf16 v[28:31], a[228:231], a[140:143], v[28:31]\n" \
"v_mfma_f32_16x16x32_bf16 v[52:55], a[200:203], a[144:147], v[52:55]\n" \
"buffer_load_dwordx4 a[92:95], v217, %[srdz], s51 offen offset:3072 sc1\n" \
"v_mfma_f32_16x16x32_bf16 v[52:55], a[204:207], a[148:151], v[52:55]\n" \
"v_mfma_f32_16x16x32_bf16 v[52:55], a[220:223], a[152:155], v[52:55]\n" \
"v_mfma_f32_16x16x32_bf16 v[52:55], a[212:215], a[156:159], v[52:55]\n" \
"v_cvt_pk_bf16_f32 v20, v20, v21\n" \
"v_cvt_pk_bf16_f32 v21, v22, v23\n" \
"v_mfma_f32_16x16x32_bf16 v[60:63], a[208:211], a[144:147], v[60:63]\n" \
"buffer_load_dwordx2 v[228:229], v226, %[srdz], s68 offen offset:64 sc1\n" \
"v_mfma_f32_16x16x32_bf16 v[60:63], a[216:219], a[148:151], v[60:63]\n" \
"v_cvt_pk_bf16_f32 v22, v28, v29\n" \
"v_cvt_pk_bf16_f32 v23, v30, v31\n" \
"v_mfma_f32_16x16x32_bf16 v[60:63], a[224:227], a[152:155], v[60:63]\n" \
"s_add_u32 s55, s46, 12288\n" \
"buffer_store_dwordx4 v[20:23], %[vxs], %[srdz], s55 offen offset:0 sc1\n" \
"v_mfma_f32_16x16x32_bf16 v[60:63], a[228:231], a[156:159], v[60:63]\n" \
"v_mfma_f32_16x16x32_bf16 v[84:87], a[200:203], a[160:163], v[84:87]\n" \
"v_mfma_f32_16x16x32_bf16 v[84:87], a[204:207], a[164:167], v[84:87]\n" \
"v_mfma_f32_16x16x32_bf16 v[84:87], a[220:223], a[168:171], v[84:87]\n" \
"v_mfma_f32_16x16x32_bf16 v[84:87], a[212:215], a[172:175], v[84:87]\n" \
"v_cvt_pk_bf16_f32 v52, v52, v53\n" \
"v_cvt_pk_bf16_f32 v53, v54, v55\n" \
"v_mfma_f32_16x16x32_bf16 v[92:95], a[208:211], a[160:163], v[92:95]\n" \
"v_mfma_f32_16x16x32_bf16 v[92:95], a[216:219], a[164:167], v[92:95]\n" \
"v_cvt_pk_bf16_f32 v54, v60, v61\n" \
"v_cvt_pk_bf16_f32 v55, v62, v63\n" \
"v_mfma_f32_16x16x32_bf16 v[92:95], a[224:227], a[168:171], v[92:95]\n" \
"s_add_u32 s55, s46, 316416\n" \
"buffer_store_dwordx4 v[52:55], %[vxs], %[srdz], s55 offen offset:0 sc1\n" \
"v_mfma_f32_16x16x32_bf16 v[92:95], a[228:231], a[172:175], v[92:95]\n" \
"v_mfma_f32_16x16x32_bf16 v[116:119], a[200:203], a[176:179], v[116:119]\n" \
"v_mfma_f32_16x16x32_bf16 v[116:119], a[204:207], a[180:183], v[116:119]\n" \
"v_mfma_f32_16x16x32_bf16 v[116:119], a[220:223], a[184:187], v[116:119]\n" \
"v_mfma_f32_16x16x32_bf16 v[116:119], a[212:215], a[188:191], v[116:119]\n" \
"v_cvt_pk_bf16_f32 v84, v84, v85\n" \
"v_cvt_pk_bf16_f32 v85, v86, v87\n" \
"v_mfma_f32_16x16x32_bf16 v[124:127], a[208:211], a[176:179], v[124:127]\n" \
"v_mfma_f32_16x16x32_bf16 v[124:127], a[216:219], a[180:183], v[124:127]\n" \
"v_cvt_pk_bf16_f32 v86, v92, v93\n" \
"v_cvt_pk_bf16_f32 v87, v94, v95\n" \
"v_mfma_f32_16x16x32_bf16 v[124:127], a[224:227], a[184:187], v[124:127]\n" \
"s_add_u32 s55, s46, 620544\n" \
"buffer_store_dwordx4 v[84:87], %[vxs], %[srdz], s55 offen offset:0 sc1\n" \
"v_mfma_f32_16x16x32_bf16 v[124:127], a[228:231], a[188:191], v[124:127]\n" \
"v_cvt_pk_bf16_f32 v116, v116, v117\n" \
"v_cvt_pk_bf16_f32 v117, v118, v119\n" \
"s_waitcnt lgkmcnt(0)\n" \
"s_nop 4\n" \
"v_cvt_pk_bf16_f32 v118, v124, v125\n" \
"v_cvt_pk_bf16_f32 v119, v126, v127\n" \
"s_add_u32 s55, s46, 924672\n" \
"buffer_store_dwordx4 v[116:119], %[vxs], %[srdz], s55 offen offset:0 sc1\n" \
"s_branch Lend_%=\n" \
"s_nop 0\n" \
"Lsp1_%=:\n" \
"s_movk_i32 s51, 0x1000\n" \
"s_cmp_gt_u32 s48, 0\n" \
"s_cselect_b32 s61, 0, 1\n" \
"s_nop 0\n" \
"s_mul_i32 s61, s61, 152064\n" \
"Lsq1_%=:\n" \
"buffer_load_dwordx4 a[64:67], %[oq0], %[srdz], s61 offen offset:0 sc1\n" \
"buffer_load_dwordx4 a[68:71], %[oq0], %[srdz], s61 offen offset:1024 sc1\n" \
"buffer_load_dwordx4 a[72:75], %[oq0], %[srdz], s61 offen offset:2048 sc1\n" \
"buffer_load_dwordx4 a[76:79], %[oq0], %[srdz], s61 offen offset:3072 sc1\n" \
"buffer_load_dwordx4 a[80:83], v208, %[srdz], s61 offen offset:0 sc1\n" \
"buffer_load_dwordx4 a[84:87], v208, %[srdz], s61 offen offset:1024 sc1\n" \
"buffer_load_dwordx4 a[88:91], v208, %[srdz], s61 offen offset:2048 sc1\n" \
"buffer_load_dwordx4 a[92:95], v208, %[srdz], s61 offen offset:3072 sc1\n" \
"buffer_load_dwordx4 a[96:99], v209, %[srdz], s61 offen offset:0 sc1\n" \
"buffer_load_dwordx4 a[100:103], v209, %[srdz], s61 offen offset:1024 sc1\n" \
"buffer_load_dwordx4 a[104:107], v209, %[srdz], s61 offen offset:2048 sc1\n" \
"buffer_load_dwordx4 a[108:111], v209, %[srdz], s61 offen offset:3072 sc1\n" \
"buffer_load_dwordx4 a[112:115], v210, %[srdz], s61 offen offset:0 sc1\n" \
"buffer_load_dwordx4 a[116:119], v210, %[srdz], s61 offen offset:1024 sc1\n" \
"buffer_load_dwordx4 a[120:123], v210, %[srdz], s61 offen offset:2048 sc1\n" \
"buffer_load_dwordx4 a[124:127], v210, %[srdz], s61 offen offset:3072 sc1\n" \
"s_waitcnt vmcnt(0)\n" \
"s_nop 0\n" \
"v_mfma_f32_16x16x32_bf16 v[128:131], a[236:239], a[64:67], v[192:195]\n" \
"v_mfma_f32_16x16x32_bf16 v[128:131], a[196:199], a[68:71], v[128:131]\n" \
"v_mfma_f32_16x16x32_bf16 v[128:131], a[200:203], a[72:75], v[128:131]\n" \
"v_mfma_f32_16x16x32_bf16 v[128:131], a[204:207], a[76:79], v[128:131]\n" \
"v_mfma_f32_16x16x32_bf16 v[132:135], a[236:239], a[80:83], v[196:199]\n" \
"v_mfma_f32_16x16x32_bf16 v[132:135], a[196:199], a[84:87], v[132:135]\n" \
"v_mfma_f32_16x16x32_bf16 v[132:135], a[200:203], a[88:91], v[132:135]\n" \
"v_mfma_f32_16x16x32_bf16 v[132:135], a[204:207], a[92:95], v[132:135]\n" \
"v_mfma_f32_16x16x32_bf16 v[136:139], a[236:239], a[96:99], v[200:203]\n" \
"v_mfma_f32_16x16x32_bf16 v[136:139], a[196:199], a[100:103], v[136:139]\n" \
"v_mfma_f32_16x16x32_bf16 v[136:139], a[200:203], a[104:107], v[136:139]\n" \
"v_mfma_f32_16x16x32_bf16 v[136:139], a[204:207], a[108:111], v[136:139]\n" \
"v_mfma_f32_16x16x32_bf16 v[140:143], a[236:239], a[112:115], v[204:207]\n" \
"v_mfma_f32_16x16x32_bf16 v[140:143], a[196:199], a[116:119], v[140:143]\n" \
"v_mfma_f32_16x16x32_bf16 v[140:143], a[200:203], a[120:123], v[140:143]\n" \
"v_mfma_f32_16x16x32_bf16 v[140:143], a[204:207], a[124:127], v[140:143]\n" \
"s_nop 7\n" \
"s_nop 0\n" \
"v_cmp_u_f32_e64 s[44:45], v128, v132\n" \
"v_cmp_u_f32_e64 s[56:57], v136, v140\n" \
"s_or_b64 s[56:57], s[56:57], s[44:45]\n" \
"s_cbranch_scc0 Lbk1_%=\n" \
"s_sub_u32 s51, s51, 1\n" \
"s_cbranch_scc0 Lsq1_%=\n" \
"s_branch Lbk1_%=\n" \
"Lsp2_%=:\n" \
"s_movk_i32 s51, 0x1000\n" \
"s_cmp_gt_u32 s48, 1\n" \
"s_cselect_b32 s61, 1, 2\n" \
"s_mul_i32 s61, s61, 152064\n" \
"Lsq2_%=:\n" \
"buffer_load_dwordx4 a[0:3], %[oq0], %[srdz], s61 offen offset:0 sc1\n" \
"buffer_load_dwordx4 a[4:7], %[oq0], %[srdz], s61 offen offset:1024 sc1\n" \
"buffer_load_dwordx4 a[8:11], %[oq0], %[srdz], s61 offen offset:2048 sc1\n" \
"buffer_load_dwordx4 a[12:15], %[oq0], %[srdz], s61 offen offset:3072 sc1\n" \
"buffer_load_dwordx4 a[16:19], v208, %[srdz], s61 offen offset:0 sc1\n" \
"buffer_load_dwordx4 a[20:23], v208, %[srdz], s61 offen offset:1024 sc1\n" \
"buffer_load_dwordx4 a[24:27], v208, %[srdz], s61 offen offset:2048 sc1\n" \
"buffer_load_dwordx4 a[28:31], v208, %[srdz], s61 offen offset:3072 sc1\n" \
"buffer_load_dwordx4 a[32:35], v209, %[srdz], s61 offen offset:0 sc1\n" \
"buffer_load_dwordx4 a[36:39], v209, %[srdz], s61 offen offset:1024 sc1\n" \
"buffer_load_dwordx4 a[40:43], v209, %[srdz], s61 offen offset:2048 sc1\n" \
"buffer_load_dwordx4 a[44:47], v209, %[srdz], s61 offen offset:3072 sc1\n" \
"buffer_load_dwordx4 a[48:51], v210, %[srdz], s61 offen offset:0 sc1\n" \
"buffer_load_dwordx4 a[52:55], v210, %[srdz], s61 offen offset:1024 sc1\n" \
"buffer_load_dwordx4 a[56:59], v210, %[srdz], s61 offen offset:2048 sc1\n" \
"buffer_load_dwordx4 a[60:63], v210, %[srdz], s61 offen offset:3072 sc1\n" \
"s_waitcnt vmcnt(0)\n" \
"s_nop 0\n" \
"v_mfma_f32_16x16x32_bf16 v[192:195], a[196:199], a[0:3], v[128:131]\n" \
"v_mfma_f32_16x16x32_bf16 v[192:195], a[200:203], a[4:7], v[192:195]\n" \
"v_mfma_f32_16x16x32_bf16 v[192:195], a[204:207], a[8:11], v[192:195]\n" \
"v_mfma_f32_16x16x32_bf16 v[192:195], a[232:235], a[12:15], v[192:195]\n" \
"v_mfma_f32_16x16x32_bf16 v[196:199], a[196:199], a[16:19], v[132:135]\n" \
"v_mfma_f32_16x16x32_bf16 v[196:199], a[200:203], a[20:23], v[196:199]\n" \
"v_mfma_f32_16x16x32_bf16 v[196:199], a[204:207], a[24:27], v[196:199]\n" \
"v_mfma_f32_16x16x32_bf16 v[196:199], a[232:235], a[28:31], v[196:199]\n" \
"v_mfma_f32_16x16x32_bf16 v[200:203], a[196:199], a[32:35], v[136:139]\n" \
"v_mfma_f32_16x16x32_bf16 v[200:203], a[200:203], a[36:39], v[200:203]\n" \
"v_mfma_f32_16x16x32_bf16 v[200:203], a[204:207], a[40:43], v[200:203]\n" \
"v_mfma_f32_16x16x32_bf16 v[200:203], a[232:235], a[44:47], v[200:203]\n" \
"v_mfma_f32_16x16x32_bf16 v[204:207], a[196:199], a[48:51], v[140:143]\n" \
"v_mfma_f32_16x16x32_bf16 v[204:207], a[200:203], a[52:55], v[204:207]\n" \
"v_mfma_f32_16x16x32_bf16 v[204:207], a[204:207], a[56:59], v[204:207]\n" \
"v_mfma_f32_16x16x32_bf16 v[204:207], a[232:235], a[60:63], v[204:207]\n" \
"s_nop 7\n" \
"s_nop 0\n" \
"v_cmp_u_f32_e64 s[44:45], v192, v196\n" \
"v_cmp_u_f32_e64 s[56:57], v200, v204\n" \
"s_or_b64 s[56:57], s[56:57], s[44:45]\n" \
"s_cbranch_scc0 Lbk2_%=\n" \
"s_sub_u32 s51, s51, 1\n" \
"s_cbranch_scc0 Lsq2_%=\n" \
"s_branch Lbk2_%=\n" \
"Lsp3_%=:\n" \
"s_movk_i32 s51, 0x1000\n" \
"s_cmp_gt_u32 s48, 2\n" \
"s_cselect_b32 s61, 2, 3\n" \
"s_mul_i32 s61, s61, 152064\n" \
"Lsq3_%=:\n" \
"buffer_load_dwordx4 a[128:131], %[oq0], %[srdz], s61 offen offset:0 sc1\n" \
"buffer_load_dwordx4 a[132:135], %[oq0], %[srdz], s61 offen offset:1024 sc1\n" \
"buffer_load_dwordx4 a[136:139], %[oq0], %[srdz], s61 offen offset:2048 sc1\n" \
"buffer_load_dwordx4 a[140:143], %[oq0], %[srdz], s61 offen offset:3072 sc1\n" \
"buffer_load_dwordx4 a[144:147], v208, %[srdz], s61 offen offset:0 sc1\n" \
"buffer_load_dwordx4 a[148:151], v208, %[srdz], s61 offen offset:1024 sc1\n" \
"buffer_load_dwordx4 a[152:155], v208, %[srdz], s61 offen offset:2048 sc1\n" \
"buffer_load_dwordx4 a[156:159], v208, %[srdz], s61 offen offset:3072 sc1\n" \
"buffer_load_dwordx4 a[160:163], v209, %[srdz], s61 offen offset:0 sc1\n" \
"buffer_load_dwordx4 a[164:167], v209, %[srdz], s61 offen offset:1024 sc1\n" \
"buffer_load_dwordx4 a[168:171], v209, %[srdz], s61 offen offset:2048 sc1\n" \
"buffer_load_dwordx4 a[172:175], v209, %[srdz], s61 offen offset:3072 sc1\n" \
"buffer_load_dwordx4 a[176:179], v210, %[srdz], s61 offen offset:0 sc1\n" \
"buffer_load_dwordx4 a[180:183], v210, %[srdz], s61 offen offset:1024 sc1\n" \
"buffer_load_dwordx4 a[184:187], v210, %[srdz], s61 offen offset:2048 sc1\n" \
"buffer_load_dwordx4 a[188:191], v210, %[srdz], s61 offen offset:3072 sc1\n" \
"s_waitcnt vmcnt(0)\n" \
"s_nop 0\n" \
"v_mfma_f32_16x16x32_bf16 v[128:131], a[196:199], a[128:131], v[192:195]\n" \
"v_mfma_f32_16x16x32_bf16 v[128:131], a[200:203], a[132:135], v[128:131]\n" \
"v_mfma_f32_16x16x32_bf16 v[128:131], a[204:207], a[136:139], v[128:131]\n" \
"v_mfma_f32_16x16x32_bf16 v[128:131], a[224:227], a[140:143], v[128:131]\n" \
"v_mfma_f32_16x16x32_bf16 v[132:135], a[196:199], a[144:147], v[196:199]\n" \
"v_mfma_f32_16x16x32_bf16 v[132:135], a[200:203], a[148:151], v[132:135]\n" \
"v_mfma_f32_16x16x32_bf16 v[132:135], a[204:207], a[152:155], v[132:135]\n" \
"v_mfma_f32_16x16x32_bf16 v[132:135], a[224:227], a[156:159], v[132:135]\n" \
"v_mfma_f32_16x16x32_bf16 v[136:139], a[196:199], a[160:163], v[200:203]\n" \
"v_mfma_f32_16x16x32_bf16 v[136:139], a[200:203], a[164:167], v[136:139]\n" \
"v_mfma_f32_16x16x32_bf16 v[136:139], a[204:207], a[168:171], v[136:139]\n" \
"v_mfma_f32_16x16x32_bf16 v[136:139], a[224:227], a[172:175], v[136:139]\n" \
"v_mfma_f32_16x16x32_bf16 v[140:143], a[196:199], a[176:179], v[204:207]\n" \
"v_mfma_f32_16x16x32_bf16 v[140:143], a[200:203], a[180:183], v[140:143]\n" \
"v_mfma_f32_16x16x32_bf16 v[140:143], a[204:207], a[184:187], v[140:143]\n" \
"v_mfma_f32_16x16x32_bf16 v[140:143], a[224:227], a[188:191], v[140:143]\n" \
"s_nop 7\n" \
"s_nop 0\n" \
"v_cmp_u_f32_e64 s[44:45], v128, v132\n" \
"v_cmp_u_f32_e64 s[56:57], v136, v140\n" \
"s_or_b64 s[56:57], s[56:57], s[44:45]\n" \
"s_cbranch_scc0 Lbk3_%=\n" \
"s_sub_u32 s51, s51, 1\n" \
"s_cbranch_scc0 Lsq3_%=\n" \
"s_branch Lbk3_%=\n" \
"Lsp4_%=:\n" \
"s_movk_i32 s51, 0x1000\n" \
"s_cmp_gt_u32 s48, 3\n" \
"s_cselect_b32 s61, 3, 4\n" \
"s_mul_i32 s61, s61, 152064\n" \
"Lsq4_%=:\n" \
"buffer_load_dwordx4 a[64:67], %[oq0], %[srdz], s61 offen offset:0 sc1\n" \
"buffer_load_dwordx4 a[68:71], %[oq0], %[srdz], s61 offen offset:1024 sc1\n" \
"buffer_load_dwordx4 a[72:75], %[oq0], %[srdz], s61 offen offset:2048 sc1\n" \
"buffer_load_dwordx4 a[76:79], %[oq0], %[srdz], s61 offen offset:3072 sc1\n" \
"buffer_load_dwordx4 a[80:83], v208, %[srdz], s61 offen offset:0 sc1\n" \
"buffer_load_dwordx4 a[84:87], v208, %[srdz], s61 offen offset:1024 sc1\n" \
"buffer_load_dwordx4 a[88:91], v208, %[srdz], s61 offen offset:2048 sc1\n" \
"buffer_load_dwordx4 a[92:95], v208, %[srdz], s61 offen offset:3072 sc1\n" \
"buffer_load_dwordx4 a[96:99], v209, %[srdz], s61 offen offset:0 sc1\n" \
"buffer_load_dwordx4 a[100:103], v209, %[srdz], s61 offen offset:1024 sc1\n" \
"buffer_load_dwordx4 a[104:107], v209, %[srdz], s61 offen offset:2048 sc1\n" \
"buffer_load_dwordx4 a[108:111], v209, %[srdz], s61 offen offset:3072 sc1\n" \
"buffer_load_dwordx4 a[112:115], v210, %[srdz], s61 offen offset:0 sc1\n" \
"buffer_load_dwordx4 a[116:119], v210, %[srdz], s61 offen offset:1024 sc1\n" \
"buffer_load_dwordx4 a[120:123], v210, %[srdz], s61 offen offset:2048 sc1\n" \
"buffer_load_dwordx4 a[124:127], v210, %[srdz], s61 offen offset:3072 sc1\n" \
"s_waitcnt vmcnt(0)\n" \
"s_nop 0\n" \
"v_mfma_f32_16x16x32_bf16 v[192:195], a[196:199], a[64:67], v[128:131]\n" \
"v_mfma_f32_16x16x32_bf16 v[192:195], a[200:203], a[68:71], v[192:195]\n" \
"v_mfma_f32_16x16x32_bf16 v[192:195], a[204:207], a[72:75], v[192:195]\n" \
"v_mfma_f32_16x16x32_bf16 v[192:195], a[220:223], a[76:79], v[192:195]\n" \
"v_mfma_f32_16x16x32_bf16 v[196:199], a[196:199], a[80:83], v[132:135]\n" \
"v_mfma_f32_16x16x32_bf16 v[196:199], a[200:203], a[84:87], v[196:199]\n" \
"v_mfma_f32_16x16x32_bf16 v[196:199], a[204:207], a[88:91], v[196:199]\n" \
"v_mfma_f32_16x16x32_bf16 v[196:199], a[220:223], a[92:95], v[196:199]\n" \
"v_mfma_f32_16x16x32_bf16 v[200:203], a[196:199], a[96:99], v[136:139]\n" \
"v_mfma_f32_16x16x32_bf16 v[200:203], a[200:203], a[100:103], v[200:203]\n" \
"v_mfma_f32_16x16x32_bf16 v[200:203], a[204:207], a[104:107], v[200:203]\n" \
"v_mfma_f32_16x16x32_bf16 v[200:203], a[220:223], a[108:111], v[200:203]\n" \
"v_mfma_f32_16x16x32_bf16 v[204:207], a[196:199], a[112:115], v[140:143]\n" \
"v_mfma_f32_16x16x32_bf16 v[204:207], a[200:203], a[116:119], v[204:207]\n" \
"v_mfma_f32_16x16x32_bf16 v[204:207], a[204:207], a[120:123], v[204:207]\n" \
"v_mfma_f32_16x16x32_bf16 v[204:207], a[220:223], a[124:127], v[204:207]\n" \
"s_nop 7\n" \
"s_nop 0\n" \
"v_cmp_u_f32_e64 s[44:45], v192, v196\n" \
"v_cmp_u_f32_e64 s[56:57], v200, v204\n" \
"s_or_b64 s[56:57], s[56:57], s[44:45]\n" \
"s_cbranch_scc0 Lbk4_%=\n" \
"s_sub_u32 s51, s51, 1\n" \
"s_cbranch_scc0 Lsq4_%=\n" \
"s_branch Lbk4_%=\n" \
"Lsp5_%=:\n" \
"s_movk_i32 s51, 0x1000\n" \
"s_cmp_gt_u32 s48, 4\n" \
"s_cselect_b32 s61, 4, 5\n" \
"s_mul_i32 s61, s61, 152064\n" \
"Lsq5_%=:\n" \
"buffer_load_dwordx4 v[0:3], %[oq0], %[srdz], s61 offen offset:0 sc1\n" \
"buffer_load_dwordx4 v[8:11], %[oq0], %[srdz], s61 offen offset:1024 sc1\n" \
"buffer_load_dwordx4 v[16:19], %[oq0], %[srdz], s61 offen offset:2048 sc1\n" \
"buffer_load_dwordx4 v[24:27], %[oq0], %[srdz], s61 offen offset:3072 sc1\n" \
"buffer_load_dwordx4 v[32:35], v208, %[srdz], s61 offen offset:0 sc1\n" \
"buffer_load_dwordx4 v[40:43], v208, %[srdz], s61 offen offset:1024 sc1\n" \
"buffer_load_dwordx4 v[48:51], v208, %[srdz], s61 offen offset:2048 sc1\n" \
"buffer_load_dwordx4 v[56:59], v208, %[srdz], s61 offen offset:3072 sc1\n" \
"buffer_load_dwordx4 v[64:67], v209, %[srdz], s61 offen offset:0 sc1\n" \
"buffer_load_dwordx4 v[72:75], v209, %[srdz], s61 offen offset:1024 sc1\n" \
"buffer_load_dwordx4 v[80:83], v209, %[srdz], s61 offen offset:2048 sc1\n" \
"buffer_load_dwordx4 v[88:91], v209, %[srdz], s61 offen offset:3072 sc1\n" \
"buffer_load_dwordx4 v[96:99], v210, %[srdz], s61 offen offset:0 sc1\n" \
"buffer_load_dwordx4 v[104:107], v210, %[srdz], s61 offen offset:1024 sc1\n" \
"buffer_load_dwordx4 v[112:115], v210, %[srdz], s61 offen offset:2048 sc1\n" \
"buffer_load_dwordx4 v[120:123], v210, %[srdz], s61 offen offset:3072 sc1\n" \
"s_waitcnt vmcnt(0)\n" \
"s_nop 0\n" \
"v_mfma_f32_16x16x32_bf16 v[128:131], a[232:235], v[0:3], v[192:195]\n" \
"v_mfma_f32_16x16x32_bf16 v[128:131], a[236:239], v[8:11], v[128:131]\n" \
"v_mfma_f32_16x16x32_bf16 v[128:131], a[208:211], v[16:19], v[128:131]\n" \
"v_mfma_f32_16x16x32_bf16 v[128:131], a[228:231], v[24:27], v[128:131]\n" \
"v_mfma_f32_16x16x32_bf16 v[132:135], a[232:235], v[32:35], v[196:199]\n" \
"v_mfma_f32_16x16x32_bf16 v[132:135], a[236:239], v[40:43], v[132:135]\n" \
"v_mfma_f32_16x16x32_bf16 v[132:135], a[208:211], v[48:51], v[132:135]\n" \
"v_mfma_f32_16x16x32_bf16 v[132:135], a[228:231], v[56:59], v[132:135]\n" \
"v_mfma_f32_16x16x32_bf16 v[136:139], a[232:235], v[64:67], v[200:203]\n" \
"v_mfma_f32_16x16x32_bf16 v[136:139], a[236:239], v[72:75], v[136:139]\n" \
"v_mfma_f32_16x16x32_bf16 v[136:139], a[208:211], v[80:83], v[136:139]\n" \
"v_mfma_f32_16x16x32_bf16 v[136:139], a[228:231], v[88:91], v[136:139]\n" \
"v_mfma_f32_16x16x32_bf16 v[140:143], a[232:235], v[96:99], v[204:207]\n" \
"v_mfma_f32_16x16x32_bf16 v[140:143], a[236:239], v[104:107], v[140:143]\n" \
"v_mfma_f32_16x16x32_bf16 v[140:143], a[208:211], v[112:115], v[140:143]\n" \
"v_mfma_f32_16x16x32_bf16 v[140:143], a[228:231], v[120:123], v[140:143]\n" \
"s_nop 7\n" \
"s_nop 0\n" \
"v_cmp_u_f32_e64 s[44:45], v128, v132\n" \
"v_cmp_u_f32_e64 s[56:57], v136, v140\n" \
"s_or_b64 s[56:57], s[56:57], s[44:45]\n" \
"s_cbranch_scc0 Lbk5_%=\n" \
"s_sub_u32 s51, s51, 1\n" \
"s_cbranch_scc0 Lsq5_%=\n" \
"s_branch Lbk5_%=\n" \
"Lsp6_%=:\n" \
"s_movk_i32 s51, 0x1000\n" \
"s_cmp_gt_u32 s48, 5\n" \
"s_cselect_b32 s61, 5, 6\n" \
"s_mul_i32 s61, s61, 152064\n" \
"Lsq6_%=:\n" \
"buffer_load_dwordx4 a[0:3], %[oq0], %[srdz], s61 offen offset:0 sc1\n" \
"buffer_load_dwordx4 a[4:7], %[oq0], %[srdz], s61 offen offset:1024 sc1\n" \
"buffer_load_dwordx4 a[8:11], %[oq0], %[srdz], s61 offen offset:2048 sc1\n" \
"buffer_load_dwordx4 a[12:15], %[oq0], %[srdz], s61 offen offset:3072 sc1\n" \
"buffer_load_dwordx4 a[16:19], v208, %[srdz], s61 offen offset:0 sc1\n" \
"buffer_load_dwordx4 a[20:23], v208, %[srdz], s61 offen offset:1024 sc1\n" \
"buffer_load_dwordx4 a[24:27], v208, %[srdz], s61 offen offset:2048 sc1\n" \
"buffer_load_dwordx4 a[28:31], v208, %[srdz], s61 offen offset:3072 sc1\n" \
"buffer_load_dwordx4 a[32:35], v209, %[srdz], s61 offen offset:0 sc1\n" \
"buffer_load_dwordx4 a[36:39], v209, %[srdz], s61 offen offset:1024 sc1\n" \
"buffer_load_dwordx4 a[40:43], v209, %[srdz], s61 offen offset:2048 sc1\n" \
"buffer_load_dwordx4 a[44:47], v209, %[srdz], s61 offen offset:3072 sc1\n" \
"buffer_load_dwordx4 a[48:51], v210, %[srdz], s61 offen offset:0 sc1\n" \
"buffer_load_dwordx4 a[52:55], v210, %[srdz], s61 offen offset:1024 sc1\n" \
"buffer_load_dwordx4 a[56:59], v210, %[srdz], s61 offen offset:2048 sc1\n" \
"buffer_load_dwordx4 a[60:63], v210, %[srdz], s61 offen offset:3072 sc1\n" \
"s_waitcnt vmcnt(0)\n" \
"s_nop 0\n" \
"v_mfma_f32_16x16x32_bf16 v[192:195], a[196:199], a[0:3], v[128:131]\n" \
"v_mfma_f32_16x16x32_bf16 v[192:195], a[200:203], a[4:7], v[192:195]\n" \
"v_mfma_f32_16x16x32_bf16 v[192:195], a[204:207], a[8:11], v[192:195]\n" \
"v_mfma_f32_16x16x32_bf16 v[192:195], a[220:223], a[12:15], v[192:195]\n" \
"v_mfma_f32_16x16x32_bf16 v[196:199], a[196:199], a[16:19], v[132:135]\n" \
"v_mfma_f32_16x16x32_bf16 v[196:199], a[200:203], a[20:23], v[196:199]\n" \
"v_mfma_f32_16x16x32_bf16 v[196:199], a[204:207], a[24:27], v[196:199]\n" \
"v_mfma_f32_16x16x32_bf16 v[196:199], a[220:223], a[28:31], v[196:199]\n" \
"v_mfma_f32_16x16x32_bf16 v[200:203], a[196:199], a[32:35], v[136:139]\n" \
"v_mfma_f32_16x16x32_bf16 v[200:203], a[200:203], a[36:39], v[200:203]\n" \
"v_mfma_f32_16x16x32_bf16 v[200:203], a[204:207], a[40:43], v[200:203]\n" \
"v_mfma_f32_16x16x32_bf16 v[200:203], a[220:223], a[44:47], v[200:203]\n" \
"v_mfma_f32_16x16x32_bf16 v[204:207], a[196:199], a[48:51], v[140:143]\n" \
"v_mfma_f32_16x16x32_bf16 v[204:207], a[200:203], a[52:55], v[204:207]\n" \
"v_mfma_f32_16x16x32_bf16 v[204:207], a[204:207], a[56:59], v[204:207]\n" \
"v_mfma_f32_16x16x32_bf16 v[204:207], a[220:223], a[60:63], v[204:207]\n" \
"s_nop 7\n" \
"s_nop 0\n" \
"v_cmp_u_f32_e64 s[44:45], v192, v196\n" \
"v_cmp_u_f32_e64 s[56:57], v200, v204\n" \
"s_or_b64 s[56:57], s[56:57], s[44:45]\n" \
"s_cbranch_scc0 Lbk6_%=\n" \
"s_sub_u32 s51, s51, 1\n" \
"s_cbranch_scc0 Lsq6_%=\n" \
"s_branch Lbk6_%=\n" \
"Lsp7_%=:\n" \
"s_movk_i32 s51, 0x1000\n" \
"s_cmp_gt_u32 s48, 6\n" \
"s_cselect_b32 s61, 6, 7\n" \
"s_mul_i32 s61, s61, 152064\n" \
"Lsq7_%=:\n" \
"buffer_load_dwordx4 a[128:131], %[oq0], %[srdz], s61 offen offset:0 sc1\n" \
"buffer_load_dwordx4 a[132:135], %[oq0], %[srdz], s61 offen offset:1024 sc1\n" \
"buffer_load_dwordx4 a[136:139], %[oq0], %[srdz], s61 offen offset:2048 sc1\n" \
"buffer_load_dwordx4 a[140:143], %[oq0], %[srdz], s61 offen offset:3072 sc1\n" \
"buffer_load_dwordx4 a[144:147], v208, %[srdz], s61 offen offset:0 sc1\n" \
"buffer_load_dwordx4 a[148:151], v208, %[srdz], s61 offen offset:1024 sc1\n" \
"buffer_load_dwordx4 a[152:155], v208, %[srdz], s61 offen offset:2048 sc1\n" \
"buffer_load_dwordx4 a[156:159], v208, %[srdz], s61 offen offset:3072 sc1\n" \
"buffer_load_dwordx4 a[160:163], v209, %[srdz], s61 offen offset:0 sc1\n" \
"buffer_load_dwordx4 a[164:167], v209, %[srdz], s61 offen offset:1024 sc1\n" \
"buffer_load_dwordx4 a[168:171], v209, %[srdz], s61 offen offset:2048 sc1\n" \
"buffer_load_dwordx4 a[172:175], v209, %[srdz], s61 offen offset:3072 sc1\n" \
"buffer_load_dwordx4 a[176:179], v210, %[srdz], s61 offen offset:0 sc1\n" \
"buffer_load_dwordx4 a[180:183], v210, %[srdz], s61 offen offset:1024 sc1\n" \
"buffer_load_dwordx4 a[184:187], v210, %[srdz], s61 offen offset:2048 sc1\n" \
"buffer_load_dwordx4 a[188:191], v210, %[srdz], s61 offen offset:3072 sc1\n" \
"s_waitcnt vmcnt(0)\n" \
"s_nop 0\n" \
"v_mfma_f32_16x16x32_bf16 v[128:131], a[208:211], a[128:131], v[192:195]\n" \
"v_mfma_f32_16x16x32_bf16 v[128:131], a[224:227], a[132:135], v[128:131]\n" \
"v_mfma_f32_16x16x32_bf16 v[128:131], a[228:231], a[136:139], v[128:131]\n" \
"v_mfma_f32_16x16x32_bf16 v[128:131], a[236:239], a[140:143], v[128:131]\n" \
"v_mfma_f32_16x16x32_bf16 v[132:135], a[208:211], a[144:147], v[196:199]\n" \
"v_mfma_f32_16x16x32_bf16 v[132:135], a[224:227], a[148:151], v[132:135]\n" \
"v_mfma_f32_16x16x32_bf16 v[132:135], a[228:231], a[152:155], v[132:135]\n" \
"v_mfma_f32_16x16x32_bf16 v[132:135], a[236:239], a[156:159], v[132:135]\n" \
"v_mfma_f32_16x16x32_bf16 v[136:139], a[208:211], a[160:163], v[200:203]\n" \
"v_mfma_f32_16x16x32_bf16 v[136:139], a[224:227], a[164:167], v[136:139]\n" \
"v_mfma_f32_16x16x32_bf16 v[136:139], a[228:231], a[168:171], v[136:139]\n" \
"v_mfma_f32_16x16x32_bf16 v[136:139], a[236:239], a[172:175], v[136:139]\n" \
"v_mfma_f32_16x16x32_bf16 v[140:143], a[208:211], a[176:179], v[204:207]\n" \
"v_mfma_f32_16x16x32_bf16 v[140:143], a[224:227], a[180:183], v[140:143]\n" \
"v_mfma_f32_16x16x32_bf16 v[140:143], a[228:231], a[184:187], v[140:143]\n" \
"v_mfma_f32_16x16x32_bf16 v[140:143], a[236:239], a[188:191], v[140:143]\n" \
"s_nop 7\n" \
"s_nop 0\n" \
"v_cmp_u_f32_e64 s[44:45], v128, v132\n" \
"v_cmp_u_f32_e64 s[56:57], v136, v140\n" \
"s_or_b64 s[56:57], s[56:57], s[44:45]\n" \
"s_cbranch_scc0 Lbk7_%=\n" \
"s_sub_u32 s51, s51, 1\n" \
"s_cbranch_scc0 Lsq7_%=\n" \
"s_branch Lbk7_%=\n" \
"Lspk_%=:\n" \
"s_nop 0\n" \
"s_mov_b32 s51, 0x10000\n" \
"Lsqk_%=:\n" \
"buffer_load_dword v229, v226, %[srdz], s50 offen sc1\n" \
"s_waitcnt vmcnt(0)\n" \
"s_nop 0\n" \
"v_cmp_eq_u32_e64 s[56:57], 0, v229\n" \
"s_cmp_lg_u64 s[56:57], 0\n" \
"s_cbranch_scc0 Lbkk_%=\n" \
"s_sub_u32 s51, s51, 1\n" \
"s_cbranch_scc0 Lsqk_%=\n" \
"s_branch Lbkk_%=\n" \
"Lspp_%=:\n" \
"s_nop 0\n" \
"s_mov_b32 s51, 0x10000\n" \
"Lsqp_%=:\n" \
"buffer_load_dword v232, v226, %[srdz], s54 offen sc1\n" \
"s_waitcnt vmcnt(0)\n" \
"v_cmp_eq_u32_e32 vcc, 0, v232\n" \
"s_cbranch_vccz Lbkp_%=\n" \
"s_sub_u32 s51, s51, 1\n" \
"s_cbranch_scc0 Lsqp_%=\n" \
"s_branch Lbkp_%=\n" \
"Lwalk_%=:\n" \
"s_mov_b64 exec, 0\n" \
"s_cmp_eq_u32 %[wid], 1\n" \
"s_cbranch_scc1 Lk0_%=\n" \
"s_cmp_eq_u32 %[wid], 2\n" \
"s_cbranch_scc1 Lk6_%=\n" \
"s_branch Lk12_%=\n" \
"adaln_kr:\n" \
"s_mov_b64 exec, -1\n" \
"s_branch Lgo_%=\n" \
"Lslow_%=:\n" \
"s_mov_b32 s52, 1\n" \
"s_mov_b32 s48, 0\n" \
"s_mov_b32 s51, 32\n" \
"s_nop 0\n" \
"s_mov_b32 s62, 0x80000000\n" \
"s_mov_b32 s63, 0x80000000\n" \
"s_waitcnt vmcnt(0)\n" \
"s_nop 0\n" \
"v_readlane_b32 s64, %[tlo], 9\n" \
"v_readlane_b32 s65, %[thi], 9\n" \
"s_mov_b32 s66, %[wbytes]\n" \
"v_mov_b32_e32 v229, %[vg0]\n" \
"s_mov_b32 s44, %[m0t]\n" \
"s_mov_b32 s45, 0\n" \
"v_xor_b32_e64 v208, 64, %[kb0]\n" \
"v_xor_b32_e32 v209, 0x80, %[kb0]\n" \
"v_xor_b32_e32 v210, 0xc0, %[kb0]\n" \
"v_xor_b32_e64 v212, 64, %[kd0]\n" \
"v_xor_b32_e32 v213, 0x80, %[kd0]\n" \
"v_xor_b32_e32 v214, 0xc0, %[kd0]\n" \
"v_xor_b32_e64 v211, 32, %[vvb]\n" \
"v_add_u32_e32 v215, 0xfffffc80, %[vd0]\n" \
"s_mov_b32 s66, 0\n" \
"s_add_u32 s40, %[m0b], 0\n" \
"s_add_u32 s41, %[m0b], 16384\n" \
"s_add_u32 s42, %[m0b], 32768\n" \
"s_add_u32 s43, %[m0b], 49152\n" \
"v_mov_b32_e32 v225, 0xff800000\n" \
"v_mov_b32_e32 v226, 0xff800000\n" \
"v_mov_b32_e32 v227, 0xff800000\n" \
"v_mov_b32_e32 v228, 0xff800000\n" \
"s_waitcnt vmcnt(0) lgkmcnt(0)\n" \
"s_barrier\n" \
"Lsl_%=:\n" \
"s_mov_b32 m0, s40\n" \
"s_nop 0\n" \
"buffer_load_dwordx4 %[kd0], %[srdk], s48 offen offset:0 lds\n" \
"buffer_load_dwordx4 v212, %[srdk], s48 offen offset:1024 lds\n" \
"buffer_load_dwordx4 v213, %[srdk], s48 offen offset:2048 lds\n" \
"buffer_load_dwordx4 v214, %[srdk], s48 offen offset:3072 lds\n" \
"s_waitcnt vmcnt(0)\n" \
"s_barrier\n" \
"ds_read_b128 a[64:67], %[kb0] offset:0\n" \
"ds_read_b128 a[68:71], v208 offset:0\n" \
"ds_read_b128 a[72:75], v209 offset:0\n" \
"ds_read_b128 a[76:79], v210 offset:0\n" \
"ds_read_b128 a[92:95], v210 offset:4096\n" \
"ds_read_b128 a[88:91], v209 offset:4096\n" \
"ds_read_b128 a[84:87], v208 offset:4096\n" \
"ds_read_b128 a[80:83], %[kb0] offset:4096\n" \
"ds_read_b128 a[96:99], %[kb0] offset:8192\n" \
"ds_read_b128 a[100:103], v208 offset:8192\n" \
"ds_read_b128 a[104:107], v209 offset:8192\n" \
"ds_read_b128 a[108:111], v210 offset:8192\n" \
"ds_read_b128 a[124:127], v210 offset:12288\n" \
"ds_read_b128 a[120:123], v209 offset:12288\n" \
"ds_read_b128 a[116:119], v208 offset:12288\n" \
"ds_read_b128 a[112:115], %[kb0] offset:12288\n" \
"s_waitcnt lgkmcnt(0)\n" \
"s_barrier\n" \
"v_mfma_f32_16x16x32_bf16 v[128:131], a[64:67], a[0:3], 0\n" \
"v_mfma_f32_16x16x32_bf16 v[128:131], a[68:71], a[4:7], v[128:131]\n" \
"v_mfma_f32_16x16x32_bf16 v[128:131], a[72:75], a[8:11], v[128:131]\n" \
"v_mfma_f32_16x16x32_bf16 v[128:131], a[76:79], a[12:15], v[128:131]\n" \
"v_mfma_f32_16x16x32_bf16 v[132:135], a[92:95], a[12:15], 0\n" \
"v_mfma_f32_16x16x32_bf16 v[132:135], a[88:91], a[8:11], v[132:135]\n" \
"v_mfma_f32_16x16x32_bf16 v[132:135], a[84:87], a[4:7], v[132:135]\n" \
"v_mfma_f32_16x16x32_bf16 v[132:135], a[80:83], a[0:3], v[132:135]\n" \
"v_mfma_f32_16x16x32_bf16 v[136:139], a[64:67], a[16:19], 0\n" \
"v_mfma_f32_16x16x32_bf16 v[136:139], a[68:71], a[20:23], v[136:139]\n" \
"v_mfma_f32_16x16x32_bf16 v[136:139], a[72:75], a[24:27], v[136:139]\n" \
"v_mfma_f32_16x16x32_bf16 v[136:139], a[76:79], a[28:31], v[136:139]\n" \
"v_mfma_f32_16x16x32_bf16 v[140:143], a[92:95], a[28:31], 0\n" \
"v_mfma_f32_16x16x32_bf16 v[140:143], a[88:91], a[24:27], v[140:143]\n" \
"v_mfma_f32_16x16x32_bf16 v[140:143], a[84:87], a[20:23], v[140:143]\n" \
"v_mfma_f32_16x16x32_bf16 v[140:143], a[80:83], a[16:19], v[140:143]\n" \
"v_mfma_f32_16x16x32_bf16 v[144:147], a[64:67], a[32:35], 0\n" \
"v_mfma_f32_16x16x32_bf16 v[144:147], a[68:71], a[36:39], v[144:147]\n" \
"v_mfma_f32_16x16x32_bf16 v[144:147], a[72:75], a[40:43], v[144:147]\n" \
"v_mfma_f32_16x16x32_bf16 v[144:147], a[76:79], a[44:47], v[144:147]\n" \
"v_mfma_f32_16x16x32_bf16 v[148:151], a[92:95], a[44:47], 0\n" \
"v_mfma_f32_16x16x32_bf16 v[148:151], a[88:91], a[40:43], v[148:151]\n" \
"v_mfma_f32_16x16x32_bf16 v[148:151], a[84:87], a[36:39], v[148:151]\n" \
"v_mfma_f32_16x16x32_bf16 v[148:151], a[80:83], a[32:35], v[148:151]\n" \
"v_mfma_f32_16x16x32_bf16 v[152:155], a[64:67], a[48:51], 0\n" \
"v_mfma_f32_16x16x32_bf16 v[152:155], a[68:71], a[52:55], v[152:155]\n" \
"v_mfma_f32_16x16x32_bf16 v[152:155], a[72:75], a[56:59], v[152:155]\n" \
"v_mfma_f32_16x16x32_bf16 v[152:155], a[76:79], a[60:63], v[152:155]\n" \
"v_mfma_f32_16x16x32_bf16 v[156:159], a[92:95], a[60:63], 0\n" \
"v_mfma_f32_16x16x32_bf16 v[156:159], a[88:91], a[56:59], v[156:159]\n" \
"v_mfma_f32_16x16x32_bf16 v[156:159], a[84:87], a[52:55], v[156:159]\n" \
"v_mfma_f32_16x16x32_bf16 v[156:159], a[80:83], a[48:51], v[156:159]\n" \
"s_nop 7\n" \
"s_nop 0\n" \
"v_max3_f32 v230, v128, v129, v130\n" \
"v_max3_f32 v231, v131, v132, v133\n" \
"v_max3_f32 v230, v230, v134, v135\n" \
"v_max3_f32 v225, v230, v231, v225\n" \
"v_max3_f32 v230, v136, v137, v138\n" \
"v_max3_f32 v231, v139, v140, v141\n" \
"v_max3_f32 v230, v230, v142, v143\n" \
"v_max3_f32 v226, v230, v231, v226\n" \
"v_max3_f32 v230, v144, v145, v146\n" \
"v_max3_f32 v231, v147, v148, v149\n" \
"v_max3_f32 v230, v230, v150, v151\n" \
"v_max3_f32 v227, v230, v231, v227\n" \
"v_max3_f32 v230, v152, v153, v154\n" \
"v_max3_f32 v231, v155, v156, v157\n" \
"v_max3_f32 v230, v230, v158, v159\n" \
"v_max3_f32 v228, v230, v231, v228\n" \
"s_nop 3\n" \
"s_nop 0\n" \
"v_mfma_f32_16x16x32_bf16 v[128:131], a[96:99], a[0:3], 0\n" \
"v_mfma_f32_16x16x32_bf16 v[128:131], a[100:103], a[4:7], v[128:131]\n" \
"v_mfma_f32_16x16x32_bf16 v[128:131], a[104:107], a[8:11], v[128:131]\n" \
"v_mfma_f32_16x16x32_bf16 v[128:131], a[108:111], a[12:15], v[128:131]\n" \
"v_mfma_f32_16x16x32_bf16 v[132:135], a[124:127], a[12:15], 0\n" \
"v_mfma_f32_16x16x32_bf16 v[132:135], a[120:123], a[8:11], v[132:135]\n" \
"v_mfma_f32_16x16x32_bf16 v[132:135], a[116:119], a[4:7], v[132:135]\n" \
"v_mfma_f32_16x16x32_bf16 v[132:135], a[112:115], a[0:3], v[132:135]\n" \
"v_mfma_f32_16x16x32_bf16 v[136:139], a[96:99], a[16:19], 0\n" \
"v_mfma_f32_16x16x32_bf16 v[136:139], a[100:103], a[20:23], v[136:139]\n" \
"v_mfma_f32_16x16x32_bf16 v[136:139], a[104:107], a[24:27], v[136:139]\n" \
"v_mfma_f32_16x16x32_bf16 v[136:139], a[108:111], a[28:31], v[136:139]\n" \
"v_mfma_f32_16x16x32_bf16 v[140:143], a[124:127], a[28:31], 0\n" \
"v_mfma_f32_16x16x32_bf16 v[140:143], a[120:123], a[24:27], v[140:143]\n" \
"v_mfma_f32_16x16x32_bf16 v[140:143], a[116:119], a[20:23], v[140:143]\n" \
"v_mfma_f32_16x16x32_bf16 v[140:143], a[112:115], a[16:19], v[140:143]\n" \
"v_mfma_f32_16x16x32_bf16 v[144:147], a[96:99], a[32:35], 0\n" \
"v_mfma_f32_16x16x32_bf16 v[144:147], a[100:103], a[36:39], v[144:147]\n" \
"v_mfma_f32_16x16x32_bf16 v[144:147], a[104:107], a[40:43], v[144:147]\n" \
"v_mfma_f32_16x16x32_bf16 v[144:147], a[108:111], a[44:47], v[144:147]\n" \
"v_mfma_f32_16x16x32_bf16 v[148:151], a[124:127], a[44:47], 0\n" \
"v_mfma_f32_16x16x32_bf16 v[148:151], a[120:123], a[40:43], v[148:151]\n" \
"v_mfma_f32_16x16x32_bf16 v[148:151], a[116:119], a[36:39], v[148:151]\n" \
"v_mfma_f32_16x16x32_bf16 v[148:151], a[112:115], a[32:35], v[148:151]\n" \
"v_mfma_f32_16x16x32_bf16 v[152:155], a[96:99], a[48:51], 0\n" \
"v_mfma_f32_16x16x32_bf16 v[152:155], a[100:103], a[52:55], v[152:155]\n" \
"v_mfma_f32_16x16x32_bf16 v[152:155], a[104:107], a[56:59], v[152:155]\n" \
"v_mfma_f32_16x16x32_bf16 v[152:155], a[108:111], a[60:63], v[152:155]\n" \
"v_mfma_f32_16x16x32_bf16 v[156:159], a[124:127], a[60:63], 0\n" \
"v_mfma_f32_16x16x32_bf16 v[156:159], a[120:123], a[56:59], v[156:159]\n" \
"v_mfma_f32_16x16x32_bf16 v[156:159], a[116:119], a[52:55], v[156:159]\n" \
"v_mfma_f32_16x16x32_bf16 v[156:159], a[112:115], a[48:51], v[156:159]\n" \
"s_nop 7\n" \
"s_nop 0\n" \
"v_max3_f32 v230, v128, v129, v130\n" \
"v_max3_f32 v231, v131, v132, v133\n" \
"v_max3_f32 v230, v230, v134, v135\n" \
"v_max3_f32 v225, v230, v231, v225\n" \
"v_max3_f32 v230, v136, v137, v138\n" \
"v_max3_f32 v231, v139, v140, v141\n" \
"v_max3_f32 v230, v230, v142, v143\n" \
"v_max3_f32 v226, v230, v231, v226\n" \
"v_max3_f32 v230, v144, v145, v146\n" \
"v_max3_f32 v231, v147, v148, v149\n" \
"v_max3_f32 v230, v230, v150, v151\n" \
"v_max3_f32 v227, v230, v231, v227\n" \
"v_max3_f32 v230, v152, v153, v154\n" \
"v_max3_f32 v231, v155, v156, v157\n" \
"v_max3_f32 v230, v230, v158, v159\n" \
"v_max3_f32 v228, v230, v231, v228\n" \
"s_nop 3\n" \
"s_add_u32 s48, s48, s54\n" \
"s_sub_u32 s51, s51, 1\n" \
"s_cmp_lg_u32 s51, 0\n" \
"s_cbranch_scc1 Lsl_%=\n" \
"s_nop 0\n" \
"v_mov_b32_e32 v232, v225\n" \
"v_mov_b32_e32 v233, v226\n" \
"v_mov_b32_e32 v234, v227\n" \
"v_mov_b32_e32 v235, v228\n" \
"s_nop 0\n" \
"v_permlane16_swap_b32_e32 v225, v232\n" \
"v_permlane16_swap_b32_e32 v226, v233\n" \
"v_permlane16_swap_b32_e32 v227, v234\n" \
"v_permlane16_swap_b32_e32 v228, v235\n" \
"v_max_f32_e32 v225, v225, v232\n" \
"v_max_f32_e32 v226, v226, v233\n" \
"v_max_f32_e32 v227, v227, v234\n" \
"v_max_f32_e32 v228, v228, v235\n" \
"v_mov_b32_e32 v232, v225\n" \
"v_mov_b32_e32 v233, v226\n" \
"v_mov_b32_e32 v234, v227\n" \
"v_mov_b32_e32 v235, v228\n" \
"s_nop 0\n" \
"v_permlane32_swap_b32_e32 v225, v232\n" \
"v_permlane32_swap_b32_e32 v226, v233\n" \
"v_permlane32_swap_b32_e32 v227, v234\n" \
"v_permlane32_swap_b32_e32 v228, v235\n" \
"v_max_f32_e32 v225, v225, v232\n" \
"v_max_f32_e32 v226, v226, v233\n" \
"v_max_f32_e32 v227, v227, v234\n" \
"v_max_f32_e32 v228, v228, v235\n" \
"s_cmp_lg_u32 s69, 0\n" \
"s_cbranch_scc1 Lnewm_%=\n" \
"v_sub_f32_e32 v225, 0, v160\n" \
"v_sub_f32_e32 v226, 0, v164\n" \
"v_sub_f32_e64 v227, 0, v168\n" \
"v_sub_f32_e32 v228, 0, v172\n" \
"Lnewm_%=:\n" \
"s_mov_b32 s48, 0\n" \
"s_mov_b32 m0, s40\n" \
"s_nop 0\n" \
"buffer_load_dwordx4 %[kd0], %[srdk], s48 offen offset:0 lds\n" \
"buffer_load_dwordx4 v212, %[srdk], s48 offen offset:1024 lds\n" \
"buffer_load_dwordx4 v213, %[srdk], s48 offen offset:2048 lds\n" \
"buffer_load_dwordx4 v214, %[srdk], s48 offen offset:3072 lds\n" \
"s_mov_b32 s48, 16384\n" \
"s_mov_b32 m0, s41\n" \
"s_nop 0\n" \
"buffer_load_dwordx4 %[kd0], %[srdk], s48 offen offset:0 lds\n" \
"buffer_load_dwordx4 v212, %[srdk], s48 offen offset:1024 lds\n" \
"buffer_load_dwordx4 v213, %[srdk], s48 offen offset:2048 lds\n" \
"buffer_load_dwordx4 v214, %[srdk], s48 offen offset:3072 lds\n" \
"s_add_u32 m0, %[m0b], 65536\n" \
"s_nop 0\n" \
"s_nop 0\n" \
"buffer_load_dwordx4 %[vd0], %[srdv], s48 offen offset:0 lds\n" \
"buffer_load_dwordx4 v215, %[srdv], s48 offen offset:1024 lds\n" \
"buffer_load_dwordx4 %[vd0], %[srdv], s48 offen offset:2048 lds\n" \
"buffer_load_dwordx4 v215, %[srdv], s48 offen offset:3072 lds\n" \
"s_mov_b32 s48, 32768\n" \
"s_mov_b32 m0, s42\n" \
"s_nop 0\n" \
"buffer_load_dwordx4 %[kd0], %[srdk], s48 offen offset:0 lds\n" \
"buffer_load_dwordx4 v212, %[srdk], s48 offen offset:1024 lds\n" \
"buffer_load_dwordx4 v213, %[srdk], s48 offen offset:2048 lds\n" \
"buffer_load_dwordx4 v214, %[srdk], s48 offen offset:3072 lds\n" \
"s_add_u32 m0, %[m0b], 81920\n" \
"s_nop 0\n" \
"s_nop 0\n" \
"buffer_load_dwordx4 %[vd0], %[srdv], s48 offen offset:0 lds\n" \
"buffer_load_dwordx4 v215, %[srdv], s48 offen offset:1024 lds\n" \
"buffer_load_dwordx4 %[vd0], %[srdv], s48 offen offset:2048 lds\n" \
"buffer_load_dwordx4 v215, %[srdv], s48 offen offset:3072 lds\n" \
"s_mov_b32 s48, 49152\n" \
"s_mov_b32 m0, s43\n" \
"s_nop 0\n" \
"buffer_load_dwordx4 %[kd0], %[srdk], s48 offen offset:0 lds\n" \
"buffer_load_dwordx4 v212, %[srdk], s48 offen offset:1024 lds\n" \
"buffer_load_dwordx4 v213, %[srdk], s48 offen offset:2048 lds\n" \
"buffer_load_dwordx4 v214, %[srdk], s48 offen offset:3072 lds\n" \
"s_add_u32 m0, %[m0b], 98304\n" \
"s_nop 0\n" \
"s_nop 0\n" \
"buffer_load_dwordx4 %[vd0], %[srdv], s48 offen offset:0 lds\n" \
"buffer_load_dwordx4 v215, %[srdv], s48 offen offset:1024 lds\n" \
"buffer_load_dwordx4 %[vd0], %[srdv], s48 offen offset:2048 lds\n" \
"buffer_load_dwordx4 v215, %[srdv], s48 offen offset:3072 lds\n" \
"s_waitcnt vmcnt(0)\n" \
"s_barrier\n" \
"ds_read_b128 a[64:67], %[kb0] offset:0\n" \
"ds_read_b128 a[68:71], v208 offset:0\n" \
"ds_read_b128 a[72:75], v209 offset:0\n" \
"ds_read_b128 a[76:79], v210 offset:0\n" \
"ds_read_b128 a[92:95], v210 offset:4096\n" \
"ds_read_b128 a[88:91], v209 offset:4096\n" \
"ds_read_b128 a[84:87], v208 offset:4096\n" \
"ds_read_b128 a[80:83], %[kb0] offset:4096\n" \
"s_mov_b32 s48, 65536\n" \
"s_mov_b32 s60, 0\n" \
"s_waitcnt lgkmcnt(0)\n" \
"v_mfma_f32_16x16x32_bf16 v[128:131], a[64:67], a[0:3], 0\n" \
"ds_read_b128 a[96:99], %[kb0] offset:8192\n" \
"v_mfma_f32_16x16x32_bf16 v[128:131], a[68:71], a[4:7], v[128:131]\n" \
"ds_read_b128 a[100:103], v208 offset:8192\n" \
"v_mfma_f32_16x16x32_bf16 v[128:131], a[72:75], a[8:11], v[128:131]\n" \
"ds_read_b128 a[104:107], v209 offset:8192\n" \
"v_mfma_f32_16x16x32_bf16 v[128:131], a[76:79], a[12:15], v[128:131]\n" \
"ds_read_b128 a[108:111], v210 offset:8192\n" \
"v_mfma_f32_16x16x32_bf16 v[132:135], a[92:95], a[12:15], 0\n" \
"ds_read_b128 a[124:127], v210 offset:12288\n" \
"v_mfma_f32_16x16x32_bf16 v[132:135], a[88:91], a[8:11], v[132:135]\n" \
"ds_read_b128 a[120:123], v209 offset:12288\n" \
"v_mfma_f32_16x16x32_bf16 v[132:135], a[84:87], a[4:7], v[132:135]\n" \
"ds_read_b128 a[116:119], v208 offset:12288\n" \
"v_mfma_f32_16x16x32_bf16 v[132:135], a[80:83], a[0:3], v[132:135]\n" \
"ds_read_b128 a[112:115], %[kb0] offset:12288\n" \
"v_mfma_f32_16x16x32_bf16 v[136:139], a[64:67], a[16:19], 0\n" \
"ds_read_b64_tr_b16 a[128:129], %[vvb] offset:0\n" \
"v_mfma_f32_16x16x32_bf16 v[136:139], a[68:71], a[20:23], v[136:139]\n" \
"ds_read_b64_tr_b16 a[130:131], %[vvb] offset:4096\n" \
"v_mfma_f32_16x16x32_bf16 v[136:139], a[72:75], a[24:27], v[136:139]\n" \
"ds_read_b64_tr_b16 a[132:133], v211 offset:0\n" \
"v_mfma_f32_16x16x32_bf16 v[136:139], a[76:79], a[28:31], v[136:139]\n" \
"ds_read_b64_tr_b16 a[134:135], v211 offset:4096\n" \
"v_mfma_f32_16x16x32_bf16 v[140:143], a[92:95], a[28:31], 0\n" \
"ds_read_b64_tr_b16 a[136:137], %[vvb] offset:512\n" \
"v_mfma_f32_16x16x32_bf16 v[140:143], a[88:91], a[24:27], v[140:143]\n" \
"ds_read_b64_tr_b16 a[138:139], %[vvb] offset:4608\n" \
"v_mfma_f32_16x16x32_bf16 v[140:143], a[84:87], a[20:23], v[140:143]\n" \
"ds_read_b64_tr_b16 a[140:141], v211 offset:512\n" \
"v_mfma_f32_16x16x32_bf16 v[140:143], a[80:83], a[16:19], v[140:143]\n" \
"ds_read_b64_tr_b16 a[142:143], v211 offset:4608\n" \
"v_mfma_f32_16x16x32_bf16 v[144:147], a[64:67], a[32:35], 0\n" \
"ds_read_b64_tr_b16 a[144:145], %[vvb] offset:1024\n" \
"v_mfma_f32_16x16x32_bf16 v[144:147], a[68:71], a[36:39], v[144:147]\n" \
"ds_read_b64_tr_b16 a[146:147], %[vvb] offset:5120\n" \
"v_mfma_f32_16x16x32_bf16 v[144:147], a[72:75], a[40:43], v[144:147]\n" \
"ds_read_b64_tr_b16 a[148:149], v211 offset:1024\n" \
"v_mfma_f32_16x16x32_bf16 v[144:147], a[76:79], a[44:47], v[144:147]\n" \
"ds_read_b64_tr_b16 a[150:151], v211 offset:5120\n" \
"v_mfma_f32_16x16x32_bf16 v[148:151], a[92:95], a[44:47], 0\n" \
"ds_read_b64_tr_b16 a[152:153], %[vvb] offset:1536\n" \
"v_mfma_f32_16x16x32_bf16 v[148:151], a[88:91], a[40:43], v[148:151]\n" \
"ds_read_b64_tr_b16 a[154:155], %[vvb] offset:5632\n" \
"v_mfma_f32_16x16x32_bf16 v[148:151], a[84:87], a[36:39], v[148:151]\n" \
"ds_read_b64_tr_b16 a[156:157], v211 offset:1536\n" \
"v_mfma_f32_16x16x32_bf16 v[148:151], a[80:83], a[32:35], v[148:151]\n" \
"ds_read_b64_tr_b16 a[158:159], v211 offset:5632\n" \
"v_mfma_f32_16x16x32_bf16 v[152:155], a[64:67], a[48:51], 0\n" \
"v_mfma_f32_16x16x32_bf16 v[152:155], a[68:71], a[52:55], v[152:155]\n" \
"v_mfma_f32_16x16x32_bf16 v[152:155], a[72:75], a[56:59], v[152:155]\n" \
"v_mfma_f32_16x16x32_bf16 v[152:155], a[76:79], a[60:63], v[152:155]\n" \
"v_mfma_f32_16x16x32_bf16 v[156:159], a[92:95], a[60:63], 0\n" \
"v_mfma_f32_16x16x32_bf16 v[156:159], a[88:91], a[56:59], v[156:159]\n" \
"v_mfma_f32_16x16x32_bf16 v[156:159], a[84:87], a[52:55], v[156:159]\n" \
"v_mfma_f32_16x16x32_bf16 v[156:159], a[80:83], a[48:51], v[156:159]\n" \
"s_branch Lhavem_%=\n" \
"s_nop 0\n" \
".p2align 12\n" \
"adaln_pg0:\n" \
"s_cbranch_execz adaln_pg1\n" \
"Lend_%=:\n" \
""

#define SRD_WORD3 0x00020000
DI u32x4 ld16_sc1(const void* p) { u32x4 v; asm volatile("global_load_dwordx4 %0, %1, off sc1" : "=v"(v) : "v"(p) : "memory"); return v; }
DI unsigned ord_key(float r, unsigned row) {
  const unsigned b = __float_as_uint(r), o = (b & 0x80000000u) ? ~b : (b | 0x80000000u);
  return max(o & 0xfffff800u, 0x800u) | (2047u - row);
}
DI void top2_merge(unsigned& a1, unsigned& a2, unsigned b1, unsigned b2) {
  const unsigned n1 = max(a1, b1), n2 = max(min(a1, b1), max(a2, b2)); a1 = n1; a2 = n2;
}
template <int CTRL> DI void top2_dpp(unsigned& k1, unsigned& k2) {
  const unsigned p1 = __builtin_amdgcn_update_dpp(0u, k1, CTRL, 0xf, 0xf, true), p2 = __builtin_amdgcn_update_dpp(0u, k2, CTRL, 0xf, 0xf, true); top2_merge(k1, k2, p1, p2);
}
DI float row_sum(float v) {
  v = dpp_add<DPP_XOR1>(v); v = dpp_add<DPP_XOR2>(v); v = dpp_add<DPP_ROR8>(v);
  v = swap16_add(v, v); return swap32_add(v, v);
}
DI unsigned h2_pair(float x0, float x1, float m, float rden, unsigned c1w, unsigned sbw) { return ln_pair(x0, x1, m, rden, lo16(c1w), hi16(c1w), lo16(sbw), hi16(sbw)); }
#define PAGE(n, nx) asm volatile("s_branch adaln_pe" #n "\n.p2align 12\nadaln_pg" #n ":\n\ts_cbranch_execz " nx "\nadaln_pe" #n ":")
#define STR_(x) #x
#define STR(x) STR_(x)
constexpr int MOE_WGS = DFF / 16;
constexpr unsigned RETRY_CAP = 4096;
#define ASLEEP 2
#define KLOOK 6
#define YSPLIT 8
#define FSLEEP 14
#define KSLEEP 3
#define YEARLY 8
#define FREST 0
#define KL0 1u
#define KLS 2u
#define KST 2u
#define KCHK 8
#define KPAUSE 2
#define KEY_SEL b

#define WBYTES (moe ? DM * DFF * 2 : 0)
namespace fa {
constexpr unsigned LDS_BYTES = 163840;
constexpr float QSCALE = 0.12751743082f;
struct Args { const u16 *x, *wmod, *wq, *kc, *vc, *wo, *wg, *wu, *wd, *c, *wr; char* ws; u16* out;
};
__global__ __launch_bounds__(256, 1) void fattn_k(const Args p) {
  extern __shared__ __attribute__((aligned(16))) char lds[];
  const unsigned wid = __builtin_amdgcn_readfirstlane(threadIdx.x >> 6);
  const unsigned b = blockIdx.x, idx = b >> 3, kg = b & 3u, m = idx & 7u, hd = 8u * kg + m, y = (b & 4u) + (idx >> 3);
  const unsigned ldsb = (unsigned)(size_t)lds;
  const unsigned m0b = ldsb + 4096u * wid, m0t = ldsb + 131072u + 8192u * wid;
  const __amdgpu_buffer_rsrc_t srdz = __builtin_amdgcn_make_buffer_rsrc((void*)p.ws, (short)0, (int)WS_END, SRD_WORD3);
  const bool moe = b < MOE_WGS;
  const unsigned sl = 4u * ((b & 7u) * 7u + (idx >> 2)) + (idx & 3u), yp2 = (y & 1u) * 2u;
  const unsigned sin = (unsigned)WS_XIN + ((y * 32u + hd) * 4u + wid) * BOX;
  u32x2 xq[8]; unsigned fl, sd0, got, got2; unsigned long long tag; u16* pout;
  f32x2 xf[16]; f32x4 gaf[8], aw[8]; u32x4 alo, ahi; unsigned late; float arow;
#define FQ(i)
#define TQ(i)
  {
    const unsigned tid = threadIdx.x, lane = tid & 63u, l16 = lane & 15u, g = lane >> 4, hi = lane >> 5, r8 = (lane >> 2) & 7u;
    unsigned kb0 = ldsb + 256u * l16 + 16u * (g ^ l16);
    unsigned vvb = ldsb + 65536u + 2048u * hi + 288u * (g & 1u) + 64u * ((lane >> 2) & 3u) + 8u * (lane & 3u);
    unsigned vwb = ldsb + 65536u + 2048u * g + 64u * ((lane >> 2) & 3u) + 16u * (lane & 3u);
    unsigned kd0 = 4096u * wid + 256u * g + 16u * (l16 ^ g);
    unsigned vd0 = 4096u * wid + 256u * r8 + 64u * hi + 16u * ((lane & 3u) ^ (2u * (r8 >> 2))) + 1024u;
    unsigned wd0 = 8192u * (16u * wid + r8) + 64u * hi + 16u * (lane & 3u) + 4096u;
    unsigned hq0 = (unsigned)WS_H + (16u * y + 4u * wid) * 32768u + 16u * lane;
    unsigned oq0 = (unsigned)WS_XIN + ((y * 32u + kg * 8u) * 4u + wid) * BOX + 20480u + 16u * lane;
    unsigned flag = ldsb + 49152u + 4u * wid, wbytes = moe ? DM * DFF * 2 : 0;
    const __amdgpu_buffer_rsrc_t srdw = __builtin_amdgcn_make_buffer_rsrc((void*)((const char*)p.wq + 256u * hd - 4096), (short)0, (int)(DM * DQ * 2 - 256u * hd + 4096u), SRD_WORD3);
    __amdgpu_buffer_rsrc_t srdk = __builtin_amdgcn_make_buffer_rsrc((void*)((const char*)p.kc + (size_t)hd * (SEQ * 256)), (short)0, SEQ * 256, SRD_WORD3);
    __amdgpu_buffer_rsrc_t srdv = __builtin_amdgcn_make_buffer_rsrc((void*)((const char*)p.vc + (size_t)hd * (SEQ * 256) - 17408), (short)0, SEQ * 256 + 17408, SRD_WORD3);
    unsigned long long pct; unsigned clen;
    asm volatile("s_getpc_b64 %0\n\ts_mov_b32 %1, adaln_text_end@rel32@lo+4" : "=s"(pct), "=s"(clen));
    const unsigned vct = min(((idx * 4u + wid) * 6u + min(lane, 5u)) * 128u, (clen - 4u) & ~3u);
      const unsigned gr = 128u * (b & 7u) + 4u * (tid >> 3), gc = 64u * (b >> 3) + 8u * (tid & 7u);
      u32x4 w[4], xs[4]; u32x2 ep, cv;
#define XV(i) ({ u32x4 v_; asm volatile("v_accvgpr_read_b32 %0, a[%4]\n\tv_accvgpr_read_b32 %1, a[%4 + 1]\n\tv_accvgpr_read_b32 %2, a[%4 + 2]\n\tv_accvgpr_read_b32 %3, a[%4 + 3]" : "=v"(v_[0]), "=v"(v_[1]), "=v"(v_[2]), "=v"(v_[3]) : "n"(200 + 4 * (i))); v_; })
      asm volatile("global_load_dwordx2 %0, %1, %2" : "=v"(cv) : "v"(gr * 2u), "s"(p.c) : "memory");
      #pragma unroll
      for (int j = 0; j < 4; ++j) asm volatile("global_load_dwordx4 %0, %1, %2 nt" : "=v"(w[j]) : "v"(((gr + j) * NMOD + gc) * 2u), "s"(p.wmod) : "memory");
      #pragma unroll
      for (int q = 0; q < 4; ++q) asm volatile("global_load_dwordx4 %0, %1, %2 offset:%3 nt" : "=v"(xs[q]) : "v"((8u * (y * 32u + hd) + 2u * wid) * 2048u + 16u * lane), "s"(p.x), "n"(1024 * q) : "memory");
      asm volatile("global_load_dwordx2 %0, %1, %2 sc1" : "=v"(ep) : "v"(0u), "s"(p.ws + WS_EP) : "memory");
      asm volatile("buffer_store_dwordx4 %0, %1, %2, 0 offen sc1\n\ts_nop 1" :: "v"(u32x4{0u, 0u, 0u, 0u}), "v"(tid < Z_SHARE / 16u ? (unsigned)WS_Z + b * Z_SHARE + tid * 16u : 0x80000000u), "s"(srdz) : "memory");
      FQ(6);
      asm volatile("global_load_dword a255, %0, %1" :: "v"(vct), "s"(pct) : "memory");
      asm volatile("s_mov_b64 exec, 0\n\ts_cmp_eq_u32 %0, 1\n\ts_cbranch_scc1 adaln_pg5\n\ts_cmp_eq_u32 %0, 2\n\ts_cbranch_scc1 adaln_pg6\n\ts_cmp_eq_u32 %0, 3\n\ts_cbranch_scc1 adaln_pg7\nadaln_fr:\n\ts_mov_b64 exec, -1" :: "s"(wid) : "scc");
      FQ(7);
      unsigned tq0 = (idx * 8u + wid * 2u + ((lane >> 3) & 1u)) * 8192u + (b & 4u) * 256u + (lane & 7u) * 128u + 4096u - 256u * m;
      const unsigned rb0 = 16u * y + 4u * (hd >> 3), ch = 16u * (hd & 7u) + (tid >> 4);
      unsigned vrw = ldsb + 135168u + wid * 256u + 32u * (tid & 7u) + 8u * g, vrr = ldsb + 135168u + 4u * lane;
      unsigned vmo = wid ? 0x80000000u : (unsigned)WS_MOD + b * 512u + lane * 8u, vst = (lane & 31u) ? 0x80000000u : (unsigned)WS_RST + (8u * (y * 32u + hd) + 2u * wid + hi) * 16u;
      unsigned vx2 = (rb0 * 16u + l16) * 2048u + ch * 16u;
      pout = p.out;
      asm volatile("s_waitcnt vmcnt(11)" : "+v"(cv), "+v"(tq0), "+v"(vrw), "+v"(vrr), "+v"(vmo), "+v"(vst), "+v"(vx2), "+s"(pout) :: "memory");
      auto silu2 = [](unsigned v) { const f32x2 f = unpk(v); return cvtpk(f[0] * __builtin_amdgcn_rcpf(1.f + __builtin_amdgcn_exp2f(f[0] * -1.44269504089f)), f[1] * __builtin_amdgcn_rcpf(1.f + __builtin_amdgcn_exp2f(f[1] * -1.44269504089f))); };
      auto dot2 = [](unsigned x, unsigned z, float c) { return __builtin_amdgcn_fdot2_f32_bf16(__builtin_bit_cast(bf16x2, x), __builtin_bit_cast(bf16x2, z), c, false); };
      unsigned s01 = silu2(cv[0]), s23 = silu2(cv[1]);
      asm volatile("s_waitcnt vmcnt(7)" : "+v"(s01), "+v"(s23), "+v"(w[0]), "+v"(w[1]), "+v"(w[2]), "+v"(w[3]) :: "memory");
      FQ(4);
      asm volatile("buffer_load_dword a255, %0, %1, 0 offen" :: "v"(tq0), "s"(srdw) : "memory");
      {
        float acc[8];
        #pragma unroll
        for (int e = 0; e < 4; ++e) {
          acc[2 * e]     = dot2(__builtin_amdgcn_perm(w[3][e], w[2][e], 0x05040100u), s23, dot2(__builtin_amdgcn_perm(w[1][e], w[0][e], 0x05040100u), s01, 0.f));
          acc[2 * e + 1] = dot2(__builtin_amdgcn_perm(w[3][e], w[2][e], 0x07060302u), s23, dot2(__builtin_amdgcn_perm(w[1][e], w[0][e], 0x07060302u), s01, 0.f));
        }
        float t[4], u[2];
        #pragma unroll
        for (int e = 0; e < 4; ++e) t[e] = swap32_add(acc[e], acc[e + 4]);
        #pragma unroll
        for (int e = 0; e < 2; ++e) u[e] = dpp_add<DPP_ROR8>(swap16_add(t[e], t[e + 2]));
        asm volatile("ds_write_b64 %5, %6\n\ts_waitcnt vmcnt(3)" : "+v"(xs[0]), "+v"(xs[1]), "+v"(xs[2]), "+v"(xs[3]), "+v"(ep) : "v"(vrw), "v"(f32x2{u[0], u[1]}) : "memory");
      }
      tag = (((unsigned long long)(unsigned)__builtin_amdgcn_readfirstlane(ep[1]) << 32) | (unsigned)__builtin_amdgcn_readfirstlane(ep[0])) + 1ull;
      const unsigned tl = (unsigned)tag & 0xffu, th = (unsigned)(tag >> 8);
      u32x4 sg;
      {
        float t4[4] = {0.f, 0.f, 0.f, 0.f};
        #pragma unroll
        for (int e = 0; e < 16; ++e) { const unsigned xw = xs[e >> 2][e & 3]; t4[2 * (e >> 3)] = dot2(xw, 0x3f803f80u, t4[2 * (e >> 3)]); t4[2 * (e >> 3) + 1] = dot2(xw, xw, t4[2 * (e >> 3) + 1]); }
        float u = swap16_add(swap32_add(t4[0], t4[2]), swap32_add(t4[1], t4[3]));
        u = dpp_add<DPP_XOR1>(u); u = dpp_add<DPP_XOR2>(u); u = dpp_add<DPP_ROR4>(u); u = dpp_add<DPP_ROR8>(u);
        const auto r16 = __builtin_amdgcn_permlane16_swap(__float_as_uint(u), __float_as_uint(u), false, false);
        const float mean = __uint_as_float(r16[0]) * (1.f / DM), m2 = mean * mean;
        float var = fmaf(__uint_as_float(r16[1]), 1.f / DM, -m2);
        if (__builtin_expect(__any(!(m2 <= var)), 0)) {
          const f32x2 mm = {__uint_as_float(__builtin_amdgcn_readlane(__float_as_uint(mean), 0)), __uint_as_float(__builtin_amdgcn_readlane(__float_as_uint(mean), 32))};
          f32x2 sq = {0.f, 0.f};
          #pragma unroll
          for (int e = 0; e < 8; ++e) {
            const f32x2 d0 = f32x2{lo16(xs[e >> 2][e & 3]), lo16(xs[2 + (e >> 2)][e & 3])} - mm, d1 = f32x2{hi16(xs[e >> 2][e & 3]), hi16(xs[2 + (e >> 2)][e & 3])} - mm;
            sq = __builtin_elementwise_fma(d1, d1, __builtin_elementwise_fma(d0, d0, sq));
          }
          const float v0 = wave_sum(sq[0]), v1 = wave_sum(sq[1]);
          var = (hi ? v1 : v0) * (1.f / DM);
        }
        sg = u32x4{cvtpk(0.f, mean) | tl, th, cvtpk(0.f, __builtin_amdgcn_sqrtf(rbh(rbh(var) + 1e-5f))) | tl, th};
      }
      {
        f32x2 ra, rc;
        asm volatile("s_waitcnt vmcnt(2) lgkmcnt(0)\n\ts_barrier\n\tds_read2st64_b32 %0, %2 offset1:1\n\tds_read2st64_b32 %1, %2 offset0:2 offset1:3\n\t"
                     "buffer_store_dwordx4 %3, %4, %5, 0 offen sc1\n\ts_waitcnt lgkmcnt(0)" : "=&v"(ra), "=&v"(rc) : "v"(vrr), "v"(sg), "v"(vst), "s"(srdz) : "memory");
        ra += rc;
        asm volatile("buffer_store_dwordx2 %0, %1, %2, 0 offen sc1" :: "v"(u32x2{((__float_as_uint(ra[0] + ra[1]) + 0x80u) & 0xffffff00u) | tl, th}), "v"(vmo), "s"(srdz) : "memory");
      }
      FQ(5);
      asm volatile("global_load_dwordx2 a[216:217], %0, %1" :: "v"(tid * 8u), "s"(p.c) : "memory");
      #pragma unroll
      for (int i = 0; i < 4; ++i) asm volatile("global_load_dwordx4 a[%2:%2 + 3], %0, %1" :: "v"(vx2 + i * 32768u), "s"(p.x), "n"(200 + 4 * i) : "memory");
      PAGE(5, "adaln_fr");
      unsigned lane2 = lane; asm volatile("" : "+v"(lane2));
#define lane lane2
    unsigned wo0 = (32u * wid + r8) * 2048u + m * 256u + 64u * hi + 16u * ((lane & 3u) ^ (2u * (r8 >> 2)));
    __amdgpu_buffer_rsrc_t srdt = __builtin_amdgcn_make_buffer_rsrc((void*)((const char*)p.wo + (size_t)kg * (DM * DM * 2)), (short)0, DM * DM * 2, SRD_WORD3);
    __amdgpu_buffer_rsrc_t srdp = __builtin_amdgcn_make_buffer_rsrc((void*)((const char*)(y < 4u ? p.kc : p.vc) + (size_t)hd * (SEQ * 256) + (y & 3u) * 4096u), (short)0, (int)(SEQ * 256 - (y & 3u) * 4096u), SRD_WORD3);
    unsigned vg0 = (256u * wid + (lane >> 1)) * (DFF * 2u) + sl * 32u + 16u * (lane & 1u);
    unsigned vdn = (16u * sl + hi) * 2048u + 512u * wid + 16u * (lane & 31u);
    const unsigned mc0 = 2u * DM + 64u * ((idx >> 2) * 8u + (b & 7u)) + 16u * (idx & 3u);
    const unsigned md0 = (256u * wid + (lane >> 1)) * (NMOD * 2u) + mc0 * 2u + 16u * (lane & 1u);
    sd0 = ldsb + 131072u + 8192u + 512u * wid + 16u * g;
    fl = md0;
    unsigned rd0 = ldsb + 131072u + 7168u + 8192u * ((lane >> 2) & 3u) + 256u * wid + 4u * (lane & 3u);
    unsigned ta0 = m0t + 256u * g + 32u * ((lane >> 2) & 3u) + 8u * (lane & 3u);
    const unsigned c8 = (b * 4u + wid) * 8192u, cd = moe ? sl * 32768u + wid * 8192u : 0u, cw = (idx * 4u + wid) * 16384u;
    const unsigned cm = moe ? ((sl & 3u) * 4u + wid) * 64u * (DFF * 2u) + (sl >> 2) * 128u : 0u;
    unsigned vxs = BOX * (l16 >> 1) + 64u * (l16 & 1u) + 16u * g;
    const unsigned sxs = (unsigned)WS_XIN + (y * 128u + wid * 32u) * BOX + (m >> 1) * 1024u + kg * 256u + (m & 1u) * 128u;
    unsigned vx0 = ((256u * y + 8u * hd + 2u * wid + ((lane >> 2) & 1u)) * DM + 256u * hi + 128u * (l16 >> 3) + 16u * (g & 1u) + 4u * (lane & 3u) + 32u * yp2) * 2u;
    unsigned tlo = 0, thi = 0;
#define tab(ln, lo, hi2) asm("v_writelane_b32 %0, %2, " #ln "\n\tv_writelane_b32 %1, %3, " #ln : "+v"(tlo), "+v"(thi) : "s"(lo), "s"(hi2))
#define tabp(ln, q, off) do { const unsigned long long a_ = (size_t)(q) + (off); tab(ln, (unsigned)a_, (unsigned)(a_ >> 32)); } while (0)
#define TORD 0
    tabp(1, p.x, ((y * 32u + hd) * 2u + (wid & 1u)) * 8192u); tabp(2, p.wu, moe ? c8 : 0u); tabp(3, p.wo, c8); tabp(4, p.wd, cd);
    tabp(5, p.wo, kg * 2097152u + cw); tabp(6, p.wo, kg * 2097152u + cw + 8192u); tabp(7, p.wg, cm);
    tabp(17, p.wmod, 0u); tab(18, NP_FILL == 8 ? sin + 20480u : (unsigned)WS_XIN + ((y * 32u + kg * 8u + (m >> 1)) * 4u + wid) * BOX + 20480u + (m & 1u) * 8192u, (unsigned)WS_OFL + y * 16384u + kg * 128u);
    tab(19, (unsigned)WS_OFL + (y * 32u + hd) * 512u + wid * 128u, (unsigned)WS_OFL + (y * 32u + kg * 8u) * 512u + wid * 128u);
    tabp(8, p.wg, 0u); tabp(9, p.wu, 0u); tabp(10, p.wd, 0u);
    tab(11, m, m * 4u * BOX); tab(12, (y & 1u) * 128u, 128u - (y & 1u) * 128u);
    tab(13, 0u, (unsigned)WS_PFL + (y * 32u + hd) * 512u + kg * 128u); tab(14, sxs, 0u);
    tab(15, (unsigned)WS_MODT + (mc0 - 2u * DM + 4u * wid) * 4u, (mc0 >> 10) == 4u ? 0x3f800000u : 0u);
    tab(16, (unsigned)WS_PFL + y * 16384u + (hd * 4u + wid) * 4u, (unsigned)WS_PFL + (y * 32u + hd) * 512u + wid * 128u);
    const char* psig = p.ws + WS_CNT + y * 8192u + hd * 8u;
    const char* pcnt = p.ws + WS_CNT + y * 8192u + hd * 256u;
    tabp(20, p.wr, 0u); tab(21, (unsigned)WS_PART + b * 8192u + wid * 2048u, 0u);
      asm volatile("" : "+v"(tlo), "+v"(thi), "+v"(wo0), "+v"(vg0), "+v"(vdn), "+v"(ta0), "+v"(vxs), "+v"(vx0), "+v"(rd0), "+v"(fl), "+v"(sd0), "+v"(kb0), "+v"(vvb), "+v"(vwb), "+v"(kd0), "+v"(vd0), "+v"(wd0), "+v"(hq0), "+v"(oq0));
      asm volatile("" : "+s"(srdk), "+s"(srdv), "+s"(srdt), "+s"(srdp), "+s"(psig), "+s"(pcnt), "+s"(flag), "+s"(wbytes));
#undef lane
      u32x2 mp[8]; u32x4 rs;
      unsigned vlm = ((wid >> 1) * 16u + 2u * (hd & 7u) + (wid & 1u)) * 4096u + lane * 8u, vlr = (unsigned)WS_RST + ((rb0 + wid) * 16u + l16) * 16u;
#define LOOK9 "buffer_load_dwordx2 %0, %[vlm], %[srdz], %[som] offen sc1\n\tbuffer_load_dwordx2 %1, %[vlm], %[srdz], %[som] offen offset:512 sc1\n\tbuffer_load_dwordx2 %2, %[vlm], %[srdz], %[som] offen offset:1024 sc1\n\t" \
              "buffer_load_dwordx2 %3, %[vlm], %[srdz], %[som] offen offset:1536 sc1\n\tbuffer_load_dwordx2 %4, %[vlm], %[srdz], %[som] offen offset:2048 sc1\n\tbuffer_load_dwordx2 %5, %[vlm], %[srdz], %[som] offen offset:2560 sc1\n\t" \
              "buffer_load_dwordx2 %6, %[vlm], %[srdz], %[som] offen offset:3072 sc1\n\tbuffer_load_dwordx2 %7, %[vlm], %[srdz], %[som] offen offset:3584 sc1\n\tbuffer_load_dwordx4 %8, %[vlr], %[srdz], 0 offen sc1\n\t"
#define LREG(c) c "{v[0:1]}"(mp[0]), c "{v[2:3]}"(mp[1]), c "{v[4:5]}"(mp[2]), c "{v[6:7]}"(mp[3]), c "{v[8:9]}"(mp[4]), c "{v[10:11]}"(mp[5]), c "{v[12:13]}"(mp[6]), c "{v[14:15]}"(mp[7]), c "{v[16:19]}"(rs)
      asm volatile(LOOK9 : LREG("=&") : [vlm] "v"(vlm), [vlr] "v"(vlr), [srdz] "s"(srdz), [som] "s"((unsigned)WS_MOD) : "memory");
      #pragma unroll
      for (int q = 0; q < 14; ++q)
        asm volatile("s_mov_b32 m0, %0\n\ts_nop 0\n\tbuffer_load_dwordx4 %1, %2, %3 offen lds" :: "s"(m0b + 65536u + 16384u * (q >> 2) + 1024u * (q & 3)), "v"(wd0), "s"(srdw),
                     "s"(0x80000u * (q >> 2) + 65536u * ((q >> 1) & 1) + 128u * (q & 1)) : "m0", "memory");
      asm volatile("s_waitcnt vmcnt(23)" ::: "memory");
      { const u32x4 cs = XV(4); *(u32x2*)(lds + 131072 + 8192 + tid * 8u) = u32x2{silu2(cs[0]), silu2(cs[1])}; }
      f32x2 xu[16];
      #pragma unroll
      for (int i = 0; i < 4; ++i) { const u32x4 o = XV(i);
        #pragma unroll
        for (int e = 0; e < 4; ++e) { xu[4 * i + e] = unpk(o[e]); asm volatile("" : "+v"(xu[4 * i + e])); } }
      unsigned vpw = ldsb + 131072u + 4u * tid, vpr = ldsb + 131072u + 1024u + 32u * l16 + 8u * wid, vpc = ldsb + 131072u + 32u * (tid >> 4), vpq = ldsb + 131072u + 1024u + 32u * l16;
      unsigned vh = (unsigned)WS_H + rb0 * 32768u + ch * 256u + 16u * l16; float one = wid >> 1 ? 1.f : 0.f;
      {
        unsigned bl, bh, cnt = SPIN_CAP;
#define XO(d, r, t) "v_bitop3_b32 %[" #d "], v" #r ", %[" #t "], %[" #d "] bitop3:0xbe\n\t"
        asm volatile(
          "s_waitcnt vmcnt(14)\n\t"
          "s_branch Lm1_%=\n"
          "Lm0_%=:\n\t"
          "s_sleep " STR(ASLEEP) "\n\t"
          LOOK9
          "s_waitcnt vmcnt(0)\n"
          "Lm1_%=:\n\t"
          "v_bitop3_b32 %[bl], v0, v2, %[tl] bitop3:0x7e\n\t"
          "v_bitop3_b32 %[bh], v1, v3, %[th] bitop3:0x7e\n\t"
          XO(bl, 4, tl) XO(bh, 5, th) XO(bl, 6, tl) XO(bh, 7, th) XO(bl, 8, tl) XO(bh, 9, th) XO(bl, 10, tl) XO(bh, 11, th) XO(bl, 12, tl) XO(bh, 13, th) XO(bl, 14, tl) XO(bh, 15, th)
          XO(bl, 16, tl) XO(bh, 17, th) XO(bl, 18, tl) XO(bh, 19, th)
          "v_and_or_b32 %[bl], %[bl], %[ff], %[bh]\n\t"
          "v_cmp_ne_u32 vcc, 0, %[bl]\n\t"
          "s_sub_u32 %[cnt], %[cnt], 1\n\t"
          "s_cbranch_scc1 Lm2_%=\n\t"
          "s_cbranch_vccnz Lm0_%=\n"
          "Lm2_%=:"
          : LREG("+"), [bl] "=&v"(bl), [bh] "=&v"(bh), [cnt] "+s"(cnt), "+v"(vpw), "+v"(vpr), "+v"(vpc), "+v"(vpq), "+v"(vh), "+v"(one)
          : [vlm] "v"(vlm), [vlr] "v"(vlr), [srdz] "s"(srdz), [som] "s"((unsigned)WS_MOD), [tl] "s"(tl), [th] "s"(th), [ff] "v"(0xffu) : "vcc", "scc", "memory");
      }
      f32x4 a0, a1, c0, c1, t0, t1;
      {
        auto val = [&](int k) { return __uint_as_float(mp[k][0] & 0xffffff00u); };
        const float v = rbh(one + rbh(((val(0) + val(1)) + (val(2) + val(3))) + ((val(4) + val(5)) + (val(6) + val(7)))));
        const float rd = __builtin_amdgcn_rcpf(hi16(rs[2]));
        asm volatile("ds_write_b32 %0, %1\n\tds_write_b64 %2, %3" :: "v"(vpw), "v"(v * QSCALE), "v"(vpr), "v"(f32x2{-hi16(rs[0]) * rd, rd}) : "memory");
      }
      PAGE(6, "adaln_fr");
      asm volatile("s_waitcnt lgkmcnt(0)\n\ts_barrier\n\tds_read_b128 %4, %7\n\tds_read_b128 %0, %6\n\tds_read_b128 %2, %6 offset:512\n\tds_read_b128 %1, %6 offset:16\n\tds_read_b128 %3, %6 offset:528\n\tds_read_b128 %5, %7 offset:16\n\t"
                   "buffer_load_dword a255, %8, %9, %10 offen\n\ts_waitcnt lgkmcnt(0)"
                   : "=&v"(a0), "=&v"(a1), "=&v"(c0), "=&v"(c1), "=&v"(t0), "=&v"(t1) : "v"(vpc), "v"(vpq), "v"(tq0), "s"(srdw), "s"(0x200000u) : "memory");
      FQ(0);
      #pragma unroll
      for (int i = 0; i < 4; ++i) {
        const float mn = i < 2 ? t0[2 * i] : t1[2 * i - 4], rd = i < 2 ? t0[2 * i + 1] : t1[2 * i - 3];
        u32x4 o;
        #pragma unroll
        for (int e = 0; e < 4; ++e) {
          const f32x2 sc = e < 2 ? f32x2{c0[2 * e], c0[2 * e + 1]} : f32x2{c1[2 * e - 4], c1[2 * e - 3]}, sh = e < 2 ? f32x2{a0[2 * e], a0[2 * e + 1]} : f32x2{a1[2 * e - 4], a1[2 * e - 3]};
          o[e] = cvtpk2(__builtin_elementwise_fma(__builtin_elementwise_fma(xu[4 * i + e], f32x2{rd, rd}, f32x2{mn, mn}), sc, sh));
        }
        asm volatile("buffer_store_dwordx4 %0, %1, %2, %3 offen sc1\n\ts_nop 1" :: "v"(o), "v"(vh), "s"(srdz), "s"(32768u * i) : "memory");
      }
      FQ(1);
    asm volatile(FA_BODY
      : FA_OUTS
      : [kb0] "v"(kb0), [vvb] "v"(vvb), [kd0] "v"(kd0), [vd0] "v"(vd0), [oq0] "v"(oq0),
        [m0b] "s"(m0b), [flag] "s"(flag), [srdz] "s"(srdz), [srdw] "s"(srdw), [srdk] "s"(srdk), [srdv] "s"(srdv), [m0t] "s"(m0t), [srdt] "s"(srdt), [srdp] "s"(srdp),
        [tag] "s"(tag), [pcnt] "s"(pcnt), [psig] "s"(psig), [wid] "s"(wid), [rd0] "v"(rd0), [wo0] "v"(wo0), [vg0] "v"(vg0), [vx0] "v"(vx0), [px] "s"(p.x), [vct] "v"(vct), [pct] "s"(pct), [sin] "s"(sin), [vdn] "v"(vdn), [ta0] "v"(ta0), [vxs] "v"(vxs),
        [tlo] "v"(tlo), [thi] "v"(thi), [wbytes] "s"(wbytes)
      : FA_CLOBBERS);
  }
#define RDELAYN 24
#define RDELAY2 8
#define RDELAY3 16
#define RCHKN 5
#define EARLYOUT 1
  unsigned lane; asm volatile("v_mbcnt_lo_u32_b32 %0, -1, 0\n\tv_mbcnt_hi_u32_b32 %0, -1, %0" : "=v"(lane));
  const unsigned tid = wid * 64u + lane, n = lane & 15u, g = lane >> 4;
  const unsigned r0 = 256u * y + 8u * hd + 2u * wid, row = r0 + ((n >> 2) & 1u);
  const unsigned colb = 256u * (g >> 1) + 128u * (n >> 3) + 16u * (g & 1u) + 4u * (n & 3u);
  char* par = lds;
  const unsigned cq0 = colb + 32u * yp2, cq1 = colb + 32u * (2u ^ yp2);
  auto parq = [&](int o, int k) { return *(const u32x2*)(par + o + (k >> 2 ? cq1 : cq0) * 2u + 64 * ((k >> 1) & 1) + 1024 * (k & 1)); };
  auto parf = [&](int o, int k) { return *(const f32x4*)(par + o + (k >> 2 ? cq1 : cq0) * 4u + 128 * ((k >> 1) & 1) + 2048 * (k & 1)); };
  if (__builtin_expect(__any(late != 0u), 0)) {
    u32x4 mt[4]; u32x2 wr;
    asm volatile("s_barrier" ::: "memory");
    for (unsigned spins = 0; spins <= SPIN_CAP; ++spins) {
      asm volatile("s_nop 4\n\tglobal_load_dwordx2 %0, %1, %2" : "=v"(wr) : "v"(tid * 8u), "s"(p.wr) : "memory");
      #pragma unroll
      for (int k = 0; k < 4; ++k) asm volatile("buffer_load_dwordx4 %0, %1, %2, %3 offen sc1" : "=v"(mt[k]) : "v"(tid * 16u), "s"(srdz), "s"((unsigned)WS_MODT + 4096u * k) : "memory");
      asm volatile("s_waitcnt vmcnt(0)" : "+v"(mt[0]), "+v"(mt[1]), "+v"(mt[2]), "+v"(mt[3]), "+v"(wr) :: "memory");
      const u32x4 t = mt[0] & mt[1] & mt[2] & mt[3];
      if (__all((t[0] & t[1] & t[2] & t[3] & 1u) != 0u)) break;
    }
    const f32x4 a = __builtin_bit_cast(f32x4, mt[2]) * f32x4{lo16(wr[0]), hi16(wr[0]), lo16(wr[1]), hi16(wr[1])};
    *(u32x4*)(par + tid * 16u) = mt[0]; *(f32x4*)(par + 4096 + tid * 16u) = a;
    #pragma unroll
    for (int k = 1; k < 4; ++k) *(u32x2*)(par + (k == 3 ? 14336 : 6144 + 2048 * k) + tid * 8u) = u32x2{__builtin_amdgcn_perm(mt[k][1], mt[k][0], 0x07060302u), __builtin_amdgcn_perm(mt[k][3], mt[k][2], 0x07060302u)};
    *(float*)(par + 12424 + 16u * wid) = wave_sum((a[0] + a[1]) + (a[2] + a[3]));
    asm volatile("s_waitcnt lgkmcnt(0)\n\ts_barrier" ::: "memory");
    #pragma unroll
    for (int k = 0; k < 8; ++k) { gaf[k] = parf(0, k); aw[k] = parf(4096, k); }
    arow = dpp_add<DPP_XOR2>(dpp_add<DPP_XOR1>(*(const float*)(par + 12424 + 16u * (lane & 3u))));
  }
  unsigned vin = lane * 16u, vin3 = lane * 16u + 12288u, vin2 = lane * 16u + 8192u, kp = (unsigned)WS_KEYS + (KEY_SEL & (KEY_REP - 1u)) * (16u * KSTR) + (lane >> 2) * KSTR + 32u * (lane & 3u);
  TQ(0);
  f32x2 x1[16], s2, d2, q2, sbf[16], gbf[16], c1f[16];
  unsigned vkey, vkb, vkr, nrow, vpub, ta, tu, tb; char* pk;
  {
#define SUM2(R) asm volatile("s_nop 1\n\tv_mfma_f32_16x16x32_bf16 %0, %2, a[" #R ":" #R "+3], 0\n\tv_mfma_f32_16x16x32_bf16 %1, %2, a[" #R "+8:" #R "+11], 0\n\t" \
                             "v_mfma_f32_16x16x32_bf16 %0, %3, a[" #R "+4:" #R "+7], %0\n\tv_mfma_f32_16x16x32_bf16 %1, %3, a[" #R "+12:" #R "+15], %1\n\ts_nop 7" : "=&v"(d[0]), "=&v"(d[1]) : "v"(alo), "v"(ahi))
    auto part_ = [&](int c2, f32x2& sa, f32x2& da, f32x2& qa) __attribute__((always_inline)) {
      f32x4 d[2];
      if (c2 == 0) SUM2(64); else if (c2 == 1) SUM2(80); else if (c2 == 2) SUM2(96); else SUM2(112);
      #pragma unroll
      for (int i = 4 * c2; i < 4 * c2 + 4; ++i) {
        const int e = 2 * (i & 1);
        const f32x2 v = rb2(__builtin_elementwise_fma(f32x2{d[(i >> 1) & 1][e], d[(i >> 1) & 1][e + 1]}, f32x2{gaf[i >> 1][e], gaf[i >> 1][e + 1]}, xf[i]));
        x1[i] = v;
        sa += v; da = __builtin_elementwise_fma(v, f32x2{aw[i >> 1][e], aw[i >> 1][e + 1]}, da); qa = __builtin_elementwise_fma(v, v, qa);
      }
    };
#define RCV4(o, a, b, c, d) \
      "buffer_load_dwordx4 " #a ", %[vin" #o "], %[srdz], %[sin] offen sc1\n\t" \
      "buffer_load_dwordx4 " #b ", %[vin" #o "], %[srdz], %[sin] offen offset:1024 sc1\n\t" \
      "buffer_load_dwordx4 " #c ", %[vin" #o "], %[srdz], %[sin] offen offset:2048 sc1\n\t" \
      "buffer_load_dwordx4 " #d ", %[vin" #o "], %[srdz], %[sin] offen offset:3072 sc1\n\t"
#define RWAIT(lv, w) \
      "Lrq" #lv "_%=:\n\t" \
      "v_mov_b32 " w ", 0\n\t" \
      "s_mov_b32 %[sc], " STR(RCHKN) "\n\t" \
      LOOK2 \
      "Lrs" #lv "_%=:\n\t" \
      "ds_read_b32 %[tmp], %[vsh]\n\t" \
      "s_waitcnt lgkmcnt(0)\n\t" \
      "v_and_or_b32 %[tmp], %[tmp], " #lv ", " w "\n\t" \
      "v_cmp_eq_u32 vcc, 0, %[tmp]\n\t" \
      "s_cbranch_vccz Lrd" #lv "_%=\n\t" \
      "s_sub_u32 %[sc], %[sc], 1\n\t" \
      "s_cbranch_scc0 Lrs" #lv "_%=\n\t" \
      "s_sub_u32 %[cnt], %[cnt], 1\n\t" \
      "s_cselect_b32 %[cnt], 0, %[cnt]\n\t" \
      "s_waitcnt vmcnt(0)\n\t" \
      "v_cmp_eq_u32 vcc, 0, " w "\n\t" \
      "s_cbranch_scc1 Lrl" #lv "_%=\n\t" \
      "s_cbranch_vccnz Lrq" #lv "_%=\n" \
      "Lrd" #lv "_%=:\n\t" \
      "v_mov_b32 %[tmp], " #lv "\n\t" \
      "s_mov_b64 exec, 1\n\t" \
      "ds_or_b32 %[vsh], %[tmp]\n\t" \
      "s_mov_b64 exec, -1\n" \
      "Lrl" #lv "_%=:\n\t"
#define RCV8A RCV4(0, a[64:67], a[68:71], a[72:75], a[76:79]) RCV4(1, a[80:83], a[84:87], a[88:91], a[92:95])
#define RCV4C RCV4(2, a[96:99], a[100:103], a[104:107], a[108:111])
#define RCV4D RCV4(3, a[112:115], a[116:119], a[120:123], a[124:127])
#define RCV8B RCV4C RCV4D
#define LOOK2 "buffer_load_dwordx2 v[228:229], %[vfl], %[srdz], %[sfl] offen offset:64 sc1\n\t"
    unsigned cnt, tmp, sc;
    u32x2 sbq[8], gbq[8], c1q[8];
    #pragma unroll
    for (int k = 0; k < 8; ++k) { sbq[k] = parq(8192, k); c1q[k] = parq(10240, k); gbq[k] = parq(14336, k); }
    #pragma unroll
    for (int i = 0; i < 16; ++i) { sbf[i] = unpk(sbq[i >> 1][i & 1]); c1f[i] = unpk(c1q[i >> 1][i & 1]); asm volatile("" : "+v"(sbf[i]), "+v"(c1f[i])); }
    unsigned vfl = (lane & 31u) * 512u, vin1 = lane * 16u + 4096u; const unsigned sfl = (unsigned)WS_OFL + y * 16384u + kg * 128u;
    PAGE(1, "adaln_pg2");
    asm volatile(
      "s_mov_b32 %[go], 0\n\t"
      "s_cmp_lg_u32 %[got], 0\n\t"
      "s_waitcnt vmcnt(4)\n\t"
      "s_cbranch_scc1 Lrf_%=\n\t"
      "v_cmp_eq_u32 vcc, 0, v229\n\t"
      "s_cbranch_vccnz Lrk_%=\n\t"
      "s_mov_b32 %[got], 2\n\t"
      RCV8A
      "Lrf_%=:\n\t"
      "v_cmp_eq_u32 vcc, 0, v228\n\t"
      "s_cbranch_vccnz Lrk_%=\n\t"
      "s_mov_b32 %[go], 1\n\t"
      RCV4C
      "s_branch Lrn_%=\n"
      "Lrk_%=:\n\t"
      LOOK2
      "Lrn_%=:"
      : "+{v228}"(sd0), "+{v229}"(fl), [got] "+s"(got), [go] "=&s"(got2), [vfl] "+v"(vfl), [vin1] "+v"(vin1), "+v"(gbq[0]), "+v"(gbq[1]), "+v"(gbq[2]), "+v"(gbq[3]), "+v"(gbq[4]), "+v"(gbq[5]), "+v"(gbq[6]), "+v"(gbq[7])
      : [srdz] "s"(srdz), [sfl] "s"(sfl), [sin] "s"(sin), [vin0] "v"(vin), [vin2] "v"(vin2) : "vcc", "scc", "memory");
#define NRE
    #pragma unroll
    for (int i = 0; i < 16; ++i) { gbf[i] = unpk(gbq[i >> 1][i & 1]); asm volatile("" : "+v"(gbf[i])); }
    vkey = lane * (16u * KSTR) + (b >> 4) * KSTR + (b & 15u) * 8u; vkb = ldsb + 12288u + 8u * wid + (lane & 4u); vkr = ldsb + 12288u + 4u * (lane & 7u); nrow = 2047u - row;
    vpub = (unsigned)WS_XIN + (y * 32u + hd) * 4u * BOX + 16384u + (g >> 1) * 512u + ((n >> 3) * 8u + (g & 1u) * 4u + (n & 3u)) * 16u;
    ta = m0b + 65536u + 8u * lane; tu = m0t + 8u * lane; tb = m0b + 98304u + 16384u * (g >> 1) + 2048u * (g & 1u) + 512u * ((lane >> 2) & 3u) + 8u * (lane & 3u);
    pk = p.ws + WS_KEYS;
    s2 = f32x2{0.f, 0.f}; d2 = s2; q2 = s2;
    f32x2 s3 = s2, d3 = s2, q3 = s2;
    asm volatile(
      "s_mov_b32 %[cnt], 0x10000\n\t"
      "s_cmp_lg_u32 %[go], 0\n\t"
      "s_cbranch_scc1 Lro_%=\n\t"
      "s_cmp_lg_u32 %[got], 0\n\t"
      "s_waitcnt vmcnt(0)\n\t"
      "s_cbranch_scc1 Lrf_%=\n\t"
      "v_cmp_eq_u32 vcc, 0, v229\n\t"
      "s_cbranch_vccz Lrl2_%=\n\t"
      RWAIT(2, "v229")
      RCV8A
      "Lrf_%=:\n\t"
      "v_cmp_eq_u32 vcc, 0, v228\n\t"
      "s_cbranch_vccz Lrm_%=\n\t"
      RWAIT(4, "v228")
      RCV4C
      "s_sleep " STR(RDELAYN) "\n\t"
      "s_branch Lrp_%=\n"
      "Lrm_%=:\n\t"
      RCV4C
      "s_sleep " STR(RDELAY3) "\n\t"
      "s_branch Lrp_%=\n"
      "Lro_%=:\n\t"
      "s_sleep " STR(RDELAY2) "\n"
      "Lrp_%=:\n\t"
      RCV4D
      "s_cmp_eq_u32 %[got], 1\n\t"
      "s_cbranch_scc1 Lrz_%=\n\t"
      "s_waitcnt vmcnt(8)\n"
      "Lrz_%=:"
      : "+{v228}"(sd0), "+{v229}"(fl), [cnt] "=&s"(cnt), [tmp] "=&v"(tmp), [sc] "=&s"(sc), "+v"(s2),
        "+v"(vkey), "+v"(vkb), "+v"(vkr), "+v"(nrow), "+v"(vpub), "+v"(ta), "+v"(tu), "+v"(tb), "+s"(pk)
      : [srdz] "s"(srdz), [got] "s"(got), [go] "s"(got2), [vfl] "v"(vfl), [sfl] "s"(sfl), [sin] "s"(sin), [vin0] "v"(vin), [vin1] "v"(vin1), [vin2] "v"(vin2), [vin3] "v"(vin3), [vsh] "v"(ldsb + 12420u)
      : "vcc", "scc", "memory");
    part_(0, s2, d2, q2); part_(1, s2, d2, q2);
    TQ(2);
    asm volatile("s_waitcnt vmcnt(4)" : "+{v228}"(sd0), "+{v229}"(fl), "+v"(s2), "+v"(d2), "+v"(q2), "+v"(s3) :: "memory");
    part_(2, s3, d3, q3);
    asm volatile("s_waitcnt vmcnt(0)" : "+v"(s3), "+v"(d3), "+v"(q3) :: "memory");
    TQ(3);
    part_(3, s3, d3, q3);
    if (__builtin_expect(__any(s3[0] + s3[1] != s3[0] + s3[1]), 0)) {
      unsigned spins = 0;
      do {
        asm volatile(RCV8B "s_waitcnt vmcnt(0)" :: [srdz] "s"(srdz), [sin] "s"(sin), [vin2] "v"(vin2), [vin3] "v"(vin3) : "memory"); NRE;
        s3 = f32x2{0.f, 0.f}; d3 = s3; q3 = s3;
        part_(2, s3, d3, q3); part_(3, s3, d3, q3);
      } while (__any(s3[0] + s3[1] != s3[0] + s3[1]) && ++spins < RETRY_CAP);
    }
    asm volatile("; all in");
    s2 += s3; d2 += d3; q2 += q3;
  }
  float mrow, rdrow, drow;
  {
    const float sv = s2[0] + s2[1], dv = d2[0] + d2[1], qv = q2[0] + q2[1];
    float u = swap16_add(swap32_add(sv, dv), swap32_add(qv, qv));
    u = dpp_add<DPP_XOR1>(u); u = dpp_add<DPP_XOR2>(u); u = dpp_add<DPP_ROR8>(u);
    const auto r16 = __builtin_amdgcn_permlane16_swap(__float_as_uint(u), __float_as_uint(u), false, false);
    const auto r32 = __builtin_amdgcn_permlane32_swap(r16[0], r16[0], false, false);
    const float mean = __uint_as_float(r32[0]) * (1.f / DM);
    drow = __uint_as_float(r32[1]);
    float var = __uint_as_float(r16[1]) * (1.f / DM) - mean * mean;
    if (__builtin_expect(__any(!(mean * mean <= var)), 0)) {
      f32x2 qq = {0.f, 0.f};
      #pragma unroll
      for (int e = 0; e < 16; ++e) { const f32x2 t = x1[e] - mean; qq = __builtin_elementwise_fma(t, t, qq); }
      var = row_sum(qq[0] + qq[1]) * (1.f / DM);
    }
    mrow = rbh(mean); rdrow = __builtin_amdgcn_rcpf(rbh(__builtin_amdgcn_sqrtf(rbh(rbh(var) + 1e-5f))));
  }
  unsigned* kb = (unsigned*)(lds + 12288);
  unsigned mykey, k1w;
  { const unsigned bb = __float_as_uint(fmaf(-mrow, arow, drow) * rdrow); mykey = max((bb ^ ((unsigned)((int)bb >> 31) | 0x80000000u)) & 0xfffff800u, 0x800u) | nrow; }
  asm volatile("ds_write_b32 %1, %2\n\ts_waitcnt lgkmcnt(0)\n\ts_barrier\n\tds_read_b32 %0, %3\n\ts_waitcnt lgkmcnt(0)" : "=&v"(k1w) : "v"(vkb), "v"(mykey), "v"(vkr) : "memory");
  bool cand;
  u32x4 ka, kc;
#define KSC " sc1"
  {
    unsigned k1 = k1w, k2 = 0;
    top2_dpp<DPP_XOR1>(k1, k2); top2_dpp<DPP_XOR2>(k1, k2); top2_dpp<0x141>(k1, k2);
    asm volatile("s_cmp_lg_u32 %[wid], 0\n\ts_cbranch_scc1 Lka_%=\n\ts_mov_b64 exec, %[mk]\n\tglobal_atomic_or_x2 %[va], %[kk], %[pk]" KSC "\n\ts_mov_b64 exec, -1\nLka_%=:"
                 :: [wid] "s"(wid), [mk] "n"((1u << KEY_REP) - 1u), [va] "v"(vkey), [kk] "v"(u32x2{k2, k1}), [pk] "s"(pk) : "scc", "memory");
    cand = mykey >= k2;
    vpub += mykey == k1 ? 0u : 2u * BOX;
  }
  TQ(4);
  PAGE(2, "adaln_pg3");
#define TRA(r, a, o) asm volatile("ds_read_b64_tr_b16 a[%0:%0 + 1], %1 offset:%2" :: "n"(r), "v"(a), "n"(o) : "memory")
#define FRAGS(k) do { \
      TRA(128 + 8 * (k), ta, 2048 * ((k) >> 2) + 512 * ((k) & 1) + 16384 * (((k) >> 1) & 1)); TRA(130 + 8 * (k), ta, 2048 * ((k) >> 2) + 512 * ((k) & 1) + 16384 * (((k) >> 1) & 1) + 1024); \
      TRA(132 + 8 * (k), tu, 2048 * ((k) >> 2) + 512 * ((k) & 1) + 4096 * (((k) >> 1) & 1)); TRA(134 + 8 * (k), tu, 2048 * ((k) >> 2) + 512 * ((k) & 1) + 4096 * (((k) >> 1) & 1) + 1024); \
      TRA(192 + 4 * (k), tb, 64 * (k)); TRA(194 + 4 * (k), tb, 64 * (k) + 32); } while (0)
  f32x2 h2[16];
  {
    asm volatile("" : "+v"(mrow));
    const float nm = -mrow * rdrow;
    #pragma unroll
    for (int k = 0; k < 8; ++k) {
      #pragma unroll
      for (int e = 0; e < 2; ++e) { const int i = 2 * k + e; h2[i] = __builtin_elementwise_fma(__builtin_elementwise_fma(x1[i], f32x2{rdrow, rdrow}, f32x2{nm, nm}), c1f[i], sbf[i]); }
      if (k & 1) { asm volatile("" : "+v"(h2[2 * k]), "+v"(h2[2 * k + 1])); FRAGS(k >> 1); }
    }
  }
  const unsigned cw = __builtin_amdgcn_readfirstlane((unsigned)__ballot(cand) & 0x11u);
  auto publish = [&](const f32x2* v, unsigned off) __attribute__((always_inline)) {
    #pragma unroll
    for (int k = 0; k < 4; ++k) { const int q = 4 * (k >> 1) + (k & 1);
      asm volatile("buffer_store_dwordx4 %0, %1, %2, %3 offen sc1\n\ts_nop 1" :: "v"(u32x4{cvtpk2(v[2 * q]), cvtpk2(v[2 * q + 1]), cvtpk2(v[2 * q + 4]), cvtpk2(v[2 * q + 5])}), "v"(vpub), "s"(srdz), "s"(off + (k & 1) * BOX + (((k >> 1) * 2u) ^ yp2) * 128u) : "memory"); }
  };
  if (cand) publish(h2, 0u);
  char* st = lds + 32768 + 4096u * wid;
  auto look1 = [&]() __attribute__((always_inline)) {
    asm volatile("s_cmp_lg_u32 %2, 0\n\ts_cbranch_scc1 Lkl_%=\n\tbuffer_load_dwordx4 %0, %3, %4, 0 offen sc1\n\tbuffer_load_dwordx4 %1, %3, %4, 0 offen offset:16 sc1\nLkl_%=:" : "=&v"(ka), "=&v"(kc) : "s"(wid), "v"(kp), "s"(srdz) : "scc", "memory");
  };
  if (KLOOK == 0) look1();
  #pragma unroll
  for (int k = 0; k < 8; ++k) {
    u32x2 yq;
    #pragma unroll
    for (int e = 0; e < 2; ++e) yq[e] = cvtpk2(__builtin_elementwise_fma(gbf[2 * k + e], h2[2 * k + e], x1[2 * k + e]));
    *(u32x2*)(st + (row & 1u) * 2048u + (colb + 32u * ((k >> 2) * 2u ^ yp2)) * 2u + 64 * ((k >> 1) & 1) + 1024 * (k & 1)) = yq;
    if (k & 1) FRAGS(4 + (k >> 1));
    if (k == KLOOK - 1) look1();
  }
  auto out_rows = [&](unsigned which) __attribute__((always_inline)) {
    #pragma unroll
    for (int r = 0; r < 2; ++r) {
      if ((which >> (4 * r)) & 1u) {
        const u32x4 a = *(const u32x4*)(st + 2048 * r + 16u * lane), c = *(const u32x4*)(st + 2048 * r + 1024 + 16u * lane);
        asm volatile("s_nop 4\n\tglobal_store_dwordx4 %0, %1, %3 offset:%4 sc1\n\tglobal_store_dwordx4 %0, %2, %3 offset:%5 sc1\n\ts_nop 1" :: "v"(r0 * 2048u + 16u * lane), "v"(a), "v"(c), "s"(pout), "n"(2048 * r), "n"(2048 * r + 1024) : "memory");
      }
    }
  };
  const unsigned un = tid & 127u, ca = 256u * (un >> 5) + 64u * ((un >> 4) & 1u) + 128u * ((un >> 3) & 1u) + 16u * ((un >> 2) & 1u) + 4u * (un & 3u);
  unsigned vrow = (unsigned)WS_XIN + 16384u + (moe ? (wid >> 1) * BOX + (wid & 1u) * 512u + 16u * g : (un >> 6) * BOX + (un & 63u) * 16u), vsel = ldsb + 12288u + 40u + 4u * (moe ? lane & 1u : wid >> 1);
  unsigned mk[8];
  #pragma unroll
  for (int t = 0; t < 8; ++t) { mk[t] = (unsigned)__builtin_amdgcn_sbfe((int)(1u << (n >> 1)), t, 1); asm volatile("" : "+v"(mk[t])); }
  unsigned vred = ldsb + 24576u + wid * 256u + (lane & 1u) * 64u + 16u * g, vrf = ldsb + 24576u + ((lane & 1u) * 4u + g) * 16u + ((lane >> 1) & 3u) * 4u, vpt = (unsigned)WS_PART + b * 8192u + wid * 2048u + lane * 16u, vkk = ldsb + 12288u + 32u;
  unsigned long long m2 = 0x0003000300030003ull;
  asm volatile("" : "+v"(vrow), "+v"(vsel), "+v"(vred), "+v"(vrf), "+v"(vpt), "+v"(vkk), "+s"(m2));
  const unsigned later = __builtin_amdgcn_readfirstlane(wid && EARLYOUT ? cw : 0x11u);
  out_rows(~later);
  TQ(1);
  PAGE(3, "adaln_pg8");
  if (wid == 0) {
    asm volatile("s_waitcnt vmcnt(0)\nLk0_%=:" : "+v"(ka), "+v"(kc) :: "memory");
    for (unsigned spins = 0;; ++spins) {
      const u32x4 t = __builtin_elementwise_min(ka, kc);
      if (__all(min(min(t[0], t[1]), min(t[2], t[3])) != 0u) || spins > SPIN_CAP) break;
      __builtin_amdgcn_s_sleep(KSLEEP);
      asm volatile("buffer_load_dwordx4 %0, %2, %3, 0 offen sc1\n\tbuffer_load_dwordx4 %1, %2, %3, 0 offen offset:16 sc1\n\ts_waitcnt vmcnt(0)" : "=&v"(ka), "=&v"(kc) : "v"(kp), "s"(srdz) : "memory");
    }
    unsigned k1 = ka[1], k2 = ka[0];
    unsigned l1 = kc[1], l2 = kc[0];
    top2_merge(k1, k2, ka[3], ka[2]); top2_merge(l1, l2, kc[3], kc[2]); top2_merge(k1, k2, l1, l2);
    top2_dpp<DPP_XOR1>(k1, k2); top2_dpp<DPP_XOR2>(k1, k2); top2_dpp<0x141>(k1, k2); top2_dpp<0x140>(k1, k2);
    { auto r1 = __builtin_amdgcn_permlane16_swap(k1, k1, false, false); auto r2 = __builtin_amdgcn_permlane16_swap(k2, k2, false, false); k1 = r1[0]; k2 = r2[0]; top2_merge(k1, k2, r1[1], r2[1]); }
    { auto r1 = __builtin_amdgcn_permlane32_swap(k1, k1, false, false); auto r2 = __builtin_amdgcn_permlane32_swap(k2, k2, false, false); k1 = r1[0]; k2 = r2[0]; top2_merge(k1, k2, r1[1], r2[1]); }
    k1 = ~k1 & 2047u; k2 = ~k2 & 2047u;
    *(u32x4*)(kb + 8) = u32x4{k1, k2, (k1 >> 3) * (4u * BOX), (k2 >> 3) * (4u * BOX) + ((k1 ^ k2) < 8u ? 2u * BOX : 0u)};
  }
  u32x2 kk; unsigned vo;
  asm volatile("s_waitcnt lgkmcnt(0)\n\ts_barrier\n\tds_read_b32 %0, %3\n\tds_read_b64 %1, %4\n\ts_waitcnt lgkmcnt(0)\n\tv_add_u32 %2, %2, %0" : "=&v"(vo), "=&v"(kk), "+v"(vrow) : "v"(vsel), "v"(vkk) : "memory");
  TQ(5);
  float* part = (float*)(p.ws + WS_PART);
  unsigned k1, k2, rest;
  auto routed = [&]() __attribute__((always_inline)) {
    asm volatile("" : "+v"(kk));
    k1 = __builtin_amdgcn_readfirstlane(kk[0]); k2 = __builtin_amdgcn_readfirstlane(kk[1]);
    rest = __builtin_amdgcn_readfirstlane(later & ((r0 != k1 && r0 != k2 ? 1u : 0u) | (r0 + 1u != k1 && r0 + 1u != k2 ? 16u : 0u)));
    if (__builtin_expect(rest != later, 0)) { if (row == k1 || row == k2) publish(x1, 20480u); }
  };
  if (__builtin_expect(!moe, 0)) {
    asm volatile("adaln_pg8:\n\ts_cbranch_execz adaln_pg4");
    PAGE(4, "adaln_kr");
    u32x4 ha; u32x2 w0, w1;
    asm volatile("s_nop 4\n\tglobal_load_dwordx2 %0, %2, %3\n\tglobal_load_dwordx2 %1, %2, %3 offset:64" : "=&v"(w0), "=&v"(w1) : "v"(ca * 2u), "s"(p.wr) : "memory");
    asm volatile("buffer_load_dwordx4 %0, %3, %4, 0 offen sc1\n\ts_waitcnt vmcnt(0)" : "=&v"(ha), "+v"(w0), "+v"(w1) : "v"(vrow), "s"(srdz) : "memory");
    for (unsigned spins = 0; __builtin_expect(__any(max(max(ha[0], ha[1]), max(ha[2], ha[3])) == 0xffffffffu), 0) && spins < RETRY_CAP; ++spins)
      asm volatile("buffer_load_dwordx4 %0, %1, %2, 0 offen sc1\n\ts_waitcnt vmcnt(0)" : "=&v"(ha) : "v"(vrow), "s"(srdz) : "memory");
    routed();
    if (b == 255u && tid == 0u) __hip_atomic_store((gu64*)(p.ws + WS_EP), tag, RLX_AGENT);
    char* rows = lds + 16384 + (tid >> 7) * 2048u + ca * 2u;
    *(u32x2*)rows = u32x2{ha[0], ha[1]}; *(u32x2*)(rows + 64) = u32x2{ha[2], ha[3]};
    {
      float dot = 0.f;
      #pragma unroll
      for (int e = 0; e < 4; ++e) { const unsigned w = e < 2 ? w0[e] : w1[e - 2]; dot = fmaf(hi16(ha[e]), hi16(w), fmaf(lo16(ha[e]), lo16(w), dot)); }
      dot = wave_sum(dot);
      if (lane == 0) kb[16u + wid] = __float_as_uint(dot);
    }
    asm volatile("s_waitcnt lgkmcnt(0)\n\ts_barrier" ::: "memory");
    TQ(6);
    const unsigned pp = (b - MOE_WGS) * 64u + lane, sq = pp & 1u, nn = 128u * (pp >> 8) + 16u * (2u * ((pp >> 6) & 3u) + ((pp >> 1) & 1u)) + ((pp >> 2) & 15u);
    float gbv = bf2f(*(const u16*)(par + 14336 + nn * 2u)), hv = bf2f(*(const u16*)(lds + 16384 + sq * 2048u + nn * 2u));
    float rsel = rbf(__builtin_amdgcn_rcpf(1.f + __builtin_amdgcn_exp2f(rbf(__uint_as_float(kb[16u + 2u * sq]) + __uint_as_float(kb[17u + 2u * sq])) * -1.44269504089f)));
    u16* vout = pout + (sq ? k2 : k1) * DM + nn;
    unsigned vp = (unsigned)WS_PART + (tid >> 4) * 8192u + (b - MOE_WGS) * 256u + (tid & 15u) * 16u;
    unsigned vw = ldsb + 24576u + wid * 256u + (lane & 15u) * 16u + 4u * g, vr = ldsb + 24576u + 4u * lane;
    unsigned vx = (unsigned)WS_XIN + 36864u + kb[10u + sq] + (nn >> 9) * BOX + (32u * ((nn >> 8) & 1u) + 16u * ((nn >> 6) & 1u) + 8u * ((nn >> 7) & 1u) + 4u * ((nn >> 4) & 1u) + ((nn >> 2) & 3u)) * 16u + ((nn >> 5) & 1u) * 8u + (nn & 3u) * 2u;
    asm volatile("" : "+v"(gbv), "+v"(hv), "+v"(rsel), "+v"(vout), "+v"(vp), "+v"(vw), "+v"(vr), "+v"(vx));
    out_rows(rest);
    TQ(7);
    __builtin_amdgcn_s_sleep(FSLEEP);
    float u; unsigned xw;
    for (unsigned spins = 0;; ++spins) {
      f32x4 v[14];
      asm volatile("buffer_load_ushort %0, %1, %2, 0 offen sc1" : "=v"(xw) : "v"(vx), "s"(srdz) : "memory");
      #pragma unroll
      for (int k = 0; k < 14; ++k) asm volatile("buffer_load_dwordx4 %0, %1, %2, %3 offen sc1" : "=v"(v[k]) : "v"(vp), "s"(srdz), "s"(131072u * k) : "memory");
      asm volatile("s_waitcnt vmcnt(12)" : "+v"(v[0]), "+v"(v[1]), "+v"(xw) :: "memory");
      f32x4 sum = v[0] + v[1];
#define NEXT2(k) asm volatile("s_waitcnt vmcnt(%3)" : "+v"(v[k]), "+v"(v[k + 1]), "+v"(sum) : "n"(12 - k) : "memory"); sum += v[k]; sum += v[k + 1]
      NEXT2(2); NEXT2(4); NEXT2(6); NEXT2(8); NEXT2(10); NEXT2(12);
      u = swap16_add(swap32_add(sum[0], sum[2]), swap32_add(sum[1], sum[3]));
      if (!__any(u != u || xw == 0xffffu) || spins > RETRY_CAP) break;
    }
    TQ(1);
    f32x2 ra, rc;
    asm volatile("ds_write_b32 %0, %1\n\ts_waitcnt lgkmcnt(0)\n\ts_barrier" :: "v"(vw), "v"(u) : "memory");
    if (wid == 0) {
      asm volatile("ds_read2st64_b32 %0, %2 offset1:1\n\tds_read2st64_b32 %1, %2 offset0:2 offset1:3\n\ts_waitcnt lgkmcnt(0)" : "=&v"(ra), "=&v"(rc) : "v"(vr) : "memory");
      ra += rc;
      const unsigned ov = cvtpk(fmaf(gbv, fmaf(ra[0] + ra[1], rsel, hv), lo16(xw)), 0.f);
      asm volatile("global_store_short %0, %1, off sc1" :: "v"(vout), "v"(ov) : "memory");
    }
  } else {
    f32x4 dg, du;
    bf16x8 hb[8];
    auto fetch = [&]() __attribute__((always_inline)) {
      asm volatile("s_mov_b64 exec, %8\n\t"
                   "buffer_load_dwordx4 %0, %9, %10, 0 offen sc1\n\tbuffer_load_dwordx4 %1, %9, %10, 0 offen offset:64 sc1\n\tbuffer_load_dwordx4 %2, %9, %10, 0 offen offset:128 sc1\n\tbuffer_load_dwordx4 %3, %9, %10, 0 offen offset:192 sc1\n\t"
                   "buffer_load_dwordx4 %4, %9, %10, 0 offen offset:256 sc1\n\tbuffer_load_dwordx4 %5, %9, %10, 0 offen offset:320 sc1\n\tbuffer_load_dwordx4 %6, %9, %10, 0 offen offset:384 sc1\n\tbuffer_load_dwordx4 %7, %9, %10, 0 offen offset:448 sc1\n\t"
                   "s_mov_b64 exec, -1"
                   : "=&v"(hb[0]), "=&v"(hb[1]), "=&v"(hb[2]), "=&v"(hb[3]), "=&v"(hb[4]), "=&v"(hb[5]), "=&v"(hb[6]), "=&v"(hb[7]) : "s"(m2), "v"(vrow), "s"(srdz) : "memory");
    };
#define MM2(a, h, c0, c1) "v_mfma_f32_16x16x32_bf16 %0, a[" #a ":" #a "+3], %" #h ", " c0 "\n\tv_mfma_f32_16x16x32_bf16 %1, a[" #a "+4:" #a "+7], %" #h ", " c1 "\n\t"
#define MM16 MM2(128, 2, "0", "0") MM2(136, 3, "%0", "%1") MM2(144, 4, "%0", "%1") MM2(152, 5, "%0", "%1") MM2(160, 6, "%0", "%1") MM2(168, 7, "%0", "%1") MM2(176, 8, "%0", "%1") MM2(184, 9, "%0", "%1") "s_nop 7"
#define HB "+v"(hb[0]), "+v"(hb[1]), "+v"(hb[2]), "+v"(hb[3]), "+v"(hb[4]), "+v"(hb[5]), "+v"(hb[6]), "+v"(hb[7])
    fetch();
    routed();
    asm volatile("s_cmp_lg_u32 %10, %11\n\ts_waitcnt vmcnt(4)\n\ts_cbranch_scc1 Lh_%=\n\ts_waitcnt vmcnt(0)\nLh_%=:\n\t" MM16 : "=&v"(dg), "=&v"(du), HB : "s"(rest), "s"(later) : "scc", "memory");
    TQ(6);
    for (unsigned spins = 0; __builtin_expect((__ballot(dg[0] != dg[0]) & m2) != 0ull, 0) && spins < RETRY_CAP; ++spins) {
      fetch();
      asm volatile("s_waitcnt vmcnt(0)\n\t" MM16 : "=&v"(dg), "=&v"(du), HB :: "memory");
    }
    f32x2 ra, rc, rd, re;
    asm volatile("s_mov_b64 exec, %7\n\tds_write_b128 %4, %5\n\tds_write_b128 %4, %6 offset:128\n\ts_mov_b64 exec, -1\n\ts_waitcnt lgkmcnt(0)\n\ts_barrier\n\t"
                 "ds_read2_b32 %0, %8 offset1:32\n\tds_read2_b32 %1, %8 offset0:64 offset1:96\n\tds_read2_b32 %2, %8 offset0:128 offset1:160\n\tds_read2_b32 %3, %8 offset0:192 offset1:224\n\ts_waitcnt lgkmcnt(0)"
                 : "=&v"(ra), "=&v"(rc), "=&v"(rd), "=&v"(re) : "v"(vred), "v"(dg), "v"(du), "s"(m2), "v"(vrf) : "memory");
    u32x2 actp;
    {
      const f32x2 gu = rb2((ra + rc) + (rd + re));
      const float sg = __uint_as_float(cvtpk(0.f, gu[0] * __builtin_amdgcn_rcpf(1.f + __builtin_amdgcn_exp2f(gu[0] * -1.44269504089f))));
      const unsigned a = cvtpk(0.f, sg * gu[1]);
      const unsigned o2 = __builtin_amdgcn_update_dpp(0u, a, DPP_XOR2, 0xf, 0xf, true);
      const unsigned pr = (lane & 2u) ? ((o2 >> 16) | a) : ((a >> 16) | o2);
      const unsigned o4 = __builtin_amdgcn_update_dpp(0u, pr, DPP_ROR4, 0xf, 0xf, true);
      actp = (lane & 4u) ? u32x2{o4, pr} : u32x2{pr, o4};
    }
    f32x4 yv[2];
#define ABD(t) "v"(u32x4{actp[0] & mk[t], actp[1] & mk[t], actp[0] & mk[t + 1], actp[1] & mk[t + 1]})
#define MM3(a, w, c0, c1) "v_mfma_f32_16x16x32_bf16 %0, %" #a ", a[" #w ":" #w "+3], " c0 "\n\tv_mfma_f32_16x16x32_bf16 %1, %" #a ", a[" #w "+16:" #w "+19], " c1 "\n\t"
    asm volatile("s_nop 1\n\t" MM3(2, 192, "0", "0") MM3(3, 196, "%0", "%1") MM3(4, 200, "%0", "%1") MM3(5, 204, "%0", "%1") "s_nop 6\n\t"
                 "buffer_store_dwordx4 %0, %6, %7, 0 offen sc1\n\tbuffer_store_dwordx4 %1, %6, %7, 0 offen offset:1024 sc1\n\ts_nop 1"
                 : "=&v"(yv[0]), "=&v"(yv[1]) : ABD(0), ABD(2), ABD(4), ABD(6), "v"(vpt), "s"(srdz) : "memory");
    TQ(7);
    out_rows(rest);
  }
  asm volatile("adaln_text_end:");
}
}

extern "C" void kernel_launch(void* const* d_in, const int* in_sizes, int n_in, void* d_out, int out_size, void* d_ws, size_t ws_size, hipStream_t stream) {
  (void)in_sizes; (void)n_in; (void)out_size;
  if (ws_size < WS_END) return;
  const u16* x    = (const u16*)d_in[0];
  const u16* kc   = (const u16*)d_in[1];
  const u16* vc   = (const u16*)d_in[2];
  const u16* wq   = (const u16*)d_in[3];
  const u16* wo   = (const u16*)d_in[4];
  const u16* wr   = (const u16*)d_in[6];
  const u16* wg   = (const u16*)d_in[7];
  const u16* wu   = (const u16*)d_in[8];
  const u16* wd   = (const u16*)d_in[9];
  const u16* c    = (const u16*)d_in[10];
  const u16* wmod = (const u16*)d_in[11];
  u16* out = (u16*)d_out;
  char* ws = (char*)d_ws;
  fa::Args fr;
  fr.x = x; fr.wmod = wmod; fr.wq = wq; fr.kc = kc; fr.vc = vc; fr.wo = wo; fr.wg = wg; fr.wu = wu; fr.wd = wd; fr.ws = ws; fr.out = out; fr.c = c; fr.wr = wr;
  hipFuncSetAttribute((const void*)fa::fattn_k, hipFuncAttributeMaxDynamicSharedMemorySize, (int)fa::LDS_BYTES);
  hipLaunchKernelGGL(fa::fattn_k, dim3(256), dim3(256), fa::LDS_BYTES, stream, fr);
}
```

```cpp
#include <hip/hip_runtime.h>
#include <hip/hip_bf16.h>
#include <utility>
#include <cstdint>

#pragma clang fp contract(off)
typedef float    f32x16 __attribute__((ext_vector_type(16)));
typedef float    f32x2  __attribute__((ext_vector_type(2)));
typedef float    f32x4  __attribute__((ext_vector_type(4)));
typedef __bf16   bf16x8 __attribute__((ext_vector_type(8)));
typedef __bf16   bf16x4 __attribute__((ext_vector_type(4)));
typedef __bf16   bf16x2 __attribute__((ext_vector_type(2)));
typedef unsigned u32x4  __attribute__((ext_vector_type(4)));
typedef unsigned u32x2  __attribute__((ext_vector_type(2)));
typedef short    s16x4  __attribute__((ext_vector_type(4)));
typedef unsigned short u16;
template <int... Is> using iseq = std::integer_sequence<int, Is...>;
template <int N> using make_iseq = std::make_integer_sequence<int, N>;
#define DI __device__ __forceinline__
#define TAILB
#define PACE2
#define SELB

constexpr int SEQ = 2048, DM = 1024, NHEAD = 32, DHEAD = 128, DQ = NHEAD * DHEAD, DFF = 3584, NMOD = 6 * DM;
constexpr size_t WS_EP    = 0;
constexpr size_t WS_RST   = 32768;
constexpr size_t WS_CNT   = 65536;
constexpr size_t WS_MOD   = 131072;
constexpr size_t WS_Z     = 262144;
constexpr size_t WS_MODT  = WS_Z;
#define KEY_REP 8u
#define KSTR 128u
#define RSTR (16u * KSTR)
constexpr size_t WS_KEYS  = WS_Z + 16384;
constexpr unsigned Z_SHARE = 1280;
static_assert(KEY_REP <= 8 && KSTR <= 384 && Z_SHARE * 256 == 327680, "zeroed area");
constexpr size_t WS_PFL   = WS_Z + 65536;
constexpr size_t WS_OFL   = WS_Z + 196608;
constexpr size_t WS_H     = 1048576;
constexpr size_t WS_PART  = 6u << 20;
constexpr size_t WS_ROWS  = 8u << 20;
constexpr size_t WS_XIN   = 10u << 20;
constexpr unsigned BOX    = 65536;
constexpr size_t WS_END   = WS_XIN + (size_t)BOX * (SEQ / 4);
#define POL 0u

DI float bf2f(u16 b) { return __uint_as_float((unsigned)b << 16); }
DI float rbf(float x) { return (float)(__bf16)x; }
DI u16   f2bf(float x) { return __builtin_bit_cast(u16, (__bf16)x); }
DI float lo16(unsigned w) { return __uint_as_float(w << 16); }
DI float hi16(unsigned w) { return __uint_as_float(w & 0xffff0000u); }
DI unsigned cvtpk(float lo, float hi) {
  f32x2 v = {lo, hi}; bf16x2 b = __builtin_convertvector(v, bf16x2); return __builtin_bit_cast(unsigned, b);
}
template <int CTRL> DI float dpp_add(float v) { return v + __uint_as_float(__builtin_amdgcn_update_dpp(0u, __float_as_uint(v), CTRL, 0xf, 0xf, true)); }
DI float swap32_add(float a, float b) {
  auto r = __builtin_amdgcn_permlane32_swap(__float_as_uint(a), __float_as_uint(b), false, false); return __uint_as_float(r[0]) + __uint_as_float(r[1]);
}
DI float swap16_add(float a, float b) {
  auto r = __builtin_amdgcn_permlane16_swap(__float_as_uint(a), __float_as_uint(b), false, false); return __uint_as_float(r[0]) + __uint_as_float(r[1]);
}
constexpr int DPP_XOR1 = 0xB1, DPP_XOR2 = 0x4E, DPP_ROR4 = 0x124, DPP_ROR8 = 0x128;
DI float wave_sum(float v) {
  v = dpp_add<DPP_XOR1>(v); v = dpp_add<DPP_XOR2>(v); v = dpp_add<DPP_ROR4>(v); v = dpp_add<DPP_ROR8>(v);
  v = swap16_add(v, v); return swap32_add(v, v);
}

DI void st16_wt(void* p, u32x4 v) { asm volatile("global_store_dwordx4 %0, %1, off sc1\n\ts_nop 1" :: "v"(p), "v"(v) : "memory"); }

DI f32x2 unpk(unsigned w) { return f32x2{lo16(w), hi16(w)}; }
DI unsigned cvtpk2(f32x2 v) { return __builtin_bit_cast(unsigned, __builtin_convertvector(v, bf16x2)); }
DI float rbh(float v) { return __uint_as_float(cvtpk(0.f, v)); }
DI f32x2 rb2(f32x2 v) { return f32x2{__uint_as_float(cvtpk(0.f, v[0])), __uint_as_float(cvtpk(0.f, v[1]))}; }
DI unsigned ln_pair(float x0, float x1, float m, float rden, float c0, float c1, float s0, float s1) {
  const unsigned d = cvtpk2(f32x2{x0, x1} - m), l = cvtpk2(unpk(d) * rden), t = cvtpk2(unpk(l) * f32x2{c0, c1});
  return cvtpk2(unpk(t) + f32x2{s0, s1});
}
#define GAS __attribute__((address_space(1)))
#define LAS __attribute__((address_space(3)))
typedef GAS unsigned long long gu64;
typedef short v4i16_t __attribute__((ext_vector_type(4)));
DI bf16x4 lds_tr(unsigned a) { return __builtin_bit_cast(bf16x4, __builtin_amdgcn_ds_read_tr16_b64_v4i16((LAS v4i16_t*)a)); }
#define RLX_AGENT __ATOMIC_RELAXED, __HIP_MEMORY_SCOPE_AGENT
constexpr unsigned SPIN_CAP = 1u << 16;

static_assert(BOX == 65536, "box size");
#define NP_FILL 8
#define GOT_WAIT "8"
#define FA_CLOBBERS "v0", "v1", "v2", "v3", "v8", "v9", "v10", "v11", "v16", "v17", "v18", "v19", "v20", "v21", "v22", "v23", "v24", "v25", "v26", "v27", "v28", "v29", "v30", "v31", "v32", "v33", "v34", "v35", "v40", "v41", "v42", "v43", "v48", "v49", "v50", "v51", "v52", "v53", "v54", "v55", "v56", "v57", "v58", "v59", "v60", "v61", "v62", "v63", "v64", "v65", "v66", "v67", "v72", "v73", "v74", "v75", "v80", "v81", "v82", "v83", "v84", "v85", "v86", "v87", "v88", "v89", "v90", "v91", "v92", "v93", "v94", "v95", "v96", "v97", "v98", "v99", "v104", "v105", "v106", "v107", "v112", "v113", "v114", "v115", "v116", "v117", "v118", "v119", "v120", "v121", "v122", "v123", "v124", "v125", "v126", "v127", "v200", "v201", "v202", "v203", "v205", "v207", "v208", "v209", "v210", "v211", "v212", "v213", "v214", "v215", "v216", "v217", "v218", "v219", "v220", "v221", "v222", "v223", "v224", "v230", "v231", "v232", "v233", "v234", "v235", "v237", "a0", "a1", "a2", "a3", "a4", "a5", "a6", "a7", "a8", "a9", "a10", "a11", "a12", "a13", "a14", "a15", "a16", "a17", "a18", "a19", "a20", "a21", "a22", "a23", "a24", "a25", "a26", "a27", "a28", "a29", "a30", "a31", "a32", "a33", "a34", "a35", "a36", "a37", "a38", "a39", "a40", "a41", "a42", "a43", "a44", "a45", "a46", "a47", "a48", "a49", "a50", "a51", "a52", "a53", "a54", "a55", "a56", "a57", "a58", "a59", "a60", "a61", "a62", "a63", "a64", "a65", "a66", "a67", "a68", "a69", "a70", "a71", "a72", "a73", "a74", "a75", "a76", "a77", "a78", "a79", "a80", "a81", "a82", "a83", "a84", "a85", "a86", "a87", "a88", "a89", "a90", "a91", "a92", "a93", "a94", "a95", "a96", "a97", "a98", "a99", "a100", "a101", "a102", "a103", "a104", "a105", "a106", "a107", "a108", "a109", "a110", "a111", "a112", "a113", "a114", "a115", "a116", "a117", "a118", "a119", "a120", "a121", "a122", "a123", "a124", "a125", "a126", "a127", "a128", "a129", "a130", "a131", "a132", "a133", "a134", "a135", "a136", "a137", "a138", "a139", "a140", "a141", "a142", "a143", "a144", "a145", "a146", "a147", "a148", "a149", "a150", "a151", "a152", "a153", "a154", "a155", "a156", "a157", "a158", "a159", "a160", "a161", "a162", "a163", "a164", "a165", "a166", "a167", "a168", "a169", "a170", "a171", "a172", "a173", "a174", "a175", "a176", "a177", "a178", "a179", "a180", "a181", "a182", "a183", "a184", "a185", "a186", "a187", "a188", "a189", "a190", "a191", "a192", "a193", "a194", "a195", "a196", "a197", "a198", "a199", "a200", "a201", "a202", "a203", "a204", "a205", "a206", "a207", "a208", "a209", "a210", "a211", "a212", "a213", "a214", "a215", "a216", "a217", "a218", "a219", "a220", "a221", "a222", "a223", "a224", "a225", "a226", "a227", "a228", "a229", "a230", "a231", "a232", "a233", "a234", "a235", "a236", "a237", "a238", "a239", "a240", "a241", "a242", "a243", "a244", "a245", "a246", "a247", "a248", "a249", "a250", "a251", "a252", "a253", "a254", "a255", "s40", "s41", "s42", "s43", "s44", "s45", "s46", "s47", "s48", "s49", "s50", "s51", "s52", "s53", "s54", "s55", "s56", "s57", "s58", "s59", "s60", "s61", "s62", "s63", "s64", "s65", "s66", "s67", "s68", "s69", "vcc", "m0", "scc", "memory"
#define EARLY2 0
#define SHW 1
#define QROT 0
#define SKV 0
#define KVPAR 0
#define QT2 0
#define QSTAG 0
#define TLATE 0
#define NTV 5
#define BPERM 0
#define NEAR 3
#define FA_OUTS [got] "=&s"(got), "=&{v[128:129]}"(xf[0]), "=&{v[130:131]}"(xf[1]), "=&{v[132:133]}"(xf[2]), "=&{v[134:135]}"(xf[3]), "=&{v[136:137]}"(xf[4]), "=&{v[138:139]}"(xf[5]), "=&{v[140:141]}"(xf[6]), "=&{v[142:143]}"(xf[7]), "=&{v[144:145]}"(xf[8]), "=&{v[146:147]}"(xf[9]), "=&{v[148:149]}"(xf[10]), "=&{v[150:151]}"(xf[11]), "=&{v[152:153]}"(xf[12]), "=&{v[154:155]}"(xf[13]), "=&{v[156:157]}"(xf[14]), "=&{v[158:159]}"(xf[15]), "=&{v[160:163]}"(gaf[0]), "=&{v[164:167]}"(gaf[1]), "=&{v[168:171]}"(gaf[2]), "=&{v[172:175]}"(gaf[3]), "=&{v[176:179]}"(gaf[4]), "=&{v[180:183]}"(gaf[5]), "=&{v[184:187]}"(gaf[6]), "=&{v[188:191]}"(gaf[7]), "=&{v[4:7]}"(aw[0]), "=&{v[12:15]}"(aw[1]), "=&{v[36:39]}"(aw[2]), "=&{v[44:47]}"(aw[3]), "=&{v[68:71]}"(aw[4]), "=&{v[76:79]}"(aw[5]), "=&{v[100:103]}"(aw[6]), "=&{v[108:111]}"(aw[7]), "=&{v[192:195]}"(alo), "=&{v[196:199]}"(ahi), "=&{v204}"(late), "=&{v206}"(arow), "+{v229}"(fl), "+{v225}"(vwb), "+{v226}"(wd0), "+{v227}"(hq0), "+{v228}"(sd0), "+{v236}"(tq0)
#define FA_BODY \
"s_branch adaln_body\n" \
".p2align 12\n" \
"adaln_pg7:\n" \
"adaln_body:\n" \
"s_cbranch_execz adaln_fr\n" \
"s_nop 0\n" \
"s_mov_b32 s55, 0x80000\n" \
"v_add_u32_e32 v219, 0x8000, v227\n" \
"v_add_u32_e32 v220, 0x10000, v227\n" \
"v_add_u32_e32 v221, 0x18000, v227\n" \
"s_mov_b32 s50, 0\n" \
"s_movk_i32 s53, 0x1000\n" \
"s_mov_b32 s52, 0\n" \
"s_nop 0\n" \
"s_mov_b32 s49, 1572864\n" \
"v_add_u32_e32 v216, 0xfffffc80, v226\n" \
"v_add_u32_e32 v217, 0xf800, v226\n" \
"v_add_u32_e32 v218, 0xf480, v226\n" \
"s_add_u32 s40, %[m0b], 0\n" \
"s_nop 0\n" \
"s_add_u32 s44, %[m0b], 65536\n" \
"s_add_u32 s41, %[m0b], 16384\n" \
"s_add_u32 s45, %[m0b], 81920\n" \
"s_add_u32 s42, %[m0b], 32768\n" \
"s_add_u32 s46, %[m0b], 98304\n" \
"s_add_u32 s43, %[m0b], 49152\n" \
"s_add_u32 s47, %[m0b], 114688\n" \
"v_xor_b32_e64 v208, 64, %[kb0]\n" \
"v_xor_b32_e32 v209, 0x80, %[kb0]\n" \
"v_xor_b32_e32 v210, 0xc0, %[kb0]\n" \
"v_xor_b32_e64 v212, 64, %[kd0]\n" \
"v_xor_b32_e32 v213, 0x80, %[kd0]\n" \
"v_xor_b32_e32 v214, 0xc0, %[kd0]\n" \
"v_xor_b32_e64 v211, 32, %[vvb]\n" \
"v_add_u32_e32 v215, 0xfffffc80, %[vd0]\n" \
"v_readlane_b32 s64, %[tlo], 17\n" \
"v_readlane_b32 s65, %[thi], 17\n" \
"s_mov_b32 s66, 12582912\n" \
"s_mov_b32 s67, 0x20000\n" \
"v_mov_b64_e32 v[0:1], 0\n" \
"v_mov_b64_e32 v[2:3], 0\n" \
"v_mov_b64_e32 v[4:5], 0\n" \
"v_mov_b64_e32 v[6:7], 0\n" \
"v_mov_b64_e32 v[8:9], 0\n" \
"v_mov_b64_e32 v[10:11], 0\n" \
"v_mov_b64_e32 v[12:13], 0\n" \
"v_mov_b64_e32 v[14:15], 0\n" \
"v_mov_b64_e32 v[16:17], 0\n" \
"v_mov_b64_e32 v[18:19], 0\n" \
"v_mov_b64_e32 v[20:21], 0\n" \
"v_mov_b64_e32 v[22:23], 0\n" \
"v_mov_b64_e32 v[24:25], 0\n" \
"v_mov_b64_e32 v[26:27], 0\n" \
"v_mov_b64_e32 v[28:29], 0\n" \
"v_mov_b64_e32 v[30:31], 0\n" \
"v_mov_b64_e32 v[32:33], 0\n" \
"v_mov_b64_e32 v[34:35], 0\n" \
"v_mov_b64_e32 v[36:37], 0\n" \
"v_mov_b64_e32 v[38:39], 0\n" \
"v_mov_b64_e32 v[40:41], 0\n" \
"v_mov_b64_e32 v[42:43], 0\n" \
"v_mov_b64_e32 v[44:45], 0\n" \
"v_mov_b64_e32 v[46:47], 0\n" \
"v_mov_b64_e32 v[48:49], 0\n" \
"v_mov_b64_e32 v[50:51], 0\n" \
"v_mov_b64_e32 v[52:53], 0\n" \
"v_mov_b64_e32 v[54:55], 0\n" \
"v_mov_b64_e32 v[56:57], 0\n" \
"v_mov_b64_e32 v[58:59], 0\n" \
"v_mov_b64_e32 v[60:61], 0\n" \
"v_mov_b64_e32 v[62:63], 0\n" \
"v_mov_b64_e32 v[64:65], 0\n" \
"v_mov_b64_e32 v[66:67], 0\n" \
"v_mov_b64_e32 v[68:69], 0\n" \
"v_mov_b64_e32 v[70:71], 0\n" \
"v_mov_b64_e32 v[72:73], 0\n" \
"v_mov_b64_e32 v[74:75], 0\n" \
"v_mov_b64_e32 v[76:77], 0\n" \
"v_mov_b64_e32 v[78:79], 0\n" \
"v_mov_b64_e32 v[80:81], 0\n" \
"v_mov_b64_e32 v[82:83], 0\n" \
"v_mov_b64_e32 v[84:85], 0\n" \
"v_mov_b64_e32 v[86:87], 0\n" \
"v_mov_b64_e32 v[88:89], 0\n" \
"v_mov_b64_e32 v[90:91], 0\n" \
"v_mov_b64_e32 v[92:93], 0\n" \
"v_mov_b64_e32 v[94:95], 0\n" \
"v_mov_b64_e32 v[96:97], 0\n" \
"v_mov_b64_e32 v[98:99], 0\n" \
"v_mov_b64_e32 v[100:101], 0\n" \
"v_mov_b64_e32 v[102:103], 0\n" \
"v_mov_b64_e32 v[104:105], 0\n" \
"v_mov_b64_e32 v[106:107], 0\n" \
"v_mov_b64_e32 v[108:109], 0\n" \
"v_mov_b64_e32 v[110:111], 0\n" \
"v_mov_b64_e32 v[112:113], 0\n" \
"v_mov_b64_e32 v[114:115], 0\n" \
"v_mov_b64_e32 v[116:117], 0\n" \
"v_mov_b64_e32 v[118:119], 0\n" \
"v_mov_b64_e32 v[120:121], 0\n" \
"v_mov_b64_e32 v[122:123], 0\n" \
"v_mov_b64_e32 v[124:125], 0\n" \
"v_mov_b64_e32 v[126:127], 0\n" \
"s_mov_b32 s51, 0x10000\n" \
"v_mbcnt_lo_u32_b32 v232, -1, 0\n" \
"v_mov_b64_e32 v[234:235], %[tag]\n" \
"v_lshlrev_b32_e32 v233, 3, v232\n" \
"v_lshlrev_b32_e32 v232, 8, v232\n" \
"s_waitcnt vmcnt(0)\n" \
"s_barrier\n" \
"s_cmp_lg_u32 %[wid], 0\n" \
"s_cbranch_scc1 Lwalk_%=\n" \
"s_mov_b32 exec_hi, 0\n" \
"global_store_dwordx2 v232, v[234:235], %[psig] sc1\n" \
"s_mov_b32 exec_hi, -1\n" \
"s_nop 0\n" \
"v_readlane_b32 s62, %[tlo], 13\n" \
"s_nop 3\n" \
"s_cmpk_eq_u32 s62, 252\n" \
"s_cbranch_scc1 Lprobe_%=\n" \
"Lgo_%=:\n" \
"s_nop 0\n" \
"ds_read_b64_tr_b16 v[128:129], v225 offset:0\n" \
"ds_read_b64_tr_b16 v[130:131], v225 offset:256\n" \
"ds_read_b64_tr_b16 v[132:133], v225 offset:8192\n" \
"ds_read_b64_tr_b16 v[134:135], v225 offset:8448\n" \
"ds_read_b64_tr_b16 v[136:137], v225 offset:8\n" \
"ds_read_b64_tr_b16 v[138:139], v225 offset:264\n" \
"ds_read_b64_tr_b16 v[140:141], v225 offset:8200\n" \
"ds_read_b64_tr_b16 v[142:143], v225 offset:8456\n" \
"ds_read_b64_tr_b16 v[144:145], v225 offset:512\n" \
"ds_read_b64_tr_b16 v[146:147], v225 offset:768\n" \
"ds_read_b64_tr_b16 v[148:149], v225 offset:8704\n" \
"ds_read_b64_tr_b16 v[150:151], v225 offset:8960\n" \
"ds_read_b64_tr_b16 v[152:153], v225 offset:520\n" \
"ds_read_b64_tr_b16 v[154:155], v225 offset:776\n" \
"ds_read_b64_tr_b16 v[156:157], v225 offset:8712\n" \
"ds_read_b64_tr_b16 v[158:159], v225 offset:8968\n" \
"s_mov_b32 s60, 4194304\n" \
"v_and_b32_e32 v233, 56, v233\n" \
"s_lshl_b32 s61, %[wid], 6\n" \
"v_add_u32_e64 v233, s61, v233\n" \
"Lpoll_%=:\n" \
"global_load_dwordx2 v[230:231], v233, %[pcnt] sc1\n" \
"s_waitcnt vmcnt(0)\n" \
"v_cmp_ne_u64_e32 vcc, v[230:231], v[234:235]\n" \
"s_sub_u32 s51, s51, 1\n" \
"s_cbranch_scc1 Lpd_%=\n" \
"s_nop 3\n" \
"s_cbranch_vccnz Lpoll_%=\n" \
"Lpd_%=:\n" \
"buffer_load_dwordx4 a[128:131], v227, %[srdz], s50 offen offset:0 sc1\n" \
"buffer_load_dwordx4 a[132:135], v219, %[srdz], s50 offen offset:0 sc1\n" \
"buffer_load_dwordx4 a[136:139], v220, %[srdz], s50 offen offset:0 sc1\n" \
"buffer_load_dwordx4 a[140:143], v221, %[srdz], s50 offen offset:0 sc1\n" \
"buffer_load_dwordx4 a[144:147], v227, %[srdz], s50 offen offset:1024 sc1\n" \
"buffer_load_dwordx4 a[148:151], v219, %[srdz], s50 offen offset:1024 sc1\n" \
"buffer_load_dwordx4 a[152:155], v220, %[srdz], s50 offen offset:1024 sc1\n" \
"buffer_load_dwordx4 a[156:159], v221, %[srdz], s50 offen offset:1024 sc1\n" \
"buffer_load_dwordx4 a[160:163], v227, %[srdz], s50 offen offset:2048 sc1\n" \
"buffer_load_dwordx4 a[164:167], v219, %[srdz], s50 offen offset:2048 sc1\n" \
"buffer_load_dwordx4 a[168:171], v220, %[srdz], s50 offen offset:2048 sc1\n" \
"buffer_load_dwordx4 a[172:175], v221, %[srdz], s50 offen offset:2048 sc1\n" \
"buffer_load_dwordx4 a[176:179], v227, %[srdz], s50 offen offset:3072 sc1\n" \
"buffer_load_dwordx4 a[180:183], v219, %[srdz], s50 offen offset:3072 sc1\n" \
"buffer_load_dwordx4 a[184:187], v220, %[srdz], s50 offen offset:3072 sc1\n" \
"buffer_load_dwordx4 a[188:191], v221, %[srdz], s50 offen offset:3072 sc1\n" \
"s_add_u32 s50, s50, s53\n" \
"s_nop 0\n" \
"buffer_load_dwordx4 a[192:195], v227, %[srdz], s50 offen offset:0 sc1\n" \
"buffer_load_dwordx4 a[196:199], v219, %[srdz], s50 offen offset:0 sc1\n" \
"buffer_load_dwordx4 a[200:203], v220, %[srdz], s50 offen offset:0 sc1\n" \
"buffer_load_dwordx4 a[204:207], v221, %[srdz], s50 offen offset:0 sc1\n" \
"buffer_load_dwordx4 a[208:211], v227, %[srdz], s50 offen offset:1024 sc1\n" \
"buffer_load_dwordx4 a[212:215], v219, %[srdz], s50 offen offset:1024 sc1\n" \
"buffer_load_dwordx4 a[216:219], v220, %[srdz], s50 offen offset:1024 sc1\n" \
"buffer_load_dwordx4 a[220:223], v221, %[srdz], s50 offen offset:1024 sc1\n" \
"s_mov_b32 s51, 3\n" \
"s_waitcnt vmcnt(16) lgkmcnt(0)\n" \
"Lq_%=:\n" \
"v_mfma_f32_16x16x32_bf16 v[0:3], v[128:131], a[128:131], v[0:3]\n" \
"ds_read_b64_tr_b16 v[160:161], v225 offset:1024\n" \
"v_mfma_f32_16x16x32_bf16 v[0:3], v[132:135], a[144:147], v[0:3]\n" \
"ds_read_b64_tr_b16 v[162:163], v225 offset:1280\n" \
"v_mfma_f32_16x16x32_bf16 v[32:35], v[128:131], a[132:135], v[32:35]\n" \
"s_mov_b32 m0, s47\n" \
"s_nop 0\n" \
"buffer_load_dwordx4 v217, %[srdw], s49 offen offset:2048 lds\n" \
"v_mfma_f32_16x16x32_bf16 v[32:35], v[132:135], a[148:151], v[32:35]\n" \
"ds_read_b64_tr_b16 v[164:165], v225 offset:9216\n" \
"v_mfma_f32_16x16x32_bf16 v[64:67], v[128:131], a[136:139], v[64:67]\n" \
"ds_read_b64_tr_b16 v[166:167], v225 offset:9472\n" \
"v_mfma_f32_16x16x32_bf16 v[64:67], v[132:135], a[152:155], v[64:67]\n" \
"v_mfma_f32_16x16x32_bf16 v[96:99], v[128:131], a[140:143], v[96:99]\n" \
"ds_read_b64_tr_b16 v[168:169], v225 offset:1032\n" \
"v_mfma_f32_16x16x32_bf16 v[96:99], v[132:135], a[156:159], v[96:99]\n" \
"ds_read_b64_tr_b16 v[170:171], v225 offset:1288\n" \
"v_mfma_f32_16x16x32_bf16 v[4:7], v[136:139], a[128:131], v[4:7]\n" \
"buffer_load_dwordx4 v218, %[srdw], s49 offen offset:3072 lds\n" \
"v_mfma_f32_16x16x32_bf16 v[4:7], v[140:143], a[144:147], v[4:7]\n" \
"ds_read_b64_tr_b16 v[172:173], v225 offset:9224\n" \
"v_mfma_f32_16x16x32_bf16 v[36:39], v[136:139], a[132:135], v[36:39]\n" \
"ds_read_b64_tr_b16 v[174:175], v225 offset:9480\n" \
"v_mfma_f32_16x16x32_bf16 v[36:39], v[140:143], a[148:151], v[36:39]\n" \
"v_mfma_f32_16x16x32_bf16 v[68:71], v[136:139], a[136:139], v[68:71]\n" \
"ds_read_b64_tr_b16 v[176:177], v225 offset:1536\n" \
"v_mfma_f32_16x16x32_bf16 v[68:71], v[140:143], a[152:155], v[68:71]\n" \
"ds_read_b64_tr_b16 v[178:179], v225 offset:1792\n" \
"v_mfma_f32_16x16x32_bf16 v[100:103], v[136:139], a[140:143], v[100:103]\n" \
"buffer_load_dwordx4 a[224:227], v227, %[srdz], s50 offen offset:2048 sc1\n" \
"v_mfma_f32_16x16x32_bf16 v[100:103], v[140:143], a[156:159], v[100:103]\n" \
"ds_read_b64_tr_b16 v[180:181], v225 offset:9728\n" \
"v_mfma_f32_16x16x32_bf16 v[8:11], v[144:147], a[128:131], v[8:11]\n" \
"ds_read_b64_tr_b16 v[182:183], v225 offset:9984\n" \
"v_mfma_f32_16x16x32_bf16 v[8:11], v[148:151], a[144:147], v[8:11]\n" \
"v_mfma_f32_16x16x32_bf16 v[40:43], v[144:147], a[132:135], v[40:43]\n" \
"ds_read_b64_tr_b16 v[184:185], v225 offset:1544\n" \
"v_mfma_f32_16x16x32_bf16 v[40:43], v[148:151], a[148:151], v[40:43]\n" \
"ds_read_b64_tr_b16 v[186:187], v225 offset:1800\n" \
"v_mfma_f32_16x16x32_bf16 v[72:75], v[144:147], a[136:139], v[72:75]\n" \
"buffer_load_dwordx4 a[228:231], v219, %[srdz], s50 offen offset:2048 sc1\n" \
"v_mfma_f32_16x16x32_bf16 v[72:75], v[148:151], a[152:155], v[72:75]\n" \
"ds_read_b64_tr_b16 v[188:189], v225 offset:9736\n" \
"v_mfma_f32_16x16x32_bf16 v[104:107], v[144:147], a[140:143], v[104:107]\n" \
"ds_read_b64_tr_b16 v[190:191], v225 offset:9992\n" \
"v_mfma_f32_16x16x32_bf16 v[104:107], v[148:151], a[156:159], v[104:107]\n" \
"v_mfma_f32_16x16x32_bf16 v[12:15], v[152:155], a[128:131], v[12:15]\n" \
"buffer_load_dwordx4 a[232:235], v220, %[srdz], s50 offen offset:2048 sc1\n" \
"v_mfma_f32_16x16x32_bf16 v[12:15], v[156:159], a[144:147], v[12:15]\n" \
"v_mfma_f32_16x16x32_bf16 v[44:47], v[152:155], a[132:135], v[44:47]\n" \
"v_mfma_f32_16x16x32_bf16 v[44:47], v[156:159], a[148:151], v[44:47]\n" \
"v_mfma_f32_16x16x32_bf16 v[76:79], v[152:155], a[136:139], v[76:79]\n" \
"buffer_load_dwordx4 a[236:239], v221, %[srdz], s50 offen offset:2048 sc1\n" \
"v_mfma_f32_16x16x32_bf16 v[76:79], v[156:159], a[152:155], v[76:79]\n" \
"v_mfma_f32_16x16x32_bf16 v[108:111], v[152:155], a[140:143], v[108:111]\n" \
"v_mfma_f32_16x16x32_bf16 v[108:111], v[156:159], a[156:159], v[108:111]\n" \
"s_waitcnt vmcnt(29) lgkmcnt(0)\n" \
"s_barrier\n" \
"v_mfma_f32_16x16x32_bf16 v[16:19], v[160:163], a[128:131], v[16:19]\n" \
"ds_read_b64_tr_b16 v[128:129], v225 offset:16384\n" \
"v_mfma_f32_16x16x32_bf16 v[16:19], v[164:167], a[144:147], v[16:19]\n" \
"ds_read_b64_tr_b16 v[130:131], v225 offset:16640\n" \
"v_mfma_f32_16x16x32_bf16 v[48:51], v[160:163], a[132:135], v[48:51]\n" \
"s_add_u32 s49, s49, s55\n" \
"s_mov_b32 m0, s44\n" \
"s_nop 0\n" \
"buffer_load_dwordx4 v226, %[srdw], s49 offen offset:0 lds\n" \
"v_mfma_f32_16x16x32_bf16 v[48:51], v[164:167], a[148:151], v[48:51]\n" \
"ds_read_b64_tr_b16 v[132:133], v225 offset:24576\n" \
"v_mfma_f32_16x16x32_bf16 v[80:83], v[160:163], a[136:139], v[80:83]\n" \
"ds_read_b64_tr_b16 v[134:135], v225 offset:24832\n" \
"v_mfma_f32_16x16x32_bf16 v[80:83], v[164:167], a[152:155], v[80:83]\n" \
"v_mfma_f32_16x16x32_bf16 v[112:115], v[160:163], a[140:143], v[112:115]\n" \
"ds_read_b64_tr_b16 v[136:137], v225 offset:16392\n" \
"v_mfma_f32_16x16x32_bf16 v[112:115], v[164:167], a[156:159], v[112:115]\n" \
"ds_read_b64_tr_b16 v[138:139], v225 offset:16648\n" \
"v_mfma_f32_16x16x32_bf16 v[20:23], v[168:171], a[128:131], v[20:23]\n" \
"buffer_load_dwordx4 v216, %[srdw], s49 offen offset:1024 lds\n" \
"v_mfma_f32_16x16x32_bf16 v[20:23], v[172:175], a[144:147], v[20:23]\n" \
"ds_read_b64_tr_b16 v[140:141], v225 offset:24584\n" \
"v_mfma_f32_16x16x32_bf16 v[52:55], v[168:171], a[132:135], v[52:55]\n" \
"ds_read_b64_tr_b16 v[142:143], v225 offset:24840\n" \
"v_mfma_f32_16x16x32_bf16 v[52:55], v[172:175], a[148:151], v[52:55]\n" \
"v_mfma_f32_16x16x32_bf16 v[84:87], v[168:171], a[136:139], v[84:87]\n" \
"ds_read_b64_tr_b16 v[144:145], v225 offset:16896\n" \
"v_mfma_f32_16x16x32_bf16 v[84:87], v[172:175], a[152:155], v[84:87]\n" \
"ds_read_b64_tr_b16 v[146:147], v225 offset:17152\n" \
"v_mfma_f32_16x16x32_bf16 v[116:119], v[168:171], a[140:143], v[116:119]\n" \
"buffer_load_dwordx4 a[240:243], v227, %[srdz], s50 offen offset:3072 sc1\n" \
"v_mfma_f32_16x16x32_bf16 v[116:119], v[172:175], a[156:159], v[116:119]\n" \
"ds_read_b64_tr_b16 v[148:149], v225 offset:25088\n" \
"v_mfma_f32_16x16x32_bf16 v[24:27], v[176:179], a[128:131], v[24:27]\n" \
"ds_read_b64_tr_b16 v[150:151], v225 offset:25344\n" \
"v_mfma_f32_16x16x32_bf16 v[24:27], v[180:183], a[144:147], v[24:27]\n" \
"v_mfma_f32_16x16x32_bf16 v[56:59], v[176:179], a[132:135], v[56:59]\n" \
"ds_read_b64_tr_b16 v[152:153], v225 offset:16904\n" \
"v_mfma_f32_16x16x32_bf16 v[56:59], v[180:183], a[148:151], v[56:59]\n" \
"ds_read_b64_tr_b16 v[154:155], v225 offset:17160\n" \
"v_mfma_f32_16x16x32_bf16 v[88:91], v[176:179], a[136:139], v[88:91]\n" \
"buffer_load_dwordx4 a[244:247], v219, %[srdz], s50 offen offset:3072 sc1\n" \
"v_mfma_f32_16x16x32_bf16 v[88:91], v[180:183], a[152:155], v[88:91]\n" \
"ds_read_b64_tr_b16 v[156:157], v225 offset:25096\n" \
"v_mfma_f32_16x16x32_bf16 v[120:123], v[176:179], a[140:143], v[120:123]\n" \
"ds_read_b64_tr_b16 v[158:159], v225 offset:25352\n" \
"v_mfma_f32_16x16x32_bf16 v[120:123], v[180:183], a[156:159], v[120:123]\n" \
"v_mfma_f32_16x16x32_bf16 v[28:31], v[184:187], a[128:131], v[28:31]\n" \
"buffer_load_dwordx4 a[248:251], v220, %[srdz], s50 offen offset:3072 sc1\n" \
"v_mfma_f32_16x16x32_bf16 v[28:31], v[188:191], a[144:147], v[28:31]\n" \
"v_mfma_f32_16x16x32_bf16 v[60:63], v[184:187], a[132:135], v[60:63]\n" \
"v_mfma_f32_16x16x32_bf16 v[60:63], v[188:191], a[148:151], v[60:63]\n" \
"v_mfma_f32_16x16x32_bf16 v[92:95], v[184:187], a[136:139], v[92:95]\n" \
"buffer_load_dwordx4 a[252:255], v221, %[srdz], s50 offen offset:3072 sc1\n" \
"v_mfma_f32_16x16x32_bf16 v[92:95], v[188:191], a[152:155], v[92:95]\n" \
"v_mfma_f32_16x16x32_bf16 v[124:127], v[184:187], a[140:143], v[124:127]\n" \
"v_mfma_f32_16x16x32_bf16 v[124:127], v[188:191], a[156:159], v[124:127]\n" \
"s_waitcnt vmcnt(20) lgkmcnt(0)\n" \
"v_mfma_f32_16x16x32_bf16 v[0:3], v[128:131], a[160:163], v[0:3]\n" \
"ds_read_b64_tr_b16 v[160:161], v225 offset:17408\n" \
"v_mfma_f32_16x16x32_bf16 v[0:3], v[132:135], a[176:179], v[0:3]\n" \
"ds_read_b64_tr_b16 v[162:163], v225 offset:17664\n" \
"v_mfma_f32_16x16x32_bf16 v[32:35], v[128:131], a[164:167], v[32:35]\n" \
"s_mov_b32 m0, s44\n" \
"s_nop 0\n" \
"buffer_load_dwordx4 v217, %[srdw], s49 offen offset:2048 lds\n" \
"v_mfma_f32_16x16x32_bf16 v[32:35], v[132:135], a[180:183], v[32:35]\n" \
"ds_read_b64_tr_b16 v[164:165], v225 offset:25600\n" \
"v_mfma_f32_16x16x32_bf16 v[64:67], v[128:131], a[168:171], v[64:67]\n" \
"ds_read_b64_tr_b16 v[166:167], v225 offset:25856\n" \
"v_mfma_f32_16x16x32_bf16 v[64:67], v[132:135], a[184:187], v[64:67]\n" \
"v_mfma_f32_16x16x32_bf16 v[96:99], v[128:131], a[172:175], v[96:99]\n" \
"ds_read_b64_tr_b16 v[168:169], v225 offset:17416\n" \
"v_mfma_f32_16x16x32_bf16 v[96:99], v[132:135], a[188:191], v[96:99]\n" \
"ds_read_b64_tr_b16 v[170:171], v225 offset:17672\n" \
"v_mfma_f32_16x16x32_bf16 v[4:7], v[136:139], a[160:163], v[4:7]\n" \
"buffer_load_dwordx4 v218, %[srdw], s49 offen offset:3072 lds\n" \
"v_mfma_f32_16x16x32_bf16 v[4:7], v[140:143], a[176:179], v[4:7]\n" \
"ds_read_b64_tr_b16 v[172:173], v225 offset:25608\n" \
"v_mfma_f32_16x16x32_bf16 v[36:39], v[136:139], a[164:167], v[36:39]\n" \
"ds_read_b64_tr_b16 v[174:175], v225 offset:25864\n" \
"v_mfma_f32_16x16x32_bf16 v[36:39], v[140:143], a[180:183], v[36:39]\n" \
"v_mfma_f32_16x16x32_bf16 v[68:71], v[136:139], a[168:171], v[68:71]\n" \
"ds_read_b64_tr_b16 v[176:177], v225 offset:17920\n" \
"v_mfma_f32_16x16x32_bf16 v[68:71], v[140:143], a[184:187], v[68:71]\n" \
"ds_read_b64_tr_b16 v[178:179], v225 offset:18176\n" \
"v_mfma_f32_16x16x32_bf16 v[100:103], v[136:139], a[172:175], v[100:103]\n" \
"s_add_u32 s50, s50, s53\n" \
"buffer_load_dwordx4 a[128:131], v227, %[srdz], s50 offen offset:0 sc1\n" \
"v_mfma_f32_16x16x32_bf16 v[100:103], v[140:143], a[188:191], v[100:103]\n" \
"ds_read_b64_tr_b16 v[180:181], v225 offset:26112\n" \
"v_mfma_f32_16x16x32_bf16 v[8:11], v[144:147], a[160:163], v[8:11]\n" \
"ds_read_b64_tr_b16 v[182:183], v225 offset:26368\n" \
"v_mfma_f32_16x16x32_bf16 v[8:11], v[148:151], a[176:179], v[8:11]\n" \
"v_mfma_f32_16x16x32_bf16 v[40:43], v[144:147], a[164:167], v[40:43]\n" \
"ds_read_b64_tr_b16 v[184:185], v225 offset:17928\n" \
"v_mfma_f32_16x16x32_bf16 v[40:43], v[148:151], a[180:183], v[40:43]\n" \
"ds_read_b64_tr_b16 v[186:187], v225 offset:18184\n" \
"v_mfma_f32_16x16x32_bf16 v[72:75], v[144:147], a[168:171], v[72:75]\n" \
"buffer_load_dwordx4 a[132:135], v219, %[srdz], s50 offen offset:0 sc1\n" \
"v_mfma_f32_16x16x32_bf16 v[72:75], v[148:151], a[184:187], v[72:75]\n" \
"ds_read_b64_tr_b16 v[188:189], v225 offset:26120\n" \
"v_mfma_f32_16x16x32_bf16 v[104:107], v[144:147], a[172:175], v[104:107]\n" \
"ds_read_b64_tr_b16 v[190:191], v225 offset:26376\n" \
"v_mfma_f32_16x16x32_bf16 v[104:107], v[148:151], a[188:191], v[104:107]\n" \
"v_mfma_f32_16x16x32_bf16 v[12:15], v[152:155], a[160:163], v[12:15]\n" \
"buffer_load_dwordx4 a[136:139], v220, %[srdz], s50 offen offset:0 sc1\n" \
"v_mfma_f32_16x16x32_bf16 v[12:15], v[156:159], a[176:179], v[12:15]\n" \
"v_mfma_f32_16x16x32_bf16 v[44:47], v[152:155], a[164:167], v[44:47]\n" \
"v_mfma_f32_16x16x32_bf16 v[44:47], v[156:159], a[180:183], v[44:47]\n" \
"v_mfma_f32_16x16x32_bf16 v[76:79], v[152:155], a[168:171], v[76:79]\n" \
"buffer_load_dwordx4 a[140:143], v221, %[srdz], s50 offen offset:0 sc1\n" \
"v_mfma_f32_16x16x32_bf16 v[76:79], v[156:159], a[184:187], v[76:79]\n" \
"v_mfma_f32_16x16x32_bf16 v[108:111], v[152:155], a[172:175], v[108:111]\n" \
"v_mfma_f32_16x16x32_bf16 v[108:111], v[156:159], a[188:191], v[108:111]\n" \
"s_waitcnt vmcnt(29) lgkmcnt(0)\n" \
"s_barrier\n" \
"v_mfma_f32_16x16x32_bf16 v[16:19], v[160:163], a[160:163], v[16:19]\n" \
"ds_read_b64_tr_b16 v[128:129], v225 offset:32768\n" \
"v_mfma_f32_16x16x32_bf16 v[16:19], v[164:167], a[176:179], v[16:19]\n" \
"ds_read_b64_tr_b16 v[130:131], v225 offset:33024\n" \
"v_mfma_f32_16x16x32_bf16 v[48:51], v[160:163], a[164:167], v[48:51]\n" \
"s_add_u32 s49, s49, s55\n" \
"s_mov_b32 m0, s45\n" \
"s_nop 0\n" \
"buffer_load_dwordx4 v226, %[srdw], s49 offen offset:0 lds\n" \
"v_mfma_f32_16x16x32_bf16 v[48:51], v[164:167], a[180:183], v[48:51]\n" \
"ds_read_b64_tr_b16 v[132:133], v225 offset:40960\n" \
"v_mfma_f32_16x16x32_bf16 v[80:83], v[160:163], a[168:171], v[80:83]\n" \
"ds_read_b64_tr_b16 v[134:135], v225 offset:41216\n" \
"v_mfma_f32_16x16x32_bf16 v[80:83], v[164:167], a[184:187], v[80:83]\n" \
"v_mfma_f32_16x16x32_bf16 v[112:115], v[160:163], a[172:175], v[112:115]\n" \
"ds_read_b64_tr_b16 v[136:137], v225 offset:32776\n" \
"v_mfma_f32_16x16x32_bf16 v[112:115], v[164:167], a[188:191], v[112:115]\n" \
"ds_read_b64_tr_b16 v[138:139], v225 offset:33032\n" \
"v_mfma_f32_16x16x32_bf16 v[20:23], v[168:171], a[160:163], v[20:23]\n" \
"buffer_load_dwordx4 v216, %[srdw], s49 offen offset:1024 lds\n" \
"v_mfma_f32_16x16x32_bf16 v[20:23], v[172:175], a[176:179], v[20:23]\n" \
"ds_read_b64_tr_b16 v[140:141], v225 offset:40968\n" \
"v_mfma_f32_16x16x32_bf16 v[52:55], v[168:171], a[164:167], v[52:55]\n" \
"ds_read_b64_tr_b16 v[142:143], v225 offset:41224\n" \
"v_mfma_f32_16x16x32_bf16 v[52:55], v[172:175], a[180:183], v[52:55]\n" \
"v_mfma_f32_16x16x32_bf16 v[84:87], v[168:171], a[168:171], v[84:87]\n" \
"ds_read_b64_tr_b16 v[144:145], v225 offset:33280\n" \
"v_mfma_f32_16x16x32_bf16 v[84:87], v[172:175], a[184:187], v[84:87]\n" \
"ds_read_b64_tr_b16 v[146:147], v225 offset:33536\n" \
"v_mfma_f32_16x16x32_bf16 v[116:119], v[168:171], a[172:175], v[116:119]\n" \
"buffer_load_dwordx4 a[144:147], v227, %[srdz], s50 offen offset:1024 sc1\n" \
"v_mfma_f32_16x16x32_bf16 v[116:119], v[172:175], a[188:191], v[116:119]\n" \
"ds_read_b64_tr_b16 v[148:149], v225 offset:41472\n" \
"v_mfma_f32_16x16x32_bf16 v[24:27], v[176:179], a[160:163], v[24:27]\n" \
"ds_read_b64_tr_b16 v[150:151], v225 offset:41728\n" \
"v_mfma_f32_16x16x32_bf16 v[24:27], v[180:183], a[176:179], v[24:27]\n" \
"v_mfma_f32_16x16x32_bf16 v[56:59], v[176:179], a[164:167], v[56:59]\n" \
"ds_read_b64_tr_b16 v[152:153], v225 offset:33288\n" \
"v_mfma_f32_16x16x32_bf16 v[56:59], v[180:183], a[180:183], v[56:59]\n" \
"ds_read_b64_tr_b16 v[154:155], v225 offset:33544\n" \
"v_mfma_f32_16x16x32_bf16 v[88:91], v[176:179], a[168:171], v[88:91]\n" \
"buffer_load_dwordx4 a[148:151], v219, %[srdz], s50 offen offset:1024 sc1\n" \
"v_mfma_f32_16x16x32_bf16 v[88:91], v[180:183], a[184:187], v[88:91]\n" \
"ds_read_b64_tr_b16 v[156:157], v225 offset:41480\n" \
"v_mfma_f32_16x16x32_bf16 v[120:123], v[176:179], a[172:175], v[120:123]\n" \
"ds_read_b64_tr_b16 v[158:159], v225 offset:41736\n" \
"v_mfma_f32_16x16x32_bf16 v[120:123], v[180:183], a[188:191], v[120:123]\n" \
"v_mfma_f32_16x16x32_bf16 v[28:31], v[184:187], a[160:163], v[28:31]\n" \
"buffer_load_dwordx4 a[152:155], v220, %[srdz], s50 offen offset:1024 sc1\n" \
"v_mfma_f32_16x16x32_bf16 v[28:31], v[188:191], a[176:179], v[28:31]\n" \
"v_mfma_f32_16x16x32_bf16 v[60:63], v[184:187], a[164:167], v[60:63]\n" \
"v_mfma_f32_16x16x32_bf16 v[60:63], v[188:191], a[180:183], v[60:63]\n" \
"v_mfma_f32_16x16x32_bf16 v[92:95], v[184:187], a[168:171], v[92:95]\n" \
"buffer_load_dwordx4 a[156:159], v221, %[srdz], s50 offen offset:1024 sc1\n" \
"v_mfma_f32_16x16x32_bf16 v[92:95], v[188:191], a[184:187], v[92:95]\n" \
"v_mfma_f32_16x16x32_bf16 v[124:127], v[184:187], a[172:175], v[124:127]\n" \
"v_mfma_f32_16x16x32_bf16 v[124:127], v[188:191], a[188:191], v[124:127]\n" \
"s_waitcnt vmcnt(24) lgkmcnt(0)\n" \
"v_mfma_f32_16x16x32_bf16 v[0:3], v[128:131], a[192:195], v[0:3]\n" \
"ds_read_b64_tr_b16 v[160:161], v225 offset:33792\n" \
"v_mfma_f32_16x16x32_bf16 v[0:3], v[132:135], a[208:211], v[0:3]\n" \
"ds_read_b64_tr_b16 v[162:163], v225 offset:34048\n" \
"v_mfma_f32_16x16x32_bf16 v[32:35], v[128:131], a[196:199], v[32:35]\n" \
"s_mov_b32 m0, s45\n" \
"s_nop 0\n" \
"buffer_load_dwordx4 v217, %[srdw], s49 offen offset:2048 lds\n" \
"v_mfma_f32_16x16x32_bf16 v[32:35], v[132:135], a[212:215], v[32:35]\n" \
"ds_read_b64_tr_b16 v[164:165], v225 offset:41984\n" \
"v_mfma_f32_16x16x32_bf16 v[64:67], v[128:131], a[200:203], v[64:67]\n" \
"ds_read_b64_tr_b16 v[166:167], v225 offset:42240\n" \
"v_mfma_f32_16x16x32_bf16 v[64:67], v[132:135], a[216:219], v[64:67]\n" \
"v_mfma_f32_16x16x32_bf16 v[96:99], v[128:131], a[204:207], v[96:99]\n" \
"ds_read_b64_tr_b16 v[168:169], v225 offset:33800\n" \
"v_mfma_f32_16x16x32_bf16 v[96:99], v[132:135], a[220:223], v[96:99]\n" \
"ds_read_b64_tr_b16 v[170:171], v225 offset:34056\n" \
"v_mfma_f32_16x16x32_bf16 v[4:7], v[136:139], a[192:195], v[4:7]\n" \
"buffer_load_dwordx4 v218, %[srdw], s49 offen offset:3072 lds\n" \
"v_mfma_f32_16x16x32_bf16 v[4:7], v[140:143], a[208:211], v[4:7]\n" \
"ds_read_b64_tr_b16 v[172:173], v225 offset:41992\n" \
"v_mfma_f32_16x16x32_bf16 v[36:39], v[136:139], a[196:199], v[36:39]\n" \
"ds_read_b64_tr_b16 v[174:175], v225 offset:42248\n" \
"v_mfma_f32_16x16x32_bf16 v[36:39], v[140:143], a[212:215], v[36:39]\n" \
"v_mfma_f32_16x16x32_bf16 v[68:71], v[136:139], a[200:203], v[68:71]\n" \
"ds_read_b64_tr_b16 v[176:177], v225 offset:34304\n" \
"v_mfma_f32_16x16x32_bf16 v[68:71], v[140:143], a[216:219], v[68:71]\n" \
"ds_read_b64_tr_b16 v[178:179], v225 offset:34560\n" \
"v_mfma_f32_16x16x32_bf16 v[100:103], v[136:139], a[204:207], v[100:103]\n" \
"buffer_load_dwordx4 a[160:163], v227, %[srdz], s50 offen offset:2048 sc1\n" \
"v_mfma_f32_16x16x32_bf16 v[100:103], v[140:143], a[220:223], v[100:103]\n" \
"ds_read_b64_tr_b16 v[180:181], v225 offset:42496\n" \
"v_mfma_f32_16x16x32_bf16 v[8:11], v[144:147], a[192:195], v[8:11]\n" \
"ds_read_b64_tr_b16 v[182:183], v225 offset:42752\n" \
"v_mfma_f32_16x16x32_bf16 v[8:11], v[148:151], a[208:211], v[8:11]\n" \
"v_mfma_f32_16x16x32_bf16 v[40:43], v[144:147], a[196:199], v[40:43]\n" \
"ds_read_b64_tr_b16 v[184:185], v225 offset:34312\n" \
"v_mfma_f32_16x16x32_bf16 v[40:43], v[148:151], a[212:215], v[40:43]\n" \
"ds_read_b64_tr_b16 v[186:187], v225 offset:34568\n" \
"v_mfma_f32_16x16x32_bf16 v[72:75], v[144:147], a[200:203], v[72:75]\n" \
"buffer_load_dwordx4 a[164:167], v219, %[srdz], s50 offen offset:2048 sc1\n" \
"v_mfma_f32_16x16x32_bf16 v[72:75], v[148:151], a[216:219], v[72:75]\n" \
"ds_read_b64_tr_b16 v[188:189], v225 offset:42504\n" \
"v_mfma_f32_16x16x32_bf16 v[104:107], v[144:147], a[204:207], v[104:107]\n" \
"ds_read_b64_tr_b16 v[190:191], v225 offset:42760\n" \
"v_mfma_f32_16x16x32_bf16 v[104:107], v[148:151], a[220:223], v[104:107]\n" \
"v_mfma_f32_16x16x32_bf16 v[12:15], v[152:155], a[192:195], v[12:15]\n" \
"buffer_load_dwordx4 a[168:171], v220, %[srdz], s50 offen offset:2048 sc1\n" \
"v_mfma_f32_16x16x32_bf16 v[12:15], v[156:159], a[208:211], v[12:15]\n" \
"v_mfma_f32_16x16x32_bf16 v[44:47], v[152:155], a[196:199], v[44:47]\n" \
"v_mfma_f32_16x16x32_bf16 v[44:47], v[156:159], a[212:215], v[44:47]\n" \
"v_mfma_f32_16x16x32_bf16 v[76:79], v[152:155], a[200:203], v[76:79]\n" \
"buffer_load_dwordx4 a[172:175], v221, %[srdz], s50 offen offset:2048 sc1\n" \
"v_mfma_f32_16x16x32_bf16 v[76:79], v[156:159], a[216:219], v[76:79]\n" \
"v_mfma_f32_16x16x32_bf16 v[108:111], v[152:155], a[204:207], v[108:111]\n" \
"v_mfma_f32_16x16x32_bf16 v[108:111], v[156:159], a[220:223], v[108:111]\n" \
"s_waitcnt vmcnt(28) lgkmcnt(0)\n" \
"s_barrier\n" \
"v_mfma_f32_16x16x32_bf16 v[16:19], v[160:163], a[192:195], v[16:19]\n" \
"ds_read_b64_tr_b16 v[128:129], v225 offset:49152\n" \
"v_mfma_f32_16x16x32_bf16 v[16:19], v[164:167], a[208:211], v[16:19]\n" \
"ds_read_b64_tr_b16 v[130:131], v225 offset:49408\n" \
"v_mfma_f32_16x16x32_bf16 v[48:51], v[160:163], a[196:199], v[48:51]\n" \
"s_add_u32 s49, s49, s55\n" \
"s_mov_b32 m0, s46\n" \
"s_nop 0\n" \
"buffer_load_dwordx4 v226, %[srdw], s49 offen offset:0 lds\n" \
"v_mfma_f32_16x16x32_bf16 v[48:51], v[164:167], a[212:215], v[48:51]\n" \
"ds_read_b64_tr_b16 v[132:133], v225 offset:57344\n" \
"v_mfma_f32_16x16x32_bf16 v[80:83], v[160:163], a[200:203], v[80:83]\n" \
"ds_read_b64_tr_b16 v[134:135], v225 offset:57600\n" \
"v_mfma_f32_16x16x32_bf16 v[80:83], v[164:167], a[216:219], v[80:83]\n" \
"v_mfma_f32_16x16x32_bf16 v[112:115], v[160:163], a[204:207], v[112:115]\n" \
"ds_read_b64_tr_b16 v[136:137], v225 offset:49160\n" \
"v_mfma_f32_16x16x32_bf16 v[112:115], v[164:167], a[220:223], v[112:115]\n" \
"ds_read_b64_tr_b16 v[138:139], v225 offset:49416\n" \
"v_mfma_f32_16x16x32_bf16 v[20:23], v[168:171], a[192:195], v[20:23]\n" \
"buffer_load_dwordx4 v216, %[srdw], s49 offen offset:1024 lds\n" \
"v_mfma_f32_16x16x32_bf16 v[20:23], v[172:175], a[208:211], v[20:23]\n" \
"ds_read_b64_tr_b16 v[140:141], v225 offset:57352\n" \
"v_mfma_f32_16x16x32_bf16 v[52:55], v[168:171], a[196:199], v[52:55]\n" \
"ds_read_b64_tr_b16 v[142:143], v225 offset:57608\n" \
"v_mfma_f32_16x16x32_bf16 v[52:55], v[172:175], a[212:215], v[52:55]\n" \
"v_mfma_f32_16x16x32_bf16 v[84:87], v[168:171], a[200:203], v[84:87]\n" \
"ds_read_b64_tr_b16 v[144:145], v225 offset:49664\n" \
"v_mfma_f32_16x16x32_bf16 v[84:87], v[172:175], a[216:219], v[84:87]\n" \
"ds_read_b64_tr_b16 v[146:147], v225 offset:49920\n" \
"v_mfma_f32_16x16x32_bf16 v[116:119], v[168:171], a[204:207], v[116:119]\n" \
"buffer_load_dwordx4 a[176:179], v227, %[srdz], s50 offen offset:3072 sc1\n" \
"v_mfma_f32_16x16x32_bf16 v[116:119], v[172:175], a[220:223], v[116:119]\n" \
"ds_read_b64_tr_b16 v[148:149], v225 offset:57856\n" \
"v_mfma_f32_16x16x32_bf16 v[24:27], v[176:179], a[192:195], v[24:27]\n" \
"ds_read_b64_tr_b16 v[150:151], v225 offset:58112\n" \
"v_mfma_f32_16x16x32_bf16 v[24:27], v[180:183], a[208:211], v[24:27]\n" \
"v_mfma_f32_16x16x32_bf16 v[56:59], v[176:179], a[196:199], v[56:59]\n" \
"ds_read_b64_tr_b16 v[152:153], v225 offset:49672\n" \
"v_mfma_f32_16x16x32_bf16 v[56:59], v[180:183], a[212:215], v[56:59]\n" \
"ds_read_b64_tr_b16 v[154:155], v225 offset:49928\n" \
"v_mfma_f32_16x16x32_bf16 v[88:91], v[176:179], a[200:203], v[88:91]\n" \
"buffer_load_dwordx4 a[180:183], v219, %[srdz], s50 offen offset:3072 sc1\n" \
"v_mfma_f32_16x16x32_bf16 v[88:91], v[180:183], a[216:219], v[88:91]\n" \
"ds_read_b64_tr_b16 v[156:157], v225 offset:57864\n" \
"v_mfma_f32_16x16x32_bf16 v[120:123], v[176:179], a[204:207], v[120:123]\n" \
"ds_read_b64_tr_b16 v[158:159], v225 offset:58120\n" \
"v_mfma_f32_16x16x32_bf16 v[120:123], v[180:183], a[220:223], v[120:123]\n" \
"v_mfma_f32_16x16x32_bf16 v[28:31], v[184:187], a[192:195], v[28:31]\n" \
"buffer_load_dwordx4 a[184:187], v220, %[srdz], s50 offen offset:3072 sc1\n" \
"v_mfma_f32_16x16x32_bf16 v[28:31], v[188:191], a[208:211], v[28:31]\n" \
"v_mfma_f32_16x16x32_bf16 v[60:63], v[184:187], a[196:199], v[60:63]\n" \
"v_mfma_f32_16x16x32_bf16 v[60:63], v[188:191], a[212:215], v[60:63]\n" \
"v_mfma_f32_16x16x32_bf16 v[92:95], v[184:187], a[200:203], v[92:95]\n" \
"buffer_load_dwordx4 a[188:191], v221, %[srdz], s50 offen offset:3072 sc1\n" \
"v_mfma_f32_16x16x32_bf16 v[92:95], v[188:191], a[216:219], v[92:95]\n" \
"v_mfma_f32_16x16x32_bf16 v[124:127], v[184:187], a[204:207], v[124:127]\n" \
"v_mfma_f32_16x16x32_bf16 v[124:127], v[188:191], a[220:223], v[124:127]\n" \
"s_waitcnt vmcnt(24) lgkmcnt(0)\n" \
"v_mfma_f32_16x16x32_bf16 v[0:3], v[128:131], a[224:227], v[0:3]\n" \
"ds_read_b64_tr_b16 v[160:161], v225 offset:50176\n" \
"v_mfma_f32_16x16x32_bf16 v[0:3], v[132:135], a[240:243], v[0:3]\n" \
"ds_read_b64_tr_b16 v[162:163], v225 offset:50432\n" \
"v_mfma_f32_16x16x32_bf16 v[32:35], v[128:131], a[228:231], v[32:35]\n" \
"s_mov_b32 m0, s46\n" \
"s_nop 0\n" \
"buffer_load_dwordx4 v217, %[srdw], s49 offen offset:2048 lds\n" \
"v_mfma_f32_16x16x32_bf16 v[32:35], v[132:135], a[244:247], v[32:35]\n" \
"ds_read_b64_tr_b16 v[164:165], v225 offset:58368\n" \
"v_mfma_f32_16x16x32_bf16 v[64:67], v[128:131], a[232:235], v[64:67]\n" \
"ds_read_b64_tr_b16 v[166:167], v225 offset:58624\n" \
"v_mfma_f32_16x16x32_bf16 v[64:67], v[132:135], a[248:251], v[64:67]\n" \
"v_mfma_f32_16x16x32_bf16 v[96:99], v[128:131], a[236:239], v[96:99]\n" \
"ds_read_b64_tr_b16 v[168:169], v225 offset:50184\n" \
"v_mfma_f32_16x16x32_bf16 v[96:99], v[132:135], a[252:255], v[96:99]\n" \
"ds_read_b64_tr_b16 v[170:171], v225 offset:50440\n" \
"v_mfma_f32_16x16x32_bf16 v[4:7], v[136:139], a[224:227], v[4:7]\n" \
"buffer_load_dwordx4 v218, %[srdw], s49 offen offset:3072 lds\n" \
"v_mfma_f32_16x16x32_bf16 v[4:7], v[140:143], a[240:243], v[4:7]\n" \
"ds_read_b64_tr_b16 v[172:173], v225 offset:58376\n" \
"v_mfma_f32_16x16x32_bf16 v[36:39], v[136:139], a[228:231], v[36:39]\n" \
"ds_read_b64_tr_b16 v[174:175], v225 offset:58632\n" \
"v_mfma_f32_16x16x32_bf16 v[36:39], v[140:143], a[244:247], v[36:39]\n" \
"v_mfma_f32_16x16x32_bf16 v[68:71], v[136:139], a[232:235], v[68:71]\n" \
"ds_read_b64_tr_b16 v[176:177], v225 offset:50688\n" \
"v_mfma_f32_16x16x32_bf16 v[68:71], v[140:143], a[248:251], v[68:71]\n" \
"ds_read_b64_tr_b16 v[178:179], v225 offset:50944\n" \
"v_mfma_f32_16x16x32_bf16 v[100:103], v[136:139], a[236:239], v[100:103]\n" \
"s_add_u32 s50, s50, s53\n" \
"buffer_load_dwordx4 a[192:195], v227, %[srdz], s50 offen offset:0 sc1\n" \
"v_mfma_f32_16x16x32_bf16 v[100:103], v[140:143], a[252:255], v[100:103]\n" \
"ds_read_b64_tr_b16 v[180:181], v225 offset:58880\n" \
"v_mfma_f32_16x16x32_bf16 v[8:11], v[144:147], a[224:227], v[8:11]\n" \
"ds_read_b64_tr_b16 v[182:183], v225 offset:59136\n" \
"v_mfma_f32_16x16x32_bf16 v[8:11], v[148:151], a[240:243], v[8:11]\n" \
"v_mfma_f32_16x16x32_bf16 v[40:43], v[144:147], a[228:231], v[40:43]\n" \
"ds_read_b64_tr_b16 v[184:185], v225 offset:50696\n" \
"v_mfma_f32_16x16x32_bf16 v[40:43], v[148:151], a[244:247], v[40:43]\n" \
"ds_read_b64_tr_b16 v[186:187], v225 offset:50952\n" \
"v_mfma_f32_16x16x32_bf16 v[72:75], v[144:147], a[232:235], v[72:75]\n" \
"buffer_load_dwordx4 a[196:199], v219, %[srdz], s50 offen offset:0 sc1\n" \
"v_mfma_f32_16x16x32_bf16 v[72:75], v[148:151], a[248:251], v[72:75]\n" \
"ds_read_b64_tr_b16 v[188:189], v225 offset:58888\n" \
"v_mfma_f32_16x16x32_bf16 v[104:107], v[144:147], a[236:239], v[104:107]\n" \
"ds_read_b64_tr_b16 v[190:191], v225 offset:59144\n" \
"v_mfma_f32_16x16x32_bf16 v[104:107], v[148:151], a[252:255], v[104:107]\n" \
"v_mfma_f32_16x16x32_bf16 v[12:15], v[152:155], a[224:227], v[12:15]\n" \
"buffer_load_dwordx4 a[200:203], v220, %[srdz], s50 offen offset:0 sc1\n" \
"v_mfma_f32_16x16x32_bf16 v[12:15], v[156:159], a[240:243], v[12:15]\n" \
"v_mfma_f32_16x16x32_bf16 v[44:47], v[152:155], a[228:231], v[44:47]\n" \
"buffer_load_dword v237, v236, %[srdw], s60 offen\n" \
"s_add_u32 s60, s60, 0x200000\n" \
"v_mfma_f32_16x16x32_bf16 v[44:47], v[156:159], a[244:247], v[44:47]\n" \
".if . - adaln_body != 4096\n" \
".error \"size model\"\n" \
".endif\n" \
"Lk0_%=:\n" \
"s_cbranch_execz Lk1_%=\n" \
"s_nop 0\n" \
"v_mfma_f32_16x16x32_bf16 v[76:79], v[152:155], a[232:235], v[76:79]\n" \
"buffer_load_dwordx4 a[204:207], v221, %[srdz], s50 offen offset:0 sc1\n" \
"v_mfma_f32_16x16x32_bf16 v[76:79], v[156:159], a[248:251], v[76:79]\n" \
"v_mfma_f32_16x16x32_bf16 v[108:111], v[152:155], a[236:239], v[108:111]\n" \
"v_mfma_f32_16x16x32_bf16 v[108:111], v[156:159], a[252:255], v[108:111]\n" \
"s_waitcnt vmcnt(29) lgkmcnt(0)\n" \
"s_barrier\n" \
"v_mfma_f32_16x16x32_bf16 v[16:19], v[160:163], a[224:227], v[16:19]\n" \
"ds_read_b64_tr_b16 v[128:129], v225 offset:0\n" \
"v_mfma_f32_16x16x32_bf16 v[16:19], v[164:167], a[240:243], v[16:19]\n" \
"ds_read_b64_tr_b16 v[130:131], v225 offset:256\n" \
"v_mfma_f32_16x16x32_bf16 v[48:51], v[160:163], a[228:231], v[48:51]\n" \
"s_add_u32 s49, s49, s55\n" \
"s_mov_b32 m0, s47\n" \
"s_nop 0\n" \
"buffer_load_dwordx4 v226, %[srdw], s49 offen offset:0 lds\n" \
"v_mfma_f32_16x16x32_bf16 v[48:51], v[164:167], a[244:247], v[48:51]\n" \
"ds_read_b64_tr_b16 v[132:133], v225 offset:8192\n" \
"v_mfma_f32_16x16x32_bf16 v[80:83], v[160:163], a[232:235], v[80:83]\n" \
"ds_read_b64_tr_b16 v[134:135], v225 offset:8448\n" \
"v_mfma_f32_16x16x32_bf16 v[80:83], v[164:167], a[248:251], v[80:83]\n" \
"v_mfma_f32_16x16x32_bf16 v[112:115], v[160:163], a[236:239], v[112:115]\n" \
"ds_read_b64_tr_b16 v[136:137], v225 offset:8\n" \
"v_mfma_f32_16x16x32_bf16 v[112:115], v[164:167], a[252:255], v[112:115]\n" \
"ds_read_b64_tr_b16 v[138:139], v225 offset:264\n" \
"v_mfma_f32_16x16x32_bf16 v[20:23], v[168:171], a[224:227], v[20:23]\n" \
"buffer_load_dwordx4 v216, %[srdw], s49 offen offset:1024 lds\n" \
"v_mfma_f32_16x16x32_bf16 v[20:23], v[172:175], a[240:243], v[20:23]\n" \
"ds_read_b64_tr_b16 v[140:141], v225 offset:8200\n" \
"v_mfma_f32_16x16x32_bf16 v[52:55], v[168:171], a[228:231], v[52:55]\n" \
"ds_read_b64_tr_b16 v[142:143], v225 offset:8456\n" \
"v_mfma_f32_16x16x32_bf16 v[52:55], v[172:175], a[244:247], v[52:55]\n" \
"v_mfma_f32_16x16x32_bf16 v[84:87], v[168:171], a[232:235], v[84:87]\n" \
"ds_read_b64_tr_b16 v[144:145], v225 offset:512\n" \
"v_mfma_f32_16x16x32_bf16 v[84:87], v[172:175], a[248:251], v[84:87]\n" \
"ds_read_b64_tr_b16 v[146:147], v225 offset:768\n" \
"v_mfma_f32_16x16x32_bf16 v[116:119], v[168:171], a[236:239], v[116:119]\n" \
"buffer_load_dwordx4 a[208:211], v227, %[srdz], s50 offen offset:1024 sc1\n" \
"v_mfma_f32_16x16x32_bf16 v[116:119], v[172:175], a[252:255], v[116:119]\n" \
"ds_read_b64_tr_b16 v[148:149], v225 offset:8704\n" \
"v_mfma_f32_16x16x32_bf16 v[24:27], v[176:179], a[224:227], v[24:27]\n" \
"ds_read_b64_tr_b16 v[150:151], v225 offset:8960\n" \
"v_mfma_f32_16x16x32_bf16 v[24:27], v[180:183], a[240:243], v[24:27]\n" \
"v_mfma_f32_16x16x32_bf16 v[56:59], v[176:179], a[228:231], v[56:59]\n" \
"ds_read_b64_tr_b16 v[152:153], v225 offset:520\n" \
"v_mfma_f32_16x16x32_bf16 v[56:59], v[180:183], a[244:247], v[56:59]\n" \
"ds_read_b64_tr_b16 v[154:155], v225 offset:776\n" \
"v_mfma_f32_16x16x32_bf16 v[88:91], v[176:179], a[232:235], v[88:91]\n" \
"buffer_load_dwordx4 a[212:215], v219, %[srdz], s50 offen offset:1024 sc1\n" \
"v_mfma_f32_16x16x32_bf16 v[88:91], v[180:183], a[248:251], v[88:91]\n" \
"ds_read_b64_tr_b16 v[156:157], v225 offset:8712\n" \
"v_mfma_f32_16x16x32_bf16 v[120:123], v[176:179], a[236:239], v[120:123]\n" \
"ds_read_b64_tr_b16 v[158:159], v225 offset:8968\n" \
"v_mfma_f32_16x16x32_bf16 v[120:123], v[180:183], a[252:255], v[120:123]\n" \
"v_mfma_f32_16x16x32_bf16 v[28:31], v[184:187], a[224:227], v[28:31]\n" \
"buffer_load_dwordx4 a[216:219], v220, %[srdz], s50 offen offset:1024 sc1\n" \
"v_mfma_f32_16x16x32_bf16 v[28:31], v[188:191], a[240:243], v[28:31]\n" \
"v_mfma_f32_16x16x32_bf16 v[60:63], v[184:187], a[228:231], v[60:63]\n" \
"v_mfma_f32_16x16x32_bf16 v[60:63], v[188:191], a[244:247], v[60:63]\n" \
"v_mfma_f32_16x16x32_bf16 v[92:95], v[184:187], a[232:235], v[92:95]\n" \
"buffer_load_dwordx4 a[220:223], v221, %[srdz], s50 offen offset:1024 sc1\n" \
"v_mfma_f32_16x16x32_bf16 v[92:95], v[188:191], a[248:251], v[92:95]\n" \
"v_mfma_f32_16x16x32_bf16 v[124:127], v[184:187], a[236:239], v[124:127]\n" \
"v_mfma_f32_16x16x32_bf16 v[124:127], v[188:191], a[252:255], v[124:127]\n" \
"s_waitcnt vmcnt(25) lgkmcnt(0)\n" \
"s_sub_u32 s51, s51, 1\n" \
"s_cmp_lg_u32 s51, 0\n" \
"s_cbranch_scc1 Lq_%=\n" \
"s_nop 0\n" \
"v_mfma_f32_16x16x32_bf16 v[0:3], v[128:131], a[128:131], v[0:3]\n" \
"ds_read_b64_tr_b16 v[160:161], v225 offset:1024\n" \
"v_mfma_f32_16x16x32_bf16 v[0:3], v[132:135], a[144:147], v[0:3]\n" \
"ds_read_b64_tr_b16 v[162:163], v225 offset:1280\n" \
"v_mfma_f32_16x16x32_bf16 v[32:35], v[128:131], a[132:135], v[32:35]\n" \
"s_mov_b32 m0, s47\n" \
"s_nop 0\n" \
"buffer_load_dwordx4 v217, %[srdw], s49 offen offset:2048 lds\n" \
"v_mfma_f32_16x16x32_bf16 v[32:35], v[132:135], a[148:151], v[32:35]\n" \
"ds_read_b64_tr_b16 v[164:165], v225 offset:9216\n" \
"v_mfma_f32_16x16x32_bf16 v[64:67], v[128:131], a[136:139], v[64:67]\n" \
"ds_read_b64_tr_b16 v[166:167], v225 offset:9472\n" \
"v_mfma_f32_16x16x32_bf16 v[64:67], v[132:135], a[152:155], v[64:67]\n" \
"v_mfma_f32_16x16x32_bf16 v[96:99], v[128:131], a[140:143], v[96:99]\n" \
"ds_read_b64_tr_b16 v[168:169], v225 offset:1032\n" \
"v_mfma_f32_16x16x32_bf16 v[96:99], v[132:135], a[156:159], v[96:99]\n" \
"ds_read_b64_tr_b16 v[170:171], v225 offset:1288\n" \
"v_mfma_f32_16x16x32_bf16 v[4:7], v[136:139], a[128:131], v[4:7]\n" \
"buffer_load_dwordx4 v218, %[srdw], s49 offen offset:3072 lds\n" \
"v_mfma_f32_16x16x32_bf16 v[4:7], v[140:143], a[144:147], v[4:7]\n" \
"ds_read_b64_tr_b16 v[172:173], v225 offset:9224\n" \
"v_mfma_f32_16x16x32_bf16 v[36:39], v[136:139], a[132:135], v[36:39]\n" \
"ds_read_b64_tr_b16 v[174:175], v225 offset:9480\n" \
"v_mfma_f32_16x16x32_bf16 v[36:39], v[140:143], a[148:151], v[36:39]\n" \
"v_mfma_f32_16x16x32_bf16 v[68:71], v[136:139], a[136:139], v[68:71]\n" \
"ds_read_b64_tr_b16 v[176:177], v225 offset:1536\n" \
"v_mfma_f32_16x16x32_bf16 v[68:71], v[140:143], a[152:155], v[68:71]\n" \
"ds_read_b64_tr_b16 v[178:179], v225 offset:1792\n" \
"v_mfma_f32_16x16x32_bf16 v[100:103], v[136:139], a[140:143], v[100:103]\n" \
"buffer_load_dwordx4 a[224:227], v227, %[srdz], s50 offen offset:2048 sc1\n" \
"v_mfma_f32_16x16x32_bf16 v[100:103], v[140:143], a[156:159], v[100:103]\n" \
"ds_read_b64_tr_b16 v[180:181], v225 offset:9728\n" \
"v_mfma_f32_16x16x32_bf16 v[8:11], v[144:147], a[128:131], v[8:11]\n" \
"ds_read_b64_tr_b16 v[182:183], v225 offset:9984\n" \
"v_mfma_f32_16x16x32_bf16 v[8:11], v[148:151], a[144:147], v[8:11]\n" \
"buffer_load_dwordx4 v[230:233], off, %[srdz], 0 offset:128 sc1\n" \
"v_mfma_f32_16x16x32_bf16 v[40:43], v[144:147], a[132:135], v[40:43]\n" \
"ds_read_b64_tr_b16 v[184:185], v225 offset:1544\n" \
"v_mfma_f32_16x16x32_bf16 v[40:43], v[148:151], a[148:151], v[40:43]\n" \
"ds_read_b64_tr_b16 v[186:187], v225 offset:1800\n" \
"v_mfma_f32_16x16x32_bf16 v[72:75], v[144:147], a[136:139], v[72:75]\n" \
"buffer_load_dwordx4 a[228:231], v219, %[srdz], s50 offen offset:2048 sc1\n" \
"v_mfma_f32_16x16x32_bf16 v[72:75], v[148:151], a[152:155], v[72:75]\n" \
"ds_read_b64_tr_b16 v[188:189], v225 offset:9736\n" \
"v_mfma_f32_16x16x32_bf16 v[104:107], v[144:147], a[140:143], v[104:107]\n" \
"ds_read_b64_tr_b16 v[190:191], v225 offset:9992\n" \
"v_mfma_f32_16x16x32_bf16 v[104:107], v[148:151], a[156:159], v[104:107]\n" \
"v_mfma_f32_16x16x32_bf16 v[12:15], v[152:155], a[128:131], v[12:15]\n" \
"buffer_load_dwordx4 a[232:235], v220, %[srdz], s50 offen offset:2048 sc1\n" \
"v_mfma_f32_16x16x32_bf16 v[12:15], v[156:159], a[144:147], v[12:15]\n" \
"v_mfma_f32_16x16x32_bf16 v[44:47], v[152:155], a[132:135], v[44:47]\n" \
"v_mfma_f32_16x16x32_bf16 v[44:47], v[156:159], a[148:151], v[44:47]\n" \
"v_mfma_f32_16x16x32_bf16 v[76:79], v[152:155], a[136:139], v[76:79]\n" \
"buffer_load_dwordx4 a[236:239], v221, %[srdz], s50 offen offset:2048 sc1\n" \
"v_mfma_f32_16x16x32_bf16 v[76:79], v[156:159], a[152:155], v[76:79]\n" \
"v_mfma_f32_16x16x32_bf16 v[108:111], v[152:155], a[140:143], v[108:111]\n" \
"v_mfma_f32_16x16x32_bf16 v[108:111], v[156:159], a[156:159], v[108:111]\n" \
"s_waitcnt vmcnt(30) lgkmcnt(0)\n" \
"s_barrier\n" \
"v_mfma_f32_16x16x32_bf16 v[16:19], v[160:163], a[128:131], v[16:19]\n" \
"ds_read_b64_tr_b16 v[128:129], v225 offset:16384\n" \
"v_mfma_f32_16x16x32_bf16 v[16:19], v[164:167], a[144:147], v[16:19]\n" \
"ds_read_b64_tr_b16 v[130:131], v225 offset:16640\n" \
"v_mfma_f32_16x16x32_bf16 v[48:51], v[160:163], a[132:135], v[48:51]\n" \
"s_mov_b32 s48, 16384\n" \
"s_mov_b32 m0, s44\n" \
"s_nop 0\n" \
"buffer_load_dwordx4 %[vd0], %[srdv], s48 offen offset:0 lds\n" \
"v_mfma_f32_16x16x32_bf16 v[48:51], v[164:167], a[148:151], v[48:51]\n" \
"ds_read_b64_tr_b16 v[132:133], v225 offset:24576\n" \
"v_mfma_f32_16x16x32_bf16 v[80:83], v[160:163], a[136:139], v[80:83]\n" \
"ds_read_b64_tr_b16 v[134:135], v225 offset:24832\n" \
"v_mfma_f32_16x16x32_bf16 v[80:83], v[164:167], a[152:155], v[80:83]\n" \
"v_mfma_f32_16x16x32_bf16 v[112:115], v[160:163], a[140:143], v[112:115]\n" \
"ds_read_b64_tr_b16 v[136:137], v225 offset:16392\n" \
"v_mfma_f32_16x16x32_bf16 v[112:115], v[164:167], a[156:159], v[112:115]\n" \
"ds_read_b64_tr_b16 v[138:139], v225 offset:16648\n" \
"v_mfma_f32_16x16x32_bf16 v[20:23], v[168:171], a[128:131], v[20:23]\n" \
"buffer_load_dwordx4 v215, %[srdv], s48 offen offset:1024 lds\n" \
"v_mfma_f32_16x16x32_bf16 v[20:23], v[172:175], a[144:147], v[20:23]\n" \
"ds_read_b64_tr_b16 v[140:141], v225 offset:24584\n" \
"v_mfma_f32_16x16x32_bf16 v[52:55], v[168:171], a[132:135], v[52:55]\n" \
"ds_read_b64_tr_b16 v[142:143], v225 offset:24840\n" \
"v_mfma_f32_16x16x32_bf16 v[52:55], v[172:175], a[148:151], v[52:55]\n" \
"v_mfma_f32_16x16x32_bf16 v[84:87], v[168:171], a[136:139], v[84:87]\n" \
"ds_read_b64_tr_b16 v[144:145], v225 offset:16896\n" \
"v_mfma_f32_16x16x32_bf16 v[84:87], v[172:175], a[152:155], v[84:87]\n" \
"ds_read_b64_tr_b16 v[146:147], v225 offset:17152\n" \
"v_mfma_f32_16x16x32_bf16 v[116:119], v[168:171], a[140:143], v[116:119]\n" \
"buffer_load_dwordx4 a[240:243], v227, %[srdz], s50 offen offset:3072 sc1\n" \
"v_mfma_f32_16x16x32_bf16 v[116:119], v[172:175], a[156:159], v[116:119]\n" \
"ds_read_b64_tr_b16 v[148:149], v225 offset:25088\n" \
"v_mfma_f32_16x16x32_bf16 v[24:27], v[176:179], a[128:131], v[24:27]\n" \
"ds_read_b64_tr_b16 v[150:151], v225 offset:25344\n" \
"v_mfma_f32_16x16x32_bf16 v[24:27], v[180:183], a[144:147], v[24:27]\n" \
"v_mfma_f32_16x16x32_bf16 v[56:59], v[176:179], a[132:135], v[56:59]\n" \
"ds_read_b64_tr_b16 v[152:153], v225 offset:16904\n" \
"v_mfma_f32_16x16x32_bf16 v[56:59], v[180:183], a[148:151], v[56:59]\n" \
"ds_read_b64_tr_b16 v[154:155], v225 offset:17160\n" \
"v_mfma_f32_16x16x32_bf16 v[88:91], v[176:179], a[136:139], v[88:91]\n" \
"buffer_load_dwordx4 a[244:247], v219, %[srdz], s50 offen offset:3072 sc1\n" \
"v_mfma_f32_16x16x32_bf16 v[88:91], v[180:183], a[152:155], v[88:91]\n" \
"ds_read_b64_tr_b16 v[156:157], v225 offset:25096\n" \
"v_mfma_f32_16x16x32_bf16 v[120:123], v[176:179], a[140:143], v[120:123]\n" \
"ds_read_b64_tr_b16 v[158:159], v225 offset:25352\n" \
"v_mfma_f32_16x16x32_bf16 v[120:123], v[180:183], a[156:159], v[120:123]\n" \
"v_mfma_f32_16x16x32_bf16 v[28:31], v[184:187], a[128:131], v[28:31]\n" \
"buffer_load_dwordx4 a[248:251], v220, %[srdz], s50 offen offset:3072 sc1\n" \
"v_mfma_f32_16x16x32_bf16 v[28:31], v[188:191], a[144:147], v[28:31]\n" \
"v_mfma_f32_16x16x32_bf16 v[60:63], v[184:187], a[132:135], v[60:63]\n" \
"v_mfma_f32_16x16x32_bf16 v[60:63], v[188:191], a[148:151], v[60:63]\n" \
"v_mfma_f32_16x16x32_bf16 v[92:95], v[184:187], a[136:139], v[92:95]\n" \
"buffer_load_dwordx4 a[252:255], v221, %[srdz], s50 offen offset:3072 sc1\n" \
"v_mfma_f32_16x16x32_bf16 v[92:95], v[188:191], a[152:155], v[92:95]\n" \
"v_mfma_f32_16x16x32_bf16 v[124:127], v[184:187], a[140:143], v[124:127]\n" \
"v_mfma_f32_16x16x32_bf16 v[124:127], v[188:191], a[156:159], v[124:127]\n" \
"s_waitcnt vmcnt(26) lgkmcnt(0)\n" \
"v_mfma_f32_16x16x32_bf16 v[0:3], v[128:131], a[160:163], v[0:3]\n" \
"ds_read_b64_tr_b16 v[160:161], v225 offset:17408\n" \
"v_mfma_f32_16x16x32_bf16 v[0:3], v[132:135], a[176:179], v[0:3]\n" \
"ds_read_b64_tr_b16 v[162:163], v225 offset:17664\n" \
"v_mfma_f32_16x16x32_bf16 v[32:35], v[128:131], a[164:167], v[32:35]\n" \
"s_mov_b32 s48, 16384\n" \
"s_mov_b32 m0, s44\n" \
"s_nop 0\n" \
"buffer_load_dwordx4 %[vd0], %[srdv], s48 offen offset:2048 lds\n" \
"v_mfma_f32_16x16x32_bf16 v[32:35], v[132:135], a[180:183], v[32:35]\n" \
"ds_read_b64_tr_b16 v[164:165], v225 offset:25600\n" \
"v_mfma_f32_16x16x32_bf16 v[64:67], v[128:131], a[168:171], v[64:67]\n" \
"ds_read_b64_tr_b16 v[166:167], v225 offset:25856\n" \
"v_mfma_f32_16x16x32_bf16 v[64:67], v[132:135], a[184:187], v[64:67]\n" \
"v_mfma_f32_16x16x32_bf16 v[96:99], v[128:131], a[172:175], v[96:99]\n" \
"ds_read_b64_tr_b16 v[168:169], v225 offset:17416\n" \
"v_mfma_f32_16x16x32_bf16 v[96:99], v[132:135], a[188:191], v[96:99]\n" \
"ds_read_b64_tr_b16 v[170:171], v225 offset:17672\n" \
"v_mfma_f32_16x16x32_bf16 v[4:7], v[136:139], a[160:163], v[4:7]\n" \
"buffer_load_dwordx4 v215, %[srdv], s48 offen offset:3072 lds\n" \
"v_mfma_f32_16x16x32_bf16 v[4:7], v[140:143], a[176:179], v[4:7]\n" \
"ds_read_b64_tr_b16 v[172:173], v225 offset:25608\n" \
"v_mfma_f32_16x16x32_bf16 v[36:39], v[136:139], a[164:167], v[36:39]\n" \
"ds_read_b64_tr_b16 v[174:175], v225 offset:25864\n" \
"v_mfma_f32_16x16x32_bf16 v[36:39], v[140:143], a[180:183], v[36:39]\n" \
"v_mfma_f32_16x16x32_bf16 v[68:71], v[136:139], a[168:171], v[68:71]\n" \
"ds_read_b64_tr_b16 v[176:177], v225 offset:17920\n" \
"v_mfma_f32_16x16x32_bf16 v[68:71], v[140:143], a[184:187], v[68:71]\n" \
"ds_read_b64_tr_b16 v[178:179], v225 offset:18176\n" \
"v_mfma_f32_16x16x32_bf16 v[100:103], v[136:139], a[172:175], v[100:103]\n" \
"s_mov_b32 s48, 0\n" \
"s_mov_b32 m0, s40\n" \
"s_nop 0\n" \
"buffer_load_dwordx4 %[kd0], %[srdk], s48 offen offset:0 lds\n" \
"v_mfma_f32_16x16x32_bf16 v[100:103], v[140:143], a[188:191], v[100:103]\n" \
"ds_read_b64_tr_b16 v[180:181], v225 offset:26112\n" \
"v_mfma_f32_16x16x32_bf16 v[8:11], v[144:147], a[160:163], v[8:11]\n" \
"ds_read_b64_tr_b16 v[182:183], v225 offset:26368\n" \
"v_mfma_f32_16x16x32_bf16 v[8:11], v[148:151], a[176:179], v[8:11]\n" \
"v_mfma_f32_16x16x32_bf16 v[40:43], v[144:147], a[164:167], v[40:43]\n" \
"ds_read_b64_tr_b16 v[184:185], v225 offset:17928\n" \
"v_mfma_f32_16x16x32_bf16 v[40:43], v[148:151], a[180:183], v[40:43]\n" \
"ds_read_b64_tr_b16 v[186:187], v225 offset:18184\n" \
"v_mfma_f32_16x16x32_bf16 v[72:75], v[144:147], a[168:171], v[72:75]\n" \
"buffer_load_dwordx4 v212, %[srdk], s48 offen offset:1024 lds\n" \
"v_mfma_f32_16x16x32_bf16 v[72:75], v[148:151], a[184:187], v[72:75]\n" \
"ds_read_b64_tr_b16 v[188:189], v225 offset:26120\n" \
"v_mfma_f32_16x16x32_bf16 v[104:107], v[144:147], a[172:175], v[104:107]\n" \
"ds_read_b64_tr_b16 v[190:191], v225 offset:26376\n" \
"v_mfma_f32_16x16x32_bf16 v[104:107], v[148:151], a[188:191], v[104:107]\n" \
"v_mfma_f32_16x16x32_bf16 v[12:15], v[152:155], a[160:163], v[12:15]\n" \
"buffer_load_dwordx4 v213, %[srdk], s48 offen offset:2048 lds\n" \
"v_mfma_f32_16x16x32_bf16 v[12:15], v[156:159], a[176:179], v[12:15]\n" \
"v_mfma_f32_16x16x32_bf16 v[44:47], v[152:155], a[164:167], v[44:47]\n" \
"v_mfma_f32_16x16x32_bf16 v[44:47], v[156:159], a[180:183], v[44:47]\n" \
"v_mfma_f32_16x16x32_bf16 v[76:79], v[152:155], a[168:171], v[76:79]\n" \
"buffer_load_dwordx4 v214, %[srdk], s48 offen offset:3072 lds\n" \
"v_mfma_f32_16x16x32_bf16 v[76:79], v[156:159], a[184:187], v[76:79]\n" \
"v_mfma_f32_16x16x32_bf16 v[108:111], v[152:155], a[172:175], v[108:111]\n" \
"v_mfma_f32_16x16x32_bf16 v[108:111], v[156:159], a[188:191], v[108:111]\n" \
"s_waitcnt vmcnt(30) lgkmcnt(0)\n" \
"s_barrier\n" \
"v_mfma_f32_16x16x32_bf16 v[16:19], v[160:163], a[160:163], v[16:19]\n" \
"ds_read_b64_tr_b16 v[128:129], v225 offset:32768\n" \
"v_mfma_f32_16x16x32_bf16 v[16:19], v[164:167], a[176:179], v[16:19]\n" \
"ds_read_b64_tr_b16 v[130:131], v225 offset:33024\n" \
"v_mfma_f32_16x16x32_bf16 v[48:51], v[160:163], a[164:167], v[48:51]\n" \
"s_mov_b32 s48, 32768\n" \
"s_mov_b32 m0, s45\n" \
"s_nop 0\n" \
"buffer_load_dwordx4 %[vd0], %[srdv], s48 offen offset:0 lds\n" \
"v_mfma_f32_16x16x32_bf16 v[48:51], v[164:167], a[180:183], v[48:51]\n" \
"ds_read_b64_tr_b16 v[132:133], v225 offset:40960\n" \
"v_mfma_f32_16x16x32_bf16 v[80:83], v[160:163], a[168:171], v[80:83]\n" \
"ds_read_b64_tr_b16 v[134:135], v225 offset:41216\n" \
"v_mfma_f32_16x16x32_bf16 v[80:83], v[164:167], a[184:187], v[80:83]\n" \
"v_mfma_f32_16x16x32_bf16 v[112:115], v[160:163], a[172:175], v[112:115]\n" \
"ds_read_b64_tr_b16 v[136:137], v225 offset:32776\n" \
"v_mfma_f32_16x16x32_bf16 v[112:115], v[164:167], a[188:191], v[112:115]\n" \
"ds_read_b64_tr_b16 v[138:139], v225 offset:33032\n" \
"v_mfma_f32_16x16x32_bf16 v[20:23], v[168:171], a[160:163], v[20:23]\n" \
"buffer_load_dwordx4 v215, %[srdv], s48 offen offset:1024 lds\n" \
"v_mfma_f32_16x16x32_bf16 v[20:23], v[172:175], a[176:179], v[20:23]\n" \
"ds_read_b64_tr_b16 v[140:141], v225 offset:40968\n" \
"v_mfma_f32_16x16x32_bf16 v[52:55], v[168:171], a[164:167], v[52:55]\n" \
"ds_read_b64_tr_b16 v[142:143], v225 offset:41224\n" \
"v_mfma_f32_16x16x32_bf16 v[52:55], v[172:175], a[180:183], v[52:55]\n" \
"v_mfma_f32_16x16x32_bf16 v[84:87], v[168:171], a[168:171], v[84:87]\n" \
"ds_read_b64_tr_b16 v[144:145], v225 offset:33280\n" \
"v_mfma_f32_16x16x32_bf16 v[84:87], v[172:175], a[184:187], v[84:87]\n" \
"ds_read_b64_tr_b16 v[146:147], v225 offset:33536\n" \
"v_mfma_f32_16x16x32_bf16 v[116:119], v[168:171], a[172:175], v[116:119]\n" \
"s_mov_b32 s48, 16384\n" \
"s_mov_b32 m0, s41\n" \
"s_nop 0\n" \
"buffer_load_dwordx4 %[kd0], %[srdk], s48 offen offset:0 lds\n" \
"v_mfma_f32_16x16x32_bf16 v[116:119], v[172:175], a[188:191], v[116:119]\n" \
"ds_read_b64_tr_b16 v[148:149], v225 offset:41472\n" \
"v_mfma_f32_16x16x32_bf16 v[24:27], v[176:179], a[160:163], v[24:27]\n" \
"ds_read_b64_tr_b16 v[150:151], v225 offset:41728\n" \
"v_mfma_f32_16x16x32_bf16 v[24:27], v[180:183], a[176:179], v[24:27]\n" \
"s_waitcnt vmcnt(18)\n" \
"v_cmp_eq_u64_e32 vcc, v[230:231], v[234:235]\n" \
"v_cmp_eq_u64_e64 s[56:57], v[232:233], v[234:235]\n" \
"s_nop 3\n" \
"s_or_b64 s[58:59], vcc, s[56:57]\n" \
"s_cbranch_scc0 Lnsp_%=\n" \
"Lnbk_%=:\n" \
"v_mfma_f32_16x16x32_bf16 v[56:59], v[176:179], a[164:167], v[56:59]\n" \
"ds_read_b64_tr_b16 v[152:153], v225 offset:33288\n" \
"v_mfma_f32_16x16x32_bf16 v[56:59], v[180:183], a[180:183], v[56:59]\n" \
"ds_read_b64_tr_b16 v[154:155], v225 offset:33544\n" \
"s_cmp_lg_u64 s[56:57], 0\n" \
"s_cselect_b32 %[cx], 0x2000, 0\n" \
"s_xor_b32 %[sin], %[sin], %[cx]\n" \
"v_mfma_f32_16x16x32_bf16 v[88:91], v[176:179], a[168:171], v[88:91]\n" \
"buffer_load_dwordx4 v212, %[srdk], s48 offen offset:1024 lds\n" \
"v_mfma_f32_16x16x32_bf16 v[88:91], v[180:183], a[184:187], v[88:91]\n" \
"ds_read_b64_tr_b16 v[156:157], v225 offset:41480\n" \
"v_mfma_f32_16x16x32_bf16 v[120:123], v[176:179], a[172:175], v[120:123]\n" \
"ds_read_b64_tr_b16 v[158:159], v225 offset:41736\n" \
"v_mfma_f32_16x16x32_bf16 v[120:123], v[180:183], a[188:191], v[120:123]\n" \
"s_mov_b64 exec, 0x2c4000\n" \
"v_xor_b32_e32 %[tlo], %[cx], %[tlo]\n" \
"s_mov_b64 exec, 0x40000\n" \
"v_xor_b32_e32 %[thi], %[cx], %[thi]\n" \
"s_mov_b64 exec, -1\n" \
"v_mfma_f32_16x16x32_bf16 v[28:31], v[184:187], a[160:163], v[28:31]\n" \
"buffer_load_dwordx4 v213, %[srdk], s48 offen offset:2048 lds\n" \
"v_mfma_f32_16x16x32_bf16 v[28:31], v[188:191], a[176:179], v[28:31]\n" \
"v_xor_b32_e32 %[oq0], %[cx], %[oq0]\n" \
"v_mfma_f32_16x16x32_bf16 v[60:63], v[184:187], a[164:167], v[60:63]\n" \
"v_mfma_f32_16x16x32_bf16 v[60:63], v[188:191], a[180:183], v[60:63]\n" \
"v_mfma_f32_16x16x32_bf16 v[92:95], v[184:187], a[168:171], v[92:95]\n" \
"buffer_load_dwordx4 v214, %[srdk], s48 offen offset:3072 lds\n" \
"v_mfma_f32_16x16x32_bf16 v[92:95], v[188:191], a[184:187], v[92:95]\n" \
"v_mfma_f32_16x16x32_bf16 v[124:127], v[184:187], a[172:175], v[124:127]\n" \
"v_mfma_f32_16x16x32_bf16 v[124:127], v[188:191], a[188:191], v[124:127]\n" \
"s_waitcnt vmcnt(25) lgkmcnt(0)\n" \
"v_mfma_f32_16x16x32_bf16 v[0:3], v[128:131], a[192:195], v[0:3]\n" \
"ds_read_b64_tr_b16 v[160:161], v225 offset:33792\n" \
"v_mfma_f32_16x16x32_bf16 v[0:3], v[132:135], a[208:211], v[0:3]\n" \
"ds_read_b64_tr_b16 v[162:163], v225 offset:34048\n" \
"v_mfma_f32_16x16x32_bf16 v[32:35], v[128:131], a[196:199], v[32:35]\n" \
"s_mov_b32 s48, 32768\n" \
"s_mov_b32 m0, s45\n" \
"s_nop 0\n" \
"buffer_load_dwordx4 %[vd0], %[srdv], s48 offen offset:2048 lds\n" \
"v_mfma_f32_16x16x32_bf16 v[32:35], v[132:135], a[212:215], v[32:35]\n" \
"ds_read_b64_tr_b16 v[164:165], v225 offset:41984\n" \
"v_mfma_f32_16x16x32_bf16 v[64:67], v[128:131], a[200:203], v[64:67]\n" \
"ds_read_b64_tr_b16 v[166:167], v225 offset:42240\n" \
"v_mfma_f32_16x16x32_bf16 v[64:67], v[132:135], a[216:219], v[64:67]\n" \
"v_mfma_f32_16x16x32_bf16 v[96:99], v[128:131], a[204:207], v[96:99]\n" \
"ds_read_b64_tr_b16 v[168:169], v225 offset:33800\n" \
"v_mfma_f32_16x16x32_bf16 v[96:99], v[132:135], a[220:223], v[96:99]\n" \
"ds_read_b64_tr_b16 v[170:171], v225 offset:34056\n" \
"v_mfma_f32_16x16x32_bf16 v[4:7], v[136:139], a[192:195], v[4:7]\n" \
"buffer_load_dwordx4 v215, %[srdv], s48 offen offset:3072 lds\n" \
"v_mfma_f32_16x16x32_bf16 v[4:7], v[140:143], a[208:211], v[4:7]\n" \
"ds_read_b64_tr_b16 v[172:173], v225 offset:41992\n" \
"v_mfma_f32_16x16x32_bf16 v[36:39], v[136:139], a[196:199], v[36:39]\n" \
"ds_read_b64_tr_b16 v[174:175], v225 offset:42248\n" \
"v_mfma_f32_16x16x32_bf16 v[36:39], v[140:143], a[212:215], v[36:39]\n" \
"v_mfma_f32_16x16x32_bf16 v[68:71], v[136:139], a[200:203], v[68:71]\n" \
"ds_read_b64_tr_b16 v[176:177], v225 offset:34304\n" \
"v_mfma_f32_16x16x32_bf16 v[68:71], v[140:143], a[216:219], v[68:71]\n" \
"ds_read_b64_tr_b16 v[178:179], v225 offset:34560\n" \
"v_mfma_f32_16x16x32_bf16 v[100:103], v[136:139], a[204:207], v[100:103]\n" \
"s_mov_b32 s48, 32768\n" \
"s_mov_b32 m0, s42\n" \
"s_nop 0\n" \
"buffer_load_dwordx4 %[kd0], %[srdk], s48 offen offset:0 lds\n" \
"v_mfma_f32_16x16x32_bf16 v[100:103], v[140:143], a[220:223], v[100:103]\n" \
"ds_read_b64_tr_b16 v[180:181], v225 offset:42496\n" \
"v_mfma_f32_16x16x32_bf16 v[8:11], v[144:147], a[192:195], v[8:11]\n" \
"ds_read_b64_tr_b16 v[182:183], v225 offset:42752\n" \
"v_mfma_f32_16x16x32_bf16 v[8:11], v[148:151], a[208:211], v[8:11]\n" \
"v_mfma_f32_16x16x32_bf16 v[40:43], v[144:147], a[196:199], v[40:43]\n" \
"ds_read_b64_tr_b16 v[184:185], v225 offset:34312\n" \
"v_mfma_f32_16x16x32_bf16 v[40:43], v[148:151], a[212:215], v[40:43]\n" \
"ds_read_b64_tr_b16 v[186:187], v225 offset:34568\n" \
"v_mfma_f32_16x16x32_bf16 v[72:75], v[144:147], a[200:203], v[72:75]\n" \
"buffer_load_dwordx4 v212, %[srdk], s48 offen offset:1024 lds\n" \
"v_mfma_f32_16x16x32_bf16 v[72:75], v[148:151], a[216:219], v[72:75]\n" \
"ds_read_b64_tr_b16 v[188:189], v225 offset:42504\n" \
"v_mfma_f32_16x16x32_bf16 v[104:107], v[144:147], a[204:207], v[104:107]\n" \
"ds_read_b64_tr_b16 v[190:191], v225 offset:42760\n" \
"v_mfma_f32_16x16x32_bf16 v[104:107], v[148:151], a[220:223], v[104:107]\n" \
"v_mfma_f32_16x16x32_bf16 v[12:15], v[152:155], a[192:195], v[12:15]\n" \
"buffer_load_dwordx4 v213, %[srdk], s48 offen offset:2048 lds\n" \
"v_mfma_f32_16x16x32_bf16 v[12:15], v[156:159], a[208:211], v[12:15]\n" \
"v_mfma_f32_16x16x32_bf16 v[44:47], v[152:155], a[196:199], v[44:47]\n" \
"v_mfma_f32_16x16x32_bf16 v[44:47], v[156:159], a[212:215], v[44:47]\n" \
"v_mfma_f32_16x16x32_bf16 v[76:79], v[152:155], a[200:203], v[76:79]\n" \
"buffer_load_dwordx4 v214, %[srdk], s48 offen offset:3072 lds\n" \
"v_mfma_f32_16x16x32_bf16 v[76:79], v[156:159], a[216:219], v[76:79]\n" \
"v_mfma_f32_16x16x32_bf16 v[108:111], v[152:155], a[204:207], v[108:111]\n" \
"v_mfma_f32_16x16x32_bf16 v[108:111], v[156:159], a[220:223], v[108:111]\n" \
"s_waitcnt vmcnt(29) lgkmcnt(0)\n" \
"s_barrier\n" \
"v_mfma_f32_16x16x32_bf16 v[16:19], v[160:163], a[192:195], v[16:19]\n" \
"ds_read_b64_tr_b16 v[128:129], v225 offset:49152\n" \
"s_mov_b32 s54, 0x4000\n" \
"v_mfma_f32_16x16x32_bf16 v[16:19], v[164:167], a[208:211], v[16:19]\n" \
"ds_read_b64_tr_b16 v[130:131], v225 offset:49408\n" \
"v_mfma_f32_16x16x32_bf16 v[48:51], v[160:163], a[196:199], v[48:51]\n" \
"s_mov_b32 s48, 49152\n" \
"s_mov_b32 m0, s46\n" \
"s_nop 0\n" \
"buffer_load_dwordx4 %[vd0], %[srdv], s48 offen offset:0 lds\n" \
"v_mbcnt_lo_u32_b32 v231, -1, 0\n" \
"v_mfma_f32_16x16x32_bf16 v[48:51], v[164:167], a[212:215], v[48:51]\n" \
"ds_read_b64_tr_b16 v[132:133], v225 offset:57344\n" \
"v_mfma_f32_16x16x32_bf16 v[80:83], v[160:163], a[200:203], v[80:83]\n" \
"ds_read_b64_tr_b16 v[134:135], v225 offset:57600\n" \
"v_mfma_f32_16x16x32_bf16 v[80:83], v[164:167], a[216:219], v[80:83]\n" \
"v_mbcnt_hi_u32_b32 v231, -1, v231\n" \
"v_mfma_f32_16x16x32_bf16 v[112:115], v[160:163], a[204:207], v[112:115]\n" \
"ds_read_b64_tr_b16 v[136:137], v225 offset:49160\n" \
"v_mfma_f32_16x16x32_bf16 v[112:115], v[164:167], a[220:223], v[112:115]\n" \
"ds_read_b64_tr_b16 v[138:139], v225 offset:49416\n" \
"v_mfma_f32_16x16x32_bf16 v[20:23], v[168:171], a[192:195], v[20:23]\n" \
"buffer_load_dwordx4 v215, %[srdv], s48 offen offset:1024 lds\n" \
"v_lshrrev_b32_e64 v232, 4, v231\n" \
"v_mfma_f32_16x16x32_bf16 v[20:23], v[172:175], a[208:211], v[20:23]\n" \
"ds_read_b64_tr_b16 v[140:141], v225 offset:57352\n" \
"v_mfma_f32_16x16x32_bf16 v[52:55], v[168:171], a[196:199], v[52:55]\n" \
"ds_read_b64_tr_b16 v[142:143], v225 offset:57608\n" \
"v_xor_b32_e64 v232, v232, v231\n" \
"v_mfma_f32_16x16x32_bf16 v[52:55], v[172:175], a[212:215], v[52:55]\n" \
"v_mfma_f32_16x16x32_bf16 v[84:87], v[168:171], a[200:203], v[84:87]\n" \
"ds_read_b64_tr_b16 v[144:145], v225 offset:49664\n" \
"v_mfma_f32_16x16x32_bf16 v[84:87], v[172:175], a[216:219], v[84:87]\n" \
"ds_read_b64_tr_b16 v[146:147], v225 offset:49920\n" \
"v_bfe_i32 v232, v232, 1, 1\n" \
"v_mfma_f32_16x16x32_bf16 v[116:119], v[168:171], a[204:207], v[116:119]\n" \
"s_mov_b32 s48, 49152\n" \
"s_mov_b32 m0, s43\n" \
"s_nop 0\n" \
"buffer_load_dwordx4 %[kd0], %[srdk], s48 offen offset:0 lds\n" \
"v_mfma_f32_16x16x32_bf16 v[116:119], v[172:175], a[220:223], v[116:119]\n" \
"ds_read_b64_tr_b16 v[148:149], v225 offset:57856\n" \
"v_mfma_f32_16x16x32_bf16 v[24:27], v[176:179], a[192:195], v[24:27]\n" \
"ds_read_b64_tr_b16 v[150:151], v225 offset:58112\n" \
"v_not_b32_e64 v232, v232\n" \
"v_mfma_f32_16x16x32_bf16 v[24:27], v[180:183], a[208:211], v[24:27]\n" \
"v_mfma_f32_16x16x32_bf16 v[56:59], v[176:179], a[196:199], v[56:59]\n" \
"ds_read_b64_tr_b16 v[152:153], v225 offset:49672\n" \
"v_mfma_f32_16x16x32_bf16 v[56:59], v[180:183], a[212:215], v[56:59]\n" \
"ds_read_b64_tr_b16 v[154:155], v225 offset:49928\n" \
"v_and_b32_e32 v230, 0x3f803f80, v232\n" \
"v_mfma_f32_16x16x32_bf16 v[88:91], v[176:179], a[200:203], v[88:91]\n" \
"buffer_load_dwordx4 v212, %[srdk], s48 offen offset:1024 lds\n" \
"v_mfma_f32_16x16x32_bf16 v[88:91], v[180:183], a[216:219], v[88:91]\n" \
"ds_read_b64_tr_b16 v[156:157], v225 offset:57864\n" \
"v_readlane_b32 s62, %[tlo], 18\n" \
"v_mfma_f32_16x16x32_bf16 v[120:123], v[176:179], a[204:207], v[120:123]\n" \
"ds_read_b64_tr_b16 v[158:159], v225 offset:58120\n" \
"v_mfma_f32_16x16x32_bf16 v[120:123], v[180:183], a[220:223], v[120:123]\n" \
"v_mfma_f32_16x16x32_bf16 v[28:31], v[184:187], a[192:195], v[28:31]\n" \
"buffer_load_dwordx4 v213, %[srdk], s48 offen offset:2048 lds\n" \
"v_readlane_b32 s61, %[tlo], 21\n" \
"v_mfma_f32_16x16x32_bf16 v[28:31], v[188:191], a[208:211], v[28:31]\n" \
"v_mfma_f32_16x16x32_bf16 v[60:63], v[184:187], a[196:199], v[60:63]\n" \
"v_mfma_f32_16x16x32_bf16 v[60:63], v[188:191], a[212:215], v[60:63]\n" \
"v_lshlrev_b32_e64 v224, 7, v231\n" \
"v_mfma_f32_16x16x32_bf16 v[92:95], v[184:187], a[200:203], v[92:95]\n" \
"buffer_load_dwordx4 v214, %[srdk], s48 offen offset:3072 lds\n" \
"v_mfma_f32_16x16x32_bf16 v[92:95], v[188:191], a[216:219], v[92:95]\n" \
"v_mfma_f32_16x16x32_bf16 v[124:127], v[184:187], a[204:207], v[124:127]\n" \
"v_mfma_f32_16x16x32_bf16 v[124:127], v[188:191], a[220:223], v[124:127]\n" \
"s_waitcnt vmcnt(24) lgkmcnt(0)\n" \
"v_mfma_f32_16x16x32_bf16 v[0:3], v[128:131], a[224:227], v[0:3]\n" \
"ds_read_b64_tr_b16 v[160:161], v225 offset:50176\n" \
"v_accvgpr_write_b32 a192, v230\n" \
"v_mfma_f32_16x16x32_bf16 v[0:3], v[132:135], a[240:243], v[0:3]\n" \
"ds_read_b64_tr_b16 v[162:163], v225 offset:50432\n" \
"v_mfma_f32_16x16x32_bf16 v[32:35], v[128:131], a[228:231], v[32:35]\n" \
"s_mov_b32 s48, 49152\n" \
"s_mov_b32 m0, s46\n" \
"s_nop 0\n" \
"buffer_load_dwordx4 %[vd0], %[srdv], s48 offen offset:2048 lds\n" \
"v_accvgpr_write_b32 a193, v230\n" \
"v_mfma_f32_16x16x32_bf16 v[32:35], v[132:135], a[244:247], v[32:35]\n" \
"ds_read_b64_tr_b16 v[164:165], v225 offset:58368\n" \
"v_mfma_f32_16x16x32_bf16 v[64:67], v[128:131], a[232:235], v[64:67]\n" \
"ds_read_b64_tr_b16 v[166:167], v225 offset:58624\n" \
"v_accvgpr_write_b32 a194, v230\n" \
"v_mfma_f32_16x16x32_bf16 v[64:67], v[132:135], a[248:251], v[64:67]\n" \
"v_mfma_f32_16x16x32_bf16 v[96:99], v[128:131], a[236:239], v[96:99]\n" \
"ds_read_b64_tr_b16 v[168:169], v225 offset:50184\n" \
"v_accvgpr_write_b32 a195, v230\n" \
"v_mfma_f32_16x16x32_bf16 v[96:99], v[132:135], a[252:255], v[96:99]\n" \
"ds_read_b64_tr_b16 v[170:171], v225 offset:50440\n" \
"v_mfma_f32_16x16x32_bf16 v[4:7], v[136:139], a[224:227], v[4:7]\n" \
"buffer_load_dwordx4 v215, %[srdv], s48 offen offset:3072 lds\n" \
"v_accvgpr_write_b32 a196, -1\n" \
"v_mfma_f32_16x16x32_bf16 v[4:7], v[140:143], a[240:243], v[4:7]\n" \
"ds_read_b64_tr_b16 v[172:173], v225 offset:58376\n" \
"v_mfma_f32_16x16x32_bf16 v[36:39], v[136:139], a[228:231], v[36:39]\n" \
"ds_read_b64_tr_b16 v[174:175], v225 offset:58632\n" \
"v_accvgpr_write_b32 a197, -1\n" \
"v_mfma_f32_16x16x32_bf16 v[36:39], v[140:143], a[244:247], v[36:39]\n" \
"v_mfma_f32_16x16x32_bf16 v[68:71], v[136:139], a[232:235], v[68:71]\n" \
"ds_read_b64_tr_b16 v[176:177], v225 offset:50688\n" \
"v_accvgpr_write_b32 a198, -1\n" \
"v_mfma_f32_16x16x32_bf16 v[68:71], v[140:143], a[248:251], v[68:71]\n" \
"ds_read_b64_tr_b16 v[178:179], v225 offset:50944\n" \
"v_mfma_f32_16x16x32_bf16 v[100:103], v[136:139], a[236:239], v[100:103]\n" \
"v_accvgpr_write_b32 a199, -1\n" \
"v_mfma_f32_16x16x32_bf16 v[100:103], v[140:143], a[252:255], v[100:103]\n" \
"ds_read_b64_tr_b16 v[180:181], v225 offset:58880\n" \
"v_mfma_f32_16x16x32_bf16 v[8:11], v[144:147], a[224:227], v[8:11]\n" \
"ds_read_b64_tr_b16 v[182:183], v225 offset:59136\n" \
"v_lshrrev_b32_e32 v231, 3, v224\n" \
"v_mfma_f32_16x16x32_bf16 v[8:11], v[148:151], a[240:243], v[8:11]\n" \
"v_mfma_f32_16x16x32_bf16 v[40:43], v[144:147], a[228:231], v[40:43]\n" \
"ds_read_b64_tr_b16 v[184:185], v225 offset:50696\n" \
"v_mov_b32_e64 v232, -1\n" \
"v_mfma_f32_16x16x32_bf16 v[40:43], v[148:151], a[244:247], v[40:43]\n" \
"ds_read_b64_tr_b16 v[186:187], v225 offset:50952\n" \
"v_mfma_f32_16x16x32_bf16 v[72:75], v[144:147], a[232:235], v[72:75]\n" \
"v_mov_b32_e64 v233, -1\n" \
"v_mfma_f32_16x16x32_bf16 v[72:75], v[148:151], a[248:251], v[72:75]\n" \
"ds_read_b64_tr_b16 v[188:189], v225 offset:58888\n" \
"v_mfma_f32_16x16x32_bf16 v[104:107], v[144:147], a[236:239], v[104:107]\n" \
"ds_read_b64_tr_b16 v[190:191], v225 offset:59144\n" \
"v_mov_b32_e64 v234, -1\n" \
"v_mfma_f32_16x16x32_bf16 v[104:107], v[148:151], a[252:255], v[104:107]\n" \
"v_mfma_f32_16x16x32_bf16 v[12:15], v[152:155], a[224:227], v[12:15]\n" \
"v_mov_b32_e64 v235, -1\n" \
"v_mfma_f32_16x16x32_bf16 v[12:15], v[156:159], a[240:243], v[12:15]\n" \
"v_mfma_f32_16x16x32_bf16 v[44:47], v[152:155], a[228:231], v[44:47]\n" \
"buffer_store_dwordx4 v[232:235], v231, %[srdz], s61 offen offset:0 sc1\n" \
"v_mfma_f32_16x16x32_bf16 v[44:47], v[156:159], a[244:247], v[44:47]\n" \
"v_mfma_f32_16x16x32_bf16 v[76:79], v[152:155], a[232:235], v[76:79]\n" \
"buffer_store_dwordx4 v[232:235], v231, %[srdz], s61 offen offset:1024 sc1\n" \
"v_mfma_f32_16x16x32_bf16 v[76:79], v[156:159], a[248:251], v[76:79]\n" \
"v_mfma_f32_16x16x32_bf16 v[108:111], v[152:155], a[236:239], v[108:111]\n" \
"v_mfma_f32_16x16x32_bf16 v[108:111], v[156:159], a[252:255], v[108:111]\n" \
"s_waitcnt vmcnt(22) lgkmcnt(0)\n" \
"s_barrier\n" \
"v_mfma_f32_16x16x32_bf16 v[16:19], v[160:163], a[224:227], v[16:19]\n" \
"ds_read_b128 a[64:67], %[kb0] offset:0\n" \
"v_mfma_f32_16x16x32_bf16 v[16:19], v[164:167], a[240:243], v[16:19]\n" \
"v_mfma_f32_16x16x32_bf16 v[20:23], v[168:171], a[224:227], v[20:23]\n" \
"v_cvt_pk_bf16_f32 v230, v0, v1\n" \
"v_cvt_pk_bf16_f32 v231, v2, v3\n" \
"v_cvt_pk_bf16_f32 v232, v4, v5\n" \
"ds_read_b128 a[68:71], v208 offset:0\n" \
"v_mfma_f32_16x16x32_bf16 v[20:23], v[172:175], a[240:243], v[20:23]\n" \
"v_cvt_pk_bf16_f32 v233, v6, v7\n" \
"v_accvgpr_write_b32 a0, v230\n" \
".if . - adaln_body != 8192\n" \
".error \"size model\"\n" \
".endif\n" \
"Lk1_%=:\n" \
"s_cbranch_execz Lk2_%=\n" \
"s_nop 0\n" \
"v_accvgpr_write_b32 a1, v231\n" \
"v_mfma_f32_16x16x32_bf16 v[24:27], v[176:179], a[224:227], v[24:27]\n" \
"v_accvgpr_write_b32 a2, v232\n" \
"v_accvgpr_write_b32 a3, v233\n" \
"v_cvt_pk_bf16_f32 v234, v8, v9\n" \
"ds_read_b128 a[72:75], v209 offset:0\n" \
"v_mfma_f32_16x16x32_bf16 v[24:27], v[180:183], a[240:243], v[24:27]\n" \
"v_cvt_pk_bf16_f32 v235, v10, v11\n" \
"v_cvt_pk_bf16_f32 v236, v12, v13\n" \
"v_cvt_pk_bf16_f32 v237, v14, v15\n" \
"v_mfma_f32_16x16x32_bf16 v[28:31], v[184:187], a[224:227], v[28:31]\n" \
"v_accvgpr_write_b32 a4, v234\n" \
"v_accvgpr_write_b32 a5, v235\n" \
"v_accvgpr_write_b32 a6, v236\n" \
"ds_read_b128 a[76:79], v210 offset:0\n" \
"v_mfma_f32_16x16x32_bf16 v[28:31], v[188:191], a[240:243], v[28:31]\n" \
"v_accvgpr_write_b32 a7, v237\n" \
"v_cvt_pk_bf16_f32 v230, v32, v33\n" \
"v_cvt_pk_bf16_f32 v231, v34, v35\n" \
"v_mfma_f32_16x16x32_bf16 v[48:51], v[160:163], a[228:231], v[48:51]\n" \
"v_cvt_pk_bf16_f32 v232, v36, v37\n" \
"v_cvt_pk_bf16_f32 v233, v38, v39\n" \
"v_accvgpr_write_b32 a16, v230\n" \
"ds_read_b128 a[92:95], v210 offset:4096\n" \
"v_mfma_f32_16x16x32_bf16 v[48:51], v[164:167], a[244:247], v[48:51]\n" \
"v_accvgpr_write_b32 a17, v231\n" \
"v_accvgpr_write_b32 a18, v232\n" \
"v_accvgpr_write_b32 a19, v233\n" \
"v_mfma_f32_16x16x32_bf16 v[52:55], v[168:171], a[228:231], v[52:55]\n" \
"v_cvt_pk_bf16_f32 v234, v40, v41\n" \
"v_cvt_pk_bf16_f32 v235, v42, v43\n" \
"v_cvt_pk_bf16_f32 v236, v44, v45\n" \
"ds_read_b128 a[88:91], v209 offset:4096\n" \
"v_mfma_f32_16x16x32_bf16 v[52:55], v[172:175], a[244:247], v[52:55]\n" \
"v_cvt_pk_bf16_f32 v237, v46, v47\n" \
"v_accvgpr_write_b32 a20, v234\n" \
"v_accvgpr_write_b32 a21, v235\n" \
"v_mfma_f32_16x16x32_bf16 v[56:59], v[176:179], a[228:231], v[56:59]\n" \
"v_accvgpr_write_b32 a22, v236\n" \
"v_accvgpr_write_b32 a23, v237\n" \
"v_cvt_pk_bf16_f32 v230, v16, v17\n" \
"ds_read_b128 a[84:87], v208 offset:4096\n" \
"v_mfma_f32_16x16x32_bf16 v[56:59], v[180:183], a[244:247], v[56:59]\n" \
"v_cvt_pk_bf16_f32 v231, v18, v19\n" \
"v_cvt_pk_bf16_f32 v232, v20, v21\n" \
"v_cvt_pk_bf16_f32 v233, v22, v23\n" \
"v_mfma_f32_16x16x32_bf16 v[60:63], v[184:187], a[228:231], v[60:63]\n" \
"v_accvgpr_write_b32 a8, v230\n" \
"v_accvgpr_write_b32 a9, v231\n" \
"v_accvgpr_write_b32 a10, v232\n" \
"ds_read_b128 a[80:83], %[kb0] offset:4096\n" \
"v_mfma_f32_16x16x32_bf16 v[60:63], v[188:191], a[244:247], v[60:63]\n" \
"v_accvgpr_write_b32 a11, v233\n" \
"v_cvt_pk_bf16_f32 v234, v24, v25\n" \
"v_cvt_pk_bf16_f32 v235, v26, v27\n" \
"v_mfma_f32_16x16x32_bf16 v[80:83], v[160:163], a[232:235], v[80:83]\n" \
"v_cvt_pk_bf16_f32 v236, v28, v29\n" \
"v_cvt_pk_bf16_f32 v237, v30, v31\n" \
"v_accvgpr_write_b32 a12, v234\n" \
"v_mfma_f32_16x16x32_bf16 v[80:83], v[164:167], a[248:251], v[80:83]\n" \
"v_accvgpr_write_b32 a13, v235\n" \
"v_accvgpr_write_b32 a14, v236\n" \
"v_accvgpr_write_b32 a15, v237\n" \
"v_mfma_f32_16x16x32_bf16 v[84:87], v[168:171], a[232:235], v[84:87]\n" \
"v_cvt_pk_bf16_f32 v230, v64, v65\n" \
"v_cvt_pk_bf16_f32 v231, v66, v67\n" \
"v_cvt_pk_bf16_f32 v232, v68, v69\n" \
"v_mfma_f32_16x16x32_bf16 v[84:87], v[172:175], a[248:251], v[84:87]\n" \
"v_cvt_pk_bf16_f32 v233, v70, v71\n" \
"v_accvgpr_write_b32 a32, v230\n" \
"v_accvgpr_write_b32 a33, v231\n" \
"v_mfma_f32_16x16x32_bf16 v[88:91], v[176:179], a[232:235], v[88:91]\n" \
"v_accvgpr_write_b32 a34, v232\n" \
"v_accvgpr_write_b32 a35, v233\n" \
"v_cvt_pk_bf16_f32 v234, v72, v73\n" \
"v_mfma_f32_16x16x32_bf16 v[88:91], v[180:183], a[248:251], v[88:91]\n" \
"v_cvt_pk_bf16_f32 v235, v74, v75\n" \
"v_cvt_pk_bf16_f32 v236, v76, v77\n" \
"v_cvt_pk_bf16_f32 v237, v78, v79\n" \
"v_mfma_f32_16x16x32_bf16 v[92:95], v[184:187], a[232:235], v[92:95]\n" \
"v_accvgpr_write_b32 a36, v234\n" \
"v_accvgpr_write_b32 a37, v235\n" \
"v_accvgpr_write_b32 a38, v236\n" \
"v_mfma_f32_16x16x32_bf16 v[92:95], v[188:191], a[248:251], v[92:95]\n" \
"v_accvgpr_write_b32 a39, v237\n" \
"v_cvt_pk_bf16_f32 v230, v48, v49\n" \
"v_cvt_pk_bf16_f32 v231, v50, v51\n" \
"v_mfma_f32_16x16x32_bf16 v[112:115], v[160:163], a[236:239], v[112:115]\n" \
"v_cvt_pk_bf16_f32 v232, v52, v53\n" \
"v_cvt_pk_bf16_f32 v233, v54, v55\n" \
"v_accvgpr_write_b32 a24, v230\n" \
"v_mfma_f32_16x16x32_bf16 v[112:115], v[164:167], a[252:255], v[112:115]\n" \
"v_accvgpr_write_b32 a25, v231\n" \
"v_accvgpr_write_b32 a26, v232\n" \
"v_accvgpr_write_b32 a27, v233\n" \
"v_mfma_f32_16x16x32_bf16 v[116:119], v[168:171], a[236:239], v[116:119]\n" \
"v_cvt_pk_bf16_f32 v234, v56, v57\n" \
"v_cvt_pk_bf16_f32 v235, v58, v59\n" \
"v_cvt_pk_bf16_f32 v236, v60, v61\n" \
"v_mfma_f32_16x16x32_bf16 v[116:119], v[172:175], a[252:255], v[116:119]\n" \
"v_cvt_pk_bf16_f32 v237, v62, v63\n" \
"v_accvgpr_write_b32 a28, v234\n" \
"v_accvgpr_write_b32 a29, v235\n" \
"v_mfma_f32_16x16x32_bf16 v[120:123], v[176:179], a[236:239], v[120:123]\n" \
"v_accvgpr_write_b32 a30, v236\n" \
"v_accvgpr_write_b32 a31, v237\n" \
"v_cvt_pk_bf16_f32 v230, v96, v97\n" \
"v_mfma_f32_16x16x32_bf16 v[120:123], v[180:183], a[252:255], v[120:123]\n" \
"v_cvt_pk_bf16_f32 v231, v98, v99\n" \
"v_cvt_pk_bf16_f32 v232, v100, v101\n" \
"v_cvt_pk_bf16_f32 v233, v102, v103\n" \
"v_mfma_f32_16x16x32_bf16 v[124:127], v[184:187], a[236:239], v[124:127]\n" \
"v_accvgpr_write_b32 a48, v230\n" \
"v_accvgpr_write_b32 a49, v231\n" \
"v_accvgpr_write_b32 a50, v232\n" \
"v_mfma_f32_16x16x32_bf16 v[124:127], v[188:191], a[252:255], v[124:127]\n" \
"v_accvgpr_write_b32 a51, v233\n" \
"s_waitcnt vmcnt(63) lgkmcnt(0)\n" \
"s_nop 0\n" \
"s_mov_b32 s48, 65536\n" \
"s_mov_b32 s60, 0\n" \
"s_waitcnt lgkmcnt(0)\n" \
"v_mfma_f32_16x16x32_bf16 v[128:131], a[64:67], a[0:3], 0\n" \
"ds_read_b128 a[96:99], %[kb0] offset:8192\n" \
"v_cvt_pk_bf16_f32 v234, v80, v81\n" \
"v_cvt_pk_bf16_f32 v235, v82, v83\n" \
"v_mfma_f32_16x16x32_bf16 v[128:131], a[68:71], a[4:7], v[128:131]\n" \
"ds_read_b128 a[100:103], v208 offset:8192\n" \
"v_cvt_pk_bf16_f32 v236, v84, v85\n" \
"v_cvt_pk_bf16_f32 v237, v86, v87\n" \
"v_mfma_f32_16x16x32_bf16 v[128:131], a[72:75], a[8:11], v[128:131]\n" \
"ds_read_b128 a[104:107], v209 offset:8192\n" \
"v_accvgpr_write_b32 a40, v234\n" \
"v_accvgpr_write_b32 a41, v235\n" \
"v_mfma_f32_16x16x32_bf16 v[128:131], a[76:79], a[12:15], v[128:131]\n" \
"ds_read_b128 a[108:111], v210 offset:8192\n" \
"v_accvgpr_write_b32 a42, v236\n" \
"v_accvgpr_write_b32 a43, v237\n" \
"v_mfma_f32_16x16x32_bf16 v[132:135], a[92:95], a[12:15], 0\n" \
"ds_read_b128 a[124:127], v210 offset:12288\n" \
"v_cvt_pk_bf16_f32 v230, v88, v89\n" \
"v_cvt_pk_bf16_f32 v231, v90, v91\n" \
"v_mfma_f32_16x16x32_bf16 v[132:135], a[88:91], a[8:11], v[132:135]\n" \
"ds_read_b128 a[120:123], v209 offset:12288\n" \
"v_cvt_pk_bf16_f32 v232, v92, v93\n" \
"v_cvt_pk_bf16_f32 v233, v94, v95\n" \
"v_mfma_f32_16x16x32_bf16 v[132:135], a[84:87], a[4:7], v[132:135]\n" \
"ds_read_b128 a[116:119], v208 offset:12288\n" \
"v_accvgpr_write_b32 a44, v230\n" \
"v_accvgpr_write_b32 a45, v231\n" \
"v_mfma_f32_16x16x32_bf16 v[132:135], a[80:83], a[0:3], v[132:135]\n" \
"ds_read_b128 a[112:115], %[kb0] offset:12288\n" \
"v_accvgpr_write_b32 a46, v232\n" \
"v_accvgpr_write_b32 a47, v233\n" \
"v_mfma_f32_16x16x32_bf16 v[136:139], a[64:67], a[16:19], 0\n" \
"ds_read_b64_tr_b16 a[128:129], %[vvb] offset:0\n" \
"v_cvt_pk_bf16_f32 v234, v104, v105\n" \
"v_cvt_pk_bf16_f32 v235, v106, v107\n" \
"v_mfma_f32_16x16x32_bf16 v[136:139], a[68:71], a[20:23], v[136:139]\n" \
"ds_read_b64_tr_b16 a[130:131], %[vvb] offset:4096\n" \
"v_cvt_pk_bf16_f32 v236, v108, v109\n" \
"v_cvt_pk_bf16_f32 v237, v110, v111\n" \
"v_mfma_f32_16x16x32_bf16 v[136:139], a[72:75], a[24:27], v[136:139]\n" \
"ds_read_b64_tr_b16 a[132:133], v211 offset:0\n" \
"v_accvgpr_write_b32 a52, v234\n" \
"v_accvgpr_write_b32 a53, v235\n" \
"v_mfma_f32_16x16x32_bf16 v[136:139], a[76:79], a[28:31], v[136:139]\n" \
"ds_read_b64_tr_b16 a[134:135], v211 offset:4096\n" \
"v_accvgpr_write_b32 a54, v236\n" \
"v_accvgpr_write_b32 a55, v237\n" \
"v_mfma_f32_16x16x32_bf16 v[140:143], a[92:95], a[28:31], 0\n" \
"ds_read_b64_tr_b16 a[136:137], %[vvb] offset:512\n" \
"v_cvt_pk_bf16_f32 v230, v112, v113\n" \
"v_cvt_pk_bf16_f32 v231, v114, v115\n" \
"v_mfma_f32_16x16x32_bf16 v[140:143], a[88:91], a[24:27], v[140:143]\n" \
"ds_read_b64_tr_b16 a[138:139], %[vvb] offset:4608\n" \
"v_cvt_pk_bf16_f32 v232, v116, v117\n" \
"v_cvt_pk_bf16_f32 v233, v118, v119\n" \
"v_mfma_f32_16x16x32_bf16 v[140:143], a[84:87], a[20:23], v[140:143]\n" \
"ds_read_b64_tr_b16 a[140:141], v211 offset:512\n" \
"v_accvgpr_write_b32 a56, v230\n" \
"v_accvgpr_write_b32 a57, v231\n" \
"v_mfma_f32_16x16x32_bf16 v[140:143], a[80:83], a[16:19], v[140:143]\n" \
"ds_read_b64_tr_b16 a[142:143], v211 offset:4608\n" \
"v_accvgpr_write_b32 a58, v232\n" \
"v_accvgpr_write_b32 a59, v233\n" \
"v_mfma_f32_16x16x32_bf16 v[144:147], a[64:67], a[32:35], 0\n" \
"ds_read_b64_tr_b16 a[144:145], %[vvb] offset:1024\n" \
"v_cvt_pk_bf16_f32 v234, v120, v121\n" \
"v_cvt_pk_bf16_f32 v235, v122, v123\n" \
"v_mfma_f32_16x16x32_bf16 v[144:147], a[68:71], a[36:39], v[144:147]\n" \
"ds_read_b64_tr_b16 a[146:147], %[vvb] offset:5120\n" \
"v_cvt_pk_bf16_f32 v236, v124, v125\n" \
"v_cvt_pk_bf16_f32 v237, v126, v127\n" \
"v_mfma_f32_16x16x32_bf16 v[144:147], a[72:75], a[40:43], v[144:147]\n" \
"ds_read_b64_tr_b16 a[148:149], v211 offset:1024\n" \
"v_accvgpr_write_b32 a60, v234\n" \
"v_accvgpr_write_b32 a61, v235\n" \
"v_mfma_f32_16x16x32_bf16 v[144:147], a[76:79], a[44:47], v[144:147]\n" \
"ds_read_b64_tr_b16 a[150:151], v211 offset:5120\n" \
"v_accvgpr_write_b32 a62, v236\n" \
"v_accvgpr_write_b32 a63, v237\n" \
"v_mfma_f32_16x16x32_bf16 v[148:151], a[92:95], a[44:47], 0\n" \
"ds_read_b64_tr_b16 a[152:153], %[vvb] offset:1536\n" \
"v_max3_f32 v192, v128, v129, v130\n" \
"v_max3_f32 v193, v131, v132, v133\n" \
"v_mfma_f32_16x16x32_bf16 v[148:151], a[88:91], a[40:43], v[148:151]\n" \
"ds_read_b64_tr_b16 a[154:155], %[vvb] offset:5632\n" \
"v_max3_f32 v192, v192, v134, v135\n" \
"v_max_f32_e64 v225, v192, v193\n" \
"v_mfma_f32_16x16x32_bf16 v[148:151], a[84:87], a[36:39], v[148:151]\n" \
"ds_read_b64_tr_b16 a[156:157], v211 offset:1536\n" \
"v_max3_f32 v192, v136, v137, v138\n" \
"v_max3_f32 v193, v139, v140, v141\n" \
"v_mfma_f32_16x16x32_bf16 v[148:151], a[80:83], a[32:35], v[148:151]\n" \
"ds_read_b64_tr_b16 a[158:159], v211 offset:5632\n" \
"v_max3_f32 v192, v192, v142, v143\n" \
"v_max_f32_e64 v226, v192, v193\n" \
"v_mfma_f32_16x16x32_bf16 v[152:155], a[64:67], a[48:51], 0\n" \
"ds_read_b128 a[200:203], v228 offset:0\n" \
"s_mov_b32 s44, %[m0t]\n" \
"s_mov_b32 s45, 0\n" \
"v_mfma_f32_16x16x32_bf16 v[152:155], a[68:71], a[52:55], v[152:155]\n" \
"ds_read_b128 a[204:207], v228 offset:64\n" \
"v_mfma_f32_16x16x32_bf16 v[152:155], a[72:75], a[56:59], v[152:155]\n" \
"ds_read_b128 a[208:211], v228 offset:128\n" \
"v_mfma_f32_16x16x32_bf16 v[152:155], a[76:79], a[60:63], v[152:155]\n" \
"ds_read_b128 a[212:215], v228 offset:192\n" \
"v_mfma_f32_16x16x32_bf16 v[156:159], a[92:95], a[60:63], 0\n" \
"ds_read_b128 a[216:219], v228 offset:256\n" \
"v_mfma_f32_16x16x32_bf16 v[156:159], a[88:91], a[56:59], v[156:159]\n" \
"ds_read_b128 a[220:223], v228 offset:320\n" \
"v_mfma_f32_16x16x32_bf16 v[156:159], a[84:87], a[52:55], v[156:159]\n" \
"ds_read_b128 a[224:227], v228 offset:384\n" \
"v_max3_f32 v192, v144, v145, v146\n" \
"v_max3_f32 v193, v147, v148, v149\n" \
"v_mfma_f32_16x16x32_bf16 v[156:159], a[80:83], a[48:51], v[156:159]\n" \
"ds_read_b128 a[228:231], v228 offset:448\n" \
"v_max3_f32 v192, v192, v150, v151\n" \
"v_max_f32_e64 v227, v192, v193\n" \
"s_nop 3\n" \
"s_nop 0\n" \
"v_max3_f32 v230, v152, v153, v154\n" \
"v_max3_f32 v231, v155, v156, v157\n" \
"v_max3_f32 v230, v230, v158, v159\n" \
"v_max_f32_e32 v228, v230, v231\n" \
"v_mov_b32_e32 v232, v225\n" \
"v_mov_b32_e32 v233, v226\n" \
"v_mov_b32_e32 v234, v227\n" \
"v_mov_b32_e32 v235, v228\n" \
"s_nop 0\n" \
"v_permlane16_swap_b32_e32 v225, v232\n" \
"v_permlane16_swap_b32_e32 v226, v233\n" \
"v_permlane16_swap_b32_e32 v227, v234\n" \
"v_permlane16_swap_b32_e32 v228, v235\n" \
"v_max_f32_e32 v225, v225, v232\n" \
"v_max_f32_e32 v226, v226, v233\n" \
"v_max_f32_e32 v227, v227, v234\n" \
"v_max_f32_e32 v228, v228, v235\n" \
"v_mov_b32_e32 v232, v225\n" \
"v_mov_b32_e32 v233, v226\n" \
"v_mov_b32_e32 v234, v227\n" \
"v_mov_b32_e32 v235, v228\n" \
"s_nop 0\n" \
"v_permlane32_swap_b32_e32 v225, v232\n" \
"v_permlane32_swap_b32_e32 v226, v233\n" \
"v_permlane32_swap_b32_e32 v227, v234\n" \
"v_permlane32_swap_b32_e32 v228, v235\n" \
"v_max_f32_e32 v225, v225, v232\n" \
"v_max_f32_e32 v226, v226, v233\n" \
"v_max_f32_e32 v227, v227, v234\n" \
"v_max_f32_e64 v228, v228, v235\n" \
"v_add_f32_e32 v225, 0x41800000, v225\n" \
"v_add_f32_e32 v226, 0x41800000, v226\n" \
"v_add_f32_e32 v227, 0x41800000, v227\n" \
"v_add_f32_e32 v228, 0x41800000, v228\n" \
"Lhavem_%=:\n" \
"s_nop 3\n" \
"v_mov_b64_e32 v[176:177], 0\n" \
"v_mov_b64_e32 v[178:179], 0\n" \
"v_mov_b64_e32 v[180:181], 0\n" \
"v_mov_b64_e32 v[182:183], 0\n" \
"s_and_b32 s68, s52, s69\n" \
"s_cselect_b32 s68, 0, 0x39400000\n" \
"s_waitcnt lgkmcnt(14)\n" \
"v_mfma_f32_16x16x32_bf16 v[230:233], a[128:131], a[128:131], 0\n" \
"v_sub_f32_e32 v160, 0, v225\n" \
"v_sub_f32_e32 v161, 0, v225\n" \
"v_sub_f32_e32 v162, 0, v225\n" \
"v_sub_f32_e32 v163, 0, v225\n" \
"v_sub_f32_e32 v164, 0, v226\n" \
"v_sub_f32_e32 v165, 0, v226\n" \
"v_sub_f32_e32 v166, 0, v226\n" \
"v_sub_f32_e32 v167, 0, v226\n" \
"v_sub_f32_e32 v168, 0, v227\n" \
"v_sub_f32_e32 v169, 0, v227\n" \
"v_sub_f32_e32 v170, 0, v227\n" \
"v_sub_f32_e32 v171, 0, v227\n" \
"v_sub_f32_e32 v172, 0, v228\n" \
"v_sub_f32_e32 v173, 0, v228\n" \
"v_sub_f32_e32 v174, 0, v228\n" \
"v_sub_f32_e32 v175, 0, v228\n" \
"v_sub_f32_e32 v128, v128, v225\n" \
"v_sub_f32_e32 v129, v129, v225\n" \
"v_sub_f32_e32 v130, v130, v225\n" \
"v_sub_f32_e32 v131, v131, v225\n" \
"v_sub_f32_e32 v132, v132, v225\n" \
"v_sub_f32_e32 v133, v133, v225\n" \
"v_sub_f32_e32 v134, v134, v225\n" \
"v_sub_f32_e32 v135, v135, v225\n" \
"v_sub_f32_e32 v136, v136, v226\n" \
"v_sub_f32_e32 v137, v137, v226\n" \
"v_sub_f32_e32 v138, v138, v226\n" \
"v_sub_f32_e32 v139, v139, v226\n" \
"v_sub_f32_e32 v140, v140, v226\n" \
"v_sub_f32_e32 v141, v141, v226\n" \
"v_sub_f32_e32 v142, v142, v226\n" \
"v_sub_f32_e32 v143, v143, v226\n" \
"v_sub_f32_e32 v144, v144, v227\n" \
"v_sub_f32_e32 v145, v145, v227\n" \
"v_sub_f32_e32 v146, v146, v227\n" \
"v_sub_f32_e32 v147, v147, v227\n" \
"v_sub_f32_e32 v148, v148, v227\n" \
"v_sub_f32_e32 v149, v149, v227\n" \
"v_sub_f32_e32 v150, v150, v227\n" \
"v_sub_f32_e32 v151, v151, v227\n" \
"v_sub_f32_e32 v152, v152, v228\n" \
"v_sub_f32_e32 v153, v153, v228\n" \
"v_sub_f32_e32 v154, v154, v228\n" \
"v_sub_f32_e32 v155, v155, v228\n" \
"v_sub_f32_e32 v156, v156, v228\n" \
"v_sub_f32_e32 v157, v157, v228\n" \
"v_sub_f32_e32 v158, v158, v228\n" \
"v_sub_f32_e32 v159, v159, v228\n" \
"v_exp_f32_e32 v128, v128\n" \
"v_exp_f32_e32 v129, v129\n" \
"v_exp_f32_e32 v130, v130\n" \
"v_exp_f32_e32 v131, v131\n" \
"v_exp_f32_e32 v132, v132\n" \
"v_exp_f32_e32 v133, v133\n" \
"v_exp_f32_e32 v134, v134\n" \
"v_exp_f32_e32 v135, v135\n" \
"v_exp_f32_e32 v136, v136\n" \
"v_exp_f32_e32 v137, v137\n" \
"v_exp_f32_e32 v138, v138\n" \
"v_exp_f32_e32 v139, v139\n" \
"v_exp_f32_e32 v140, v140\n" \
"v_exp_f32_e32 v141, v141\n" \
"v_exp_f32_e32 v142, v142\n" \
"v_exp_f32_e32 v143, v143\n" \
"v_exp_f32_e32 v144, v144\n" \
"v_exp_f32_e32 v145, v145\n" \
"v_exp_f32_e32 v146, v146\n" \
"v_exp_f32_e32 v147, v147\n" \
"v_exp_f32_e32 v148, v148\n" \
"v_exp_f32_e32 v149, v149\n" \
"v_exp_f32_e32 v150, v150\n" \
"v_exp_f32_e32 v151, v151\n" \
"v_exp_f32_e32 v152, v152\n" \
"v_exp_f32_e32 v153, v153\n" \
"v_exp_f32_e32 v154, v154\n" \
"v_exp_f32_e64 v155, v155\n" \
"v_bfe_i32 v228, v224, 7, 1\n" \
"v_and_b32_e32 v228, 0xaaaa, v228\n" \
"v_or_b32_e32 v228, 0x4444, v228\n" \
"v_cvt_pk_bf16_f32 v184, v128, v129\n" \
"v_cvt_pk_bf16_f32 v185, v130, v131\n" \
"v_cvt_pk_bf16_f32 v186, v132, v133\n" \
"v_cvt_pk_bf16_f32 v187, v134, v135\n" \
"v_cvt_pk_bf16_f32 v188, v136, v137\n" \
"v_cvt_pk_bf16_f32 v189, v138, v139\n" \
"v_cvt_pk_bf16_f32 v190, v140, v141\n" \
"v_cvt_pk_bf16_f32 v191, v142, v143\n" \
"v_cvt_pk_bf16_f32 v216, v144, v145\n" \
"v_cvt_pk_bf16_f32 v217, v146, v147\n" \
"v_cvt_pk_bf16_f32 v218, v148, v149\n" \
"v_cvt_pk_bf16_f32 v219, v150, v151\n" \
"v_max3_f32 v230, |v230|, |v231|, |v232|\n" \
"v_max_f32_e64 v230, v230, |v233|\n" \
"v_sqrt_f32_e32 v230, v230\n" \
"s_nop 0\n" \
"v_mul_legacy_f32 v225, s68, v230\n" \
"v_mov_b32_e32 v230, v225\n" \
"v_mov_b32_e32 v231, v225\n" \
"v_mov_b32_e32 v232, v225\n" \
"v_mov_b32_e32 v233, v225\n" \
"v_lshrrev_b32_e64 v235, 3, v224\n" \
"s_waitcnt vmcnt(2) lgkmcnt(0)\n" \
"s_barrier\n" \
"v_mfma_f32_16x16x32_bf16 v[128:131], a[96:99], a[0:3], v[160:163]\n" \
"v_exp_f32_e64 v156, v156\n" \
"v_mfma_f32_16x16x32_bf16 v[128:131], a[100:103], a[4:7], v[128:131]\n" \
"s_add_u32 m0, %[m0b], 114688\n" \
"v_mfma_f32_16x16x32_bf16 v[128:131], a[104:107], a[8:11], v[128:131]\n" \
"v_exp_f32_e64 v157, v157\n" \
"v_mfma_f32_16x16x32_bf16 v[128:131], a[108:111], a[12:15], v[128:131]\n" \
"v_cvt_pk_bf16_f32 v220, v152, v153\n" \
"v_cvt_pk_bf16_f32 v221, v154, v155\n" \
"v_mfma_f32_16x16x32_bf16 v[132:135], a[124:127], a[12:15], v[160:163]\n" \
"v_exp_f32_e64 v158, v158\n" \
"v_mfma_f32_16x16x32_bf16 v[132:135], a[120:123], a[8:11], v[132:135]\n" \
"v_mfma_f32_16x16x32_bf16 v[132:135], a[116:119], a[4:7], v[132:135]\n" \
"buffer_load_dwordx4 %[vd0], %[srdv], s48 offen offset:0 lds\n" \
"v_mfma_f32_16x16x32_bf16 v[132:135], a[112:115], a[0:3], v[132:135]\n" \
"v_exp_f32_e64 v159, v159\n" \
"v_mfma_f32_16x16x32_bf16 v[0:3], a[128:131], v[184:187], v[230:233]\n" \
"ds_read_b128 a[64:67], %[kb0] offset:16384\n" \
"v_mfma_f32_16x16x32_bf16 v[32:35], a[128:131], v[188:191], v[230:233]\n" \
"v_exp_f32_e64 v128, v128\n" \
"v_mfma_f32_16x16x32_bf16 v[4:7], a[132:135], v[184:187], v[230:233]\n" \
"v_cvt_pk_bf16_f32 v222, v156, v157\n" \
"v_cvt_pk_bf16_f32 v223, v158, v159\n" \
"v_mfma_f32_16x16x32_bf16 v[36:39], a[132:135], v[188:191], v[230:233]\n" \
"v_exp_f32_e64 v129, v129\n" \
"v_mfma_f32_16x16x32_bf16 v[8:11], a[136:139], v[184:187], v[230:233]\n" \
"ds_read_b128 a[68:71], v208 offset:16384\n" \
"v_mfma_f32_16x16x32_bf16 v[40:43], a[136:139], v[188:191], v[230:233]\n" \
"v_exp_f32_e64 v130, v130\n" \
"v_mfma_f32_16x16x32_bf16 v[12:15], a[140:143], v[184:187], v[230:233]\n" \
"ds_read_b128 a[72:75], v209 offset:16384\n" \
"v_mfma_f32_16x16x32_bf16 v[44:47], a[140:143], v[188:191], v[230:233]\n" \
"v_exp_f32_e64 v131, v131\n" \
"v_smfmac_f32_16x16x64_bf16 v[176:179], a[192:195], v[184:191], v228\n" \
"v_mfma_f32_16x16x32_bf16 v[136:139], a[96:99], a[16:19], v[164:167]\n" \
"v_exp_f32_e64 v132, v132\n" \
"v_mfma_f32_16x16x32_bf16 v[136:139], a[100:103], a[20:23], v[136:139]\n" \
"ds_read_b128 a[76:79], v210 offset:16384\n" \
"v_mfma_f32_16x16x32_bf16 v[136:139], a[104:107], a[24:27], v[136:139]\n" \
"v_exp_f32_e64 v133, v133\n" \
"v_mfma_f32_16x16x32_bf16 v[136:139], a[108:111], a[28:31], v[136:139]\n" \
"v_cvt_pk_bf16_f32 v192, v128, v129\n" \
"v_cvt_pk_bf16_f32 v193, v130, v131\n" \
"v_mfma_f32_16x16x32_bf16 v[140:143], a[124:127], a[28:31], v[164:167]\n" \
"v_exp_f32_e64 v134, v134\n" \
"v_mfma_f32_16x16x32_bf16 v[140:143], a[120:123], a[24:27], v[140:143]\n" \
"ds_read_b128 a[92:95], v210 offset:20480\n" \
"v_mfma_f32_16x16x32_bf16 v[140:143], a[116:119], a[20:23], v[140:143]\n" \
"buffer_load_dwordx4 v215, %[srdv], s48 offen offset:1024 lds\n" \
"v_mfma_f32_16x16x32_bf16 v[140:143], a[112:115], a[16:19], v[140:143]\n" \
"v_exp_f32_e64 v135, v135\n" \
"v_mfma_f32_16x16x32_bf16 v[16:19], a[144:147], v[184:187], v[230:233]\n" \
"ds_read_b128 a[88:91], v209 offset:20480\n" \
"v_mfma_f32_16x16x32_bf16 v[48:51], a[144:147], v[188:191], v[230:233]\n" \
"v_exp_f32_e64 v136, v136\n" \
"v_mfma_f32_16x16x32_bf16 v[20:23], a[148:151], v[184:187], v[230:233]\n" \
"v_cvt_pk_bf16_f32 v194, v132, v133\n" \
"v_cvt_pk_bf16_f32 v195, v134, v135\n" \
"v_mfma_f32_16x16x32_bf16 v[52:55], a[148:151], v[188:191], v[230:233]\n" \
"v_exp_f32_e64 v137, v137\n" \
"v_mfma_f32_16x16x32_bf16 v[24:27], a[152:155], v[184:187], v[230:233]\n" \
"v_exp_f32_e64 v138, v138\n" \
"v_mfma_f32_16x16x32_bf16 v[56:59], a[152:155], v[188:191], v[230:233]\n" \
"ds_read_b128 a[84:87], v208 offset:20480\n" \
"v_mfma_f32_16x16x32_bf16 v[28:31], a[156:159], v[184:187], v[230:233]\n" \
"v_exp_f32_e64 v139, v139\n" \
"v_mfma_f32_16x16x32_bf16 v[60:63], a[156:159], v[188:191], v[230:233]\n" \
"v_mfma_f32_16x16x32_bf16 v[144:147], a[96:99], a[32:35], v[168:171]\n" \
"v_exp_f32_e64 v140, v140\n" \
"v_mfma_f32_16x16x32_bf16 v[144:147], a[100:103], a[36:39], v[144:147]\n" \
"ds_read_b128 a[80:83], %[kb0] offset:20480\n" \
"v_mfma_f32_16x16x32_bf16 v[144:147], a[104:107], a[40:43], v[144:147]\n" \
"v_exp_f32_e64 v141, v141\n" \
"v_mfma_f32_16x16x32_bf16 v[144:147], a[108:111], a[44:47], v[144:147]\n" \
"v_cvt_pk_bf16_f32 v196, v136, v137\n" \
"v_cvt_pk_bf16_f32 v197, v138, v139\n" \
"v_mfma_f32_16x16x32_bf16 v[148:151], a[124:127], a[44:47], v[168:171]\n" \
"v_exp_f32_e64 v142, v142\n" \
"v_mfma_f32_16x16x32_bf16 v[148:151], a[120:123], a[40:43], v[148:151]\n" \
"v_mfma_f32_16x16x32_bf16 v[148:151], a[116:119], a[36:39], v[148:151]\n" \
"buffer_load_dwordx4 %[vd0], %[srdv], s48 offen offset:2048 lds\n" \
"v_mfma_f32_16x16x32_bf16 v[148:151], a[112:115], a[32:35], v[148:151]\n" \
"v_exp_f32_e64 v143, v143\n" \
"v_mfma_f32_16x16x32_bf16 v[64:67], a[128:131], v[216:219], v[230:233]\n" \
"ds_read_b64_tr_b16 a[160:161], %[vvb] offset:8192\n" \
"v_mfma_f32_16x16x32_bf16 v[96:99], a[128:131], v[220:223], v[230:233]\n" \
"v_exp_f32_e64 v144, v144\n" \
"v_mfma_f32_16x16x32_bf16 v[68:71], a[132:135], v[216:219], v[230:233]\n" \
"v_cvt_pk_bf16_f32 v198, v140, v141\n" \
"v_cvt_pk_bf16_f32 v199, v142, v143\n" \
"v_mfma_f32_16x16x32_bf16 v[100:103], a[132:135], v[220:223], v[230:233]\n" \
"v_exp_f32_e64 v145, v145\n" \
"v_mfma_f32_16x16x32_bf16 v[72:75], a[136:139], v[216:219], v[230:233]\n" \
"ds_read_b64_tr_b16 a[162:163], %[vvb] offset:12288\n" \
"ds_read_b64_tr_b16 a[164:165], v211 offset:8192\n" \
"v_mfma_f32_16x16x32_bf16 v[104:107], a[136:139], v[220:223], v[230:233]\n" \
"v_exp_f32_e64 v146, v146\n" \
"v_mfma_f32_16x16x32_bf16 v[76:79], a[140:143], v[216:219], v[230:233]\n" \
"ds_read_b64_tr_b16 a[166:167], v211 offset:12288\n" \
"v_mfma_f32_16x16x32_bf16 v[108:111], a[140:143], v[220:223], v[230:233]\n" \
"v_exp_f32_e64 v147, v147\n" \
"v_smfmac_f32_16x16x64_bf16 v[180:183], a[192:195], v[216:223], v228\n" \
"ds_read_b64_tr_b16 a[168:169], %[vvb] offset:8704\n" \
"v_mfma_f32_16x16x32_bf16 v[152:155], a[96:99], a[48:51], v[172:175]\n" \
"v_exp_f32_e64 v148, v148\n" \
"v_mfma_f32_16x16x32_bf16 v[152:155], a[100:103], a[52:55], v[152:155]\n" \
"ds_read_b64_tr_b16 a[170:171], %[vvb] offset:12800\n" \
"v_mfma_f32_16x16x32_bf16 v[152:155], a[104:107], a[56:59], v[152:155]\n" \
"v_exp_f32_e64 v149, v149\n" \
"v_mfma_f32_16x16x32_bf16 v[152:155], a[108:111], a[60:63], v[152:155]\n" \
"v_cvt_pk_bf16_f32 v200, v144, v145\n" \
"v_cvt_pk_bf16_f32 v201, v146, v147\n" \
"v_mfma_f32_16x16x32_bf16 v[156:159], a[124:127], a[60:63], v[172:175]\n" \
"v_exp_f32_e64 v150, v150\n" \
"v_mfma_f32_16x16x32_bf16 v[156:159], a[120:123], a[56:59], v[156:159]\n" \
"ds_read_b64_tr_b16 a[172:173], v211 offset:8704\n" \
"v_mfma_f32_16x16x32_bf16 v[156:159], a[116:119], a[52:55], v[156:159]\n" \
"buffer_load_dwordx4 v215, %[srdv], s48 offen offset:3072 lds\n" \
"v_mfma_f32_16x16x32_bf16 v[156:159], a[112:115], a[48:51], v[156:159]\n" \
"v_exp_f32_e64 v151, v151\n" \
"v_mfma_f32_16x16x32_bf16 v[80:83], a[144:147], v[216:219], v[230:233]\n" \
"ds_read_b64_tr_b16 a[174:175], v211 offset:12800\n" \
"ds_read_b64_tr_b16 a[176:177], %[vvb] offset:9216\n" \
"v_mfma_f32_16x16x32_bf16 v[112:115], a[144:147], v[220:223], v[230:233]\n" \
"v_exp_f32_e64 v152, v152\n" \
"v_mfma_f32_16x16x32_bf16 v[84:87], a[148:151], v[216:219], v[230:233]\n" \
"v_cvt_pk_bf16_f32 v202, v148, v149\n" \
"v_cvt_pk_bf16_f32 v203, v150, v151\n" \
"v_mfma_f32_16x16x32_bf16 v[116:119], a[148:151], v[220:223], v[230:233]\n" \
"v_exp_f32_e64 v153, v153\n" \
"v_mfma_f32_16x16x32_bf16 v[88:91], a[152:155], v[216:219], v[230:233]\n" \
"v_exp_f32_e64 v154, v154\n" \
"v_mfma_f32_16x16x32_bf16 v[120:123], a[152:155], v[220:223], v[230:233]\n" \
"ds_read_b64_tr_b16 a[178:179], %[vvb] offset:13312\n" \
"v_mfma_f32_16x16x32_bf16 v[92:95], a[156:159], v[216:219], v[230:233]\n" \
"v_exp_f32_e64 v155, v155\n" \
"v_mov_b64_e32 v[216:217], 0\n" \
"v_mov_b64_e32 v[218:219], 0\n" \
"v_mfma_f32_16x16x32_bf16 v[124:127], a[156:159], v[220:223], v[230:233]\n" \
"ds_read_b64_tr_b16 a[180:181], v211 offset:9216\n" \
"s_waitcnt lgkmcnt(13)\n" \
"s_nop 0\n" \
"LmA_%=:\n" \
"v_mfma_f32_16x16x32_bf16 v[128:131], a[64:67], a[0:3], v[160:163]\n" \
"v_exp_f32_e64 v156, v156\n" \
"v_mfma_f32_16x16x32_bf16 v[128:131], a[68:71], a[4:7], v[128:131]\n" \
"ds_read_b64_tr_b16 a[182:183], v211 offset:13312\n" \
"s_add_u32 m0, %[m0b], 0\n" \
"v_mfma_f32_16x16x32_bf16 v[128:131], a[72:75], a[8:11], v[128:131]\n" \
"v_exp_f32_e32 v157, v157\n" \
"v_mfma_f32_16x16x32_bf16 v[128:131], a[76:79], a[12:15], v[128:131]\n" \
"v_cvt_pk_bf16_f32 v204, v152, v153\n" \
"v_cvt_pk_bf16_f32 v205, v154, v155\n" \
"v_mfma_f32_16x16x32_bf16 v[132:135], a[92:95], a[12:15], v[160:163]\n" \
"v_exp_f32_e64 v158, v158\n" \
"v_mfma_f32_16x16x32_bf16 v[132:135], a[88:91], a[8:11], v[132:135]\n" \
"ds_read_b64_tr_b16 a[184:185], %[vvb] offset:9728\n" \
"s_waitcnt lgkmcnt(5)\n" \
"v_mfma_f32_16x16x32_bf16 v[132:135], a[84:87], a[4:7], v[132:135]\n" \
"buffer_load_dwordx4 %[kd0], %[srdk], s48 offen offset:0 lds\n" \
"v_mfma_f32_16x16x32_bf16 v[132:135], a[80:83], a[0:3], v[132:135]\n" \
"v_exp_f32_e32 v159, v159\n" \
"v_mfma_f32_16x16x32_bf16 v[64:67], a[128:131], v[216:219], v[64:67]\n" \
"ds_read_b64_tr_b16 a[186:187], %[vvb] offset:13824\n" \
"ds_read_b64_tr_b16 a[188:189], v211 offset:9728\n" \
"v_mfma_f32_16x16x32_bf16 v[64:67], a[160:163], v[200:203], v[64:67]\n" \
"v_exp_f32_e64 v128, v128\n" \
"v_mfma_f32_16x16x32_bf16 v[68:71], a[132:135], v[216:219], v[68:71]\n" \
"v_cvt_pk_bf16_f32 v206, v156, v157\n" \
"v_cvt_pk_bf16_f32 v207, v158, v159\n" \
"v_mfma_f32_16x16x32_bf16 v[68:71], a[164:167], v[200:203], v[68:71]\n" \
"v_exp_f32_e64 v129, v129\n" \
"v_mfma_f32_16x16x32_bf16 v[72:75], a[136:139], v[216:219], v[72:75]\n" \
"ds_read_b64_tr_b16 a[190:191], v211 offset:13824\n" \
"ds_read_b64_tr_b16 a[128:129], %[vvb] offset:16384\n" \
"s_add_u32 s60, s60, 1\n" \
"s_nop 0\n" \
"v_readlane_b32 s58, %[tlo], s60\n" \
"v_mfma_f32_16x16x32_bf16 v[72:75], a[168:171], v[200:203], v[72:75]\n" \
"v_exp_f32_e64 v130, v130\n" \
"v_mfma_f32_16x16x32_bf16 v[76:79], a[140:143], v[216:219], v[76:79]\n" \
"ds_read_b64_tr_b16 a[130:131], %[vvb] offset:20480\n" \
"ds_read_b64_tr_b16 a[132:133], v211 offset:16384\n" \
"v_readlane_b32 s59, %[thi], s60\n" \
"v_mfma_f32_16x16x32_bf16 v[76:79], a[172:175], v[200:203], v[76:79]\n" \
"v_exp_f32_e64 v131, v131\n" \
"v_smfmac_f32_16x16x64_bf16 v[176:179], a[192:195], v[192:199], v228\n" \
"ds_read_b64_tr_b16 a[134:135], v211 offset:20480\n" \
"ds_read_b64_tr_b16 a[136:137], %[vvb] offset:16896\n" \
"v_mfma_f32_16x16x32_bf16 v[136:139], a[64:67], a[16:19], v[164:167]\n" \
"v_exp_f32_e64 v132, v132\n" \
".if . - adaln_body != 12288\n" \
".error \"size model\"\n" \
".endif\n" \
"Lk2_%=:\n" \
"s_cbranch_execz Lk3_%=\n" \
"s_nop 0\n" \
"v_mfma_f32_16x16x32_bf16 v[136:139], a[68:71], a[20:23], v[136:139]\n" \
"ds_read_b64_tr_b16 a[138:139], %[vvb] offset:20992\n" \
"ds_read_b64_tr_b16 a[140:141], v211 offset:16896\n" \
"v_mfma_f32_16x16x32_bf16 v[136:139], a[72:75], a[24:27], v[136:139]\n" \
"v_exp_f32_e64 v133, v133\n" \
"v_mfma_f32_16x16x32_bf16 v[136:139], a[76:79], a[28:31], v[136:139]\n" \
"v_cvt_pk_bf16_f32 v184, v128, v129\n" \
"v_cvt_pk_bf16_f32 v185, v130, v131\n" \
"v_mfma_f32_16x16x32_bf16 v[140:143], a[92:95], a[28:31], v[164:167]\n" \
"v_exp_f32_e64 v134, v134\n" \
"v_mfma_f32_16x16x32_bf16 v[140:143], a[88:91], a[24:27], v[140:143]\n" \
"ds_read_b64_tr_b16 a[142:143], v211 offset:20992\n" \
"s_waitcnt lgkmcnt(8)\n" \
"v_mfma_f32_16x16x32_bf16 v[140:143], a[84:87], a[20:23], v[140:143]\n" \
"buffer_load_dwordx4 v212, %[srdk], s48 offen offset:1024 lds\n" \
"v_mfma_f32_16x16x32_bf16 v[140:143], a[80:83], a[16:19], v[140:143]\n" \
"v_exp_f32_e32 v135, v135\n" \
"v_mfma_f32_16x16x32_bf16 v[80:83], a[144:147], v[216:219], v[80:83]\n" \
"ds_read_b64_tr_b16 a[144:145], %[vvb] offset:17408\n" \
"ds_read_b64_tr_b16 a[146:147], %[vvb] offset:21504\n" \
"v_mfma_f32_16x16x32_bf16 v[80:83], a[176:179], v[200:203], v[80:83]\n" \
"v_exp_f32_e64 v136, v136\n" \
"v_mfma_f32_16x16x32_bf16 v[84:87], a[148:151], v[216:219], v[84:87]\n" \
"v_cvt_pk_bf16_f32 v186, v132, v133\n" \
"v_cvt_pk_bf16_f32 v187, v134, v135\n" \
"v_mfma_f32_16x16x32_bf16 v[84:87], a[180:183], v[200:203], v[84:87]\n" \
"v_exp_f32_e64 v137, v137\n" \
"v_mfma_f32_16x16x32_bf16 v[88:91], a[152:155], v[216:219], v[88:91]\n" \
"v_exp_f32_e64 v138, v138\n" \
"v_mfma_f32_16x16x32_bf16 v[88:91], a[184:187], v[200:203], v[88:91]\n" \
"ds_read_b64_tr_b16 a[148:149], v211 offset:17408\n" \
"ds_read_b64_tr_b16 a[150:151], v211 offset:21504\n" \
"v_mfma_f32_16x16x32_bf16 v[92:95], a[156:159], v[216:219], v[92:95]\n" \
"v_exp_f32_e64 v139, v139\n" \
"v_mfma_f32_16x16x32_bf16 v[92:95], a[188:191], v[200:203], v[92:95]\n" \
"ds_read_b64_tr_b16 a[152:153], %[vvb] offset:17920\n" \
"v_mfma_f32_16x16x32_bf16 v[144:147], a[64:67], a[32:35], v[168:171]\n" \
"v_exp_f32_e64 v140, v140\n" \
"v_mfma_f32_16x16x32_bf16 v[144:147], a[68:71], a[36:39], v[144:147]\n" \
"ds_read_b64_tr_b16 a[154:155], %[vvb] offset:22016\n" \
"ds_read_b64_tr_b16 a[156:157], v211 offset:17920\n" \
"v_mfma_f32_16x16x32_bf16 v[144:147], a[72:75], a[40:43], v[144:147]\n" \
"v_exp_f32_e64 v141, v141\n" \
"v_mfma_f32_16x16x32_bf16 v[144:147], a[76:79], a[44:47], v[144:147]\n" \
"v_cvt_pk_bf16_f32 v188, v136, v137\n" \
"v_cvt_pk_bf16_f32 v189, v138, v139\n" \
"v_mfma_f32_16x16x32_bf16 v[148:151], a[92:95], a[44:47], v[168:171]\n" \
"v_exp_f32_e64 v142, v142\n" \
"v_mfma_f32_16x16x32_bf16 v[148:151], a[88:91], a[40:43], v[148:151]\n" \
"ds_read_b64_tr_b16 a[158:159], v211 offset:22016\n" \
"s_waitcnt lgkmcnt(8)\n" \
"v_mfma_f32_16x16x32_bf16 v[148:151], a[84:87], a[36:39], v[148:151]\n" \
"buffer_load_dwordx4 v213, %[srdk], s48 offen offset:2048 lds\n" \
"v_mfma_f32_16x16x32_bf16 v[148:151], a[80:83], a[32:35], v[148:151]\n" \
"v_exp_f32_e32 v143, v143\n" \
"v_mfma_f32_16x16x32_bf16 v[0:3], a[160:163], v[192:195], v[0:3]\n" \
"ds_read_b128 a[96:99], %[kb0] offset:24576\n" \
"v_mfma_f32_16x16x32_bf16 v[0:3], a[128:131], v[184:187], v[0:3]\n" \
"v_exp_f32_e64 v144, v144\n" \
"v_mfma_f32_16x16x32_bf16 v[4:7], a[164:167], v[192:195], v[4:7]\n" \
"v_cvt_pk_bf16_f32 v190, v140, v141\n" \
"v_cvt_pk_bf16_f32 v191, v142, v143\n" \
"v_mfma_f32_16x16x32_bf16 v[4:7], a[132:135], v[184:187], v[4:7]\n" \
"v_exp_f32_e64 v145, v145\n" \
"v_mfma_f32_16x16x32_bf16 v[8:11], a[168:171], v[192:195], v[8:11]\n" \
"ds_read_b128 a[100:103], v208 offset:24576\n" \
"v_mfma_f32_16x16x32_bf16 v[8:11], a[136:139], v[184:187], v[8:11]\n" \
"v_exp_f32_e64 v146, v146\n" \
"v_mfma_f32_16x16x32_bf16 v[12:15], a[172:175], v[192:195], v[12:15]\n" \
"ds_read_b128 a[104:107], v209 offset:24576\n" \
"v_mfma_f32_16x16x32_bf16 v[12:15], a[140:143], v[184:187], v[12:15]\n" \
"v_exp_f32_e64 v147, v147\n" \
"v_smfmac_f32_16x16x64_bf16 v[180:183], a[192:195], v[200:207], v228\n" \
"ds_read_b128 a[108:111], v210 offset:24576\n" \
"v_mfma_f32_16x16x32_bf16 v[152:155], a[64:67], a[48:51], v[172:175]\n" \
"v_exp_f32_e64 v148, v148\n" \
"v_mfma_f32_16x16x32_bf16 v[152:155], a[68:71], a[52:55], v[152:155]\n" \
"ds_read_b128 a[124:127], v210 offset:28672\n" \
"v_mfma_f32_16x16x32_bf16 v[152:155], a[72:75], a[56:59], v[152:155]\n" \
"v_exp_f32_e64 v149, v149\n" \
"v_mfma_f32_16x16x32_bf16 v[152:155], a[76:79], a[60:63], v[152:155]\n" \
"v_cvt_pk_bf16_f32 v216, v144, v145\n" \
"v_cvt_pk_bf16_f32 v217, v146, v147\n" \
"v_mfma_f32_16x16x32_bf16 v[156:159], a[92:95], a[60:63], v[172:175]\n" \
"v_exp_f32_e64 v150, v150\n" \
"v_mfma_f32_16x16x32_bf16 v[156:159], a[88:91], a[56:59], v[156:159]\n" \
"ds_read_b128 a[120:123], v209 offset:28672\n" \
"s_waitcnt lgkmcnt(6)\n" \
"v_mfma_f32_16x16x32_bf16 v[156:159], a[84:87], a[52:55], v[156:159]\n" \
"buffer_load_dwordx4 v214, %[srdk], s48 offen offset:3072 lds\n" \
"v_mfma_f32_16x16x32_bf16 v[156:159], a[80:83], a[48:51], v[156:159]\n" \
"v_exp_f32_e32 v151, v151\n" \
"v_mfma_f32_16x16x32_bf16 v[16:19], a[176:179], v[192:195], v[16:19]\n" \
"ds_read_b128 a[116:119], v208 offset:28672\n" \
"v_mfma_f32_16x16x32_bf16 v[16:19], a[144:147], v[184:187], v[16:19]\n" \
"v_exp_f32_e64 v152, v152\n" \
"v_mfma_f32_16x16x32_bf16 v[20:23], a[180:183], v[192:195], v[20:23]\n" \
"v_cvt_pk_bf16_f32 v218, v148, v149\n" \
"v_cvt_pk_bf16_f32 v219, v150, v151\n" \
"v_mfma_f32_16x16x32_bf16 v[20:23], a[148:151], v[184:187], v[20:23]\n" \
"v_exp_f32_e64 v153, v153\n" \
"v_mfma_f32_16x16x32_bf16 v[24:27], a[184:187], v[192:195], v[24:27]\n" \
"v_exp_f32_e64 v154, v154\n" \
"v_mfma_f32_16x16x32_bf16 v[24:27], a[152:155], v[184:187], v[24:27]\n" \
"ds_read_b128 a[112:115], %[kb0] offset:28672\n" \
"s_add_u32 s48, s48, 0x4000\n" \
"s_mov_b32 m0, s44\n" \
"s_addk_i32 s44, 0x400\n" \
"buffer_load_dwordx4 v229, s[64:67], s45 offen offset:0 nt lds\n" \
"s_add_u32 s45, s45, 393216\n" \
"v_mfma_f32_16x16x32_bf16 v[28:31], a[188:191], v[192:195], v[28:31]\n" \
"v_exp_f32_e64 v155, v155\n" \
"v_mfma_f32_16x16x32_bf16 v[28:31], a[156:159], v[184:187], v[28:31]\n" \
"s_waitcnt vmcnt(14) lgkmcnt(2)\n" \
"s_barrier\n" \
"v_mfma_f32_16x16x32_bf16 v[128:131], a[96:99], a[0:3], v[160:163]\n" \
"v_exp_f32_e64 v156, v156\n" \
"v_mfma_f32_16x16x32_bf16 v[128:131], a[100:103], a[4:7], v[128:131]\n" \
"s_add_u32 m0, %[m0b], 65536\n" \
"v_mfma_f32_16x16x32_bf16 v[128:131], a[104:107], a[8:11], v[128:131]\n" \
"v_exp_f32_e64 v157, v157\n" \
"v_mfma_f32_16x16x32_bf16 v[128:131], a[108:111], a[12:15], v[128:131]\n" \
"v_cvt_pk_bf16_f32 v220, v152, v153\n" \
"v_cvt_pk_bf16_f32 v221, v154, v155\n" \
"v_mfma_f32_16x16x32_bf16 v[132:135], a[124:127], a[12:15], v[160:163]\n" \
"v_exp_f32_e64 v158, v158\n" \
"v_mfma_f32_16x16x32_bf16 v[132:135], a[120:123], a[8:11], v[132:135]\n" \
"s_waitcnt lgkmcnt(0)\n" \
"v_mfma_f32_16x16x32_bf16 v[132:135], a[116:119], a[4:7], v[132:135]\n" \
"buffer_load_dwordx4 %[vd0], %[srdv], s48 offen offset:0 lds\n" \
"v_mfma_f32_16x16x32_bf16 v[132:135], a[112:115], a[0:3], v[132:135]\n" \
"v_exp_f32_e32 v159, v159\n" \
"v_mfma_f32_16x16x32_bf16 v[32:35], a[160:163], v[196:199], v[32:35]\n" \
"ds_read_b128 a[64:67], %[kb0] offset:32768\n" \
"v_mfma_f32_16x16x32_bf16 v[32:35], a[128:131], v[188:191], v[32:35]\n" \
"v_exp_f32_e64 v128, v128\n" \
"v_mfma_f32_16x16x32_bf16 v[36:39], a[164:167], v[196:199], v[36:39]\n" \
"v_cvt_pk_bf16_f32 v222, v156, v157\n" \
"v_cvt_pk_bf16_f32 v223, v158, v159\n" \
"v_mfma_f32_16x16x32_bf16 v[36:39], a[132:135], v[188:191], v[36:39]\n" \
"v_exp_f32_e64 v129, v129\n" \
"v_mfma_f32_16x16x32_bf16 v[40:43], a[168:171], v[196:199], v[40:43]\n" \
"ds_read_b128 a[68:71], v208 offset:32768\n" \
"v_mfma_f32_16x16x32_bf16 v[40:43], a[136:139], v[188:191], v[40:43]\n" \
"v_exp_f32_e64 v130, v130\n" \
"v_mfma_f32_16x16x32_bf16 v[44:47], a[172:175], v[196:199], v[44:47]\n" \
"ds_read_b128 a[72:75], v209 offset:32768\n" \
"v_mfma_f32_16x16x32_bf16 v[44:47], a[140:143], v[188:191], v[44:47]\n" \
"v_exp_f32_e64 v131, v131\n" \
"v_smfmac_f32_16x16x64_bf16 v[176:179], a[192:195], v[184:191], v228\n" \
"v_mfma_f32_16x16x32_bf16 v[136:139], a[96:99], a[16:19], v[164:167]\n" \
"v_exp_f32_e64 v132, v132\n" \
"v_mfma_f32_16x16x32_bf16 v[136:139], a[100:103], a[20:23], v[136:139]\n" \
"ds_read_b128 a[76:79], v210 offset:32768\n" \
"v_mfma_f32_16x16x32_bf16 v[136:139], a[104:107], a[24:27], v[136:139]\n" \
"v_exp_f32_e64 v133, v133\n" \
"v_mfma_f32_16x16x32_bf16 v[136:139], a[108:111], a[28:31], v[136:139]\n" \
"v_cvt_pk_bf16_f32 v192, v128, v129\n" \
"v_cvt_pk_bf16_f32 v193, v130, v131\n" \
"v_mfma_f32_16x16x32_bf16 v[140:143], a[124:127], a[28:31], v[164:167]\n" \
"v_exp_f32_e64 v134, v134\n" \
"v_mfma_f32_16x16x32_bf16 v[140:143], a[120:123], a[24:27], v[140:143]\n" \
"ds_read_b128 a[92:95], v210 offset:36864\n" \
"v_mfma_f32_16x16x32_bf16 v[140:143], a[116:119], a[20:23], v[140:143]\n" \
"buffer_load_dwordx4 v215, %[srdv], s48 offen offset:1024 lds\n" \
"v_mfma_f32_16x16x32_bf16 v[140:143], a[112:115], a[16:19], v[140:143]\n" \
"v_exp_f32_e64 v135, v135\n" \
"v_mfma_f32_16x16x32_bf16 v[48:51], a[176:179], v[196:199], v[48:51]\n" \
"ds_read_b128 a[88:91], v209 offset:36864\n" \
"v_mfma_f32_16x16x32_bf16 v[48:51], a[144:147], v[188:191], v[48:51]\n" \
"v_exp_f32_e64 v136, v136\n" \
"v_mfma_f32_16x16x32_bf16 v[52:55], a[180:183], v[196:199], v[52:55]\n" \
"v_cvt_pk_bf16_f32 v194, v132, v133\n" \
"v_cvt_pk_bf16_f32 v195, v134, v135\n" \
"v_mfma_f32_16x16x32_bf16 v[52:55], a[148:151], v[188:191], v[52:55]\n" \
"v_exp_f32_e64 v137, v137\n" \
"v_mfma_f32_16x16x32_bf16 v[56:59], a[184:187], v[196:199], v[56:59]\n" \
"v_exp_f32_e64 v138, v138\n" \
"v_mfma_f32_16x16x32_bf16 v[56:59], a[152:155], v[188:191], v[56:59]\n" \
"ds_read_b128 a[84:87], v208 offset:36864\n" \
"v_mfma_f32_16x16x32_bf16 v[60:63], a[188:191], v[196:199], v[60:63]\n" \
"v_exp_f32_e64 v139, v139\n" \
"v_mfma_f32_16x16x32_bf16 v[60:63], a[156:159], v[188:191], v[60:63]\n" \
"s_cmp_lg_u32 %[wid], 0\n" \
"s_cbranch_scc1 LtA3_%=\n" \
"global_load_dword v237, v224, s[58:59]\n" \
"s_add_u32 s61, s48, 49152\n" \
"s_mov_b32 exec_hi, 0\n" \
"s_nop 0\n" \
"buffer_load_dword v237, v224, %[srdp], s61 offen\n" \
"s_mov_b32 exec_hi, -1\n" \
"s_nop 0\n" \
"LtA3_%=:\n" \
"v_mfma_f32_16x16x32_bf16 v[144:147], a[96:99], a[32:35], v[168:171]\n" \
"v_exp_f32_e64 v140, v140\n" \
"v_mfma_f32_16x16x32_bf16 v[144:147], a[100:103], a[36:39], v[144:147]\n" \
"ds_read_b128 a[80:83], %[kb0] offset:36864\n" \
"v_mfma_f32_16x16x32_bf16 v[144:147], a[104:107], a[40:43], v[144:147]\n" \
"v_exp_f32_e64 v141, v141\n" \
"v_mfma_f32_16x16x32_bf16 v[144:147], a[108:111], a[44:47], v[144:147]\n" \
"v_cvt_pk_bf16_f32 v196, v136, v137\n" \
"v_cvt_pk_bf16_f32 v197, v138, v139\n" \
"v_mfma_f32_16x16x32_bf16 v[148:151], a[124:127], a[44:47], v[168:171]\n" \
"v_exp_f32_e64 v142, v142\n" \
"v_mfma_f32_16x16x32_bf16 v[148:151], a[120:123], a[40:43], v[148:151]\n" \
"v_mfma_f32_16x16x32_bf16 v[148:151], a[116:119], a[36:39], v[148:151]\n" \
"buffer_load_dwordx4 %[vd0], %[srdv], s48 offen offset:2048 lds\n" \
"v_mfma_f32_16x16x32_bf16 v[148:151], a[112:115], a[32:35], v[148:151]\n" \
"v_exp_f32_e64 v143, v143\n" \
"v_mfma_f32_16x16x32_bf16 v[96:99], a[160:163], v[204:207], v[96:99]\n" \
"ds_read_b64_tr_b16 a[160:161], %[vvb] offset:24576\n" \
"v_mfma_f32_16x16x32_bf16 v[96:99], a[128:131], v[220:223], v[96:99]\n" \
"v_exp_f32_e64 v144, v144\n" \
"v_mfma_f32_16x16x32_bf16 v[100:103], a[164:167], v[204:207], v[100:103]\n" \
"v_cvt_pk_bf16_f32 v198, v140, v141\n" \
"v_cvt_pk_bf16_f32 v199, v142, v143\n" \
"v_mfma_f32_16x16x32_bf16 v[100:103], a[132:135], v[220:223], v[100:103]\n" \
"v_exp_f32_e64 v145, v145\n" \
"v_mfma_f32_16x16x32_bf16 v[104:107], a[168:171], v[204:207], v[104:107]\n" \
"ds_read_b64_tr_b16 a[162:163], %[vvb] offset:28672\n" \
"ds_read_b64_tr_b16 a[164:165], v211 offset:24576\n" \
"v_mfma_f32_16x16x32_bf16 v[104:107], a[136:139], v[220:223], v[104:107]\n" \
"v_exp_f32_e64 v146, v146\n" \
"v_mfma_f32_16x16x32_bf16 v[108:111], a[172:175], v[204:207], v[108:111]\n" \
"ds_read_b64_tr_b16 a[166:167], v211 offset:28672\n" \
"v_mfma_f32_16x16x32_bf16 v[108:111], a[140:143], v[220:223], v[108:111]\n" \
"v_exp_f32_e64 v147, v147\n" \
"v_smfmac_f32_16x16x64_bf16 v[180:183], a[192:195], v[216:223], v228\n" \
"ds_read_b64_tr_b16 a[168:169], %[vvb] offset:25088\n" \
"v_mfma_f32_16x16x32_bf16 v[152:155], a[96:99], a[48:51], v[172:175]\n" \
"v_exp_f32_e64 v148, v148\n" \
"v_mfma_f32_16x16x32_bf16 v[152:155], a[100:103], a[52:55], v[152:155]\n" \
"ds_read_b64_tr_b16 a[170:171], %[vvb] offset:29184\n" \
"v_mfma_f32_16x16x32_bf16 v[152:155], a[104:107], a[56:59], v[152:155]\n" \
"v_exp_f32_e64 v149, v149\n" \
"v_mfma_f32_16x16x32_bf16 v[152:155], a[108:111], a[60:63], v[152:155]\n" \
"v_cvt_pk_bf16_f32 v200, v144, v145\n" \
"v_cvt_pk_bf16_f32 v201, v146, v147\n" \
"v_mfma_f32_16x16x32_bf16 v[156:159], a[124:127], a[60:63], v[172:175]\n" \
"v_exp_f32_e64 v150, v150\n" \
"v_mfma_f32_16x16x32_bf16 v[156:159], a[120:123], a[56:59], v[156:159]\n" \
"ds_read_b64_tr_b16 a[172:173], v211 offset:25088\n" \
"v_mfma_f32_16x16x32_bf16 v[156:159], a[116:119], a[52:55], v[156:159]\n" \
"buffer_load_dwordx4 v215, %[srdv], s48 offen offset:3072 lds\n" \
"v_mfma_f32_16x16x32_bf16 v[156:159], a[112:115], a[48:51], v[156:159]\n" \
"v_exp_f32_e64 v151, v151\n" \
"v_mfma_f32_16x16x32_bf16 v[112:115], a[176:179], v[204:207], v[112:115]\n" \
"ds_read_b64_tr_b16 a[174:175], v211 offset:29184\n" \
"ds_read_b64_tr_b16 a[176:177], %[vvb] offset:25600\n" \
"v_mfma_f32_16x16x32_bf16 v[112:115], a[144:147], v[220:223], v[112:115]\n" \
"v_exp_f32_e64 v152, v152\n" \
"v_mfma_f32_16x16x32_bf16 v[116:119], a[180:183], v[204:207], v[116:119]\n" \
"v_cvt_pk_bf16_f32 v202, v148, v149\n" \
"v_cvt_pk_bf16_f32 v203, v150, v151\n" \
"v_mfma_f32_16x16x32_bf16 v[116:119], a[148:151], v[220:223], v[116:119]\n" \
"v_exp_f32_e64 v153, v153\n" \
"v_mfma_f32_16x16x32_bf16 v[120:123], a[184:187], v[204:207], v[120:123]\n" \
"v_exp_f32_e64 v154, v154\n" \
"v_mfma_f32_16x16x32_bf16 v[120:123], a[152:155], v[220:223], v[120:123]\n" \
"ds_read_b64_tr_b16 a[178:179], %[vvb] offset:29696\n" \
"v_mfma_f32_16x16x32_bf16 v[124:127], a[188:191], v[204:207], v[124:127]\n" \
"v_exp_f32_e64 v155, v155\n" \
"v_mfma_f32_16x16x32_bf16 v[124:127], a[156:159], v[220:223], v[124:127]\n" \
"ds_read_b64_tr_b16 a[180:181], v211 offset:25600\n" \
"s_waitcnt lgkmcnt(13)\n" \
"v_mfma_f32_16x16x32_bf16 v[128:131], a[64:67], a[0:3], v[160:163]\n" \
"v_exp_f32_e32 v156, v156\n" \
"v_mfma_f32_16x16x32_bf16 v[128:131], a[68:71], a[4:7], v[128:131]\n" \
"ds_read_b64_tr_b16 a[182:183], v211 offset:29696\n" \
"s_add_u32 m0, %[m0b], 16384\n" \
"v_mfma_f32_16x16x32_bf16 v[128:131], a[72:75], a[8:11], v[128:131]\n" \
"v_exp_f32_e64 v157, v157\n" \
"v_mfma_f32_16x16x32_bf16 v[128:131], a[76:79], a[12:15], v[128:131]\n" \
"v_cvt_pk_bf16_f32 v204, v152, v153\n" \
"v_cvt_pk_bf16_f32 v205, v154, v155\n" \
"v_mfma_f32_16x16x32_bf16 v[132:135], a[92:95], a[12:15], v[160:163]\n" \
"v_exp_f32_e64 v158, v158\n" \
"v_mfma_f32_16x16x32_bf16 v[132:135], a[88:91], a[8:11], v[132:135]\n" \
"ds_read_b64_tr_b16 a[184:185], %[vvb] offset:26112\n" \
"s_waitcnt lgkmcnt(5)\n" \
"v_mfma_f32_16x16x32_bf16 v[132:135], a[84:87], a[4:7], v[132:135]\n" \
"buffer_load_dwordx4 %[kd0], %[srdk], s48 offen offset:0 lds\n" \
"v_mfma_f32_16x16x32_bf16 v[132:135], a[80:83], a[0:3], v[132:135]\n" \
"v_exp_f32_e32 v159, v159\n" \
"v_mfma_f32_16x16x32_bf16 v[64:67], a[128:131], v[216:219], v[64:67]\n" \
"ds_read_b64_tr_b16 a[186:187], %[vvb] offset:30208\n" \
"ds_read_b64_tr_b16 a[188:189], v211 offset:26112\n" \
"v_mfma_f32_16x16x32_bf16 v[64:67], a[160:163], v[200:203], v[64:67]\n" \
"v_exp_f32_e64 v128, v128\n" \
"v_mfma_f32_16x16x32_bf16 v[68:71], a[132:135], v[216:219], v[68:71]\n" \
"v_cvt_pk_bf16_f32 v206, v156, v157\n" \
"v_cvt_pk_bf16_f32 v207, v158, v159\n" \
"v_mfma_f32_16x16x32_bf16 v[68:71], a[164:167], v[200:203], v[68:71]\n" \
"v_exp_f32_e64 v129, v129\n" \
"v_mfma_f32_16x16x32_bf16 v[72:75], a[136:139], v[216:219], v[72:75]\n" \
"ds_read_b64_tr_b16 a[190:191], v211 offset:30208\n" \
"ds_read_b64_tr_b16 a[128:129], %[vvb] offset:32768\n" \
"v_mfma_f32_16x16x32_bf16 v[72:75], a[168:171], v[200:203], v[72:75]\n" \
"v_exp_f32_e64 v130, v130\n" \
"v_mfma_f32_16x16x32_bf16 v[76:79], a[140:143], v[216:219], v[76:79]\n" \
"ds_read_b64_tr_b16 a[130:131], %[vvb] offset:36864\n" \
"ds_read_b64_tr_b16 a[132:133], v211 offset:32768\n" \
"v_mfma_f32_16x16x32_bf16 v[76:79], a[172:175], v[200:203], v[76:79]\n" \
"v_exp_f32_e64 v131, v131\n" \
"v_smfmac_f32_16x16x64_bf16 v[176:179], a[192:195], v[192:199], v228\n" \
"ds_read_b64_tr_b16 a[134:135], v211 offset:36864\n" \
"ds_read_b64_tr_b16 a[136:137], %[vvb] offset:33280\n" \
"v_mfma_f32_16x16x32_bf16 v[136:139], a[64:67], a[16:19], v[164:167]\n" \
"v_exp_f32_e64 v132, v132\n" \
"v_mfma_f32_16x16x32_bf16 v[136:139], a[68:71], a[20:23], v[136:139]\n" \
"ds_read_b64_tr_b16 a[138:139], %[vvb] offset:37376\n" \
"ds_read_b64_tr_b16 a[140:141], v211 offset:33280\n" \
"v_mfma_f32_16x16x32_bf16 v[136:139], a[72:75], a[24:27], v[136:139]\n" \
"v_exp_f32_e64 v133, v133\n" \
"v_mfma_f32_16x16x32_bf16 v[136:139], a[76:79], a[28:31], v[136:139]\n" \
"v_cvt_pk_bf16_f32 v184, v128, v129\n" \
"v_cvt_pk_bf16_f32 v185, v130, v131\n" \
"v_mfma_f32_16x16x32_bf16 v[140:143], a[92:95], a[28:31], v[164:167]\n" \
"v_exp_f32_e64 v134, v134\n" \
"v_mfma_f32_16x16x32_bf16 v[140:143], a[88:91], a[24:27], v[140:143]\n" \
"ds_read_b64_tr_b16 a[142:143], v211 offset:37376\n" \
"s_waitcnt lgkmcnt(8)\n" \
"v_mfma_f32_16x16x32_bf16 v[140:143], a[84:87], a[20:23], v[140:143]\n" \
"buffer_load_dwordx4 v212, %[srdk], s48 offen offset:1024 lds\n" \
"v_mfma_f32_16x16x32_bf16 v[140:143], a[80:83], a[16:19], v[140:143]\n" \
"v_exp_f32_e32 v135, v135\n" \
"v_mfma_f32_16x16x32_bf16 v[80:83], a[144:147], v[216:219], v[80:83]\n" \
"ds_read_b64_tr_b16 a[144:145], %[vvb] offset:33792\n" \
"ds_read_b64_tr_b16 a[146:147], %[vvb] offset:37888\n" \
"v_mfma_f32_16x16x32_bf16 v[80:83], a[176:179], v[200:203], v[80:83]\n" \
"v_exp_f32_e64 v136, v136\n" \
"v_mfma_f32_16x16x32_bf16 v[84:87], a[148:151], v[216:219], v[84:87]\n" \
"v_cvt_pk_bf16_f32 v186, v132, v133\n" \
"v_cvt_pk_bf16_f32 v187, v134, v135\n" \
"v_mfma_f32_16x16x32_bf16 v[84:87], a[180:183], v[200:203], v[84:87]\n" \
"v_exp_f32_e64 v137, v137\n" \
"v_mfma_f32_16x16x32_bf16 v[88:91], a[152:155], v[216:219], v[88:91]\n" \
"v_exp_f32_e64 v138, v138\n" \
"v_mfma_f32_16x16x32_bf16 v[88:91], a[184:187], v[200:203], v[88:91]\n" \
"ds_read_b64_tr_b16 a[148:149], v211 offset:33792\n" \
"ds_read_b64_tr_b16 a[150:151], v211 offset:37888\n" \
"v_mfma_f32_16x16x32_bf16 v[92:95], a[156:159], v[216:219], v[92:95]\n" \
"v_exp_f32_e64 v139, v139\n" \
"v_mfma_f32_16x16x32_bf16 v[92:95], a[188:191], v[200:203], v[92:95]\n" \
"ds_read_b64_tr_b16 a[152:153], %[vvb] offset:34304\n" \
"buffer_store_dwordx4 a[196:199], v235, %[srdz], s62 offen offset:0 sc1\n" \
"v_mfma_f32_16x16x32_bf16 v[144:147], a[64:67], a[32:35], v[168:171]\n" \
"v_exp_f32_e64 v140, v140\n" \
"v_mfma_f32_16x16x32_bf16 v[144:147], a[68:71], a[36:39], v[144:147]\n" \
"ds_read_b64_tr_b16 a[154:155], %[vvb] offset:38400\n" \
"ds_read_b64_tr_b16 a[156:157], v211 offset:34304\n" \
"v_mfma_f32_16x16x32_bf16 v[144:147], a[72:75], a[40:43], v[144:147]\n" \
"v_exp_f32_e64 v141, v141\n" \
"v_mfma_f32_16x16x32_bf16 v[144:147], a[76:79], a[44:47], v[144:147]\n" \
"v_cvt_pk_bf16_f32 v188, v136, v137\n" \
"v_cvt_pk_bf16_f32 v189, v138, v139\n" \
"v_mfma_f32_16x16x32_bf16 v[148:151], a[92:95], a[44:47], v[168:171]\n" \
"v_exp_f32_e64 v142, v142\n" \
"v_mfma_f32_16x16x32_bf16 v[148:151], a[88:91], a[40:43], v[148:151]\n" \
"ds_read_b64_tr_b16 a[158:159], v211 offset:38400\n" \
"s_waitcnt lgkmcnt(8)\n" \
"v_mfma_f32_16x16x32_bf16 v[148:151], a[84:87], a[36:39], v[148:151]\n" \
"buffer_load_dwordx4 v213, %[srdk], s48 offen offset:2048 lds\n" \
"v_mfma_f32_16x16x32_bf16 v[148:151], a[80:83], a[32:35], v[148:151]\n" \
"v_exp_f32_e32 v143, v143\n" \
"v_mfma_f32_16x16x32_bf16 v[0:3], a[160:163], v[192:195], v[0:3]\n" \
"ds_read_b128 a[96:99], %[kb0] offset:40960\n" \
"v_mfma_f32_16x16x32_bf16 v[0:3], a[128:131], v[184:187], v[0:3]\n" \
"v_exp_f32_e64 v144, v144\n" \
"v_mfma_f32_16x16x32_bf16 v[4:7], a[164:167], v[192:195], v[4:7]\n" \
"v_cvt_pk_bf16_f32 v190, v140, v141\n" \
"v_cvt_pk_bf16_f32 v191, v142, v143\n" \
"v_mfma_f32_16x16x32_bf16 v[4:7], a[132:135], v[184:187], v[4:7]\n" \
"v_exp_f32_e64 v145, v145\n" \
"v_mfma_f32_16x16x32_bf16 v[8:11], a[168:171], v[192:195], v[8:11]\n" \
"ds_read_b128 a[100:103], v208 offset:40960\n" \
"v_mfma_f32_16x16x32_bf16 v[8:11], a[136:139], v[184:187], v[8:11]\n" \
"v_exp_f32_e64 v146, v146\n" \
"v_mfma_f32_16x16x32_bf16 v[12:15], a[172:175], v[192:195], v[12:15]\n" \
"ds_read_b128 a[104:107], v209 offset:40960\n" \
"v_mfma_f32_16x16x32_bf16 v[12:15], a[140:143], v[184:187], v[12:15]\n" \
"v_exp_f32_e64 v147, v147\n" \
"v_smfmac_f32_16x16x64_bf16 v[180:183], a[192:195], v[200:207], v228\n" \
"ds_read_b128 a[108:111], v210 offset:40960\n" \
"v_mfma_f32_16x16x32_bf16 v[152:155], a[64:67], a[48:51], v[172:175]\n" \
"v_exp_f32_e64 v148, v148\n" \
"v_mfma_f32_16x16x32_bf16 v[152:155], a[68:71], a[52:55], v[152:155]\n" \
"ds_read_b128 a[124:127], v210 offset:45056\n" \
"v_mfma_f32_16x16x32_bf16 v[152:155], a[72:75], a[56:59], v[152:155]\n" \
"v_exp_f32_e64 v149, v149\n" \
"v_mfma_f32_16x16x32_bf16 v[152:155], a[76:79], a[60:63], v[152:155]\n" \
"v_cvt_pk_bf16_f32 v216, v144, v145\n" \
"v_cvt_pk_bf16_f32 v217, v146, v147\n" \
"v_mfma_f32_16x16x32_bf16 v[156:159], a[92:95], a[60:63], v[172:175]\n" \
"v_exp_f32_e64 v150, v150\n" \
"v_mfma_f32_16x16x32_bf16 v[156:159], a[88:91], a[56:59], v[156:159]\n" \
"ds_read_b128 a[120:123], v209 offset:45056\n" \
"s_waitcnt lgkmcnt(6)\n" \
"v_mfma_f32_16x16x32_bf16 v[156:159], a[84:87], a[52:55], v[156:159]\n" \
"buffer_load_dwordx4 v214, %[srdk], s48 offen offset:3072 lds\n" \
"v_mfma_f32_16x16x32_bf16 v[156:159], a[80:83], a[48:51], v[156:159]\n" \
"v_exp_f32_e32 v151, v151\n" \
"v_mfma_f32_16x16x32_bf16 v[16:19], a[176:179], v[192:195], v[16:19]\n" \
"ds_read_b128 a[116:119], v208 offset:45056\n" \
"v_mfma_f32_16x16x32_bf16 v[16:19], a[144:147], v[184:187], v[16:19]\n" \
"v_exp_f32_e64 v152, v152\n" \
"v_mfma_f32_16x16x32_bf16 v[20:23], a[180:183], v[192:195], v[20:23]\n" \
"v_cvt_pk_bf16_f32 v218, v148, v149\n" \
"v_cvt_pk_bf16_f32 v219, v150, v151\n" \
"v_mfma_f32_16x16x32_bf16 v[20:23], a[148:151], v[184:187], v[20:23]\n" \
"v_exp_f32_e64 v153, v153\n" \
"v_mfma_f32_16x16x32_bf16 v[24:27], a[184:187], v[192:195], v[24:27]\n" \
"v_exp_f32_e64 v154, v154\n" \
"v_mfma_f32_16x16x32_bf16 v[24:27], a[152:155], v[184:187], v[24:27]\n" \
"ds_read_b128 a[112:115], %[kb0] offset:45056\n" \
"s_add_u32 s48, s48, 0x4000\n" \
"v_mfma_f32_16x16x32_bf16 v[28:31], a[188:191], v[192:195], v[28:31]\n" \
"v_exp_f32_e64 v155, v155\n" \
"v_mfma_f32_16x16x32_bf16 v[28:31], a[156:159], v[184:187], v[28:31]\n" \
"s_waitcnt vmcnt(14) lgkmcnt(2)\n" \
"s_barrier\n" \
"v_mfma_f32_16x16x32_bf16 v[128:131], a[96:99], a[0:3], v[160:163]\n" \
"v_exp_f32_e64 v156, v156\n" \
"v_mfma_f32_16x16x32_bf16 v[128:131], a[100:103], a[4:7], v[128:131]\n" \
"s_add_u32 m0, %[m0b], 81920\n" \
"v_mfma_f32_16x16x32_bf16 v[128:131], a[104:107], a[8:11], v[128:131]\n" \
"v_exp_f32_e64 v157, v157\n" \
"v_mfma_f32_16x16x32_bf16 v[128:131], a[108:111], a[12:15], v[128:131]\n" \
"v_cvt_pk_bf16_f32 v220, v152, v153\n" \
"v_cvt_pk_bf16_f32 v221, v154, v155\n" \
"v_mfma_f32_16x16x32_bf16 v[132:135], a[124:127], a[12:15], v[160:163]\n" \
"v_exp_f32_e64 v158, v158\n" \
"v_mfma_f32_16x16x32_bf16 v[132:135], a[120:123], a[8:11], v[132:135]\n" \
"s_waitcnt lgkmcnt(0)\n" \
"v_mfma_f32_16x16x32_bf16 v[132:135], a[116:119], a[4:7], v[132:135]\n" \
"buffer_load_dwordx4 %[vd0], %[srdv], s48 offen offset:0 lds\n" \
"v_mfma_f32_16x16x32_bf16 v[132:135], a[112:115], a[0:3], v[132:135]\n" \
"v_exp_f32_e32 v159, v159\n" \
"v_mfma_f32_16x16x32_bf16 v[32:35], a[160:163], v[196:199], v[32:35]\n" \
"ds_read_b128 a[64:67], %[kb0] offset:49152\n" \
"v_mfma_f32_16x16x32_bf16 v[32:35], a[128:131], v[188:191], v[32:35]\n" \
"v_exp_f32_e64 v128, v128\n" \
"v_mfma_f32_16x16x32_bf16 v[36:39], a[164:167], v[196:199], v[36:39]\n" \
"v_cvt_pk_bf16_f32 v222, v156, v157\n" \
"v_cvt_pk_bf16_f32 v223, v158, v159\n" \
"v_mfma_f32_16x16x32_bf16 v[36:39], a[132:135], v[188:191], v[36:39]\n" \
"v_exp_f32_e64 v129, v129\n" \
"v_mfma_f32_16x16x32_bf16 v[40:43], a[168:171], v[196:199], v[40:43]\n" \
"ds_read_b128 a[68:71], v208 offset:49152\n" \
"v_mfma_f32_16x16x32_bf16 v[40:43], a[136:139], v[188:191], v[40:43]\n" \
"v_exp_f32_e64 v130, v130\n" \
"v_mfma_f32_16x16x32_bf16 v[44:47], a[172:175], v[196:199], v[44:47]\n" \
"ds_read_b128 a[72:75], v209 offset:49152\n" \
"v_mfma_f32_16x16x32_bf16 v[44:47], a[140:143], v[188:191], v[44:47]\n" \
"v_exp_f32_e64 v131, v131\n" \
"v_smfmac_f32_16x16x64_bf16 v[176:179], a[192:195], v[184:191], v228\n" \
"v_mfma_f32_16x16x32_bf16 v[136:139], a[96:99], a[16:19], v[164:167]\n" \
"v_exp_f32_e64 v132, v132\n" \
"v_mfma_f32_16x16x32_bf16 v[136:139], a[100:103], a[20:23], v[136:139]\n" \
"ds_read_b128 a[76:79], v210 offset:49152\n" \
"v_mfma_f32_16x16x32_bf16 v[136:139], a[104:107], a[24:27], v[136:139]\n" \
"v_exp_f32_e64 v133, v133\n" \
"v_mfma_f32_16x16x32_bf16 v[136:139], a[108:111], a[28:31], v[136:139]\n" \
"v_cvt_pk_bf16_f32 v192, v128, v129\n" \
"v_cvt_pk_bf16_f32 v193, v130, v131\n" \
"v_mfma_f32_16x16x32_bf16 v[140:143], a[124:127], a[28:31], v[164:167]\n" \
"v_exp_f32_e64 v134, v134\n" \
"v_mfma_f32_16x16x32_bf16 v[140:143], a[120:123], a[24:27], v[140:143]\n" \
"ds_read_b128 a[92:95], v210 offset:53248\n" \
"v_mfma_f32_16x16x32_bf16 v[140:143], a[116:119], a[20:23], v[140:143]\n" \
"buffer_load_dwordx4 v215, %[srdv], s48 offen offset:1024 lds\n" \
"v_mfma_f32_16x16x32_bf16 v[140:143], a[112:115], a[16:19], v[140:143]\n" \
"v_exp_f32_e64 v135, v135\n" \
"v_mfma_f32_16x16x32_bf16 v[48:51], a[176:179], v[196:199], v[48:51]\n" \
"ds_read_b128 a[88:91], v209 offset:53248\n" \
"v_mfma_f32_16x16x32_bf16 v[48:51], a[144:147], v[188:191], v[48:51]\n" \
"v_exp_f32_e64 v136, v136\n" \
"v_mfma_f32_16x16x32_bf16 v[52:55], a[180:183], v[196:199], v[52:55]\n" \
"v_cvt_pk_bf16_f32 v194, v132, v133\n" \
"v_cvt_pk_bf16_f32 v195, v134, v135\n" \
"v_mfma_f32_16x16x32_bf16 v[52:55], a[148:151], v[188:191], v[52:55]\n" \
"v_exp_f32_e64 v137, v137\n" \
"v_mfma_f32_16x16x32_bf16 v[56:59], a[184:187], v[196:199], v[56:59]\n" \
"v_exp_f32_e64 v138, v138\n" \
"v_mfma_f32_16x16x32_bf16 v[56:59], a[152:155], v[188:191], v[56:59]\n" \
"ds_read_b128 a[84:87], v208 offset:53248\n" \
"v_mfma_f32_16x16x32_bf16 v[60:63], a[188:191], v[196:199], v[60:63]\n" \
"v_exp_f32_e64 v139, v139\n" \
"v_mfma_f32_16x16x32_bf16 v[60:63], a[156:159], v[188:191], v[60:63]\n" \
"s_cmp_lg_u32 %[wid], 1\n" \
"s_cbranch_scc1 LtA5_%=\n" \
"global_load_dword v237, v224, s[58:59]\n" \
"s_add_u32 s61, s48, 49152\n" \
"s_mov_b32 exec_hi, 0\n" \
"s_nop 0\n" \
"buffer_load_dword v237, v224, %[srdp], s61 offen\n" \
"s_mov_b32 exec_hi, -1\n" \
"s_nop 0\n" \
"LtA5_%=:\n" \
"v_mfma_f32_16x16x32_bf16 v[144:147], a[96:99], a[32:35], v[168:171]\n" \
"v_exp_f32_e64 v140, v140\n" \
"v_mfma_f32_16x16x32_bf16 v[144:147], a[100:103], a[36:39], v[144:147]\n" \
"ds_read_b128 a[80:83], %[kb0] offset:53248\n" \
"v_mfma_f32_16x16x32_bf16 v[144:147], a[104:107], a[40:43], v[144:147]\n" \
"v_exp_f32_e64 v141, v141\n" \
"v_mfma_f32_16x16x32_bf16 v[144:147], a[108:111], a[44:47], v[144:147]\n" \
"v_cvt_pk_bf16_f32 v196, v136, v137\n" \
"v_cvt_pk_bf16_f32 v197, v138, v139\n" \
"v_mfma_f32_16x16x32_bf16 v[148:151], a[124:127], a[44:47], v[168:171]\n" \
"v_exp_f32_e64 v142, v142\n" \
"v_mfma_f32_16x16x32_bf16 v[148:151], a[120:123], a[40:43], v[148:151]\n" \
"v_mfma_f32_16x16x32_bf16 v[148:151], a[116:119], a[36:39], v[148:151]\n" \
"buffer_load_dwordx4 %[vd0], %[srdv], s48 offen offset:2048 lds\n" \
"v_mfma_f32_16x16x32_bf16 v[148:151], a[112:115], a[32:35], v[148:151]\n" \
"v_exp_f32_e64 v143, v143\n" \
"v_mfma_f32_16x16x32_bf16 v[96:99], a[160:163], v[204:207], v[96:99]\n" \
"ds_read_b64_tr_b16 a[160:161], %[vvb] offset:40960\n" \
"v_mfma_f32_16x16x32_bf16 v[96:99], a[128:131], v[220:223], v[96:99]\n" \
"v_exp_f32_e64 v144, v144\n" \
"v_mfma_f32_16x16x32_bf16 v[100:103], a[164:167], v[204:207], v[100:103]\n" \
"v_cvt_pk_bf16_f32 v198, v140, v141\n" \
"v_cvt_pk_bf16_f32 v199, v142, v143\n" \
"v_mfma_f32_16x16x32_bf16 v[100:103], a[132:135], v[220:223], v[100:103]\n" \
"v_exp_f32_e64 v145, v145\n" \
"v_mfma_f32_16x16x32_bf16 v[104:107], a[168:171], v[204:207], v[104:107]\n" \
"ds_read_b64_tr_b16 a[162:163], %[vvb] offset:45056\n" \
"ds_read_b64_tr_b16 a[164:165], v211 offset:40960\n" \
"v_mfma_f32_16x16x32_bf16 v[104:107], a[136:139], v[220:223], v[104:107]\n" \
"v_exp_f32_e64 v146, v146\n" \
"v_mfma_f32_16x16x32_bf16 v[108:111], a[172:175], v[204:207], v[108:111]\n" \
"ds_read_b64_tr_b16 a[166:167], v211 offset:45056\n" \
"v_mfma_f32_16x16x32_bf16 v[108:111], a[140:143], v[220:223], v[108:111]\n" \
".if . - adaln_body != 16384\n" \
".error \"size model\"\n" \
".endif\n" \
"Lk3_%=:\n" \
"s_cbranch_execz Lk4_%=\n" \
"s_nop 0\n" \
"v_exp_f32_e64 v147, v147\n" \
"v_smfmac_f32_16x16x64_bf16 v[180:183], a[192:195], v[216:223], v228\n" \
"ds_read_b64_tr_b16 a[168:169], %[vvb] offset:41472\n" \
"v_mfma_f32_16x16x32_bf16 v[152:155], a[96:99], a[48:51], v[172:175]\n" \
"v_exp_f32_e64 v148, v148\n" \
"v_mfma_f32_16x16x32_bf16 v[152:155], a[100:103], a[52:55], v[152:155]\n" \
"ds_read_b64_tr_b16 a[170:171], %[vvb] offset:45568\n" \
"v_mfma_f32_16x16x32_bf16 v[152:155], a[104:107], a[56:59], v[152:155]\n" \
"v_exp_f32_e64 v149, v149\n" \
"v_mfma_f32_16x16x32_bf16 v[152:155], a[108:111], a[60:63], v[152:155]\n" \
"v_cvt_pk_bf16_f32 v200, v144, v145\n" \
"v_cvt_pk_bf16_f32 v201, v146, v147\n" \
"v_mfma_f32_16x16x32_bf16 v[156:159], a[124:127], a[60:63], v[172:175]\n" \
"v_exp_f32_e64 v150, v150\n" \
"v_mfma_f32_16x16x32_bf16 v[156:159], a[120:123], a[56:59], v[156:159]\n" \
"ds_read_b64_tr_b16 a[172:173], v211 offset:41472\n" \
"v_mfma_f32_16x16x32_bf16 v[156:159], a[116:119], a[52:55], v[156:159]\n" \
"buffer_load_dwordx4 v215, %[srdv], s48 offen offset:3072 lds\n" \
"v_mfma_f32_16x16x32_bf16 v[156:159], a[112:115], a[48:51], v[156:159]\n" \
"v_exp_f32_e64 v151, v151\n" \
"v_mfma_f32_16x16x32_bf16 v[112:115], a[176:179], v[204:207], v[112:115]\n" \
"ds_read_b64_tr_b16 a[174:175], v211 offset:45568\n" \
"ds_read_b64_tr_b16 a[176:177], %[vvb] offset:41984\n" \
"v_mfma_f32_16x16x32_bf16 v[112:115], a[144:147], v[220:223], v[112:115]\n" \
"v_exp_f32_e64 v152, v152\n" \
"v_mfma_f32_16x16x32_bf16 v[116:119], a[180:183], v[204:207], v[116:119]\n" \
"v_cvt_pk_bf16_f32 v202, v148, v149\n" \
"v_cvt_pk_bf16_f32 v203, v150, v151\n" \
"v_mfma_f32_16x16x32_bf16 v[116:119], a[148:151], v[220:223], v[116:119]\n" \
"v_exp_f32_e64 v153, v153\n" \
"v_mfma_f32_16x16x32_bf16 v[120:123], a[184:187], v[204:207], v[120:123]\n" \
"v_exp_f32_e64 v154, v154\n" \
"v_mfma_f32_16x16x32_bf16 v[120:123], a[152:155], v[220:223], v[120:123]\n" \
"ds_read_b64_tr_b16 a[178:179], %[vvb] offset:46080\n" \
"v_mfma_f32_16x16x32_bf16 v[124:127], a[188:191], v[204:207], v[124:127]\n" \
"v_exp_f32_e64 v155, v155\n" \
"v_mfma_f32_16x16x32_bf16 v[124:127], a[156:159], v[220:223], v[124:127]\n" \
"ds_read_b64_tr_b16 a[180:181], v211 offset:41984\n" \
"s_waitcnt lgkmcnt(13)\n" \
"v_mfma_f32_16x16x32_bf16 v[128:131], a[64:67], a[0:3], v[160:163]\n" \
"v_exp_f32_e32 v156, v156\n" \
"v_mfma_f32_16x16x32_bf16 v[128:131], a[68:71], a[4:7], v[128:131]\n" \
"ds_read_b64_tr_b16 a[182:183], v211 offset:46080\n" \
"s_add_u32 m0, %[m0b], 32768\n" \
"v_mfma_f32_16x16x32_bf16 v[128:131], a[72:75], a[8:11], v[128:131]\n" \
"v_exp_f32_e64 v157, v157\n" \
"v_mfma_f32_16x16x32_bf16 v[128:131], a[76:79], a[12:15], v[128:131]\n" \
"v_cvt_pk_bf16_f32 v204, v152, v153\n" \
"v_cvt_pk_bf16_f32 v205, v154, v155\n" \
"v_mfma_f32_16x16x32_bf16 v[132:135], a[92:95], a[12:15], v[160:163]\n" \
"v_exp_f32_e64 v158, v158\n" \
"v_mfma_f32_16x16x32_bf16 v[132:135], a[88:91], a[8:11], v[132:135]\n" \
"ds_read_b64_tr_b16 a[184:185], %[vvb] offset:42496\n" \
"s_waitcnt lgkmcnt(5)\n" \
"v_mfma_f32_16x16x32_bf16 v[132:135], a[84:87], a[4:7], v[132:135]\n" \
"buffer_load_dwordx4 %[kd0], %[srdk], s48 offen offset:0 lds\n" \
"v_mfma_f32_16x16x32_bf16 v[132:135], a[80:83], a[0:3], v[132:135]\n" \
"v_exp_f32_e32 v159, v159\n" \
"v_mfma_f32_16x16x32_bf16 v[64:67], a[128:131], v[216:219], v[64:67]\n" \
"ds_read_b64_tr_b16 a[186:187], %[vvb] offset:46592\n" \
"ds_read_b64_tr_b16 a[188:189], v211 offset:42496\n" \
"v_mfma_f32_16x16x32_bf16 v[64:67], a[160:163], v[200:203], v[64:67]\n" \
"v_exp_f32_e64 v128, v128\n" \
"v_mfma_f32_16x16x32_bf16 v[68:71], a[132:135], v[216:219], v[68:71]\n" \
"v_cvt_pk_bf16_f32 v206, v156, v157\n" \
"v_cvt_pk_bf16_f32 v207, v158, v159\n" \
"v_mfma_f32_16x16x32_bf16 v[68:71], a[164:167], v[200:203], v[68:71]\n" \
"v_exp_f32_e64 v129, v129\n" \
"v_mfma_f32_16x16x32_bf16 v[72:75], a[136:139], v[216:219], v[72:75]\n" \
"ds_read_b64_tr_b16 a[190:191], v211 offset:46592\n" \
"ds_read_b64_tr_b16 a[128:129], %[vvb] offset:49152\n" \
"v_mfma_f32_16x16x32_bf16 v[72:75], a[168:171], v[200:203], v[72:75]\n" \
"v_exp_f32_e64 v130, v130\n" \
"v_mfma_f32_16x16x32_bf16 v[76:79], a[140:143], v[216:219], v[76:79]\n" \
"ds_read_b64_tr_b16 a[130:131], %[vvb] offset:53248\n" \
"ds_read_b64_tr_b16 a[132:133], v211 offset:49152\n" \
"v_mfma_f32_16x16x32_bf16 v[76:79], a[172:175], v[200:203], v[76:79]\n" \
"v_exp_f32_e64 v131, v131\n" \
"v_smfmac_f32_16x16x64_bf16 v[176:179], a[192:195], v[192:199], v228\n" \
"ds_read_b64_tr_b16 a[134:135], v211 offset:53248\n" \
"ds_read_b64_tr_b16 a[136:137], %[vvb] offset:49664\n" \
"v_mfma_f32_16x16x32_bf16 v[136:139], a[64:67], a[16:19], v[164:167]\n" \
"v_exp_f32_e64 v132, v132\n" \
"v_mfma_f32_16x16x32_bf16 v[136:139], a[68:71], a[20:23], v[136:139]\n" \
"ds_read_b64_tr_b16 a[138:139], %[vvb] offset:53760\n" \
"ds_read_b64_tr_b16 a[140:141], v211 offset:49664\n" \
"v_mfma_f32_16x16x32_bf16 v[136:139], a[72:75], a[24:27], v[136:139]\n" \
"v_exp_f32_e64 v133, v133\n" \
"v_mfma_f32_16x16x32_bf16 v[136:139], a[76:79], a[28:31], v[136:139]\n" \
"v_cvt_pk_bf16_f32 v184, v128, v129\n" \
"v_cvt_pk_bf16_f32 v185, v130, v131\n" \
"v_mfma_f32_16x16x32_bf16 v[140:143], a[92:95], a[28:31], v[164:167]\n" \
"v_exp_f32_e64 v134, v134\n" \
"v_mfma_f32_16x16x32_bf16 v[140:143], a[88:91], a[24:27], v[140:143]\n" \
"ds_read_b64_tr_b16 a[142:143], v211 offset:53760\n" \
"s_waitcnt lgkmcnt(8)\n" \
"v_mfma_f32_16x16x32_bf16 v[140:143], a[84:87], a[20:23], v[140:143]\n" \
"buffer_load_dwordx4 v212, %[srdk], s48 offen offset:1024 lds\n" \
"v_mfma_f32_16x16x32_bf16 v[140:143], a[80:83], a[16:19], v[140:143]\n" \
"v_exp_f32_e32 v135, v135\n" \
"v_mfma_f32_16x16x32_bf16 v[80:83], a[144:147], v[216:219], v[80:83]\n" \
"ds_read_b64_tr_b16 a[144:145], %[vvb] offset:50176\n" \
"ds_read_b64_tr_b16 a[146:147], %[vvb] offset:54272\n" \
"v_mfma_f32_16x16x32_bf16 v[80:83], a[176:179], v[200:203], v[80:83]\n" \
"v_exp_f32_e64 v136, v136\n" \
"v_mfma_f32_16x16x32_bf16 v[84:87], a[148:151], v[216:219], v[84:87]\n" \
"v_cvt_pk_bf16_f32 v186, v132, v133\n" \
"v_cvt_pk_bf16_f32 v187, v134, v135\n" \
"v_mfma_f32_16x16x32_bf16 v[84:87], a[180:183], v[200:203], v[84:87]\n" \
"v_exp_f32_e64 v137, v137\n" \
"v_mfma_f32_16x16x32_bf16 v[88:91], a[152:155], v[216:219], v[88:91]\n" \
"v_exp_f32_e64 v138, v138\n" \
"v_mfma_f32_16x16x32_bf16 v[88:91], a[184:187], v[200:203], v[88:91]\n" \
"ds_read_b64_tr_b16 a[148:149], v211 offset:50176\n" \
"ds_read_b64_tr_b16 a[150:151], v211 offset:54272\n" \
"v_mfma_f32_16x16x32_bf16 v[92:95], a[156:159], v[216:219], v[92:95]\n" \
"v_exp_f32_e64 v139, v139\n" \
"v_mfma_f32_16x16x32_bf16 v[92:95], a[188:191], v[200:203], v[92:95]\n" \
"ds_read_b64_tr_b16 a[152:153], %[vvb] offset:50688\n" \
"global_load_dword v237, %[vct], %[pct]\n" \
"v_mfma_f32_16x16x32_bf16 v[144:147], a[64:67], a[32:35], v[168:171]\n" \
"v_exp_f32_e64 v140, v140\n" \
"v_mfma_f32_16x16x32_bf16 v[144:147], a[68:71], a[36:39], v[144:147]\n" \
"ds_read_b64_tr_b16 a[154:155], %[vvb] offset:54784\n" \
"ds_read_b64_tr_b16 a[156:157], v211 offset:50688\n" \
"v_mfma_f32_16x16x32_bf16 v[144:147], a[72:75], a[40:43], v[144:147]\n" \
"v_exp_f32_e64 v141, v141\n" \
"v_mfma_f32_16x16x32_bf16 v[144:147], a[76:79], a[44:47], v[144:147]\n" \
"v_cvt_pk_bf16_f32 v188, v136, v137\n" \
"v_cvt_pk_bf16_f32 v189, v138, v139\n" \
"v_mfma_f32_16x16x32_bf16 v[148:151], a[92:95], a[44:47], v[168:171]\n" \
"v_exp_f32_e64 v142, v142\n" \
"v_mfma_f32_16x16x32_bf16 v[148:151], a[88:91], a[40:43], v[148:151]\n" \
"ds_read_b64_tr_b16 a[158:159], v211 offset:54784\n" \
"s_waitcnt lgkmcnt(8)\n" \
"v_mfma_f32_16x16x32_bf16 v[148:151], a[84:87], a[36:39], v[148:151]\n" \
"buffer_load_dwordx4 v213, %[srdk], s48 offen offset:2048 lds\n" \
"v_mfma_f32_16x16x32_bf16 v[148:151], a[80:83], a[32:35], v[148:151]\n" \
"v_exp_f32_e32 v143, v143\n" \
"v_mfma_f32_16x16x32_bf16 v[0:3], a[160:163], v[192:195], v[0:3]\n" \
"ds_read_b128 a[96:99], %[kb0] offset:57344\n" \
"v_mfma_f32_16x16x32_bf16 v[0:3], a[128:131], v[184:187], v[0:3]\n" \
"v_exp_f32_e64 v144, v144\n" \
"v_mfma_f32_16x16x32_bf16 v[4:7], a[164:167], v[192:195], v[4:7]\n" \
"v_cvt_pk_bf16_f32 v190, v140, v141\n" \
"v_cvt_pk_bf16_f32 v191, v142, v143\n" \
"v_mfma_f32_16x16x32_bf16 v[4:7], a[132:135], v[184:187], v[4:7]\n" \
"v_exp_f32_e64 v145, v145\n" \
"v_mfma_f32_16x16x32_bf16 v[8:11], a[168:171], v[192:195], v[8:11]\n" \
"ds_read_b128 a[100:103], v208 offset:57344\n" \
"v_mfma_f32_16x16x32_bf16 v[8:11], a[136:139], v[184:187], v[8:11]\n" \
"v_exp_f32_e64 v146, v146\n" \
"v_mfma_f32_16x16x32_bf16 v[12:15], a[172:175], v[192:195], v[12:15]\n" \
"ds_read_b128 a[104:107], v209 offset:57344\n" \
"v_mfma_f32_16x16x32_bf16 v[12:15], a[140:143], v[184:187], v[12:15]\n" \
"v_exp_f32_e64 v147, v147\n" \
"v_smfmac_f32_16x16x64_bf16 v[180:183], a[192:195], v[200:207], v228\n" \
"ds_read_b128 a[108:111], v210 offset:57344\n" \
"v_mfma_f32_16x16x32_bf16 v[152:155], a[64:67], a[48:51], v[172:175]\n" \
"v_exp_f32_e64 v148, v148\n" \
"v_mfma_f32_16x16x32_bf16 v[152:155], a[68:71], a[52:55], v[152:155]\n" \
"ds_read_b128 a[124:127], v210 offset:61440\n" \
"v_mfma_f32_16x16x32_bf16 v[152:155], a[72:75], a[56:59], v[152:155]\n" \
"v_exp_f32_e64 v149, v149\n" \
"v_mfma_f32_16x16x32_bf16 v[152:155], a[76:79], a[60:63], v[152:155]\n" \
"v_cvt_pk_bf16_f32 v216, v144, v145\n" \
"v_cvt_pk_bf16_f32 v217, v146, v147\n" \
"v_mfma_f32_16x16x32_bf16 v[156:159], a[92:95], a[60:63], v[172:175]\n" \
"v_exp_f32_e64 v150, v150\n" \
"v_mfma_f32_16x16x32_bf16 v[156:159], a[88:91], a[56:59], v[156:159]\n" \
"ds_read_b128 a[120:123], v209 offset:61440\n" \
"s_waitcnt lgkmcnt(6)\n" \
"v_mfma_f32_16x16x32_bf16 v[156:159], a[84:87], a[52:55], v[156:159]\n" \
"buffer_load_dwordx4 v214, %[srdk], s48 offen offset:3072 lds\n" \
"v_mfma_f32_16x16x32_bf16 v[156:159], a[80:83], a[48:51], v[156:159]\n" \
"v_exp_f32_e32 v151, v151\n" \
"v_mfma_f32_16x16x32_bf16 v[16:19], a[176:179], v[192:195], v[16:19]\n" \
"ds_read_b128 a[116:119], v208 offset:61440\n" \
"v_mfma_f32_16x16x32_bf16 v[16:19], a[144:147], v[184:187], v[16:19]\n" \
"v_exp_f32_e64 v152, v152\n" \
"v_mfma_f32_16x16x32_bf16 v[20:23], a[180:183], v[192:195], v[20:23]\n" \
"v_cvt_pk_bf16_f32 v218, v148, v149\n" \
"v_cvt_pk_bf16_f32 v219, v150, v151\n" \
"v_mfma_f32_16x16x32_bf16 v[20:23], a[148:151], v[184:187], v[20:23]\n" \
"v_exp_f32_e64 v153, v153\n" \
"v_mfma_f32_16x16x32_bf16 v[24:27], a[184:187], v[192:195], v[24:27]\n" \
"v_exp_f32_e64 v154, v154\n" \
"v_mfma_f32_16x16x32_bf16 v[24:27], a[152:155], v[184:187], v[24:27]\n" \
"ds_read_b128 a[112:115], %[kb0] offset:61440\n" \
"s_add_u32 s48, s48, 0x4000\n" \
"s_mov_b32 m0, s44\n" \
"s_addk_i32 s44, 0x400\n" \
"buffer_load_dwordx4 v229, s[64:67], s45 offen offset:0 nt lds\n" \
"s_add_u32 s45, s45, 393216\n" \
"v_mfma_f32_16x16x32_bf16 v[28:31], a[188:191], v[192:195], v[28:31]\n" \
"v_exp_f32_e64 v155, v155\n" \
"v_mfma_f32_16x16x32_bf16 v[28:31], a[156:159], v[184:187], v[28:31]\n" \
"s_waitcnt vmcnt(15) lgkmcnt(2)\n" \
"s_barrier\n" \
"v_mfma_f32_16x16x32_bf16 v[128:131], a[96:99], a[0:3], v[160:163]\n" \
"v_exp_f32_e64 v156, v156\n" \
"v_mfma_f32_16x16x32_bf16 v[128:131], a[100:103], a[4:7], v[128:131]\n" \
"s_add_u32 m0, %[m0b], 98304\n" \
"v_mfma_f32_16x16x32_bf16 v[128:131], a[104:107], a[8:11], v[128:131]\n" \
"v_exp_f32_e64 v157, v157\n" \
"v_mfma_f32_16x16x32_bf16 v[128:131], a[108:111], a[12:15], v[128:131]\n" \
"v_cvt_pk_bf16_f32 v220, v152, v153\n" \
"v_cvt_pk_bf16_f32 v221, v154, v155\n" \
"v_mfma_f32_16x16x32_bf16 v[132:135], a[124:127], a[12:15], v[160:163]\n" \
"v_exp_f32_e64 v158, v158\n" \
"v_mfma_f32_16x16x32_bf16 v[132:135], a[120:123], a[8:11], v[132:135]\n" \
"s_waitcnt lgkmcnt(0)\n" \
"v_mfma_f32_16x16x32_bf16 v[132:135], a[116:119], a[4:7], v[132:135]\n" \
"buffer_load_dwordx4 %[vd0], %[srdv], s48 offen offset:0 lds\n" \
"v_mfma_f32_16x16x32_bf16 v[132:135], a[112:115], a[0:3], v[132:135]\n" \
"v_exp_f32_e32 v159, v159\n" \
"v_mfma_f32_16x16x32_bf16 v[32:35], a[160:163], v[196:199], v[32:35]\n" \
"ds_read_b128 a[64:67], %[kb0] offset:0\n" \
"v_mfma_f32_16x16x32_bf16 v[32:35], a[128:131], v[188:191], v[32:35]\n" \
"v_exp_f32_e64 v128, v128\n" \
"v_mfma_f32_16x16x32_bf16 v[36:39], a[164:167], v[196:199], v[36:39]\n" \
"v_cvt_pk_bf16_f32 v222, v156, v157\n" \
"v_cvt_pk_bf16_f32 v223, v158, v159\n" \
"v_mfma_f32_16x16x32_bf16 v[36:39], a[132:135], v[188:191], v[36:39]\n" \
"v_exp_f32_e64 v129, v129\n" \
"v_mfma_f32_16x16x32_bf16 v[40:43], a[168:171], v[196:199], v[40:43]\n" \
"ds_read_b128 a[68:71], v208 offset:0\n" \
"v_mfma_f32_16x16x32_bf16 v[40:43], a[136:139], v[188:191], v[40:43]\n" \
"v_exp_f32_e64 v130, v130\n" \
"v_mfma_f32_16x16x32_bf16 v[44:47], a[172:175], v[196:199], v[44:47]\n" \
"ds_read_b128 a[72:75], v209 offset:0\n" \
"v_mfma_f32_16x16x32_bf16 v[44:47], a[140:143], v[188:191], v[44:47]\n" \
"v_exp_f32_e64 v131, v131\n" \
"v_smfmac_f32_16x16x64_bf16 v[176:179], a[192:195], v[184:191], v228\n" \
"v_mfma_f32_16x16x32_bf16 v[136:139], a[96:99], a[16:19], v[164:167]\n" \
"v_exp_f32_e64 v132, v132\n" \
"v_mfma_f32_16x16x32_bf16 v[136:139], a[100:103], a[20:23], v[136:139]\n" \
"ds_read_b128 a[76:79], v210 offset:0\n" \
"v_mfma_f32_16x16x32_bf16 v[136:139], a[104:107], a[24:27], v[136:139]\n" \
"v_exp_f32_e64 v133, v133\n" \
"v_mfma_f32_16x16x32_bf16 v[136:139], a[108:111], a[28:31], v[136:139]\n" \
"v_cvt_pk_bf16_f32 v192, v128, v129\n" \
"v_cvt_pk_bf16_f32 v193, v130, v131\n" \
"v_mfma_f32_16x16x32_bf16 v[140:143], a[124:127], a[28:31], v[164:167]\n" \
"v_exp_f32_e64 v134, v134\n" \
"v_mfma_f32_16x16x32_bf16 v[140:143], a[120:123], a[24:27], v[140:143]\n" \
"ds_read_b128 a[92:95], v210 offset:4096\n" \
"v_mfma_f32_16x16x32_bf16 v[140:143], a[116:119], a[20:23], v[140:143]\n" \
"buffer_load_dwordx4 v215, %[srdv], s48 offen offset:1024 lds\n" \
"v_mfma_f32_16x16x32_bf16 v[140:143], a[112:115], a[16:19], v[140:143]\n" \
"v_exp_f32_e64 v135, v135\n" \
"v_mfma_f32_16x16x32_bf16 v[48:51], a[176:179], v[196:199], v[48:51]\n" \
"ds_read_b128 a[88:91], v209 offset:4096\n" \
"v_mfma_f32_16x16x32_bf16 v[48:51], a[144:147], v[188:191], v[48:51]\n" \
"v_exp_f32_e64 v136, v136\n" \
"v_mfma_f32_16x16x32_bf16 v[52:55], a[180:183], v[196:199], v[52:55]\n" \
"v_cvt_pk_bf16_f32 v194, v132, v133\n" \
"v_cvt_pk_bf16_f32 v195, v134, v135\n" \
"v_mfma_f32_16x16x32_bf16 v[52:55], a[148:151], v[188:191], v[52:55]\n" \
"v_exp_f32_e64 v137, v137\n" \
"v_mfma_f32_16x16x32_bf16 v[56:59], a[184:187], v[196:199], v[56:59]\n" \
"v_exp_f32_e64 v138, v138\n" \
"v_mfma_f32_16x16x32_bf16 v[56:59], a[152:155], v[188:191], v[56:59]\n" \
"ds_read_b128 a[84:87], v208 offset:4096\n" \
"v_mfma_f32_16x16x32_bf16 v[60:63], a[188:191], v[196:199], v[60:63]\n" \
"v_exp_f32_e64 v139, v139\n" \
"v_mfma_f32_16x16x32_bf16 v[60:63], a[156:159], v[188:191], v[60:63]\n" \
"s_cmp_lg_u32 %[wid], 2\n" \
"s_cbranch_scc1 LtA7_%=\n" \
"global_load_dword v237, v224, s[58:59]\n" \
"s_add_u32 s61, s48, 49152\n" \
"s_mov_b32 exec_hi, 0\n" \
"s_nop 0\n" \
"buffer_load_dword v237, v224, %[srdp], s61 offen\n" \
"s_mov_b32 exec_hi, -1\n" \
"s_nop 0\n" \
"LtA7_%=:\n" \
"v_mfma_f32_16x16x32_bf16 v[144:147], a[96:99], a[32:35], v[168:171]\n" \
"v_exp_f32_e64 v140, v140\n" \
"v_mfma_f32_16x16x32_bf16 v[144:147], a[100:103], a[36:39], v[144:147]\n" \
"ds_read_b128 a[80:83], %[kb0] offset:4096\n" \
"v_mfma_f32_16x16x32_bf16 v[144:147], a[104:107], a[40:43], v[144:147]\n" \
"v_exp_f32_e64 v141, v141\n" \
"v_mfma_f32_16x16x32_bf16 v[144:147], a[108:111], a[44:47], v[144:147]\n" \
"v_cvt_pk_bf16_f32 v196, v136, v137\n" \
"v_cvt_pk_bf16_f32 v197, v138, v139\n" \
"v_mfma_f32_16x16x32_bf16 v[148:151], a[124:127], a[44:47], v[168:171]\n" \
"v_exp_f32_e64 v142, v142\n" \
"v_mfma_f32_16x16x32_bf16 v[148:151], a[120:123], a[40:43], v[148:151]\n" \
"v_mfma_f32_16x16x32_bf16 v[148:151], a[116:119], a[36:39], v[148:151]\n" \
"buffer_load_dwordx4 %[vd0], %[srdv], s48 offen offset:2048 lds\n" \
"v_mfma_f32_16x16x32_bf16 v[148:151], a[112:115], a[32:35], v[148:151]\n" \
"v_exp_f32_e64 v143, v143\n" \
"v_mfma_f32_16x16x32_bf16 v[96:99], a[160:163], v[204:207], v[96:99]\n" \
"ds_read_b64_tr_b16 a[160:161], %[vvb] offset:57344\n" \
"v_mfma_f32_16x16x32_bf16 v[96:99], a[128:131], v[220:223], v[96:99]\n" \
"v_exp_f32_e64 v144, v144\n" \
"v_mfma_f32_16x16x32_bf16 v[100:103], a[164:167], v[204:207], v[100:103]\n" \
"v_cvt_pk_bf16_f32 v198, v140, v141\n" \
"v_cvt_pk_bf16_f32 v199, v142, v143\n" \
"v_mfma_f32_16x16x32_bf16 v[100:103], a[132:135], v[220:223], v[100:103]\n" \
"v_exp_f32_e64 v145, v145\n" \
"v_mfma_f32_16x16x32_bf16 v[104:107], a[168:171], v[204:207], v[104:107]\n" \
"ds_read_b64_tr_b16 a[162:163], %[vvb] offset:61440\n" \
"ds_read_b64_tr_b16 a[164:165], v211 offset:57344\n" \
"v_mfma_f32_16x16x32_bf16 v[104:107], a[136:139], v[220:223], v[104:107]\n" \
"v_exp_f32_e64 v146, v146\n" \
"v_mfma_f32_16x16x32_bf16 v[108:111], a[172:175], v[204:207], v[108:111]\n" \
"ds_read_b64_tr_b16 a[166:167], v211 offset:61440\n" \
"v_mfma_f32_16x16x32_bf16 v[108:111], a[140:143], v[220:223], v[108:111]\n" \
"v_exp_f32_e64 v147, v147\n" \
"v_smfmac_f32_16x16x64_bf16 v[180:183], a[192:195], v[216:223], v228\n" \
"ds_read_b64_tr_b16 a[168:169], %[vvb] offset:57856\n" \
"v_mfma_f32_16x16x32_bf16 v[152:155], a[96:99], a[48:51], v[172:175]\n" \
"v_exp_f32_e64 v148, v148\n" \
"v_mfma_f32_16x16x32_bf16 v[152:155], a[100:103], a[52:55], v[152:155]\n" \
"ds_read_b64_tr_b16 a[170:171], %[vvb] offset:61952\n" \
"v_mfma_f32_16x16x32_bf16 v[152:155], a[104:107], a[56:59], v[152:155]\n" \
"v_exp_f32_e64 v149, v149\n" \
"v_mfma_f32_16x16x32_bf16 v[152:155], a[108:111], a[60:63], v[152:155]\n" \
"v_cvt_pk_bf16_f32 v200, v144, v145\n" \
"v_cvt_pk_bf16_f32 v201, v146, v147\n" \
"v_mfma_f32_16x16x32_bf16 v[156:159], a[124:127], a[60:63], v[172:175]\n" \
"v_exp_f32_e64 v150, v150\n" \
"v_mfma_f32_16x16x32_bf16 v[156:159], a[120:123], a[56:59], v[156:159]\n" \
"ds_read_b64_tr_b16 a[172:173], v211 offset:57856\n" \
"v_mfma_f32_16x16x32_bf16 v[156:159], a[116:119], a[52:55], v[156:159]\n" \
"buffer_load_dwordx4 v215, %[srdv], s48 offen offset:3072 lds\n" \
"v_mfma_f32_16x16x32_bf16 v[156:159], a[112:115], a[48:51], v[156:159]\n" \
"v_exp_f32_e64 v151, v151\n" \
"v_mfma_f32_16x16x32_bf16 v[112:115], a[176:179], v[204:207], v[112:115]\n" \
"ds_read_b64_tr_b16 a[174:175], v211 offset:61952\n" \
"ds_read_b64_tr_b16 a[176:177], %[vvb] offset:58368\n" \
"v_mfma_f32_16x16x32_bf16 v[112:115], a[144:147], v[220:223], v[112:115]\n" \
"v_exp_f32_e64 v152, v152\n" \
"v_mfma_f32_16x16x32_bf16 v[116:119], a[180:183], v[204:207], v[116:119]\n" \
"v_cvt_pk_bf16_f32 v202, v148, v149\n" \
"v_cvt_pk_bf16_f32 v203, v150, v151\n" \
"v_mfma_f32_16x16x32_bf16 v[116:119], a[148:151], v[220:223], v[116:119]\n" \
"v_exp_f32_e64 v153, v153\n" \
"v_mfma_f32_16x16x32_bf16 v[120:123], a[184:187], v[204:207], v[120:123]\n" \
"v_exp_f32_e64 v154, v154\n" \
"v_mfma_f32_16x16x32_bf16 v[120:123], a[152:155], v[220:223], v[120:123]\n" \
"ds_read_b64_tr_b16 a[178:179], %[vvb] offset:62464\n" \
"v_mfma_f32_16x16x32_bf16 v[124:127], a[188:191], v[204:207], v[124:127]\n" \
"v_exp_f32_e64 v155, v155\n" \
"v_mfma_f32_16x16x32_bf16 v[124:127], a[156:159], v[220:223], v[124:127]\n" \
"ds_read_b64_tr_b16 a[180:181], v211 offset:58368\n" \
"s_waitcnt lgkmcnt(13)\n" \
"v_mfma_f32_16x16x32_bf16 v[128:131], a[64:67], a[0:3], v[160:163]\n" \
"v_exp_f32_e32 v156, v156\n" \
"v_mfma_f32_16x16x32_bf16 v[128:131], a[68:71], a[4:7], v[128:131]\n" \
"ds_read_b64_tr_b16 a[182:183], v211 offset:62464\n" \
"s_add_u32 m0, %[m0b], 49152\n" \
"v_mfma_f32_16x16x32_bf16 v[128:131], a[72:75], a[8:11], v[128:131]\n" \
"v_exp_f32_e64 v157, v157\n" \
"v_mfma_f32_16x16x32_bf16 v[128:131], a[76:79], a[12:15], v[128:131]\n" \
"v_cvt_pk_bf16_f32 v204, v152, v153\n" \
"v_cvt_pk_bf16_f32 v205, v154, v155\n" \
"v_mfma_f32_16x16x32_bf16 v[132:135], a[92:95], a[12:15], v[160:163]\n" \
"v_exp_f32_e64 v158, v158\n" \
"v_mfma_f32_16x16x32_bf16 v[132:135], a[88:91], a[8:11], v[132:135]\n" \
"ds_read_b64_tr_b16 a[184:185], %[vvb] offset:58880\n" \
"s_waitcnt lgkmcnt(5)\n" \
"v_mfma_f32_16x16x32_bf16 v[132:135], a[84:87], a[4:7], v[132:135]\n" \
"buffer_load_dwordx4 %[kd0], %[srdk], s48 offen offset:0 lds\n" \
"v_mfma_f32_16x16x32_bf16 v[132:135], a[80:83], a[0:3], v[132:135]\n" \
"v_exp_f32_e32 v159, v159\n" \
"v_mfma_f32_16x16x32_bf16 v[64:67], a[128:131], v[216:219], v[64:67]\n" \
"ds_read_b64_tr_b16 a[186:187], %[vvb] offset:62976\n" \
"ds_read_b64_tr_b16 a[188:189], v211 offset:58880\n" \
"v_mfma_f32_16x16x32_bf16 v[64:67], a[160:163], v[200:203], v[64:67]\n" \
"v_exp_f32_e64 v128, v128\n" \
"v_mfma_f32_16x16x32_bf16 v[68:71], a[132:135], v[216:219], v[68:71]\n" \
"v_cvt_pk_bf16_f32 v206, v156, v157\n" \
"v_cvt_pk_bf16_f32 v207, v158, v159\n" \
"v_mfma_f32_16x16x32_bf16 v[68:71], a[164:167], v[200:203], v[68:71]\n" \
"v_exp_f32_e64 v129, v129\n" \
"v_mfma_f32_16x16x32_bf16 v[72:75], a[136:139], v[216:219], v[72:75]\n" \
"ds_read_b64_tr_b16 a[190:191], v211 offset:62976\n" \
"ds_read_b64_tr_b16 a[128:129], %[vvb] offset:0\n" \
"v_mfma_f32_16x16x32_bf16 v[72:75], a[168:171], v[200:203], v[72:75]\n" \
"v_exp_f32_e64 v130, v130\n" \
"v_mfma_f32_16x16x32_bf16 v[76:79], a[140:143], v[216:219], v[76:79]\n" \
"ds_read_b64_tr_b16 a[130:131], %[vvb] offset:4096\n" \
"ds_read_b64_tr_b16 a[132:133], v211 offset:0\n" \
"v_mfma_f32_16x16x32_bf16 v[76:79], a[172:175], v[200:203], v[76:79]\n" \
"v_exp_f32_e64 v131, v131\n" \
"v_smfmac_f32_16x16x64_bf16 v[176:179], a[192:195], v[192:199], v228\n" \
"ds_read_b64_tr_b16 a[134:135], v211 offset:4096\n" \
"ds_read_b64_tr_b16 a[136:137], %[vvb] offset:512\n" \
"v_mfma_f32_16x16x32_bf16 v[136:139], a[64:67], a[16:19], v[164:167]\n" \
"v_exp_f32_e64 v132, v132\n" \
"v_mfma_f32_16x16x32_bf16 v[136:139], a[68:71], a[20:23], v[136:139]\n" \
"ds_read_b64_tr_b16 a[138:139], %[vvb] offset:4608\n" \
"ds_read_b64_tr_b16 a[140:141], v211 offset:512\n" \
"v_mfma_f32_16x16x32_bf16 v[136:139], a[72:75], a[24:27], v[136:139]\n" \
"v_exp_f32_e64 v133, v133\n" \
"v_mfma_f32_16x16x32_bf16 v[136:139], a[76:79], a[28:31], v[136:139]\n" \
"v_cvt_pk_bf16_f32 v184, v128, v129\n" \
"v_cvt_pk_bf16_f32 v185, v130, v131\n" \
"v_mfma_f32_16x16x32_bf16 v[140:143], a[92:95], a[28:31], v[164:167]\n" \
"v_exp_f32_e64 v134, v134\n" \
"v_mfma_f32_16x16x32_bf16 v[140:143], a[88:91], a[24:27], v[140:143]\n" \
"ds_read_b64_tr_b16 a[142:143], v211 offset:4608\n" \
"s_waitcnt lgkmcnt(8)\n" \
"v_mfma_f32_16x16x32_bf16 v[140:143], a[84:87], a[20:23], v[140:143]\n" \
"buffer_load_dwordx4 v212, %[srdk], s48 offen offset:1024 lds\n" \
"v_mfma_f32_16x16x32_bf16 v[140:143], a[80:83], a[16:19], v[140:143]\n" \
"v_exp_f32_e32 v135, v135\n" \
"v_mfma_f32_16x16x32_bf16 v[80:83], a[144:147], v[216:219], v[80:83]\n" \
"ds_read_b64_tr_b16 a[144:145], %[vvb] offset:1024\n" \
"ds_read_b64_tr_b16 a[146:147], %[vvb] offset:5120\n" \
"v_mfma_f32_16x16x32_bf16 v[80:83], a[176:179], v[200:203], v[80:83]\n" \
"v_exp_f32_e64 v136, v136\n" \
"v_mfma_f32_16x16x32_bf16 v[84:87], a[148:151], v[216:219], v[84:87]\n" \
"v_cvt_pk_bf16_f32 v186, v132, v133\n" \
"v_cvt_pk_bf16_f32 v187, v134, v135\n" \
"v_mfma_f32_16x16x32_bf16 v[84:87], a[180:183], v[200:203], v[84:87]\n" \
"v_exp_f32_e64 v137, v137\n" \
"v_mfma_f32_16x16x32_bf16 v[88:91], a[152:155], v[216:219], v[88:91]\n" \
"v_exp_f32_e64 v138, v138\n" \
"v_mfma_f32_16x16x32_bf16 v[88:91], a[184:187], v[200:203], v[88:91]\n" \
"ds_read_b64_tr_b16 a[148:149], v211 offset:1024\n" \
"ds_read_b64_tr_b16 a[150:151], v211 offset:5120\n" \
"v_mfma_f32_16x16x32_bf16 v[92:95], a[156:159], v[216:219], v[92:95]\n" \
"v_exp_f32_e64 v139, v139\n" \
"v_mfma_f32_16x16x32_bf16 v[92:95], a[188:191], v[200:203], v[92:95]\n" \
"ds_read_b64_tr_b16 a[152:153], %[vvb] offset:1536\n" \
"buffer_store_dwordx4 a[196:199], v235, %[srdz], s62 offen offset:1024 sc1\n" \
"s_add_u32 s62, s62, 2048\n" \
"v_mfma_f32_16x16x32_bf16 v[144:147], a[64:67], a[32:35], v[168:171]\n" \
"v_exp_f32_e64 v140, v140\n" \
"v_mfma_f32_16x16x32_bf16 v[144:147], a[68:71], a[36:39], v[144:147]\n" \
"ds_read_b64_tr_b16 a[154:155], %[vvb] offset:5632\n" \
"ds_read_b64_tr_b16 a[156:157], v211 offset:1536\n" \
"v_mfma_f32_16x16x32_bf16 v[144:147], a[72:75], a[40:43], v[144:147]\n" \
"v_exp_f32_e64 v141, v141\n" \
"v_mfma_f32_16x16x32_bf16 v[144:147], a[76:79], a[44:47], v[144:147]\n" \
"v_cvt_pk_bf16_f32 v188, v136, v137\n" \
"v_cvt_pk_bf16_f32 v189, v138, v139\n" \
"v_mfma_f32_16x16x32_bf16 v[148:151], a[92:95], a[44:47], v[168:171]\n" \
"v_exp_f32_e64 v142, v142\n" \
"v_mfma_f32_16x16x32_bf16 v[148:151], a[88:91], a[40:43], v[148:151]\n" \
"ds_read_b64_tr_b16 a[158:159], v211 offset:5632\n" \
"s_waitcnt lgkmcnt(8)\n" \
"v_mfma_f32_16x16x32_bf16 v[148:151], a[84:87], a[36:39], v[148:151]\n" \
"buffer_load_dwordx4 v213, %[srdk], s48 offen offset:2048 lds\n" \
"v_mfma_f32_16x16x32_bf16 v[148:151], a[80:83], a[32:35], v[148:151]\n" \
"v_exp_f32_e32 v143, v143\n" \
"v_mfma_f32_16x16x32_bf16 v[0:3], a[160:163], v[192:195], v[0:3]\n" \
"ds_read_b128 a[96:99], %[kb0] offset:8192\n" \
"v_mfma_f32_16x16x32_bf16 v[0:3], a[128:131], v[184:187], v[0:3]\n" \
"v_exp_f32_e64 v144, v144\n" \
"v_mfma_f32_16x16x32_bf16 v[4:7], a[164:167], v[192:195], v[4:7]\n" \
"v_cvt_pk_bf16_f32 v190, v140, v141\n" \
"v_cvt_pk_bf16_f32 v191, v142, v143\n" \
"v_mfma_f32_16x16x32_bf16 v[4:7], a[132:135], v[184:187], v[4:7]\n" \
"v_exp_f32_e64 v145, v145\n" \
"v_mfma_f32_16x16x32_bf16 v[8:11], a[168:171], v[192:195], v[8:11]\n" \
"ds_read_b128 a[100:103], v208 offset:8192\n" \
"v_mfma_f32_16x16x32_bf16 v[8:11], a[136:139], v[184:187], v[8:11]\n" \
"v_exp_f32_e64 v146, v146\n" \
"v_mfma_f32_16x16x32_bf16 v[12:15], a[172:175], v[192:195], v[12:15]\n" \
"ds_read_b128 a[104:107], v209 offset:8192\n" \
"v_mfma_f32_16x16x32_bf16 v[12:15], a[140:143], v[184:187], v[12:15]\n" \
"v_exp_f32_e64 v147, v147\n" \
"v_smfmac_f32_16x16x64_bf16 v[180:183], a[192:195], v[200:207], v228\n" \
"ds_read_b128 a[108:111], v210 offset:8192\n" \
"v_mfma_f32_16x16x32_bf16 v[152:155], a[64:67], a[48:51], v[172:175]\n" \
"v_exp_f32_e64 v148, v148\n" \
"v_mfma_f32_16x16x32_bf16 v[152:155], a[68:71], a[52:55], v[152:155]\n" \
"ds_read_b128 a[124:127], v210 offset:12288\n" \
"v_mfma_f32_16x16x32_bf16 v[152:155], a[72:75], a[56:59], v[152:155]\n" \
"v_exp_f32_e64 v149, v149\n" \
"v_mfma_f32_16x16x32_bf16 v[152:155], a[76:79], a[60:63], v[152:155]\n" \
"v_cvt_pk_bf16_f32 v216, v144, v145\n" \
"v_cvt_pk_bf16_f32 v217, v146, v147\n" \
"v_mfma_f32_16x16x32_bf16 v[156:159], a[92:95], a[60:63], v[172:175]\n" \
"v_exp_f32_e64 v150, v150\n" \
"v_mfma_f32_16x16x32_bf16 v[156:159], a[88:91], a[56:59], v[156:159]\n" \
"ds_read_b128 a[120:123], v209 offset:12288\n" \
"s_waitcnt lgkmcnt(6)\n" \
"v_mfma_f32_16x16x32_bf16 v[156:159], a[84:87], a[52:55], v[156:159]\n" \
"buffer_load_dwordx4 v214, %[srdk], s48 offen offset:3072 lds\n" \
"v_mfma_f32_16x16x32_bf16 v[156:159], a[80:83], a[48:51], v[156:159]\n" \
"v_exp_f32_e32 v151, v151\n" \
"v_mfma_f32_16x16x32_bf16 v[16:19], a[176:179], v[192:195], v[16:19]\n" \
"ds_read_b128 a[116:119], v208 offset:12288\n" \
"v_mfma_f32_16x16x32_bf16 v[16:19], a[144:147], v[184:187], v[16:19]\n" \
"v_exp_f32_e64 v152, v152\n" \
"v_mfma_f32_16x16x32_bf16 v[20:23], a[180:183], v[192:195], v[20:23]\n" \
"v_cvt_pk_bf16_f32 v218, v148, v149\n" \
"v_cvt_pk_bf16_f32 v219, v150, v151\n" \
"v_mfma_f32_16x16x32_bf16 v[20:23], a[148:151], v[184:187], v[20:23]\n" \
"v_exp_f32_e64 v153, v153\n" \
"v_mfma_f32_16x16x32_bf16 v[24:27], a[184:187], v[192:195], v[24:27]\n" \
"v_exp_f32_e64 v154, v154\n" \
"v_mfma_f32_16x16x32_bf16 v[24:27], a[152:155], v[184:187], v[24:27]\n" \
"ds_read_b128 a[112:115], %[kb0] offset:12288\n" \
"s_add_u32 s48, s48, 0x4000\n" \
"v_mfma_f32_16x16x32_bf16 v[28:31], a[188:191], v[192:195], v[28:31]\n" \
"v_exp_f32_e64 v155, v155\n" \
"v_mfma_f32_16x16x32_bf16 v[28:31], a[156:159], v[184:187], v[28:31]\n" \
"s_waitcnt vmcnt(15) lgkmcnt(2)\n" \
"s_barrier\n" \
"v_mfma_f32_16x16x32_bf16 v[128:131], a[96:99], a[0:3], v[160:163]\n" \
"v_exp_f32_e64 v156, v156\n" \
"v_mfma_f32_16x16x32_bf16 v[128:131], a[100:103], a[4:7], v[128:131]\n" \
"s_add_u32 m0, %[m0b], 114688\n" \
"v_mfma_f32_16x16x32_bf16 v[128:131], a[104:107], a[8:11], v[128:131]\n" \
"v_exp_f32_e64 v157, v157\n" \
"v_mfma_f32_16x16x32_bf16 v[128:131], a[108:111], a[12:15], v[128:131]\n" \
"v_cvt_pk_bf16_f32 v220, v152, v153\n" \
"v_cvt_pk_bf16_f32 v221, v154, v155\n" \
"v_mfma_f32_16x16x32_bf16 v[132:135], a[124:127], a[12:15], v[160:163]\n" \
"v_exp_f32_e64 v158, v158\n" \
"v_mfma_f32_16x16x32_bf16 v[132:135], a[120:123], a[8:11], v[132:135]\n" \
"s_waitcnt lgkmcnt(0)\n" \
"v_mfma_f32_16x16x32_bf16 v[132:135], a[116:119], a[4:7], v[132:135]\n" \
"buffer_load_dwordx4 %[vd0], %[srdv], s48 offen offset:0 lds\n" \
"v_mfma_f32_16x16x32_bf16 v[132:135], a[112:115], a[0:3], v[132:135]\n" \
"v_exp_f32_e32 v159, v159\n" \
"v_mfma_f32_16x16x32_bf16 v[32:35], a[160:163], v[196:199], v[32:35]\n" \
"ds_read_b128 a[64:67], %[kb0] offset:16384\n" \
"v_mfma_f32_16x16x32_bf16 v[32:35], a[128:131], v[188:191], v[32:35]\n" \
"v_exp_f32_e64 v128, v128\n" \
"v_mfma_f32_16x16x32_bf16 v[36:39], a[164:167], v[196:199], v[36:39]\n" \
"v_cvt_pk_bf16_f32 v222, v156, v157\n" \
"v_cvt_pk_bf16_f32 v223, v158, v159\n" \
".if . - adaln_body != 20480\n" \
".error \"size model\"\n" \
".endif\n" \
"Lk4_%=:\n" \
"s_cbranch_execz Lk5_%=\n" \
"s_nop 0\n" \
"v_mfma_f32_16x16x32_bf16 v[36:39], a[132:135], v[188:191], v[36:39]\n" \
"v_exp_f32_e64 v129, v129\n" \
"v_mfma_f32_16x16x32_bf16 v[40:43], a[168:171], v[196:199], v[40:43]\n" \
"ds_read_b128 a[68:71], v208 offset:16384\n" \
"v_mfma_f32_16x16x32_bf16 v[40:43], a[136:139], v[188:191], v[40:43]\n" \
"v_exp_f32_e64 v130, v130\n" \
"v_mfma_f32_16x16x32_bf16 v[44:47], a[172:175], v[196:199], v[44:47]\n" \
"ds_read_b128 a[72:75], v209 offset:16384\n" \
"v_mfma_f32_16x16x32_bf16 v[44:47], a[140:143], v[188:191], v[44:47]\n" \
"v_exp_f32_e64 v131, v131\n" \
"v_smfmac_f32_16x16x64_bf16 v[176:179], a[192:195], v[184:191], v228\n" \
"v_mfma_f32_16x16x32_bf16 v[136:139], a[96:99], a[16:19], v[164:167]\n" \
"v_exp_f32_e64 v132, v132\n" \
"v_mfma_f32_16x16x32_bf16 v[136:139], a[100:103], a[20:23], v[136:139]\n" \
"ds_read_b128 a[76:79], v210 offset:16384\n" \
"v_mfma_f32_16x16x32_bf16 v[136:139], a[104:107], a[24:27], v[136:139]\n" \
"v_exp_f32_e64 v133, v133\n" \
"v_mfma_f32_16x16x32_bf16 v[136:139], a[108:111], a[28:31], v[136:139]\n" \
"v_cvt_pk_bf16_f32 v192, v128, v129\n" \
"v_cvt_pk_bf16_f32 v193, v130, v131\n" \
"v_mfma_f32_16x16x32_bf16 v[140:143], a[124:127], a[28:31], v[164:167]\n" \
"v_exp_f32_e64 v134, v134\n" \
"v_mfma_f32_16x16x32_bf16 v[140:143], a[120:123], a[24:27], v[140:143]\n" \
"ds_read_b128 a[92:95], v210 offset:20480\n" \
"v_mfma_f32_16x16x32_bf16 v[140:143], a[116:119], a[20:23], v[140:143]\n" \
"buffer_load_dwordx4 v215, %[srdv], s48 offen offset:1024 lds\n" \
"v_mfma_f32_16x16x32_bf16 v[140:143], a[112:115], a[16:19], v[140:143]\n" \
"v_exp_f32_e64 v135, v135\n" \
"v_mfma_f32_16x16x32_bf16 v[48:51], a[176:179], v[196:199], v[48:51]\n" \
"ds_read_b128 a[88:91], v209 offset:20480\n" \
"v_mfma_f32_16x16x32_bf16 v[48:51], a[144:147], v[188:191], v[48:51]\n" \
"v_exp_f32_e64 v136, v136\n" \
"v_mfma_f32_16x16x32_bf16 v[52:55], a[180:183], v[196:199], v[52:55]\n" \
"v_cvt_pk_bf16_f32 v194, v132, v133\n" \
"v_cvt_pk_bf16_f32 v195, v134, v135\n" \
"v_mfma_f32_16x16x32_bf16 v[52:55], a[148:151], v[188:191], v[52:55]\n" \
"v_exp_f32_e64 v137, v137\n" \
"v_mfma_f32_16x16x32_bf16 v[56:59], a[184:187], v[196:199], v[56:59]\n" \
"v_exp_f32_e64 v138, v138\n" \
"v_mfma_f32_16x16x32_bf16 v[56:59], a[152:155], v[188:191], v[56:59]\n" \
"ds_read_b128 a[84:87], v208 offset:20480\n" \
"v_mfma_f32_16x16x32_bf16 v[60:63], a[188:191], v[196:199], v[60:63]\n" \
"v_exp_f32_e64 v139, v139\n" \
"v_mfma_f32_16x16x32_bf16 v[60:63], a[156:159], v[188:191], v[60:63]\n" \
"s_cmp_lg_u32 %[wid], 3\n" \
"s_cbranch_scc1 LtA1_%=\n" \
"global_load_dword v237, v224, s[58:59]\n" \
"s_add_u32 s61, s48, 49152\n" \
"s_mov_b32 exec_hi, 0\n" \
"s_nop 0\n" \
"buffer_load_dword v237, v224, %[srdp], s61 offen\n" \
"s_mov_b32 exec_hi, -1\n" \
"s_nop 0\n" \
"LtA1_%=:\n" \
"v_mfma_f32_16x16x32_bf16 v[144:147], a[96:99], a[32:35], v[168:171]\n" \
"v_exp_f32_e64 v140, v140\n" \
"v_mfma_f32_16x16x32_bf16 v[144:147], a[100:103], a[36:39], v[144:147]\n" \
"ds_read_b128 a[80:83], %[kb0] offset:20480\n" \
"v_mfma_f32_16x16x32_bf16 v[144:147], a[104:107], a[40:43], v[144:147]\n" \
"v_exp_f32_e64 v141, v141\n" \
"v_mfma_f32_16x16x32_bf16 v[144:147], a[108:111], a[44:47], v[144:147]\n" \
"v_cvt_pk_bf16_f32 v196, v136, v137\n" \
"v_cvt_pk_bf16_f32 v197, v138, v139\n" \
"v_mfma_f32_16x16x32_bf16 v[148:151], a[124:127], a[44:47], v[168:171]\n" \
"v_exp_f32_e64 v142, v142\n" \
"v_mfma_f32_16x16x32_bf16 v[148:151], a[120:123], a[40:43], v[148:151]\n" \
"v_mfma_f32_16x16x32_bf16 v[148:151], a[116:119], a[36:39], v[148:151]\n" \
"buffer_load_dwordx4 %[vd0], %[srdv], s48 offen offset:2048 lds\n" \
"v_mfma_f32_16x16x32_bf16 v[148:151], a[112:115], a[32:35], v[148:151]\n" \
"v_exp_f32_e64 v143, v143\n" \
"v_mfma_f32_16x16x32_bf16 v[96:99], a[160:163], v[204:207], v[96:99]\n" \
"ds_read_b64_tr_b16 a[160:161], %[vvb] offset:8192\n" \
"v_mfma_f32_16x16x32_bf16 v[96:99], a[128:131], v[220:223], v[96:99]\n" \
"v_exp_f32_e64 v144, v144\n" \
"v_mfma_f32_16x16x32_bf16 v[100:103], a[164:167], v[204:207], v[100:103]\n" \
"v_cvt_pk_bf16_f32 v198, v140, v141\n" \
"v_cvt_pk_bf16_f32 v199, v142, v143\n" \
"v_mfma_f32_16x16x32_bf16 v[100:103], a[132:135], v[220:223], v[100:103]\n" \
"v_exp_f32_e64 v145, v145\n" \
"v_mfma_f32_16x16x32_bf16 v[104:107], a[168:171], v[204:207], v[104:107]\n" \
"ds_read_b64_tr_b16 a[162:163], %[vvb] offset:12288\n" \
"ds_read_b64_tr_b16 a[164:165], v211 offset:8192\n" \
"v_mfma_f32_16x16x32_bf16 v[104:107], a[136:139], v[220:223], v[104:107]\n" \
"v_exp_f32_e64 v146, v146\n" \
"v_mfma_f32_16x16x32_bf16 v[108:111], a[172:175], v[204:207], v[108:111]\n" \
"ds_read_b64_tr_b16 a[166:167], v211 offset:12288\n" \
"v_mfma_f32_16x16x32_bf16 v[108:111], a[140:143], v[220:223], v[108:111]\n" \
"v_exp_f32_e64 v147, v147\n" \
"v_smfmac_f32_16x16x64_bf16 v[180:183], a[192:195], v[216:223], v228\n" \
"ds_read_b64_tr_b16 a[168:169], %[vvb] offset:8704\n" \
"v_mfma_f32_16x16x32_bf16 v[152:155], a[96:99], a[48:51], v[172:175]\n" \
"v_exp_f32_e64 v148, v148\n" \
"v_mfma_f32_16x16x32_bf16 v[152:155], a[100:103], a[52:55], v[152:155]\n" \
"ds_read_b64_tr_b16 a[170:171], %[vvb] offset:12800\n" \
"v_mfma_f32_16x16x32_bf16 v[152:155], a[104:107], a[56:59], v[152:155]\n" \
"v_exp_f32_e64 v149, v149\n" \
"v_mfma_f32_16x16x32_bf16 v[152:155], a[108:111], a[60:63], v[152:155]\n" \
"v_cvt_pk_bf16_f32 v200, v144, v145\n" \
"v_cvt_pk_bf16_f32 v201, v146, v147\n" \
"v_mfma_f32_16x16x32_bf16 v[156:159], a[124:127], a[60:63], v[172:175]\n" \
"v_exp_f32_e64 v150, v150\n" \
"v_mfma_f32_16x16x32_bf16 v[156:159], a[120:123], a[56:59], v[156:159]\n" \
"ds_read_b64_tr_b16 a[172:173], v211 offset:8704\n" \
"v_mfma_f32_16x16x32_bf16 v[156:159], a[116:119], a[52:55], v[156:159]\n" \
"buffer_load_dwordx4 v215, %[srdv], s48 offen offset:3072 lds\n" \
"v_mfma_f32_16x16x32_bf16 v[156:159], a[112:115], a[48:51], v[156:159]\n" \
"v_exp_f32_e64 v151, v151\n" \
"v_mfma_f32_16x16x32_bf16 v[112:115], a[176:179], v[204:207], v[112:115]\n" \
"ds_read_b64_tr_b16 a[174:175], v211 offset:12800\n" \
"ds_read_b64_tr_b16 a[176:177], %[vvb] offset:9216\n" \
"v_mfma_f32_16x16x32_bf16 v[112:115], a[144:147], v[220:223], v[112:115]\n" \
"v_exp_f32_e64 v152, v152\n" \
"v_mfma_f32_16x16x32_bf16 v[116:119], a[180:183], v[204:207], v[116:119]\n" \
"v_cvt_pk_bf16_f32 v202, v148, v149\n" \
"v_cvt_pk_bf16_f32 v203, v150, v151\n" \
"v_mfma_f32_16x16x32_bf16 v[116:119], a[148:151], v[220:223], v[116:119]\n" \
"v_exp_f32_e64 v153, v153\n" \
"v_mfma_f32_16x16x32_bf16 v[120:123], a[184:187], v[204:207], v[120:123]\n" \
"v_exp_f32_e64 v154, v154\n" \
"v_mfma_f32_16x16x32_bf16 v[120:123], a[152:155], v[220:223], v[120:123]\n" \
"ds_read_b64_tr_b16 a[178:179], %[vvb] offset:13312\n" \
"v_mfma_f32_16x16x32_bf16 v[124:127], a[188:191], v[204:207], v[124:127]\n" \
"v_exp_f32_e64 v155, v155\n" \
"v_mfma_f32_16x16x32_bf16 v[124:127], a[156:159], v[220:223], v[124:127]\n" \
"ds_read_b64_tr_b16 a[180:181], v211 offset:9216\n" \
"s_waitcnt lgkmcnt(13)\n" \
"s_cmp_lg_u32 s60, 4\n" \
"s_cbranch_scc1 LmA_%=\n" \
"s_nop 0\n" \
"v_readlane_b32 s64, %[tlo], 9\n" \
"v_readlane_b32 s65, %[thi], 9\n" \
"s_mov_b32 s66, %[wbytes]\n" \
"v_mov_b32_e32 v229, %[vg0]\n" \
"s_mov_b32 s44, %[m0t]\n" \
"s_mov_b32 s45, 0\n" \
"s_cmp_lg_u32 s52, 0\n" \
"s_cbranch_scc1 LmB_%=\n" \
"v_readlane_b32 s61, %[thi], 21\n" \
"s_add_u32 s62, %[sin], 16384\n" \
"s_add_u32 s63, %[sin], 49152\n" \
"s_add_u32 s55, s61, 4096\n" \
"s_nop 1\n" \
"s_nop 0\n" \
"buffer_store_dwordx4 a[196:199], v235, %[srdz], s61 offen offset:0 sc1\n" \
"buffer_store_dwordx4 a[196:199], v235, %[srdz], s55 offen offset:0 sc1\n" \
"s_waitcnt vmcnt(15)\n" \
"s_nop 0\n" \
"ds_read_b64_tr_b16 a[232:233], %[ta0] offset:0\n" \
"ds_read_b64_tr_b16 a[234:235], %[ta0] offset:128\n" \
"ds_read_b64_tr_b16 a[236:237], %[ta0] offset:1024\n" \
"ds_read_b64_tr_b16 a[238:239], %[ta0] offset:1152\n" \
"ds_read_b64_tr_b16 a[240:241], %[ta0] offset:2048\n" \
"ds_read_b64_tr_b16 a[242:243], %[ta0] offset:2176\n" \
"ds_read_b64_tr_b16 a[244:245], %[ta0] offset:3072\n" \
"ds_read_b64_tr_b16 a[246:247], %[ta0] offset:3200\n" \
"ds_read_b64_tr_b16 a[248:249], %[ta0] offset:4096\n" \
"ds_read_b64_tr_b16 a[250:251], %[ta0] offset:4224\n" \
"ds_read_b64_tr_b16 a[252:253], %[ta0] offset:5120\n" \
"ds_read_b64_tr_b16 a[254:255], %[ta0] offset:5248\n" \
"v_add_u32_e32 v234, %[m0t], v235\n" \
"s_waitcnt lgkmcnt(8)\n" \
"v_mfma_f32_16x16x32_bf16 v[230:233], a[232:235], a[200:203], 0\n" \
"v_mfma_f32_16x16x32_bf16 v[230:233], a[236:239], a[204:207], v[230:233]\n" \
"ds_read_b64_tr_b16 a[232:233], %[ta0] offset:6144\n" \
"ds_read_b64_tr_b16 a[234:235], %[ta0] offset:6272\n" \
"ds_read_b64_tr_b16 a[236:237], %[ta0] offset:7168\n" \
"ds_read_b64_tr_b16 a[238:239], %[ta0] offset:7296\n" \
"s_waitcnt lgkmcnt(4)\n" \
"s_nop 0\n" \
"v_mfma_f32_16x16x32_bf16 v[230:233], a[240:243], a[208:211], v[230:233]\n" \
"v_mfma_f32_16x16x32_bf16 v[230:233], a[244:247], a[212:215], v[230:233]\n" \
"v_mfma_f32_16x16x32_bf16 v[230:233], a[248:251], a[216:219], v[230:233]\n" \
"v_mfma_f32_16x16x32_bf16 v[230:233], a[252:255], a[220:223], v[230:233]\n" \
"s_waitcnt lgkmcnt(0)\n" \
"s_nop 0\n" \
"v_mfma_f32_16x16x32_bf16 v[230:233], a[232:235], a[224:227], v[230:233]\n" \
"v_mfma_f32_16x16x32_bf16 v[230:233], a[236:239], a[228:231], v[230:233]\n" \
"s_nop 7\n" \
"s_nop 0\n" \
"ds_write_b128 v234, v[230:233] offset:7168\n" \
"LmB_%=:\n" \
"v_mfma_f32_16x16x32_bf16 v[128:131], a[64:67], a[0:3], v[160:163]\n" \
"v_exp_f32_e64 v156, v156\n" \
"v_mfma_f32_16x16x32_bf16 v[128:131], a[68:71], a[4:7], v[128:131]\n" \
"ds_read_b64_tr_b16 a[182:183], v211 offset:13312\n" \
"s_add_u32 m0, %[m0b], 0\n" \
"v_mfma_f32_16x16x32_bf16 v[128:131], a[72:75], a[8:11], v[128:131]\n" \
"v_exp_f32_e32 v157, v157\n" \
"v_mfma_f32_16x16x32_bf16 v[128:131], a[76:79], a[12:15], v[128:131]\n" \
"v_cvt_pk_bf16_f32 v204, v152, v153\n" \
"v_cvt_pk_bf16_f32 v205, v154, v155\n" \
"v_mfma_f32_16x16x32_bf16 v[132:135], a[92:95], a[12:15], v[160:163]\n" \
"v_exp_f32_e64 v158, v158\n" \
"v_mfma_f32_16x16x32_bf16 v[132:135], a[88:91], a[8:11], v[132:135]\n" \
"ds_read_b64_tr_b16 a[184:185], %[vvb] offset:9728\n" \
"s_waitcnt lgkmcnt(5)\n" \
"v_mfma_f32_16x16x32_bf16 v[132:135], a[84:87], a[4:7], v[132:135]\n" \
"buffer_load_dwordx4 %[kd0], %[srdk], s48 offen offset:0 lds\n" \
"v_mfma_f32_16x16x32_bf16 v[132:135], a[80:83], a[0:3], v[132:135]\n" \
"v_exp_f32_e32 v159, v159\n" \
"v_mfma_f32_16x16x32_bf16 v[64:67], a[128:131], v[216:219], v[64:67]\n" \
"ds_read_b64_tr_b16 a[186:187], %[vvb] offset:13824\n" \
"ds_read_b64_tr_b16 a[188:189], v211 offset:9728\n" \
"v_mfma_f32_16x16x32_bf16 v[64:67], a[160:163], v[200:203], v[64:67]\n" \
"v_exp_f32_e64 v128, v128\n" \
"v_mfma_f32_16x16x32_bf16 v[68:71], a[132:135], v[216:219], v[68:71]\n" \
"v_cvt_pk_bf16_f32 v206, v156, v157\n" \
"v_cvt_pk_bf16_f32 v207, v158, v159\n" \
"v_mfma_f32_16x16x32_bf16 v[68:71], a[164:167], v[200:203], v[68:71]\n" \
"v_exp_f32_e64 v129, v129\n" \
"v_mfma_f32_16x16x32_bf16 v[72:75], a[136:139], v[216:219], v[72:75]\n" \
"ds_read_b64_tr_b16 a[190:191], v211 offset:13824\n" \
"ds_read_b64_tr_b16 a[128:129], %[vvb] offset:16384\n" \
"s_add_u32 s60, s60, 1\n" \
"s_nop 0\n" \
"v_readlane_b32 s58, %[tlo], s60\n" \
"v_mfma_f32_16x16x32_bf16 v[72:75], a[168:171], v[200:203], v[72:75]\n" \
"v_exp_f32_e64 v130, v130\n" \
"v_mfma_f32_16x16x32_bf16 v[76:79], a[140:143], v[216:219], v[76:79]\n" \
"ds_read_b64_tr_b16 a[130:131], %[vvb] offset:20480\n" \
"ds_read_b64_tr_b16 a[132:133], v211 offset:16384\n" \
"v_readlane_b32 s59, %[thi], s60\n" \
"v_mfma_f32_16x16x32_bf16 v[76:79], a[172:175], v[200:203], v[76:79]\n" \
"v_exp_f32_e64 v131, v131\n" \
"v_smfmac_f32_16x16x64_bf16 v[176:179], a[192:195], v[192:199], v228\n" \
"ds_read_b64_tr_b16 a[134:135], v211 offset:20480\n" \
"ds_read_b64_tr_b16 a[136:137], %[vvb] offset:16896\n" \
"s_cmp_eq_u32 s60, 7\n" \
"s_cselect_b32 s61, 56, 1\n" \
"v_mul_u32_u24_e64 v236, s61, v224\n" \
"v_mfma_f32_16x16x32_bf16 v[136:139], a[64:67], a[16:19], v[164:167]\n" \
"v_exp_f32_e64 v132, v132\n" \
"v_mfma_f32_16x16x32_bf16 v[136:139], a[68:71], a[20:23], v[136:139]\n" \
"ds_read_b64_tr_b16 a[138:139], %[vvb] offset:20992\n" \
"ds_read_b64_tr_b16 a[140:141], v211 offset:16896\n" \
"v_mfma_f32_16x16x32_bf16 v[136:139], a[72:75], a[24:27], v[136:139]\n" \
"v_exp_f32_e64 v133, v133\n" \
"v_mfma_f32_16x16x32_bf16 v[136:139], a[76:79], a[28:31], v[136:139]\n" \
"v_cvt_pk_bf16_f32 v184, v128, v129\n" \
"v_cvt_pk_bf16_f32 v185, v130, v131\n" \
"v_mfma_f32_16x16x32_bf16 v[140:143], a[92:95], a[28:31], v[164:167]\n" \
"v_exp_f32_e64 v134, v134\n" \
"v_mfma_f32_16x16x32_bf16 v[140:143], a[88:91], a[24:27], v[140:143]\n" \
"ds_read_b64_tr_b16 a[142:143], v211 offset:20992\n" \
"s_waitcnt lgkmcnt(8)\n" \
"v_mfma_f32_16x16x32_bf16 v[140:143], a[84:87], a[20:23], v[140:143]\n" \
"buffer_load_dwordx4 v212, %[srdk], s48 offen offset:1024 lds\n" \
"v_mfma_f32_16x16x32_bf16 v[140:143], a[80:83], a[16:19], v[140:143]\n" \
"v_exp_f32_e32 v135, v135\n" \
"v_mfma_f32_16x16x32_bf16 v[80:83], a[144:147], v[216:219], v[80:83]\n" \
"ds_read_b64_tr_b16 a[144:145], %[vvb] offset:17408\n" \
"ds_read_b64_tr_b16 a[146:147], %[vvb] offset:21504\n" \
"v_mfma_f32_16x16x32_bf16 v[80:83], a[176:179], v[200:203], v[80:83]\n" \
"v_exp_f32_e64 v136, v136\n" \
"v_mfma_f32_16x16x32_bf16 v[84:87], a[148:151], v[216:219], v[84:87]\n" \
"v_cvt_pk_bf16_f32 v186, v132, v133\n" \
"v_cvt_pk_bf16_f32 v187, v134, v135\n" \
"v_mfma_f32_16x16x32_bf16 v[84:87], a[180:183], v[200:203], v[84:87]\n" \
"v_exp_f32_e64 v137, v137\n" \
"v_mfma_f32_16x16x32_bf16 v[88:91], a[152:155], v[216:219], v[88:91]\n" \
"v_exp_f32_e64 v138, v138\n" \
"v_mfma_f32_16x16x32_bf16 v[88:91], a[184:187], v[200:203], v[88:91]\n" \
"ds_read_b64_tr_b16 a[148:149], v211 offset:17408\n" \
"ds_read_b64_tr_b16 a[150:151], v211 offset:21504\n" \
"v_mfma_f32_16x16x32_bf16 v[92:95], a[156:159], v[216:219], v[92:95]\n" \
"v_exp_f32_e64 v139, v139\n" \
"v_mfma_f32_16x16x32_bf16 v[92:95], a[188:191], v[200:203], v[92:95]\n" \
"ds_read_b64_tr_b16 a[152:153], %[vvb] offset:17920\n" \
"buffer_store_dwordx4 a[196:199], v235, %[srdz], s63 offen offset:0 sc1\n" \
"v_mfma_f32_16x16x32_bf16 v[144:147], a[64:67], a[32:35], v[168:171]\n" \
"v_exp_f32_e64 v140, v140\n" \
"v_mfma_f32_16x16x32_bf16 v[144:147], a[68:71], a[36:39], v[144:147]\n" \
"ds_read_b64_tr_b16 a[154:155], %[vvb] offset:22016\n" \
"ds_read_b64_tr_b16 a[156:157], v211 offset:17920\n" \
"v_mfma_f32_16x16x32_bf16 v[144:147], a[72:75], a[40:43], v[144:147]\n" \
"v_exp_f32_e64 v141, v141\n" \
"v_mfma_f32_16x16x32_bf16 v[144:147], a[76:79], a[44:47], v[144:147]\n" \
"v_cvt_pk_bf16_f32 v188, v136, v137\n" \
"v_cvt_pk_bf16_f32 v189, v138, v139\n" \
"v_mfma_f32_16x16x32_bf16 v[148:151], a[92:95], a[44:47], v[168:171]\n" \
"v_exp_f32_e64 v142, v142\n" \
"v_mfma_f32_16x16x32_bf16 v[148:151], a[88:91], a[40:43], v[148:151]\n" \
"ds_read_b64_tr_b16 a[158:159], v211 offset:22016\n" \
"s_waitcnt lgkmcnt(8)\n" \
"v_mfma_f32_16x16x32_bf16 v[148:151], a[84:87], a[36:39], v[148:151]\n" \
"buffer_load_dwordx4 v213, %[srdk], s48 offen offset:2048 lds\n" \
"v_mfma_f32_16x16x32_bf16 v[148:151], a[80:83], a[32:35], v[148:151]\n" \
"v_exp_f32_e32 v143, v143\n" \
"v_mfma_f32_16x16x32_bf16 v[0:3], a[160:163], v[192:195], v[0:3]\n" \
"ds_read_b128 a[96:99], %[kb0] offset:24576\n" \
"v_mfma_f32_16x16x32_bf16 v[0:3], a[128:131], v[184:187], v[0:3]\n" \
"v_exp_f32_e64 v144, v144\n" \
"v_mfma_f32_16x16x32_bf16 v[4:7], a[164:167], v[192:195], v[4:7]\n" \
"v_cvt_pk_bf16_f32 v190, v140, v141\n" \
"v_cvt_pk_bf16_f32 v191, v142, v143\n" \
"v_mfma_f32_16x16x32_bf16 v[4:7], a[132:135], v[184:187], v[4:7]\n" \
"v_exp_f32_e64 v145, v145\n" \
"v_mfma_f32_16x16x32_bf16 v[8:11], a[168:171], v[192:195], v[8:11]\n" \
"ds_read_b128 a[100:103], v208 offset:24576\n" \
"v_mfma_f32_16x16x32_bf16 v[8:11], a[136:139], v[184:187], v[8:11]\n" \
"v_exp_f32_e64 v146, v146\n" \
"v_mfma_f32_16x16x32_bf16 v[12:15], a[172:175], v[192:195], v[12:15]\n" \
"ds_read_b128 a[104:107], v209 offset:24576\n" \
"v_mfma_f32_16x16x32_bf16 v[12:15], a[140:143], v[184:187], v[12:15]\n" \
"v_exp_f32_e64 v147, v147\n" \
"v_smfmac_f32_16x16x64_bf16 v[180:183], a[192:195], v[200:207], v228\n" \
"ds_read_b128 a[108:111], v210 offset:24576\n" \
"v_mfma_f32_16x16x32_bf16 v[152:155], a[64:67], a[48:51], v[172:175]\n" \
"v_exp_f32_e64 v148, v148\n" \
"v_mfma_f32_16x16x32_bf16 v[152:155], a[68:71], a[52:55], v[152:155]\n" \
"ds_read_b128 a[124:127], v210 offset:28672\n" \
"v_mfma_f32_16x16x32_bf16 v[152:155], a[72:75], a[56:59], v[152:155]\n" \
"v_exp_f32_e64 v149, v149\n" \
"v_mfma_f32_16x16x32_bf16 v[152:155], a[76:79], a[60:63], v[152:155]\n" \
"v_cvt_pk_bf16_f32 v216, v144, v145\n" \
"v_cvt_pk_bf16_f32 v217, v146, v147\n" \
"v_mfma_f32_16x16x32_bf16 v[156:159], a[92:95], a[60:63], v[172:175]\n" \
"v_exp_f32_e64 v150, v150\n" \
"v_mfma_f32_16x16x32_bf16 v[156:159], a[88:91], a[56:59], v[156:159]\n" \
"ds_read_b128 a[120:123], v209 offset:28672\n" \
"s_waitcnt lgkmcnt(6)\n" \
"v_mfma_f32_16x16x32_bf16 v[156:159], a[84:87], a[52:55], v[156:159]\n" \
"buffer_load_dwordx4 v214, %[srdk], s48 offen offset:3072 lds\n" \
"v_mfma_f32_16x16x32_bf16 v[156:159], a[80:83], a[48:51], v[156:159]\n" \
"v_exp_f32_e32 v151, v151\n" \
"v_mfma_f32_16x16x32_bf16 v[16:19], a[176:179], v[192:195], v[16:19]\n" \
"ds_read_b128 a[116:119], v208 offset:28672\n" \
"v_mfma_f32_16x16x32_bf16 v[16:19], a[144:147], v[184:187], v[16:19]\n" \
"v_exp_f32_e64 v152, v152\n" \
"v_mfma_f32_16x16x32_bf16 v[20:23], a[180:183], v[192:195], v[20:23]\n" \
"v_cvt_pk_bf16_f32 v218, v148, v149\n" \
"v_cvt_pk_bf16_f32 v219, v150, v151\n" \
"v_mfma_f32_16x16x32_bf16 v[20:23], a[148:151], v[184:187], v[20:23]\n" \
"v_exp_f32_e64 v153, v153\n" \
"v_mfma_f32_16x16x32_bf16 v[24:27], a[184:187], v[192:195], v[24:27]\n" \
"v_exp_f32_e64 v154, v154\n" \
"v_mfma_f32_16x16x32_bf16 v[24:27], a[152:155], v[184:187], v[24:27]\n" \
"ds_read_b128 a[112:115], %[kb0] offset:28672\n" \
"s_add_u32 s48, s48, 0x4000\n" \
"s_mov_b32 m0, s44\n" \
"s_addk_i32 s44, 0x400\n" \
"buffer_load_dwordx4 v229, s[64:67], s45 offen offset:0 lds\n" \
"s_add_u32 s45, s45, 229376\n" \
"v_mfma_f32_16x16x32_bf16 v[28:31], a[188:191], v[192:195], v[28:31]\n" \
"v_exp_f32_e64 v155, v155\n" \
"v_mfma_f32_16x16x32_bf16 v[28:31], a[156:159], v[184:187], v[28:31]\n" \
"s_waitcnt vmcnt(15) lgkmcnt(2)\n" \
"s_barrier\n" \
"v_mfma_f32_16x16x32_bf16 v[128:131], a[96:99], a[0:3], v[160:163]\n" \
"v_exp_f32_e64 v156, v156\n" \
"v_mfma_f32_16x16x32_bf16 v[128:131], a[100:103], a[4:7], v[128:131]\n" \
"s_add_u32 m0, %[m0b], 65536\n" \
"v_mfma_f32_16x16x32_bf16 v[128:131], a[104:107], a[8:11], v[128:131]\n" \
"v_exp_f32_e64 v157, v157\n" \
"v_mfma_f32_16x16x32_bf16 v[128:131], a[108:111], a[12:15], v[128:131]\n" \
"v_cvt_pk_bf16_f32 v220, v152, v153\n" \
"v_cvt_pk_bf16_f32 v221, v154, v155\n" \
"v_mfma_f32_16x16x32_bf16 v[132:135], a[124:127], a[12:15], v[160:163]\n" \
"v_exp_f32_e64 v158, v158\n" \
"v_mfma_f32_16x16x32_bf16 v[132:135], a[120:123], a[8:11], v[132:135]\n" \
"s_waitcnt lgkmcnt(0)\n" \
"v_mfma_f32_16x16x32_bf16 v[132:135], a[116:119], a[4:7], v[132:135]\n" \
"buffer_load_dwordx4 %[vd0], %[srdv], s48 offen offset:0 lds\n" \
"v_mfma_f32_16x16x32_bf16 v[132:135], a[112:115], a[0:3], v[132:135]\n" \
"v_exp_f32_e32 v159, v159\n" \
"v_mfma_f32_16x16x32_bf16 v[32:35], a[160:163], v[196:199], v[32:35]\n" \
"ds_read_b128 a[64:67], %[kb0] offset:32768\n" \
"v_mfma_f32_16x16x32_bf16 v[32:35], a[128:131], v[188:191], v[32:35]\n" \
"v_exp_f32_e64 v128, v128\n" \
"v_mfma_f32_16x16x32_bf16 v[36:39], a[164:167], v[196:199], v[36:39]\n" \
"v_cvt_pk_bf16_f32 v222, v156, v157\n" \
"v_cvt_pk_bf16_f32 v223, v158, v159\n" \
"v_mfma_f32_16x16x32_bf16 v[36:39], a[132:135], v[188:191], v[36:39]\n" \
"v_exp_f32_e64 v129, v129\n" \
"v_mfma_f32_16x16x32_bf16 v[40:43], a[168:171], v[196:199], v[40:43]\n" \
"ds_read_b128 a[68:71], v208 offset:32768\n" \
"v_mfma_f32_16x16x32_bf16 v[40:43], a[136:139], v[188:191], v[40:43]\n" \
"v_exp_f32_e64 v130, v130\n" \
"v_mfma_f32_16x16x32_bf16 v[44:47], a[172:175], v[196:199], v[44:47]\n" \
"ds_read_b128 a[72:75], v209 offset:32768\n" \
"v_mfma_f32_16x16x32_bf16 v[44:47], a[140:143], v[188:191], v[44:47]\n" \
"v_exp_f32_e64 v131, v131\n" \
"v_smfmac_f32_16x16x64_bf16 v[176:179], a[192:195], v[184:191], v228\n" \
"v_mfma_f32_16x16x32_bf16 v[136:139], a[96:99], a[16:19], v[164:167]\n" \
"v_exp_f32_e64 v132, v132\n" \
"v_mfma_f32_16x16x32_bf16 v[136:139], a[100:103], a[20:23], v[136:139]\n" \
"ds_read_b128 a[76:79], v210 offset:32768\n" \
"v_mfma_f32_16x16x32_bf16 v[136:139], a[104:107], a[24:27], v[136:139]\n" \
"v_exp_f32_e64 v133, v133\n" \
"v_mfma_f32_16x16x32_bf16 v[136:139], a[108:111], a[28:31], v[136:139]\n" \
"v_cvt_pk_bf16_f32 v192, v128, v129\n" \
"v_cvt_pk_bf16_f32 v193, v130, v131\n" \
"v_mfma_f32_16x16x32_bf16 v[140:143], a[124:127], a[28:31], v[164:167]\n" \
"v_exp_f32_e64 v134, v134\n" \
"v_mfma_f32_16x16x32_bf16 v[140:143], a[120:123], a[24:27], v[140:143]\n" \
"ds_read_b128 a[92:95], v210 offset:36864\n" \
"v_mfma_f32_16x16x32_bf16 v[140:143], a[116:119], a[20:23], v[140:143]\n" \
"buffer_load_dwordx4 v215, %[srdv], s48 offen offset:1024 lds\n" \
"v_mfma_f32_16x16x32_bf16 v[140:143], a[112:115], a[16:19], v[140:143]\n" \
"v_exp_f32_e64 v135, v135\n" \
"v_mfma_f32_16x16x32_bf16 v[48:51], a[176:179], v[196:199], v[48:51]\n" \
"ds_read_b128 a[88:91], v209 offset:36864\n" \
"v_mfma_f32_16x16x32_bf16 v[48:51], a[144:147], v[188:191], v[48:51]\n" \
"v_exp_f32_e64 v136, v136\n" \
"v_mfma_f32_16x16x32_bf16 v[52:55], a[180:183], v[196:199], v[52:55]\n" \
"v_cvt_pk_bf16_f32 v194, v132, v133\n" \
"v_cvt_pk_bf16_f32 v195, v134, v135\n" \
"v_mfma_f32_16x16x32_bf16 v[52:55], a[148:151], v[188:191], v[52:55]\n" \
"v_exp_f32_e64 v137, v137\n" \
"v_mfma_f32_16x16x32_bf16 v[56:59], a[184:187], v[196:199], v[56:59]\n" \
"v_exp_f32_e64 v138, v138\n" \
"v_mfma_f32_16x16x32_bf16 v[56:59], a[152:155], v[188:191], v[56:59]\n" \
"ds_read_b128 a[84:87], v208 offset:36864\n" \
"v_mfma_f32_16x16x32_bf16 v[60:63], a[188:191], v[196:199], v[60:63]\n" \
"v_exp_f32_e64 v139, v139\n" \
"v_mfma_f32_16x16x32_bf16 v[60:63], a[156:159], v[188:191], v[60:63]\n" \
"s_cmp_lg_u32 %[wid], 0\n" \
"s_cbranch_scc1 LtB3_%=\n" \
"global_load_dword v237, v236, s[58:59]\n" \
"s_add_u32 s61, s48, 49152\n" \
"s_mov_b32 exec_hi, 0\n" \
"s_nop 0\n" \
"buffer_load_dword v237, v224, %[srdp], s61 offen\n" \
"s_mov_b32 exec_hi, -1\n" \
"s_nop 0\n" \
"LtB3_%=:\n" \
"v_mfma_f32_16x16x32_bf16 v[144:147], a[96:99], a[32:35], v[168:171]\n" \
"v_exp_f32_e64 v140, v140\n" \
"v_mfma_f32_16x16x32_bf16 v[144:147], a[100:103], a[36:39], v[144:147]\n" \
"ds_read_b128 a[80:83], %[kb0] offset:36864\n" \
"v_mfma_f32_16x16x32_bf16 v[144:147], a[104:107], a[40:43], v[144:147]\n" \
"v_exp_f32_e64 v141, v141\n" \
"v_mfma_f32_16x16x32_bf16 v[144:147], a[108:111], a[44:47], v[144:147]\n" \
"v_cvt_pk_bf16_f32 v196, v136, v137\n" \
"v_cvt_pk_bf16_f32 v197, v138, v139\n" \
"v_mfma_f32_16x16x32_bf16 v[148:151], a[124:127], a[44:47], v[168:171]\n" \
"v_exp_f32_e64 v142, v142\n" \
"v_mfma_f32_16x16x32_bf16 v[148:151], a[120:123], a[40:43], v[148:151]\n" \
"v_mfma_f32_16x16x32_bf16 v[148:151], a[116:119], a[36:39], v[148:151]\n" \
"buffer_load_dwordx4 %[vd0], %[srdv], s48 offen offset:2048 lds\n" \
"v_mfma_f32_16x16x32_bf16 v[148:151], a[112:115], a[32:35], v[148:151]\n" \
"v_exp_f32_e64 v143, v143\n" \
"v_mfma_f32_16x16x32_bf16 v[96:99], a[160:163], v[204:207], v[96:99]\n" \
"ds_read_b64_tr_b16 a[160:161], %[vvb] offset:24576\n" \
"v_mfma_f32_16x16x32_bf16 v[96:99], a[128:131], v[220:223], v[96:99]\n" \
"v_exp_f32_e64 v144, v144\n" \
"v_mfma_f32_16x16x32_bf16 v[100:103], a[164:167], v[204:207], v[100:103]\n" \
"v_cvt_pk_bf16_f32 v198, v140, v141\n" \
"v_cvt_pk_bf16_f32 v199, v142, v143\n" \
"v_mfma_f32_16x16x32_bf16 v[100:103], a[132:135], v[220:223], v[100:103]\n" \
"v_exp_f32_e64 v145, v145\n" \
"v_mfma_f32_16x16x32_bf16 v[104:107], a[168:171], v[204:207], v[104:107]\n" \
"ds_read_b64_tr_b16 a[162:163], %[vvb] offset:28672\n" \
"ds_read_b64_tr_b16 a[164:165], v211 offset:24576\n" \
"v_mfma_f32_16x16x32_bf16 v[104:107], a[136:139], v[220:223], v[104:107]\n" \
"v_exp_f32_e64 v146, v146\n" \
"v_mfma_f32_16x16x32_bf16 v[108:111], a[172:175], v[204:207], v[108:111]\n" \
"ds_read_b64_tr_b16 a[166:167], v211 offset:28672\n" \
"v_mfma_f32_16x16x32_bf16 v[108:111], a[140:143], v[220:223], v[108:111]\n" \
"v_exp_f32_e64 v147, v147\n" \
"v_smfmac_f32_16x16x64_bf16 v[180:183], a[192:195], v[216:223], v228\n" \
"ds_read_b64_tr_b16 a[168:169], %[vvb] offset:25088\n" \
"buffer_store_dwordx4 a[196:199], v235, %[srdz], s62 offen offset:0 sc1\n" \
"v_mfma_f32_16x16x32_bf16 v[152:155], a[96:99], a[48:51], v[172:175]\n" \
"v_exp_f32_e64 v148, v148\n" \
"v_mfma_f32_16x16x32_bf16 v[152:155], a[100:103], a[52:55], v[152:155]\n" \
"ds_read_b64_tr_b16 a[170:171], %[vvb] offset:29184\n" \
"v_mfma_f32_16x16x32_bf16 v[152:155], a[104:107], a[56:59], v[152:155]\n" \
"v_exp_f32_e64 v149, v149\n" \
"v_mfma_f32_16x16x32_bf16 v[152:155], a[108:111], a[60:63], v[152:155]\n" \
"v_cvt_pk_bf16_f32 v200, v144, v145\n" \
"v_cvt_pk_bf16_f32 v201, v146, v147\n" \
"v_mfma_f32_16x16x32_bf16 v[156:159], a[124:127], a[60:63], v[172:175]\n" \
"v_exp_f32_e64 v150, v150\n" \
"v_mfma_f32_16x16x32_bf16 v[156:159], a[120:123], a[56:59], v[156:159]\n" \
"ds_read_b64_tr_b16 a[172:173], v211 offset:25088\n" \
"v_mfma_f32_16x16x32_bf16 v[156:159], a[116:119], a[52:55], v[156:159]\n" \
"buffer_load_dwordx4 v215, %[srdv], s48 offen offset:3072 lds\n" \
"v_mfma_f32_16x16x32_bf16 v[156:159], a[112:115], a[48:51], v[156:159]\n" \
"v_exp_f32_e64 v151, v151\n" \
"v_mfma_f32_16x16x32_bf16 v[112:115], a[176:179], v[204:207], v[112:115]\n" \
"ds_read_b64_tr_b16 a[174:175], v211 offset:29184\n" \
"ds_read_b64_tr_b16 a[176:177], %[vvb] offset:25600\n" \
"v_mfma_f32_16x16x32_bf16 v[112:115], a[144:147], v[220:223], v[112:115]\n" \
"v_exp_f32_e64 v152, v152\n" \
"v_mfma_f32_16x16x32_bf16 v[116:119], a[180:183], v[204:207], v[116:119]\n" \
"v_cvt_pk_bf16_f32 v202, v148, v149\n" \
"v_cvt_pk_bf16_f32 v203, v150, v151\n" \
"v_mfma_f32_16x16x32_bf16 v[116:119], a[148:151], v[220:223], v[116:119]\n" \
"v_exp_f32_e64 v153, v153\n" \
"v_mfma_f32_16x16x32_bf16 v[120:123], a[184:187], v[204:207], v[120:123]\n" \
"v_exp_f32_e64 v154, v154\n" \
"v_mfma_f32_16x16x32_bf16 v[120:123], a[152:155], v[220:223], v[120:123]\n" \
"ds_read_b64_tr_b16 a[178:179], %[vvb] offset:29696\n" \
"v_mfma_f32_16x16x32_bf16 v[124:127], a[188:191], v[204:207], v[124:127]\n" \
"v_exp_f32_e64 v155, v155\n" \
"v_mfma_f32_16x16x32_bf16 v[124:127], a[156:159], v[220:223], v[124:127]\n" \
"ds_read_b64_tr_b16 a[180:181], v211 offset:25600\n" \
"s_waitcnt lgkmcnt(13)\n" \
"v_mfma_f32_16x16x32_bf16 v[128:131], a[64:67], a[0:3], v[160:163]\n" \
"v_exp_f32_e32 v156, v156\n" \
"v_mfma_f32_16x16x32_bf16 v[128:131], a[68:71], a[4:7], v[128:131]\n" \
"ds_read_b64_tr_b16 a[182:183], v211 offset:29696\n" \
"s_add_u32 m0, %[m0b], 16384\n" \
"v_mfma_f32_16x16x32_bf16 v[128:131], a[72:75], a[8:11], v[128:131]\n" \
"v_exp_f32_e64 v157, v157\n" \
"v_mfma_f32_16x16x32_bf16 v[128:131], a[76:79], a[12:15], v[128:131]\n" \
"v_cvt_pk_bf16_f32 v204, v152, v153\n" \
"v_cvt_pk_bf16_f32 v205, v154, v155\n" \
"v_mfma_f32_16x16x32_bf16 v[132:135], a[92:95], a[12:15], v[160:163]\n" \
"v_exp_f32_e64 v158, v158\n" \
"v_mfma_f32_16x16x32_bf16 v[132:135], a[88:91], a[8:11], v[132:135]\n" \
"ds_read_b64_tr_b16 a[184:185], %[vvb] offset:26112\n" \
"s_waitcnt lgkmcnt(5)\n" \
"v_mfma_f32_16x16x32_bf16 v[132:135], a[84:87], a[4:7], v[132:135]\n" \
"buffer_load_dwordx4 %[kd0], %[srdk], s48 offen offset:0 lds\n" \
"v_mfma_f32_16x16x32_bf16 v[132:135], a[80:83], a[0:3], v[132:135]\n" \
"v_exp_f32_e32 v159, v159\n" \
"v_mfma_f32_16x16x32_bf16 v[64:67], a[128:131], v[216:219], v[64:67]\n" \
"ds_read_b64_tr_b16 a[186:187], %[vvb] offset:30208\n" \
"ds_read_b64_tr_b16 a[188:189], v211 offset:26112\n" \
"v_mfma_f32_16x16x32_bf16 v[64:67], a[160:163], v[200:203], v[64:67]\n" \
"v_exp_f32_e64 v128, v128\n" \
"v_mfma_f32_16x16x32_bf16 v[68:71], a[132:135], v[216:219], v[68:71]\n" \
"v_cvt_pk_bf16_f32 v206, v156, v157\n" \
"v_cvt_pk_bf16_f32 v207, v158, v159\n" \
"v_mfma_f32_16x16x32_bf16 v[68:71], a[164:167], v[200:203], v[68:71]\n" \
"v_exp_f32_e64 v129, v129\n" \
"v_mfma_f32_16x16x32_bf16 v[72:75], a[136:139], v[216:219], v[72:75]\n" \
"ds_read_b64_tr_b16 a[190:191], v211 offset:30208\n" \
"ds_read_b64_tr_b16 a[128:129], %[vvb] offset:32768\n" \
"v_mfma_f32_16x16x32_bf16 v[72:75], a[168:171], v[200:203], v[72:75]\n" \
"v_exp_f32_e64 v130, v130\n" \
"v_mfma_f32_16x16x32_bf16 v[76:79], a[140:143], v[216:219], v[76:79]\n" \
"ds_read_b64_tr_b16 a[130:131], %[vvb] offset:36864\n" \
"ds_read_b64_tr_b16 a[132:133], v211 offset:32768\n" \
"v_mfma_f32_16x16x32_bf16 v[76:79], a[172:175], v[200:203], v[76:79]\n" \
"v_exp_f32_e64 v131, v131\n" \
"v_smfmac_f32_16x16x64_bf16 v[176:179], a[192:195], v[192:199], v228\n" \
"ds_read_b64_tr_b16 a[134:135], v211 offset:36864\n" \
"ds_read_b64_tr_b16 a[136:137], %[vvb] offset:33280\n" \
"v_mfma_f32_16x16x32_bf16 v[136:139], a[64:67], a[16:19], v[164:167]\n" \
"v_exp_f32_e64 v132, v132\n" \
".if . - adaln_body != 24576\n" \
".error \"size model\"\n" \
".endif\n" \
"Lk5_%=:\n" \
"s_cbranch_execz adaln_kr\n" \
"s_nop 0\n" \
"v_mfma_f32_16x16x32_bf16 v[136:139], a[68:71], a[20:23], v[136:139]\n" \
"ds_read_b64_tr_b16 a[138:139], %[vvb] offset:37376\n" \
"ds_read_b64_tr_b16 a[140:141], v211 offset:33280\n" \
"v_mfma_f32_16x16x32_bf16 v[136:139], a[72:75], a[24:27], v[136:139]\n" \
"v_exp_f32_e64 v133, v133\n" \
"v_mfma_f32_16x16x32_bf16 v[136:139], a[76:79], a[28:31], v[136:139]\n" \
"v_cvt_pk_bf16_f32 v184, v128, v129\n" \
"v_cvt_pk_bf16_f32 v185, v130, v131\n" \
"v_mfma_f32_16x16x32_bf16 v[140:143], a[92:95], a[28:31], v[164:167]\n" \
"v_exp_f32_e64 v134, v134\n" \
"v_mfma_f32_16x16x32_bf16 v[140:143], a[88:91], a[24:27], v[140:143]\n" \
"ds_read_b64_tr_b16 a[142:143], v211 offset:37376\n" \
"s_waitcnt lgkmcnt(8)\n" \
"v_mfma_f32_16x16x32_bf16 v[140:143], a[84:87], a[20:23], v[140:143]\n" \
"buffer_load_dwordx4 v212, %[srdk], s48 offen offset:1024 lds\n" \
"v_mfma_f32_16x16x32_bf16 v[140:143], a[80:83], a[16:19], v[140:143]\n" \
"v_exp_f32_e32 v135, v135\n" \
"v_mfma_f32_16x16x32_bf16 v[80:83], a[144:147], v[216:219], v[80:83]\n" \
"ds_read_b64_tr_b16 a[144:145], %[vvb] offset:33792\n" \
"ds_read_b64_tr_b16 a[146:147], %[vvb] offset:37888\n" \
"v_mfma_f32_16x16x32_bf16 v[80:83], a[176:179], v[200:203], v[80:83]\n" \
"v_exp_f32_e64 v136, v136\n" \
"v_mfma_f32_16x16x32_bf16 v[84:87], a[148:151], v[216:219], v[84:87]\n" \
"v_cvt_pk_bf16_f32 v186, v132, v133\n" \
"v_cvt_pk_bf16_f32 v187, v134, v135\n" \
"v_mfma_f32_16x16x32_bf16 v[84:87], a[180:183], v[200:203], v[84:87]\n" \
"v_exp_f32_e64 v137, v137\n" \
"v_mfma_f32_16x16x32_bf16 v[88:91], a[152:155], v[216:219], v[88:91]\n" \
"v_exp_f32_e64 v138, v138\n" \
"v_mfma_f32_16x16x32_bf16 v[88:91], a[184:187], v[200:203], v[88:91]\n" \
"ds_read_b64_tr_b16 a[148:149], v211 offset:33792\n" \
"ds_read_b64_tr_b16 a[150:151], v211 offset:37888\n" \
"v_mfma_f32_16x16x32_bf16 v[92:95], a[156:159], v[216:219], v[92:95]\n" \
"v_exp_f32_e64 v139, v139\n" \
"v_mfma_f32_16x16x32_bf16 v[92:95], a[188:191], v[200:203], v[92:95]\n" \
"ds_read_b64_tr_b16 a[152:153], %[vvb] offset:34304\n" \
"buffer_store_dwordx4 a[196:199], v235, %[srdz], s63 offen offset:1024 sc1\n" \
"v_mfma_f32_16x16x32_bf16 v[144:147], a[64:67], a[32:35], v[168:171]\n" \
"v_exp_f32_e64 v140, v140\n" \
"v_mfma_f32_16x16x32_bf16 v[144:147], a[68:71], a[36:39], v[144:147]\n" \
"ds_read_b64_tr_b16 a[154:155], %[vvb] offset:38400\n" \
"ds_read_b64_tr_b16 a[156:157], v211 offset:34304\n" \
"v_mfma_f32_16x16x32_bf16 v[144:147], a[72:75], a[40:43], v[144:147]\n" \
"v_exp_f32_e64 v141, v141\n" \
"v_mfma_f32_16x16x32_bf16 v[144:147], a[76:79], a[44:47], v[144:147]\n" \
"v_cvt_pk_bf16_f32 v188, v136, v137\n" \
"v_cvt_pk_bf16_f32 v189, v138, v139\n" \
"v_mfma_f32_16x16x32_bf16 v[148:151], a[92:95], a[44:47], v[168:171]\n" \
"v_exp_f32_e64 v142, v142\n" \
"v_mfma_f32_16x16x32_bf16 v[148:151], a[88:91], a[40:43], v[148:151]\n" \
"ds_read_b64_tr_b16 a[158:159], v211 offset:38400\n" \
"s_waitcnt lgkmcnt(8)\n" \
"v_mfma_f32_16x16x32_bf16 v[148:151], a[84:87], a[36:39], v[148:151]\n" \
"buffer_load_dwordx4 v213, %[srdk], s48 offen offset:2048 lds\n" \
"v_mfma_f32_16x16x32_bf16 v[148:151], a[80:83], a[32:35], v[148:151]\n" \
"v_exp_f32_e32 v143, v143\n" \
"v_mfma_f32_16x16x32_bf16 v[0:3], a[160:163], v[192:195], v[0:3]\n" \
"ds_read_b128 a[96:99], %[kb0] offset:40960\n" \
"v_mfma_f32_16x16x32_bf16 v[0:3], a[128:131], v[184:187], v[0:3]\n" \
"v_exp_f32_e64 v144, v144\n" \
"v_mfma_f32_16x16x32_bf16 v[4:7], a[164:167], v[192:195], v[4:7]\n" \
"v_cvt_pk_bf16_f32 v190, v140, v141\n" \
"v_cvt_pk_bf16_f32 v191, v142, v143\n" \
"v_mfma_f32_16x16x32_bf16 v[4:7], a[132:135], v[184:187], v[4:7]\n" \
"v_exp_f32_e64 v145, v145\n" \
"v_mfma_f32_16x16x32_bf16 v[8:11], a[168:171], v[192:195], v[8:11]\n" \
"ds_read_b128 a[100:103], v208 offset:40960\n" \
"v_mfma_f32_16x16x32_bf16 v[8:11], a[136:139], v[184:187], v[8:11]\n" \
"v_exp_f32_e64 v146, v146\n" \
"v_mfma_f32_16x16x32_bf16 v[12:15], a[172:175], v[192:195], v[12:15]\n" \
"ds_read_b128 a[104:107], v209 offset:40960\n" \
"v_mfma_f32_16x16x32_bf16 v[12:15], a[140:143], v[184:187], v[12:15]\n" \
"v_exp_f32_e64 v147, v147\n" \
"v_smfmac_f32_16x16x64_bf16 v[180:183], a[192:195], v[200:207], v228\n" \
"ds_read_b128 a[108:111], v210 offset:40960\n" \
"v_mfma_f32_16x16x32_bf16 v[152:155], a[64:67], a[48:51], v[172:175]\n" \
"v_exp_f32_e64 v148, v148\n" \
"v_mfma_f32_16x16x32_bf16 v[152:155], a[68:71], a[52:55], v[152:155]\n" \
"ds_read_b128 a[124:127], v210 offset:45056\n" \
"v_mfma_f32_16x16x32_bf16 v[152:155], a[72:75], a[56:59], v[152:155]\n" \
"v_exp_f32_e64 v149, v149\n" \
"v_mfma_f32_16x16x32_bf16 v[152:155], a[76:79], a[60:63], v[152:155]\n" \
"v_cvt_pk_bf16_f32 v216, v144, v145\n" \
"v_cvt_pk_bf16_f32 v217, v146, v147\n" \
"v_mfma_f32_16x16x32_bf16 v[156:159], a[92:95], a[60:63], v[172:175]\n" \
"v_exp_f32_e64 v150, v150\n" \
"v_mfma_f32_16x16x32_bf16 v[156:159], a[88:91], a[56:59], v[156:159]\n" \
"ds_read_b128 a[120:123], v209 offset:45056\n" \
"s_waitcnt lgkmcnt(6)\n" \
"v_mfma_f32_16x16x32_bf16 v[156:159], a[84:87], a[52:55], v[156:159]\n" \
"buffer_load_dwordx4 v214, %[srdk], s48 offen offset:3072 lds\n" \
"v_mfma_f32_16x16x32_bf16 v[156:159], a[80:83], a[48:51], v[156:159]\n" \
"v_exp_f32_e32 v151, v151\n" \
"v_mfma_f32_16x16x32_bf16 v[16:19], a[176:179], v[192:195], v[16:19]\n" \
"ds_read_b128 a[116:119], v208 offset:45056\n" \
"v_mfma_f32_16x16x32_bf16 v[16:19], a[144:147], v[184:187], v[16:19]\n" \
"v_exp_f32_e64 v152, v152\n" \
"v_mfma_f32_16x16x32_bf16 v[20:23], a[180:183], v[192:195], v[20:23]\n" \
"v_cvt_pk_bf16_f32 v218, v148, v149\n" \
"v_cvt_pk_bf16_f32 v219, v150, v151\n" \
"v_mfma_f32_16x16x32_bf16 v[20:23], a[148:151], v[184:187], v[20:23]\n" \
"v_exp_f32_e64 v153, v153\n" \
"v_mfma_f32_16x16x32_bf16 v[24:27], a[184:187], v[192:195], v[24:27]\n" \
"v_exp_f32_e64 v154, v154\n" \
"v_mfma_f32_16x16x32_bf16 v[24:27], a[152:155], v[184:187], v[24:27]\n" \
"ds_read_b128 a[112:115], %[kb0] offset:45056\n" \
"s_add_u32 s48, s48, 0x4000\n" \
"v_mfma_f32_16x16x32_bf16 v[28:31], a[188:191], v[192:195], v[28:31]\n" \
"v_exp_f32_e64 v155, v155\n" \
"v_mfma_f32_16x16x32_bf16 v[28:31], a[156:159], v[184:187], v[28:31]\n" \
"s_waitcnt vmcnt(16) lgkmcnt(2)\n" \
"s_barrier\n" \
"v_mfma_f32_16x16x32_bf16 v[128:131], a[96:99], a[0:3], v[160:163]\n" \
"v_exp_f32_e64 v156, v156\n" \
"v_mfma_f32_16x16x32_bf16 v[128:131], a[100:103], a[4:7], v[128:131]\n" \
"s_add_u32 m0, %[m0b], 81920\n" \
"v_mfma_f32_16x16x32_bf16 v[128:131], a[104:107], a[8:11], v[128:131]\n" \
"v_exp_f32_e64 v157, v157\n" \
"v_mfma_f32_16x16x32_bf16 v[128:131], a[108:111], a[12:15], v[128:131]\n" \
"v_cvt_pk_bf16_f32 v220, v152, v153\n" \
"v_cvt_pk_bf16_f32 v221, v154, v155\n" \
"v_mfma_f32_16x16x32_bf16 v[132:135], a[124:127], a[12:15], v[160:163]\n" \
"v_exp_f32_e64 v158, v158\n" \
"v_mfma_f32_16x16x32_bf16 v[132:135], a[120:123], a[8:11], v[132:135]\n" \
"s_waitcnt lgkmcnt(0)\n" \
"v_mfma_f32_16x16x32_bf16 v[132:135], a[116:119], a[4:7], v[132:135]\n" \
"buffer_load_dwordx4 %[vd0], %[srdv], s48 offen offset:0 lds\n" \
"v_mfma_f32_16x16x32_bf16 v[132:135], a[112:115], a[0:3], v[132:135]\n" \
"v_exp_f32_e32 v159, v159\n" \
"v_mfma_f32_16x16x32_bf16 v[32:35], a[160:163], v[196:199], v[32:35]\n" \
"ds_read_b128 a[64:67], %[kb0] offset:49152\n" \
"v_mfma_f32_16x16x32_bf16 v[32:35], a[128:131], v[188:191], v[32:35]\n" \
"v_exp_f32_e64 v128, v128\n" \
"v_mfma_f32_16x16x32_bf16 v[36:39], a[164:167], v[196:199], v[36:39]\n" \
"v_cvt_pk_bf16_f32 v222, v156, v157\n" \
"v_cvt_pk_bf16_f32 v223, v158, v159\n" \
"v_mfma_f32_16x16x32_bf16 v[36:39], a[132:135], v[188:191], v[36:39]\n" \
"v_exp_f32_e64 v129, v129\n" \
"v_mfma_f32_16x16x32_bf16 v[40:43], a[168:171], v[196:199], v[40:43]\n" \
"ds_read_b128 a[68:71], v208 offset:49152\n" \
"v_mfma_f32_16x16x32_bf16 v[40:43], a[136:139], v[188:191], v[40:43]\n" \
"v_exp_f32_e64 v130, v130\n" \
"v_mfma_f32_16x16x32_bf16 v[44:47], a[172:175], v[196:199], v[44:47]\n" \
"ds_read_b128 a[72:75], v209 offset:49152\n" \
"v_mfma_f32_16x16x32_bf16 v[44:47], a[140:143], v[188:191], v[44:47]\n" \
"v_exp_f32_e64 v131, v131\n" \
"v_smfmac_f32_16x16x64_bf16 v[176:179], a[192:195], v[184:191], v228\n" \
"v_mfma_f32_16x16x32_bf16 v[136:139], a[96:99], a[16:19], v[164:167]\n" \
"v_exp_f32_e64 v132, v132\n" \
"v_mfma_f32_16x16x32_bf16 v[136:139], a[100:103], a[20:23], v[136:139]\n" \
"ds_read_b128 a[76:79], v210 offset:49152\n" \
"v_mfma_f32_16x16x32_bf16 v[136:139], a[104:107], a[24:27], v[136:139]\n" \
"v_exp_f32_e64 v133, v133\n" \
"v_mfma_f32_16x16x32_bf16 v[136:139], a[108:111], a[28:31], v[136:139]\n" \
"v_cvt_pk_bf16_f32 v192, v128, v129\n" \
"v_cvt_pk_bf16_f32 v193, v130, v131\n" \
"v_mfma_f32_16x16x32_bf16 v[140:143], a[124:127], a[28:31], v[164:167]\n" \
"v_exp_f32_e64 v134, v134\n" \
"v_mfma_f32_16x16x32_bf16 v[140:143], a[120:123], a[24:27], v[140:143]\n" \
"ds_read_b128 a[92:95], v210 offset:53248\n" \
"v_mfma_f32_16x16x32_bf16 v[140:143], a[116:119], a[20:23], v[140:143]\n" \
"buffer_load_dwordx4 v215, %[srdv], s48 offen offset:1024 lds\n" \
"v_mfma_f32_16x16x32_bf16 v[140:143], a[112:115], a[16:19], v[140:143]\n" \
"v_exp_f32_e64 v135, v135\n" \
"v_mfma_f32_16x16x32_bf16 v[48:51], a[176:179], v[196:199], v[48:51]\n" \
"ds_read_b128 a[88:91], v209 offset:53248\n" \
"v_mfma_f32_16x16x32_bf16 v[48:51], a[144:147], v[188:191], v[48:51]\n" \
"v_exp_f32_e64 v136, v136\n" \
"v_mfma_f32_16x16x32_bf16 v[52:55], a[180:183], v[196:199], v[52:55]\n" \
"v_cvt_pk_bf16_f32 v194, v132, v133\n" \
"v_cvt_pk_bf16_f32 v195, v134, v135\n" \
"v_mfma_f32_16x16x32_bf16 v[52:55], a[148:151], v[188:191], v[52:55]\n" \
"v_exp_f32_e64 v137, v137\n" \
"v_mfma_f32_16x16x32_bf16 v[56:59], a[184:187], v[196:199], v[56:59]\n" \
"v_exp_f32_e64 v138, v138\n" \
"v_mfma_f32_16x16x32_bf16 v[56:59], a[152:155], v[188:191], v[56:59]\n" \
"ds_read_b128 a[84:87], v208 offset:53248\n" \
"v_mfma_f32_16x16x32_bf16 v[60:63], a[188:191], v[196:199], v[60:63]\n" \
"v_exp_f32_e64 v139, v139\n" \
"v_mfma_f32_16x16x32_bf16 v[60:63], a[156:159], v[188:191], v[60:63]\n" \
"s_cmp_lg_u32 %[wid], 1\n" \
"s_cbranch_scc1 LtB5_%=\n" \
"global_load_dword v237, v236, s[58:59]\n" \
"s_add_u32 s61, s48, 49152\n" \
"s_mov_b32 exec_hi, 0\n" \
"s_nop 0\n" \
"buffer_load_dword v237, v224, %[srdp], s61 offen\n" \
"s_mov_b32 exec_hi, -1\n" \
"s_nop 0\n" \
"LtB5_%=:\n" \
"v_mfma_f32_16x16x32_bf16 v[144:147], a[96:99], a[32:35], v[168:171]\n" \
"v_exp_f32_e64 v140, v140\n" \
"v_mfma_f32_16x16x32_bf16 v[144:147], a[100:103], a[36:39], v[144:147]\n" \
"ds_read_b128 a[80:83], %[kb0] offset:53248\n" \
"v_mfma_f32_16x16x32_bf16 v[144:147], a[104:107], a[40:43], v[144:147]\n" \
"v_exp_f32_e64 v141, v141\n" \
"v_mfma_f32_16x16x32_bf16 v[144:147], a[108:111], a[44:47], v[144:147]\n" \
"v_cvt_pk_bf16_f32 v196, v136, v137\n" \
"v_cvt_pk_bf16_f32 v197, v138, v139\n" \
"v_mfma_f32_16x16x32_bf16 v[148:151], a[124:127], a[44:47], v[168:171]\n" \
"v_exp_f32_e64 v142, v142\n" \
"v_mfma_f32_16x16x32_bf16 v[148:151], a[120:123], a[40:43], v[148:151]\n" \
"v_mfma_f32_16x16x32_bf16 v[148:151], a[116:119], a[36:39], v[148:151]\n" \
"buffer_load_dwordx4 %[vd0], %[srdv], s48 offen offset:2048 lds\n" \
"v_mfma_f32_16x16x32_bf16 v[148:151], a[112:115], a[32:35], v[148:151]\n" \
"v_exp_f32_e64 v143, v143\n" \
"v_mfma_f32_16x16x32_bf16 v[96:99], a[160:163], v[204:207], v[96:99]\n" \
"ds_read_b64_tr_b16 a[160:161], %[vvb] offset:40960\n" \
"v_mfma_f32_16x16x32_bf16 v[96:99], a[128:131], v[220:223], v[96:99]\n" \
"v_exp_f32_e64 v144, v144\n" \
"v_mfma_f32_16x16x32_bf16 v[100:103], a[164:167], v[204:207], v[100:103]\n" \
"v_cvt_pk_bf16_f32 v198, v140, v141\n" \
"v_cvt_pk_bf16_f32 v199, v142, v143\n" \
"v_mfma_f32_16x16x32_bf16 v[100:103], a[132:135], v[220:223], v[100:103]\n" \
"v_exp_f32_e64 v145, v145\n" \
"v_mfma_f32_16x16x32_bf16 v[104:107], a[168:171], v[204:207], v[104:107]\n" \
"ds_read_b64_tr_b16 a[162:163], %[vvb] offset:45056\n" \
"ds_read_b64_tr_b16 a[164:165], v211 offset:40960\n" \
"v_mfma_f32_16x16x32_bf16 v[104:107], a[136:139], v[220:223], v[104:107]\n" \
"v_exp_f32_e64 v146, v146\n" \
"v_mfma_f32_16x16x32_bf16 v[108:111], a[172:175], v[204:207], v[108:111]\n" \
"ds_read_b64_tr_b16 a[166:167], v211 offset:45056\n" \
"v_mfma_f32_16x16x32_bf16 v[108:111], a[140:143], v[220:223], v[108:111]\n" \
"v_exp_f32_e64 v147, v147\n" \
"v_smfmac_f32_16x16x64_bf16 v[180:183], a[192:195], v[216:223], v228\n" \
"ds_read_b64_tr_b16 a[168:169], %[vvb] offset:41472\n" \
"v_mfma_f32_16x16x32_bf16 v[152:155], a[96:99], a[48:51], v[172:175]\n" \
"v_exp_f32_e64 v148, v148\n" \
"v_mfma_f32_16x16x32_bf16 v[152:155], a[100:103], a[52:55], v[152:155]\n" \
"ds_read_b64_tr_b16 a[170:171], %[vvb] offset:45568\n" \
"v_mfma_f32_16x16x32_bf16 v[152:155], a[104:107], a[56:59], v[152:155]\n" \
"v_exp_f32_e64 v149, v149\n" \
"v_mfma_f32_16x16x32_bf16 v[152:155], a[108:111], a[60:63], v[152:155]\n" \
"v_cvt_pk_bf16_f32 v200, v144, v145\n" \
"v_cvt_pk_bf16_f32 v201, v146, v147\n" \
"v_mfma_f32_16x16x32_bf16 v[156:159], a[124:127], a[60:63], v[172:175]\n" \
"v_exp_f32_e64 v150, v150\n" \
"v_mfma_f32_16x16x32_bf16 v[156:159], a[120:123], a[56:59], v[156:159]\n" \
"ds_read_b64_tr_b16 a[172:173], v211 offset:41472\n" \
"v_mfma_f32_16x16x32_bf16 v[156:159], a[116:119], a[52:55], v[156:159]\n" \
"buffer_load_dwordx4 v215, %[srdv], s48 offen offset:3072 lds\n" \
"v_mfma_f32_16x16x32_bf16 v[156:159], a[112:115], a[48:51], v[156:159]\n" \
"v_exp_f32_e64 v151, v151\n" \
"v_mfma_f32_16x16x32_bf16 v[112:115], a[176:179], v[204:207], v[112:115]\n" \
"ds_read_b64_tr_b16 a[174:175], v211 offset:45568\n" \
"ds_read_b64_tr_b16 a[176:177], %[vvb] offset:41984\n" \
"v_mfma_f32_16x16x32_bf16 v[112:115], a[144:147], v[220:223], v[112:115]\n" \
"v_exp_f32_e64 v152, v152\n" \
"v_mfma_f32_16x16x32_bf16 v[116:119], a[180:183], v[204:207], v[116:119]\n" \
"v_cvt_pk_bf16_f32 v202, v148, v149\n" \
"v_cvt_pk_bf16_f32 v203, v150, v151\n" \
"v_mfma_f32_16x16x32_bf16 v[116:119], a[148:151], v[220:223], v[116:119]\n" \
"v_exp_f32_e64 v153, v153\n" \
"v_mfma_f32_16x16x32_bf16 v[120:123], a[184:187], v[204:207], v[120:123]\n" \
"v_exp_f32_e64 v154, v154\n" \
"v_mfma_f32_16x16x32_bf16 v[120:123], a[152:155], v[220:223], v[120:123]\n" \
"ds_read_b64_tr_b16 a[178:179], %[vvb] offset:46080\n" \
"v_mfma_f32_16x16x32_bf16 v[124:127], a[188:191], v[204:207], v[124:127]\n" \
"v_exp_f32_e64 v155, v155\n" \
"v_mfma_f32_16x16x32_bf16 v[124:127], a[156:159], v[220:223], v[124:127]\n" \
"ds_read_b64_tr_b16 a[180:181], v211 offset:41984\n" \
"s_waitcnt lgkmcnt(13)\n" \
"v_mfma_f32_16x16x32_bf16 v[128:131], a[64:67], a[0:3], v[160:163]\n" \
"v_exp_f32_e32 v156, v156\n" \
"v_mfma_f32_16x16x32_bf16 v[128:131], a[68:71], a[4:7], v[128:131]\n" \
"ds_read_b64_tr_b16 a[182:183], v211 offset:46080\n" \
"s_add_u32 m0, %[m0b], 32768\n" \
"v_mfma_f32_16x16x32_bf16 v[128:131], a[72:75], a[8:11], v[128:131]\n" \
"v_exp_f32_e64 v157, v157\n" \
"v_mfma_f32_16x16x32_bf16 v[128:131], a[76:79], a[12:15], v[128:131]\n" \
"v_cvt_pk_bf16_f32 v204, v152, v153\n" \
"v_cvt_pk_bf16_f32 v205, v154, v155\n" \
"v_mfma_f32_16x16x32_bf16 v[132:135], a[92:95], a[12:15], v[160:163]\n" \
"v_exp_f32_e64 v158, v158\n" \
"v_mfma_f32_16x16x32_bf16 v[132:135], a[88:91], a[8:11], v[132:135]\n" \
"ds_read_b64_tr_b16 a[184:185], %[vvb] offset:42496\n" \
"s_waitcnt lgkmcnt(5)\n" \
"v_mfma_f32_16x16x32_bf16 v[132:135], a[84:87], a[4:7], v[132:135]\n" \
"buffer_load_dwordx4 %[kd0], %[srdk], s48 offen offset:0 lds\n" \
"v_mfma_f32_16x16x32_bf16 v[132:135], a[80:83], a[0:3], v[132:135]\n" \
"v_exp_f32_e32 v159, v159\n" \
"v_mfma_f32_16x16x32_bf16 v[64:67], a[128:131], v[216:219], v[64:67]\n" \
"ds_read_b64_tr_b16 a[186:187], %[vvb] offset:46592\n" \
"ds_read_b64_tr_b16 a[188:189], v211 offset:42496\n" \
"v_mfma_f32_16x16x32_bf16 v[64:67], a[160:163], v[200:203], v[64:67]\n" \
"v_exp_f32_e64 v128, v128\n" \
"v_mfma_f32_16x16x32_bf16 v[68:71], a[132:135], v[216:219], v[68:71]\n" \
"v_cvt_pk_bf16_f32 v206, v156, v157\n" \
"v_cvt_pk_bf16_f32 v207, v158, v159\n" \
"v_mfma_f32_16x16x32_bf16 v[68:71], a[164:167], v[200:203], v[68:71]\n" \
"v_exp_f32_e64 v129, v129\n" \
"v_mfma_f32_16x16x32_bf16 v[72:75], a[136:139], v[216:219], v[72:75]\n" \
"ds_read_b64_tr_b16 a[190:191], v211 offset:46592\n" \
"ds_read_b64_tr_b16 a[128:129], %[vvb] offset:49152\n" \
"v_mfma_f32_16x16x32_bf16 v[72:75], a[168:171], v[200:203], v[72:75]\n" \
"v_exp_f32_e64 v130, v130\n" \
"v_mfma_f32_16x16x32_bf16 v[76:79], a[140:143], v[216:219], v[76:79]\n" \
"ds_read_b64_tr_b16 a[130:131], %[vvb] offset:53248\n" \
"ds_read_b64_tr_b16 a[132:133], v211 offset:49152\n" \
"v_mfma_f32_16x16x32_bf16 v[76:79], a[172:175], v[200:203], v[76:79]\n" \
"v_exp_f32_e64 v131, v131\n" \
"v_smfmac_f32_16x16x64_bf16 v[176:179], a[192:195], v[192:199], v228\n" \
"ds_read_b64_tr_b16 a[134:135], v211 offset:53248\n" \
"ds_read_b64_tr_b16 a[136:137], %[vvb] offset:49664\n" \
"buffer_store_dwordx4 a[196:199], v235, %[srdz], s62 offen offset:1024 sc1\n" \
"v_mfma_f32_16x16x32_bf16 v[136:139], a[64:67], a[16:19], v[164:167]\n" \
"v_exp_f32_e64 v132, v132\n" \
"v_mfma_f32_16x16x32_bf16 v[136:139], a[68:71], a[20:23], v[136:139]\n" \
"ds_read_b64_tr_b16 a[138:139], %[vvb] offset:53760\n" \
"ds_read_b64_tr_b16 a[140:141], v211 offset:49664\n" \
"v_mfma_f32_16x16x32_bf16 v[136:139], a[72:75], a[24:27], v[136:139]\n" \
"v_exp_f32_e64 v133, v133\n" \
"v_mfma_f32_16x16x32_bf16 v[136:139], a[76:79], a[28:31], v[136:139]\n" \
"v_cvt_pk_bf16_f32 v184, v128, v129\n" \
"v_cvt_pk_bf16_f32 v185, v130, v131\n" \
"v_mfma_f32_16x16x32_bf16 v[140:143], a[92:95], a[28:31], v[164:167]\n" \
"v_exp_f32_e64 v134, v134\n" \
"v_mfma_f32_16x16x32_bf16 v[140:143], a[88:91], a[24:27], v[140:143]\n" \
"ds_read_b64_tr_b16 a[142:143], v211 offset:53760\n" \
"s_waitcnt lgkmcnt(8)\n" \
"v_mfma_f32_16x16x32_bf16 v[140:143], a[84:87], a[20:23], v[140:143]\n" \
"buffer_load_dwordx4 v212, %[srdk], s48 offen offset:1024 lds\n" \
"v_mfma_f32_16x16x32_bf16 v[140:143], a[80:83], a[16:19], v[140:143]\n" \
"v_exp_f32_e32 v135, v135\n" \
"v_mfma_f32_16x16x32_bf16 v[80:83], a[144:147], v[216:219], v[80:83]\n" \
"ds_read_b64_tr_b16 a[144:145], %[vvb] offset:50176\n" \
"ds_read_b64_tr_b16 a[146:147], %[vvb] offset:54272\n" \
"v_mfma_f32_16x16x32_bf16 v[80:83], a[176:179], v[200:203], v[80:83]\n" \
"v_exp_f32_e64 v136, v136\n" \
"v_mfma_f32_16x16x32_bf16 v[84:87], a[148:151], v[216:219], v[84:87]\n" \
"v_cvt_pk_bf16_f32 v186, v132, v133\n" \
"v_cvt_pk_bf16_f32 v187, v134, v135\n" \
"v_mfma_f32_16x16x32_bf16 v[84:87], a[180:183], v[200:203], v[84:87]\n" \
"v_exp_f32_e64 v137, v137\n" \
"v_mfma_f32_16x16x32_bf16 v[88:91], a[152:155], v[216:219], v[88:91]\n" \
"v_exp_f32_e64 v138, v138\n" \
"v_mfma_f32_16x16x32_bf16 v[88:91], a[184:187], v[200:203], v[88:91]\n" \
"ds_read_b64_tr_b16 a[148:149], v211 offset:50176\n" \
"ds_read_b64_tr_b16 a[150:151], v211 offset:54272\n" \
"v_mfma_f32_16x16x32_bf16 v[92:95], a[156:159], v[216:219], v[92:95]\n" \
"v_exp_f32_e64 v139, v139\n" \
"v_mfma_f32_16x16x32_bf16 v[92:95], a[188:191], v[200:203], v[92:95]\n" \
"ds_read_b64_tr_b16 a[152:153], %[vvb] offset:50688\n" \
"global_load_dword v237, %[vct], %[pct]\n" \
"v_mfma_f32_16x16x32_bf16 v[144:147], a[64:67], a[32:35], v[168:171]\n" \
"v_exp_f32_e64 v140, v140\n" \
"v_mfma_f32_16x16x32_bf16 v[144:147], a[68:71], a[36:39], v[144:147]\n" \
"ds_read_b64_tr_b16 a[154:155], %[vvb] offset:54784\n" \
"ds_read_b64_tr_b16 a[156:157], v211 offset:50688\n" \
"v_mfma_f32_16x16x32_bf16 v[144:147], a[72:75], a[40:43], v[144:147]\n" \
"v_exp_f32_e64 v141, v141\n" \
"v_mfma_f32_16x16x32_bf16 v[144:147], a[76:79], a[44:47], v[144:147]\n" \
"v_cvt_pk_bf16_f32 v188, v136, v137\n" \
"v_cvt_pk_bf16_f32 v189, v138, v139\n" \
"v_mfma_f32_16x16x32_bf16 v[148:151], a[92:95], a[44:47], v[168:171]\n" \
"v_exp_f32_e64 v142, v142\n" \
"v_mfma_f32_16x16x32_bf16 v[148:151], a[88:91], a[40:43], v[148:151]\n" \
"ds_read_b64_tr_b16 a[158:159], v211 offset:54784\n" \
"s_waitcnt lgkmcnt(8)\n" \
"v_mfma_f32_16x16x32_bf16 v[148:151], a[84:87], a[36:39], v[148:151]\n" \
"buffer_load_dwordx4 v213, %[srdk], s48 offen offset:2048 lds\n" \
"v_mfma_f32_16x16x32_bf16 v[148:151], a[80:83], a[32:35], v[148:151]\n" \
"v_exp_f32_e32 v143, v143\n" \
"v_mfma_f32_16x16x32_bf16 v[0:3], a[160:163], v[192:195], v[0:3]\n" \
"ds_read_b128 a[96:99], %[kb0] offset:57344\n" \
"v_mfma_f32_16x16x32_bf16 v[0:3], a[128:131], v[184:187], v[0:3]\n" \
"v_exp_f32_e64 v144, v144\n" \
"v_mfma_f32_16x16x32_bf16 v[4:7], a[164:167], v[192:195], v[4:7]\n" \
"v_cvt_pk_bf16_f32 v190, v140, v141\n" \
"v_cvt_pk_bf16_f32 v191, v142, v143\n" \
"v_mfma_f32_16x16x32_bf16 v[4:7], a[132:135], v[184:187], v[4:7]\n" \
"v_exp_f32_e64 v145, v145\n" \
"v_mfma_f32_16x16x32_bf16 v[8:11], a[168:171], v[192:195], v[8:11]\n" \
"ds_read_b128 a[100:103], v208 offset:57344\n" \
"v_mfma_f32_16x16x32_bf16 v[8:11], a[136:139], v[184:187], v[8:11]\n" \
"v_exp_f32_e64 v146, v146\n" \
"v_mfma_f32_16x16x32_bf16 v[12:15], a[172:175], v[192:195], v[12:15]\n" \
"ds_read_b128 a[104:107], v209 offset:57344\n" \
"v_mfma_f32_16x16x32_bf16 v[12:15], a[140:143], v[184:187], v[12:15]\n" \
"v_exp_f32_e64 v147, v147\n" \
"v_smfmac_f32_16x16x64_bf16 v[180:183], a[192:195], v[200:207], v228\n" \
"ds_read_b128 a[108:111], v210 offset:57344\n" \
"v_mfma_f32_16x16x32_bf16 v[152:155], a[64:67], a[48:51], v[172:175]\n" \
"v_exp_f32_e64 v148, v148\n" \
"v_mfma_f32_16x16x32_bf16 v[152:155], a[68:71], a[52:55], v[152:155]\n" \
"ds_read_b128 a[124:127], v210 offset:61440\n" \
"v_mfma_f32_16x16x32_bf16 v[152:155], a[72:75], a[56:59], v[152:155]\n" \
"v_exp_f32_e64 v149, v149\n" \
"v_mfma_f32_16x16x32_bf16 v[152:155], a[76:79], a[60:63], v[152:155]\n" \
"v_cvt_pk_bf16_f32 v216, v144, v145\n" \
"v_cvt_pk_bf16_f32 v217, v146, v147\n" \
"v_mfma_f32_16x16x32_bf16 v[156:159], a[92:95], a[60:63], v[172:175]\n" \
"v_exp_f32_e64 v150, v150\n" \
"v_mfma_f32_16x16x32_bf16 v[156:159], a[88:91], a[56:59], v[156:159]\n" \
"ds_read_b128 a[120:123], v209 offset:61440\n" \
"s_waitcnt lgkmcnt(6)\n" \
"v_mfma_f32_16x16x32_bf16 v[156:159], a[84:87], a[52:55], v[156:159]\n" \
"buffer_load_dwordx4 v214, %[srdk], s48 offen offset:3072 lds\n" \
"v_mfma_f32_16x16x32_bf16 v[156:159], a[80:83], a[48:51], v[156:159]\n" \
"v_exp_f32_e32 v151, v151\n" \
"v_mfma_f32_16x16x32_bf16 v[16:19], a[176:179], v[192:195], v[16:19]\n" \
"ds_read_b128 a[116:119], v208 offset:61440\n" \
"v_mfma_f32_16x16x32_bf16 v[16:19], a[144:147], v[184:187], v[16:19]\n" \
"v_exp_f32_e64 v152, v152\n" \
"v_mfma_f32_16x16x32_bf16 v[20:23], a[180:183], v[192:195], v[20:23]\n" \
"v_cvt_pk_bf16_f32 v218, v148, v149\n" \
"v_cvt_pk_bf16_f32 v219, v150, v151\n" \
"v_mfma_f32_16x16x32_bf16 v[20:23], a[148:151], v[184:187], v[20:23]\n" \
"v_exp_f32_e64 v153, v153\n" \
"v_mfma_f32_16x16x32_bf16 v[24:27], a[184:187], v[192:195], v[24:27]\n" \
"v_exp_f32_e64 v154, v154\n" \
"v_mfma_f32_16x16x32_bf16 v[24:27], a[152:155], v[184:187], v[24:27]\n" \
"ds_read_b128 a[112:115], %[kb0] offset:61440\n" \
"s_add_u32 s48, s48, 0x4000\n" \
"s_mov_b32 m0, s44\n" \
"s_addk_i32 s44, 0x400\n" \
"buffer_load_dwordx4 v229, s[64:67], s45 offen offset:0 lds\n" \
"s_add_u32 s45, s45, 229376\n" \
"v_mfma_f32_16x16x32_bf16 v[28:31], a[188:191], v[192:195], v[28:31]\n" \
"v_exp_f32_e64 v155, v155\n" \
"v_mfma_f32_16x16x32_bf16 v[28:31], a[156:159], v[184:187], v[28:31]\n" \
"s_waitcnt vmcnt(16) lgkmcnt(2)\n" \
"s_barrier\n" \
"v_mfma_f32_16x16x32_bf16 v[128:131], a[96:99], a[0:3], v[160:163]\n" \
"v_exp_f32_e64 v156, v156\n" \
"v_mfma_f32_16x16x32_bf16 v[128:131], a[100:103], a[4:7], v[128:131]\n" \
"s_add_u32 m0, %[m0b], 98304\n" \
"v_mfma_f32_16x16x32_bf16 v[128:131], a[104:107], a[8:11], v[128:131]\n" \
"v_exp_f32_e64 v157, v157\n" \
"v_mfma_f32_16x16x32_bf16 v[128:131], a[108:111], a[12:15], v[128:131]\n" \
"v_cvt_pk_bf16_f32 v220, v152, v153\n" \
"v_cvt_pk_bf16_f32 v221, v154, v155\n" \
"v_mfma_f32_16x16x32_bf16 v[132:135], a[124:127], a[12:15], v[160:163]\n" \
"v_exp_f32_e64 v158, v158\n" \
"v_mfma_f32_16x16x32_bf16 v[132:135], a[120:123], a[8:11], v[132:135]\n" \
"s_waitcnt lgkmcnt(0)\n" \
"v_mfma_f32_16x16x32_bf16 v[132:135], a[116:119], a[4:7], v[132:135]\n" \
"buffer_load_dwordx4 %[vd0], %[srdv], s48 offen offset:0 lds\n" \
"v_mfma_f32_16x16x32_bf16 v[132:135], a[112:115], a[0:3], v[132:135]\n" \
"v_exp_f32_e32 v159, v159\n" \
"v_mfma_f32_16x16x32_bf16 v[32:35], a[160:163], v[196:199], v[32:35]\n" \
"ds_read_b128 a[64:67], %[kb0] offset:0\n" \
"v_mfma_f32_16x16x32_bf16 v[32:35], a[128:131], v[188:191], v[32:35]\n" \
"v_exp_f32_e64 v128, v128\n" \
"v_mfma_f32_16x16x32_bf16 v[36:39], a[164:167], v[196:199], v[36:39]\n" \
"v_cvt_pk_bf16_f32 v222, v156, v157\n" \
"v_cvt_pk_bf16_f32 v223, v158, v159\n" \
"v_mfma_f32_16x16x32_bf16 v[36:39], a[132:135], v[188:191], v[36:39]\n" \
"v_exp_f32_e64 v129, v129\n" \
"v_mfma_f32_16x16x32_bf16 v[40:43], a[168:171], v[196:199], v[40:43]\n" \
"ds_read_b128 a[68:71], v208 offset:0\n" \
"v_mfma_f32_16x16x32_bf16 v[40:43], a[136:139], v[188:191], v[40:43]\n" \
"v_exp_f32_e64 v130, v130\n" \
"v_mfma_f32_16x16x32_bf16 v[44:47], a[172:175], v[196:199], v[44:47]\n" \
"ds_read_b128 a[72:75], v209 offset:0\n" \
"v_mfma_f32_16x16x32_bf16 v[44:47], a[140:143], v[188:191], v[44:47]\n" \
"v_exp_f32_e64 v131, v131\n" \
"v_smfmac_f32_16x16x64_bf16 v[176:179], a[192:195], v[184:191], v228\n" \
"buffer_store_dwordx4 a[196:199], v235, %[srdz], s63 offen offset:2048 sc1\n" \
"s_add_u32 s63, s63, 2560\n" \
"v_mfma_f32_16x16x32_bf16 v[136:139], a[96:99], a[16:19], v[164:167]\n" \
"v_exp_f32_e64 v132, v132\n" \
"v_mfma_f32_16x16x32_bf16 v[136:139], a[100:103], a[20:23], v[136:139]\n" \
"ds_read_b128 a[76:79], v210 offset:0\n" \
"v_mfma_f32_16x16x32_bf16 v[136:139], a[104:107], a[24:27], v[136:139]\n" \
"v_exp_f32_e64 v133, v133\n" \
"v_mfma_f32_16x16x32_bf16 v[136:139], a[108:111], a[28:31], v[136:139]\n" \
"v_cvt_pk_bf16_f32 v192, v128, v129\n" \
"v_cvt_pk_bf16_f32 v193, v130, v131\n" \
"v_mfma_f32_16x16x32_bf16 v[140:143], a[124:127], a[28:31], v[164:167]\n" \
"v_exp_f32_e64 v134, v134\n" \
"v_mfma_f32_16x16x32_bf16 v[140:143], a[120:123], a[24:27], v[140:143]\n" \
"ds_read_b128 a[92:95], v210 offset:4096\n" \
"v_mfma_f32_16x16x32_bf16 v[140:143], a[116:119], a[20:23], v[140:143]\n" \
"buffer_load_dwordx4 v215, %[srdv], s48 offen offset:1024 lds\n" \
"v_mfma_f32_16x16x32_bf16 v[140:143], a[112:115], a[16:19], v[140:143]\n" \
"v_exp_f32_e64 v135, v135\n" \
"v_mfma_f32_16x16x32_bf16 v[48:51], a[176:179], v[196:199], v[48:51]\n" \
"ds_read_b128 a[88:91], v209 offset:4096\n" \
"v_mfma_f32_16x16x32_bf16 v[48:51], a[144:147], v[188:191], v[48:51]\n" \
"v_exp_f32_e64 v136, v136\n" \
"v_mfma_f32_16x16x32_bf16 v[52:55], a[180:183], v[196:199], v[52:55]\n" \
"v_cvt_pk_bf16_f32 v194, v132, v133\n" \
"v_cvt_pk_bf16_f32 v195, v134, v135\n" \
"v_mfma_f32_16x16x32_bf16 v[52:55], a[148:151], v[188:191], v[52:55]\n" \
"v_exp_f32_e64 v137, v137\n" \
"v_mfma_f32_16x16x32_bf16 v[56:59], a[184:187], v[196:199], v[56:59]\n" \
"v_exp_f32_e64 v138, v138\n" \
"v_mfma_f32_16x16x32_bf16 v[56:59], a[152:155], v[188:191], v[56:59]\n" \
"ds_read_b128 a[84:87], v208 offset:4096\n" \
"v_mfma_f32_16x16x32_bf16 v[60:63], a[188:191], v[196:199], v[60:63]\n" \
"v_exp_f32_e64 v139, v139\n" \
"v_mfma_f32_16x16x32_bf16 v[60:63], a[156:159], v[188:191], v[60:63]\n" \
"s_cmp_lg_u32 %[wid], 2\n" \
"s_cbranch_scc1 LtB7_%=\n" \
"global_load_dword v237, v236, s[58:59]\n" \
"s_add_u32 s61, s48, 49152\n" \
"s_mov_b32 exec_hi, 0\n" \
"s_nop 0\n" \
"buffer_load_dword v237, v224, %[srdp], s61 offen\n" \
"s_mov_b32 exec_hi, -1\n" \
"s_nop 0\n" \
"LtB7_%=:\n" \
"v_mfma_f32_16x16x32_bf16 v[144:147], a[96:99], a[32:35], v[168:171]\n" \
"v_exp_f32_e64 v140, v140\n" \
"v_mfma_f32_16x16x32_bf16 v[144:147], a[100:103], a[36:39], v[144:147]\n" \
"ds_read_b128 a[80:83], %[kb0] offset:4096\n" \
"v_mfma_f32_16x16x32_bf16 v[144:147], a[104:107], a[40:43], v[144:147]\n" \
"v_exp_f32_e64 v141, v141\n" \
"v_mfma_f32_16x16x32_bf16 v[144:147], a[108:111], a[44:47], v[144:147]\n" \
"v_cvt_pk_bf16_f32 v196, v136, v137\n" \
"v_cvt_pk_bf16_f32 v197, v138, v139\n" \
"v_mfma_f32_16x16x32_bf16 v[148:151], a[124:127], a[44:47], v[168:171]\n" \
"v_exp_f32_e64 v142, v142\n" \
"v_mfma_f32_16x16x32_bf16 v[148:151], a[120:123], a[40:43], v[148:151]\n" \
"v_mfma_f32_16x16x32_bf16 v[148:151], a[116:119], a[36:39], v[148:151]\n" \
"buffer_load_dwordx4 %[vd0], %[srdv], s48 offen offset:2048 lds\n" \
"v_mfma_f32_16x16x32_bf16 v[148:151], a[112:115], a[32:35], v[148:151]\n" \
"v_exp_f32_e64 v143, v143\n" \
"v_mfma_f32_16x16x32_bf16 v[96:99], a[160:163], v[204:207], v[96:99]\n" \
"ds_read_b64_tr_b16 a[160:161], %[vvb] offset:57344\n" \
"v_mfma_f32_16x16x32_bf16 v[96:99], a[128:131], v[220:223], v[96:99]\n" \
"v_exp_f32_e64 v144, v144\n" \
"v_mfma_f32_16x16x32_bf16 v[100:103], a[164:167], v[204:207], v[100:103]\n" \
"v_cvt_pk_bf16_f32 v198, v140, v141\n" \
"v_cvt_pk_bf16_f32 v199, v142, v143\n" \
"v_mfma_f32_16x16x32_bf16 v[100:103], a[132:135], v[220:223], v[100:103]\n" \
"v_exp_f32_e64 v145, v145\n" \
"v_mfma_f32_16x16x32_bf16 v[104:107], a[168:171], v[204:207], v[104:107]\n" \
"ds_read_b64_tr_b16 a[162:163], %[vvb] offset:61440\n" \
"ds_read_b64_tr_b16 a[164:165], v211 offset:57344\n" \
"v_mfma_f32_16x16x32_bf16 v[104:107], a[136:139], v[220:223], v[104:107]\n" \
".if . - adaln_body != 28672\n" \
".error \"size model\"\n" \
".endif\n" \
"Lk6_%=:\n" \
"s_cbranch_execz Lk7_%=\n" \
"s_nop 0\n" \
"v_exp_f32_e64 v146, v146\n" \
"v_mfma_f32_16x16x32_bf16 v[108:111], a[172:175], v[204:207], v[108:111]\n" \
"ds_read_b64_tr_b16 a[166:167], v211 offset:61440\n" \
"v_mfma_f32_16x16x32_bf16 v[108:111], a[140:143], v[220:223], v[108:111]\n" \
"v_exp_f32_e64 v147, v147\n" \
"v_smfmac_f32_16x16x64_bf16 v[180:183], a[192:195], v[216:223], v228\n" \
"ds_read_b64_tr_b16 a[168:169], %[vvb] offset:57856\n" \
"v_mfma_f32_16x16x32_bf16 v[152:155], a[96:99], a[48:51], v[172:175]\n" \
"v_exp_f32_e64 v148, v148\n" \
"v_mfma_f32_16x16x32_bf16 v[152:155], a[100:103], a[52:55], v[152:155]\n" \
"ds_read_b64_tr_b16 a[170:171], %[vvb] offset:61952\n" \
"v_mfma_f32_16x16x32_bf16 v[152:155], a[104:107], a[56:59], v[152:155]\n" \
"v_exp_f32_e64 v149, v149\n" \
"v_mfma_f32_16x16x32_bf16 v[152:155], a[108:111], a[60:63], v[152:155]\n" \
"v_cvt_pk_bf16_f32 v200, v144, v145\n" \
"v_cvt_pk_bf16_f32 v201, v146, v147\n" \
"v_mfma_f32_16x16x32_bf16 v[156:159], a[124:127], a[60:63], v[172:175]\n" \
"v_exp_f32_e64 v150, v150\n" \
"v_mfma_f32_16x16x32_bf16 v[156:159], a[120:123], a[56:59], v[156:159]\n" \
"ds_read_b64_tr_b16 a[172:173], v211 offset:57856\n" \
"v_mfma_f32_16x16x32_bf16 v[156:159], a[116:119], a[52:55], v[156:159]\n" \
"buffer_load_dwordx4 v215, %[srdv], s48 offen offset:3072 lds\n" \
"v_mfma_f32_16x16x32_bf16 v[156:159], a[112:115], a[48:51], v[156:159]\n" \
"v_exp_f32_e64 v151, v151\n" \
"v_mfma_f32_16x16x32_bf16 v[112:115], a[176:179], v[204:207], v[112:115]\n" \
"ds_read_b64_tr_b16 a[174:175], v211 offset:61952\n" \
"ds_read_b64_tr_b16 a[176:177], %[vvb] offset:58368\n" \
"v_mfma_f32_16x16x32_bf16 v[112:115], a[144:147], v[220:223], v[112:115]\n" \
"v_exp_f32_e64 v152, v152\n" \
"v_mfma_f32_16x16x32_bf16 v[116:119], a[180:183], v[204:207], v[116:119]\n" \
"v_cvt_pk_bf16_f32 v202, v148, v149\n" \
"v_cvt_pk_bf16_f32 v203, v150, v151\n" \
"v_mfma_f32_16x16x32_bf16 v[116:119], a[148:151], v[220:223], v[116:119]\n" \
"v_exp_f32_e64 v153, v153\n" \
"v_mfma_f32_16x16x32_bf16 v[120:123], a[184:187], v[204:207], v[120:123]\n" \
"v_exp_f32_e64 v154, v154\n" \
"v_mfma_f32_16x16x32_bf16 v[120:123], a[152:155], v[220:223], v[120:123]\n" \
"ds_read_b64_tr_b16 a[178:179], %[vvb] offset:62464\n" \
"v_mfma_f32_16x16x32_bf16 v[124:127], a[188:191], v[204:207], v[124:127]\n" \
"v_exp_f32_e64 v155, v155\n" \
"v_mfma_f32_16x16x32_bf16 v[124:127], a[156:159], v[220:223], v[124:127]\n" \
"ds_read_b64_tr_b16 a[180:181], v211 offset:58368\n" \
"s_waitcnt lgkmcnt(13)\n" \
"v_mfma_f32_16x16x32_bf16 v[128:131], a[64:67], a[0:3], v[160:163]\n" \
"v_exp_f32_e32 v156, v156\n" \
"v_mfma_f32_16x16x32_bf16 v[128:131], a[68:71], a[4:7], v[128:131]\n" \
"ds_read_b64_tr_b16 a[182:183], v211 offset:62464\n" \
"s_add_u32 m0, %[m0b], 49152\n" \
"v_mfma_f32_16x16x32_bf16 v[128:131], a[72:75], a[8:11], v[128:131]\n" \
"v_exp_f32_e64 v157, v157\n" \
"v_mfma_f32_16x16x32_bf16 v[128:131], a[76:79], a[12:15], v[128:131]\n" \
"v_cvt_pk_bf16_f32 v204, v152, v153\n" \
"v_cvt_pk_bf16_f32 v205, v154, v155\n" \
"v_mfma_f32_16x16x32_bf16 v[132:135], a[92:95], a[12:15], v[160:163]\n" \
"v_exp_f32_e64 v158, v158\n" \
"v_mfma_f32_16x16x32_bf16 v[132:135], a[88:91], a[8:11], v[132:135]\n" \
"ds_read_b64_tr_b16 a[184:185], %[vvb] offset:58880\n" \
"s_waitcnt lgkmcnt(5)\n" \
"v_mfma_f32_16x16x32_bf16 v[132:135], a[84:87], a[4:7], v[132:135]\n" \
"buffer_load_dwordx4 %[kd0], %[srdk], s48 offen offset:0 lds\n" \
"v_mfma_f32_16x16x32_bf16 v[132:135], a[80:83], a[0:3], v[132:135]\n" \
"v_exp_f32_e32 v159, v159\n" \
"v_mfma_f32_16x16x32_bf16 v[64:67], a[128:131], v[216:219], v[64:67]\n" \
"ds_read_b64_tr_b16 a[186:187], %[vvb] offset:62976\n" \
"ds_read_b64_tr_b16 a[188:189], v211 offset:58880\n" \
"v_mfma_f32_16x16x32_bf16 v[64:67], a[160:163], v[200:203], v[64:67]\n" \
"v_exp_f32_e64 v128, v128\n" \
"v_mfma_f32_16x16x32_bf16 v[68:71], a[132:135], v[216:219], v[68:71]\n" \
"v_cvt_pk_bf16_f32 v206, v156, v157\n" \
"v_cvt_pk_bf16_f32 v207, v158, v159\n" \
"v_mfma_f32_16x16x32_bf16 v[68:71], a[164:167], v[200:203], v[68:71]\n" \
"v_exp_f32_e64 v129, v129\n" \
"v_mfma_f32_16x16x32_bf16 v[72:75], a[136:139], v[216:219], v[72:75]\n" \
"ds_read_b64_tr_b16 a[190:191], v211 offset:62976\n" \
"ds_read_b64_tr_b16 a[128:129], %[vvb] offset:0\n" \
"v_mfma_f32_16x16x32_bf16 v[72:75], a[168:171], v[200:203], v[72:75]\n" \
"v_exp_f32_e64 v130, v130\n" \
"v_mfma_f32_16x16x32_bf16 v[76:79], a[140:143], v[216:219], v[76:79]\n" \
"ds_read_b64_tr_b16 a[130:131], %[vvb] offset:4096\n" \
"ds_read_b64_tr_b16 a[132:133], v211 offset:0\n" \
"v_mfma_f32_16x16x32_bf16 v[76:79], a[172:175], v[200:203], v[76:79]\n" \
"v_exp_f32_e64 v131, v131\n" \
"v_smfmac_f32_16x16x64_bf16 v[176:179], a[192:195], v[192:199], v228\n" \
"ds_read_b64_tr_b16 a[134:135], v211 offset:4096\n" \
"ds_read_b64_tr_b16 a[136:137], %[vvb] offset:512\n" \
"v_mfma_f32_16x16x32_bf16 v[136:139], a[64:67], a[16:19], v[164:167]\n" \
"v_exp_f32_e64 v132, v132\n" \
"v_mfma_f32_16x16x32_bf16 v[136:139], a[68:71], a[20:23], v[136:139]\n" \
"ds_read_b64_tr_b16 a[138:139], %[vvb] offset:4608\n" \
"ds_read_b64_tr_b16 a[140:141], v211 offset:512\n" \
"v_mfma_f32_16x16x32_bf16 v[136:139], a[72:75], a[24:27], v[136:139]\n" \
"v_exp_f32_e64 v133, v133\n" \
"v_mfma_f32_16x16x32_bf16 v[136:139], a[76:79], a[28:31], v[136:139]\n" \
"v_cvt_pk_bf16_f32 v184, v128, v129\n" \
"v_cvt_pk_bf16_f32 v185, v130, v131\n" \
"v_mfma_f32_16x16x32_bf16 v[140:143], a[92:95], a[28:31], v[164:167]\n" \
"v_exp_f32_e64 v134, v134\n" \
"v_mfma_f32_16x16x32_bf16 v[140:143], a[88:91], a[24:27], v[140:143]\n" \
"ds_read_b64_tr_b16 a[142:143], v211 offset:4608\n" \
"s_waitcnt lgkmcnt(8)\n" \
"v_mfma_f32_16x16x32_bf16 v[140:143], a[84:87], a[20:23], v[140:143]\n" \
"buffer_load_dwordx4 v212, %[srdk], s48 offen offset:1024 lds\n" \
"v_mfma_f32_16x16x32_bf16 v[140:143], a[80:83], a[16:19], v[140:143]\n" \
"v_exp_f32_e32 v135, v135\n" \
"v_mfma_f32_16x16x32_bf16 v[80:83], a[144:147], v[216:219], v[80:83]\n" \
"ds_read_b64_tr_b16 a[144:145], %[vvb] offset:1024\n" \
"ds_read_b64_tr_b16 a[146:147], %[vvb] offset:5120\n" \
"v_mfma_f32_16x16x32_bf16 v[80:83], a[176:179], v[200:203], v[80:83]\n" \
"v_exp_f32_e64 v136, v136\n" \
"v_mfma_f32_16x16x32_bf16 v[84:87], a[148:151], v[216:219], v[84:87]\n" \
"v_cvt_pk_bf16_f32 v186, v132, v133\n" \
"v_cvt_pk_bf16_f32 v187, v134, v135\n" \
"v_mfma_f32_16x16x32_bf16 v[84:87], a[180:183], v[200:203], v[84:87]\n" \
"v_exp_f32_e64 v137, v137\n" \
"v_mfma_f32_16x16x32_bf16 v[88:91], a[152:155], v[216:219], v[88:91]\n" \
"v_exp_f32_e64 v138, v138\n" \
"v_mfma_f32_16x16x32_bf16 v[88:91], a[184:187], v[200:203], v[88:91]\n" \
"ds_read_b64_tr_b16 a[148:149], v211 offset:1024\n" \
"ds_read_b64_tr_b16 a[150:151], v211 offset:5120\n" \
"v_mfma_f32_16x16x32_bf16 v[92:95], a[156:159], v[216:219], v[92:95]\n" \
"v_exp_f32_e64 v139, v139\n" \
"v_mfma_f32_16x16x32_bf16 v[92:95], a[188:191], v[200:203], v[92:95]\n" \
"ds_read_b64_tr_b16 a[152:153], %[vvb] offset:1536\n" \
"buffer_store_dwordx4 a[196:199], v235, %[srdz], s62 offen offset:2048 sc1\n" \
"s_add_u32 s62, s62, 2560\n" \
"v_mfma_f32_16x16x32_bf16 v[144:147], a[64:67], a[32:35], v[168:171]\n" \
"v_exp_f32_e64 v140, v140\n" \
"v_mfma_f32_16x16x32_bf16 v[144:147], a[68:71], a[36:39], v[144:147]\n" \
"ds_read_b64_tr_b16 a[154:155], %[vvb] offset:5632\n" \
"ds_read_b64_tr_b16 a[156:157], v211 offset:1536\n" \
"v_mfma_f32_16x16x32_bf16 v[144:147], a[72:75], a[40:43], v[144:147]\n" \
"v_exp_f32_e64 v141, v141\n" \
"v_mfma_f32_16x16x32_bf16 v[144:147], a[76:79], a[44:47], v[144:147]\n" \
"v_cvt_pk_bf16_f32 v188, v136, v137\n" \
"v_cvt_pk_bf16_f32 v189, v138, v139\n" \
"v_mfma_f32_16x16x32_bf16 v[148:151], a[92:95], a[44:47], v[168:171]\n" \
"v_exp_f32_e64 v142, v142\n" \
"v_mfma_f32_16x16x32_bf16 v[148:151], a[88:91], a[40:43], v[148:151]\n" \
"ds_read_b64_tr_b16 a[158:159], v211 offset:5632\n" \
"s_waitcnt lgkmcnt(8)\n" \
"v_mfma_f32_16x16x32_bf16 v[148:151], a[84:87], a[36:39], v[148:151]\n" \
"buffer_load_dwordx4 v213, %[srdk], s48 offen offset:2048 lds\n" \
"v_mfma_f32_16x16x32_bf16 v[148:151], a[80:83], a[32:35], v[148:151]\n" \
"v_exp_f32_e32 v143, v143\n" \
"v_mfma_f32_16x16x32_bf16 v[0:3], a[160:163], v[192:195], v[0:3]\n" \
"ds_read_b128 a[96:99], %[kb0] offset:8192\n" \
"v_mfma_f32_16x16x32_bf16 v[0:3], a[128:131], v[184:187], v[0:3]\n" \
"v_exp_f32_e64 v144, v144\n" \
"v_mfma_f32_16x16x32_bf16 v[4:7], a[164:167], v[192:195], v[4:7]\n" \
"v_cvt_pk_bf16_f32 v190, v140, v141\n" \
"v_cvt_pk_bf16_f32 v191, v142, v143\n" \
"v_mfma_f32_16x16x32_bf16 v[4:7], a[132:135], v[184:187], v[4:7]\n" \
"v_exp_f32_e64 v145, v145\n" \
"v_mfma_f32_16x16x32_bf16 v[8:11], a[168:171], v[192:195], v[8:11]\n" \
"ds_read_b128 a[100:103], v208 offset:8192\n" \
"v_mfma_f32_16x16x32_bf16 v[8:11], a[136:139], v[184:187], v[8:11]\n" \
"v_exp_f32_e64 v146, v146\n" \
"v_mfma_f32_16x16x32_bf16 v[12:15], a[172:175], v[192:195], v[12:15]\n" \
"ds_read_b128 a[104:107], v209 offset:8192\n" \
"v_mfma_f32_16x16x32_bf16 v[12:15], a[140:143], v[184:187], v[12:15]\n" \
"v_exp_f32_e64 v147, v147\n" \
"v_smfmac_f32_16x16x64_bf16 v[180:183], a[192:195], v[200:207], v228\n" \
"ds_read_b128 a[108:111], v210 offset:8192\n" \
"v_mfma_f32_16x16x32_bf16 v[152:155], a[64:67], a[48:51], v[172:175]\n" \
"v_exp_f32_e64 v148, v148\n" \
"v_mfma_f32_16x16x32_bf16 v[152:155], a[68:71], a[52:55], v[152:155]\n" \
"ds_read_b128 a[124:127], v210 offset:12288\n" \
"v_mfma_f32_16x16x32_bf16 v[152:155], a[72:75], a[56:59], v[152:155]\n" \
"v_exp_f32_e64 v149, v149\n" \
"v_mfma_f32_16x16x32_bf16 v[152:155], a[76:79], a[60:63], v[152:155]\n" \
"v_cvt_pk_bf16_f32 v216, v144, v145\n" \
"v_cvt_pk_bf16_f32 v217, v146, v147\n" \
"v_mfma_f32_16x16x32_bf16 v[156:159], a[92:95], a[60:63], v[172:175]\n" \
"v_exp_f32_e64 v150, v150\n" \
"v_mfma_f32_16x16x32_bf16 v[156:159], a[88:91], a[56:59], v[156:159]\n" \
"ds_read_b128 a[120:123], v209 offset:12288\n" \
"s_waitcnt lgkmcnt(6)\n" \
"v_mfma_f32_16x16x32_bf16 v[156:159], a[84:87], a[52:55], v[156:159]\n" \
"buffer_load_dwordx4 v214, %[srdk], s48 offen offset:3072 lds\n" \
"v_mfma_f32_16x16x32_bf16 v[156:159], a[80:83], a[48:51], v[156:159]\n" \
"v_exp_f32_e32 v151, v151\n" \
"v_mfma_f32_16x16x32_bf16 v[16:19], a[176:179], v[192:195], v[16:19]\n" \
"ds_read_b128 a[116:119], v208 offset:12288\n" \
"v_mfma_f32_16x16x32_bf16 v[16:19], a[144:147], v[184:187], v[16:19]\n" \
"v_exp_f32_e64 v152, v152\n" \
"v_mfma_f32_16x16x32_bf16 v[20:23], a[180:183], v[192:195], v[20:23]\n" \
"v_cvt_pk_bf16_f32 v218, v148, v149\n" \
"v_cvt_pk_bf16_f32 v219, v150, v151\n" \
"v_mfma_f32_16x16x32_bf16 v[20:23], a[148:151], v[184:187], v[20:23]\n" \
"v_exp_f32_e64 v153, v153\n" \
"v_mfma_f32_16x16x32_bf16 v[24:27], a[184:187], v[192:195], v[24:27]\n" \
"v_exp_f32_e64 v154, v154\n" \
"v_mfma_f32_16x16x32_bf16 v[24:27], a[152:155], v[184:187], v[24:27]\n" \
"ds_read_b128 a[112:115], %[kb0] offset:12288\n" \
"s_add_u32 s48, s48, 0x4000\n" \
"v_mfma_f32_16x16x32_bf16 v[28:31], a[188:191], v[192:195], v[28:31]\n" \
"v_exp_f32_e64 v155, v155\n" \
"v_mfma_f32_16x16x32_bf16 v[28:31], a[156:159], v[184:187], v[28:31]\n" \
"s_waitcnt vmcnt(17) lgkmcnt(2)\n" \
"s_barrier\n" \
"v_mfma_f32_16x16x32_bf16 v[128:131], a[96:99], a[0:3], v[160:163]\n" \
"v_exp_f32_e64 v156, v156\n" \
"v_mfma_f32_16x16x32_bf16 v[128:131], a[100:103], a[4:7], v[128:131]\n" \
"s_add_u32 m0, %[m0b], 114688\n" \
"v_mfma_f32_16x16x32_bf16 v[128:131], a[104:107], a[8:11], v[128:131]\n" \
"v_exp_f32_e64 v157, v157\n" \
"v_mfma_f32_16x16x32_bf16 v[128:131], a[108:111], a[12:15], v[128:131]\n" \
"v_cvt_pk_bf16_f32 v220, v152, v153\n" \
"v_cvt_pk_bf16_f32 v221, v154, v155\n" \
"v_mfma_f32_16x16x32_bf16 v[132:135], a[124:127], a[12:15], v[160:163]\n" \
"v_exp_f32_e64 v158, v158\n" \
"v_mfma_f32_16x16x32_bf16 v[132:135], a[120:123], a[8:11], v[132:135]\n" \
"s_waitcnt lgkmcnt(0)\n" \
"v_mfma_f32_16x16x32_bf16 v[132:135], a[116:119], a[4:7], v[132:135]\n" \
"buffer_load_dwordx4 %[vd0], %[srdv], s48 offen offset:0 lds\n" \
"v_mfma_f32_16x16x32_bf16 v[132:135], a[112:115], a[0:3], v[132:135]\n" \
"v_exp_f32_e32 v159, v159\n" \
"v_mfma_f32_16x16x32_bf16 v[32:35], a[160:163], v[196:199], v[32:35]\n" \
"ds_read_b128 a[64:67], %[kb0] offset:16384\n" \
"v_mfma_f32_16x16x32_bf16 v[32:35], a[128:131], v[188:191], v[32:35]\n" \
"v_exp_f32_e64 v128, v128\n" \
"v_mfma_f32_16x16x32_bf16 v[36:39], a[164:167], v[196:199], v[36:39]\n" \
"v_cvt_pk_bf16_f32 v222, v156, v157\n" \
"v_cvt_pk_bf16_f32 v223, v158, v159\n" \
"v_mfma_f32_16x16x32_bf16 v[36:39], a[132:135], v[188:191], v[36:39]\n" \
"v_exp_f32_e64 v129, v129\n" \
"v_mfma_f32_16x16x32_bf16 v[40:43], a[168:171], v[196:199], v[40:43]\n" \
"ds_read_b128 a[68:71], v208 offset:16384\n" \
"v_mfma_f32_16x16x32_bf16 v[40:43], a[136:139], v[188:191], v[40:43]\n" \
"v_exp_f32_e64 v130, v130\n" \
"v_mfma_f32_16x16x32_bf16 v[44:47], a[172:175], v[196:199], v[44:47]\n" \
"ds_read_b128 a[72:75], v209 offset:16384\n" \
"v_mfma_f32_16x16x32_bf16 v[44:47], a[140:143], v[188:191], v[44:47]\n" \
"v_exp_f32_e64 v131, v131\n" \
"v_smfmac_f32_16x16x64_bf16 v[176:179], a[192:195], v[184:191], v228\n" \
"v_mfma_f32_16x16x32_bf16 v[136:139], a[96:99], a[16:19], v[164:167]\n" \
"v_exp_f32_e64 v132, v132\n" \
"v_mfma_f32_16x16x32_bf16 v[136:139], a[100:103], a[20:23], v[136:139]\n" \
"ds_read_b128 a[76:79], v210 offset:16384\n" \
"v_mfma_f32_16x16x32_bf16 v[136:139], a[104:107], a[24:27], v[136:139]\n" \
"v_exp_f32_e64 v133, v133\n" \
"v_mfma_f32_16x16x32_bf16 v[136:139], a[108:111], a[28:31], v[136:139]\n" \
"v_cvt_pk_bf16_f32 v192, v128, v129\n" \
"v_cvt_pk_bf16_f32 v193, v130, v131\n" \
"v_mfma_f32_16x16x32_bf16 v[140:143], a[124:127], a[28:31], v[164:167]\n" \
"v_exp_f32_e64 v134, v134\n" \
"v_mfma_f32_16x16x32_bf16 v[140:143], a[120:123], a[24:27], v[140:143]\n" \
"ds_read_b128 a[92:95], v210 offset:20480\n" \
"v_mfma_f32_16x16x32_bf16 v[140:143], a[116:119], a[20:23], v[140:143]\n" \
"buffer_load_dwordx4 v215, %[srdv], s48 offen offset:1024 lds\n" \
"v_mfma_f32_16x16x32_bf16 v[140:143], a[112:115], a[16:19], v[140:143]\n" \
"v_exp_f32_e64 v135, v135\n" \
"v_mfma_f32_16x16x32_bf16 v[48:51], a[176:179], v[196:199], v[48:51]\n" \
"ds_read_b128 a[88:91], v209 offset:20480\n" \
"v_mfma_f32_16x16x32_bf16 v[48:51], a[144:147], v[188:191], v[48:51]\n" \
"v_exp_f32_e64 v136, v136\n" \
"v_mfma_f32_16x16x32_bf16 v[52:55], a[180:183], v[196:199], v[52:55]\n" \
"v_cvt_pk_bf16_f32 v194, v132, v133\n" \
"v_cvt_pk_bf16_f32 v195, v134, v135\n" \
"v_mfma_f32_16x16x32_bf16 v[52:55], a[148:151], v[188:191], v[52:55]\n" \
"v_exp_f32_e64 v137, v137\n" \
"v_mfma_f32_16x16x32_bf16 v[56:59], a[184:187], v[196:199], v[56:59]\n" \
"v_exp_f32_e64 v138, v138\n" \
"v_mfma_f32_16x16x32_bf16 v[56:59], a[152:155], v[188:191], v[56:59]\n" \
"ds_read_b128 a[84:87], v208 offset:20480\n" \
"v_mfma_f32_16x16x32_bf16 v[60:63], a[188:191], v[196:199], v[60:63]\n" \
"v_exp_f32_e64 v139, v139\n" \
"v_mfma_f32_16x16x32_bf16 v[60:63], a[156:159], v[188:191], v[60:63]\n" \
"s_cmp_lg_u32 %[wid], 3\n" \
"s_cbranch_scc1 LtB1_%=\n" \
"global_load_dword v237, v236, s[58:59]\n" \
"s_add_u32 s61, s48, 49152\n" \
"s_mov_b32 exec_hi, 0\n" \
"s_nop 0\n" \
"buffer_load_dword v237, v224, %[srdp], s61 offen\n" \
"s_mov_b32 exec_hi, -1\n" \
"s_nop 0\n" \
"LtB1_%=:\n" \
"v_mfma_f32_16x16x32_bf16 v[144:147], a[96:99], a[32:35], v[168:171]\n" \
"v_exp_f32_e64 v140, v140\n" \
"v_mfma_f32_16x16x32_bf16 v[144:147], a[100:103], a[36:39], v[144:147]\n" \
"ds_read_b128 a[80:83], %[kb0] offset:20480\n" \
"v_mfma_f32_16x16x32_bf16 v[144:147], a[104:107], a[40:43], v[144:147]\n" \
"v_exp_f32_e64 v141, v141\n" \
"v_mfma_f32_16x16x32_bf16 v[144:147], a[108:111], a[44:47], v[144:147]\n" \
"v_cvt_pk_bf16_f32 v196, v136, v137\n" \
"v_cvt_pk_bf16_f32 v197, v138, v139\n" \
"v_mfma_f32_16x16x32_bf16 v[148:151], a[124:127], a[44:47], v[168:171]\n" \
"v_exp_f32_e64 v142, v142\n" \
"v_mfma_f32_16x16x32_bf16 v[148:151], a[120:123], a[40:43], v[148:151]\n" \
"v_mfma_f32_16x16x32_bf16 v[148:151], a[116:119], a[36:39], v[148:151]\n" \
"buffer_load_dwordx4 %[vd0], %[srdv], s48 offen offset:2048 lds\n" \
"v_mfma_f32_16x16x32_bf16 v[148:151], a[112:115], a[32:35], v[148:151]\n" \
"v_exp_f32_e64 v143, v143\n" \
"v_mfma_f32_16x16x32_bf16 v[96:99], a[160:163], v[204:207], v[96:99]\n" \
"ds_read_b64_tr_b16 a[160:161], %[vvb] offset:8192\n" \
"v_mfma_f32_16x16x32_bf16 v[96:99], a[128:131], v[220:223], v[96:99]\n" \
"v_exp_f32_e64 v144, v144\n" \
"v_mfma_f32_16x16x32_bf16 v[100:103], a[164:167], v[204:207], v[100:103]\n" \
"v_cvt_pk_bf16_f32 v198, v140, v141\n" \
"v_cvt_pk_bf16_f32 v199, v142, v143\n" \
"v_mfma_f32_16x16x32_bf16 v[100:103], a[132:135], v[220:223], v[100:103]\n" \
"v_exp_f32_e64 v145, v145\n" \
"v_mfma_f32_16x16x32_bf16 v[104:107], a[168:171], v[204:207], v[104:107]\n" \
"ds_read_b64_tr_b16 a[162:163], %[vvb] offset:12288\n" \
"ds_read_b64_tr_b16 a[164:165], v211 offset:8192\n" \
"v_mfma_f32_16x16x32_bf16 v[104:107], a[136:139], v[220:223], v[104:107]\n" \
"v_exp_f32_e64 v146, v146\n" \
"v_mfma_f32_16x16x32_bf16 v[108:111], a[172:175], v[204:207], v[108:111]\n" \
"ds_read_b64_tr_b16 a[166:167], v211 offset:12288\n" \
"v_mfma_f32_16x16x32_bf16 v[108:111], a[140:143], v[220:223], v[108:111]\n" \
"v_exp_f32_e64 v147, v147\n" \
"v_smfmac_f32_16x16x64_bf16 v[180:183], a[192:195], v[216:223], v228\n" \
"ds_read_b64_tr_b16 a[168:169], %[vvb] offset:8704\n" \
"v_mfma_f32_16x16x32_bf16 v[152:155], a[96:99], a[48:51], v[172:175]\n" \
"v_exp_f32_e64 v148, v148\n" \
"v_mfma_f32_16x16x32_bf16 v[152:155], a[100:103], a[52:55], v[152:155]\n" \
"ds_read_b64_tr_b16 a[170:171], %[vvb] offset:12800\n" \
"v_mfma_f32_16x16x32_bf16 v[152:155], a[104:107], a[56:59], v[152:155]\n" \
"v_exp_f32_e64 v149, v149\n" \
"v_mfma_f32_16x16x32_bf16 v[152:155], a[108:111], a[60:63], v[152:155]\n" \
"v_cvt_pk_bf16_f32 v200, v144, v145\n" \
"v_cvt_pk_bf16_f32 v201, v146, v147\n" \
"v_mfma_f32_16x16x32_bf16 v[156:159], a[124:127], a[60:63], v[172:175]\n" \
"v_exp_f32_e64 v150, v150\n" \
"v_mfma_f32_16x16x32_bf16 v[156:159], a[120:123], a[56:59], v[156:159]\n" \
"ds_read_b64_tr_b16 a[172:173], v211 offset:8704\n" \
"v_mfma_f32_16x16x32_bf16 v[156:159], a[116:119], a[52:55], v[156:159]\n" \
"buffer_load_dwordx4 v215, %[srdv], s48 offen offset:3072 lds\n" \
"v_mfma_f32_16x16x32_bf16 v[156:159], a[112:115], a[48:51], v[156:159]\n" \
"v_exp_f32_e64 v151, v151\n" \
"v_mfma_f32_16x16x32_bf16 v[112:115], a[176:179], v[204:207], v[112:115]\n" \
"ds_read_b64_tr_b16 a[174:175], v211 offset:12800\n" \
"ds_read_b64_tr_b16 a[176:177], %[vvb] offset:9216\n" \
"v_mfma_f32_16x16x32_bf16 v[112:115], a[144:147], v[220:223], v[112:115]\n" \
"v_exp_f32_e64 v152, v152\n" \
"v_mfma_f32_16x16x32_bf16 v[116:119], a[180:183], v[204:207], v[116:119]\n" \
"v_cvt_pk_bf16_f32 v202, v148, v149\n" \
"v_cvt_pk_bf16_f32 v203, v150, v151\n" \
"v_mfma_f32_16x16x32_bf16 v[116:119], a[148:151], v[220:223], v[116:119]\n" \
"v_exp_f32_e64 v153, v153\n" \
"v_mfma_f32_16x16x32_bf16 v[120:123], a[184:187], v[204:207], v[120:123]\n" \
"v_exp_f32_e64 v154, v154\n" \
"v_mfma_f32_16x16x32_bf16 v[120:123], a[152:155], v[220:223], v[120:123]\n" \
"ds_read_b64_tr_b16 a[178:179], %[vvb] offset:13312\n" \
"v_mfma_f32_16x16x32_bf16 v[124:127], a[188:191], v[204:207], v[124:127]\n" \
"v_exp_f32_e64 v155, v155\n" \
"v_mfma_f32_16x16x32_bf16 v[124:127], a[156:159], v[220:223], v[124:127]\n" \
"ds_read_b64_tr_b16 a[180:181], v211 offset:9216\n" \
"s_waitcnt lgkmcnt(13)\n" \
"s_cmp_lg_u32 s60, 7\n" \
"s_cbranch_scc1 LmB_%=\n" \
"s_nop 0\n" \
"v_readlane_b32 s48, %[tlo], 11\n" \
"v_readlane_b32 s58, %[tlo], 12\n" \
"v_mfma_f32_16x16x32_bf16 v[128:131], a[64:67], a[0:3], v[160:163]\n" \
"v_exp_f32_e64 v156, v156\n" \
"v_mfma_f32_16x16x32_bf16 v[128:131], a[68:71], a[4:7], v[128:131]\n" \
"ds_read_b64_tr_b16 a[182:183], v211 offset:13312\n" \
"s_add_u32 m0, %[m0b], 0\n" \
"s_mov_b32 s49, s48\n" \
"v_mfma_f32_16x16x32_bf16 v[128:131], a[72:75], a[8:11], v[128:131]\n" \
"v_exp_f32_e64 v157, v157\n" \
"v_mfma_f32_16x16x32_bf16 v[128:131], a[76:79], a[12:15], v[128:131]\n" \
"v_cvt_pk_bf16_f32 v204, v152, v153\n" \
"v_cvt_pk_bf16_f32 v205, v154, v155\n" \
"v_mfma_f32_16x16x32_bf16 v[132:135], a[92:95], a[12:15], v[160:163]\n" \
"v_exp_f32_e64 v158, v158\n" \
"v_mfma_f32_16x16x32_bf16 v[132:135], a[88:91], a[8:11], v[132:135]\n" \
"ds_read_b64_tr_b16 a[184:185], %[vvb] offset:9728\n" \
"s_lshl_b32 s49, s49, 18\n" \
"s_or_b32 s49, s49, s58\n" \
"v_accvgpr_write_b32 a228, v225\n" \
"s_waitcnt lgkmcnt(5)\n" \
"v_mfma_f32_16x16x32_bf16 v[132:135], a[84:87], a[4:7], v[132:135]\n" \
"buffer_load_dwordx4 %[wo0], %[srdt], s49 offen offset:0 lds\n" \
"v_mfma_f32_16x16x32_bf16 v[132:135], a[80:83], a[0:3], v[132:135]\n" \
"v_exp_f32_e32 v159, v159\n" \
"v_mfma_f32_16x16x32_bf16 v[64:67], a[128:131], v[216:219], v[64:67]\n" \
"ds_read_b64_tr_b16 a[186:187], %[vvb] offset:13824\n" \
"ds_read_b64_tr_b16 a[188:189], v211 offset:9728\n" \
"v_readlane_b32 s60, %[thi], 11\n" \
"v_mfma_f32_16x16x32_bf16 v[64:67], a[160:163], v[200:203], v[64:67]\n" \
"v_exp_f32_e64 v128, v128\n" \
"v_mfma_f32_16x16x32_bf16 v[68:71], a[132:135], v[216:219], v[68:71]\n" \
"v_cvt_pk_bf16_f32 v206, v156, v157\n" \
"v_cvt_pk_bf16_f32 v207, v158, v159\n" \
"v_mfma_f32_16x16x32_bf16 v[68:71], a[164:167], v[200:203], v[68:71]\n" \
"v_exp_f32_e64 v129, v129\n" \
"v_mfma_f32_16x16x32_bf16 v[72:75], a[136:139], v[216:219], v[72:75]\n" \
"ds_read_b64_tr_b16 a[190:191], v211 offset:13824\n" \
"ds_read_b64_tr_b16 a[128:129], %[vvb] offset:16384\n" \
"v_readlane_b32 s59, %[thi], 12\n" \
"v_mfma_f32_16x16x32_bf16 v[72:75], a[168:171], v[200:203], v[72:75]\n" \
"v_exp_f32_e64 v130, v130\n" \
"v_mfma_f32_16x16x32_bf16 v[76:79], a[140:143], v[216:219], v[76:79]\n" \
"ds_read_b64_tr_b16 a[130:131], %[vvb] offset:20480\n" \
"ds_read_b64_tr_b16 a[132:133], v211 offset:16384\n" \
"v_readlane_b32 s53, %[tlo], 13\n" \
"v_mfma_f32_16x16x32_bf16 v[76:79], a[172:175], v[200:203], v[76:79]\n" \
"v_exp_f32_e64 v131, v131\n" \
"v_smfmac_f32_16x16x64_bf16 v[176:179], a[192:195], v[192:199], v228\n" \
"ds_read_b64_tr_b16 a[134:135], v211 offset:20480\n" \
"ds_read_b64_tr_b16 a[136:137], %[vvb] offset:16896\n" \
"v_readlane_b32 s50, %[thi], 13\n" \
"v_mfma_f32_16x16x32_bf16 v[136:139], a[64:67], a[16:19], v[164:167]\n" \
"v_exp_f32_e64 v132, v132\n" \
"v_mfma_f32_16x16x32_bf16 v[136:139], a[68:71], a[20:23], v[136:139]\n" \
"ds_read_b64_tr_b16 a[138:139], %[vvb] offset:20992\n" \
"ds_read_b64_tr_b16 a[140:141], v211 offset:16896\n" \
"v_readlane_b32 s46, %[tlo], 14\n" \
"v_add_u32_e32 v212, 0x3c00, %[wo0]\n" \
"v_mfma_f32_16x16x32_bf16 v[136:139], a[72:75], a[24:27], v[136:139]\n" \
"v_exp_f32_e64 v133, v133\n" \
"v_mfma_f32_16x16x32_bf16 v[136:139], a[76:79], a[28:31], v[136:139]\n" \
"v_cvt_pk_bf16_f32 v184, v128, v129\n" \
"v_cvt_pk_bf16_f32 v185, v130, v131\n" \
"v_mfma_f32_16x16x32_bf16 v[140:143], a[92:95], a[28:31], v[164:167]\n" \
"v_exp_f32_e64 v134, v134\n" \
"v_mfma_f32_16x16x32_bf16 v[140:143], a[88:91], a[24:27], v[140:143]\n" \
"ds_read_b64_tr_b16 a[142:143], v211 offset:20992\n" \
"v_readlane_b32 s47, %[thi], 14\n" \
"v_add_u32_e32 v213, 0x7800, %[wo0]\n" \
"s_waitcnt lgkmcnt(8)\n" \
"v_mfma_f32_16x16x32_bf16 v[140:143], a[84:87], a[20:23], v[140:143]\n" \
"buffer_load_dwordx4 v212, %[srdt], s49 offen offset:1024 lds\n" \
"v_mfma_f32_16x16x32_bf16 v[140:143], a[80:83], a[16:19], v[140:143]\n" \
"v_exp_f32_e32 v135, v135\n" \
"v_mfma_f32_16x16x32_bf16 v[80:83], a[144:147], v[216:219], v[80:83]\n" \
"ds_read_b64_tr_b16 a[144:145], %[vvb] offset:17408\n" \
"ds_read_b64_tr_b16 a[146:147], %[vvb] offset:21504\n" \
"ds_read_b32 v230, %[rd0]\n" \
"v_mfma_f32_16x16x32_bf16 v[80:83], a[176:179], v[200:203], v[80:83]\n" \
"v_exp_f32_e64 v136, v136\n" \
"v_mfma_f32_16x16x32_bf16 v[84:87], a[148:151], v[216:219], v[84:87]\n" \
"v_cvt_pk_bf16_f32 v186, v132, v133\n" \
"v_cvt_pk_bf16_f32 v187, v134, v135\n" \
"v_mfma_f32_16x16x32_bf16 v[84:87], a[180:183], v[200:203], v[84:87]\n" \
"v_exp_f32_e64 v137, v137\n" \
"v_mfma_f32_16x16x32_bf16 v[88:91], a[152:155], v[216:219], v[88:91]\n" \
"v_exp_f32_e64 v138, v138\n" \
"v_mfma_f32_16x16x32_bf16 v[88:91], a[184:187], v[200:203], v[88:91]\n" \
"ds_read_b64_tr_b16 a[148:149], v211 offset:17408\n" \
"v_readlane_b32 s55, %[thi], 15\n" \
"ds_read_b64_tr_b16 a[150:151], v211 offset:21504\n" \
"v_lshrrev_b32_e64 v234, 5, v224\n" \
"v_mfma_f32_16x16x32_bf16 v[92:95], a[156:159], v[216:219], v[92:95]\n" \
"v_exp_f32_e64 v139, v139\n" \
"v_mfma_f32_16x16x32_bf16 v[92:95], a[188:191], v[200:203], v[92:95]\n" \
"ds_read_b64_tr_b16 a[152:153], %[vvb] offset:17920\n" \
"v_mfma_f32_16x16x32_bf16 v[144:147], a[64:67], a[32:35], v[168:171]\n" \
"v_exp_f32_e64 v140, v140\n" \
"v_mfma_f32_16x16x32_bf16 v[144:147], a[68:71], a[36:39], v[144:147]\n" \
"ds_read_b64_tr_b16 a[154:155], %[vvb] offset:22016\n" \
"ds_read_b64_tr_b16 a[156:157], v211 offset:17920\n" \
"s_waitcnt lgkmcnt(5)\n" \
"v_add_f32_dpp v230, v230, v230 row_ror:4 row_mask:0xf bank_mask:0xf\n" \
"v_mfma_f32_16x16x32_bf16 v[144:147], a[72:75], a[40:43], v[144:147]\n" \
"v_exp_f32_e32 v141, v141\n" \
"v_mfma_f32_16x16x32_bf16 v[144:147], a[76:79], a[44:47], v[144:147]\n" \
"v_cvt_pk_bf16_f32 v188, v136, v137\n" \
"v_cvt_pk_bf16_f32 v189, v138, v139\n" \
"v_mfma_f32_16x16x32_bf16 v[148:151], a[92:95], a[44:47], v[168:171]\n" \
"v_exp_f32_e64 v142, v142\n" \
"v_mfma_f32_16x16x32_bf16 v[148:151], a[88:91], a[40:43], v[148:151]\n" \
"ds_read_b64_tr_b16 a[158:159], v211 offset:22016\n" \
"v_add_f32_dpp v230, v230, v230 row_ror:8 row_mask:0xf bank_mask:0xf\n" \
"s_waitcnt lgkmcnt(9)\n" \
"v_mfma_f32_16x16x32_bf16 v[148:151], a[84:87], a[36:39], v[148:151]\n" \
"buffer_load_dwordx4 v213, %[srdt], s49 offen offset:2048 lds\n" \
"v_mfma_f32_16x16x32_bf16 v[148:151], a[80:83], a[32:35], v[148:151]\n" \
"v_exp_f32_e32 v143, v143\n" \
"v_mfma_f32_16x16x32_bf16 v[0:3], a[160:163], v[192:195], v[0:3]\n" \
"ds_read_b128 a[96:99], %[kb0] offset:24576\n" \
"v_cvt_pk_bf16_f32 v230, v230, v230\n" \
"v_mfma_f32_16x16x32_bf16 v[0:3], a[128:131], v[184:187], v[0:3]\n" \
"v_exp_f32_e64 v144, v144\n" \
"v_mfma_f32_16x16x32_bf16 v[4:7], a[164:167], v[192:195], v[4:7]\n" \
"v_cvt_pk_bf16_f32 v190, v140, v141\n" \
"v_cvt_pk_bf16_f32 v191, v142, v143\n" \
"v_mfma_f32_16x16x32_bf16 v[4:7], a[132:135], v[184:187], v[4:7]\n" \
"v_exp_f32_e64 v145, v145\n" \
"v_mfma_f32_16x16x32_bf16 v[8:11], a[168:171], v[192:195], v[8:11]\n" \
"ds_read_b128 a[100:103], v208 offset:24576\n" \
"v_lshlrev_b32_e64 v230, 16, v230\n" \
"v_mfma_f32_16x16x32_bf16 v[8:11], a[136:139], v[184:187], v[8:11]\n" \
"v_exp_f32_e64 v146, v146\n" \
"v_mfma_f32_16x16x32_bf16 v[12:15], a[172:175], v[192:195], v[12:15]\n" \
"ds_read_b128 a[104:107], v209 offset:24576\n" \
"v_add_f32_e64 v230, s55, v230\n" \
"v_add_u32_e32 v214, 0xb400, %[wo0]\n" \
"v_mfma_f32_16x16x32_bf16 v[12:15], a[140:143], v[184:187], v[12:15]\n" \
"v_exp_f32_e64 v147, v147\n" \
"v_smfmac_f32_16x16x64_bf16 v[180:183], a[192:195], v[200:207], v228\n" \
"ds_read_b128 a[108:111], v210 offset:24576\n" \
"v_cvt_pk_bf16_f32 v230, v230, v230\n" \
"v_readlane_b32 s55, %[tlo], 15\n" \
"v_mfma_f32_16x16x32_bf16 v[152:155], a[64:67], a[48:51], v[172:175]\n" \
"v_exp_f32_e64 v148, v148\n" \
"v_mfma_f32_16x16x32_bf16 v[152:155], a[68:71], a[52:55], v[152:155]\n" \
"ds_read_b128 a[124:127], v210 offset:28672\n" \
"v_lshl_or_b32 v230, v230, 16, 1\n" \
"v_mfma_f32_16x16x32_bf16 v[152:155], a[72:75], a[56:59], v[152:155]\n" \
"v_exp_f32_e64 v149, v149\n" \
"v_mfma_f32_16x16x32_bf16 v[152:155], a[76:79], a[60:63], v[152:155]\n" \
"v_cvt_pk_bf16_f32 v216, v144, v145\n" \
"v_cvt_pk_bf16_f32 v217, v146, v147\n" \
"v_mfma_f32_16x16x32_bf16 v[156:159], a[92:95], a[60:63], v[172:175]\n" \
"v_exp_f32_e64 v150, v150\n" \
"v_mfma_f32_16x16x32_bf16 v[156:159], a[88:91], a[56:59], v[156:159]\n" \
"ds_read_b128 a[120:123], v209 offset:28672\n" \
"s_cmp_lg_u32 s52, 0\n" \
"s_cselect_b32 s55, 0x80000000, s55\n" \
"s_mov_b64 exec, 15\n" \
"buffer_store_dword v230, v234, %[srdz], s55 offen sc1\n" \
"s_mov_b64 exec, -1\n" \
"s_waitcnt lgkmcnt(6)\n" \
"v_mfma_f32_16x16x32_bf16 v[156:159], a[84:87], a[52:55], v[156:159]\n" \
"buffer_load_dwordx4 v214, %[srdt], s49 offen offset:3072 lds\n" \
"v_mfma_f32_16x16x32_bf16 v[156:159], a[80:83], a[48:51], v[156:159]\n" \
"v_exp_f32_e64 v151, v151\n" \
"v_mfma_f32_16x16x32_bf16 v[16:19], a[176:179], v[192:195], v[16:19]\n" \
"ds_read_b128 a[116:119], v208 offset:28672\n" \
"v_mfma_f32_16x16x32_bf16 v[16:19], a[144:147], v[184:187], v[16:19]\n" \
"v_exp_f32_e64 v152, v152\n" \
"v_mfma_f32_16x16x32_bf16 v[20:23], a[180:183], v[192:195], v[20:23]\n" \
"v_cvt_pk_bf16_f32 v218, v148, v149\n" \
"v_cvt_pk_bf16_f32 v219, v150, v151\n" \
"v_mfma_f32_16x16x32_bf16 v[20:23], a[148:151], v[184:187], v[20:23]\n" \
"v_exp_f32_e64 v153, v153\n" \
"v_mfma_f32_16x16x32_bf16 v[24:27], a[184:187], v[192:195], v[24:27]\n" \
"v_exp_f32_e64 v154, v154\n" \
".if . - adaln_body != 32768\n" \
".error \"size model\"\n" \
".endif\n" \
"Lk7_%=:\n" \
"s_cbranch_execz Lk8_%=\n" \
"s_nop 0\n" \
"v_mfma_f32_16x16x32_bf16 v[24:27], a[152:155], v[184:187], v[24:27]\n" \
"ds_read_b128 a[112:115], %[kb0] offset:28672\n" \
"s_mov_b32 m0, s44\n" \
"s_addk_i32 s44, 0x400\n" \
"buffer_load_dwordx4 v229, s[64:67], s45 offen offset:0 lds\n" \
"s_add_u32 s45, s45, 229376\n" \
"v_mfma_f32_16x16x32_bf16 v[28:31], a[188:191], v[192:195], v[28:31]\n" \
"v_exp_f32_e64 v155, v155\n" \
"v_mfma_f32_16x16x32_bf16 v[28:31], a[156:159], v[184:187], v[28:31]\n" \
"s_waitcnt vmcnt(6) lgkmcnt(2)\n" \
"s_barrier\n" \
"v_mfma_f32_16x16x32_bf16 v[128:131], a[96:99], a[0:3], v[160:163]\n" \
"v_exp_f32_e64 v156, v156\n" \
"s_mov_b32 m0, s44\n" \
"s_addk_i32 s44, 0x400\n" \
"buffer_load_dwordx4 v229, s[64:67], s45 offen offset:0 lds\n" \
"s_add_u32 s45, s45, 229376\n" \
"v_readlane_b32 s64, %[tlo], 8\n" \
"v_readlane_b32 s65, %[thi], 8\n" \
"v_mfma_f32_16x16x32_bf16 v[128:131], a[100:103], a[4:7], v[128:131]\n" \
"v_mbcnt_lo_u32_b32 v231, -1, 0\n" \
"v_mfma_f32_16x16x32_bf16 v[128:131], a[104:107], a[8:11], v[128:131]\n" \
"v_exp_f32_e64 v157, v157\n" \
"v_mfma_f32_16x16x32_bf16 v[128:131], a[108:111], a[12:15], v[128:131]\n" \
"v_cvt_pk_bf16_f32 v220, v152, v153\n" \
"v_cvt_pk_bf16_f32 v221, v154, v155\n" \
"v_mfma_f32_16x16x32_bf16 v[132:135], a[124:127], a[12:15], v[160:163]\n" \
"v_exp_f32_e64 v158, v158\n" \
"v_mfma_f32_16x16x32_bf16 v[132:135], a[120:123], a[8:11], v[132:135]\n" \
"v_mbcnt_hi_u32_b32 v231, -1, v231\n" \
"s_waitcnt lgkmcnt(0)\n" \
"v_mfma_f32_16x16x32_bf16 v[132:135], a[116:119], a[4:7], v[132:135]\n" \
"s_add_u32 m0, %[m0b], 65536\n" \
"s_mov_b32 s61, 0\n" \
"buffer_load_dwordx4 %[vg0], s[64:67], s61 offen offset:0 lds\n" \
"v_mfma_f32_16x16x32_bf16 v[132:135], a[112:115], a[0:3], v[132:135]\n" \
"v_exp_f32_e64 v159, v159\n" \
"v_mfma_f32_16x16x32_bf16 v[32:35], a[160:163], v[196:199], v[32:35]\n" \
"ds_read_b128 a[64:67], %[kb0] offset:32768\n" \
"v_and_b32_e64 v226, 31, v231\n" \
"v_mfma_f32_16x16x32_bf16 v[32:35], a[128:131], v[188:191], v[32:35]\n" \
"v_exp_f32_e64 v128, v128\n" \
"v_mfma_f32_16x16x32_bf16 v[36:39], a[164:167], v[196:199], v[36:39]\n" \
"v_cvt_pk_bf16_f32 v222, v156, v157\n" \
"v_cvt_pk_bf16_f32 v223, v158, v159\n" \
"v_mfma_f32_16x16x32_bf16 v[36:39], a[132:135], v[188:191], v[36:39]\n" \
"v_exp_f32_e64 v129, v129\n" \
"v_mfma_f32_16x16x32_bf16 v[40:43], a[168:171], v[196:199], v[40:43]\n" \
"ds_read_b128 a[68:71], v208 offset:32768\n" \
"v_lshlrev_b32_e64 v226, 2, v226\n" \
"v_mfma_f32_16x16x32_bf16 v[40:43], a[136:139], v[188:191], v[40:43]\n" \
"v_exp_f32_e64 v130, v130\n" \
"v_mfma_f32_16x16x32_bf16 v[44:47], a[172:175], v[196:199], v[44:47]\n" \
"ds_read_b128 a[72:75], v209 offset:32768\n" \
"v_cmp_gt_u32_e32 vcc, 1, v231\n" \
"v_mfma_f32_16x16x32_bf16 v[44:47], a[140:143], v[188:191], v[44:47]\n" \
"v_exp_f32_e32 v131, v131\n" \
"v_smfmac_f32_16x16x64_bf16 v[176:179], a[192:195], v[184:191], v228\n" \
"v_mov_b32_e32 v225, 0x80000000\n" \
"v_mfma_f32_16x16x32_bf16 v[136:139], a[96:99], a[16:19], v[164:167]\n" \
"v_exp_f32_e64 v132, v132\n" \
"v_mfma_f32_16x16x32_bf16 v[136:139], a[100:103], a[20:23], v[136:139]\n" \
"ds_read_b128 a[76:79], v210 offset:32768\n" \
"v_mfma_f32_16x16x32_bf16 v[136:139], a[104:107], a[24:27], v[136:139]\n" \
"v_exp_f32_e64 v133, v133\n" \
"v_mfma_f32_16x16x32_bf16 v[136:139], a[108:111], a[28:31], v[136:139]\n" \
"v_cvt_pk_bf16_f32 v192, v128, v129\n" \
"v_cvt_pk_bf16_f32 v193, v130, v131\n" \
"v_mfma_f32_16x16x32_bf16 v[140:143], a[124:127], a[28:31], v[164:167]\n" \
"v_exp_f32_e64 v134, v134\n" \
"v_mfma_f32_16x16x32_bf16 v[140:143], a[120:123], a[24:27], v[140:143]\n" \
"ds_read_b128 a[92:95], v210 offset:36864\n" \
"v_cndmask_b32_e32 v225, v225, v224, vcc\n" \
"v_mfma_f32_16x16x32_bf16 v[140:143], a[116:119], a[20:23], v[140:143]\n" \
"s_add_u32 m0, %[m0b], 66560\n" \
"s_mov_b32 s61, 229376\n" \
"buffer_load_dwordx4 %[vg0], s[64:67], s61 offen offset:0 lds\n" \
"v_mfma_f32_16x16x32_bf16 v[140:143], a[112:115], a[16:19], v[140:143]\n" \
"v_exp_f32_e32 v135, v135\n" \
"v_mfma_f32_16x16x32_bf16 v[48:51], a[176:179], v[196:199], v[48:51]\n" \
"ds_read_b128 a[88:91], v209 offset:36864\n" \
"v_mfma_f32_16x16x32_bf16 v[48:51], a[144:147], v[188:191], v[48:51]\n" \
"v_exp_f32_e64 v136, v136\n" \
"v_mfma_f32_16x16x32_bf16 v[52:55], a[180:183], v[196:199], v[52:55]\n" \
"v_cvt_pk_bf16_f32 v194, v132, v133\n" \
"v_cvt_pk_bf16_f32 v195, v134, v135\n" \
"v_mfma_f32_16x16x32_bf16 v[52:55], a[148:151], v[188:191], v[52:55]\n" \
"v_exp_f32_e64 v137, v137\n" \
"v_mfma_f32_16x16x32_bf16 v[56:59], a[184:187], v[196:199], v[56:59]\n" \
"v_exp_f32_e64 v138, v138\n" \
"v_mfma_f32_16x16x32_bf16 v[56:59], a[152:155], v[188:191], v[56:59]\n" \
"ds_read_b128 a[84:87], v208 offset:36864\n" \
"v_mfma_f32_16x16x32_bf16 v[60:63], a[188:191], v[196:199], v[60:63]\n" \
"v_exp_f32_e64 v139, v139\n" \
"v_mfma_f32_16x16x32_bf16 v[60:63], a[156:159], v[188:191], v[60:63]\n" \
"v_mfma_f32_16x16x32_bf16 v[144:147], a[96:99], a[32:35], v[168:171]\n" \
"v_exp_f32_e64 v140, v140\n" \
"v_mfma_f32_16x16x32_bf16 v[144:147], a[100:103], a[36:39], v[144:147]\n" \
"ds_read_b128 a[80:83], %[kb0] offset:36864\n" \
"s_sub_u32 s61, s48, 0\n" \
"s_cmp_lt_u32 s61, 4\n" \
"s_cselect_b64 vcc, -1, 0\n" \
"v_mfma_f32_16x16x32_bf16 v[144:147], a[104:107], a[40:43], v[144:147]\n" \
"v_exp_f32_e32 v141, v141\n" \
"v_mfma_f32_16x16x32_bf16 v[144:147], a[108:111], a[44:47], v[144:147]\n" \
"v_cvt_pk_bf16_f32 v196, v136, v137\n" \
"v_cvt_pk_bf16_f32 v197, v138, v139\n" \
"v_mfma_f32_16x16x32_bf16 v[148:151], a[124:127], a[44:47], v[168:171]\n" \
"v_exp_f32_e64 v142, v142\n" \
"v_mfma_f32_16x16x32_bf16 v[148:151], a[120:123], a[40:43], v[148:151]\n" \
"v_readlane_b32 s55, %[tlo], 16\n" \
"v_mfma_f32_16x16x32_bf16 v[148:151], a[116:119], a[36:39], v[148:151]\n" \
"s_add_u32 m0, %[m0b], 67584\n" \
"s_mov_b32 s61, 458752\n" \
"buffer_load_dwordx4 %[vg0], s[64:67], s61 offen offset:0 lds\n" \
"v_mfma_f32_16x16x32_bf16 v[148:151], a[112:115], a[32:35], v[148:151]\n" \
"v_exp_f32_e64 v143, v143\n" \
"v_mfma_f32_16x16x32_bf16 v[96:99], a[160:163], v[204:207], v[96:99]\n" \
"ds_read_b64_tr_b16 a[160:161], %[vvb] offset:24576\n" \
"v_cmp_gt_u32_e64 s[56:57], 32, v231\n" \
"v_mfma_f32_16x16x32_bf16 v[96:99], a[128:131], v[220:223], v[96:99]\n" \
"v_exp_f32_e64 v144, v144\n" \
"v_mfma_f32_16x16x32_bf16 v[100:103], a[164:167], v[204:207], v[100:103]\n" \
"v_cvt_pk_bf16_f32 v198, v140, v141\n" \
"v_cvt_pk_bf16_f32 v199, v142, v143\n" \
"v_mfma_f32_16x16x32_bf16 v[100:103], a[132:135], v[220:223], v[100:103]\n" \
"v_exp_f32_e64 v145, v145\n" \
"v_mfma_f32_16x16x32_bf16 v[104:107], a[168:171], v[204:207], v[104:107]\n" \
"ds_read_b64_tr_b16 a[162:163], %[vvb] offset:28672\n" \
"ds_read_b64_tr_b16 a[164:165], v211 offset:24576\n" \
"v_lshlrev_b32_e64 v232, 2, v224\n" \
"v_mfma_f32_16x16x32_bf16 v[104:107], a[136:139], v[220:223], v[104:107]\n" \
"v_exp_f32_e64 v146, v146\n" \
"v_mfma_f32_16x16x32_bf16 v[108:111], a[172:175], v[204:207], v[108:111]\n" \
"ds_read_b64_tr_b16 a[166:167], v211 offset:28672\n" \
"v_mov_b32_e32 v233, 0x80000000\n" \
"v_mfma_f32_16x16x32_bf16 v[108:111], a[140:143], v[220:223], v[108:111]\n" \
"v_exp_f32_e64 v147, v147\n" \
"v_smfmac_f32_16x16x64_bf16 v[180:183], a[192:195], v[216:223], v228\n" \
"ds_read_b64_tr_b16 a[168:169], %[vvb] offset:25088\n" \
"v_mfma_f32_16x16x32_bf16 v[152:155], a[96:99], a[48:51], v[172:175]\n" \
"v_exp_f32_e64 v148, v148\n" \
"v_mfma_f32_16x16x32_bf16 v[152:155], a[100:103], a[52:55], v[152:155]\n" \
"ds_read_b64_tr_b16 a[170:171], %[vvb] offset:29184\n" \
"v_cndmask_b32_e64 v232, v233, v232, s[56:57]\n" \
"v_mfma_f32_16x16x32_bf16 v[152:155], a[104:107], a[56:59], v[152:155]\n" \
"v_exp_f32_e64 v149, v149\n" \
"v_mfma_f32_16x16x32_bf16 v[152:155], a[108:111], a[60:63], v[152:155]\n" \
"v_cvt_pk_bf16_f32 v200, v144, v145\n" \
"v_cvt_pk_bf16_f32 v201, v146, v147\n" \
"v_mfma_f32_16x16x32_bf16 v[156:159], a[124:127], a[60:63], v[172:175]\n" \
"v_exp_f32_e64 v150, v150\n" \
"v_mfma_f32_16x16x32_bf16 v[156:159], a[120:123], a[56:59], v[156:159]\n" \
"ds_read_b64_tr_b16 a[172:173], v211 offset:25088\n" \
"v_mov_b32_e64 v230, 1\n" \
"v_mfma_f32_16x16x32_bf16 v[156:159], a[116:119], a[52:55], v[156:159]\n" \
"s_add_u32 m0, %[m0b], 68608\n" \
"s_mov_b32 s61, 688128\n" \
"buffer_load_dwordx4 %[vg0], s[64:67], s61 offen offset:0 lds\n" \
"v_mfma_f32_16x16x32_bf16 v[156:159], a[112:115], a[48:51], v[156:159]\n" \
"v_exp_f32_e64 v151, v151\n" \
"v_mfma_f32_16x16x32_bf16 v[112:115], a[176:179], v[204:207], v[112:115]\n" \
"ds_read_b64_tr_b16 a[174:175], v211 offset:29184\n" \
"ds_read_b64_tr_b16 a[176:177], %[vvb] offset:25600\n" \
"v_mfma_f32_16x16x32_bf16 v[112:115], a[144:147], v[220:223], v[112:115]\n" \
"v_exp_f32_e64 v152, v152\n" \
"v_mfma_f32_16x16x32_bf16 v[116:119], a[180:183], v[204:207], v[116:119]\n" \
"v_cvt_pk_bf16_f32 v202, v148, v149\n" \
"v_cvt_pk_bf16_f32 v203, v150, v151\n" \
"v_mfma_f32_16x16x32_bf16 v[116:119], a[148:151], v[220:223], v[116:119]\n" \
"v_exp_f32_e64 v153, v153\n" \
"v_mfma_f32_16x16x32_bf16 v[120:123], a[184:187], v[204:207], v[120:123]\n" \
"v_exp_f32_e64 v154, v154\n" \
"v_mfma_f32_16x16x32_bf16 v[120:123], a[152:155], v[220:223], v[120:123]\n" \
"ds_read_b64_tr_b16 a[178:179], %[vvb] offset:29696\n" \
"buffer_store_dword v230, v232, %[srdz], s55 offen sc1\n" \
"v_mfma_f32_16x16x32_bf16 v[124:127], a[188:191], v[204:207], v[124:127]\n" \
"v_exp_f32_e64 v155, v155\n" \
"v_mfma_f32_16x16x32_bf16 v[124:127], a[156:159], v[220:223], v[124:127]\n" \
"ds_read_b64_tr_b16 a[180:181], v211 offset:25600\n" \
"s_waitcnt lgkmcnt(13)\n" \
"v_mfma_f32_16x16x32_bf16 v[128:131], a[64:67], a[0:3], v[160:163]\n" \
"v_exp_f32_e32 v156, v156\n" \
"v_mfma_f32_16x16x32_bf16 v[128:131], a[68:71], a[4:7], v[128:131]\n" \
"ds_read_b64_tr_b16 a[182:183], v211 offset:29696\n" \
"s_add_u32 m0, %[m0b], 16384\n" \
"s_mov_b32 s49, s48\n" \
"v_mfma_f32_16x16x32_bf16 v[128:131], a[72:75], a[8:11], v[128:131]\n" \
"v_exp_f32_e32 v157, v157\n" \
"v_mfma_f32_16x16x32_bf16 v[128:131], a[76:79], a[12:15], v[128:131]\n" \
"v_cvt_pk_bf16_f32 v204, v152, v153\n" \
"v_cvt_pk_bf16_f32 v205, v154, v155\n" \
"v_mfma_f32_16x16x32_bf16 v[132:135], a[92:95], a[12:15], v[160:163]\n" \
"v_exp_f32_e64 v158, v158\n" \
"v_mfma_f32_16x16x32_bf16 v[132:135], a[88:91], a[8:11], v[132:135]\n" \
"ds_read_b64_tr_b16 a[184:185], %[vvb] offset:26112\n" \
"s_lshl_b32 s49, s49, 18\n" \
"s_or_b32 s49, s49, s59\n" \
"s_waitcnt lgkmcnt(5)\n" \
"v_mfma_f32_16x16x32_bf16 v[132:135], a[84:87], a[4:7], v[132:135]\n" \
"buffer_load_dwordx4 %[wo0], %[srdt], s49 offen offset:0 lds\n" \
"v_mfma_f32_16x16x32_bf16 v[132:135], a[80:83], a[0:3], v[132:135]\n" \
"v_exp_f32_e32 v159, v159\n" \
"v_mfma_f32_16x16x32_bf16 v[64:67], a[128:131], v[216:219], v[64:67]\n" \
"ds_read_b64_tr_b16 a[186:187], %[vvb] offset:30208\n" \
"ds_read_b64_tr_b16 a[188:189], v211 offset:26112\n" \
"v_xor_b32_e32 v232, 0x80, %[vx0]\n" \
"v_mfma_f32_16x16x32_bf16 v[64:67], a[160:163], v[200:203], v[64:67]\n" \
"v_exp_f32_e64 v128, v128\n" \
"v_mfma_f32_16x16x32_bf16 v[68:71], a[132:135], v[216:219], v[68:71]\n" \
"v_cvt_pk_bf16_f32 v206, v156, v157\n" \
"v_cvt_pk_bf16_f32 v207, v158, v159\n" \
"v_mfma_f32_16x16x32_bf16 v[68:71], a[164:167], v[200:203], v[68:71]\n" \
"v_exp_f32_e64 v129, v129\n" \
"v_mfma_f32_16x16x32_bf16 v[72:75], a[136:139], v[216:219], v[72:75]\n" \
"ds_read_b64_tr_b16 a[190:191], v211 offset:30208\n" \
"ds_read_b64_tr_b16 a[128:129], %[vvb] offset:32768\n" \
"v_mfma_f32_16x16x32_bf16 v[72:75], a[168:171], v[200:203], v[72:75]\n" \
"v_exp_f32_e64 v130, v130\n" \
"v_mfma_f32_16x16x32_bf16 v[76:79], a[140:143], v[216:219], v[76:79]\n" \
"ds_read_b64_tr_b16 a[130:131], %[vvb] offset:36864\n" \
"ds_read_b64_tr_b16 a[132:133], v211 offset:32768\n" \
"v_mfma_f32_16x16x32_bf16 v[76:79], a[172:175], v[200:203], v[76:79]\n" \
"v_exp_f32_e64 v131, v131\n" \
"v_smfmac_f32_16x16x64_bf16 v[176:179], a[192:195], v[192:199], v228\n" \
"ds_read_b64_tr_b16 a[134:135], v211 offset:36864\n" \
"ds_read_b64_tr_b16 a[136:137], %[vvb] offset:33280\n" \
"global_load_dwordx2 a[240:241], %[vx0], %[px] offset:0\n" \
"v_mfma_f32_16x16x32_bf16 v[136:139], a[64:67], a[16:19], v[164:167]\n" \
"v_exp_f32_e64 v132, v132\n" \
"v_mfma_f32_16x16x32_bf16 v[136:139], a[68:71], a[20:23], v[136:139]\n" \
"ds_read_b64_tr_b16 a[138:139], %[vvb] offset:37376\n" \
"ds_read_b64_tr_b16 a[140:141], v211 offset:33280\n" \
"v_mfma_f32_16x16x32_bf16 v[136:139], a[72:75], a[24:27], v[136:139]\n" \
"v_exp_f32_e64 v133, v133\n" \
"v_mfma_f32_16x16x32_bf16 v[136:139], a[76:79], a[28:31], v[136:139]\n" \
"v_cvt_pk_bf16_f32 v184, v128, v129\n" \
"v_cvt_pk_bf16_f32 v185, v130, v131\n" \
"v_mfma_f32_16x16x32_bf16 v[140:143], a[92:95], a[28:31], v[164:167]\n" \
"v_exp_f32_e64 v134, v134\n" \
"v_mfma_f32_16x16x32_bf16 v[140:143], a[88:91], a[24:27], v[140:143]\n" \
"ds_read_b64_tr_b16 a[142:143], v211 offset:37376\n" \
"s_waitcnt lgkmcnt(8)\n" \
"v_mfma_f32_16x16x32_bf16 v[140:143], a[84:87], a[20:23], v[140:143]\n" \
"buffer_load_dwordx4 v212, %[srdt], s49 offen offset:1024 lds\n" \
"v_mfma_f32_16x16x32_bf16 v[140:143], a[80:83], a[16:19], v[140:143]\n" \
"v_exp_f32_e32 v135, v135\n" \
"v_mfma_f32_16x16x32_bf16 v[80:83], a[144:147], v[216:219], v[80:83]\n" \
"ds_read_b64_tr_b16 a[144:145], %[vvb] offset:33792\n" \
"ds_read_b64_tr_b16 a[146:147], %[vvb] offset:37888\n" \
"v_mfma_f32_16x16x32_bf16 v[80:83], a[176:179], v[200:203], v[80:83]\n" \
"v_exp_f32_e64 v136, v136\n" \
"v_mfma_f32_16x16x32_bf16 v[84:87], a[148:151], v[216:219], v[84:87]\n" \
"v_cvt_pk_bf16_f32 v186, v132, v133\n" \
"v_cvt_pk_bf16_f32 v187, v134, v135\n" \
"v_mfma_f32_16x16x32_bf16 v[84:87], a[180:183], v[200:203], v[84:87]\n" \
"v_exp_f32_e64 v137, v137\n" \
"v_mfma_f32_16x16x32_bf16 v[88:91], a[152:155], v[216:219], v[88:91]\n" \
"v_exp_f32_e64 v138, v138\n" \
"v_mfma_f32_16x16x32_bf16 v[88:91], a[184:187], v[200:203], v[88:91]\n" \
"ds_read_b64_tr_b16 a[148:149], v211 offset:33792\n" \
"ds_read_b64_tr_b16 a[150:151], v211 offset:37888\n" \
"v_mfma_f32_16x16x32_bf16 v[92:95], a[156:159], v[216:219], v[92:95]\n" \
"v_exp_f32_e64 v139, v139\n" \
"v_mfma_f32_16x16x32_bf16 v[92:95], a[188:191], v[200:203], v[92:95]\n" \
"ds_read_b64_tr_b16 a[152:153], %[vvb] offset:34304\n" \
"global_load_dwordx2 a[242:243], %[vx0], %[px] offset:1024\n" \
"v_mfma_f32_16x16x32_bf16 v[144:147], a[64:67], a[32:35], v[168:171]\n" \
"v_exp_f32_e64 v140, v140\n" \
"v_mfma_f32_16x16x32_bf16 v[144:147], a[68:71], a[36:39], v[144:147]\n" \
"ds_read_b64_tr_b16 a[154:155], %[vvb] offset:38400\n" \
"ds_read_b64_tr_b16 a[156:157], v211 offset:34304\n" \
"v_mfma_f32_16x16x32_bf16 v[144:147], a[72:75], a[40:43], v[144:147]\n" \
"v_exp_f32_e64 v141, v141\n" \
"v_mfma_f32_16x16x32_bf16 v[144:147], a[76:79], a[44:47], v[144:147]\n" \
"v_cvt_pk_bf16_f32 v188, v136, v137\n" \
"v_cvt_pk_bf16_f32 v189, v138, v139\n" \
"v_mfma_f32_16x16x32_bf16 v[148:151], a[92:95], a[44:47], v[168:171]\n" \
"v_exp_f32_e64 v142, v142\n" \
"v_mfma_f32_16x16x32_bf16 v[148:151], a[88:91], a[40:43], v[148:151]\n" \
"ds_read_b64_tr_b16 a[158:159], v211 offset:38400\n" \
"s_waitcnt lgkmcnt(8)\n" \
"v_mfma_f32_16x16x32_bf16 v[148:151], a[84:87], a[36:39], v[148:151]\n" \
"buffer_load_dwordx4 v213, %[srdt], s49 offen offset:2048 lds\n" \
"v_mfma_f32_16x16x32_bf16 v[148:151], a[80:83], a[32:35], v[148:151]\n" \
"v_exp_f32_e32 v143, v143\n" \
"v_mfma_f32_16x16x32_bf16 v[0:3], a[160:163], v[192:195], v[0:3]\n" \
"ds_read_b128 a[96:99], %[kb0] offset:40960\n" \
"v_mfma_f32_16x16x32_bf16 v[0:3], a[128:131], v[184:187], v[0:3]\n" \
"v_exp_f32_e64 v144, v144\n" \
"v_mfma_f32_16x16x32_bf16 v[4:7], a[164:167], v[192:195], v[4:7]\n" \
"v_cvt_pk_bf16_f32 v190, v140, v141\n" \
"v_cvt_pk_bf16_f32 v191, v142, v143\n" \
"v_mfma_f32_16x16x32_bf16 v[4:7], a[132:135], v[184:187], v[4:7]\n" \
"v_exp_f32_e64 v145, v145\n" \
"v_mfma_f32_16x16x32_bf16 v[8:11], a[168:171], v[192:195], v[8:11]\n" \
"ds_read_b128 a[100:103], v208 offset:40960\n" \
"v_mfma_f32_16x16x32_bf16 v[8:11], a[136:139], v[184:187], v[8:11]\n" \
"v_exp_f32_e64 v146, v146\n" \
"v_mfma_f32_16x16x32_bf16 v[12:15], a[172:175], v[192:195], v[12:15]\n" \
"ds_read_b128 a[104:107], v209 offset:40960\n" \
"v_mfma_f32_16x16x32_bf16 v[12:15], a[140:143], v[184:187], v[12:15]\n" \
"v_exp_f32_e64 v147, v147\n" \
"v_smfmac_f32_16x16x64_bf16 v[180:183], a[192:195], v[200:207], v228\n" \
"ds_read_b128 a[108:111], v210 offset:40960\n" \
"global_load_dwordx2 a[248:249], v232, %[px] offset:0\n" \
"v_mfma_f32_16x16x32_bf16 v[152:155], a[64:67], a[48:51], v[172:175]\n" \
"v_exp_f32_e64 v148, v148\n" \
"v_mfma_f32_16x16x32_bf16 v[152:155], a[68:71], a[52:55], v[152:155]\n" \
"ds_read_b128 a[124:127], v210 offset:45056\n" \
"v_mfma_f32_16x16x32_bf16 v[152:155], a[72:75], a[56:59], v[152:155]\n" \
"v_exp_f32_e64 v149, v149\n" \
"v_mfma_f32_16x16x32_bf16 v[152:155], a[76:79], a[60:63], v[152:155]\n" \
"v_cvt_pk_bf16_f32 v216, v144, v145\n" \
"v_cvt_pk_bf16_f32 v217, v146, v147\n" \
"v_mfma_f32_16x16x32_bf16 v[156:159], a[92:95], a[60:63], v[172:175]\n" \
"v_exp_f32_e64 v150, v150\n" \
"v_mfma_f32_16x16x32_bf16 v[156:159], a[88:91], a[56:59], v[156:159]\n" \
"ds_read_b128 a[120:123], v209 offset:45056\n" \
"s_waitcnt lgkmcnt(6)\n" \
"v_mfma_f32_16x16x32_bf16 v[156:159], a[84:87], a[52:55], v[156:159]\n" \
"buffer_load_dwordx4 v214, %[srdt], s49 offen offset:3072 lds\n" \
"v_mfma_f32_16x16x32_bf16 v[156:159], a[80:83], a[48:51], v[156:159]\n" \
"v_exp_f32_e32 v151, v151\n" \
"v_mfma_f32_16x16x32_bf16 v[16:19], a[176:179], v[192:195], v[16:19]\n" \
"ds_read_b128 a[116:119], v208 offset:45056\n" \
"v_mfma_f32_16x16x32_bf16 v[16:19], a[144:147], v[184:187], v[16:19]\n" \
"v_exp_f32_e64 v152, v152\n" \
"v_mfma_f32_16x16x32_bf16 v[20:23], a[180:183], v[192:195], v[20:23]\n" \
"v_cvt_pk_bf16_f32 v218, v148, v149\n" \
"v_cvt_pk_bf16_f32 v219, v150, v151\n" \
"v_mfma_f32_16x16x32_bf16 v[20:23], a[148:151], v[184:187], v[20:23]\n" \
"v_exp_f32_e64 v153, v153\n" \
"v_mfma_f32_16x16x32_bf16 v[24:27], a[184:187], v[192:195], v[24:27]\n" \
"v_exp_f32_e64 v154, v154\n" \
"v_mfma_f32_16x16x32_bf16 v[24:27], a[152:155], v[184:187], v[24:27]\n" \
"ds_read_b128 a[112:115], %[kb0] offset:45056\n" \
"global_load_dwordx2 a[250:251], v232, %[px] offset:1024\n" \
"v_mfma_f32_16x16x32_bf16 v[28:31], a[188:191], v[192:195], v[28:31]\n" \
"v_exp_f32_e64 v155, v155\n" \
"v_mfma_f32_16x16x32_bf16 v[28:31], a[156:159], v[184:187], v[28:31]\n" \
"s_waitcnt vmcnt(63) lgkmcnt(2)\n" \
"s_barrier\n" \
"v_mfma_f32_16x16x32_bf16 v[128:131], a[96:99], a[0:3], v[160:163]\n" \
"v_exp_f32_e64 v156, v156\n" \
"v_mfma_f32_16x16x32_bf16 v[128:131], a[100:103], a[4:7], v[128:131]\n" \
"v_lshrrev_b32_e64 v227, 5, v231\n" \
"v_mfma_f32_16x16x32_bf16 v[128:131], a[104:107], a[8:11], v[128:131]\n" \
"v_exp_f32_e64 v157, v157\n" \
"v_mfma_f32_16x16x32_bf16 v[128:131], a[108:111], a[12:15], v[128:131]\n" \
"v_cvt_pk_bf16_f32 v220, v152, v153\n" \
"v_cvt_pk_bf16_f32 v221, v154, v155\n" \
"v_mfma_f32_16x16x32_bf16 v[132:135], a[124:127], a[12:15], v[160:163]\n" \
"v_exp_f32_e64 v158, v158\n" \
"v_mfma_f32_16x16x32_bf16 v[132:135], a[120:123], a[8:11], v[132:135]\n" \
"v_lshlrev_b32_e32 v227, 10, v227\n" \
"s_waitcnt lgkmcnt(0)\n" \
"v_mfma_f32_16x16x32_bf16 v[132:135], a[116:119], a[4:7], v[132:135]\n" \
"s_add_u32 m0, %[m0b], 81920\n" \
"s_mov_b32 s61, 917504\n" \
"buffer_load_dwordx4 %[vg0], s[64:67], s61 offen offset:0 lds\n" \
"v_mfma_f32_16x16x32_bf16 v[132:135], a[112:115], a[0:3], v[132:135]\n" \
"v_exp_f32_e64 v159, v159\n" \
"v_mfma_f32_16x16x32_bf16 v[32:35], a[160:163], v[196:199], v[32:35]\n" \
"ds_read_b128 a[64:67], %[kb0] offset:49152\n" \
"v_add_u32_e32 v227, 0x10000, v227\n" \
"v_mfma_f32_16x16x32_bf16 v[32:35], a[128:131], v[188:191], v[32:35]\n" \
"v_exp_f32_e64 v128, v128\n" \
"v_mfma_f32_16x16x32_bf16 v[36:39], a[164:167], v[196:199], v[36:39]\n" \
"v_cvt_pk_bf16_f32 v222, v156, v157\n" \
"v_cvt_pk_bf16_f32 v223, v158, v159\n" \
"v_mfma_f32_16x16x32_bf16 v[36:39], a[132:135], v[188:191], v[36:39]\n" \
"v_exp_f32_e64 v129, v129\n" \
"v_mfma_f32_16x16x32_bf16 v[40:43], a[168:171], v[196:199], v[40:43]\n" \
"ds_read_b128 a[68:71], v208 offset:49152\n" \
"v_sub_u32_e64 v215, %[vvb], v227\n" \
"v_mfma_f32_16x16x32_bf16 v[40:43], a[136:139], v[188:191], v[40:43]\n" \
"v_exp_f32_e64 v130, v130\n" \
"v_mfma_f32_16x16x32_bf16 v[44:47], a[172:175], v[196:199], v[44:47]\n" \
"ds_read_b128 a[72:75], v209 offset:49152\n" \
"v_xor_b32_e64 v227, 32, v215\n" \
"v_mfma_f32_16x16x32_bf16 v[44:47], a[140:143], v[188:191], v[44:47]\n" \
"v_exp_f32_e64 v131, v131\n" \
"v_smfmac_f32_16x16x64_bf16 v[176:179], a[192:195], v[184:191], v228\n" \
"global_load_dwordx2 a[244:245], %[vx0], %[px] offset:64\n" \
"v_mfma_f32_16x16x32_bf16 v[136:139], a[96:99], a[16:19], v[164:167]\n" \
"v_exp_f32_e64 v132, v132\n" \
"v_mfma_f32_16x16x32_bf16 v[136:139], a[100:103], a[20:23], v[136:139]\n" \
"ds_read_b128 a[76:79], v210 offset:49152\n" \
"v_mfma_f32_16x16x32_bf16 v[136:139], a[104:107], a[24:27], v[136:139]\n" \
"v_exp_f32_e64 v133, v133\n" \
"v_mfma_f32_16x16x32_bf16 v[136:139], a[108:111], a[28:31], v[136:139]\n" \
"v_cvt_pk_bf16_f32 v192, v128, v129\n" \
"v_cvt_pk_bf16_f32 v193, v130, v131\n" \
"v_mfma_f32_16x16x32_bf16 v[140:143], a[124:127], a[28:31], v[164:167]\n" \
"v_exp_f32_e64 v134, v134\n" \
"v_mfma_f32_16x16x32_bf16 v[140:143], a[120:123], a[24:27], v[140:143]\n" \
"ds_read_b128 a[92:95], v210 offset:53248\n" \
"v_mfma_f32_16x16x32_bf16 v[140:143], a[116:119], a[20:23], v[140:143]\n" \
"s_add_u32 m0, %[m0b], 82944\n" \
"s_mov_b32 s61, 1146880\n" \
"buffer_load_dwordx4 %[vg0], s[64:67], s61 offen offset:0 lds\n" \
"v_mfma_f32_16x16x32_bf16 v[140:143], a[112:115], a[16:19], v[140:143]\n" \
"v_exp_f32_e64 v135, v135\n" \
"v_mfma_f32_16x16x32_bf16 v[48:51], a[176:179], v[196:199], v[48:51]\n" \
"ds_read_b128 a[88:91], v209 offset:53248\n" \
"v_mfma_f32_16x16x32_bf16 v[48:51], a[144:147], v[188:191], v[48:51]\n" \
"v_exp_f32_e64 v136, v136\n" \
"v_mfma_f32_16x16x32_bf16 v[52:55], a[180:183], v[196:199], v[52:55]\n" \
"v_cvt_pk_bf16_f32 v194, v132, v133\n" \
"v_cvt_pk_bf16_f32 v195, v134, v135\n" \
"v_mfma_f32_16x16x32_bf16 v[52:55], a[148:151], v[188:191], v[52:55]\n" \
"v_exp_f32_e64 v137, v137\n" \
"v_mfma_f32_16x16x32_bf16 v[56:59], a[184:187], v[196:199], v[56:59]\n" \
"v_exp_f32_e64 v138, v138\n" \
"v_mfma_f32_16x16x32_bf16 v[56:59], a[152:155], v[188:191], v[56:59]\n" \
"ds_read_b128 a[84:87], v208 offset:53248\n" \
"v_mfma_f32_16x16x32_bf16 v[60:63], a[188:191], v[196:199], v[60:63]\n" \
"v_exp_f32_e64 v139, v139\n" \
"v_mfma_f32_16x16x32_bf16 v[60:63], a[156:159], v[188:191], v[60:63]\n" \
"global_load_dwordx2 a[246:247], %[vx0], %[px] offset:1088\n" \
"v_mfma_f32_16x16x32_bf16 v[144:147], a[96:99], a[32:35], v[168:171]\n" \
"v_exp_f32_e64 v140, v140\n" \
"v_mfma_f32_16x16x32_bf16 v[144:147], a[100:103], a[36:39], v[144:147]\n" \
"ds_read_b128 a[80:83], %[kb0] offset:53248\n" \
"v_mfma_f32_16x16x32_bf16 v[144:147], a[104:107], a[40:43], v[144:147]\n" \
"v_exp_f32_e64 v141, v141\n" \
"v_mfma_f32_16x16x32_bf16 v[144:147], a[108:111], a[44:47], v[144:147]\n" \
"v_cvt_pk_bf16_f32 v196, v136, v137\n" \
"v_cvt_pk_bf16_f32 v197, v138, v139\n" \
"v_mfma_f32_16x16x32_bf16 v[148:151], a[124:127], a[44:47], v[168:171]\n" \
"v_exp_f32_e64 v142, v142\n" \
"v_mfma_f32_16x16x32_bf16 v[148:151], a[120:123], a[40:43], v[148:151]\n" \
"v_mfma_f32_16x16x32_bf16 v[148:151], a[116:119], a[36:39], v[148:151]\n" \
"s_add_u32 m0, %[m0b], 83968\n" \
"s_mov_b32 s61, 1376256\n" \
"buffer_load_dwordx4 %[vg0], s[64:67], s61 offen offset:0 lds\n" \
"v_mfma_f32_16x16x32_bf16 v[148:151], a[112:115], a[32:35], v[148:151]\n" \
"v_exp_f32_e64 v143, v143\n" \
"v_mfma_f32_16x16x32_bf16 v[96:99], a[160:163], v[204:207], v[96:99]\n" \
"ds_read_b64_tr_b16 a[160:161], %[vvb] offset:40960\n" \
"v_mfma_f32_16x16x32_bf16 v[96:99], a[128:131], v[220:223], v[96:99]\n" \
"v_exp_f32_e64 v144, v144\n" \
"v_mfma_f32_16x16x32_bf16 v[100:103], a[164:167], v[204:207], v[100:103]\n" \
"v_cvt_pk_bf16_f32 v198, v140, v141\n" \
"v_cvt_pk_bf16_f32 v199, v142, v143\n" \
"v_mfma_f32_16x16x32_bf16 v[100:103], a[132:135], v[220:223], v[100:103]\n" \
"v_exp_f32_e64 v145, v145\n" \
"v_mfma_f32_16x16x32_bf16 v[104:107], a[168:171], v[204:207], v[104:107]\n" \
"ds_read_b64_tr_b16 a[162:163], %[vvb] offset:45056\n" \
"ds_read_b64_tr_b16 a[164:165], v211 offset:40960\n" \
"v_mfma_f32_16x16x32_bf16 v[104:107], a[136:139], v[220:223], v[104:107]\n" \
"v_exp_f32_e64 v146, v146\n" \
"v_mfma_f32_16x16x32_bf16 v[108:111], a[172:175], v[204:207], v[108:111]\n" \
"ds_read_b64_tr_b16 a[166:167], v211 offset:45056\n" \
"v_mfma_f32_16x16x32_bf16 v[108:111], a[140:143], v[220:223], v[108:111]\n" \
"v_exp_f32_e64 v147, v147\n" \
"v_smfmac_f32_16x16x64_bf16 v[180:183], a[192:195], v[216:223], v228\n" \
"ds_read_b64_tr_b16 a[168:169], %[vvb] offset:41472\n" \
"global_load_dwordx2 a[252:253], v232, %[px] offset:64\n" \
"v_mfma_f32_16x16x32_bf16 v[152:155], a[96:99], a[48:51], v[172:175]\n" \
"v_exp_f32_e64 v148, v148\n" \
"v_mfma_f32_16x16x32_bf16 v[152:155], a[100:103], a[52:55], v[152:155]\n" \
"ds_read_b64_tr_b16 a[170:171], %[vvb] offset:45568\n" \
"v_mfma_f32_16x16x32_bf16 v[152:155], a[104:107], a[56:59], v[152:155]\n" \
"v_exp_f32_e64 v149, v149\n" \
"v_mfma_f32_16x16x32_bf16 v[152:155], a[108:111], a[60:63], v[152:155]\n" \
"v_cvt_pk_bf16_f32 v200, v144, v145\n" \
"v_cvt_pk_bf16_f32 v201, v146, v147\n" \
"v_mfma_f32_16x16x32_bf16 v[156:159], a[124:127], a[60:63], v[172:175]\n" \
"v_exp_f32_e64 v150, v150\n" \
"v_mfma_f32_16x16x32_bf16 v[156:159], a[120:123], a[56:59], v[156:159]\n" \
"ds_read_b64_tr_b16 a[172:173], v211 offset:41472\n" \
"v_mfma_f32_16x16x32_bf16 v[156:159], a[116:119], a[52:55], v[156:159]\n" \
"s_add_u32 m0, %[m0b], 84992\n" \
"s_mov_b32 s61, 1605632\n" \
"buffer_load_dwordx4 %[vg0], s[64:67], s61 offen offset:0 lds\n" \
"v_mfma_f32_16x16x32_bf16 v[156:159], a[112:115], a[48:51], v[156:159]\n" \
"v_exp_f32_e64 v151, v151\n" \
"v_mfma_f32_16x16x32_bf16 v[112:115], a[176:179], v[204:207], v[112:115]\n" \
"ds_read_b64_tr_b16 a[174:175], v211 offset:45568\n" \
"ds_read_b64_tr_b16 a[176:177], %[vvb] offset:41984\n" \
"v_mfma_f32_16x16x32_bf16 v[112:115], a[144:147], v[220:223], v[112:115]\n" \
"v_exp_f32_e64 v152, v152\n" \
"v_mfma_f32_16x16x32_bf16 v[116:119], a[180:183], v[204:207], v[116:119]\n" \
"v_cvt_pk_bf16_f32 v202, v148, v149\n" \
"v_cvt_pk_bf16_f32 v203, v150, v151\n" \
"v_mfma_f32_16x16x32_bf16 v[116:119], a[148:151], v[220:223], v[116:119]\n" \
"v_exp_f32_e64 v153, v153\n" \
"v_mfma_f32_16x16x32_bf16 v[120:123], a[184:187], v[204:207], v[120:123]\n" \
"v_exp_f32_e64 v154, v154\n" \
"v_mfma_f32_16x16x32_bf16 v[120:123], a[152:155], v[220:223], v[120:123]\n" \
"ds_read_b64_tr_b16 a[178:179], %[vvb] offset:46080\n" \
"global_load_dwordx2 a[254:255], v232, %[px] offset:1088\n" \
"v_mfma_f32_16x16x32_bf16 v[124:127], a[188:191], v[204:207], v[124:127]\n" \
"v_exp_f32_e64 v155, v155\n" \
"v_mfma_f32_16x16x32_bf16 v[124:127], a[156:159], v[220:223], v[124:127]\n" \
"ds_read_b64_tr_b16 a[180:181], v211 offset:41984\n" \
"s_waitcnt lgkmcnt(13)\n" \
"v_mfma_f32_16x16x32_bf16 v[128:131], a[64:67], a[0:3], v[160:163]\n" \
"v_exp_f32_e32 v156, v156\n" \
"v_mfma_f32_16x16x32_bf16 v[128:131], a[68:71], a[4:7], v[128:131]\n" \
"ds_read_b64_tr_b16 a[182:183], v211 offset:46080\n" \
"s_add_u32 m0, %[m0b], 32768\n" \
"s_cmp_gt_u32 s48, 0\n" \
"v_mfma_f32_16x16x32_bf16 v[128:131], a[72:75], a[8:11], v[128:131]\n" \
"v_exp_f32_e32 v157, v157\n" \
"v_mfma_f32_16x16x32_bf16 v[128:131], a[76:79], a[12:15], v[128:131]\n" \
"v_cvt_pk_bf16_f32 v204, v152, v153\n" \
"v_cvt_pk_bf16_f32 v205, v154, v155\n" \
"v_mfma_f32_16x16x32_bf16 v[132:135], a[92:95], a[12:15], v[160:163]\n" \
"v_exp_f32_e64 v158, v158\n" \
"v_mfma_f32_16x16x32_bf16 v[132:135], a[88:91], a[8:11], v[132:135]\n" \
"ds_read_b64_tr_b16 a[184:185], %[vvb] offset:42496\n" \
"s_cselect_b32 s49, 0, 1\n" \
"s_lshl_b32 s49, s49, 18\n" \
"s_or_b32 s49, s49, s58\n" \
"s_waitcnt lgkmcnt(5)\n" \
"v_mfma_f32_16x16x32_bf16 v[132:135], a[84:87], a[4:7], v[132:135]\n" \
"buffer_load_dwordx4 %[wo0], %[srdt], s49 offen offset:0 lds\n" \
"v_mfma_f32_16x16x32_bf16 v[132:135], a[80:83], a[0:3], v[132:135]\n" \
"v_exp_f32_e64 v159, v159\n" \
"v_mfma_f32_16x16x32_bf16 v[64:67], a[128:131], v[216:219], v[64:67]\n" \
"ds_read_b64_tr_b16 a[186:187], %[vvb] offset:46592\n" \
"ds_read_b64_tr_b16 a[188:189], v211 offset:42496\n" \
"v_mfma_f32_16x16x32_bf16 v[64:67], a[160:163], v[200:203], v[64:67]\n" \
"v_exp_f32_e64 v128, v128\n" \
"v_mfma_f32_16x16x32_bf16 v[68:71], a[132:135], v[216:219], v[68:71]\n" \
"v_cvt_pk_bf16_f32 v206, v156, v157\n" \
"v_cvt_pk_bf16_f32 v207, v158, v159\n" \
"v_mfma_f32_16x16x32_bf16 v[68:71], a[164:167], v[200:203], v[68:71]\n" \
"v_exp_f32_e64 v129, v129\n" \
".if . - adaln_body != 36864\n" \
".error \"size model\"\n" \
".endif\n" \
"Lk8_%=:\n" \
"s_cbranch_execz Lk9_%=\n" \
"s_nop 0\n" \
"v_mfma_f32_16x16x32_bf16 v[72:75], a[136:139], v[216:219], v[72:75]\n" \
"ds_read_b64_tr_b16 a[190:191], v211 offset:46592\n" \
"ds_read_b64_tr_b16 a[128:129], %[vvb] offset:49152\n" \
"v_mfma_f32_16x16x32_bf16 v[72:75], a[168:171], v[200:203], v[72:75]\n" \
"v_exp_f32_e64 v130, v130\n" \
"v_mfma_f32_16x16x32_bf16 v[76:79], a[140:143], v[216:219], v[76:79]\n" \
"ds_read_b64_tr_b16 a[130:131], %[vvb] offset:53248\n" \
"ds_read_b64_tr_b16 a[132:133], v211 offset:49152\n" \
"v_mfma_f32_16x16x32_bf16 v[76:79], a[172:175], v[200:203], v[76:79]\n" \
"v_exp_f32_e64 v131, v131\n" \
"v_smfmac_f32_16x16x64_bf16 v[176:179], a[192:195], v[192:199], v228\n" \
"ds_read_b64_tr_b16 a[134:135], v211 offset:53248\n" \
"ds_read_b64_tr_b16 a[136:137], %[vvb] offset:49664\n" \
"v_mfma_f32_16x16x32_bf16 v[136:139], a[64:67], a[16:19], v[164:167]\n" \
"v_exp_f32_e64 v132, v132\n" \
"v_mfma_f32_16x16x32_bf16 v[136:139], a[68:71], a[20:23], v[136:139]\n" \
"ds_read_b64_tr_b16 a[138:139], %[vvb] offset:53760\n" \
"ds_read_b64_tr_b16 a[140:141], v211 offset:49664\n" \
"v_mfma_f32_16x16x32_bf16 v[136:139], a[72:75], a[24:27], v[136:139]\n" \
"v_exp_f32_e64 v133, v133\n" \
"v_mfma_f32_16x16x32_bf16 v[136:139], a[76:79], a[28:31], v[136:139]\n" \
"v_cvt_pk_bf16_f32 v184, v128, v129\n" \
"v_cvt_pk_bf16_f32 v185, v130, v131\n" \
"v_mfma_f32_16x16x32_bf16 v[140:143], a[92:95], a[28:31], v[164:167]\n" \
"v_exp_f32_e64 v134, v134\n" \
"v_mfma_f32_16x16x32_bf16 v[140:143], a[88:91], a[24:27], v[140:143]\n" \
"ds_read_b64_tr_b16 a[142:143], v211 offset:53760\n" \
"s_waitcnt lgkmcnt(8)\n" \
"v_mfma_f32_16x16x32_bf16 v[140:143], a[84:87], a[20:23], v[140:143]\n" \
"buffer_load_dwordx4 v212, %[srdt], s49 offen offset:1024 lds\n" \
"v_mfma_f32_16x16x32_bf16 v[140:143], a[80:83], a[16:19], v[140:143]\n" \
"v_exp_f32_e32 v135, v135\n" \
"v_mfma_f32_16x16x32_bf16 v[80:83], a[144:147], v[216:219], v[80:83]\n" \
"ds_read_b64_tr_b16 a[144:145], %[vvb] offset:50176\n" \
"ds_read_b64_tr_b16 a[146:147], %[vvb] offset:54272\n" \
"v_mfma_f32_16x16x32_bf16 v[80:83], a[176:179], v[200:203], v[80:83]\n" \
"v_exp_f32_e64 v136, v136\n" \
"v_mfma_f32_16x16x32_bf16 v[84:87], a[148:151], v[216:219], v[84:87]\n" \
"v_cvt_pk_bf16_f32 v186, v132, v133\n" \
"v_cvt_pk_bf16_f32 v187, v134, v135\n" \
"v_mfma_f32_16x16x32_bf16 v[84:87], a[180:183], v[200:203], v[84:87]\n" \
"v_exp_f32_e64 v137, v137\n" \
"v_mfma_f32_16x16x32_bf16 v[88:91], a[152:155], v[216:219], v[88:91]\n" \
"v_exp_f32_e64 v138, v138\n" \
"v_mfma_f32_16x16x32_bf16 v[88:91], a[184:187], v[200:203], v[88:91]\n" \
"ds_read_b64_tr_b16 a[148:149], v211 offset:50176\n" \
"ds_read_b64_tr_b16 a[150:151], v211 offset:54272\n" \
"v_readlane_b32 s64, %[tlo], 10\n" \
"v_mfma_f32_16x16x32_bf16 v[92:95], a[156:159], v[216:219], v[92:95]\n" \
"v_exp_f32_e64 v139, v139\n" \
"v_mfma_f32_16x16x32_bf16 v[92:95], a[188:191], v[200:203], v[92:95]\n" \
"ds_read_b64_tr_b16 a[152:153], %[vvb] offset:50688\n" \
"v_readlane_b32 s65, %[thi], 10\n" \
"v_mfma_f32_16x16x32_bf16 v[144:147], a[64:67], a[32:35], v[168:171]\n" \
"v_exp_f32_e64 v140, v140\n" \
"v_mfma_f32_16x16x32_bf16 v[144:147], a[68:71], a[36:39], v[144:147]\n" \
"ds_read_b64_tr_b16 a[154:155], %[vvb] offset:54784\n" \
"ds_read_b64_tr_b16 a[156:157], v211 offset:50688\n" \
"v_mfma_f32_16x16x32_bf16 v[144:147], a[72:75], a[40:43], v[144:147]\n" \
"v_exp_f32_e64 v141, v141\n" \
"v_mfma_f32_16x16x32_bf16 v[144:147], a[76:79], a[44:47], v[144:147]\n" \
"v_cvt_pk_bf16_f32 v188, v136, v137\n" \
"v_cvt_pk_bf16_f32 v189, v138, v139\n" \
"v_mfma_f32_16x16x32_bf16 v[148:151], a[92:95], a[44:47], v[168:171]\n" \
"v_exp_f32_e64 v142, v142\n" \
"v_mfma_f32_16x16x32_bf16 v[148:151], a[88:91], a[40:43], v[148:151]\n" \
"ds_read_b64_tr_b16 a[158:159], v211 offset:54784\n" \
"s_waitcnt lgkmcnt(8)\n" \
"v_mfma_f32_16x16x32_bf16 v[148:151], a[84:87], a[36:39], v[148:151]\n" \
"buffer_load_dwordx4 v213, %[srdt], s49 offen offset:2048 lds\n" \
"v_max3_f32 v232, v33, v38, v43\n" \
"v_mfma_f32_16x16x32_bf16 v[148:151], a[80:83], a[32:35], v[148:151]\n" \
"v_exp_f32_e32 v143, v143\n" \
"v_mfma_f32_16x16x32_bf16 v[0:3], a[160:163], v[192:195], v[0:3]\n" \
"ds_read_b128 a[96:99], %[kb0] offset:57344\n" \
"v_min3_f32 v233, v33, v38, v43\n" \
"v_mfma_f32_16x16x32_bf16 v[0:3], a[128:131], v[184:187], v[0:3]\n" \
"v_exp_f32_e64 v144, v144\n" \
"v_mfma_f32_16x16x32_bf16 v[4:7], a[164:167], v[192:195], v[4:7]\n" \
"v_cvt_pk_bf16_f32 v190, v140, v141\n" \
"v_cvt_pk_bf16_f32 v191, v142, v143\n" \
"v_mfma_f32_16x16x32_bf16 v[4:7], a[132:135], v[184:187], v[4:7]\n" \
"v_exp_f32_e64 v145, v145\n" \
"v_mfma_f32_16x16x32_bf16 v[8:11], a[168:171], v[192:195], v[8:11]\n" \
"ds_read_b128 a[100:103], v208 offset:57344\n" \
"v_max3_f32 v232, v232, v48, v53\n" \
"v_mfma_f32_16x16x32_bf16 v[8:11], a[136:139], v[184:187], v[8:11]\n" \
"v_exp_f32_e64 v146, v146\n" \
"v_mfma_f32_16x16x32_bf16 v[12:15], a[172:175], v[192:195], v[12:15]\n" \
"ds_read_b128 a[104:107], v209 offset:57344\n" \
"v_min3_f32 v233, v233, v48, v53\n" \
"v_mfma_f32_16x16x32_bf16 v[12:15], a[140:143], v[184:187], v[12:15]\n" \
"v_exp_f32_e64 v147, v147\n" \
"v_smfmac_f32_16x16x64_bf16 v[180:183], a[192:195], v[200:207], v228\n" \
"ds_read_b128 a[108:111], v210 offset:57344\n" \
"buffer_load_dword v229, v226, %[srdz], s50 offen sc1\n" \
"v_mfma_f32_16x16x32_bf16 v[152:155], a[64:67], a[48:51], v[172:175]\n" \
"v_exp_f32_e64 v148, v148\n" \
"v_mfma_f32_16x16x32_bf16 v[152:155], a[68:71], a[52:55], v[152:155]\n" \
"ds_read_b128 a[124:127], v210 offset:61440\n" \
"v_sub_f32_e64 v234, v232, v233\n" \
"v_mfma_f32_16x16x32_bf16 v[152:155], a[72:75], a[56:59], v[152:155]\n" \
"v_exp_f32_e64 v149, v149\n" \
"v_mfma_f32_16x16x32_bf16 v[152:155], a[76:79], a[60:63], v[152:155]\n" \
"v_cvt_pk_bf16_f32 v216, v144, v145\n" \
"v_cvt_pk_bf16_f32 v217, v146, v147\n" \
"v_mfma_f32_16x16x32_bf16 v[156:159], a[92:95], a[60:63], v[172:175]\n" \
"v_exp_f32_e64 v150, v150\n" \
"v_mfma_f32_16x16x32_bf16 v[156:159], a[88:91], a[56:59], v[156:159]\n" \
"ds_read_b128 a[120:123], v209 offset:61440\n" \
"s_waitcnt lgkmcnt(6)\n" \
"v_mfma_f32_16x16x32_bf16 v[156:159], a[84:87], a[52:55], v[156:159]\n" \
"buffer_load_dwordx4 v214, %[srdt], s49 offen offset:3072 lds\n" \
"v_mfma_f32_16x16x32_bf16 v[156:159], a[80:83], a[48:51], v[156:159]\n" \
"v_exp_f32_e32 v151, v151\n" \
"v_mfma_f32_16x16x32_bf16 v[16:19], a[176:179], v[192:195], v[16:19]\n" \
"ds_read_b128 a[116:119], v208 offset:61440\n" \
"v_mfma_f32_16x16x32_bf16 v[16:19], a[144:147], v[184:187], v[16:19]\n" \
"v_exp_f32_e64 v152, v152\n" \
"v_mfma_f32_16x16x32_bf16 v[20:23], a[180:183], v[192:195], v[20:23]\n" \
"v_cvt_pk_bf16_f32 v218, v148, v149\n" \
"v_cvt_pk_bf16_f32 v219, v150, v151\n" \
"v_mfma_f32_16x16x32_bf16 v[20:23], a[148:151], v[184:187], v[20:23]\n" \
"v_exp_f32_e64 v153, v153\n" \
"v_mfma_f32_16x16x32_bf16 v[24:27], a[184:187], v[192:195], v[24:27]\n" \
"v_exp_f32_e64 v154, v154\n" \
"v_mfma_f32_16x16x32_bf16 v[24:27], a[152:155], v[184:187], v[24:27]\n" \
"ds_read_b128 a[112:115], %[kb0] offset:61440\n" \
"v_mfma_f32_16x16x32_bf16 v[28:31], a[188:191], v[192:195], v[28:31]\n" \
"v_exp_f32_e64 v155, v155\n" \
"v_mfma_f32_16x16x32_bf16 v[28:31], a[156:159], v[184:187], v[28:31]\n" \
"s_waitcnt vmcnt(28) lgkmcnt(2)\n" \
"s_barrier\n" \
"v_mfma_f32_16x16x32_bf16 v[128:131], a[96:99], a[0:3], v[160:163]\n" \
"v_exp_f32_e64 v156, v156\n" \
"v_mfma_f32_16x16x32_bf16 v[128:131], a[100:103], a[4:7], v[128:131]\n" \
"s_add_u32 m0, %[m0b], 98304\n" \
"s_cmp_gt_u32 s48, 0\n" \
"v_mfma_f32_16x16x32_bf16 v[128:131], a[104:107], a[8:11], v[128:131]\n" \
"v_exp_f32_e32 v157, v157\n" \
"v_mfma_f32_16x16x32_bf16 v[128:131], a[108:111], a[12:15], v[128:131]\n" \
"v_cvt_pk_bf16_f32 v220, v152, v153\n" \
"v_cvt_pk_bf16_f32 v221, v154, v155\n" \
"v_mfma_f32_16x16x32_bf16 v[132:135], a[124:127], a[12:15], v[160:163]\n" \
"v_exp_f32_e64 v158, v158\n" \
"v_mfma_f32_16x16x32_bf16 v[132:135], a[120:123], a[8:11], v[132:135]\n" \
"s_cselect_b32 s49, 0, 1\n" \
"s_lshl_b32 s49, s49, 18\n" \
"s_or_b32 s49, s49, s59\n" \
"v_max3_f32 v232, v97, v102, v107\n" \
"s_waitcnt lgkmcnt(0)\n" \
"v_mfma_f32_16x16x32_bf16 v[132:135], a[116:119], a[4:7], v[132:135]\n" \
"buffer_load_dwordx4 %[wo0], %[srdt], s49 offen offset:0 lds\n" \
"v_mfma_f32_16x16x32_bf16 v[132:135], a[112:115], a[0:3], v[132:135]\n" \
"v_exp_f32_e64 v159, v159\n" \
"v_mfma_f32_16x16x32_bf16 v[32:35], a[160:163], v[196:199], v[32:35]\n" \
"ds_read_b64_tr_b16 a[196:197], v215 offset:0\n" \
"v_min3_f32 v233, v97, v102, v107\n" \
"v_mfma_f32_16x16x32_bf16 v[32:35], a[128:131], v[188:191], v[32:35]\n" \
"v_exp_f32_e64 v128, v128\n" \
"v_mfma_f32_16x16x32_bf16 v[36:39], a[164:167], v[196:199], v[36:39]\n" \
"v_cvt_pk_bf16_f32 v222, v156, v157\n" \
"v_cvt_pk_bf16_f32 v223, v158, v159\n" \
"v_mfma_f32_16x16x32_bf16 v[36:39], a[132:135], v[188:191], v[36:39]\n" \
"v_exp_f32_e64 v129, v129\n" \
"v_mfma_f32_16x16x32_bf16 v[40:43], a[168:171], v[196:199], v[40:43]\n" \
"ds_read_b64_tr_b16 a[198:199], v215 offset:2048\n" \
"v_max3_f32 v232, v232, v112, v117\n" \
"v_mfma_f32_16x16x32_bf16 v[40:43], a[136:139], v[188:191], v[40:43]\n" \
"v_exp_f32_e64 v130, v130\n" \
"v_mfma_f32_16x16x32_bf16 v[44:47], a[172:175], v[196:199], v[44:47]\n" \
"ds_read_b64_tr_b16 a[200:201], v215 offset:4096\n" \
"v_min3_f32 v233, v233, v112, v117\n" \
"v_mfma_f32_16x16x32_bf16 v[44:47], a[140:143], v[188:191], v[44:47]\n" \
"v_exp_f32_e64 v131, v131\n" \
"v_smfmac_f32_16x16x64_bf16 v[176:179], a[192:195], v[184:191], v228\n" \
"v_sub_f32_e32 v232, v232, v233\n" \
"v_min_f32_e32 v234, v234, v232\n" \
"v_mfma_f32_16x16x32_bf16 v[136:139], a[96:99], a[16:19], v[164:167]\n" \
"v_exp_f32_e64 v132, v132\n" \
"v_mfma_f32_16x16x32_bf16 v[136:139], a[100:103], a[20:23], v[136:139]\n" \
"ds_read_b64_tr_b16 a[202:203], v215 offset:6144\n" \
"v_max3_f32 v232, v1, v6, v11\n" \
"v_mfma_f32_16x16x32_bf16 v[136:139], a[104:107], a[24:27], v[136:139]\n" \
"v_exp_f32_e64 v133, v133\n" \
"v_mfma_f32_16x16x32_bf16 v[136:139], a[108:111], a[28:31], v[136:139]\n" \
"v_cvt_pk_bf16_f32 v192, v128, v129\n" \
"v_cvt_pk_bf16_f32 v193, v130, v131\n" \
"v_mfma_f32_16x16x32_bf16 v[140:143], a[124:127], a[28:31], v[164:167]\n" \
"v_exp_f32_e64 v134, v134\n" \
"v_mfma_f32_16x16x32_bf16 v[140:143], a[120:123], a[24:27], v[140:143]\n" \
"ds_read_b64_tr_b16 a[204:205], v215 offset:8192\n" \
"v_min3_f32 v233, v1, v6, v11\n" \
"v_mfma_f32_16x16x32_bf16 v[140:143], a[116:119], a[20:23], v[140:143]\n" \
"buffer_load_dwordx4 v212, %[srdt], s49 offen offset:1024 lds\n" \
"v_max3_f32 v232, v232, v16, v21\n" \
"v_mfma_f32_16x16x32_bf16 v[140:143], a[112:115], a[16:19], v[140:143]\n" \
"v_exp_f32_e64 v135, v135\n" \
"v_mfma_f32_16x16x32_bf16 v[48:51], a[176:179], v[196:199], v[48:51]\n" \
"ds_read_b64_tr_b16 a[206:207], v215 offset:10240\n" \
"v_add_u32_e32 v208, 0x1000, %[oq0]\n" \
"v_mfma_f32_16x16x32_bf16 v[48:51], a[144:147], v[188:191], v[48:51]\n" \
"v_exp_f32_e64 v136, v136\n" \
"v_mfma_f32_16x16x32_bf16 v[52:55], a[180:183], v[196:199], v[52:55]\n" \
"v_cvt_pk_bf16_f32 v194, v132, v133\n" \
"v_cvt_pk_bf16_f32 v195, v134, v135\n" \
"v_mfma_f32_16x16x32_bf16 v[52:55], a[148:151], v[188:191], v[52:55]\n" \
"v_exp_f32_e64 v137, v137\n" \
"v_mfma_f32_16x16x32_bf16 v[56:59], a[184:187], v[196:199], v[56:59]\n" \
"v_exp_f32_e64 v138, v138\n" \
"v_mfma_f32_16x16x32_bf16 v[56:59], a[152:155], v[188:191], v[56:59]\n" \
"ds_read_b64_tr_b16 a[208:209], v215 offset:12288\n" \
"v_add_u32_e32 v209, 0x4000, %[oq0]\n" \
"v_mfma_f32_16x16x32_bf16 v[60:63], a[188:191], v[196:199], v[60:63]\n" \
"v_exp_f32_e64 v139, v139\n" \
"v_mfma_f32_16x16x32_bf16 v[60:63], a[156:159], v[188:191], v[60:63]\n" \
"v_add_u32_e32 v210, 0x5000, %[oq0]\n" \
"v_mfma_f32_16x16x32_bf16 v[144:147], a[96:99], a[32:35], v[168:171]\n" \
"v_exp_f32_e64 v140, v140\n" \
"v_mfma_f32_16x16x32_bf16 v[144:147], a[100:103], a[36:39], v[144:147]\n" \
"ds_read_b64_tr_b16 a[210:211], v215 offset:14336\n" \
"v_min3_f32 v233, v233, v16, v21\n" \
"v_mfma_f32_16x16x32_bf16 v[144:147], a[104:107], a[40:43], v[144:147]\n" \
"v_exp_f32_e64 v141, v141\n" \
"v_mfma_f32_16x16x32_bf16 v[144:147], a[108:111], a[44:47], v[144:147]\n" \
"v_cvt_pk_bf16_f32 v196, v136, v137\n" \
"v_cvt_pk_bf16_f32 v197, v138, v139\n" \
"v_mfma_f32_16x16x32_bf16 v[148:151], a[124:127], a[44:47], v[168:171]\n" \
"v_exp_f32_e64 v142, v142\n" \
"v_mfma_f32_16x16x32_bf16 v[148:151], a[120:123], a[40:43], v[148:151]\n" \
"v_sub_f32_e32 v232, v232, v233\n" \
"v_min_f32_e32 v234, v234, v232\n" \
"v_mfma_f32_16x16x32_bf16 v[148:151], a[116:119], a[36:39], v[148:151]\n" \
"buffer_load_dwordx4 v213, %[srdt], s49 offen offset:2048 lds\n" \
"v_max3_f32 v232, v65, v70, v75\n" \
"v_mfma_f32_16x16x32_bf16 v[148:151], a[112:115], a[32:35], v[148:151]\n" \
"v_exp_f32_e64 v143, v143\n" \
"v_mfma_f32_16x16x32_bf16 v[96:99], a[160:163], v[204:207], v[96:99]\n" \
"ds_read_b64_tr_b16 a[160:161], %[vvb] offset:57344\n" \
"v_min3_f32 v233, v65, v70, v75\n" \
"v_mfma_f32_16x16x32_bf16 v[96:99], a[128:131], v[220:223], v[96:99]\n" \
"v_exp_f32_e64 v144, v144\n" \
"v_mfma_f32_16x16x32_bf16 v[100:103], a[164:167], v[204:207], v[100:103]\n" \
"v_cvt_pk_bf16_f32 v198, v140, v141\n" \
"v_cvt_pk_bf16_f32 v199, v142, v143\n" \
"v_mfma_f32_16x16x32_bf16 v[100:103], a[132:135], v[220:223], v[100:103]\n" \
"v_exp_f32_e64 v145, v145\n" \
"v_mfma_f32_16x16x32_bf16 v[104:107], a[168:171], v[204:207], v[104:107]\n" \
"ds_read_b64_tr_b16 a[162:163], %[vvb] offset:61440\n" \
"ds_read_b64_tr_b16 a[164:165], v211 offset:57344\n" \
"v_mfma_f32_16x16x32_bf16 v[104:107], a[136:139], v[220:223], v[104:107]\n" \
"v_exp_f32_e64 v146, v146\n" \
"v_mfma_f32_16x16x32_bf16 v[108:111], a[172:175], v[204:207], v[108:111]\n" \
"ds_read_b64_tr_b16 a[166:167], v211 offset:61440\n" \
"v_max3_f32 v232, v232, v80, v85\n" \
"v_mfma_f32_16x16x32_bf16 v[108:111], a[140:143], v[220:223], v[108:111]\n" \
"v_exp_f32_e64 v147, v147\n" \
"v_smfmac_f32_16x16x64_bf16 v[180:183], a[192:195], v[216:223], v228\n" \
"ds_read_b64_tr_b16 a[168:169], %[vvb] offset:57856\n" \
"v_min3_f32 v233, v233, v80, v85\n" \
"v_mfma_f32_16x16x32_bf16 v[152:155], a[96:99], a[48:51], v[172:175]\n" \
"v_exp_f32_e64 v148, v148\n" \
"v_mfma_f32_16x16x32_bf16 v[152:155], a[100:103], a[52:55], v[152:155]\n" \
"ds_read_b64_tr_b16 a[170:171], %[vvb] offset:61952\n" \
"v_sub_f32_e64 v232, v232, v233\n" \
"v_mfma_f32_16x16x32_bf16 v[152:155], a[104:107], a[56:59], v[152:155]\n" \
"v_exp_f32_e64 v149, v149\n" \
"v_mfma_f32_16x16x32_bf16 v[152:155], a[108:111], a[60:63], v[152:155]\n" \
"v_cvt_pk_bf16_f32 v200, v144, v145\n" \
"v_cvt_pk_bf16_f32 v201, v146, v147\n" \
"v_mfma_f32_16x16x32_bf16 v[156:159], a[124:127], a[60:63], v[172:175]\n" \
"v_exp_f32_e64 v150, v150\n" \
"v_mfma_f32_16x16x32_bf16 v[156:159], a[120:123], a[56:59], v[156:159]\n" \
"ds_read_b64_tr_b16 a[172:173], v211 offset:57856\n" \
"v_min_f32_e64 v234, v234, v232\n" \
"v_mfma_f32_16x16x32_bf16 v[156:159], a[116:119], a[52:55], v[156:159]\n" \
"buffer_load_dwordx4 v214, %[srdt], s49 offen offset:3072 lds\n" \
"v_accvgpr_read_b32 v233, a228\n" \
"v_mul_f32_e32 v233, 0x3a000000, v233\n" \
"v_mfma_f32_16x16x32_bf16 v[156:159], a[112:115], a[48:51], v[156:159]\n" \
"v_exp_f32_e64 v151, v151\n" \
"v_mfma_f32_16x16x32_bf16 v[112:115], a[176:179], v[204:207], v[112:115]\n" \
"ds_read_b64_tr_b16 a[174:175], v211 offset:61952\n" \
"ds_read_b64_tr_b16 a[176:177], %[vvb] offset:58368\n" \
"v_mfma_f32_16x16x32_bf16 v[112:115], a[144:147], v[220:223], v[112:115]\n" \
"v_exp_f32_e64 v152, v152\n" \
"v_mfma_f32_16x16x32_bf16 v[116:119], a[180:183], v[204:207], v[116:119]\n" \
"v_cvt_pk_bf16_f32 v202, v148, v149\n" \
"v_cvt_pk_bf16_f32 v203, v150, v151\n" \
"v_mfma_f32_16x16x32_bf16 v[116:119], a[148:151], v[220:223], v[116:119]\n" \
"v_exp_f32_e64 v153, v153\n" \
"v_mfma_f32_16x16x32_bf16 v[120:123], a[184:187], v[204:207], v[120:123]\n" \
"v_exp_f32_e64 v154, v154\n" \
"v_mfma_f32_16x16x32_bf16 v[120:123], a[152:155], v[220:223], v[120:123]\n" \
"ds_read_b64_tr_b16 a[178:179], %[vvb] offset:62464\n" \
"s_waitcnt vmcnt(5)\n" \
"v_cmp_eq_u32_e64 s[56:57], 0, v229\n" \
"s_cmp_lg_u64 s[56:57], 0\n" \
"s_cbranch_scc1 Lspk_%=\n" \
"Lbkk_%=:\n" \
"v_mfma_f32_16x16x32_bf16 v[124:127], a[188:191], v[204:207], v[124:127]\n" \
"v_exp_f32_e32 v155, v155\n" \
"v_mfma_f32_16x16x32_bf16 v[124:127], a[156:159], v[220:223], v[124:127]\n" \
"ds_read_b64_tr_b16 a[180:181], v211 offset:58368\n" \
"ds_read_b64_tr_b16 a[182:183], v211 offset:62464\n" \
"ds_read_b64_tr_b16 a[184:185], %[vvb] offset:58880\n" \
"ds_read_b64_tr_b16 a[186:187], %[vvb] offset:62976\n" \
"ds_read_b64_tr_b16 a[188:189], v211 offset:58880\n" \
"ds_read_b64_tr_b16 a[190:191], v211 offset:62976\n" \
"s_waitcnt lgkmcnt(8)\n" \
"s_nop 0\n" \
"v_smfmac_f32_16x16x64_bf16 v[176:179], a[192:195], v[192:199], v228\n" \
"v_exp_f32_e64 v156, v156\n" \
"ds_read_b64_tr_b16 a[212:213], v227 offset:0\n" \
"v_mfma_f32_16x16x32_bf16 v[0:3], a[160:163], v[192:195], v[0:3]\n" \
"v_exp_f32_e64 v157, v157\n" \
"ds_read_b64_tr_b16 a[214:215], v227 offset:2048\n" \
"v_mfma_f32_16x16x32_bf16 v[4:7], a[164:167], v[192:195], v[4:7]\n" \
"v_cvt_pk_bf16_f32 v204, v152, v153\n" \
"ds_read_b64_tr_b16 a[216:217], v227 offset:4096\n" \
"v_mfma_f32_16x16x32_bf16 v[8:11], a[168:171], v[192:195], v[8:11]\n" \
"v_cvt_pk_bf16_f32 v205, v154, v155\n" \
"ds_read_b64_tr_b16 a[218:219], v227 offset:6144\n" \
"v_mfma_f32_16x16x32_bf16 v[12:15], a[172:175], v[192:195], v[12:15]\n" \
"v_exp_f32_e64 v158, v158\n" \
"ds_read_b64_tr_b16 a[220:221], v227 offset:8192\n" \
"s_waitcnt lgkmcnt(5)\n" \
"s_nop 0\n" \
"v_mfma_f32_16x16x32_bf16 v[16:19], a[176:179], v[192:195], v[16:19]\n" \
"v_exp_f32_e64 v159, v159\n" \
"ds_read_b64_tr_b16 a[222:223], v227 offset:10240\n" \
"v_mfma_f32_16x16x32_bf16 v[20:23], a[180:183], v[192:195], v[20:23]\n" \
"v_cvt_pk_bf16_f32 v206, v156, v157\n" \
"ds_read_b64_tr_b16 a[224:225], v227 offset:12288\n" \
"v_accvgpr_read_b32 v229, a228\n" \
"v_mfma_f32_16x16x32_bf16 v[24:27], a[184:187], v[192:195], v[24:27]\n" \
"v_cvt_pk_bf16_f32 v207, v158, v159\n" \
"ds_read_b64_tr_b16 a[226:227], v227 offset:14336\n" \
"v_mfma_f32_16x16x32_bf16 v[28:31], a[188:191], v[192:195], v[28:31]\n" \
"v_add_f32_e64 v176, v176, v177\n" \
"v_smfmac_f32_16x16x64_bf16 v[180:183], a[192:195], v[200:207], v228\n" \
"v_add_f32_e32 v178, v178, v179\n" \
"v_add_f32_e32 v231, v176, v178\n" \
"v_rcp_f32_e32 v176, v176\n" \
"v_rcp_f32_e32 v178, v178\n" \
"v_mul_f32_e64 v177, -v229, v176\n" \
"v_mul_f32_e64 v179, -v229, v178\n" \
"v_mfma_f32_16x16x32_bf16 v[32:35], a[160:163], v[196:199], v[32:35]\n" \
"v_pk_fma_f32 v[0:1], v[0:1], v[176:177], v[176:177] op_sel:[0,0,1] op_sel_hi:[1,0,1]\n" \
"v_pk_fma_f32 v[2:3], v[2:3], v[176:177], v[176:177] op_sel:[0,0,1] op_sel_hi:[1,0,1]\n" \
"v_pk_fma_f32 v[4:5], v[4:5], v[176:177], v[176:177] op_sel:[0,0,1] op_sel_hi:[1,0,1]\n" \
"v_pk_fma_f32 v[6:7], v[6:7], v[176:177], v[176:177] op_sel:[0,0,1] op_sel_hi:[1,0,1]\n" \
"v_cvt_pk_bf16_f32 v0, v0, v1\n" \
"v_cvt_pk_bf16_f32 v1, v2, v3\n" \
"v_cvt_pk_bf16_f32 v2, v4, v5\n" \
"v_cvt_pk_bf16_f32 v3, v6, v7\n" \
"v_mfma_f32_16x16x32_bf16 v[36:39], a[164:167], v[196:199], v[36:39]\n" \
"s_mov_b32 s55, 0x4e800000\n" \
"v_mov_b32_e32 v236, %[flag]\n" \
"v_add_f32_e32 v180, v180, v181\n" \
"v_add_f32_e32 v182, v182, v183\n" \
"v_add_f32_e32 v231, v231, v180\n" \
"v_add_f32_e32 v231, v231, v182\n" \
"v_rcp_f32_e32 v180, v180\n" \
"v_cmp_nle_f32_e64 s[56:57], v231, s55\n" \
"v_mfma_f32_16x16x32_bf16 v[40:43], a[168:171], v[196:199], v[40:43]\n" \
"v_rcp_f32_e64 v182, v182\n" \
"v_cmp_nle_f32_e64 s[44:45], v233, v234\n" \
"v_mul_f32_e64 v181, -v229, v180\n" \
"v_mul_f32_e64 v183, -v229, v182\n" \
"s_nop 1\n" \
"s_or_b64 s[56:57], s[56:57], s[44:45]\n" \
"s_cselect_b32 s55, 1, 0\n" \
"v_mov_b32_e32 v230, s55\n" \
"v_mfma_f32_16x16x32_bf16 v[44:47], a[172:175], v[196:199], v[44:47]\n" \
"s_mov_b32 s69, s55\n" \
"s_andn2_b32 s55, s55, s52\n" \
"s_cselect_b64 vcc, 0, vcc\n" \
"s_nop 0\n" \
"ds_write_b32 v236, v230\n" \
"v_and_b32_e32 v236, -16, v236\n" \
"s_cbranch_vccz Lg37_%=\n" \
"buffer_store_dwordx4 v[0:3], %[oq0], %[srdz], s60 offen offset:0 sc1\n" \
"Lg37_%=:\n" \
"v_pk_fma_f32 v[8:9], v[8:9], v[176:177], v[176:177] op_sel:[0,0,1] op_sel_hi:[1,0,1]\n" \
"v_pk_fma_f32 v[10:11], v[10:11], v[176:177], v[176:177] op_sel:[0,0,1] op_sel_hi:[1,0,1]\n" \
"v_mfma_f32_16x16x32_bf16 v[48:51], a[176:179], v[196:199], v[48:51]\n" \
"v_pk_fma_f32 v[12:13], v[12:13], v[176:177], v[176:177] op_sel:[0,0,1] op_sel_hi:[1,0,1]\n" \
"v_pk_fma_f32 v[14:15], v[14:15], v[176:177], v[176:177] op_sel:[0,0,1] op_sel_hi:[1,0,1]\n" \
"v_cvt_pk_bf16_f32 v8, v8, v9\n" \
"v_cvt_pk_bf16_f32 v9, v10, v11\n" \
"v_cvt_pk_bf16_f32 v10, v12, v13\n" \
"v_cvt_pk_bf16_f32 v11, v14, v15\n" \
"s_cbranch_vccz Lg38_%=\n" \
"s_nop 0\n" \
"buffer_store_dwordx4 v[8:11], %[oq0], %[srdz], s60 offen offset:1024 sc1\n" \
"Lg38_%=:\n" \
"v_pk_fma_f32 v[16:17], v[16:17], v[176:177], v[176:177] op_sel:[0,0,1] op_sel_hi:[1,0,1]\n" \
"v_mfma_f32_16x16x32_bf16 v[52:55], a[180:183], v[196:199], v[52:55]\n" \
"v_pk_fma_f32 v[18:19], v[18:19], v[176:177], v[176:177] op_sel:[0,0,1] op_sel_hi:[1,0,1]\n" \
"v_pk_fma_f32 v[20:21], v[20:21], v[176:177], v[176:177] op_sel:[0,0,1] op_sel_hi:[1,0,1]\n" \
"v_pk_fma_f32 v[22:23], v[22:23], v[176:177], v[176:177] op_sel:[0,0,1] op_sel_hi:[1,0,1]\n" \
"v_cvt_pk_bf16_f32 v16, v16, v17\n" \
"v_cvt_pk_bf16_f32 v17, v18, v19\n" \
"v_cvt_pk_bf16_f32 v18, v20, v21\n" \
"v_cvt_pk_bf16_f32 v19, v22, v23\n" \
"s_cbranch_vccz Lg39_%=\n" \
"s_nop 0\n" \
"buffer_store_dwordx4 v[16:19], %[oq0], %[srdz], s60 offen offset:2048 sc1\n" \
"Lg39_%=:\n" \
"v_mfma_f32_16x16x32_bf16 v[56:59], a[184:187], v[196:199], v[56:59]\n" \
"v_pk_fma_f32 v[24:25], v[24:25], v[176:177], v[176:177] op_sel:[0,0,1] op_sel_hi:[1,0,1]\n" \
"v_pk_fma_f32 v[26:27], v[26:27], v[176:177], v[176:177] op_sel:[0,0,1] op_sel_hi:[1,0,1]\n" \
"v_pk_fma_f32 v[28:29], v[28:29], v[176:177], v[176:177] op_sel:[0,0,1] op_sel_hi:[1,0,1]\n" \
"v_pk_fma_f32 v[30:31], v[30:31], v[176:177], v[176:177] op_sel:[0,0,1] op_sel_hi:[1,0,1]\n" \
"v_cvt_pk_bf16_f32 v24, v24, v25\n" \
"v_cvt_pk_bf16_f32 v25, v26, v27\n" \
"v_cvt_pk_bf16_f32 v26, v28, v29\n" \
"v_cvt_pk_bf16_f32 v27, v30, v31\n" \
"v_mfma_f32_16x16x32_bf16 v[60:63], a[188:191], v[196:199], v[60:63]\n" \
"s_cbranch_vccz Lg40_%=\n" \
"s_nop 0\n" \
"buffer_store_dwordx4 v[24:27], %[oq0], %[srdz], s60 offen offset:3072 sc1\n" \
"Lg40_%=:\n" \
"v_mfma_f32_16x16x32_bf16 v[64:67], a[128:131], v[216:219], v[64:67]\n" \
"v_fma_f32 v32, v32, v178, v179\n" \
"v_fma_f32 v33, v33, v178, v179\n" \
"v_fma_f32 v34, v34, v178, v179\n" \
"v_fma_f32 v35, v35, v178, v179\n" \
"v_fma_f32 v36, v36, v178, v179\n" \
"v_fma_f32 v37, v37, v178, v179\n" \
"v_mfma_f32_16x16x32_bf16 v[64:67], a[160:163], v[200:203], v[64:67]\n" \
"v_fma_f32 v38, v38, v178, v179\n" \
"v_fma_f32 v39, v39, v178, v179\n" \
"v_cvt_pk_bf16_f32 v32, v32, v33\n" \
"v_cvt_pk_bf16_f32 v33, v34, v35\n" \
"v_mfma_f32_16x16x32_bf16 v[68:71], a[132:135], v[216:219], v[68:71]\n" \
"v_cvt_pk_bf16_f32 v34, v36, v37\n" \
"v_cvt_pk_bf16_f32 v35, v38, v39\n" \
"s_cbranch_vccz Lg41_%=\n" \
"s_nop 0\n" \
"buffer_store_dwordx4 v[32:35], v208, %[srdz], s60 offen offset:0 sc1\n" \
"Lg41_%=:\n" \
"v_mfma_f32_16x16x32_bf16 v[68:71], a[164:167], v[200:203], v[68:71]\n" \
"v_fma_f32 v40, v40, v178, v179\n" \
"v_fma_f32 v41, v41, v178, v179\n" \
"v_fma_f32 v42, v42, v178, v179\n" \
"v_fma_f32 v43, v43, v178, v179\n" \
"v_fma_f32 v44, v44, v178, v179\n" \
"v_fma_f32 v45, v45, v178, v179\n" \
"v_mfma_f32_16x16x32_bf16 v[72:75], a[136:139], v[216:219], v[72:75]\n" \
"v_fma_f32 v46, v46, v178, v179\n" \
"v_fma_f32 v47, v47, v178, v179\n" \
"v_cvt_pk_bf16_f32 v40, v40, v41\n" \
"v_cvt_pk_bf16_f32 v41, v42, v43\n" \
"v_mfma_f32_16x16x32_bf16 v[72:75], a[168:171], v[200:203], v[72:75]\n" \
"v_cvt_pk_bf16_f32 v42, v44, v45\n" \
"v_cvt_pk_bf16_f32 v43, v46, v47\n" \
"s_cbranch_vccz Lg42_%=\n" \
"s_nop 0\n" \
"buffer_store_dwordx4 v[40:43], v208, %[srdz], s60 offen offset:1024 sc1\n" \
"Lg42_%=:\n" \
"v_mfma_f32_16x16x32_bf16 v[76:79], a[140:143], v[216:219], v[76:79]\n" \
"v_fma_f32 v48, v48, v178, v179\n" \
"v_fma_f32 v49, v49, v178, v179\n" \
"v_fma_f32 v50, v50, v178, v179\n" \
"v_fma_f32 v51, v51, v178, v179\n" \
"v_fma_f32 v52, v52, v178, v179\n" \
"v_fma_f32 v53, v53, v178, v179\n" \
"v_mfma_f32_16x16x32_bf16 v[76:79], a[172:175], v[200:203], v[76:79]\n" \
"v_fma_f32 v54, v54, v178, v179\n" \
"v_fma_f32 v55, v55, v178, v179\n" \
"v_cvt_pk_bf16_f32 v48, v48, v49\n" \
"v_cvt_pk_bf16_f32 v49, v50, v51\n" \
"v_mfma_f32_16x16x32_bf16 v[80:83], a[144:147], v[216:219], v[80:83]\n" \
"v_cvt_pk_bf16_f32 v50, v52, v53\n" \
"v_cvt_pk_bf16_f32 v51, v54, v55\n" \
"s_cbranch_vccz Lg43_%=\n" \
"s_nop 0\n" \
"buffer_store_dwordx4 v[48:51], v208, %[srdz], s60 offen offset:2048 sc1\n" \
"Lg43_%=:\n" \
"v_mfma_f32_16x16x32_bf16 v[80:83], a[176:179], v[200:203], v[80:83]\n" \
"v_fma_f32 v56, v56, v178, v179\n" \
"v_fma_f32 v57, v57, v178, v179\n" \
"v_fma_f32 v58, v58, v178, v179\n" \
"v_fma_f32 v59, v59, v178, v179\n" \
"v_fma_f32 v60, v60, v178, v179\n" \
"v_fma_f32 v61, v61, v178, v179\n" \
"v_mfma_f32_16x16x32_bf16 v[84:87], a[148:151], v[216:219], v[84:87]\n" \
"v_fma_f32 v62, v62, v178, v179\n" \
"v_fma_f32 v63, v63, v178, v179\n" \
"v_cvt_pk_bf16_f32 v56, v56, v57\n" \
"v_cvt_pk_bf16_f32 v57, v58, v59\n" \
"v_mfma_f32_16x16x32_bf16 v[84:87], a[180:183], v[200:203], v[84:87]\n" \
"v_cvt_pk_bf16_f32 v58, v60, v61\n" \
"v_cvt_pk_bf16_f32 v59, v62, v63\n" \
"s_cbranch_vccz Lg44_%=\n" \
"s_nop 0\n" \
"buffer_store_dwordx4 v[56:59], v208, %[srdz], s60 offen offset:3072 sc1\n" \
"Lg44_%=:\n" \
"v_mfma_f32_16x16x32_bf16 v[88:91], a[152:155], v[216:219], v[88:91]\n" \
"v_mfma_f32_16x16x32_bf16 v[88:91], a[184:187], v[200:203], v[88:91]\n" \
"v_mfma_f32_16x16x32_bf16 v[92:95], a[156:159], v[216:219], v[92:95]\n" \
"v_mfma_f32_16x16x32_bf16 v[92:95], a[188:191], v[200:203], v[92:95]\n" \
"s_waitcnt vmcnt(12) lgkmcnt(0)\n" \
"s_bitcmp1_b32 0xf, s48\n" \
"s_cbranch_scc1 Lcw1_%=\n" \
"s_waitcnt vmcnt(4)\n" \
"Lcw1_%=:\n" \
"s_barrier\n" \
"s_nop 0\n" \
"ds_read_b128 v[232:235], v236\n" \
"v_mfma_f32_16x16x32_bf16 v[96:99], a[160:163], v[204:207], v[96:99]\n" \
"v_pk_fma_f32 v[64:65], v[64:65], v[180:181], v[180:181] op_sel:[0,0,1] op_sel_hi:[1,0,1]\n" \
"v_pk_fma_f32 v[66:67], v[66:67], v[180:181], v[180:181] op_sel:[0,0,1] op_sel_hi:[1,0,1]\n" \
"v_pk_fma_f32 v[68:69], v[68:69], v[180:181], v[180:181] op_sel:[0,0,1] op_sel_hi:[1,0,1]\n" \
"v_pk_fma_f32 v[70:71], v[70:71], v[180:181], v[180:181] op_sel:[0,0,1] op_sel_hi:[1,0,1]\n" \
"v_cvt_pk_bf16_f32 v64, v64, v65\n" \
"v_mfma_f32_16x16x32_bf16 v[100:103], a[164:167], v[204:207], v[100:103]\n" \
"v_cvt_pk_bf16_f32 v65, v66, v67\n" \
"v_cvt_pk_bf16_f32 v66, v68, v69\n" \
"v_cvt_pk_bf16_f32 v67, v70, v71\n" \
"s_cbranch_vccz Lg45_%=\n" \
"s_nop 0\n" \
"buffer_store_dwordx4 v[64:67], v209, %[srdz], s60 offen offset:0 sc1\n" \
"Lg45_%=:\n" \
"v_pk_fma_f32 v[72:73], v[72:73], v[180:181], v[180:181] op_sel:[0,0,1] op_sel_hi:[1,0,1]\n" \
"v_mfma_f32_16x16x32_bf16 v[104:107], a[168:171], v[204:207], v[104:107]\n" \
"v_pk_fma_f32 v[74:75], v[74:75], v[180:181], v[180:181] op_sel:[0,0,1] op_sel_hi:[1,0,1]\n" \
"v_pk_fma_f32 v[76:77], v[76:77], v[180:181], v[180:181] op_sel:[0,0,1] op_sel_hi:[1,0,1]\n" \
"v_pk_fma_f32 v[78:79], v[78:79], v[180:181], v[180:181] op_sel:[0,0,1] op_sel_hi:[1,0,1]\n" \
"v_cvt_pk_bf16_f32 v72, v72, v73\n" \
"v_cvt_pk_bf16_f32 v73, v74, v75\n" \
"v_mfma_f32_16x16x32_bf16 v[108:111], a[172:175], v[204:207], v[108:111]\n" \
"v_cvt_pk_bf16_f32 v74, v76, v77\n" \
"v_cvt_pk_bf16_f32 v75, v78, v79\n" \
"s_cbranch_vccz Lg46_%=\n" \
"s_nop 0\n" \
"buffer_store_dwordx4 v[72:75], v209, %[srdz], s60 offen offset:1024 sc1\n" \
"Lg46_%=:\n" \
"v_pk_fma_f32 v[80:81], v[80:81], v[180:181], v[180:181] op_sel:[0,0,1] op_sel_hi:[1,0,1]\n" \
"v_pk_fma_f32 v[82:83], v[82:83], v[180:181], v[180:181] op_sel:[0,0,1] op_sel_hi:[1,0,1]\n" \
"v_mfma_f32_16x16x32_bf16 v[112:115], a[176:179], v[204:207], v[112:115]\n" \
"v_pk_fma_f32 v[84:85], v[84:85], v[180:181], v[180:181] op_sel:[0,0,1] op_sel_hi:[1,0,1]\n" \
"v_pk_fma_f32 v[86:87], v[86:87], v[180:181], v[180:181] op_sel:[0,0,1] op_sel_hi:[1,0,1]\n" \
"v_cvt_pk_bf16_f32 v80, v80, v81\n" \
"v_cvt_pk_bf16_f32 v81, v82, v83\n" \
"v_cvt_pk_bf16_f32 v82, v84, v85\n" \
"v_mfma_f32_16x16x32_bf16 v[116:119], a[180:183], v[204:207], v[116:119]\n" \
"v_cvt_pk_bf16_f32 v83, v86, v87\n" \
"s_cbranch_vccz Lg47_%=\n" \
"s_nop 0\n" \
"buffer_store_dwordx4 v[80:83], v209, %[srdz], s60 offen offset:2048 sc1\n" \
"Lg47_%=:\n" \
"v_pk_fma_f32 v[88:89], v[88:89], v[180:181], v[180:181] op_sel:[0,0,1] op_sel_hi:[1,0,1]\n" \
"v_pk_fma_f32 v[90:91], v[90:91], v[180:181], v[180:181] op_sel:[0,0,1] op_sel_hi:[1,0,1]\n" \
"v_pk_fma_f32 v[92:93], v[92:93], v[180:181], v[180:181] op_sel:[0,0,1] op_sel_hi:[1,0,1]\n" \
"v_mfma_f32_16x16x32_bf16 v[120:123], a[184:187], v[204:207], v[120:123]\n" \
"v_pk_fma_f32 v[94:95], v[94:95], v[180:181], v[180:181] op_sel:[0,0,1] op_sel_hi:[1,0,1]\n" \
"v_cvt_pk_bf16_f32 v88, v88, v89\n" \
".if . - adaln_body != 40960\n" \
".error \"size model\"\n" \
".endif\n" \
"Lk9_%=:\n" \
"s_cbranch_execz Lk10_%=\n" \
"s_nop 0\n" \
"v_cvt_pk_bf16_f32 v89, v90, v91\n" \
"v_cvt_pk_bf16_f32 v90, v92, v93\n" \
"v_cvt_pk_bf16_f32 v91, v94, v95\n" \
"v_mfma_f32_16x16x32_bf16 v[124:127], a[188:191], v[204:207], v[124:127]\n" \
"s_cbranch_vccz Lg48_%=\n" \
"s_nop 0\n" \
"buffer_store_dwordx4 v[88:91], v209, %[srdz], s60 offen offset:3072 sc1\n" \
"Lg48_%=:\n" \
"s_waitcnt lgkmcnt(0)\n" \
"s_nop 0\n" \
"v_or3_b32 v232, v232, v233, v234\n" \
"v_or_b32_e32 v232, v232, v235\n" \
"s_nop 0\n" \
"v_readfirstlane_b32 s55, v232\n" \
"s_nop 3\n" \
"s_andn2_b32 s55, s55, s52\n" \
"s_cbranch_scc1 Lslow_%=\n" \
"v_mfma_f32_16x16x32_bf16 v[192:195], a[196:199], v[0:3], 0\n" \
"v_mfma_f32_16x16x32_bf16 v[192:195], a[200:203], v[8:11], v[192:195]\n" \
"v_fma_f32 v96, v96, v182, v183\n" \
"v_fma_f32 v97, v97, v182, v183\n" \
"v_fma_f32 v98, v98, v182, v183\n" \
"v_fma_f32 v99, v99, v182, v183\n" \
"v_mfma_f32_16x16x32_bf16 v[192:195], a[204:207], v[16:19], v[192:195]\n" \
"v_fma_f32 v100, v100, v182, v183\n" \
"v_fma_f32 v101, v101, v182, v183\n" \
"v_fma_f32 v102, v102, v182, v183\n" \
"v_fma_f32 v103, v103, v182, v183\n" \
"s_add_u32 m0, %[m0b], 114688\n" \
"s_cmp_gt_u32 s48, 1\n" \
"s_cselect_b32 s49, 1, 2\n" \
"s_lshl_b32 s49, s49, 18\n" \
"s_or_b32 s49, s49, s58\n" \
"buffer_load_dwordx4 %[wo0], %[srdt], s49 offen offset:0 lds\n" \
"v_mfma_f32_16x16x32_bf16 v[192:195], a[208:211], v[24:27], v[192:195]\n" \
"v_cvt_pk_bf16_f32 v96, v96, v97\n" \
"v_cvt_pk_bf16_f32 v97, v98, v99\n" \
"v_mfma_f32_16x16x32_bf16 v[144:147], a[212:215], v[0:3], 0\n" \
"v_cvt_pk_bf16_f32 v98, v100, v101\n" \
"v_mfma_f32_16x16x32_bf16 v[144:147], a[216:219], v[8:11], v[144:147]\n" \
"v_cvt_pk_bf16_f32 v99, v102, v103\n" \
"s_cbranch_vccz Lg1_%=\n" \
"s_nop 0\n" \
"buffer_store_dwordx4 v[96:99], v210, %[srdz], s60 offen offset:0 sc1\n" \
"Lg1_%=:\n" \
"v_mfma_f32_16x16x32_bf16 v[144:147], a[220:223], v[16:19], v[144:147]\n" \
"ds_read_b64_tr_b16 a[228:229], v215 offset:512\n" \
"v_fma_f32 v104, v104, v182, v183\n" \
"v_fma_f32 v105, v105, v182, v183\n" \
"v_fma_f32 v106, v106, v182, v183\n" \
"v_fma_f32 v107, v107, v182, v183\n" \
"v_mfma_f32_16x16x32_bf16 v[144:147], a[224:227], v[24:27], v[144:147]\n" \
"ds_read_b64_tr_b16 a[230:231], v215 offset:2560\n" \
"v_fma_f32 v108, v108, v182, v183\n" \
"v_fma_f32 v109, v109, v182, v183\n" \
"v_mfma_f32_16x16x32_bf16 v[196:199], a[196:199], v[32:35], 0\n" \
"ds_read_b64_tr_b16 a[232:233], v215 offset:4608\n" \
"v_fma_f32 v110, v110, v182, v183\n" \
"v_fma_f32 v111, v111, v182, v183\n" \
"v_cvt_pk_bf16_f32 v104, v104, v105\n" \
"v_mfma_f32_16x16x32_bf16 v[196:199], a[200:203], v[40:43], v[196:199]\n" \
"ds_read_b64_tr_b16 a[234:235], v215 offset:6656\n" \
"v_cvt_pk_bf16_f32 v105, v106, v107\n" \
"v_cvt_pk_bf16_f32 v106, v108, v109\n" \
"v_mfma_f32_16x16x32_bf16 v[196:199], a[204:207], v[48:51], v[196:199]\n" \
"v_cvt_pk_bf16_f32 v107, v110, v111\n" \
"s_cbranch_vccz Lg2_%=\n" \
"s_nop 0\n" \
"buffer_store_dwordx4 v[104:107], v210, %[srdz], s60 offen offset:1024 sc1\n" \
"Lg2_%=:\n" \
"v_mfma_f32_16x16x32_bf16 v[196:199], a[208:211], v[56:59], v[196:199]\n" \
"v_fma_f32 v112, v112, v182, v183\n" \
"v_fma_f32 v113, v113, v182, v183\n" \
"v_mfma_f32_16x16x32_bf16 v[148:151], a[212:215], v[32:35], 0\n" \
"v_fma_f32 v114, v114, v182, v183\n" \
"v_fma_f32 v115, v115, v182, v183\n" \
"v_fma_f32 v116, v116, v182, v183\n" \
"v_fma_f32 v117, v117, v182, v183\n" \
"buffer_load_dwordx4 v212, %[srdt], s49 offen offset:1024 lds\n" \
"v_mfma_f32_16x16x32_bf16 v[148:151], a[216:219], v[40:43], v[148:151]\n" \
"v_fma_f32 v118, v118, v182, v183\n" \
"v_fma_f32 v119, v119, v182, v183\n" \
"v_cvt_pk_bf16_f32 v112, v112, v113\n" \
"v_mfma_f32_16x16x32_bf16 v[148:151], a[220:223], v[48:51], v[148:151]\n" \
"ds_read_b64_tr_b16 a[236:237], v215 offset:8704\n" \
"v_cvt_pk_bf16_f32 v113, v114, v115\n" \
"v_mfma_f32_16x16x32_bf16 v[148:151], a[224:227], v[56:59], v[148:151]\n" \
"v_cvt_pk_bf16_f32 v114, v116, v117\n" \
"v_cvt_pk_bf16_f32 v115, v118, v119\n" \
"v_mfma_f32_16x16x32_bf16 v[200:203], a[196:199], v[64:67], 0\n" \
"ds_read_b64_tr_b16 a[238:239], v215 offset:10752\n" \
"s_cbranch_vccz Lg3_%=\n" \
"s_nop 0\n" \
"buffer_store_dwordx4 v[112:115], v210, %[srdz], s60 offen offset:2048 sc1\n" \
"Lg3_%=:\n" \
"v_fma_f32 v120, v120, v182, v183\n" \
"v_fma_f32 v121, v121, v182, v183\n" \
"v_mfma_f32_16x16x32_bf16 v[200:203], a[200:203], v[72:75], v[200:203]\n" \
"v_fma_f32 v122, v122, v182, v183\n" \
"v_fma_f32 v123, v123, v182, v183\n" \
"v_fma_f32 v124, v124, v182, v183\n" \
"v_fma_f32 v125, v125, v182, v183\n" \
"v_mfma_f32_16x16x32_bf16 v[200:203], a[204:207], v[80:83], v[200:203]\n" \
"v_fma_f32 v126, v126, v182, v183\n" \
"v_fma_f32 v127, v127, v182, v183\n" \
"v_mfma_f32_16x16x32_bf16 v[200:203], a[208:211], v[88:91], v[200:203]\n" \
"v_cvt_pk_bf16_f32 v120, v120, v121\n" \
"v_cvt_pk_bf16_f32 v121, v122, v123\n" \
"v_mfma_f32_16x16x32_bf16 v[152:155], a[212:215], v[64:67], 0\n" \
"v_cvt_pk_bf16_f32 v122, v124, v125\n" \
"v_cvt_pk_bf16_f32 v123, v126, v127\n" \
"v_mfma_f32_16x16x32_bf16 v[152:155], a[216:219], v[72:75], v[152:155]\n" \
"s_cbranch_vccz Lg4_%=\n" \
"s_nop 0\n" \
"buffer_store_dwordx4 v[120:123], v210, %[srdz], s60 offen offset:3072 sc1\n" \
"Lg4_%=:\n" \
"v_mfma_f32_16x16x32_bf16 v[152:155], a[220:223], v[80:83], v[152:155]\n" \
"v_add_u32_e32 v220, 0x10000, v215\n" \
"v_mfma_f32_16x16x32_bf16 v[152:155], a[224:227], v[88:91], v[152:155]\n" \
"v_mov_b32_e64 v230, 1\n" \
"v_readlane_b32 s62, %[tlo], 19\n" \
"v_readlane_b32 s63, %[thi], 19\n" \
"v_and_b32_e32 v233, 0x380, v224\n" \
"v_lshlrev_b32_e64 v233, 2, v233\n" \
"buffer_load_dwordx4 v213, %[srdt], s49 offen offset:2048 lds\n" \
"v_mfma_f32_16x16x32_bf16 v[204:207], a[196:199], v[96:99], 0\n" \
"v_add_u32_e32 v221, 0x10000, v227\n" \
"v_mfma_f32_16x16x32_bf16 v[204:207], a[200:203], v[104:107], v[204:207]\n" \
"ds_read_b64_tr_b16 a[196:197], v215 offset:12800\n" \
"v_mfma_f32_16x16x32_bf16 v[204:207], a[204:207], v[112:115], v[204:207]\n" \
"ds_read_b64_tr_b16 a[198:199], v215 offset:14848\n" \
"v_mfma_f32_16x16x32_bf16 v[204:207], a[208:211], v[120:123], v[204:207]\n" \
"ds_read_b64_tr_b16 a[200:201], v227 offset:512\n" \
"v_mfma_f32_16x16x32_bf16 v[156:159], a[212:215], v[96:99], 0\n" \
"ds_read_b64_tr_b16 a[202:203], v227 offset:2560\n" \
"v_mfma_f32_16x16x32_bf16 v[156:159], a[216:219], v[104:107], v[156:159]\n" \
"ds_read_b64_tr_b16 a[204:205], v227 offset:4608\n" \
"v_mfma_f32_16x16x32_bf16 v[156:159], a[220:223], v[112:115], v[156:159]\n" \
"ds_read_b64_tr_b16 a[206:207], v227 offset:6656\n" \
"v_mfma_f32_16x16x32_bf16 v[156:159], a[224:227], v[120:123], v[156:159]\n" \
"s_waitcnt vmcnt(63) lgkmcnt(4)\n" \
"s_nop 0\n" \
"v_mfma_f32_16x16x32_bf16 v[160:163], a[228:231], v[0:3], 0\n" \
"ds_read_b64_tr_b16 a[208:209], v227 offset:8704\n" \
"v_mfma_f32_16x16x32_bf16 v[160:163], a[232:235], v[8:11], v[160:163]\n" \
"ds_read_b64_tr_b16 a[210:211], v227 offset:10752\n" \
"buffer_load_dwordx4 v214, %[srdt], s49 offen offset:3072 lds\n" \
"v_mfma_f32_16x16x32_bf16 v[164:167], a[228:231], v[32:35], 0\n" \
"ds_read_b64_tr_b16 a[212:213], v227 offset:12800\n" \
"v_mfma_f32_16x16x32_bf16 v[164:167], a[232:235], v[40:43], v[164:167]\n" \
"ds_read_b64_tr_b16 a[214:215], v227 offset:14848\n" \
"v_mfma_f32_16x16x32_bf16 v[168:171], a[228:231], v[64:67], 0\n" \
"v_mfma_f32_16x16x32_bf16 v[168:171], a[232:235], v[72:75], v[168:171]\n" \
"v_mfma_f32_16x16x32_bf16 v[172:175], a[228:231], v[96:99], 0\n" \
"ds_read_b64_tr_b16 a[216:217], v215 offset:16384\n" \
"v_mfma_f32_16x16x32_bf16 v[172:175], a[232:235], v[104:107], v[172:175]\n" \
"ds_read_b64_tr_b16 a[218:219], v215 offset:18432\n" \
"v_mfma_f32_16x16x32_bf16 v[160:163], a[236:239], v[16:19], v[160:163]\n" \
"ds_read_b64_tr_b16 a[220:221], v215 offset:20480\n" \
"v_mfma_f32_16x16x32_bf16 v[160:163], a[196:199], v[24:27], v[160:163]\n" \
"ds_read_b64_tr_b16 a[222:223], v215 offset:22528\n" \
"v_mfma_f32_16x16x32_bf16 v[164:167], a[236:239], v[48:51], v[164:167]\n" \
"v_mfma_f32_16x16x32_bf16 v[164:167], a[196:199], v[56:59], v[164:167]\n" \
"v_mfma_f32_16x16x32_bf16 v[168:171], a[236:239], v[80:83], v[168:171]\n" \
"s_waitcnt vmcnt(63) lgkmcnt(0)\n" \
"s_barrier\n" \
"v_mfma_f32_16x16x32_bf16 v[168:171], a[196:199], v[88:91], v[168:171]\n" \
"v_mfma_f32_16x16x32_bf16 v[172:175], a[236:239], v[112:115], v[172:175]\n" \
"ds_read_b64_tr_b16 a[224:225], v215 offset:24576\n" \
"v_mfma_f32_16x16x32_bf16 v[172:175], a[196:199], v[120:123], v[172:175]\n" \
"s_waitcnt vmcnt(63) lgkmcnt(5)\n" \
"s_nop 0\n" \
"v_mfma_f32_16x16x32_bf16 v[176:179], a[200:203], v[0:3], 0\n" \
"ds_read_b64_tr_b16 a[226:227], v215 offset:26624\n" \
"v_mfma_f32_16x16x32_bf16 v[176:179], a[204:207], v[8:11], v[176:179]\n" \
"ds_read_b64_tr_b16 a[228:229], v215 offset:28672\n" \
"s_add_u32 m0, %[m0b], 49152\n" \
"s_cmp_gt_u32 s48, 1\n" \
"s_cselect_b32 s49, 1, 2\n" \
"s_lshl_b32 s49, s49, 18\n" \
"s_or_b32 s49, s49, s59\n" \
"buffer_load_dwordx4 %[wo0], %[srdt], s49 offen offset:0 lds\n" \
"v_mfma_f32_16x16x32_bf16 v[180:183], a[200:203], v[32:35], 0\n" \
"ds_read_b64_tr_b16 a[230:231], v215 offset:30720\n" \
"v_mfma_f32_16x16x32_bf16 v[180:183], a[204:207], v[40:43], v[180:183]\n" \
"v_mfma_f32_16x16x32_bf16 v[184:187], a[200:203], v[64:67], 0\n" \
"v_mfma_f32_16x16x32_bf16 v[184:187], a[204:207], v[72:75], v[184:187]\n" \
"buffer_load_dwordx4 v212, %[srdt], s49 offen offset:1024 lds\n" \
"v_mfma_f32_16x16x32_bf16 v[188:191], a[200:203], v[96:99], 0\n" \
"ds_read_b64_tr_b16 a[232:233], v227 offset:16384\n" \
"v_mfma_f32_16x16x32_bf16 v[188:191], a[204:207], v[104:107], v[188:191]\n" \
"ds_read_b64_tr_b16 a[234:235], v227 offset:18432\n" \
"v_mfma_f32_16x16x32_bf16 v[176:179], a[208:211], v[16:19], v[176:179]\n" \
"ds_read_b64_tr_b16 a[236:237], v227 offset:20480\n" \
"v_mfma_f32_16x16x32_bf16 v[176:179], a[212:215], v[24:27], v[176:179]\n" \
"ds_read_b64_tr_b16 a[238:239], v227 offset:22528\n" \
"v_mfma_f32_16x16x32_bf16 v[180:183], a[208:211], v[48:51], v[180:183]\n" \
"v_mfma_f32_16x16x32_bf16 v[180:183], a[212:215], v[56:59], v[180:183]\n" \
"v_mfma_f32_16x16x32_bf16 v[184:187], a[208:211], v[80:83], v[184:187]\n" \
"v_mfma_f32_16x16x32_bf16 v[184:187], a[212:215], v[88:91], v[184:187]\n" \
"buffer_load_dwordx4 v213, %[srdt], s49 offen offset:2048 lds\n" \
"v_mfma_f32_16x16x32_bf16 v[188:191], a[208:211], v[112:115], v[188:191]\n" \
"ds_read_b64_tr_b16 a[196:197], v227 offset:24576\n" \
"v_mfma_f32_16x16x32_bf16 v[188:191], a[212:215], v[120:123], v[188:191]\n" \
"s_waitcnt vmcnt(63) lgkmcnt(5)\n" \
"s_nop 0\n" \
"v_mfma_f32_16x16x32_bf16 v[4:7], a[216:219], v[0:3], 0\n" \
"ds_read_b64_tr_b16 a[198:199], v227 offset:26624\n" \
"v_mfma_f32_16x16x32_bf16 v[4:7], a[220:223], v[8:11], v[4:7]\n" \
"ds_read_b64_tr_b16 a[200:201], v227 offset:28672\n" \
"v_mfma_f32_16x16x32_bf16 v[36:39], a[216:219], v[32:35], 0\n" \
"ds_read_b64_tr_b16 a[202:203], v227 offset:30720\n" \
"v_mfma_f32_16x16x32_bf16 v[36:39], a[220:223], v[40:43], v[36:39]\n" \
"v_mfma_f32_16x16x32_bf16 v[68:71], a[216:219], v[64:67], 0\n" \
"buffer_load_dwordx4 v214, %[srdt], s49 offen offset:3072 lds\n" \
"v_mfma_f32_16x16x32_bf16 v[68:71], a[220:223], v[72:75], v[68:71]\n" \
"v_mfma_f32_16x16x32_bf16 v[100:103], a[216:219], v[96:99], 0\n" \
"ds_read_b64_tr_b16 a[204:205], v215 offset:16896\n" \
"v_mfma_f32_16x16x32_bf16 v[100:103], a[220:223], v[104:107], v[100:103]\n" \
"ds_read_b64_tr_b16 a[206:207], v215 offset:18944\n" \
"v_mfma_f32_16x16x32_bf16 v[4:7], a[224:227], v[16:19], v[4:7]\n" \
"ds_read_b64_tr_b16 a[208:209], v215 offset:20992\n" \
"v_mfma_f32_16x16x32_bf16 v[4:7], a[228:231], v[24:27], v[4:7]\n" \
"ds_read_b64_tr_b16 a[210:211], v215 offset:23040\n" \
"v_mfma_f32_16x16x32_bf16 v[36:39], a[224:227], v[48:51], v[36:39]\n" \
"v_mfma_f32_16x16x32_bf16 v[36:39], a[228:231], v[56:59], v[36:39]\n" \
"v_mfma_f32_16x16x32_bf16 v[68:71], a[224:227], v[80:83], v[68:71]\n" \
"v_mfma_f32_16x16x32_bf16 v[68:71], a[228:231], v[88:91], v[68:71]\n" \
"v_mfma_f32_16x16x32_bf16 v[100:103], a[224:227], v[112:115], v[100:103]\n" \
"ds_read_b64_tr_b16 a[212:213], v215 offset:25088\n" \
"v_mfma_f32_16x16x32_bf16 v[100:103], a[228:231], v[120:123], v[100:103]\n" \
"s_waitcnt vmcnt(63) lgkmcnt(5)\n" \
"s_nop 0\n" \
"v_mfma_f32_16x16x32_bf16 v[12:15], a[232:235], v[0:3], 0\n" \
"ds_read_b64_tr_b16 a[214:215], v215 offset:27136\n" \
"s_cmp_gt_u32 s48, 0\n" \
"s_cselect_b32 s50, 0, 1\n" \
"s_mul_i32 s50, s50, 262144\n" \
"buffer_load_dwordx4 a[64:67], %[oq0], %[srdz], s50 offen offset:0 sc1\n" \
"v_mfma_f32_16x16x32_bf16 v[12:15], a[236:239], v[8:11], v[12:15]\n" \
"ds_read_b64_tr_b16 a[216:217], v215 offset:29184\n" \
"v_mfma_f32_16x16x32_bf16 v[44:47], a[232:235], v[32:35], 0\n" \
"ds_read_b64_tr_b16 a[218:219], v215 offset:31232\n" \
"buffer_load_dwordx4 a[68:71], %[oq0], %[srdz], s50 offen offset:1024 sc1\n" \
"v_mfma_f32_16x16x32_bf16 v[44:47], a[236:239], v[40:43], v[44:47]\n" \
"v_mfma_f32_16x16x32_bf16 v[76:79], a[232:235], v[64:67], 0\n" \
"buffer_load_dwordx4 a[80:83], v208, %[srdz], s50 offen offset:0 sc1\n" \
"v_mfma_f32_16x16x32_bf16 v[76:79], a[236:239], v[72:75], v[76:79]\n" \
"v_mfma_f32_16x16x32_bf16 v[108:111], a[232:235], v[96:99], 0\n" \
"ds_read_b64_tr_b16 a[220:221], v227 offset:16896\n" \
"buffer_load_dwordx4 a[84:87], v208, %[srdz], s50 offen offset:1024 sc1\n" \
"v_mfma_f32_16x16x32_bf16 v[108:111], a[236:239], v[104:107], v[108:111]\n" \
"ds_read_b64_tr_b16 a[222:223], v227 offset:18944\n" \
"v_mfma_f32_16x16x32_bf16 v[12:15], a[196:199], v[16:19], v[12:15]\n" \
"ds_read_b64_tr_b16 a[224:225], v227 offset:25088\n" \
"buffer_load_dwordx4 a[96:99], v209, %[srdz], s50 offen offset:0 sc1\n" \
"v_mfma_f32_16x16x32_bf16 v[12:15], a[200:203], v[24:27], v[12:15]\n" \
"ds_read_b64_tr_b16 a[226:227], v227 offset:27136\n" \
"v_mfma_f32_16x16x32_bf16 v[44:47], a[196:199], v[48:51], v[44:47]\n" \
"ds_read_b64_tr_b16 a[228:229], v227 offset:20992\n" \
"buffer_load_dwordx4 a[100:103], v209, %[srdz], s50 offen offset:1024 sc1\n" \
"v_mfma_f32_16x16x32_bf16 v[44:47], a[200:203], v[56:59], v[44:47]\n" \
"ds_read_b64_tr_b16 a[230:231], v227 offset:23040\n" \
"v_mfma_f32_16x16x32_bf16 v[76:79], a[196:199], v[80:83], v[76:79]\n" \
"ds_read_b64_tr_b16 a[232:233], v227 offset:29184\n" \
"buffer_load_dwordx4 a[112:115], v210, %[srdz], s50 offen offset:0 sc1\n" \
"v_mfma_f32_16x16x32_bf16 v[76:79], a[200:203], v[88:91], v[76:79]\n" \
"ds_read_b64_tr_b16 a[234:235], v227 offset:31232\n" \
"v_mfma_f32_16x16x32_bf16 v[108:111], a[196:199], v[112:115], v[108:111]\n" \
"ds_read_b64_tr_b16 a[236:237], v215 offset:32768\n" \
"buffer_load_dwordx4 a[116:119], v210, %[srdz], s50 offen offset:1024 sc1\n" \
"v_mfma_f32_16x16x32_bf16 v[108:111], a[200:203], v[120:123], v[108:111]\n" \
"s_waitcnt vmcnt(63) lgkmcnt(13)\n" \
"s_nop 0\n" \
"v_mfma_f32_16x16x32_bf16 v[20:23], a[204:207], v[0:3], 0\n" \
"ds_read_b64_tr_b16 a[238:239], v215 offset:34816\n" \
"buffer_load_dwordx4 a[72:75], %[oq0], %[srdz], s50 offen offset:2048 sc1\n" \
"v_mfma_f32_16x16x32_bf16 v[20:23], a[208:211], v[8:11], v[20:23]\n" \
"ds_read_b64_tr_b16 a[196:197], v215 offset:36864\n" \
"v_mfma_f32_16x16x32_bf16 v[52:55], a[204:207], v[32:35], 0\n" \
"ds_read_b64_tr_b16 a[198:199], v215 offset:38912\n" \
"buffer_load_dwordx4 a[76:79], %[oq0], %[srdz], s50 offen offset:3072 sc1\n" \
"v_mfma_f32_16x16x32_bf16 v[52:55], a[208:211], v[40:43], v[52:55]\n" \
"v_mfma_f32_16x16x32_bf16 v[84:87], a[204:207], v[64:67], 0\n" \
"s_waitcnt vmcnt(63) lgkmcnt(4)\n" \
"s_barrier\n" \
"v_mfma_f32_16x16x32_bf16 v[84:87], a[208:211], v[72:75], v[84:87]\n" \
"buffer_load_dwordx4 a[88:91], v208, %[srdz], s50 offen offset:2048 sc1\n" \
"v_mfma_f32_16x16x32_bf16 v[116:119], a[204:207], v[96:99], 0\n" \
"ds_read_b64_tr_b16 a[200:201], v215 offset:40960\n" \
"v_mfma_f32_16x16x32_bf16 v[116:119], a[208:211], v[104:107], v[116:119]\n" \
"ds_read_b64_tr_b16 a[202:203], v215 offset:43008\n" \
"buffer_load_dwordx4 a[92:95], v208, %[srdz], s50 offen offset:3072 sc1\n" \
"v_mfma_f32_16x16x32_bf16 v[20:23], a[212:215], v[16:19], v[20:23]\n" \
"ds_read_b64_tr_b16 a[204:205], v215 offset:45056\n" \
"v_mfma_f32_16x16x32_bf16 v[20:23], a[216:219], v[24:27], v[20:23]\n" \
"ds_read_b64_tr_b16 a[206:207], v215 offset:47104\n" \
"buffer_load_dwordx4 a[104:107], v209, %[srdz], s50 offen offset:2048 sc1\n" \
"v_mfma_f32_16x16x32_bf16 v[52:55], a[212:215], v[48:51], v[52:55]\n" \
"v_mfma_f32_16x16x32_bf16 v[52:55], a[216:219], v[56:59], v[52:55]\n" \
"buffer_load_dwordx4 a[108:111], v209, %[srdz], s50 offen offset:3072 sc1\n" \
"v_mfma_f32_16x16x32_bf16 v[84:87], a[212:215], v[80:83], v[84:87]\n" \
"v_mfma_f32_16x16x32_bf16 v[84:87], a[216:219], v[88:91], v[84:87]\n" \
"buffer_load_dwordx4 a[120:123], v210, %[srdz], s50 offen offset:2048 sc1\n" \
"v_mfma_f32_16x16x32_bf16 v[116:119], a[212:215], v[112:115], v[116:119]\n" \
"v_mfma_f32_16x16x32_bf16 v[116:119], a[216:219], v[120:123], v[116:119]\n" \
"s_waitcnt vmcnt(63) lgkmcnt(10)\n" \
"s_nop 0\n" \
"v_mfma_f32_16x16x32_bf16 v[28:31], a[220:223], v[0:3], 0\n" \
"buffer_load_dwordx4 a[124:127], v210, %[srdz], s50 offen offset:3072 sc1\n" \
"v_mfma_f32_16x16x32_bf16 v[28:31], a[228:231], v[8:11], v[28:31]\n" \
"s_sub_u32 s61, s48, 4\n" \
"s_cmp_lt_u32 s61, 2\n" \
"s_cselect_b64 vcc, -1, 0\n" \
"s_nop 0\n" \
"v_mfma_f32_16x16x32_bf16 v[60:63], a[220:223], v[32:35], 0\n" \
"s_cbranch_vccz Lg5_%=\n" \
"s_nop 0\n" \
"buffer_store_dwordx4 v[0:3], %[oq0], %[srdz], s60 offen offset:0 sc1\n" \
"Lg5_%=:\n" \
"v_mfma_f32_16x16x32_bf16 v[60:63], a[228:231], v[40:43], v[60:63]\n" \
"v_mfma_f32_16x16x32_bf16 v[92:95], a[220:223], v[64:67], 0\n" \
"s_cmp_gt_u32 s48, 1\n" \
"s_cselect_b32 s50, 1, 2\n" \
"s_mul_i32 s50, s50, 262144\n" \
"buffer_load_dwordx4 a[0:3], %[oq0], %[srdz], s50 offen offset:0 sc1\n" \
"v_mfma_f32_16x16x32_bf16 v[92:95], a[228:231], v[72:75], v[92:95]\n" \
"v_mfma_f32_16x16x32_bf16 v[124:127], a[220:223], v[96:99], 0\n" \
"ds_read_b64_tr_b16 a[208:209], v227 offset:32768\n" \
"v_mfma_f32_16x16x32_bf16 v[124:127], a[228:231], v[104:107], v[124:127]\n" \
"ds_read_b64_tr_b16 a[210:211], v227 offset:34816\n" \
"s_waitcnt vmcnt(15) lgkmcnt(6)\n" \
"s_nop 0\n" \
"v_mfma_f32_16x16x32_bf16 v[128:131], a[236:239], a[64:67], v[192:195]\n" \
"ds_read_b64_tr_b16 a[212:213], v227 offset:36864\n" \
"v_mfma_f32_16x16x32_bf16 v[128:131], a[196:199], a[68:71], v[128:131]\n" \
"ds_read_b64_tr_b16 a[214:215], v227 offset:38912\n" \
"s_waitcnt vmcnt(13) lgkmcnt(15)\n" \
"s_nop 0\n" \
"v_mfma_f32_16x16x32_bf16 v[132:135], a[236:239], a[80:83], v[196:199]\n" \
"s_cbranch_vccz Lg6_%=\n" \
"s_nop 0\n" \
"buffer_store_dwordx4 v[8:11], %[oq0], %[srdz], s60 offen offset:1024 sc1\n" \
"Lg6_%=:\n" \
"v_mfma_f32_16x16x32_bf16 v[132:135], a[196:199], a[84:87], v[132:135]\n" \
"s_waitcnt vmcnt(13) lgkmcnt(15)\n" \
"s_bitcmp1_b32 0x30, s48\n" \
"s_cbranch_scc1 Lcw2_%=\n" \
"s_waitcnt vmcnt(11)\n" \
"Lcw2_%=:\n" \
"v_mfma_f32_16x16x32_bf16 v[136:139], a[236:239], a[96:99], v[200:203]\n" \
"buffer_load_dwordx4 a[4:7], %[oq0], %[srdz], s50 offen offset:1024 sc1\n" \
"v_mfma_f32_16x16x32_bf16 v[136:139], a[196:199], a[100:103], v[136:139]\n" \
"s_waitcnt vmcnt(12) lgkmcnt(15)\n" \
"s_bitcmp1_b32 0x30, s48\n" \
"s_cbranch_scc1 Lcw3_%=\n" \
"s_waitcnt vmcnt(10)\n" \
"Lcw3_%=:\n" \
"v_mfma_f32_16x16x32_bf16 v[140:143], a[236:239], a[112:115], v[204:207]\n" \
"ds_read_b64_tr_b16 a[216:217], v227 offset:40960\n" \
"v_mfma_f32_16x16x32_bf16 v[140:143], a[196:199], a[116:119], v[140:143]\n" \
"s_waitcnt vmcnt(10) lgkmcnt(5)\n" \
"s_bitcmp1_b32 0x30, s48\n" \
"s_cbranch_scc1 Lcw4_%=\n" \
"s_waitcnt vmcnt(8)\n" \
"Lcw4_%=:\n" \
"v_mfma_f32_16x16x32_bf16 v[128:131], a[200:203], a[72:75], v[128:131]\n" \
"ds_read_b64_tr_b16 a[218:219], v227 offset:43008\n" \
"buffer_load_dwordx4 a[8:11], %[oq0], %[srdz], s50 offen offset:2048 sc1\n" \
"v_mfma_f32_16x16x32_bf16 v[128:131], a[204:207], a[76:79], v[128:131]\n" \
"ds_read_b64_tr_b16 a[220:221], v227 offset:45056\n" \
"s_waitcnt vmcnt(9) lgkmcnt(15)\n" \
"s_bitcmp1_b32 0x30, s48\n" \
"s_cbranch_scc1 Lcw5_%=\n" \
"s_waitcnt vmcnt(7)\n" \
"Lcw5_%=:\n" \
"v_mfma_f32_16x16x32_bf16 v[132:135], a[200:203], a[88:91], v[132:135]\n" \
"ds_read_b64_tr_b16 a[222:223], v227 offset:47104\n" \
"v_mfma_f32_16x16x32_bf16 v[132:135], a[204:207], a[92:95], v[132:135]\n" \
"s_cbranch_vccz Lg7_%=\n" \
"s_nop 0\n" \
"buffer_store_dwordx4 v[16:19], %[oq0], %[srdz], s60 offen offset:2048 sc1\n" \
"Lg7_%=:\n" \
"s_waitcnt vmcnt(6) lgkmcnt(14)\n" \
"s_bitcmp1_b32 0x30, s48\n" \
"s_cbranch_scc1 Lcw6_%=\n" \
"s_waitcnt vmcnt(3)\n" \
"Lcw6_%=:\n" \
"v_mfma_f32_16x16x32_bf16 v[136:139], a[200:203], a[104:107], v[136:139]\n" \
"v_mfma_f32_16x16x32_bf16 v[136:139], a[204:207], a[108:111], v[136:139]\n" \
"buffer_load_dwordx4 a[12:15], %[oq0], %[srdz], s50 offen offset:3072 sc1\n" \
"v_mfma_f32_16x16x32_bf16 v[140:143], a[200:203], a[120:123], v[140:143]\n" \
"ds_read_b64_tr_b16 a[228:229], v215 offset:33280\n" \
"v_mfma_f32_16x16x32_bf16 v[140:143], a[204:207], a[124:127], v[140:143]\n" \
"ds_read_b64_tr_b16 a[230:231], v215 offset:35328\n" \
"v_mfma_f32_16x16x32_bf16 v[28:31], a[224:227], v[16:19], v[28:31]\n" \
"buffer_load_dwordx4 a[16:19], v208, %[srdz], s50 offen offset:0 sc1\n" \
"v_mfma_f32_16x16x32_bf16 v[28:31], a[232:235], v[24:27], v[28:31]\n" \
"v_mfma_f32_16x16x32_bf16 v[60:63], a[224:227], v[48:51], v[60:63]\n" \
"v_mfma_f32_16x16x32_bf16 v[60:63], a[232:235], v[56:59], v[60:63]\n" \
"s_cbranch_vccz Lg8_%=\n" \
"s_nop 0\n" \
"buffer_store_dwordx4 v[24:27], %[oq0], %[srdz], s60 offen offset:3072 sc1\n" \
"Lg8_%=:\n" \
"v_mfma_f32_16x16x32_bf16 v[92:95], a[224:227], v[80:83], v[92:95]\n" \
"v_mfma_f32_16x16x32_bf16 v[92:95], a[232:235], v[88:91], v[92:95]\n" \
"v_cmp_u_f32_e64 s[44:45], v128, v132\n" \
"buffer_load_dwordx4 a[20:23], v208, %[srdz], s50 offen offset:1024 sc1\n" \
"v_mfma_f32_16x16x32_bf16 v[124:127], a[224:227], v[112:115], v[124:127]\n" \
"v_mfma_f32_16x16x32_bf16 v[124:127], a[232:235], v[120:123], v[124:127]\n" \
"v_cmp_u_f32_e64 s[56:57], v136, v140\n" \
"s_or_b64 s[56:57], s[56:57], s[44:45]\n" \
"s_cbranch_scc1 Lsp1_%=\n" \
"Lbk1_%=:\n" \
"s_waitcnt vmcnt(63) lgkmcnt(2)\n" \
"s_nop 0\n" \
"v_mfma_f32_16x16x32_bf16 v[144:147], a[208:211], a[64:67], v[144:147]\n" \
"ds_read_b64_tr_b16 a[224:225], v215 offset:37376\n" \
"v_mfma_f32_16x16x32_bf16 v[144:147], a[212:215], a[68:71], v[144:147]\n" \
"ds_read_b64_tr_b16 a[226:227], v215 offset:39424\n" \
"buffer_load_dwordx4 a[24:27], v208, %[srdz], s50 offen offset:2048 sc1\n" \
"v_mfma_f32_16x16x32_bf16 v[148:151], a[208:211], a[80:83], v[148:151]\n" \
"ds_read_b64_tr_b16 a[196:197], v215 offset:41472\n" \
"v_mfma_f32_16x16x32_bf16 v[148:151], a[212:215], a[84:87], v[148:151]\n" \
"ds_read_b64_tr_b16 a[198:199], v215 offset:43520\n" \
"v_mfma_f32_16x16x32_bf16 v[152:155], a[208:211], a[96:99], v[152:155]\n" \
"ds_read_b64_tr_b16 a[200:201], v215 offset:45568\n" \
"s_cbranch_vccz Lg9_%=\n" \
"s_nop 0\n" \
"buffer_store_dwordx4 v[32:35], v208, %[srdz], s60 offen offset:0 sc1\n" \
"Lg9_%=:\n" \
"v_mfma_f32_16x16x32_bf16 v[152:155], a[212:215], a[100:103], v[152:155]\n" \
"ds_read_b64_tr_b16 a[202:203], v215 offset:47616\n" \
"v_mfma_f32_16x16x32_bf16 v[156:159], a[208:211], a[112:115], v[156:159]\n" \
"ds_read_b64_tr_b16 a[204:205], v227 offset:33280\n" \
"buffer_load_dwordx4 a[28:31], v208, %[srdz], s50 offen offset:3072 sc1\n" \
"v_mfma_f32_16x16x32_bf16 v[156:159], a[212:215], a[116:119], v[156:159]\n" \
"ds_read_b64_tr_b16 a[206:207], v227 offset:35328\n" \
"v_mfma_f32_16x16x32_bf16 v[144:147], a[216:219], a[72:75], v[144:147]\n" \
"ds_read_b64_tr_b16 a[232:233], v227 offset:37376\n" \
"v_mfma_f32_16x16x32_bf16 v[144:147], a[220:223], a[76:79], v[144:147]\n" \
"ds_read_b64_tr_b16 a[234:235], v227 offset:39424\n" \
"v_mfma_f32_16x16x32_bf16 v[148:151], a[216:219], a[88:91], v[148:151]\n" \
"buffer_load_dwordx4 a[32:35], v209, %[srdz], s50 offen offset:0 sc1\n" \
"v_mfma_f32_16x16x32_bf16 v[148:151], a[220:223], a[92:95], v[148:151]\n" \
"v_mfma_f32_16x16x32_bf16 v[152:155], a[216:219], a[104:107], v[152:155]\n" \
"s_cbranch_vccz Lg10_%=\n" \
"s_nop 0\n" \
"buffer_store_dwordx4 v[40:43], v208, %[srdz], s60 offen offset:1024 sc1\n" \
"Lg10_%=:\n" \
"v_mfma_f32_16x16x32_bf16 v[152:155], a[220:223], a[108:111], v[152:155]\n" \
"v_mfma_f32_16x16x32_bf16 v[156:159], a[216:219], a[120:123], v[156:159]\n" \
"ds_read_b64_tr_b16 a[236:237], v227 offset:41472\n" \
"buffer_load_dwordx4 a[36:39], v209, %[srdz], s50 offen offset:1024 sc1\n" \
"v_mfma_f32_16x16x32_bf16 v[156:159], a[220:223], a[124:127], v[156:159]\n" \
"s_waitcnt vmcnt(63) lgkmcnt(5)\n" \
"s_nop 0\n" \
"v_mfma_f32_16x16x32_bf16 v[160:163], a[228:231], a[64:67], v[160:163]\n" \
"ds_read_b64_tr_b16 a[238:239], v227 offset:43520\n" \
"v_mfma_f32_16x16x32_bf16 v[160:163], a[224:227], a[68:71], v[160:163]\n" \
"ds_read_b64_tr_b16 a[208:209], v227 offset:45568\n" \
"v_mfma_f32_16x16x32_bf16 v[164:167], a[228:231], a[80:83], v[164:167]\n" \
"ds_read_b64_tr_b16 a[210:211], v227 offset:47616\n" \
"buffer_load_dwordx4 a[40:43], v209, %[srdz], s50 offen offset:2048 sc1\n" \
"v_mfma_f32_16x16x32_bf16 v[164:167], a[224:227], a[84:87], v[164:167]\n" \
"v_mfma_f32_16x16x32_bf16 v[168:171], a[228:231], a[96:99], v[168:171]\n" \
"v_mfma_f32_16x16x32_bf16 v[168:171], a[224:227], a[100:103], v[168:171]\n" \
"s_cbranch_vccz Lg11_%=\n" \
"s_nop 0\n" \
"buffer_store_dwordx4 v[48:51], v208, %[srdz], s60 offen offset:2048 sc1\n" \
"Lg11_%=:\n" \
"v_mfma_f32_16x16x32_bf16 v[172:175], a[228:231], a[112:115], v[172:175]\n" \
"v_mfma_f32_16x16x32_bf16 v[172:175], a[224:227], a[116:119], v[172:175]\n" \
"buffer_load_dwordx4 a[44:47], v209, %[srdz], s50 offen offset:3072 sc1\n" \
"v_mfma_f32_16x16x32_bf16 v[160:163], a[196:199], a[72:75], v[160:163]\n" \
"v_mfma_f32_16x16x32_bf16 v[160:163], a[200:203], a[76:79], v[160:163]\n" \
"buffer_load_dwordx4 a[48:51], v210, %[srdz], s50 offen offset:0 sc1\n" \
"v_mfma_f32_16x16x32_bf16 v[164:167], a[196:199], a[88:91], v[164:167]\n" \
"v_mfma_f32_16x16x32_bf16 v[164:167], a[200:203], a[92:95], v[164:167]\n" \
"buffer_load_dwordx4 a[52:55], v210, %[srdz], s50 offen offset:1024 sc1\n" \
"v_mfma_f32_16x16x32_bf16 v[168:171], a[196:199], a[104:107], v[168:171]\n" \
"s_waitcnt vmcnt(54) lgkmcnt(0)\n" \
"s_bitcmp1_b32 0xf, s48\n" \
"s_cbranch_scc1 Lcw7_%=\n" \
"s_waitcnt vmcnt(45)\n" \
"s_bitcmp1_b32 0x30, s48\n" \
"s_cbranch_scc1 Lcw7_%=\n" \
"s_waitcnt vmcnt(38)\n" \
"Lcw7_%=:\n" \
"s_barrier\n" \
"v_mfma_f32_16x16x32_bf16 v[168:171], a[200:203], a[108:111], v[168:171]\n" \
"ds_read_b64_tr_b16 a[212:213], v220 offset:32768\n" \
"s_cbranch_vccz Lg12_%=\n" \
"s_nop 0\n" \
"buffer_store_dwordx4 v[56:59], v208, %[srdz], s60 offen offset:3072 sc1\n" \
"Lg12_%=:\n" \
"v_mfma_f32_16x16x32_bf16 v[172:175], a[196:199], a[120:123], v[172:175]\n" \
"ds_read_b64_tr_b16 a[214:215], v220 offset:34816\n" \
"v_mfma_f32_16x16x32_bf16 v[172:175], a[200:203], a[124:127], v[172:175]\n" \
"s_waitcnt vmcnt(63) lgkmcnt(2)\n" \
"s_nop 0\n" \
"v_mfma_f32_16x16x32_bf16 v[176:179], a[204:207], a[64:67], v[176:179]\n" \
"ds_read_b64_tr_b16 a[216:217], v220 offset:36864\n" \
"buffer_load_dwordx4 a[56:59], v210, %[srdz], s50 offen offset:2048 sc1\n" \
"v_mfma_f32_16x16x32_bf16 v[176:179], a[232:235], a[68:71], v[176:179]\n" \
"ds_read_b64_tr_b16 a[218:219], v220 offset:38912\n" \
"v_mfma_f32_16x16x32_bf16 v[180:183], a[204:207], a[80:83], v[180:183]\n" \
"ds_read_b64_tr_b16 a[220:221], v220 offset:40960\n" \
"v_mfma_f32_16x16x32_bf16 v[180:183], a[232:235], a[84:87], v[180:183]\n" \
"ds_read_b64_tr_b16 a[222:223], v220 offset:43008\n" \
"v_mfma_f32_16x16x32_bf16 v[184:187], a[204:207], a[96:99], v[184:187]\n" \
"ds_read_b64_tr_b16 a[228:229], v220 offset:45056\n" \
"buffer_load_dwordx4 a[60:63], v210, %[srdz], s50 offen offset:3072 sc1\n" \
"v_mfma_f32_16x16x32_bf16 v[184:187], a[232:235], a[100:103], v[184:187]\n" \
"ds_read_b64_tr_b16 a[230:231], v220 offset:47104\n" \
"v_mfma_f32_16x16x32_bf16 v[188:191], a[204:207], a[112:115], v[188:191]\n" \
"ds_read_b64_tr_b16 a[224:225], v221 offset:32768\n" \
"s_cbranch_vccz Lg13_%=\n" \
"s_nop 0\n" \
"buffer_store_dwordx4 v[64:67], v209, %[srdz], s60 offen offset:0 sc1\n" \
"Lg13_%=:\n" \
"v_mfma_f32_16x16x32_bf16 v[188:191], a[232:235], a[116:119], v[188:191]\n" \
"ds_read_b64_tr_b16 a[226:227], v221 offset:34816\n" \
"v_mfma_f32_16x16x32_bf16 v[176:179], a[236:239], a[72:75], v[176:179]\n" \
"ds_read_b64_tr_b16 a[196:197], v221 offset:36864\n" \
"v_mfma_f32_16x16x32_bf16 v[176:179], a[208:211], a[76:79], v[176:179]\n" \
"ds_read_b64_tr_b16 a[198:199], v221 offset:38912\n" \
"v_mfma_f32_16x16x32_bf16 v[180:183], a[236:239], a[88:91], v[180:183]\n" \
"s_cmp_gt_u32 s48, 2\n" \
"s_cselect_b32 s50, 2, 3\n" \
"s_mul_i32 s50, s50, 262144\n" \
"buffer_load_dwordx4 a[128:131], %[oq0], %[srdz], s50 offen offset:0 sc1\n" \
"v_mfma_f32_16x16x32_bf16 v[180:183], a[208:211], a[92:95], v[180:183]\n" \
"v_mfma_f32_16x16x32_bf16 v[184:187], a[236:239], a[104:107], v[184:187]\n" \
"v_mfma_f32_16x16x32_bf16 v[184:187], a[208:211], a[108:111], v[184:187]\n" \
"v_mfma_f32_16x16x32_bf16 v[188:191], a[236:239], a[120:123], v[188:191]\n" \
"ds_read_b64_tr_b16 a[200:201], v221 offset:40960\n" \
"s_cbranch_vccz Lg14_%=\n" \
"s_nop 0\n" \
"buffer_store_dwordx4 v[72:75], v209, %[srdz], s60 offen offset:1024 sc1\n" \
"Lg14_%=:\n" \
"v_mfma_f32_16x16x32_bf16 v[188:191], a[208:211], a[124:127], v[188:191]\n" \
"s_waitcnt vmcnt(63) lgkmcnt(5)\n" \
"s_nop 0\n" \
"v_mfma_f32_16x16x32_bf16 v[4:7], a[212:215], a[64:67], v[4:7]\n" \
"ds_read_b64_tr_b16 a[202:203], v221 offset:43008\n" \
"v_mfma_f32_16x16x32_bf16 v[4:7], a[216:219], a[68:71], v[4:7]\n" \
"ds_read_b64_tr_b16 a[204:205], v221 offset:45056\n" \
"s_add_u32 m0, %[m0b], 0\n" \
"s_cmp_gt_u32 s48, 2\n" \
"s_cselect_b32 s49, 2, 3\n" \
"s_lshl_b32 s49, s49, 18\n" \
"s_or_b32 s49, s49, s58\n" \
"s_nop 0\n" \
"buffer_load_dwordx4 %[wo0], %[srdt], s49 offen offset:0 lds\n" \
"v_mfma_f32_16x16x32_bf16 v[36:39], a[212:215], a[80:83], v[36:39]\n" \
"ds_read_b64_tr_b16 a[206:207], v221 offset:47104\n" \
"v_mfma_f32_16x16x32_bf16 v[36:39], a[216:219], a[84:87], v[36:39]\n" \
"buffer_load_dwordx4 a[132:135], %[oq0], %[srdz], s50 offen offset:1024 sc1\n" \
"v_mfma_f32_16x16x32_bf16 v[68:71], a[212:215], a[96:99], v[68:71]\n" \
"v_mfma_f32_16x16x32_bf16 v[68:71], a[216:219], a[100:103], v[68:71]\n" \
"v_mfma_f32_16x16x32_bf16 v[100:103], a[212:215], a[112:115], v[100:103]\n" \
"ds_read_b64_tr_b16 a[232:233], v220 offset:33280\n" \
"v_mfma_f32_16x16x32_bf16 v[100:103], a[216:219], a[116:119], v[100:103]\n" \
"ds_read_b64_tr_b16 a[234:235], v220 offset:35328\n" \
"s_cbranch_vccz Lg15_%=\n" \
"s_nop 0\n" \
"buffer_store_dwordx4 v[80:83], v209, %[srdz], s60 offen offset:2048 sc1\n" \
"Lg15_%=:\n" \
"v_mfma_f32_16x16x32_bf16 v[4:7], a[220:223], a[72:75], v[4:7]\n" \
"ds_read_b64_tr_b16 a[236:237], v220 offset:37376\n" \
"v_mfma_f32_16x16x32_bf16 v[4:7], a[228:231], a[76:79], v[4:7]\n" \
"ds_read_b64_tr_b16 a[238:239], v220 offset:39424\n" \
"buffer_load_dwordx4 a[136:139], %[oq0], %[srdz], s50 offen offset:2048 sc1\n" \
"v_mfma_f32_16x16x32_bf16 v[36:39], a[220:223], a[88:91], v[36:39]\n" \
"v_mfma_f32_16x16x32_bf16 v[36:39], a[228:231], a[92:95], v[36:39]\n" \
"v_mfma_f32_16x16x32_bf16 v[68:71], a[220:223], a[104:107], v[68:71]\n" \
"v_mfma_f32_16x16x32_bf16 v[68:71], a[228:231], a[108:111], v[68:71]\n" \
"buffer_load_dwordx4 a[140:143], %[oq0], %[srdz], s50 offen offset:3072 sc1\n" \
"v_mfma_f32_16x16x32_bf16 v[100:103], a[220:223], a[120:123], v[100:103]\n" \
"ds_read_b64_tr_b16 a[208:209], v220 offset:41472\n" \
"v_mfma_f32_16x16x32_bf16 v[100:103], a[228:231], a[124:127], v[100:103]\n" \
"s_waitcnt vmcnt(63) lgkmcnt(5)\n" \
"s_nop 0\n" \
"v_mfma_f32_16x16x32_bf16 v[12:15], a[224:227], a[64:67], v[12:15]\n" \
"ds_read_b64_tr_b16 a[210:211], v220 offset:43520\n" \
"s_cbranch_vccz Lg16_%=\n" \
"s_nop 0\n" \
"buffer_store_dwordx4 v[88:91], v209, %[srdz], s60 offen offset:3072 sc1\n" \
"Lg16_%=:\n" \
"v_mfma_f32_16x16x32_bf16 v[12:15], a[196:199], a[68:71], v[12:15]\n" \
"ds_read_b64_tr_b16 a[212:213], v220 offset:45568\n" \
"v_mfma_f32_16x16x32_bf16 v[44:47], a[224:227], a[80:83], v[44:47]\n" \
"ds_read_b64_tr_b16 a[214:215], v220 offset:47616\n" \
"buffer_load_dwordx4 v212, %[srdt], s49 offen offset:1024 lds\n" \
"v_mfma_f32_16x16x32_bf16 v[44:47], a[196:199], a[84:87], v[44:47]\n" \
"v_mfma_f32_16x16x32_bf16 v[76:79], a[224:227], a[96:99], v[76:79]\n" \
"v_mfma_f32_16x16x32_bf16 v[76:79], a[196:199], a[100:103], v[76:79]\n" \
".if . - adaln_body != 45056\n" \
".error \"size model\"\n" \
".endif\n" \
"Lk10_%=:\n" \
"s_cbranch_execz Lk11_%=\n" \
"s_nop 0\n" \
"buffer_load_dwordx4 a[144:147], v208, %[srdz], s50 offen offset:0 sc1\n" \
"v_mfma_f32_16x16x32_bf16 v[108:111], a[224:227], a[112:115], v[108:111]\n" \
"ds_read_b64_tr_b16 a[216:217], v221 offset:33280\n" \
"v_mfma_f32_16x16x32_bf16 v[108:111], a[196:199], a[116:119], v[108:111]\n" \
"ds_read_b64_tr_b16 a[218:219], v221 offset:35328\n" \
"v_mfma_f32_16x16x32_bf16 v[12:15], a[200:203], a[72:75], v[12:15]\n" \
"ds_read_b64_tr_b16 a[220:221], v221 offset:41472\n" \
"s_cbranch_vccz Lg17_%=\n" \
"s_nop 0\n" \
"buffer_store_dwordx4 v[96:99], v210, %[srdz], s60 offen offset:0 sc1\n" \
"Lg17_%=:\n" \
"v_mfma_f32_16x16x32_bf16 v[12:15], a[204:207], a[76:79], v[12:15]\n" \
"ds_read_b64_tr_b16 a[222:223], v221 offset:43520\n" \
"v_mfma_f32_16x16x32_bf16 v[44:47], a[200:203], a[88:91], v[44:47]\n" \
"ds_read_b64_tr_b16 a[228:229], v221 offset:37376\n" \
"v_mfma_f32_16x16x32_bf16 v[44:47], a[204:207], a[92:95], v[44:47]\n" \
"ds_read_b64_tr_b16 a[230:231], v221 offset:39424\n" \
"v_mfma_f32_16x16x32_bf16 v[76:79], a[200:203], a[104:107], v[76:79]\n" \
"ds_read_b64_tr_b16 a[224:225], v221 offset:45568\n" \
"v_mfma_f32_16x16x32_bf16 v[76:79], a[204:207], a[108:111], v[76:79]\n" \
"ds_read_b64_tr_b16 a[226:227], v221 offset:47616\n" \
"v_mfma_f32_16x16x32_bf16 v[108:111], a[200:203], a[120:123], v[108:111]\n" \
"buffer_load_dwordx4 a[148:151], v208, %[srdz], s50 offen offset:1024 sc1\n" \
"v_mfma_f32_16x16x32_bf16 v[108:111], a[204:207], a[124:127], v[108:111]\n" \
"s_waitcnt vmcnt(63) lgkmcnt(12)\n" \
"s_nop 0\n" \
"v_mfma_f32_16x16x32_bf16 v[20:23], a[232:235], a[64:67], v[20:23]\n" \
"s_cbranch_vccz Lg18_%=\n" \
"s_nop 0\n" \
"buffer_store_dwordx4 v[104:107], v210, %[srdz], s60 offen offset:1024 sc1\n" \
"Lg18_%=:\n" \
"v_mfma_f32_16x16x32_bf16 v[20:23], a[236:239], a[68:71], v[20:23]\n" \
"v_mfma_f32_16x16x32_bf16 v[52:55], a[232:235], a[80:83], v[52:55]\n" \
"buffer_load_dwordx4 v213, %[srdt], s49 offen offset:2048 lds\n" \
"v_mfma_f32_16x16x32_bf16 v[52:55], a[236:239], a[84:87], v[52:55]\n" \
"v_mfma_f32_16x16x32_bf16 v[84:87], a[232:235], a[96:99], v[84:87]\n" \
"s_waitcnt vmcnt(59) lgkmcnt(0)\n" \
"s_bitcmp1_b32 0x30, s48\n" \
"s_cbranch_scc1 Lcw8_%=\n" \
"s_waitcnt vmcnt(45)\n" \
"Lcw8_%=:\n" \
"s_barrier\n" \
"s_nop 0\n" \
"v_mfma_f32_16x16x32_bf16 v[84:87], a[236:239], a[100:103], v[84:87]\n" \
"ds_read_b64_tr_b16 a[196:197], v220 offset:49152\n" \
"v_mfma_f32_16x16x32_bf16 v[116:119], a[232:235], a[112:115], v[116:119]\n" \
"ds_read_b64_tr_b16 a[198:199], v220 offset:51200\n" \
"buffer_load_dwordx4 a[152:155], v208, %[srdz], s50 offen offset:2048 sc1\n" \
"v_mfma_f32_16x16x32_bf16 v[116:119], a[236:239], a[116:119], v[116:119]\n" \
"ds_read_b64_tr_b16 a[200:201], v220 offset:53248\n" \
"v_mfma_f32_16x16x32_bf16 v[20:23], a[208:211], a[72:75], v[20:23]\n" \
"ds_read_b64_tr_b16 a[202:203], v220 offset:55296\n" \
"v_mfma_f32_16x16x32_bf16 v[20:23], a[212:215], a[76:79], v[20:23]\n" \
"ds_read_b64_tr_b16 a[204:205], v220 offset:57344\n" \
"s_cbranch_vccz Lg19_%=\n" \
"s_nop 0\n" \
"buffer_store_dwordx4 v[112:115], v210, %[srdz], s60 offen offset:2048 sc1\n" \
"Lg19_%=:\n" \
"v_mfma_f32_16x16x32_bf16 v[52:55], a[208:211], a[88:91], v[52:55]\n" \
"ds_read_b64_tr_b16 a[206:207], v220 offset:59392\n" \
"v_mfma_f32_16x16x32_bf16 v[52:55], a[212:215], a[92:95], v[52:55]\n" \
"ds_read_b64_tr_b16 a[232:233], v220 offset:61440\n" \
"v_mfma_f32_16x16x32_bf16 v[84:87], a[208:211], a[104:107], v[84:87]\n" \
"ds_read_b64_tr_b16 a[234:235], v220 offset:63488\n" \
"v_mfma_f32_16x16x32_bf16 v[84:87], a[212:215], a[108:111], v[84:87]\n" \
"buffer_load_dwordx4 a[156:159], v208, %[srdz], s50 offen offset:3072 sc1\n" \
"v_mfma_f32_16x16x32_bf16 v[116:119], a[208:211], a[120:123], v[116:119]\n" \
"v_mfma_f32_16x16x32_bf16 v[116:119], a[212:215], a[124:127], v[116:119]\n" \
"s_waitcnt vmcnt(21) lgkmcnt(4)\n" \
"s_bitcmp1_b32 0x30, s48\n" \
"s_cbranch_scc1 Lcw9_%=\n" \
"s_waitcnt vmcnt(13)\n" \
"Lcw9_%=:\n" \
"v_mfma_f32_16x16x32_bf16 v[28:31], a[216:219], a[64:67], v[28:31]\n" \
"v_mfma_f32_16x16x32_bf16 v[28:31], a[228:231], a[68:71], v[28:31]\n" \
"s_cbranch_vccz Lg20_%=\n" \
"s_nop 0\n" \
"buffer_store_dwordx4 v[120:123], v210, %[srdz], s60 offen offset:3072 sc1\n" \
"Lg20_%=:\n" \
"v_mfma_f32_16x16x32_bf16 v[60:63], a[216:219], a[80:83], v[60:63]\n" \
"v_mfma_f32_16x16x32_bf16 v[60:63], a[228:231], a[84:87], v[60:63]\n" \
"buffer_load_dwordx4 v214, %[srdt], s49 offen offset:3072 lds\n" \
"v_mfma_f32_16x16x32_bf16 v[92:95], a[216:219], a[96:99], v[92:95]\n" \
"v_mfma_f32_16x16x32_bf16 v[92:95], a[228:231], a[100:103], v[92:95]\n" \
"buffer_load_dwordx4 a[160:163], v209, %[srdz], s50 offen offset:0 sc1\n" \
"v_mfma_f32_16x16x32_bf16 v[124:127], a[216:219], a[112:115], v[124:127]\n" \
"ds_read_b64_tr_b16 a[236:237], v221 offset:49152\n" \
"v_mfma_f32_16x16x32_bf16 v[124:127], a[228:231], a[116:119], v[124:127]\n" \
"ds_read_b64_tr_b16 a[238:239], v221 offset:51200\n" \
"v_mfma_f32_16x16x32_bf16 v[192:195], a[196:199], a[0:3], v[128:131]\n" \
"ds_read_b64_tr_b16 a[208:209], v221 offset:53248\n" \
"v_mfma_f32_16x16x32_bf16 v[192:195], a[200:203], a[4:7], v[192:195]\n" \
"ds_read_b64_tr_b16 a[210:211], v221 offset:55296\n" \
"v_mfma_f32_16x16x32_bf16 v[196:199], a[196:199], a[16:19], v[132:135]\n" \
"buffer_load_dwordx4 a[164:167], v209, %[srdz], s50 offen offset:1024 sc1\n" \
"v_mfma_f32_16x16x32_bf16 v[196:199], a[200:203], a[20:23], v[196:199]\n" \
"v_mfma_f32_16x16x32_bf16 v[200:203], a[196:199], a[32:35], v[136:139]\n" \
"v_mfma_f32_16x16x32_bf16 v[200:203], a[200:203], a[36:39], v[200:203]\n" \
"v_mfma_f32_16x16x32_bf16 v[204:207], a[196:199], a[48:51], v[140:143]\n" \
"ds_read_b64_tr_b16 a[212:213], v221 offset:57344\n" \
"s_sub_u32 s61, s48, 6\n" \
"s_cmp_lt_u32 s61, 2\n" \
"s_cselect_b64 vcc, -1, 0\n" \
"s_nop 0\n" \
"v_mfma_f32_16x16x32_bf16 v[204:207], a[200:203], a[52:55], v[204:207]\n" \
"s_waitcnt vmcnt(22) lgkmcnt(5)\n" \
"s_bitcmp1_b32 0x30, s48\n" \
"s_cbranch_scc1 Lcw10_%=\n" \
"s_waitcnt vmcnt(14)\n" \
"Lcw10_%=:\n" \
"v_mfma_f32_16x16x32_bf16 v[192:195], a[204:207], a[8:11], v[192:195]\n" \
"ds_read_b64_tr_b16 a[214:215], v221 offset:59392\n" \
"s_cbranch_vccz Lg21_%=\n" \
"s_nop 0\n" \
"buffer_store_dwordx4 v[0:3], %[oq0], %[srdz], s60 offen offset:0 sc1\n" \
"Lg21_%=:\n" \
"v_mfma_f32_16x16x32_bf16 v[192:195], a[232:235], a[12:15], v[192:195]\n" \
"ds_read_b64_tr_b16 a[216:217], v221 offset:61440\n" \
"v_mfma_f32_16x16x32_bf16 v[196:199], a[204:207], a[24:27], v[196:199]\n" \
"ds_read_b64_tr_b16 a[218:219], v221 offset:63488\n" \
"v_mfma_f32_16x16x32_bf16 v[196:199], a[232:235], a[28:31], v[196:199]\n" \
"buffer_load_dwordx4 a[168:171], v209, %[srdz], s50 offen offset:2048 sc1\n" \
"v_mfma_f32_16x16x32_bf16 v[200:203], a[204:207], a[40:43], v[200:203]\n" \
"v_mfma_f32_16x16x32_bf16 v[200:203], a[232:235], a[44:47], v[200:203]\n" \
"v_mfma_f32_16x16x32_bf16 v[204:207], a[204:207], a[56:59], v[204:207]\n" \
"ds_read_b64_tr_b16 a[228:229], v220 offset:49664\n" \
"v_mfma_f32_16x16x32_bf16 v[204:207], a[232:235], a[60:63], v[204:207]\n" \
"ds_read_b64_tr_b16 a[230:231], v220 offset:51712\n" \
"v_mfma_f32_16x16x32_bf16 v[28:31], a[220:223], a[72:75], v[28:31]\n" \
"s_cbranch_vccz Lg22_%=\n" \
"s_nop 0\n" \
"buffer_store_dwordx4 v[8:11], %[oq0], %[srdz], s60 offen offset:1024 sc1\n" \
"Lg22_%=:\n" \
"v_mfma_f32_16x16x32_bf16 v[28:31], a[224:227], a[76:79], v[28:31]\n" \
"v_mfma_f32_16x16x32_bf16 v[60:63], a[220:223], a[88:91], v[60:63]\n" \
"buffer_load_dwordx4 a[172:175], v209, %[srdz], s50 offen offset:3072 sc1\n" \
"v_mfma_f32_16x16x32_bf16 v[60:63], a[224:227], a[92:95], v[60:63]\n" \
"v_mfma_f32_16x16x32_bf16 v[92:95], a[220:223], a[104:107], v[92:95]\n" \
"v_mfma_f32_16x16x32_bf16 v[92:95], a[224:227], a[108:111], v[92:95]\n" \
"v_cmp_u_f32_e64 s[44:45], v192, v196\n" \
"v_mfma_f32_16x16x32_bf16 v[124:127], a[220:223], a[120:123], v[124:127]\n" \
"s_add_u32 m0, %[m0b], 16384\n" \
"s_cmp_gt_u32 s48, 2\n" \
"s_cselect_b32 s49, 2, 3\n" \
"s_lshl_b32 s49, s49, 18\n" \
"s_or_b32 s49, s49, s59\n" \
"buffer_load_dwordx4 %[wo0], %[srdt], s49 offen offset:0 lds\n" \
"v_mfma_f32_16x16x32_bf16 v[124:127], a[224:227], a[124:127], v[124:127]\n" \
"v_cmp_u_f32_e64 s[56:57], v200, v204\n" \
"s_or_b64 s[56:57], s[56:57], s[44:45]\n" \
"s_cbranch_scc1 Lsp2_%=\n" \
"Lbk2_%=:\n" \
"s_waitcnt vmcnt(63) lgkmcnt(2)\n" \
"s_nop 0\n" \
"v_mfma_f32_16x16x32_bf16 v[144:147], a[236:239], a[0:3], v[144:147]\n" \
"ds_read_b64_tr_b16 a[220:221], v220 offset:53760\n" \
"v_mfma_f32_16x16x32_bf16 v[144:147], a[208:211], a[4:7], v[144:147]\n" \
"ds_read_b64_tr_b16 a[222:223], v220 offset:55808\n" \
"s_cbranch_vccz Lg23_%=\n" \
"s_nop 0\n" \
"buffer_store_dwordx4 v[16:19], %[oq0], %[srdz], s60 offen offset:2048 sc1\n" \
"Lg23_%=:\n" \
"v_mfma_f32_16x16x32_bf16 v[148:151], a[236:239], a[16:19], v[148:151]\n" \
"ds_read_b64_tr_b16 a[196:197], v220 offset:57856\n" \
"v_mfma_f32_16x16x32_bf16 v[148:151], a[208:211], a[20:23], v[148:151]\n" \
"ds_read_b64_tr_b16 a[198:199], v220 offset:59904\n" \
"buffer_load_dwordx4 a[176:179], v210, %[srdz], s50 offen offset:0 sc1\n" \
"v_mfma_f32_16x16x32_bf16 v[152:155], a[236:239], a[32:35], v[152:155]\n" \
"ds_read_b64_tr_b16 a[200:201], v220 offset:61952\n" \
"v_mfma_f32_16x16x32_bf16 v[152:155], a[208:211], a[36:39], v[152:155]\n" \
"ds_read_b64_tr_b16 a[202:203], v220 offset:64000\n" \
"v_mfma_f32_16x16x32_bf16 v[156:159], a[236:239], a[48:51], v[156:159]\n" \
"ds_read_b64_tr_b16 a[204:205], v221 offset:49664\n" \
"v_mfma_f32_16x16x32_bf16 v[156:159], a[208:211], a[52:55], v[156:159]\n" \
"ds_read_b64_tr_b16 a[206:207], v221 offset:51712\n" \
"v_mfma_f32_16x16x32_bf16 v[144:147], a[212:215], a[8:11], v[144:147]\n" \
"ds_read_b64_tr_b16 a[224:225], v221 offset:53760\n" \
"v_mfma_f32_16x16x32_bf16 v[144:147], a[216:219], a[12:15], v[144:147]\n" \
"ds_read_b64_tr_b16 a[226:227], v221 offset:55808\n" \
"v_mfma_f32_16x16x32_bf16 v[148:151], a[212:215], a[24:27], v[148:151]\n" \
"s_cbranch_vccz Lg24_%=\n" \
"s_nop 0\n" \
"buffer_store_dwordx4 v[24:27], %[oq0], %[srdz], s60 offen offset:3072 sc1\n" \
"Lg24_%=:\n" \
"v_mfma_f32_16x16x32_bf16 v[148:151], a[216:219], a[28:31], v[148:151]\n" \
"v_mfma_f32_16x16x32_bf16 v[152:155], a[212:215], a[40:43], v[152:155]\n" \
"buffer_load_dwordx4 a[180:183], v210, %[srdz], s50 offen offset:1024 sc1\n" \
"v_mfma_f32_16x16x32_bf16 v[152:155], a[216:219], a[44:47], v[152:155]\n" \
"v_mfma_f32_16x16x32_bf16 v[156:159], a[212:215], a[56:59], v[156:159]\n" \
"ds_read_b64_tr_b16 a[232:233], v221 offset:57856\n" \
"v_mfma_f32_16x16x32_bf16 v[156:159], a[216:219], a[60:63], v[156:159]\n" \
"s_waitcnt vmcnt(63) lgkmcnt(5)\n" \
"s_nop 0\n" \
"v_mfma_f32_16x16x32_bf16 v[160:163], a[228:231], a[0:3], v[160:163]\n" \
"ds_read_b64_tr_b16 a[234:235], v221 offset:59904\n" \
"buffer_load_dwordx4 v212, %[srdt], s49 offen offset:1024 lds\n" \
"v_mfma_f32_16x16x32_bf16 v[160:163], a[220:223], a[4:7], v[160:163]\n" \
"ds_read_b64_tr_b16 a[236:237], v221 offset:61952\n" \
"v_mfma_f32_16x16x32_bf16 v[164:167], a[228:231], a[16:19], v[164:167]\n" \
"ds_read_b64_tr_b16 a[238:239], v221 offset:64000\n" \
"v_mfma_f32_16x16x32_bf16 v[164:167], a[220:223], a[20:23], v[164:167]\n" \
"s_cbranch_vccz Lg25_%=\n" \
"s_nop 0\n" \
"buffer_store_dwordx4 v[32:35], v208, %[srdz], s60 offen offset:0 sc1\n" \
"Lg25_%=:\n" \
"v_mfma_f32_16x16x32_bf16 v[168:171], a[228:231], a[32:35], v[168:171]\n" \
"v_mfma_f32_16x16x32_bf16 v[168:171], a[220:223], a[36:39], v[168:171]\n" \
"buffer_load_dwordx4 a[184:187], v210, %[srdz], s50 offen offset:2048 sc1\n" \
"v_mfma_f32_16x16x32_bf16 v[172:175], a[228:231], a[48:51], v[172:175]\n" \
"v_mfma_f32_16x16x32_bf16 v[172:175], a[220:223], a[52:55], v[172:175]\n" \
"buffer_load_dwordx4 a[188:191], v210, %[srdz], s50 offen offset:3072 sc1\n" \
"v_mfma_f32_16x16x32_bf16 v[160:163], a[196:199], a[8:11], v[160:163]\n" \
"v_mfma_f32_16x16x32_bf16 v[160:163], a[200:203], a[12:15], v[160:163]\n" \
"v_mfma_f32_16x16x32_bf16 v[164:167], a[196:199], a[24:27], v[164:167]\n" \
"v_mfma_f32_16x16x32_bf16 v[164:167], a[200:203], a[28:31], v[164:167]\n" \
"v_mfma_f32_16x16x32_bf16 v[168:171], a[196:199], a[40:43], v[168:171]\n" \
"s_waitcnt vmcnt(63) lgkmcnt(0)\n" \
"s_bitcmp1_b32 0x30, s48\n" \
"s_cbranch_scc1 Lcw11_%=\n" \
"s_waitcnt vmcnt(59)\n" \
"s_bitcmp1_b32 0xc0, s48\n" \
"s_cbranch_scc1 Lcw11_%=\n" \
"s_waitcnt vmcnt(54)\n" \
"Lcw11_%=:\n" \
"s_barrier\n" \
"s_nop 0\n" \
"v_mfma_f32_16x16x32_bf16 v[168:171], a[200:203], a[44:47], v[168:171]\n" \
"ds_read_b64_tr_b16 a[208:209], v215 offset:49152\n" \
"s_cbranch_vccz Lg26_%=\n" \
"s_nop 0\n" \
"buffer_store_dwordx4 v[40:43], v208, %[srdz], s60 offen offset:1024 sc1\n" \
"Lg26_%=:\n" \
"v_mfma_f32_16x16x32_bf16 v[172:175], a[196:199], a[56:59], v[172:175]\n" \
"ds_read_b64_tr_b16 a[210:211], v215 offset:51200\n" \
"v_mfma_f32_16x16x32_bf16 v[172:175], a[200:203], a[60:63], v[172:175]\n" \
"s_waitcnt vmcnt(63) lgkmcnt(2)\n" \
"s_nop 0\n" \
"v_mfma_f32_16x16x32_bf16 v[176:179], a[204:207], a[0:3], v[176:179]\n" \
"ds_read_b64_tr_b16 a[212:213], v215 offset:53248\n" \
"global_load_dword v237, %[vct], %[pct]\n" \
"v_mfma_f32_16x16x32_bf16 v[176:179], a[224:227], a[4:7], v[176:179]\n" \
"ds_read_b64_tr_b16 a[214:215], v215 offset:55296\n" \
"v_mfma_f32_16x16x32_bf16 v[180:183], a[204:207], a[16:19], v[180:183]\n" \
"ds_read_b64_tr_b16 a[216:217], v215 offset:57344\n" \
"buffer_load_dwordx4 v213, %[srdt], s49 offen offset:2048 lds\n" \
"v_mfma_f32_16x16x32_bf16 v[180:183], a[224:227], a[20:23], v[180:183]\n" \
"ds_read_b64_tr_b16 a[218:219], v215 offset:59392\n" \
"v_mfma_f32_16x16x32_bf16 v[184:187], a[204:207], a[32:35], v[184:187]\n" \
"ds_read_b64_tr_b16 a[228:229], v215 offset:61440\n" \
"v_mfma_f32_16x16x32_bf16 v[184:187], a[224:227], a[36:39], v[184:187]\n" \
"ds_read_b64_tr_b16 a[230:231], v215 offset:63488\n" \
"s_cbranch_vccz Lg27_%=\n" \
"s_nop 0\n" \
"buffer_store_dwordx4 v[48:51], v208, %[srdz], s60 offen offset:2048 sc1\n" \
"Lg27_%=:\n" \
"v_mfma_f32_16x16x32_bf16 v[188:191], a[204:207], a[48:51], v[188:191]\n" \
"ds_read_b64_tr_b16 a[220:221], v227 offset:49152\n" \
"v_mfma_f32_16x16x32_bf16 v[188:191], a[224:227], a[52:55], v[188:191]\n" \
"ds_read_b64_tr_b16 a[222:223], v227 offset:51200\n" \
"v_mfma_f32_16x16x32_bf16 v[176:179], a[232:235], a[8:11], v[176:179]\n" \
"ds_read_b64_tr_b16 a[196:197], v227 offset:53248\n" \
"v_mfma_f32_16x16x32_bf16 v[176:179], a[236:239], a[12:15], v[176:179]\n" \
"ds_read_b64_tr_b16 a[198:199], v227 offset:55296\n" \
"s_cmp_gt_u32 s48, 3\n" \
"s_cselect_b32 s50, 3, 4\n" \
"s_mul_i32 s50, s50, 262144\n" \
"buffer_load_dwordx4 a[64:67], %[oq0], %[srdz], s50 offen offset:0 sc1\n" \
"v_mfma_f32_16x16x32_bf16 v[180:183], a[232:235], a[24:27], v[180:183]\n" \
"v_mfma_f32_16x16x32_bf16 v[180:183], a[236:239], a[28:31], v[180:183]\n" \
"buffer_load_dwordx4 v214, %[srdt], s49 offen offset:3072 lds\n" \
"v_mfma_f32_16x16x32_bf16 v[184:187], a[232:235], a[40:43], v[184:187]\n" \
"v_mfma_f32_16x16x32_bf16 v[184:187], a[236:239], a[44:47], v[184:187]\n" \
"v_mfma_f32_16x16x32_bf16 v[188:191], a[232:235], a[56:59], v[188:191]\n" \
"ds_read_b64_tr_b16 a[200:201], v227 offset:57344\n" \
"s_cbranch_vccz Lg28_%=\n" \
"s_nop 0\n" \
"buffer_store_dwordx4 v[56:59], v208, %[srdz], s60 offen offset:3072 sc1\n" \
"Lg28_%=:\n" \
"v_mfma_f32_16x16x32_bf16 v[188:191], a[236:239], a[60:63], v[188:191]\n" \
"s_waitcnt vmcnt(63) lgkmcnt(5)\n" \
"s_nop 0\n" \
"v_mfma_f32_16x16x32_bf16 v[4:7], a[208:211], a[0:3], v[4:7]\n" \
"ds_read_b64_tr_b16 a[202:203], v227 offset:59392\n" \
"buffer_load_dwordx4 a[68:71], %[oq0], %[srdz], s50 offen offset:1024 sc1\n" \
"v_mfma_f32_16x16x32_bf16 v[4:7], a[212:215], a[4:7], v[4:7]\n" \
"ds_read_b64_tr_b16 a[204:205], v227 offset:61440\n" \
"v_mfma_f32_16x16x32_bf16 v[36:39], a[208:211], a[16:19], v[36:39]\n" \
"ds_read_b64_tr_b16 a[206:207], v227 offset:63488\n" \
"v_mfma_f32_16x16x32_bf16 v[36:39], a[212:215], a[20:23], v[36:39]\n" \
"v_mfma_f32_16x16x32_bf16 v[68:71], a[208:211], a[32:35], v[68:71]\n" \
"buffer_load_dwordx4 a[72:75], %[oq0], %[srdz], s50 offen offset:2048 sc1\n" \
"v_mfma_f32_16x16x32_bf16 v[68:71], a[212:215], a[36:39], v[68:71]\n" \
"v_mfma_f32_16x16x32_bf16 v[100:103], a[208:211], a[48:51], v[100:103]\n" \
"ds_read_b64_tr_b16 a[224:225], v215 offset:49664\n" \
"v_mfma_f32_16x16x32_bf16 v[100:103], a[212:215], a[52:55], v[100:103]\n" \
"ds_read_b64_tr_b16 a[226:227], v215 offset:51712\n" \
"s_cbranch_vccz Lg29_%=\n" \
"s_nop 0\n" \
"buffer_store_dwordx4 v[64:67], v209, %[srdz], s60 offen offset:0 sc1\n" \
"Lg29_%=:\n" \
"v_mfma_f32_16x16x32_bf16 v[4:7], a[216:219], a[8:11], v[4:7]\n" \
"ds_read_b64_tr_b16 a[232:233], v215 offset:53760\n" \
"v_mfma_f32_16x16x32_bf16 v[4:7], a[228:231], a[12:15], v[4:7]\n" \
"ds_read_b64_tr_b16 a[234:235], v215 offset:55808\n" \
"v_mfma_f32_16x16x32_bf16 v[36:39], a[216:219], a[24:27], v[36:39]\n" \
"v_mfma_f32_16x16x32_bf16 v[36:39], a[228:231], a[28:31], v[36:39]\n" \
"buffer_load_dwordx4 a[76:79], %[oq0], %[srdz], s50 offen offset:3072 sc1\n" \
"v_mfma_f32_16x16x32_bf16 v[68:71], a[216:219], a[40:43], v[68:71]\n" \
"v_mfma_f32_16x16x32_bf16 v[68:71], a[228:231], a[44:47], v[68:71]\n" \
"v_mfma_f32_16x16x32_bf16 v[100:103], a[216:219], a[56:59], v[100:103]\n" \
"ds_read_b64_tr_b16 a[236:237], v215 offset:57856\n" \
"v_mfma_f32_16x16x32_bf16 v[100:103], a[228:231], a[60:63], v[100:103]\n" \
"s_waitcnt vmcnt(63) lgkmcnt(5)\n" \
"s_nop 0\n" \
"v_mfma_f32_16x16x32_bf16 v[12:15], a[220:223], a[0:3], v[12:15]\n" \
"ds_read_b64_tr_b16 a[238:239], v215 offset:59904\n" \
"s_cbranch_vccz Lg30_%=\n" \
"s_nop 0\n" \
"buffer_store_dwordx4 v[72:75], v209, %[srdz], s60 offen offset:1024 sc1\n" \
"Lg30_%=:\n" \
"v_mfma_f32_16x16x32_bf16 v[12:15], a[196:199], a[4:7], v[12:15]\n" \
"ds_read_b64_tr_b16 a[208:209], v215 offset:61952\n" \
"v_mfma_f32_16x16x32_bf16 v[44:47], a[220:223], a[16:19], v[44:47]\n" \
"ds_read_b64_tr_b16 a[210:211], v215 offset:64000\n" \
"v_mfma_f32_16x16x32_bf16 v[44:47], a[196:199], a[20:23], v[44:47]\n" \
"buffer_load_dwordx4 a[80:83], v208, %[srdz], s50 offen offset:0 sc1\n" \
"v_mfma_f32_16x16x32_bf16 v[76:79], a[220:223], a[32:35], v[76:79]\n" \
"v_mfma_f32_16x16x32_bf16 v[76:79], a[196:199], a[36:39], v[76:79]\n" \
"s_add_u32 m0, %[m0b], 32768\n" \
"s_cmp_gt_u32 s48, 3\n" \
"s_cselect_b32 s49, 3, 4\n" \
"s_lshl_b32 s49, s49, 18\n" \
"s_or_b32 s49, s49, s58\n" \
"buffer_load_dwordx4 %[wo0], %[srdt], s49 offen offset:0 lds\n" \
"v_mfma_f32_16x16x32_bf16 v[108:111], a[220:223], a[48:51], v[108:111]\n" \
"ds_read_b64_tr_b16 a[212:213], v227 offset:49664\n" \
"v_mfma_f32_16x16x32_bf16 v[108:111], a[196:199], a[52:55], v[108:111]\n" \
"ds_read_b64_tr_b16 a[214:215], v227 offset:51712\n" \
"v_mfma_f32_16x16x32_bf16 v[12:15], a[200:203], a[8:11], v[12:15]\n" \
"ds_read_b64_tr_b16 a[216:217], v227 offset:57856\n" \
"v_mfma_f32_16x16x32_bf16 v[12:15], a[204:207], a[12:15], v[12:15]\n" \
"ds_read_b64_tr_b16 a[218:219], v227 offset:59904\n" \
"s_cbranch_vccz Lg31_%=\n" \
"s_nop 0\n" \
"buffer_store_dwordx4 v[80:83], v209, %[srdz], s60 offen offset:2048 sc1\n" \
"Lg31_%=:\n" \
"v_mfma_f32_16x16x32_bf16 v[44:47], a[200:203], a[24:27], v[44:47]\n" \
"ds_read_b64_tr_b16 a[228:229], v227 offset:53760\n" \
"v_mfma_f32_16x16x32_bf16 v[44:47], a[204:207], a[28:31], v[44:47]\n" \
"ds_read_b64_tr_b16 a[230:231], v227 offset:55808\n" \
"v_mfma_f32_16x16x32_bf16 v[76:79], a[200:203], a[40:43], v[76:79]\n" \
"ds_read_b64_tr_b16 a[220:221], v227 offset:61952\n" \
"buffer_load_dwordx4 a[84:87], v208, %[srdz], s50 offen offset:1024 sc1\n" \
"v_mfma_f32_16x16x32_bf16 v[76:79], a[204:207], a[44:47], v[76:79]\n" \
"ds_read_b64_tr_b16 a[222:223], v227 offset:64000\n" \
"v_mfma_f32_16x16x32_bf16 v[108:111], a[200:203], a[56:59], v[108:111]\n" \
"v_mfma_f32_16x16x32_bf16 v[108:111], a[204:207], a[60:63], v[108:111]\n" \
"s_waitcnt vmcnt(63) lgkmcnt(12)\n" \
"s_nop 0\n" \
"v_mfma_f32_16x16x32_bf16 v[20:23], a[224:227], a[0:3], v[20:23]\n" \
"v_mfma_f32_16x16x32_bf16 v[20:23], a[232:235], a[4:7], v[20:23]\n" \
"v_mfma_f32_16x16x32_bf16 v[52:55], a[224:227], a[16:19], v[52:55]\n" \
"s_cbranch_vccz Lg32_%=\n" \
"s_nop 0\n" \
"buffer_store_dwordx4 v[88:91], v209, %[srdz], s60 offen offset:3072 sc1\n" \
"Lg32_%=:\n" \
"v_mfma_f32_16x16x32_bf16 v[52:55], a[232:235], a[20:23], v[52:55]\n" \
"v_mfma_f32_16x16x32_bf16 v[84:87], a[224:227], a[32:35], v[84:87]\n" \
"s_waitcnt vmcnt(32) lgkmcnt(0)\n" \
"s_nop 0\n" \
"s_bitcmp1_b32 0xc0, s48\n" \
"s_cbranch_scc1 Lcw12_%=\n" \
"s_waitcnt vmcnt(20)\n" \
"Lcw12_%=:\n" \
"s_barrier\n" \
"s_nop 0\n" \
"v_mfma_f32_16x16x32_bf16 v[84:87], a[232:235], a[36:39], v[84:87]\n" \
"ds_read_b64_tr_b16 a[196:197], v215 offset:0\n" \
"buffer_load_dwordx4 a[88:91], v208, %[srdz], s50 offen offset:2048 sc1\n" \
"v_mfma_f32_16x16x32_bf16 v[116:119], a[224:227], a[48:51], v[116:119]\n" \
"ds_read_b64_tr_b16 a[198:199], v215 offset:2048\n" \
"v_mfma_f32_16x16x32_bf16 v[116:119], a[232:235], a[52:55], v[116:119]\n" \
"ds_read_b64_tr_b16 a[200:201], v215 offset:4096\n" \
"buffer_load_dwordx4 v212, %[srdt], s49 offen offset:1024 lds\n" \
"v_mfma_f32_16x16x32_bf16 v[20:23], a[236:239], a[8:11], v[20:23]\n" \
"ds_read_b64_tr_b16 a[202:203], v215 offset:6144\n" \
"v_mfma_f32_16x16x32_bf16 v[20:23], a[208:211], a[12:15], v[20:23]\n" \
"ds_read_b64_tr_b16 a[204:205], v215 offset:8192\n" \
"v_mfma_f32_16x16x32_bf16 v[52:55], a[236:239], a[24:27], v[52:55]\n" \
"ds_read_b64_tr_b16 a[206:207], v215 offset:10240\n" \
"v_mfma_f32_16x16x32_bf16 v[52:55], a[208:211], a[28:31], v[52:55]\n" \
"ds_read_b64_tr_b16 a[224:225], v215 offset:12288\n" \
"s_cbranch_vccz Lg33_%=\n" \
"s_nop 0\n" \
"buffer_store_dwordx4 v[96:99], v210, %[srdz], s60 offen offset:0 sc1\n" \
"Lg33_%=:\n" \
"v_mfma_f32_16x16x32_bf16 v[84:87], a[236:239], a[40:43], v[84:87]\n" \
"ds_read_b64_tr_b16 a[226:227], v215 offset:14336\n" \
"v_mfma_f32_16x16x32_bf16 v[84:87], a[208:211], a[44:47], v[84:87]\n" \
"buffer_load_dwordx4 a[92:95], v208, %[srdz], s50 offen offset:3072 sc1\n" \
"v_mfma_f32_16x16x32_bf16 v[116:119], a[236:239], a[56:59], v[116:119]\n" \
"v_mfma_f32_16x16x32_bf16 v[116:119], a[208:211], a[60:63], v[116:119]\n" \
"s_waitcnt vmcnt(25) lgkmcnt(4)\n" \
"s_nop 0\n" \
"s_bitcmp1_b32 0xc0, s48\n" \
"s_cbranch_scc1 Lcw13_%=\n" \
"s_waitcnt vmcnt(16)\n" \
"Lcw13_%=:\n" \
"v_mfma_f32_16x16x32_bf16 v[28:31], a[212:215], a[0:3], v[28:31]\n" \
"buffer_load_dwordx4 a[96:99], v209, %[srdz], s50 offen offset:0 sc1\n" \
"v_mfma_f32_16x16x32_bf16 v[28:31], a[228:231], a[4:7], v[28:31]\n" \
"v_mfma_f32_16x16x32_bf16 v[60:63], a[212:215], a[16:19], v[60:63]\n" \
"buffer_load_dwordx4 v213, %[srdt], s49 offen offset:2048 lds\n" \
"v_mfma_f32_16x16x32_bf16 v[60:63], a[228:231], a[20:23], v[60:63]\n" \
"v_mfma_f32_16x16x32_bf16 v[92:95], a[212:215], a[32:35], v[92:95]\n" \
"s_cbranch_vccz Lg34_%=\n" \
"s_nop 0\n" \
"buffer_store_dwordx4 v[104:107], v210, %[srdz], s60 offen offset:1024 sc1\n" \
"Lg34_%=:\n" \
"v_mfma_f32_16x16x32_bf16 v[92:95], a[228:231], a[36:39], v[92:95]\n" \
"v_mfma_f32_16x16x32_bf16 v[124:127], a[212:215], a[48:51], v[124:127]\n" \
"ds_read_b64_tr_b16 a[232:233], v227 offset:0\n" \
"v_mfma_f32_16x16x32_bf16 v[124:127], a[228:231], a[52:55], v[124:127]\n" \
"ds_read_b64_tr_b16 a[234:235], v227 offset:2048\n" \
"v_mfma_f32_16x16x32_bf16 v[128:131], a[196:199], a[128:131], v[192:195]\n" \
"ds_read_b64_tr_b16 a[236:237], v227 offset:4096\n" \
"v_mfma_f32_16x16x32_bf16 v[128:131], a[200:203], a[132:135], v[128:131]\n" \
"ds_read_b64_tr_b16 a[238:239], v227 offset:6144\n" \
"buffer_load_dwordx4 a[100:103], v209, %[srdz], s50 offen offset:1024 sc1\n" \
"v_mfma_f32_16x16x32_bf16 v[132:135], a[196:199], a[144:147], v[196:199]\n" \
"v_mfma_f32_16x16x32_bf16 v[132:135], a[200:203], a[148:151], v[132:135]\n" \
"v_mfma_f32_16x16x32_bf16 v[136:139], a[196:199], a[160:163], v[200:203]\n" \
"v_mfma_f32_16x16x32_bf16 v[136:139], a[200:203], a[164:167], v[136:139]\n" \
"s_cbranch_vccz Lg35_%=\n" \
"s_nop 0\n" \
"buffer_store_dwordx4 v[112:115], v210, %[srdz], s60 offen offset:2048 sc1\n" \
"Lg35_%=:\n" \
"v_mfma_f32_16x16x32_bf16 v[140:143], a[196:199], a[176:179], v[204:207]\n" \
"ds_read_b64_tr_b16 a[208:209], v227 offset:8192\n" \
"v_mfma_f32_16x16x32_bf16 v[140:143], a[200:203], a[180:183], v[140:143]\n" \
"s_waitcnt vmcnt(26) lgkmcnt(5)\n" \
"s_nop 0\n" \
"s_bitcmp1_b32 0xc0, s48\n" \
"s_cbranch_scc1 Lcw14_%=\n" \
"s_waitcnt vmcnt(16)\n" \
"Lcw14_%=:\n" \
"v_mfma_f32_16x16x32_bf16 v[128:131], a[204:207], a[136:139], v[128:131]\n" \
"ds_read_b64_tr_b16 a[210:211], v227 offset:10240\n" \
"buffer_load_dwordx4 a[104:107], v209, %[srdz], s50 offen offset:2048 sc1\n" \
"v_mfma_f32_16x16x32_bf16 v[128:131], a[224:227], a[140:143], v[128:131]\n" \
"ds_read_b64_tr_b16 a[212:213], v227 offset:12288\n" \
"v_mfma_f32_16x16x32_bf16 v[132:135], a[204:207], a[152:155], v[132:135]\n" \
"ds_read_b64_tr_b16 a[214:215], v227 offset:14336\n" \
"v_mfma_f32_16x16x32_bf16 v[132:135], a[224:227], a[156:159], v[132:135]\n" \
"buffer_load_dwordx4 v214, %[srdt], s49 offen offset:3072 lds\n" \
"v_mfma_f32_16x16x32_bf16 v[136:139], a[204:207], a[168:171], v[136:139]\n" \
"v_mfma_f32_16x16x32_bf16 v[136:139], a[224:227], a[172:175], v[136:139]\n" \
"v_mfma_f32_16x16x32_bf16 v[140:143], a[204:207], a[184:187], v[140:143]\n" \
"ds_read_b64_tr_b16 a[228:229], v215 offset:512\n" \
"s_cbranch_vccz Lg36_%=\n" \
"s_nop 0\n" \
"buffer_store_dwordx4 v[120:123], v210, %[srdz], s60 offen offset:3072 sc1\n" \
"Lg36_%=:\n" \
"v_mfma_f32_16x16x32_bf16 v[140:143], a[224:227], a[188:191], v[140:143]\n" \
"ds_read_b64_tr_b16 a[230:231], v215 offset:2560\n" \
"v_mfma_f32_16x16x32_bf16 v[28:31], a[216:219], a[8:11], v[28:31]\n" \
"buffer_load_dwordx4 a[108:111], v209, %[srdz], s50 offen offset:3072 sc1\n" \
"v_mfma_f32_16x16x32_bf16 v[28:31], a[220:223], a[12:15], v[28:31]\n" \
"v_mfma_f32_16x16x32_bf16 v[60:63], a[216:219], a[24:27], v[60:63]\n" \
"v_mfma_f32_16x16x32_bf16 v[60:63], a[220:223], a[28:31], v[60:63]\n" \
"v_mfma_f32_16x16x32_bf16 v[92:95], a[216:219], a[40:43], v[92:95]\n" \
"buffer_load_dwordx4 a[112:115], v210, %[srdz], s50 offen offset:0 sc1\n" \
"v_mfma_f32_16x16x32_bf16 v[92:95], a[220:223], a[44:47], v[92:95]\n" \
"v_cmp_u_f32_e64 s[44:45], v128, v132\n" \
"v_mfma_f32_16x16x32_bf16 v[124:127], a[216:219], a[56:59], v[124:127]\n" \
"v_mfma_f32_16x16x32_bf16 v[124:127], a[220:223], a[60:63], v[124:127]\n" \
"v_cmp_u_f32_e64 s[56:57], v136, v140\n" \
"s_or_b64 s[56:57], s[56:57], s[44:45]\n" \
"s_cbranch_scc1 Lsp3_%=\n" \
"Lbk3_%=:\n" \
"s_waitcnt vmcnt(63) lgkmcnt(2)\n" \
"s_nop 0\n" \
"v_mfma_f32_16x16x32_bf16 v[144:147], a[232:235], a[128:131], v[144:147]\n" \
"ds_read_b64_tr_b16 a[216:217], v215 offset:4608\n" \
"v_mfma_f32_16x16x32_bf16 v[144:147], a[236:239], a[132:135], v[144:147]\n" \
"ds_read_b64_tr_b16 a[218:219], v215 offset:6656\n" \
"v_mfma_f32_16x16x32_bf16 v[148:151], a[232:235], a[144:147], v[148:151]\n" \
"ds_read_b64_tr_b16 a[196:197], v215 offset:8704\n" \
"v_mfma_f32_16x16x32_bf16 v[148:151], a[236:239], a[148:151], v[148:151]\n" \
"ds_read_b64_tr_b16 a[198:199], v215 offset:10752\n" \
"v_mfma_f32_16x16x32_bf16 v[152:155], a[232:235], a[160:163], v[152:155]\n" \
"ds_read_b64_tr_b16 a[200:201], v215 offset:12800\n" \
"v_mfma_f32_16x16x32_bf16 v[152:155], a[236:239], a[164:167], v[152:155]\n" \
"ds_read_b64_tr_b16 a[202:203], v215 offset:14848\n" \
"v_mfma_f32_16x16x32_bf16 v[156:159], a[232:235], a[176:179], v[156:159]\n" \
"ds_read_b64_tr_b16 a[204:205], v227 offset:512\n" \
"buffer_load_dwordx4 a[116:119], v210, %[srdz], s50 offen offset:1024 sc1\n" \
"v_mfma_f32_16x16x32_bf16 v[156:159], a[236:239], a[180:183], v[156:159]\n" \
"ds_read_b64_tr_b16 a[206:207], v227 offset:2560\n" \
"v_mfma_f32_16x16x32_bf16 v[144:147], a[208:211], a[136:139], v[144:147]\n" \
"ds_read_b64_tr_b16 a[220:221], v227 offset:4608\n" \
"v_mfma_f32_16x16x32_bf16 v[144:147], a[212:215], a[140:143], v[144:147]\n" \
"ds_read_b64_tr_b16 a[222:223], v227 offset:6656\n" \
"v_mfma_f32_16x16x32_bf16 v[148:151], a[208:211], a[152:155], v[148:151]\n" \
"s_add_u32 m0, %[m0b], 98304\n" \
"s_cmp_gt_u32 s48, 4\n" \
"s_cselect_b32 s49, 4, 5\n" \
"s_lshl_b32 s49, s49, 18\n" \
"s_or_b32 s49, s49, s58\n" \
"buffer_load_dwordx4 %[wo0], %[srdt], s49 offen offset:0 lds\n" \
"v_mfma_f32_16x16x32_bf16 v[148:151], a[212:215], a[156:159], v[148:151]\n" \
"v_mfma_f32_16x16x32_bf16 v[152:155], a[208:211], a[168:171], v[152:155]\n" \
"buffer_load_dwordx4 a[120:123], v210, %[srdz], s50 offen offset:2048 sc1\n" \
"v_mfma_f32_16x16x32_bf16 v[152:155], a[212:215], a[172:175], v[152:155]\n" \
"v_mfma_f32_16x16x32_bf16 v[156:159], a[208:211], a[184:187], v[156:159]\n" \
"ds_read_b64_tr_b16 a[224:225], v227 offset:8704\n" \
"v_mfma_f32_16x16x32_bf16 v[156:159], a[212:215], a[188:191], v[156:159]\n" \
"s_waitcnt vmcnt(63) lgkmcnt(5)\n" \
"s_nop 0\n" \
"v_mfma_f32_16x16x32_bf16 v[160:163], a[228:231], a[128:131], v[160:163]\n" \
"ds_read_b64_tr_b16 a[226:227], v227 offset:10752\n" \
"v_mfma_f32_16x16x32_bf16 v[160:163], a[216:219], a[132:135], v[160:163]\n" \
"ds_read_b64_tr_b16 a[232:233], v227 offset:12800\n" \
"v_mfma_f32_16x16x32_bf16 v[164:167], a[228:231], a[144:147], v[164:167]\n" \
"ds_read_b64_tr_b16 a[234:235], v227 offset:14848\n" \
"v_mfma_f32_16x16x32_bf16 v[164:167], a[216:219], a[148:151], v[164:167]\n" \
"buffer_load_dwordx4 a[124:127], v210, %[srdz], s50 offen offset:3072 sc1\n" \
"v_mfma_f32_16x16x32_bf16 v[168:171], a[228:231], a[160:163], v[168:171]\n" \
"v_mfma_f32_16x16x32_bf16 v[168:171], a[216:219], a[164:167], v[168:171]\n" \
"v_mfma_f32_16x16x32_bf16 v[172:175], a[228:231], a[176:179], v[172:175]\n" \
"v_mfma_f32_16x16x32_bf16 v[172:175], a[216:219], a[180:183], v[172:175]\n" \
"v_mfma_f32_16x16x32_bf16 v[160:163], a[196:199], a[136:139], v[160:163]\n" \
"v_mfma_f32_16x16x32_bf16 v[160:163], a[200:203], a[140:143], v[160:163]\n" \
"buffer_load_dwordx4 v212, %[srdt], s49 offen offset:1024 lds\n" \
"v_mfma_f32_16x16x32_bf16 v[164:167], a[196:199], a[152:155], v[164:167]\n" \
"v_mfma_f32_16x16x32_bf16 v[164:167], a[200:203], a[156:159], v[164:167]\n" \
"v_mfma_f32_16x16x32_bf16 v[168:171], a[196:199], a[168:171], v[168:171]\n" \
"s_waitcnt vmcnt(30) lgkmcnt(0)\n" \
"s_nop 0\n" \
"s_bitcmp1_b32 0xc0, s48\n" \
"s_cbranch_scc1 Lcw15_%=\n" \
"s_waitcnt vmcnt(21)\n" \
"Lcw15_%=:\n" \
"s_barrier\n" \
"s_nop 0\n" \
"v_mfma_f32_16x16x32_bf16 v[168:171], a[200:203], a[172:175], v[168:171]\n" \
"ds_read_b64_tr_b16 a[236:237], v215 offset:16384\n" \
"v_mfma_f32_16x16x32_bf16 v[172:175], a[196:199], a[184:187], v[172:175]\n" \
"ds_read_b64_tr_b16 a[238:239], v215 offset:18432\n" \
"v_mfma_f32_16x16x32_bf16 v[172:175], a[200:203], a[188:191], v[172:175]\n" \
"s_waitcnt vmcnt(63) lgkmcnt(2)\n" \
"s_nop 0\n" \
"v_mfma_f32_16x16x32_bf16 v[176:179], a[204:207], a[128:131], v[176:179]\n" \
"ds_read_b64_tr_b16 a[208:209], v215 offset:20480\n" \
"s_cmp_gt_u32 s48, 4\n" \
"s_cselect_b32 s50, 4, 5\n" \
"s_mul_i32 s50, s50, 262144\n" \
"buffer_load_dwordx4 v[0:3], %[oq0], %[srdz], s50 offen offset:0 sc1\n" \
"v_mfma_f32_16x16x32_bf16 v[176:179], a[220:223], a[132:135], v[176:179]\n" \
"ds_read_b64_tr_b16 a[210:211], v215 offset:22528\n" \
"v_mfma_f32_16x16x32_bf16 v[180:183], a[204:207], a[144:147], v[180:183]\n" \
"ds_read_b64_tr_b16 a[212:213], v215 offset:24576\n" \
"v_mfma_f32_16x16x32_bf16 v[180:183], a[220:223], a[148:151], v[180:183]\n" \
"ds_read_b64_tr_b16 a[214:215], v215 offset:26624\n" \
"v_mfma_f32_16x16x32_bf16 v[184:187], a[204:207], a[160:163], v[184:187]\n" \
"ds_read_b64_tr_b16 a[228:229], v215 offset:28672\n" \
"buffer_load_dwordx4 v[8:11], %[oq0], %[srdz], s50 offen offset:1024 sc1\n" \
"v_mfma_f32_16x16x32_bf16 v[184:187], a[220:223], a[164:167], v[184:187]\n" \
"ds_read_b64_tr_b16 a[230:231], v215 offset:30720\n" \
"v_mfma_f32_16x16x32_bf16 v[188:191], a[204:207], a[176:179], v[188:191]\n" \
"ds_read_b64_tr_b16 a[216:217], v227 offset:16384\n" \
"v_mfma_f32_16x16x32_bf16 v[188:191], a[220:223], a[180:183], v[188:191]\n" \
"ds_read_b64_tr_b16 a[218:219], v227 offset:18432\n" \
"v_mfma_f32_16x16x32_bf16 v[176:179], a[224:227], a[136:139], v[176:179]\n" \
"ds_read_b64_tr_b16 a[196:197], v227 offset:20480\n" \
"buffer_load_dwordx4 v[16:19], %[oq0], %[srdz], s50 offen offset:2048 sc1\n" \
"v_mfma_f32_16x16x32_bf16 v[176:179], a[232:235], a[140:143], v[176:179]\n" \
"ds_read_b64_tr_b16 a[198:199], v227 offset:22528\n" \
"v_mfma_f32_16x16x32_bf16 v[180:183], a[224:227], a[152:155], v[180:183]\n" \
"buffer_load_dwordx4 v213, %[srdt], s49 offen offset:2048 lds\n" \
"v_mfma_f32_16x16x32_bf16 v[180:183], a[232:235], a[156:159], v[180:183]\n" \
"v_mfma_f32_16x16x32_bf16 v[184:187], a[224:227], a[168:171], v[184:187]\n" \
"buffer_load_dwordx4 v[24:27], %[oq0], %[srdz], s50 offen offset:3072 sc1\n" \
"v_mfma_f32_16x16x32_bf16 v[184:187], a[232:235], a[172:175], v[184:187]\n" \
"v_mfma_f32_16x16x32_bf16 v[188:191], a[224:227], a[184:187], v[188:191]\n" \
"ds_read_b64_tr_b16 a[200:201], v227 offset:24576\n" \
"v_mfma_f32_16x16x32_bf16 v[188:191], a[232:235], a[188:191], v[188:191]\n" \
"s_waitcnt vmcnt(63) lgkmcnt(5)\n" \
"s_nop 0\n" \
"v_mfma_f32_16x16x32_bf16 v[4:7], a[236:239], a[128:131], v[4:7]\n" \
"ds_read_b64_tr_b16 a[202:203], v227 offset:26624\n" \
"buffer_load_dwordx4 v[32:35], v208, %[srdz], s50 offen offset:0 sc1\n" \
"v_mfma_f32_16x16x32_bf16 v[4:7], a[208:211], a[132:135], v[4:7]\n" \
"ds_read_b64_tr_b16 a[204:205], v227 offset:28672\n" \
"v_mfma_f32_16x16x32_bf16 v[36:39], a[236:239], a[144:147], v[36:39]\n" \
"ds_read_b64_tr_b16 a[206:207], v227 offset:30720\n" \
"v_mfma_f32_16x16x32_bf16 v[36:39], a[208:211], a[148:151], v[36:39]\n" \
"buffer_load_dwordx4 v[40:43], v208, %[srdz], s50 offen offset:1024 sc1\n" \
"v_mfma_f32_16x16x32_bf16 v[68:71], a[236:239], a[160:163], v[68:71]\n" \
"v_mfma_f32_16x16x32_bf16 v[68:71], a[208:211], a[164:167], v[68:71]\n" \
"buffer_load_dwordx4 v[48:51], v208, %[srdz], s50 offen offset:2048 sc1\n" \
"v_mfma_f32_16x16x32_bf16 v[100:103], a[236:239], a[176:179], v[100:103]\n" \
"ds_read_b64_tr_b16 a[220:221], v215 offset:16896\n" \
"v_mfma_f32_16x16x32_bf16 v[100:103], a[208:211], a[180:183], v[100:103]\n" \
"ds_read_b64_tr_b16 a[222:223], v215 offset:18944\n" \
".if . - adaln_body != 49152\n" \
".error \"size model\"\n" \
".endif\n" \
"Lk11_%=:\n" \
"s_cbranch_execz adaln_kr\n" \
"s_nop 0\n" \
"v_mfma_f32_16x16x32_bf16 v[4:7], a[212:215], a[136:139], v[4:7]\n" \
"ds_read_b64_tr_b16 a[224:225], v215 offset:20992\n" \
"v_mfma_f32_16x16x32_bf16 v[4:7], a[228:231], a[140:143], v[4:7]\n" \
"ds_read_b64_tr_b16 a[226:227], v215 offset:23040\n" \
"v_mfma_f32_16x16x32_bf16 v[36:39], a[212:215], a[152:155], v[36:39]\n" \
"buffer_load_dwordx4 v214, %[srdt], s49 offen offset:3072 lds\n" \
"v_mfma_f32_16x16x32_bf16 v[36:39], a[228:231], a[156:159], v[36:39]\n" \
"v_mfma_f32_16x16x32_bf16 v[68:71], a[212:215], a[168:171], v[68:71]\n" \
"buffer_load_dwordx4 v[56:59], v208, %[srdz], s50 offen offset:3072 sc1\n" \
"v_mfma_f32_16x16x32_bf16 v[68:71], a[228:231], a[172:175], v[68:71]\n" \
"v_mfma_f32_16x16x32_bf16 v[100:103], a[212:215], a[184:187], v[100:103]\n" \
"ds_read_b64_tr_b16 a[232:233], v215 offset:25088\n" \
"v_mfma_f32_16x16x32_bf16 v[100:103], a[228:231], a[188:191], v[100:103]\n" \
"s_waitcnt vmcnt(63) lgkmcnt(5)\n" \
"s_nop 0\n" \
"v_mfma_f32_16x16x32_bf16 v[12:15], a[216:219], a[128:131], v[12:15]\n" \
"ds_read_b64_tr_b16 a[234:235], v215 offset:27136\n" \
"buffer_load_dwordx4 v[64:67], v209, %[srdz], s50 offen offset:0 sc1\n" \
"v_mfma_f32_16x16x32_bf16 v[12:15], a[196:199], a[132:135], v[12:15]\n" \
"ds_read_b64_tr_b16 a[236:237], v215 offset:29184\n" \
"v_mfma_f32_16x16x32_bf16 v[44:47], a[216:219], a[144:147], v[44:47]\n" \
"ds_read_b64_tr_b16 a[238:239], v215 offset:31232\n" \
"v_mfma_f32_16x16x32_bf16 v[44:47], a[196:199], a[148:151], v[44:47]\n" \
"buffer_load_dwordx4 v[72:75], v209, %[srdz], s50 offen offset:1024 sc1\n" \
"v_mfma_f32_16x16x32_bf16 v[76:79], a[216:219], a[160:163], v[76:79]\n" \
"v_mfma_f32_16x16x32_bf16 v[76:79], a[196:199], a[164:167], v[76:79]\n" \
"buffer_load_dwordx4 v[80:83], v209, %[srdz], s50 offen offset:2048 sc1\n" \
"v_mfma_f32_16x16x32_bf16 v[108:111], a[216:219], a[176:179], v[108:111]\n" \
"ds_read_b64_tr_b16 a[208:209], v227 offset:16896\n" \
"v_mfma_f32_16x16x32_bf16 v[108:111], a[196:199], a[180:183], v[108:111]\n" \
"ds_read_b64_tr_b16 a[210:211], v227 offset:18944\n" \
"v_mfma_f32_16x16x32_bf16 v[12:15], a[200:203], a[136:139], v[12:15]\n" \
"ds_read_b64_tr_b16 a[212:213], v227 offset:25088\n" \
"v_mfma_f32_16x16x32_bf16 v[12:15], a[204:207], a[140:143], v[12:15]\n" \
"ds_read_b64_tr_b16 a[214:215], v227 offset:27136\n" \
"v_mfma_f32_16x16x32_bf16 v[44:47], a[200:203], a[152:155], v[44:47]\n" \
"ds_read_b64_tr_b16 a[228:229], v227 offset:20992\n" \
"v_mfma_f32_16x16x32_bf16 v[44:47], a[204:207], a[156:159], v[44:47]\n" \
"ds_read_b64_tr_b16 a[230:231], v227 offset:23040\n" \
"buffer_load_dwordx4 v[88:91], v209, %[srdz], s50 offen offset:3072 sc1\n" \
"v_mfma_f32_16x16x32_bf16 v[76:79], a[200:203], a[168:171], v[76:79]\n" \
"ds_read_b64_tr_b16 a[216:217], v227 offset:29184\n" \
"v_mfma_f32_16x16x32_bf16 v[76:79], a[204:207], a[172:175], v[76:79]\n" \
"ds_read_b64_tr_b16 a[218:219], v227 offset:31232\n" \
"v_mfma_f32_16x16x32_bf16 v[108:111], a[200:203], a[184:187], v[108:111]\n" \
"buffer_load_dwordx4 v[96:99], v210, %[srdz], s50 offen offset:0 sc1\n" \
"v_mfma_f32_16x16x32_bf16 v[108:111], a[204:207], a[188:191], v[108:111]\n" \
"s_waitcnt vmcnt(63) lgkmcnt(12)\n" \
"s_nop 0\n" \
"v_mfma_f32_16x16x32_bf16 v[20:23], a[220:223], a[128:131], v[20:23]\n" \
"s_add_u32 m0, %[m0b], 114688\n" \
"s_cmp_gt_u32 s48, 5\n" \
"s_cselect_b32 s49, 5, 6\n" \
"s_lshl_b32 s49, s49, 18\n" \
"s_or_b32 s49, s49, s58\n" \
"buffer_load_dwordx4 %[wo0], %[srdt], s49 offen offset:0 lds\n" \
"v_mfma_f32_16x16x32_bf16 v[20:23], a[224:227], a[132:135], v[20:23]\n" \
"v_mfma_f32_16x16x32_bf16 v[52:55], a[220:223], a[144:147], v[52:55]\n" \
"buffer_load_dwordx4 v[104:107], v210, %[srdz], s50 offen offset:1024 sc1\n" \
"v_mfma_f32_16x16x32_bf16 v[52:55], a[224:227], a[148:151], v[52:55]\n" \
"v_mfma_f32_16x16x32_bf16 v[84:87], a[220:223], a[160:163], v[84:87]\n" \
"s_waitcnt vmcnt(24) lgkmcnt(0)\n" \
"s_barrier\n" \
"v_mfma_f32_16x16x32_bf16 v[84:87], a[224:227], a[164:167], v[84:87]\n" \
"ds_read_b64_tr_b16 a[196:197], v215 offset:32768\n" \
"v_mfma_f32_16x16x32_bf16 v[116:119], a[220:223], a[176:179], v[116:119]\n" \
"ds_read_b64_tr_b16 a[198:199], v215 offset:34816\n" \
"v_mfma_f32_16x16x32_bf16 v[116:119], a[224:227], a[180:183], v[116:119]\n" \
"ds_read_b64_tr_b16 a[200:201], v215 offset:36864\n" \
"buffer_load_dwordx4 v[112:115], v210, %[srdz], s50 offen offset:2048 sc1\n" \
"v_mfma_f32_16x16x32_bf16 v[20:23], a[232:235], a[136:139], v[20:23]\n" \
"ds_read_b64_tr_b16 a[202:203], v215 offset:38912\n" \
"v_mfma_f32_16x16x32_bf16 v[20:23], a[236:239], a[140:143], v[20:23]\n" \
"ds_read_b64_tr_b16 a[204:205], v215 offset:40960\n" \
"v_mfma_f32_16x16x32_bf16 v[52:55], a[232:235], a[152:155], v[52:55]\n" \
"ds_read_b64_tr_b16 a[206:207], v215 offset:43008\n" \
"v_mfma_f32_16x16x32_bf16 v[52:55], a[236:239], a[156:159], v[52:55]\n" \
"ds_read_b64_tr_b16 a[220:221], v215 offset:45056\n" \
"buffer_load_dwordx4 v[120:123], v210, %[srdz], s50 offen offset:3072 sc1\n" \
"v_mfma_f32_16x16x32_bf16 v[84:87], a[232:235], a[168:171], v[84:87]\n" \
"ds_read_b64_tr_b16 a[222:223], v215 offset:47104\n" \
"v_readlane_b32 s54, %[thi], 16\n" \
"v_mfma_f32_16x16x32_bf16 v[84:87], a[236:239], a[172:175], v[84:87]\n" \
"v_mfma_f32_16x16x32_bf16 v[116:119], a[232:235], a[184:187], v[116:119]\n" \
"v_mfma_f32_16x16x32_bf16 v[116:119], a[236:239], a[188:191], v[116:119]\n" \
"s_waitcnt vmcnt(23) lgkmcnt(4)\n" \
"s_nop 0\n" \
"v_mfma_f32_16x16x32_bf16 v[28:31], a[208:211], a[128:131], v[28:31]\n" \
"buffer_load_dwordx4 v212, %[srdt], s49 offen offset:1024 lds\n" \
"v_mfma_f32_16x16x32_bf16 v[28:31], a[228:231], a[132:135], v[28:31]\n" \
"v_mfma_f32_16x16x32_bf16 v[60:63], a[208:211], a[144:147], v[60:63]\n" \
"s_cmp_gt_u32 s48, 5\n" \
"s_cselect_b32 s50, 5, 6\n" \
"s_mul_i32 s50, s50, 262144\n" \
"buffer_load_dwordx4 a[0:3], %[oq0], %[srdz], s50 offen offset:0 sc1\n" \
"v_mfma_f32_16x16x32_bf16 v[60:63], a[228:231], a[148:151], v[60:63]\n" \
"v_mfma_f32_16x16x32_bf16 v[92:95], a[208:211], a[160:163], v[92:95]\n" \
"buffer_load_dword v232, v226, %[srdz], s54 offen sc1\n" \
"v_mfma_f32_16x16x32_bf16 v[92:95], a[228:231], a[164:167], v[92:95]\n" \
"v_mfma_f32_16x16x32_bf16 v[124:127], a[208:211], a[176:179], v[124:127]\n" \
"ds_read_b64_tr_b16 a[224:225], v227 offset:32768\n" \
"buffer_load_dwordx4 a[4:7], %[oq0], %[srdz], s50 offen offset:1024 sc1\n" \
"v_mfma_f32_16x16x32_bf16 v[124:127], a[228:231], a[180:183], v[124:127]\n" \
"ds_read_b64_tr_b16 a[226:227], v227 offset:34816\n" \
"v_mfma_f32_16x16x32_bf16 v[192:195], a[196:199], a[64:67], v[128:131]\n" \
"ds_read_b64_tr_b16 a[232:233], v227 offset:36864\n" \
"v_mfma_f32_16x16x32_bf16 v[192:195], a[200:203], a[68:71], v[192:195]\n" \
"ds_read_b64_tr_b16 a[234:235], v227 offset:38912\n" \
"v_mfma_f32_16x16x32_bf16 v[196:199], a[196:199], a[80:83], v[132:135]\n" \
"buffer_load_dwordx4 a[8:11], %[oq0], %[srdz], s50 offen offset:2048 sc1\n" \
"v_mfma_f32_16x16x32_bf16 v[196:199], a[200:203], a[84:87], v[196:199]\n" \
"v_mfma_f32_16x16x32_bf16 v[200:203], a[196:199], a[96:99], v[136:139]\n" \
"buffer_load_dwordx4 a[12:15], %[oq0], %[srdz], s50 offen offset:3072 sc1\n" \
"v_mfma_f32_16x16x32_bf16 v[200:203], a[200:203], a[100:103], v[200:203]\n" \
"v_mfma_f32_16x16x32_bf16 v[204:207], a[196:199], a[112:115], v[140:143]\n" \
"ds_read_b64_tr_b16 a[236:237], v227 offset:40960\n" \
"v_mfma_f32_16x16x32_bf16 v[204:207], a[200:203], a[116:119], v[204:207]\n" \
"s_waitcnt vmcnt(26) lgkmcnt(5)\n" \
"s_nop 0\n" \
"v_mfma_f32_16x16x32_bf16 v[192:195], a[204:207], a[72:75], v[192:195]\n" \
"ds_read_b64_tr_b16 a[238:239], v227 offset:43008\n" \
"v_mfma_f32_16x16x32_bf16 v[192:195], a[220:223], a[76:79], v[192:195]\n" \
"ds_read_b64_tr_b16 a[208:209], v227 offset:45056\n" \
"buffer_load_dwordx4 v213, %[srdt], s49 offen offset:2048 lds\n" \
"v_mfma_f32_16x16x32_bf16 v[196:199], a[204:207], a[88:91], v[196:199]\n" \
"ds_read_b64_tr_b16 a[210:211], v227 offset:47104\n" \
"v_mfma_f32_16x16x32_bf16 v[196:199], a[220:223], a[92:95], v[196:199]\n" \
"buffer_load_dwordx4 a[16:19], v208, %[srdz], s50 offen offset:0 sc1\n" \
"v_mfma_f32_16x16x32_bf16 v[200:203], a[204:207], a[104:107], v[200:203]\n" \
"v_mfma_f32_16x16x32_bf16 v[200:203], a[220:223], a[108:111], v[200:203]\n" \
"buffer_load_dwordx4 a[20:23], v208, %[srdz], s50 offen offset:1024 sc1\n" \
"v_mfma_f32_16x16x32_bf16 v[204:207], a[204:207], a[120:123], v[204:207]\n" \
"ds_read_b64_tr_b16 a[228:229], v215 offset:33280\n" \
"v_mfma_f32_16x16x32_bf16 v[204:207], a[220:223], a[124:127], v[204:207]\n" \
"ds_read_b64_tr_b16 a[230:231], v215 offset:35328\n" \
"v_mfma_f32_16x16x32_bf16 v[28:31], a[212:215], a[136:139], v[28:31]\n" \
"buffer_load_dwordx4 a[24:27], v208, %[srdz], s50 offen offset:2048 sc1\n" \
"v_mfma_f32_16x16x32_bf16 v[28:31], a[216:219], a[140:143], v[28:31]\n" \
"v_mfma_f32_16x16x32_bf16 v[60:63], a[212:215], a[152:155], v[60:63]\n" \
"buffer_load_dwordx4 a[28:31], v208, %[srdz], s50 offen offset:3072 sc1\n" \
"v_mfma_f32_16x16x32_bf16 v[60:63], a[216:219], a[156:159], v[60:63]\n" \
"v_mfma_f32_16x16x32_bf16 v[92:95], a[212:215], a[168:171], v[92:95]\n" \
"v_mfma_f32_16x16x32_bf16 v[92:95], a[216:219], a[172:175], v[92:95]\n" \
"v_cmp_u_f32_e64 s[44:45], v192, v196\n" \
"buffer_load_dwordx4 v214, %[srdt], s49 offen offset:3072 lds\n" \
"v_mfma_f32_16x16x32_bf16 v[124:127], a[212:215], a[184:187], v[124:127]\n" \
"v_mfma_f32_16x16x32_bf16 v[124:127], a[216:219], a[188:191], v[124:127]\n" \
"v_cmp_u_f32_e64 s[56:57], v200, v204\n" \
"s_or_b64 s[56:57], s[56:57], s[44:45]\n" \
"s_cbranch_scc1 Lsp4_%=\n" \
"Lbk4_%=:\n" \
"s_waitcnt vmcnt(63) lgkmcnt(2)\n" \
"s_nop 0\n" \
"v_mfma_f32_16x16x32_bf16 v[144:147], a[224:227], a[64:67], v[144:147]\n" \
"ds_read_b64_tr_b16 a[212:213], v215 offset:37376\n" \
"v_mfma_f32_16x16x32_bf16 v[144:147], a[232:235], a[68:71], v[144:147]\n" \
"ds_read_b64_tr_b16 a[214:215], v215 offset:39424\n" \
"buffer_load_dwordx4 a[32:35], v209, %[srdz], s50 offen offset:0 sc1\n" \
"v_mfma_f32_16x16x32_bf16 v[148:151], a[224:227], a[80:83], v[148:151]\n" \
"ds_read_b64_tr_b16 a[196:197], v215 offset:41472\n" \
"v_mfma_f32_16x16x32_bf16 v[148:151], a[232:235], a[84:87], v[148:151]\n" \
"ds_read_b64_tr_b16 a[198:199], v215 offset:43520\n" \
"v_mfma_f32_16x16x32_bf16 v[152:155], a[224:227], a[96:99], v[152:155]\n" \
"ds_read_b64_tr_b16 a[200:201], v215 offset:45568\n" \
"v_mfma_f32_16x16x32_bf16 v[152:155], a[232:235], a[100:103], v[152:155]\n" \
"ds_read_b64_tr_b16 a[202:203], v215 offset:47616\n" \
"buffer_load_dwordx4 a[36:39], v209, %[srdz], s50 offen offset:1024 sc1\n" \
"v_mfma_f32_16x16x32_bf16 v[156:159], a[224:227], a[112:115], v[156:159]\n" \
"ds_read_b64_tr_b16 a[204:205], v227 offset:33280\n" \
"v_mfma_f32_16x16x32_bf16 v[156:159], a[232:235], a[116:119], v[156:159]\n" \
"ds_read_b64_tr_b16 a[206:207], v227 offset:35328\n" \
"v_mfma_f32_16x16x32_bf16 v[144:147], a[236:239], a[72:75], v[144:147]\n" \
"ds_read_b64_tr_b16 a[216:217], v227 offset:41472\n" \
"v_mfma_f32_16x16x32_bf16 v[144:147], a[208:211], a[76:79], v[144:147]\n" \
"ds_read_b64_tr_b16 a[218:219], v227 offset:43520\n" \
"buffer_load_dwordx4 a[40:43], v209, %[srdz], s50 offen offset:2048 sc1\n" \
"v_mfma_f32_16x16x32_bf16 v[148:151], a[236:239], a[88:91], v[148:151]\n" \
"ds_read_b64_tr_b16 a[220:221], v227 offset:37376\n" \
"v_mfma_f32_16x16x32_bf16 v[148:151], a[208:211], a[92:95], v[148:151]\n" \
"ds_read_b64_tr_b16 a[222:223], v227 offset:39424\n" \
"v_mfma_f32_16x16x32_bf16 v[152:155], a[236:239], a[104:107], v[152:155]\n" \
"ds_read_b64_tr_b16 a[224:225], v227 offset:45568\n" \
"v_mfma_f32_16x16x32_bf16 v[152:155], a[208:211], a[108:111], v[152:155]\n" \
"ds_read_b64_tr_b16 a[226:227], v227 offset:47616\n" \
"buffer_load_dwordx4 a[44:47], v209, %[srdz], s50 offen offset:3072 sc1\n" \
"v_mfma_f32_16x16x32_bf16 v[156:159], a[236:239], a[120:123], v[156:159]\n" \
"v_mfma_f32_16x16x32_bf16 v[156:159], a[208:211], a[124:127], v[156:159]\n" \
"s_waitcnt vmcnt(63) lgkmcnt(12)\n" \
"s_nop 0\n" \
"v_mfma_f32_16x16x32_bf16 v[160:163], a[228:231], a[64:67], v[160:163]\n" \
"buffer_load_dwordx4 a[48:51], v210, %[srdz], s50 offen offset:0 sc1\n" \
"v_mfma_f32_16x16x32_bf16 v[160:163], a[212:215], a[68:71], v[160:163]\n" \
"v_mfma_f32_16x16x32_bf16 v[164:167], a[228:231], a[80:83], v[164:167]\n" \
"buffer_load_dwordx4 a[52:55], v210, %[srdz], s50 offen offset:1024 sc1\n" \
"v_mfma_f32_16x16x32_bf16 v[164:167], a[212:215], a[84:87], v[164:167]\n" \
"v_mfma_f32_16x16x32_bf16 v[168:171], a[228:231], a[96:99], v[168:171]\n" \
"s_waitcnt vmcnt(28) lgkmcnt(0)\n" \
"s_barrier\n" \
"v_mfma_f32_16x16x32_bf16 v[168:171], a[212:215], a[100:103], v[168:171]\n" \
"ds_read_b64_tr_b16 a[232:233], v220 offset:32768\n" \
"v_mfma_f32_16x16x32_bf16 v[172:175], a[228:231], a[112:115], v[172:175]\n" \
"ds_read_b64_tr_b16 a[234:235], v220 offset:34816\n" \
"v_mfma_f32_16x16x32_bf16 v[172:175], a[212:215], a[116:119], v[172:175]\n" \
"ds_read_b64_tr_b16 a[236:237], v220 offset:36864\n" \
"v_mfma_f32_16x16x32_bf16 v[160:163], a[196:199], a[72:75], v[160:163]\n" \
"ds_read_b64_tr_b16 a[238:239], v220 offset:38912\n" \
"v_mfma_f32_16x16x32_bf16 v[160:163], a[200:203], a[76:79], v[160:163]\n" \
"ds_read_b64_tr_b16 a[208:209], v220 offset:40960\n" \
"s_add_u32 m0, %[m0b], 49152\n" \
"s_cmp_gt_u32 s48, 6\n" \
"s_cselect_b32 s49, 6, 7\n" \
"s_lshl_b32 s49, s49, 18\n" \
"s_or_b32 s49, s49, s58\n" \
"buffer_load_dwordx4 %[wo0], %[srdt], s49 offen offset:0 lds\n" \
"v_mfma_f32_16x16x32_bf16 v[164:167], a[196:199], a[88:91], v[164:167]\n" \
"ds_read_b64_tr_b16 a[210:211], v220 offset:43008\n" \
"v_mfma_f32_16x16x32_bf16 v[164:167], a[200:203], a[92:95], v[164:167]\n" \
"ds_read_b64_tr_b16 a[228:229], v220 offset:45056\n" \
"buffer_load_dwordx4 a[56:59], v210, %[srdz], s50 offen offset:2048 sc1\n" \
"v_mfma_f32_16x16x32_bf16 v[168:171], a[196:199], a[104:107], v[168:171]\n" \
"ds_read_b64_tr_b16 a[230:231], v220 offset:47104\n" \
"v_mfma_f32_16x16x32_bf16 v[168:171], a[200:203], a[108:111], v[168:171]\n" \
"buffer_load_dwordx4 a[60:63], v210, %[srdz], s50 offen offset:3072 sc1\n" \
"v_mfma_f32_16x16x32_bf16 v[172:175], a[196:199], a[120:123], v[172:175]\n" \
"v_mfma_f32_16x16x32_bf16 v[172:175], a[200:203], a[124:127], v[172:175]\n" \
"s_waitcnt vmcnt(23) lgkmcnt(4)\n" \
"s_nop 0\n" \
"v_mfma_f32_16x16x32_bf16 v[176:179], a[204:207], a[64:67], v[176:179]\n" \
"v_mfma_f32_16x16x32_bf16 v[176:179], a[220:223], a[68:71], v[176:179]\n" \
"s_cmp_gt_u32 s48, 6\n" \
"s_cselect_b32 s50, 6, 7\n" \
"s_mul_i32 s50, s50, 262144\n" \
"buffer_load_dwordx4 a[128:131], %[oq0], %[srdz], s50 offen offset:0 sc1\n" \
"v_mfma_f32_16x16x32_bf16 v[180:183], a[204:207], a[80:83], v[180:183]\n" \
"v_mfma_f32_16x16x32_bf16 v[180:183], a[220:223], a[84:87], v[180:183]\n" \
"v_mfma_f32_16x16x32_bf16 v[184:187], a[204:207], a[96:99], v[184:187]\n" \
"v_mfma_f32_16x16x32_bf16 v[184:187], a[220:223], a[100:103], v[184:187]\n" \
"buffer_load_dwordx4 v212, %[srdt], s49 offen offset:1024 lds\n" \
"v_mfma_f32_16x16x32_bf16 v[188:191], a[204:207], a[112:115], v[188:191]\n" \
"ds_read_b64_tr_b16 a[212:213], v221 offset:32768\n" \
"v_mfma_f32_16x16x32_bf16 v[188:191], a[220:223], a[116:119], v[188:191]\n" \
"ds_read_b64_tr_b16 a[214:215], v221 offset:34816\n" \
"v_mfma_f32_16x16x32_bf16 v[128:131], a[232:235], v[0:3], v[192:195]\n" \
"ds_read_b64_tr_b16 a[196:197], v221 offset:36864\n" \
"buffer_load_dwordx4 a[132:135], %[oq0], %[srdz], s50 offen offset:1024 sc1\n" \
"v_mfma_f32_16x16x32_bf16 v[128:131], a[236:239], v[8:11], v[128:131]\n" \
"ds_read_b64_tr_b16 a[198:199], v221 offset:38912\n" \
"v_mfma_f32_16x16x32_bf16 v[132:135], a[232:235], v[32:35], v[196:199]\n" \
"buffer_load_dwordx4 a[136:139], %[oq0], %[srdz], s50 offen offset:2048 sc1\n" \
"v_mfma_f32_16x16x32_bf16 v[132:135], a[236:239], v[40:43], v[132:135]\n" \
"v_mfma_f32_16x16x32_bf16 v[136:139], a[232:235], v[64:67], v[200:203]\n" \
"v_mfma_f32_16x16x32_bf16 v[136:139], a[236:239], v[72:75], v[136:139]\n" \
"buffer_load_dwordx4 a[140:143], %[oq0], %[srdz], s50 offen offset:3072 sc1\n" \
"v_mfma_f32_16x16x32_bf16 v[140:143], a[232:235], v[96:99], v[204:207]\n" \
"ds_read_b64_tr_b16 a[200:201], v221 offset:40960\n" \
"v_mfma_f32_16x16x32_bf16 v[140:143], a[236:239], v[104:107], v[140:143]\n" \
"s_waitcnt vmcnt(26) lgkmcnt(5)\n" \
"s_nop 0\n" \
"v_mfma_f32_16x16x32_bf16 v[128:131], a[208:211], v[16:19], v[128:131]\n" \
"ds_read_b64_tr_b16 a[202:203], v221 offset:43008\n" \
"v_mfma_f32_16x16x32_bf16 v[128:131], a[228:231], v[24:27], v[128:131]\n" \
"ds_read_b64_tr_b16 a[204:205], v221 offset:45056\n" \
"v_mfma_f32_16x16x32_bf16 v[132:135], a[208:211], v[48:51], v[132:135]\n" \
"ds_read_b64_tr_b16 a[206:207], v221 offset:47104\n" \
"v_mfma_f32_16x16x32_bf16 v[132:135], a[228:231], v[56:59], v[132:135]\n" \
"buffer_load_dwordx4 a[144:147], v208, %[srdz], s50 offen offset:0 sc1\n" \
"v_mfma_f32_16x16x32_bf16 v[136:139], a[208:211], v[80:83], v[136:139]\n" \
"v_mfma_f32_16x16x32_bf16 v[136:139], a[228:231], v[88:91], v[136:139]\n" \
"buffer_load_dwordx4 v213, %[srdt], s49 offen offset:2048 lds\n" \
"v_mfma_f32_16x16x32_bf16 v[140:143], a[208:211], v[112:115], v[140:143]\n" \
"ds_read_b64_tr_b16 a[220:221], v220 offset:33280\n" \
"v_mfma_f32_16x16x32_bf16 v[140:143], a[228:231], v[120:123], v[140:143]\n" \
"ds_read_b64_tr_b16 a[222:223], v220 offset:35328\n" \
"v_mfma_f32_16x16x32_bf16 v[176:179], a[216:219], a[72:75], v[176:179]\n" \
"buffer_load_dwordx4 a[148:151], v208, %[srdz], s50 offen offset:1024 sc1\n" \
"v_mfma_f32_16x16x32_bf16 v[176:179], a[224:227], a[76:79], v[176:179]\n" \
"v_mfma_f32_16x16x32_bf16 v[180:183], a[216:219], a[88:91], v[180:183]\n" \
"buffer_load_dwordx4 a[152:155], v208, %[srdz], s50 offen offset:2048 sc1\n" \
"v_mfma_f32_16x16x32_bf16 v[180:183], a[224:227], a[92:95], v[180:183]\n" \
"v_mfma_f32_16x16x32_bf16 v[184:187], a[216:219], a[104:107], v[184:187]\n" \
"v_mfma_f32_16x16x32_bf16 v[184:187], a[224:227], a[108:111], v[184:187]\n" \
"v_cmp_u_f32_e64 s[44:45], v128, v132\n" \
"buffer_load_dwordx4 a[156:159], v208, %[srdz], s50 offen offset:3072 sc1\n" \
"v_mfma_f32_16x16x32_bf16 v[188:191], a[216:219], a[120:123], v[188:191]\n" \
"v_mfma_f32_16x16x32_bf16 v[188:191], a[224:227], a[124:127], v[188:191]\n" \
"v_cmp_u_f32_e64 s[56:57], v136, v140\n" \
"s_or_b64 s[56:57], s[56:57], s[44:45]\n" \
"s_cbranch_scc1 Lsp5_%=\n" \
"Lbk5_%=:\n" \
"s_waitcnt vmcnt(63) lgkmcnt(2)\n" \
"s_nop 0\n" \
"v_mfma_f32_16x16x32_bf16 v[144:147], a[212:215], v[0:3], v[144:147]\n" \
"ds_read_b64_tr_b16 a[216:217], v220 offset:37376\n" \
"v_mfma_f32_16x16x32_bf16 v[144:147], a[196:199], v[8:11], v[144:147]\n" \
"ds_read_b64_tr_b16 a[218:219], v220 offset:39424\n" \
"v_mfma_f32_16x16x32_bf16 v[148:151], a[212:215], v[32:35], v[148:151]\n" \
"ds_read_b64_tr_b16 a[208:209], v220 offset:41472\n" \
"v_mfma_f32_16x16x32_bf16 v[148:151], a[196:199], v[40:43], v[148:151]\n" \
"ds_read_b64_tr_b16 a[210:211], v220 offset:43520\n" \
"v_mfma_f32_16x16x32_bf16 v[152:155], a[212:215], v[64:67], v[152:155]\n" \
"ds_read_b64_tr_b16 a[224:225], v220 offset:45568\n" \
"buffer_load_dwordx4 a[160:163], v209, %[srdz], s50 offen offset:0 sc1\n" \
"v_mfma_f32_16x16x32_bf16 v[152:155], a[196:199], v[72:75], v[152:155]\n" \
"ds_read_b64_tr_b16 a[226:227], v220 offset:47616\n" \
"v_mfma_f32_16x16x32_bf16 v[156:159], a[212:215], v[96:99], v[156:159]\n" \
"ds_read_b64_tr_b16 a[228:229], v221 offset:33280\n" \
"buffer_load_dwordx4 v214, %[srdt], s49 offen offset:3072 lds\n" \
"v_mfma_f32_16x16x32_bf16 v[156:159], a[196:199], v[104:107], v[156:159]\n" \
"ds_read_b64_tr_b16 a[230:231], v221 offset:35328\n" \
"v_mfma_f32_16x16x32_bf16 v[144:147], a[200:203], v[16:19], v[144:147]\n" \
"ds_read_b64_tr_b16 a[232:233], v221 offset:41472\n" \
"buffer_load_dwordx4 a[164:167], v209, %[srdz], s50 offen offset:1024 sc1\n" \
"v_mfma_f32_16x16x32_bf16 v[144:147], a[204:207], v[24:27], v[144:147]\n" \
"ds_read_b64_tr_b16 a[234:235], v221 offset:43520\n" \
"v_mfma_f32_16x16x32_bf16 v[148:151], a[200:203], v[48:51], v[148:151]\n" \
"ds_read_b64_tr_b16 a[236:237], v221 offset:37376\n" \
"v_mfma_f32_16x16x32_bf16 v[148:151], a[204:207], v[56:59], v[148:151]\n" \
"ds_read_b64_tr_b16 a[238:239], v221 offset:39424\n" \
"v_mfma_f32_16x16x32_bf16 v[152:155], a[200:203], v[80:83], v[152:155]\n" \
"ds_read_b64_tr_b16 a[212:213], v221 offset:45568\n" \
"buffer_load_dwordx4 a[168:171], v209, %[srdz], s50 offen offset:2048 sc1\n" \
"v_mfma_f32_16x16x32_bf16 v[152:155], a[204:207], v[88:91], v[152:155]\n" \
"ds_read_b64_tr_b16 a[214:215], v221 offset:47616\n" \
"v_mfma_f32_16x16x32_bf16 v[156:159], a[200:203], v[112:115], v[156:159]\n" \
"buffer_load_dwordx4 a[172:175], v209, %[srdz], s50 offen offset:3072 sc1\n" \
"v_mfma_f32_16x16x32_bf16 v[156:159], a[204:207], v[120:123], v[156:159]\n" \
"s_waitcnt vmcnt(63) lgkmcnt(12)\n" \
"s_nop 0\n" \
"v_mfma_f32_16x16x32_bf16 v[160:163], a[220:223], v[0:3], v[160:163]\n" \
"v_mfma_f32_16x16x32_bf16 v[160:163], a[216:219], v[8:11], v[160:163]\n" \
"buffer_load_dwordx4 a[176:179], v210, %[srdz], s50 offen offset:0 sc1\n" \
"v_mfma_f32_16x16x32_bf16 v[164:167], a[220:223], v[32:35], v[164:167]\n" \
"v_mfma_f32_16x16x32_bf16 v[164:167], a[216:219], v[40:43], v[164:167]\n" \
"v_mfma_f32_16x16x32_bf16 v[168:171], a[220:223], v[64:67], v[168:171]\n" \
"s_waitcnt vmcnt(25) lgkmcnt(0)\n" \
"s_barrier\n" \
"v_mfma_f32_16x16x32_bf16 v[168:171], a[216:219], v[72:75], v[168:171]\n" \
"ds_read_b64_tr_b16 a[196:197], v220 offset:49152\n" \
"v_mfma_f32_16x16x32_bf16 v[172:175], a[220:223], v[96:99], v[172:175]\n" \
"ds_read_b64_tr_b16 a[198:199], v220 offset:51200\n" \
"v_mfma_f32_16x16x32_bf16 v[172:175], a[216:219], v[104:107], v[172:175]\n" \
"ds_read_b64_tr_b16 a[200:201], v220 offset:53248\n" \
"v_mfma_f32_16x16x32_bf16 v[160:163], a[208:211], v[16:19], v[160:163]\n" \
"ds_read_b64_tr_b16 a[202:203], v220 offset:55296\n" \
"buffer_load_dwordx4 a[180:183], v210, %[srdz], s50 offen offset:1024 sc1\n" \
"v_mfma_f32_16x16x32_bf16 v[160:163], a[224:227], v[24:27], v[160:163]\n" \
"ds_read_b64_tr_b16 a[204:205], v220 offset:57344\n" \
"v_mfma_f32_16x16x32_bf16 v[164:167], a[208:211], v[48:51], v[164:167]\n" \
"ds_read_b64_tr_b16 a[206:207], v220 offset:59392\n" \
"v_mfma_f32_16x16x32_bf16 v[164:167], a[224:227], v[56:59], v[164:167]\n" \
"ds_read_b64_tr_b16 a[220:221], v220 offset:61440\n" \
"v_mfma_f32_16x16x32_bf16 v[168:171], a[208:211], v[80:83], v[168:171]\n" \
"ds_read_b64_tr_b16 a[222:223], v220 offset:63488\n" \
"buffer_load_dwordx4 a[184:187], v210, %[srdz], s50 offen offset:2048 sc1\n" \
"v_mfma_f32_16x16x32_bf16 v[168:171], a[224:227], v[88:91], v[168:171]\n" \
"v_mfma_f32_16x16x32_bf16 v[172:175], a[208:211], v[112:115], v[172:175]\n" \
"buffer_load_dwordx4 a[188:191], v210, %[srdz], s50 offen offset:3072 sc1\n" \
"v_mfma_f32_16x16x32_bf16 v[172:175], a[224:227], v[120:123], v[172:175]\n" \
"s_waitcnt vmcnt(63) lgkmcnt(4)\n" \
"s_nop 0\n" \
"v_mfma_f32_16x16x32_bf16 v[176:179], a[228:231], v[0:3], v[176:179]\n" \
"v_mfma_f32_16x16x32_bf16 v[176:179], a[236:239], v[8:11], v[176:179]\n" \
"v_mfma_f32_16x16x32_bf16 v[180:183], a[228:231], v[32:35], v[180:183]\n" \
"v_mfma_f32_16x16x32_bf16 v[180:183], a[236:239], v[40:43], v[180:183]\n" \
"v_mfma_f32_16x16x32_bf16 v[184:187], a[228:231], v[64:67], v[184:187]\n" \
"v_mfma_f32_16x16x32_bf16 v[184:187], a[236:239], v[72:75], v[184:187]\n" \
"v_mfma_f32_16x16x32_bf16 v[188:191], a[228:231], v[96:99], v[188:191]\n" \
"ds_read_b64_tr_b16 a[216:217], v221 offset:49152\n" \
"s_waitcnt vmcnt(22)\n" \
"v_cmp_eq_u32_e32 vcc, 0, v232\n" \
"s_cbranch_vccnz Lspp_%=\n" \
"Lbkp_%=:\n" \
"s_nop 0\n" \
"v_mfma_f32_16x16x32_bf16 v[188:191], a[236:239], v[104:107], v[188:191]\n" \
"ds_read_b64_tr_b16 a[218:219], v221 offset:51200\n" \
"v_mfma_f32_16x16x32_bf16 v[192:195], a[196:199], a[0:3], v[128:131]\n" \
"ds_read_b64_tr_b16 a[208:209], v221 offset:53248\n" \
"v_mfma_f32_16x16x32_bf16 v[192:195], a[200:203], a[4:7], v[192:195]\n" \
"ds_read_b64_tr_b16 a[210:211], v221 offset:55296\n" \
"s_add_u32 m0, %[m0b], 0\n" \
"s_cmp_gt_u32 s48, 3\n" \
"s_cselect_b32 s49, 3, 4\n" \
"s_lshl_b32 s49, s49, 18\n" \
"s_or_b32 s49, s49, s59\n" \
"s_nop 0\n" \
"buffer_load_dwordx4 %[wo0], %[srdt], s49 offen offset:0 lds\n" \
"v_mfma_f32_16x16x32_bf16 v[196:199], a[196:199], a[16:19], v[132:135]\n" \
"v_mfma_f32_16x16x32_bf16 v[196:199], a[200:203], a[20:23], v[196:199]\n" \
"v_mfma_f32_16x16x32_bf16 v[200:203], a[196:199], a[32:35], v[136:139]\n" \
"v_mfma_f32_16x16x32_bf16 v[200:203], a[200:203], a[36:39], v[200:203]\n" \
"v_mfma_f32_16x16x32_bf16 v[204:207], a[196:199], a[48:51], v[140:143]\n" \
"ds_read_b64_tr_b16 a[224:225], v221 offset:57344\n" \
"v_mfma_f32_16x16x32_bf16 v[204:207], a[200:203], a[52:55], v[204:207]\n" \
"s_waitcnt vmcnt(20) lgkmcnt(5)\n" \
"s_nop 0\n" \
"v_mfma_f32_16x16x32_bf16 v[192:195], a[204:207], a[8:11], v[192:195]\n" \
"ds_read_b64_tr_b16 a[226:227], v221 offset:59392\n" \
"v_mfma_f32_16x16x32_bf16 v[192:195], a[220:223], a[12:15], v[192:195]\n" \
"ds_read_b64_tr_b16 a[228:229], v221 offset:61440\n" \
"v_mfma_f32_16x16x32_bf16 v[196:199], a[204:207], a[24:27], v[196:199]\n" \
"ds_read_b64_tr_b16 a[230:231], v221 offset:63488\n" \
"v_mfma_f32_16x16x32_bf16 v[196:199], a[220:223], a[28:31], v[196:199]\n" \
"buffer_load_dwordx4 v212, %[srdt], s49 offen offset:1024 lds\n" \
"v_mfma_f32_16x16x32_bf16 v[200:203], a[204:207], a[40:43], v[200:203]\n" \
"v_mfma_f32_16x16x32_bf16 v[200:203], a[220:223], a[44:47], v[200:203]\n" \
"v_mfma_f32_16x16x32_bf16 v[204:207], a[204:207], a[56:59], v[204:207]\n" \
"ds_read_b64_tr_b16 a[236:237], v220 offset:49664\n" \
"v_mfma_f32_16x16x32_bf16 v[204:207], a[220:223], a[60:63], v[204:207]\n" \
"ds_read_b64_tr_b16 a[238:239], v220 offset:51712\n" \
"v_mfma_f32_16x16x32_bf16 v[176:179], a[232:235], v[16:19], v[176:179]\n" \
"v_mfma_f32_16x16x32_bf16 v[176:179], a[212:215], v[24:27], v[176:179]\n" \
"v_mfma_f32_16x16x32_bf16 v[180:183], a[232:235], v[48:51], v[180:183]\n" \
"buffer_load_dwordx4 v213, %[srdt], s49 offen offset:2048 lds\n" \
"v_mfma_f32_16x16x32_bf16 v[180:183], a[212:215], v[56:59], v[180:183]\n" \
"v_mfma_f32_16x16x32_bf16 v[184:187], a[232:235], v[80:83], v[184:187]\n" \
"s_add_u32 m0, %[m0b], 98304\n" \
"s_mov_b32 s61, 0\n" \
"s_nop 0\n" \
"buffer_load_dwordx4 %[vdn], s[64:67], s61 offen offset:0 lds\n" \
"v_mfma_f32_16x16x32_bf16 v[184:187], a[212:215], v[88:91], v[184:187]\n" \
"v_cmp_u_f32_e64 s[44:45], v192, v196\n" \
"v_mfma_f32_16x16x32_bf16 v[188:191], a[232:235], v[112:115], v[188:191]\n" \
"v_mfma_f32_16x16x32_bf16 v[188:191], a[212:215], v[120:123], v[188:191]\n" \
"v_cmp_u_f32_e64 s[56:57], v200, v204\n" \
"s_or_b64 s[56:57], s[56:57], s[44:45]\n" \
"s_cbranch_scc1 Lsp6_%=\n" \
"Lbk6_%=:\n" \
"s_waitcnt vmcnt(63) lgkmcnt(2)\n" \
"s_nop 0\n" \
"v_mfma_f32_16x16x32_bf16 v[144:147], a[216:219], a[0:3], v[144:147]\n" \
"ds_read_b64_tr_b16 a[232:233], v220 offset:53760\n" \
"v_mfma_f32_16x16x32_bf16 v[144:147], a[208:211], a[4:7], v[144:147]\n" \
"ds_read_b64_tr_b16 a[234:235], v220 offset:55808\n" \
"v_mfma_f32_16x16x32_bf16 v[148:151], a[216:219], a[16:19], v[148:151]\n" \
"ds_read_b64_tr_b16 a[196:197], v220 offset:57856\n" \
"v_mfma_f32_16x16x32_bf16 v[148:151], a[208:211], a[20:23], v[148:151]\n" \
"ds_read_b64_tr_b16 a[198:199], v220 offset:59904\n" \
"v_mfma_f32_16x16x32_bf16 v[152:155], a[216:219], a[32:35], v[152:155]\n" \
"ds_read_b64_tr_b16 a[200:201], v220 offset:61952\n" \
"v_mfma_f32_16x16x32_bf16 v[152:155], a[208:211], a[36:39], v[152:155]\n" \
"ds_read_b64_tr_b16 a[202:203], v220 offset:64000\n" \
"v_mfma_f32_16x16x32_bf16 v[156:159], a[216:219], a[48:51], v[156:159]\n" \
"ds_read_b64_tr_b16 a[204:205], v221 offset:49664\n" \
"v_mfma_f32_16x16x32_bf16 v[156:159], a[208:211], a[52:55], v[156:159]\n" \
"ds_read_b64_tr_b16 a[206:207], v221 offset:51712\n" \
"s_add_u32 m0, %[m0b], 0\n" \
"s_nop 0\n" \
"buffer_load_dwordx4 v214, %[srdt], s49 offen offset:3072 lds\n" \
"v_mfma_f32_16x16x32_bf16 v[144:147], a[224:227], a[8:11], v[144:147]\n" \
"ds_read_b64_tr_b16 a[212:213], v221 offset:57856\n" \
"v_mfma_f32_16x16x32_bf16 v[144:147], a[228:231], a[12:15], v[144:147]\n" \
"ds_read_b64_tr_b16 a[214:215], v221 offset:59904\n" \
"v_mfma_f32_16x16x32_bf16 v[148:151], a[224:227], a[24:27], v[148:151]\n" \
"ds_read_b64_tr_b16 a[220:221], v221 offset:53760\n" \
"v_mfma_f32_16x16x32_bf16 v[148:151], a[228:231], a[28:31], v[148:151]\n" \
"ds_read_b64_tr_b16 a[222:223], v221 offset:55808\n" \
"v_mfma_f32_16x16x32_bf16 v[152:155], a[224:227], a[40:43], v[152:155]\n" \
"ds_read_b64_tr_b16 a[216:217], v221 offset:61952\n" \
"v_mfma_f32_16x16x32_bf16 v[152:155], a[228:231], a[44:47], v[152:155]\n" \
"ds_read_b64_tr_b16 a[218:219], v221 offset:64000\n" \
"v_mfma_f32_16x16x32_bf16 v[156:159], a[224:227], a[56:59], v[156:159]\n" \
"s_add_u32 m0, %[m0b], 99328\n" \
"s_mov_b32 s61, 4096\n" \
"buffer_load_dwordx4 %[vdn], s[64:67], s61 offen offset:0 lds\n" \
"v_mfma_f32_16x16x32_bf16 v[156:159], a[228:231], a[60:63], v[156:159]\n" \
"s_waitcnt vmcnt(63) lgkmcnt(12)\n" \
"s_nop 0\n" \
"v_mfma_f32_16x16x32_bf16 v[160:163], a[236:239], a[0:3], v[160:163]\n" \
"v_mfma_f32_16x16x32_bf16 v[160:163], a[232:235], a[4:7], v[160:163]\n" \
"v_mfma_f32_16x16x32_bf16 v[164:167], a[236:239], a[16:19], v[164:167]\n" \
"v_mfma_f32_16x16x32_bf16 v[164:167], a[232:235], a[20:23], v[164:167]\n" \
"s_add_u32 m0, %[m0b], 16384\n" \
"s_cmp_gt_u32 s48, 4\n" \
"s_cselect_b32 s49, 4, 5\n" \
"s_lshl_b32 s49, s49, 18\n" \
"s_or_b32 s49, s49, s59\n" \
"buffer_load_dwordx4 %[wo0], %[srdt], s49 offen offset:0 lds\n" \
"v_mfma_f32_16x16x32_bf16 v[168:171], a[236:239], a[32:35], v[168:171]\n" \
"s_waitcnt vmcnt(14) lgkmcnt(0)\n" \
"s_barrier\n" \
"v_mfma_f32_16x16x32_bf16 v[168:171], a[232:235], a[36:39], v[168:171]\n" \
"ds_read_b64_tr_b16 a[208:209], v215 offset:49152\n" \
"v_mfma_f32_16x16x32_bf16 v[172:175], a[236:239], a[48:51], v[172:175]\n" \
"ds_read_b64_tr_b16 a[210:211], v215 offset:51200\n" \
"v_mfma_f32_16x16x32_bf16 v[172:175], a[232:235], a[52:55], v[172:175]\n" \
"ds_read_b64_tr_b16 a[224:225], v215 offset:53248\n" \
"v_mfma_f32_16x16x32_bf16 v[160:163], a[196:199], a[8:11], v[160:163]\n" \
"ds_read_b64_tr_b16 a[226:227], v215 offset:55296\n" \
"v_mfma_f32_16x16x32_bf16 v[160:163], a[200:203], a[12:15], v[160:163]\n" \
"ds_read_b64_tr_b16 a[228:229], v215 offset:57344\n" \
"v_mfma_f32_16x16x32_bf16 v[164:167], a[196:199], a[24:27], v[164:167]\n" \
"ds_read_b64_tr_b16 a[230:231], v215 offset:59392\n" \
"v_mfma_f32_16x16x32_bf16 v[164:167], a[200:203], a[28:31], v[164:167]\n" \
"ds_read_b64_tr_b16 a[236:237], v215 offset:61440\n" \
"v_mfma_f32_16x16x32_bf16 v[168:171], a[196:199], a[40:43], v[168:171]\n" \
"ds_read_b64_tr_b16 a[238:239], v215 offset:63488\n" \
"v_mfma_f32_16x16x32_bf16 v[168:171], a[200:203], a[44:47], v[168:171]\n" \
"buffer_load_dwordx4 v212, %[srdt], s49 offen offset:1024 lds\n" \
"v_mfma_f32_16x16x32_bf16 v[172:175], a[196:199], a[56:59], v[172:175]\n" \
"v_mfma_f32_16x16x32_bf16 v[172:175], a[200:203], a[60:63], v[172:175]\n" \
"s_waitcnt vmcnt(10) lgkmcnt(4)\n" \
"s_nop 0\n" \
"v_mfma_f32_16x16x32_bf16 v[176:179], a[204:207], a[0:3], v[176:179]\n" \
"s_add_u32 m0, %[m0b], 100352\n" \
"s_mov_b32 s61, 8192\n" \
"buffer_load_dwordx4 %[vdn], s[64:67], s61 offen offset:0 lds\n" \
"v_mfma_f32_16x16x32_bf16 v[176:179], a[220:223], a[4:7], v[176:179]\n" \
"v_mfma_f32_16x16x32_bf16 v[180:183], a[204:207], a[16:19], v[180:183]\n" \
"v_mfma_f32_16x16x32_bf16 v[180:183], a[220:223], a[20:23], v[180:183]\n" \
"v_mfma_f32_16x16x32_bf16 v[184:187], a[204:207], a[32:35], v[184:187]\n" \
"v_mfma_f32_16x16x32_bf16 v[184:187], a[220:223], a[36:39], v[184:187]\n" \
"v_mfma_f32_16x16x32_bf16 v[188:191], a[204:207], a[48:51], v[188:191]\n" \
"ds_read_b64_tr_b16 a[232:233], v227 offset:49152\n" \
"v_mfma_f32_16x16x32_bf16 v[188:191], a[220:223], a[52:55], v[188:191]\n" \
"ds_read_b64_tr_b16 a[234:235], v227 offset:51200\n" \
"v_mfma_f32_16x16x32_bf16 v[128:131], a[208:211], a[128:131], v[192:195]\n" \
"ds_read_b64_tr_b16 a[196:197], v227 offset:53248\n" \
"v_mfma_f32_16x16x32_bf16 v[128:131], a[224:227], a[132:135], v[128:131]\n" \
"ds_read_b64_tr_b16 a[198:199], v227 offset:55296\n" \
"v_mfma_f32_16x16x32_bf16 v[132:135], a[208:211], a[144:147], v[196:199]\n" \
"s_add_u32 m0, %[m0b], 16384\n" \
"s_nop 0\n" \
"s_nop 0\n" \
"buffer_load_dwordx4 v213, %[srdt], s49 offen offset:2048 lds\n" \
"v_mfma_f32_16x16x32_bf16 v[132:135], a[224:227], a[148:151], v[132:135]\n" \
"v_mfma_f32_16x16x32_bf16 v[136:139], a[208:211], a[160:163], v[200:203]\n" \
"v_mfma_f32_16x16x32_bf16 v[136:139], a[224:227], a[164:167], v[136:139]\n" \
"v_mfma_f32_16x16x32_bf16 v[140:143], a[208:211], a[176:179], v[204:207]\n" \
"ds_read_b64_tr_b16 a[200:201], v227 offset:57344\n" \
"v_mfma_f32_16x16x32_bf16 v[140:143], a[224:227], a[180:183], v[140:143]\n" \
"s_waitcnt vmcnt(10) lgkmcnt(5)\n" \
"s_nop 0\n" \
"v_mfma_f32_16x16x32_bf16 v[128:131], a[228:231], a[136:139], v[128:131]\n" \
"ds_read_b64_tr_b16 a[202:203], v227 offset:59392\n" \
"v_mfma_f32_16x16x32_bf16 v[128:131], a[236:239], a[140:143], v[128:131]\n" \
"ds_read_b64_tr_b16 a[204:205], v227 offset:61440\n" \
"s_add_u32 m0, %[m0b], 101376\n" \
"s_mov_b32 s61, 12288\n" \
"buffer_load_dwordx4 %[vdn], s[64:67], s61 offen offset:0 lds\n" \
"v_mfma_f32_16x16x32_bf16 v[132:135], a[228:231], a[152:155], v[132:135]\n" \
"ds_read_b64_tr_b16 a[206:207], v227 offset:63488\n" \
"v_mfma_f32_16x16x32_bf16 v[132:135], a[236:239], a[156:159], v[132:135]\n" \
"s_add_u32 m0, %[m0b], 16384\n" \
"s_nop 0\n" \
"s_nop 0\n" \
"buffer_load_dwordx4 v214, %[srdt], s49 offen offset:3072 lds\n" \
"v_mfma_f32_16x16x32_bf16 v[136:139], a[228:231], a[168:171], v[136:139]\n" \
"v_mfma_f32_16x16x32_bf16 v[136:139], a[236:239], a[172:175], v[136:139]\n" \
"v_mfma_f32_16x16x32_bf16 v[140:143], a[228:231], a[184:187], v[140:143]\n" \
"ds_read_b64_tr_b16 a[220:221], v215 offset:49664\n" \
"v_mfma_f32_16x16x32_bf16 v[140:143], a[236:239], a[188:191], v[140:143]\n" \
"ds_read_b64_tr_b16 a[222:223], v215 offset:51712\n" \
"v_mfma_f32_16x16x32_bf16 v[176:179], a[212:215], a[8:11], v[176:179]\n" \
"v_mfma_f32_16x16x32_bf16 v[176:179], a[216:219], a[12:15], v[176:179]\n" \
"v_mfma_f32_16x16x32_bf16 v[180:183], a[212:215], a[24:27], v[180:183]\n" \
".if . - adaln_body != 53248\n" \
".error \"size model\"\n" \
".endif\n" \
"Lk12_%=:\n" \
"s_cbranch_execz Lk13_%=\n" \
"s_nop 0\n" \
"v_mfma_f32_16x16x32_bf16 v[180:183], a[216:219], a[28:31], v[180:183]\n" \
"v_mfma_f32_16x16x32_bf16 v[184:187], a[212:215], a[40:43], v[184:187]\n" \
"v_mfma_f32_16x16x32_bf16 v[184:187], a[216:219], a[44:47], v[184:187]\n" \
"v_cmp_u_f32_e64 s[44:45], v128, v132\n" \
"v_mfma_f32_16x16x32_bf16 v[188:191], a[212:215], a[56:59], v[188:191]\n" \
"v_mfma_f32_16x16x32_bf16 v[188:191], a[216:219], a[60:63], v[188:191]\n" \
"v_cmp_u_f32_e64 s[56:57], v136, v140\n" \
"s_or_b64 s[56:57], s[56:57], s[44:45]\n" \
"s_cbranch_scc1 Lsp7_%=\n" \
"Lbk7_%=:\n" \
"s_waitcnt vmcnt(63) lgkmcnt(2)\n" \
"s_nop 0\n" \
"v_mfma_f32_16x16x32_bf16 v[144:147], a[232:235], a[128:131], v[144:147]\n" \
"ds_read_b64_tr_b16 a[212:213], v215 offset:53760\n" \
"v_mfma_f32_16x16x32_bf16 v[144:147], a[196:199], a[132:135], v[144:147]\n" \
"ds_read_b64_tr_b16 a[214:215], v215 offset:55808\n" \
"v_mfma_f32_16x16x32_bf16 v[148:151], a[232:235], a[144:147], v[148:151]\n" \
"ds_read_b64_tr_b16 a[208:209], v215 offset:57856\n" \
"v_mfma_f32_16x16x32_bf16 v[148:151], a[196:199], a[148:151], v[148:151]\n" \
"ds_read_b64_tr_b16 a[210:211], v215 offset:59904\n" \
"s_lshl_b32 s61, %[wid], 10\n" \
"v_lshrrev_b32_e32 v231, 3, v224\n" \
"v_add_u32_e64 v231, s61, v231\n" \
"s_add_u32 m0, %[m0b], 32768\n" \
"s_cmp_gt_u32 s48, 5\n" \
"s_cselect_b32 s49, 5, 6\n" \
"s_lshl_b32 s49, s49, 18\n" \
"s_or_b32 s49, s49, s59\n" \
"buffer_load_dwordx4 %[wo0], %[srdt], s49 offen offset:0 lds\n" \
"v_mfma_f32_16x16x32_bf16 v[152:155], a[232:235], a[160:163], v[152:155]\n" \
"ds_read_b64_tr_b16 a[216:217], v215 offset:61952\n" \
"v_readlane_b32 s44, %[tlo], 20\n" \
"v_readlane_b32 s45, %[thi], 20\n" \
"v_lshrrev_b32_e64 v236, 1, v231\n" \
"v_mfma_f32_16x16x32_bf16 v[152:155], a[196:199], a[164:167], v[152:155]\n" \
"ds_read_b64_tr_b16 a[218:219], v215 offset:64000\n" \
"v_mfma_f32_16x16x32_bf16 v[156:159], a[232:235], a[176:179], v[156:159]\n" \
"ds_read_b64_tr_b16 a[224:225], v227 offset:49664\n" \
"v_mfma_f32_16x16x32_bf16 v[156:159], a[196:199], a[180:183], v[156:159]\n" \
"ds_read_b64_tr_b16 a[226:227], v227 offset:51712\n" \
"v_mfma_f32_16x16x32_bf16 v[144:147], a[200:203], a[136:139], v[144:147]\n" \
"ds_read_b64_tr_b16 a[228:229], v227 offset:53760\n" \
"v_mfma_f32_16x16x32_bf16 v[144:147], a[204:207], a[140:143], v[144:147]\n" \
"ds_read_b64_tr_b16 a[230:231], v227 offset:55808\n" \
"v_mfma_f32_16x16x32_bf16 v[148:151], a[200:203], a[152:155], v[148:151]\n" \
"s_mov_b32 s61, 262144\n" \
"buffer_load_dwordx4 v[192:195], v231, %[srdz], s61 offen offset:0 sc1\n" \
"v_mfma_f32_16x16x32_bf16 v[148:151], a[204:207], a[156:159], v[148:151]\n" \
"v_mfma_f32_16x16x32_bf16 v[152:155], a[200:203], a[168:171], v[152:155]\n" \
"buffer_load_dwordx4 v212, %[srdt], s49 offen offset:1024 lds\n" \
"v_mfma_f32_16x16x32_bf16 v[152:155], a[204:207], a[172:175], v[152:155]\n" \
"v_mfma_f32_16x16x32_bf16 v[156:159], a[200:203], a[184:187], v[156:159]\n" \
"ds_read_b64_tr_b16 a[236:237], v227 offset:57856\n" \
"v_mfma_f32_16x16x32_bf16 v[156:159], a[204:207], a[188:191], v[156:159]\n" \
"s_waitcnt vmcnt(63) lgkmcnt(5)\n" \
"s_nop 0\n" \
"v_mfma_f32_16x16x32_bf16 v[160:163], a[220:223], a[128:131], v[160:163]\n" \
"ds_read_b64_tr_b16 a[238:239], v227 offset:59904\n" \
"v_mfma_f32_16x16x32_bf16 v[160:163], a[212:215], a[132:135], v[160:163]\n" \
"ds_read_b64_tr_b16 a[232:233], v227 offset:61952\n" \
"v_mfma_f32_16x16x32_bf16 v[164:167], a[220:223], a[144:147], v[164:167]\n" \
"ds_read_b64_tr_b16 a[234:235], v227 offset:64000\n" \
"s_mov_b32 s61, 266240\n" \
"buffer_load_dwordx4 v[196:199], v231, %[srdz], s61 offen offset:0 sc1\n" \
"v_mfma_f32_16x16x32_bf16 v[164:167], a[212:215], a[148:151], v[164:167]\n" \
"v_mfma_f32_16x16x32_bf16 v[168:171], a[220:223], a[160:163], v[168:171]\n" \
"v_cvt_pk_bf16_f32 v128, v128, v129\n" \
"buffer_load_dwordx4 v213, %[srdt], s49 offen offset:2048 lds\n" \
"v_mfma_f32_16x16x32_bf16 v[168:171], a[212:215], a[164:167], v[168:171]\n" \
"v_cvt_pk_bf16_f32 v129, v130, v131\n" \
"v_mfma_f32_16x16x32_bf16 v[172:175], a[220:223], a[176:179], v[172:175]\n" \
"v_cvt_pk_bf16_f32 v130, v144, v145\n" \
"v_mfma_f32_16x16x32_bf16 v[172:175], a[212:215], a[180:183], v[172:175]\n" \
"v_cvt_pk_bf16_f32 v131, v146, v147\n" \
"v_mfma_f32_16x16x32_bf16 v[160:163], a[208:211], a[136:139], v[160:163]\n" \
"s_add_u32 s55, s46, 0\n" \
"s_nop 0\n" \
"buffer_store_dwordx4 v[128:131], %[vxs], %[srdz], s55 offen offset:0 sc1\n" \
"v_mfma_f32_16x16x32_bf16 v[160:163], a[216:219], a[140:143], v[160:163]\n" \
"v_mfma_f32_16x16x32_bf16 v[164:167], a[208:211], a[152:155], v[164:167]\n" \
"v_cvt_pk_bf16_f32 v132, v132, v133\n" \
"s_mov_b32 s61, 270336\n" \
"buffer_load_dwordx4 v[200:203], v231, %[srdz], s61 offen offset:0 sc1\n" \
"v_mfma_f32_16x16x32_bf16 v[164:167], a[216:219], a[156:159], v[164:167]\n" \
"v_cvt_pk_bf16_f32 v133, v134, v135\n" \
"v_mfma_f32_16x16x32_bf16 v[168:171], a[208:211], a[168:171], v[168:171]\n" \
"s_waitcnt vmcnt(14) lgkmcnt(0)\n" \
"s_barrier\n" \
"v_mfma_f32_16x16x32_bf16 v[168:171], a[216:219], a[172:175], v[168:171]\n" \
"ds_read_b64_tr_b16 a[196:197], v215 offset:0\n" \
"v_cvt_pk_bf16_f32 v134, v148, v149\n" \
"v_mfma_f32_16x16x32_bf16 v[172:175], a[208:211], a[184:187], v[172:175]\n" \
"ds_read_b64_tr_b16 a[198:199], v215 offset:2048\n" \
"v_cvt_pk_bf16_f32 v135, v150, v151\n" \
"v_mfma_f32_16x16x32_bf16 v[172:175], a[216:219], a[188:191], v[172:175]\n" \
"s_waitcnt vmcnt(63) lgkmcnt(2)\n" \
"s_nop 0\n" \
"v_mfma_f32_16x16x32_bf16 v[176:179], a[224:227], a[128:131], v[176:179]\n" \
"ds_read_b64_tr_b16 a[200:201], v215 offset:4096\n" \
"v_cvt_pk_bf16_f32 v136, v136, v137\n" \
"v_mfma_f32_16x16x32_bf16 v[176:179], a[228:231], a[132:135], v[176:179]\n" \
"ds_read_b64_tr_b16 a[202:203], v215 offset:6144\n" \
"v_cvt_pk_bf16_f32 v137, v138, v139\n" \
"v_mfma_f32_16x16x32_bf16 v[180:183], a[224:227], a[144:147], v[180:183]\n" \
"ds_read_b64_tr_b16 a[204:205], v215 offset:8192\n" \
"s_add_u32 s55, s46, 524288\n" \
"buffer_store_dwordx4 v[132:135], %[vxs], %[srdz], s55 offen offset:0 sc1\n" \
"v_mfma_f32_16x16x32_bf16 v[180:183], a[228:231], a[148:151], v[180:183]\n" \
"ds_read_b64_tr_b16 a[206:207], v215 offset:10240\n" \
"v_cvt_pk_bf16_f32 v138, v152, v153\n" \
"v_mfma_f32_16x16x32_bf16 v[184:187], a[224:227], a[160:163], v[184:187]\n" \
"ds_read_b64_tr_b16 a[220:221], v215 offset:12288\n" \
"v_cvt_pk_bf16_f32 v139, v154, v155\n" \
"s_mov_b32 s61, 274432\n" \
"buffer_load_dwordx4 v[204:207], v231, %[srdz], s61 offen offset:0 sc1\n" \
"v_mfma_f32_16x16x32_bf16 v[184:187], a[228:231], a[164:167], v[184:187]\n" \
"ds_read_b64_tr_b16 a[222:223], v215 offset:14336\n" \
"v_mfma_f32_16x16x32_bf16 v[188:191], a[224:227], a[176:179], v[188:191]\n" \
"ds_read_b64_tr_b16 a[212:213], v227 offset:0\n" \
"v_cvt_pk_bf16_f32 v140, v140, v141\n" \
"buffer_load_dwordx4 v214, %[srdt], s49 offen offset:3072 lds\n" \
"v_mfma_f32_16x16x32_bf16 v[188:191], a[228:231], a[180:183], v[188:191]\n" \
"ds_read_b64_tr_b16 a[214:215], v227 offset:2048\n" \
"v_cvt_pk_bf16_f32 v141, v142, v143\n" \
"v_mfma_f32_16x16x32_bf16 v[176:179], a[236:239], a[136:139], v[176:179]\n" \
"ds_read_b64_tr_b16 a[208:209], v227 offset:4096\n" \
"s_add_u32 s55, s46, 1048576\n" \
"buffer_store_dwordx4 v[136:139], %[vxs], %[srdz], s55 offen offset:0 sc1\n" \
"v_mfma_f32_16x16x32_bf16 v[176:179], a[232:235], a[140:143], v[176:179]\n" \
"ds_read_b64_tr_b16 a[210:211], v227 offset:6144\n" \
"v_cvt_pk_bf16_f32 v142, v156, v157\n" \
"v_mfma_f32_16x16x32_bf16 v[180:183], a[236:239], a[152:155], v[180:183]\n" \
"v_cvt_pk_bf16_f32 v143, v158, v159\n" \
"v_mfma_f32_16x16x32_bf16 v[180:183], a[232:235], a[156:159], v[180:183]\n" \
"s_add_u32 s55, s46, 1572864\n" \
"buffer_store_dwordx4 v[140:143], %[vxs], %[srdz], s55 offen offset:0 sc1\n" \
"v_mfma_f32_16x16x32_bf16 v[184:187], a[236:239], a[168:171], v[184:187]\n" \
"v_mfma_f32_16x16x32_bf16 v[184:187], a[232:235], a[172:175], v[184:187]\n" \
"global_load_dwordx2 v[232:233], v236, s[44:45]\n" \
"v_mfma_f32_16x16x32_bf16 v[188:191], a[236:239], a[184:187], v[188:191]\n" \
"ds_read_b64_tr_b16 a[216:217], v227 offset:8192\n" \
"v_mfma_f32_16x16x32_bf16 v[188:191], a[232:235], a[188:191], v[188:191]\n" \
"s_waitcnt vmcnt(63) lgkmcnt(5)\n" \
"s_nop 0\n" \
"v_mfma_f32_16x16x32_bf16 v[4:7], a[196:199], a[64:67], v[4:7]\n" \
"ds_read_b64_tr_b16 a[218:219], v227 offset:10240\n" \
"v_mfma_f32_16x16x32_bf16 v[4:7], a[200:203], a[68:71], v[4:7]\n" \
"ds_read_b64_tr_b16 a[224:225], v227 offset:12288\n" \
"v_mfma_f32_16x16x32_bf16 v[36:39], a[196:199], a[80:83], v[36:39]\n" \
"ds_read_b64_tr_b16 a[226:227], v227 offset:14336\n" \
"v_mfma_f32_16x16x32_bf16 v[36:39], a[200:203], a[84:87], v[36:39]\n" \
"v_mfma_f32_16x16x32_bf16 v[68:71], a[196:199], a[96:99], v[68:71]\n" \
"v_cvt_pk_bf16_f32 v160, v160, v161\n" \
"v_mfma_f32_16x16x32_bf16 v[68:71], a[200:203], a[100:103], v[68:71]\n" \
"v_cvt_pk_bf16_f32 v161, v162, v163\n" \
"v_mfma_f32_16x16x32_bf16 v[100:103], a[196:199], a[112:115], v[100:103]\n" \
"ds_read_b64_tr_b16 a[228:229], v215 offset:512\n" \
"v_cvt_pk_bf16_f32 v162, v176, v177\n" \
"v_mfma_f32_16x16x32_bf16 v[100:103], a[200:203], a[116:119], v[100:103]\n" \
"ds_read_b64_tr_b16 a[230:231], v215 offset:2560\n" \
"v_cvt_pk_bf16_f32 v163, v178, v179\n" \
"v_mfma_f32_16x16x32_bf16 v[4:7], a[204:207], a[72:75], v[4:7]\n" \
"ds_read_b64_tr_b16 a[236:237], v215 offset:4608\n" \
"v_mfma_f32_16x16x32_bf16 v[4:7], a[220:223], a[76:79], v[4:7]\n" \
"ds_read_b64_tr_b16 a[238:239], v215 offset:6656\n" \
"v_mfma_f32_16x16x32_bf16 v[36:39], a[204:207], a[88:91], v[36:39]\n" \
"s_add_u32 s55, s46, 4096\n" \
"buffer_store_dwordx4 v[160:163], %[vxs], %[srdz], s55 offen offset:0 sc1\n" \
"v_mfma_f32_16x16x32_bf16 v[36:39], a[220:223], a[92:95], v[36:39]\n" \
"v_cvt_pk_bf16_f32 v164, v164, v165\n" \
"v_cvt_pk_bf16_f32 v165, v166, v167\n" \
"v_mfma_f32_16x16x32_bf16 v[68:71], a[204:207], a[104:107], v[68:71]\n" \
"v_cvt_pk_bf16_f32 v166, v180, v181\n" \
"s_add_u32 m0, %[m0b], 49152\n" \
"s_cmp_gt_u32 s48, 6\n" \
"s_cselect_b32 s49, 6, 7\n" \
"s_lshl_b32 s49, s49, 18\n" \
"s_or_b32 s49, s49, s59\n" \
"buffer_load_dwordx4 %[wo0], %[srdt], s49 offen offset:0 lds\n" \
"v_mfma_f32_16x16x32_bf16 v[68:71], a[220:223], a[108:111], v[68:71]\n" \
"v_cvt_pk_bf16_f32 v167, v182, v183\n" \
"v_mfma_f32_16x16x32_bf16 v[100:103], a[204:207], a[120:123], v[100:103]\n" \
"ds_read_b64_tr_b16 a[232:233], v215 offset:8704\n" \
"v_mfma_f32_16x16x32_bf16 v[100:103], a[220:223], a[124:127], v[100:103]\n" \
"s_waitcnt vmcnt(63) lgkmcnt(5)\n" \
"s_nop 0\n" \
"v_mfma_f32_16x16x32_bf16 v[12:15], a[212:215], a[64:67], v[12:15]\n" \
"ds_read_b64_tr_b16 a[234:235], v215 offset:10752\n" \
"v_cvt_pk_bf16_f32 v168, v168, v169\n" \
"v_mfma_f32_16x16x32_bf16 v[12:15], a[208:211], a[68:71], v[12:15]\n" \
"ds_read_b64_tr_b16 a[196:197], v215 offset:12800\n" \
"s_add_u32 s55, s46, 528384\n" \
"buffer_store_dwordx4 v[164:167], %[vxs], %[srdz], s55 offen offset:0 sc1\n" \
"v_mfma_f32_16x16x32_bf16 v[44:47], a[212:215], a[80:83], v[44:47]\n" \
"ds_read_b64_tr_b16 a[198:199], v215 offset:14848\n" \
"v_cvt_pk_bf16_f32 v169, v170, v171\n" \
"v_mfma_f32_16x16x32_bf16 v[44:47], a[208:211], a[84:87], v[44:47]\n" \
"v_cvt_pk_bf16_f32 v170, v184, v185\n" \
"v_cvt_pk_bf16_f32 v171, v186, v187\n" \
"v_mfma_f32_16x16x32_bf16 v[76:79], a[212:215], a[96:99], v[76:79]\n" \
"s_add_u32 s55, s46, 1052672\n" \
"buffer_store_dwordx4 v[168:171], %[vxs], %[srdz], s55 offen offset:0 sc1\n" \
"v_mfma_f32_16x16x32_bf16 v[76:79], a[208:211], a[100:103], v[76:79]\n" \
"v_mfma_f32_16x16x32_bf16 v[108:111], a[212:215], a[112:115], v[108:111]\n" \
"ds_read_b64_tr_b16 a[200:201], v227 offset:512\n" \
"v_cvt_pk_bf16_f32 v172, v172, v173\n" \
"buffer_load_dwordx4 v212, %[srdt], s49 offen offset:1024 lds\n" \
"v_mfma_f32_16x16x32_bf16 v[108:111], a[208:211], a[116:119], v[108:111]\n" \
"ds_read_b64_tr_b16 a[202:203], v227 offset:2560\n" \
"v_cvt_pk_bf16_f32 v173, v174, v175\n" \
"v_mfma_f32_16x16x32_bf16 v[12:15], a[216:219], a[72:75], v[12:15]\n" \
"ds_read_b64_tr_b16 a[204:205], v227 offset:4608\n" \
"v_cvt_pk_bf16_f32 v174, v188, v189\n" \
"v_mfma_f32_16x16x32_bf16 v[12:15], a[224:227], a[76:79], v[12:15]\n" \
"ds_read_b64_tr_b16 a[206:207], v227 offset:6656\n" \
"v_cvt_pk_bf16_f32 v175, v190, v191\n" \
"v_mfma_f32_16x16x32_bf16 v[44:47], a[216:219], a[88:91], v[44:47]\n" \
"s_add_u32 s55, s46, 1576960\n" \
"buffer_store_dwordx4 v[172:175], %[vxs], %[srdz], s55 offen offset:0 sc1\n" \
"v_mfma_f32_16x16x32_bf16 v[44:47], a[224:227], a[92:95], v[44:47]\n" \
"v_mfma_f32_16x16x32_bf16 v[76:79], a[216:219], a[104:107], v[76:79]\n" \
"buffer_load_dwordx4 v213, %[srdt], s49 offen offset:2048 lds\n" \
"v_mfma_f32_16x16x32_bf16 v[76:79], a[224:227], a[108:111], v[76:79]\n" \
"v_mfma_f32_16x16x32_bf16 v[108:111], a[216:219], a[120:123], v[108:111]\n" \
"ds_read_b64_tr_b16 a[220:221], v227 offset:8704\n" \
"v_accvgpr_read_b32 v129, a240\n" \
"v_mfma_f32_16x16x32_bf16 v[108:111], a[224:227], a[124:127], v[108:111]\n" \
"s_waitcnt vmcnt(63) lgkmcnt(5)\n" \
"s_nop 0\n" \
"v_mfma_f32_16x16x32_bf16 v[20:23], a[228:231], a[64:67], v[20:23]\n" \
"ds_read_b64_tr_b16 a[222:223], v227 offset:10752\n" \
"v_lshlrev_b32_e64 v128, 16, v129\n" \
"v_mfma_f32_16x16x32_bf16 v[20:23], a[236:239], a[68:71], v[20:23]\n" \
"ds_read_b64_tr_b16 a[212:213], v227 offset:12800\n" \
"v_and_b32_e32 v129, 0xffff0000, v129\n" \
"v_mfma_f32_16x16x32_bf16 v[52:55], a[228:231], a[80:83], v[52:55]\n" \
"ds_read_b64_tr_b16 a[214:215], v227 offset:14848\n" \
"v_accvgpr_read_b32 v131, a241\n" \
"v_mfma_f32_16x16x32_bf16 v[52:55], a[236:239], a[84:87], v[52:55]\n" \
"v_lshlrev_b32_e64 v130, 16, v131\n" \
"v_and_b32_e32 v131, 0xffff0000, v131\n" \
"global_load_dword v237, %[vct], %[pct]\n" \
"v_mfma_f32_16x16x32_bf16 v[84:87], a[228:231], a[96:99], v[84:87]\n" \
"v_accvgpr_read_b32 v133, a242\n" \
"v_mfma_f32_16x16x32_bf16 v[84:87], a[236:239], a[100:103], v[84:87]\n" \
"v_lshlrev_b32_e64 v132, 16, v133\n" \
"s_add_u32 m0, %[m0b], 114688\n" \
"s_mov_b32 s61, 16384\n" \
"buffer_load_dwordx4 %[vdn], s[64:67], s61 offen offset:0 lds\n" \
"v_mfma_f32_16x16x32_bf16 v[116:119], a[228:231], a[112:115], v[116:119]\n" \
"v_and_b32_e32 v133, 0xffff0000, v133\n" \
"v_mfma_f32_16x16x32_bf16 v[116:119], a[236:239], a[116:119], v[116:119]\n" \
"v_accvgpr_read_b32 v135, a243\n" \
"s_add_u32 m0, %[m0b], 49152\n" \
"s_nop 0\n" \
"s_nop 0\n" \
"buffer_load_dwordx4 v214, %[srdt], s49 offen offset:3072 lds\n" \
"v_mfma_f32_16x16x32_bf16 v[20:23], a[232:235], a[72:75], v[20:23]\n" \
"v_lshlrev_b32_e64 v134, 16, v135\n" \
"v_mfma_f32_16x16x32_bf16 v[20:23], a[196:199], a[76:79], v[20:23]\n" \
"v_and_b32_e32 v135, 0xffff0000, v135\n" \
"v_mfma_f32_16x16x32_bf16 v[52:55], a[232:235], a[88:91], v[52:55]\n" \
"v_accvgpr_read_b32 v137, a244\n" \
"v_mfma_f32_16x16x32_bf16 v[52:55], a[196:199], a[92:95], v[52:55]\n" \
"v_lshlrev_b32_e64 v136, 16, v137\n" \
"v_mfma_f32_16x16x32_bf16 v[84:87], a[232:235], a[104:107], v[84:87]\n" \
"s_waitcnt vmcnt(23) lgkmcnt(0)\n" \
"s_barrier\n" \
"v_mfma_f32_16x16x32_bf16 v[84:87], a[196:199], a[108:111], v[84:87]\n" \
"ds_read_b64_tr_b16 a[208:209], v215 offset:16384\n" \
"v_and_b32_e32 v137, 0xffff0000, v137\n" \
"v_mfma_f32_16x16x32_bf16 v[116:119], a[232:235], a[120:123], v[116:119]\n" \
"ds_read_b64_tr_b16 a[210:211], v215 offset:18432\n" \
"v_accvgpr_read_b32 v139, a245\n" \
"v_mfma_f32_16x16x32_bf16 v[116:119], a[196:199], a[124:127], v[116:119]\n" \
"s_waitcnt vmcnt(63) lgkmcnt(2)\n" \
"s_nop 0\n" \
"v_mfma_f32_16x16x32_bf16 v[28:31], a[200:203], a[64:67], v[28:31]\n" \
"ds_read_b64_tr_b16 a[216:217], v215 offset:20480\n" \
"v_lshlrev_b32_e64 v138, 16, v139\n" \
"v_mfma_f32_16x16x32_bf16 v[28:31], a[204:207], a[68:71], v[28:31]\n" \
"ds_read_b64_tr_b16 a[218:219], v215 offset:22528\n" \
"v_and_b32_e32 v139, 0xffff0000, v139\n" \
"v_mfma_f32_16x16x32_bf16 v[60:63], a[200:203], a[80:83], v[60:63]\n" \
"ds_read_b64_tr_b16 a[224:225], v215 offset:24576\n" \
"v_accvgpr_read_b32 v141, a246\n" \
"v_mfma_f32_16x16x32_bf16 v[60:63], a[204:207], a[84:87], v[60:63]\n" \
"ds_read_b64_tr_b16 a[226:227], v215 offset:26624\n" \
"v_lshlrev_b32_e64 v140, 16, v141\n" \
"v_mfma_f32_16x16x32_bf16 v[92:95], a[200:203], a[96:99], v[92:95]\n" \
"ds_read_b64_tr_b16 a[228:229], v215 offset:28672\n" \
"v_and_b32_e32 v141, 0xffff0000, v141\n" \
"s_add_u32 m0, %[m0b], 115712\n" \
"s_mov_b32 s61, 20480\n" \
"buffer_load_dwordx4 %[vdn], s[64:67], s61 offen offset:0 lds\n" \
"v_mfma_f32_16x16x32_bf16 v[92:95], a[204:207], a[100:103], v[92:95]\n" \
"ds_read_b64_tr_b16 a[230:231], v215 offset:30720\n" \
"v_accvgpr_read_b32 v143, a247\n" \
"v_mfma_f32_16x16x32_bf16 v[124:127], a[200:203], a[112:115], v[124:127]\n" \
"ds_read_b64_tr_b16 a[236:237], v227 offset:16384\n" \
"v_lshlrev_b32_e64 v142, 16, v143\n" \
"v_mfma_f32_16x16x32_bf16 v[124:127], a[204:207], a[116:119], v[124:127]\n" \
"ds_read_b64_tr_b16 a[238:239], v227 offset:18432\n" \
"v_and_b32_e32 v143, 0xffff0000, v143\n" \
"v_mfma_f32_16x16x32_bf16 v[28:31], a[220:223], a[72:75], v[28:31]\n" \
"ds_read_b64_tr_b16 a[232:233], v227 offset:20480\n" \
"v_accvgpr_read_b32 v145, a248\n" \
"v_mfma_f32_16x16x32_bf16 v[28:31], a[212:215], a[76:79], v[28:31]\n" \
"ds_read_b64_tr_b16 a[234:235], v227 offset:22528\n" \
"v_lshlrev_b32_e64 v144, 16, v145\n" \
"v_mfma_f32_16x16x32_bf16 v[60:63], a[220:223], a[88:91], v[60:63]\n" \
"v_and_b32_e32 v145, 0xffff0000, v145\n" \
"v_accvgpr_read_b32 v147, a249\n" \
"v_mfma_f32_16x16x32_bf16 v[60:63], a[212:215], a[92:95], v[60:63]\n" \
"v_lshlrev_b32_e64 v146, 16, v147\n" \
"v_and_b32_e32 v147, 0xffff0000, v147\n" \
"v_mfma_f32_16x16x32_bf16 v[92:95], a[220:223], a[104:107], v[92:95]\n" \
"v_accvgpr_read_b32 v149, a250\n" \
"v_mfma_f32_16x16x32_bf16 v[92:95], a[212:215], a[108:111], v[92:95]\n" \
"v_lshlrev_b32_e64 v148, 16, v149\n" \
"v_mfma_f32_16x16x32_bf16 v[124:127], a[220:223], a[120:123], v[124:127]\n" \
"ds_read_b64_tr_b16 a[196:197], v227 offset:24576\n" \
"v_and_b32_e32 v149, 0xffff0000, v149\n" \
"v_mfma_f32_16x16x32_bf16 v[124:127], a[212:215], a[124:127], v[124:127]\n" \
"s_waitcnt vmcnt(63) lgkmcnt(5)\n" \
"s_nop 0\n" \
"v_mfma_f32_16x16x32_bf16 v[4:7], a[208:211], v[0:3], v[4:7]\n" \
"ds_read_b64_tr_b16 a[198:199], v227 offset:26624\n" \
"v_accvgpr_read_b32 v151, a251\n" \
"v_mfma_f32_16x16x32_bf16 v[4:7], a[216:219], v[8:11], v[4:7]\n" \
"ds_read_b64_tr_b16 a[200:201], v227 offset:28672\n" \
"v_lshlrev_b32_e64 v150, 16, v151\n" \
"v_mfma_f32_16x16x32_bf16 v[36:39], a[208:211], v[32:35], v[36:39]\n" \
"ds_read_b64_tr_b16 a[202:203], v227 offset:30720\n" \
"v_and_b32_e32 v151, 0xffff0000, v151\n" \
"v_mfma_f32_16x16x32_bf16 v[36:39], a[216:219], v[40:43], v[36:39]\n" \
"v_accvgpr_read_b32 v153, a252\n" \
"v_lshlrev_b32_e64 v152, 16, v153\n" \
"s_add_u32 m0, %[m0b], 116736\n" \
"s_mov_b32 s61, 24576\n" \
"buffer_load_dwordx4 %[vdn], s[64:67], s61 offen offset:0 lds\n" \
"v_mfma_f32_16x16x32_bf16 v[68:71], a[208:211], v[64:67], v[68:71]\n" \
"v_and_b32_e32 v153, 0xffff0000, v153\n" \
"v_mfma_f32_16x16x32_bf16 v[68:71], a[216:219], v[72:75], v[68:71]\n" \
"v_accvgpr_read_b32 v155, a253\n" \
"v_mfma_f32_16x16x32_bf16 v[100:103], a[208:211], v[96:99], v[100:103]\n" \
"v_lshlrev_b32_e64 v154, 16, v155\n" \
"v_mfma_f32_16x16x32_bf16 v[100:103], a[216:219], v[104:107], v[100:103]\n" \
"v_and_b32_e32 v155, 0xffff0000, v155\n" \
"v_mfma_f32_16x16x32_bf16 v[4:7], a[224:227], v[16:19], v[4:7]\n" \
"v_accvgpr_read_b32 v157, a254\n" \
"v_mfma_f32_16x16x32_bf16 v[4:7], a[228:231], v[24:27], v[4:7]\n" \
"v_lshlrev_b32_e64 v156, 16, v157\n" \
"v_mfma_f32_16x16x32_bf16 v[36:39], a[224:227], v[48:51], v[36:39]\n" \
"v_and_b32_e32 v157, 0xffff0000, v157\n" \
"v_mfma_f32_16x16x32_bf16 v[36:39], a[228:231], v[56:59], v[36:39]\n" \
"v_accvgpr_read_b32 v159, a255\n" \
"v_mfma_f32_16x16x32_bf16 v[68:71], a[224:227], v[80:83], v[68:71]\n" \
"s_waitcnt vmcnt(15) lgkmcnt(15)\n" \
"s_barrier\n" \
"v_mfma_f32_16x16x32_bf16 v[68:71], a[228:231], v[88:91], v[68:71]\n" \
"ds_read_b64_tr_b16 a[204:205], v215 offset:32768\n" \
"v_lshlrev_b32_e64 v158, 16, v159\n" \
"v_mfma_f32_16x16x32_bf16 v[100:103], a[224:227], v[112:115], v[100:103]\n" \
"ds_read_b64_tr_b16 a[206:207], v215 offset:34816\n" \
"v_and_b32_e32 v159, 0xffff0000, v159\n" \
"v_mfma_f32_16x16x32_bf16 v[100:103], a[228:231], v[120:123], v[100:103]\n" \
"s_waitcnt vmcnt(63) lgkmcnt(2)\n" \
"s_nop 0\n" \
"v_mfma_f32_16x16x32_bf16 v[12:15], a[236:239], v[0:3], v[12:15]\n" \
"ds_read_b64_tr_b16 a[220:221], v215 offset:36864\n" \
"v_and_b32_e32 v208, 0x7ff, %[vx0]\n" \
"v_mfma_f32_16x16x32_bf16 v[12:15], a[232:235], v[8:11], v[12:15]\n" \
"ds_read_b64_tr_b16 a[222:223], v215 offset:38912\n" \
"v_lshlrev_b32_e64 v208, 1, v208\n" \
"v_mfma_f32_16x16x32_bf16 v[44:47], a[236:239], v[32:35], v[44:47]\n" \
"ds_read_b64_tr_b16 a[212:213], v215 offset:40960\n" \
"v_xor_b32_e32 v209, 0x100, v208\n" \
"v_mfma_f32_16x16x32_bf16 v[44:47], a[232:235], v[40:43], v[44:47]\n" \
"ds_read_b64_tr_b16 a[214:215], v215 offset:43008\n" \
"v_lshrrev_b32_e64 v210, 3, v224\n" \
"v_mfma_f32_16x16x32_bf16 v[76:79], a[236:239], v[64:67], v[76:79]\n" \
"ds_read_b64_tr_b16 a[208:209], v215 offset:45056\n" \
"v_and_b32_e64 v210, 48, v210\n" \
"v_mfma_f32_16x16x32_bf16 v[76:79], a[232:235], v[72:75], v[76:79]\n" \
"ds_read_b64_tr_b16 a[210:211], v215 offset:47104\n" \
"s_lshl_b32 s61, %[wid], 12\n" \
"s_sub_u32 s61, %[m0b], s61\n" \
"v_add_u32_e32 v208, s61, v208\n" \
"v_add_u32_e32 v209, s61, v209\n" \
"v_add_u32_e64 v210, s61, v210\n" \
"v_mfma_f32_16x16x32_bf16 v[108:111], a[236:239], v[96:99], v[108:111]\n" \
"ds_read_b64_tr_b16 a[216:217], v227 offset:32768\n" \
"v_mfma_f32_16x16x32_bf16 v[108:111], a[232:235], v[104:107], v[108:111]\n" \
"ds_read_b64_tr_b16 a[218:219], v227 offset:34816\n" \
"s_waitcnt vmcnt(12)\n" \
"s_nop 0\n" \
"v_and_or_b32 v236, v192, v193, 0\n" \
"s_add_u32 m0, %[m0b], 117760\n" \
"s_mov_b32 s61, 28672\n" \
"buffer_load_dwordx4 %[vdn], s[64:67], s61 offen offset:0 lds\n" \
"v_mfma_f32_16x16x32_bf16 v[12:15], a[196:199], v[16:19], v[12:15]\n" \
"ds_read_b64_tr_b16 a[224:225], v227 offset:36864\n" \
"v_bitop3_b32 v236, v236, v194, v195 bitop3:0x80\n" \
"v_mfma_f32_16x16x32_bf16 v[12:15], a[200:203], v[24:27], v[12:15]\n" \
"ds_read_b64_tr_b16 a[226:227], v227 offset:38912\n" \
"v_bitop3_b32 v236, v236, v196, v197 bitop3:0x80\n" \
"v_mfma_f32_16x16x32_bf16 v[44:47], a[196:199], v[48:51], v[44:47]\n" \
"v_bitop3_b32 v236, v236, v198, v199 bitop3:0x80\n" \
"v_mfma_f32_16x16x32_bf16 v[44:47], a[200:203], v[56:59], v[44:47]\n" \
"v_bitop3_b32 v236, v236, v200, v201 bitop3:0x80\n" \
"v_mfma_f32_16x16x32_bf16 v[76:79], a[196:199], v[80:83], v[76:79]\n" \
"v_bitop3_b32 v236, v236, v202, v203 bitop3:0x80\n" \
"v_mfma_f32_16x16x32_bf16 v[76:79], a[200:203], v[88:91], v[76:79]\n" \
"v_bitop3_b32 v236, v236, v204, v205 bitop3:0x80\n" \
"v_mfma_f32_16x16x32_bf16 v[108:111], a[196:199], v[112:115], v[108:111]\n" \
"ds_read_b64_tr_b16 a[228:229], v227 offset:40960\n" \
"v_bitop3_b32 v236, v236, v206, v207 bitop3:0x80\n" \
"v_mfma_f32_16x16x32_bf16 v[108:111], a[200:203], v[120:123], v[108:111]\n" \
"s_waitcnt vmcnt(63) lgkmcnt(5)\n" \
"s_nop 0\n" \
"v_mfma_f32_16x16x32_bf16 v[4:7], a[204:207], a[0:3], v[4:7]\n" \
"ds_read_b64_tr_b16 a[230:231], v227 offset:43008\n" \
"s_mov_b32 s54, 0x07060302\n" \
"v_and_b32_e64 v236, 1, v236\n" \
"v_mfma_f32_16x16x32_bf16 v[4:7], a[220:223], a[4:7], v[4:7]\n" \
"ds_read_b64_tr_b16 a[236:237], v227 offset:45056\n" \
"v_lshrrev_b32_e64 v218, 1, v231\n" \
"v_mfma_f32_16x16x32_bf16 v[36:39], a[204:207], a[16:19], v[36:39]\n" \
"ds_read_b64_tr_b16 a[238:239], v227 offset:47104\n" \
"v_cmp_eq_u32_e64 s[56:57], 0, v236\n" \
"v_mfma_f32_16x16x32_bf16 v[36:39], a[220:223], a[20:23], v[36:39]\n" \
"s_waitcnt vmcnt(7)\n" \
"v_mov_b32_e32 v230, 1\n" \
"buffer_store_dword v230, v225, %[srdz], s62 offen offset:68 sc1\n" \
"v_mfma_f32_16x16x32_bf16 v[68:71], a[204:207], a[32:35], v[68:71]\n" \
"s_lshl_b32 s61, %[wid], 12\n" \
"s_sub_u32 s61, %[m0b], s61\n" \
"v_add_u32_e32 v231, s61, v231\n" \
"v_add_u32_e32 v218, s61, v218\n" \
"v_mov_b32_e64 v219, s61\n" \
"ds_write_b128 v231, v[192:195] offset:0\n" \
"v_mfma_f32_16x16x32_bf16 v[68:71], a[220:223], a[36:39], v[68:71]\n" \
"v_lshlrev_b32_e64 v234, 16, v233\n" \
"v_and_b32_e32 v235, 0xffff0000, v233\n" \
"v_mfma_f32_16x16x32_bf16 v[100:103], a[204:207], a[48:51], v[100:103]\n" \
"v_and_b32_e32 v233, 0xffff0000, v232\n" \
"v_mfma_f32_16x16x32_bf16 v[100:103], a[220:223], a[52:55], v[100:103]\n" \
"v_lshlrev_b32_e64 v232, 16, v232\n" \
"v_mfma_f32_16x16x32_bf16 v[4:7], a[212:215], a[8:11], v[4:7]\n" \
"v_mul_f32_e64 v234, v202, v234\n" \
"v_mfma_f32_16x16x32_bf16 v[4:7], a[208:211], a[12:15], v[4:7]\n" \
"v_mul_f32_e64 v235, v203, v235\n" \
"v_mfma_f32_16x16x32_bf16 v[36:39], a[212:215], a[24:27], v[36:39]\n" \
"v_mul_f32_e64 v233, v201, v233\n" \
"v_mfma_f32_16x16x32_bf16 v[36:39], a[208:211], a[28:31], v[36:39]\n" \
"v_mul_f32_e64 v232, v200, v232\n" \
"v_mfma_f32_16x16x32_bf16 v[68:71], a[212:215], a[40:43], v[68:71]\n" \
"s_waitcnt vmcnt(4) lgkmcnt(15)\n" \
"s_barrier\n" \
"v_mfma_f32_16x16x32_bf16 v[68:71], a[208:211], a[44:47], v[68:71]\n" \
"ds_read_b64_tr_b16 a[232:233], v215 offset:49152\n" \
"v_add_f32_e64 v223, v234, v235\n" \
"v_mfma_f32_16x16x32_bf16 v[100:103], a[212:215], a[56:59], v[100:103]\n" \
"ds_read_b64_tr_b16 a[234:235], v215 offset:51200\n" \
"v_add_f32_e64 v222, v232, v233\n" \
"v_mfma_f32_16x16x32_bf16 v[100:103], a[208:211], a[60:63], v[100:103]\n" \
"s_waitcnt vmcnt(63) lgkmcnt(3)\n" \
"s_nop 0\n" \
"v_mfma_f32_16x16x32_bf16 v[12:15], a[216:219], a[0:3], v[12:15]\n" \
"ds_read_b64_tr_b16 a[196:197], v215 offset:53248\n" \
"ds_write_b128 v231, v[232:235] offset:4096\n" \
"v_mfma_f32_16x16x32_bf16 v[12:15], a[224:227], a[4:7], v[12:15]\n" \
"ds_read_b64_tr_b16 a[198:199], v215 offset:55296\n" \
"v_add_f32_e64 v222, v222, v223\n" \
"v_mfma_f32_16x16x32_bf16 v[44:47], a[216:219], a[16:19], v[44:47]\n" \
"ds_read_b64_tr_b16 a[200:201], v215 offset:57344\n" \
"v_perm_b32 v196, v197, v196, s54\n" \
"v_mfma_f32_16x16x32_bf16 v[44:47], a[224:227], a[20:23], v[44:47]\n" \
"ds_read_b64_tr_b16 a[202:203], v215 offset:59392\n" \
"v_add_f32_dpp v222, v222, v222 quad_perm:[1,0,3,2] row_mask:0xf bank_mask:0xf\n" \
"v_mfma_f32_16x16x32_bf16 v[76:79], a[216:219], a[32:35], v[76:79]\n" \
"ds_read_b64_tr_b16 a[204:205], v215 offset:61440\n" \
"v_perm_b32 v197, v199, v198, s54\n" \
"v_mfma_f32_16x16x32_bf16 v[76:79], a[224:227], a[36:39], v[76:79]\n" \
"ds_read_b64_tr_b16 a[206:207], v215 offset:63488\n" \
"v_add_f32_dpp v222, v222, v222 quad_perm:[2,3,0,1] row_mask:0xf bank_mask:0xf\n" \
"v_mfma_f32_16x16x32_bf16 v[108:111], a[216:219], a[48:51], v[108:111]\n" \
"ds_read_b64_tr_b16 a[220:221], v227 offset:49152\n" \
"ds_write_b64 v218, v[196:197] offset:8192\n" \
"v_mfma_f32_16x16x32_bf16 v[108:111], a[224:227], a[52:55], v[108:111]\n" \
"ds_read_b64_tr_b16 a[222:223], v227 offset:51200\n" \
"v_add_f32_dpp v222, v222, v222 row_ror:4 row_mask:0xf bank_mask:0xf\n" \
"v_mfma_f32_16x16x32_bf16 v[12:15], a[228:231], a[8:11], v[12:15]\n" \
"ds_read_b64_tr_b16 a[212:213], v227 offset:53248\n" \
"v_perm_b32 v200, v201, v200, s54\n" \
"v_mfma_f32_16x16x32_bf16 v[12:15], a[236:239], a[12:15], v[12:15]\n" \
"ds_read_b64_tr_b16 a[214:215], v227 offset:55296\n" \
"v_add_f32_dpp v222, v222, v222 row_ror:8 row_mask:0xf bank_mask:0xf\n" \
"v_mfma_f32_16x16x32_bf16 v[44:47], a[228:231], a[24:27], v[44:47]\n" \
"v_perm_b32 v201, v203, v202, s54\n" \
"v_mov_b32_e64 v223, v222\n" \
"v_mfma_f32_16x16x32_bf16 v[44:47], a[236:239], a[28:31], v[44:47]\n" \
"ds_write_b64 v218, v[200:201] offset:10240\n" \
"s_nop 0\n" \
"v_permlane16_swap_b32_e32 v222, v223\n" \
"v_mfma_f32_16x16x32_bf16 v[76:79], a[228:231], a[40:43], v[76:79]\n" \
"v_perm_b32 v204, v205, v204, s54\n" \
"v_mfma_f32_16x16x32_bf16 v[76:79], a[236:239], a[44:47], v[76:79]\n" \
"v_add_f32_e64 v222, v222, v223\n" \
"v_mfma_f32_16x16x32_bf16 v[108:111], a[228:231], a[56:59], v[108:111]\n" \
"ds_read_b64_tr_b16 a[208:209], v227 offset:57344\n" \
"v_perm_b32 v205, v207, v206, s54\n" \
"v_mfma_f32_16x16x32_bf16 v[108:111], a[236:239], a[60:63], v[108:111]\n" \
"s_waitcnt vmcnt(63) lgkmcnt(7)\n" \
"s_nop 0\n" \
"v_mfma_f32_16x16x32_bf16 v[4:7], a[232:235], a[128:131], v[4:7]\n" \
"ds_read_b64_tr_b16 a[210:211], v227 offset:59392\n" \
"v_mov_b32_e64 v223, v222\n" \
"v_mfma_f32_16x16x32_bf16 v[4:7], a[196:199], a[132:135], v[4:7]\n" \
"ds_read_b64_tr_b16 a[216:217], v227 offset:61440\n" \
"ds_write_b64 v218, v[204:205] offset:14336\n" \
"v_mfma_f32_16x16x32_bf16 v[36:39], a[232:235], a[144:147], v[36:39]\n" \
"ds_read_b64_tr_b16 a[218:219], v227 offset:63488\n" \
"s_nop 0\n" \
"v_permlane32_swap_b32_e32 v222, v223\n" \
"v_mfma_f32_16x16x32_bf16 v[36:39], a[196:199], a[148:151], v[36:39]\n" \
"v_lshl_add_u32 v219, %[wid], 4, v219\n" \
"v_add_f32_e64 v222, v222, v223\n" \
"v_mfma_f32_16x16x32_bf16 v[68:71], a[232:235], a[160:163], v[68:71]\n" \
"s_cmp_lg_u64 s[56:57], 0\n" \
"s_cselect_b32 s61, 1, 0\n" \
"v_mov_b32_e64 v192, s61\n" \
"v_mfma_f32_16x16x32_bf16 v[68:71], a[196:199], a[164:167], v[68:71]\n" \
"v_mov_b32_e64 v193, v192\n" \
"v_mfma_f32_16x16x32_bf16 v[100:103], a[232:235], a[176:179], v[100:103]\n" \
"ds_read_b64_tr_b16 a[224:225], v215 offset:16896\n" \
"ds_write_b32 v219, v222 offset:12424\n" \
"v_mfma_f32_16x16x32_bf16 v[100:103], a[196:199], a[180:183], v[100:103]\n" \
"ds_read_b64_tr_b16 a[226:227], v215 offset:18944\n" \
"ds_write_b64 v219, v[192:193] offset:12416\n" \
"v_mfma_f32_16x16x32_bf16 v[4:7], a[200:203], a[136:139], v[4:7]\n" \
"ds_read_b64_tr_b16 a[228:229], v215 offset:20992\n" \
"v_mfma_f32_16x16x32_bf16 v[4:7], a[204:207], a[140:143], v[4:7]\n" \
"ds_read_b64_tr_b16 a[230:231], v215 offset:23040\n" \
".if . - adaln_body != 57344\n" \
".error \"size model\"\n" \
".endif\n" \
"Lk13_%=:\n" \
"s_cbranch_execz adaln_pg0\n" \
"s_nop 0\n" \
"v_mfma_f32_16x16x32_bf16 v[36:39], a[200:203], a[152:155], v[36:39]\n" \
"v_mfma_f32_16x16x32_bf16 v[36:39], a[204:207], a[156:159], v[36:39]\n" \
"v_mfma_f32_16x16x32_bf16 v[68:71], a[200:203], a[168:171], v[68:71]\n" \
"v_mfma_f32_16x16x32_bf16 v[68:71], a[204:207], a[172:175], v[68:71]\n" \
"v_mfma_f32_16x16x32_bf16 v[100:103], a[200:203], a[184:187], v[100:103]\n" \
"ds_read_b64_tr_b16 a[236:237], v215 offset:25088\n" \
"v_mfma_f32_16x16x32_bf16 v[100:103], a[204:207], a[188:191], v[100:103]\n" \
"s_waitcnt vmcnt(63) lgkmcnt(3)\n" \
"s_barrier\n" \
"v_mfma_f32_16x16x32_bf16 v[12:15], a[220:223], a[128:131], v[12:15]\n" \
"ds_read_b64_tr_b16 a[238:239], v215 offset:27136\n" \
"ds_read_b128 v[204:207], v210 offset:12416\n" \
"v_mfma_f32_16x16x32_bf16 v[12:15], a[212:215], a[132:135], v[12:15]\n" \
"ds_read_b64_tr_b16 a[232:233], v215 offset:29184\n" \
"v_lshrrev_b32_e64 v200, 3, v224\n" \
"v_mfma_f32_16x16x32_bf16 v[44:47], a[220:223], a[144:147], v[44:47]\n" \
"ds_read_b64_tr_b16 a[234:235], v215 offset:31232\n" \
"ds_read_b128 v[160:163], v208 offset:0\n" \
"v_mfma_f32_16x16x32_bf16 v[44:47], a[212:215], a[148:151], v[44:47]\n" \
"v_bfe_u32 v201, v224, 8, 3\n" \
"v_and_b32_e64 v200, 16, v200\n" \
"v_mfma_f32_16x16x32_bf16 v[76:79], a[220:223], a[160:163], v[76:79]\n" \
"ds_read_b128 v[164:167], v208 offset:2048\n" \
"v_lshlrev_b32_e64 v201, v201, 1\n" \
"v_mfma_f32_16x16x32_bf16 v[76:79], a[212:215], a[164:167], v[76:79]\n" \
"s_movk_i32 s61, 0x3f80\n" \
"s_nop 0\n" \
"v_lshlrev_b32_e64 v200, v200, s61\n" \
"v_readlane_b32 s68, %[thi], 18\n" \
"v_and_b32_e64 v216, 64, v226\n" \
"v_mfma_f32_16x16x32_bf16 v[108:111], a[220:223], a[176:179], v[108:111]\n" \
"ds_read_b64_tr_b16 a[196:197], v227 offset:16896\n" \
"ds_read_b128 v[168:171], v208 offset:128\n" \
"v_mfma_f32_16x16x32_bf16 v[108:111], a[212:215], a[180:183], v[108:111]\n" \
"ds_read_b64_tr_b16 a[198:199], v227 offset:18944\n" \
"v_bfe_i32 v192, v201, 0, 1\n" \
"v_add_u32_e64 v226, v216, v226\n" \
"v_mfma_f32_16x16x32_bf16 v[12:15], a[208:211], a[136:139], v[12:15]\n" \
"ds_read_b64_tr_b16 a[200:201], v227 offset:20992\n" \
"ds_read_b128 v[172:175], v208 offset:2176\n" \
"v_mfma_f32_16x16x32_bf16 v[12:15], a[216:219], a[140:143], v[12:15]\n" \
"ds_read_b64_tr_b16 a[202:203], v227 offset:23040\n" \
"v_and_b32_e32 v192, v200, v192\n" \
"v_lshlrev_b32_e32 v226, 7, v226\n" \
"v_mfma_f32_16x16x32_bf16 v[44:47], a[208:211], a[152:155], v[44:47]\n" \
"ds_read_b128 v[176:179], v209 offset:0\n" \
"v_bfe_i32 v193, v201, 1, 1\n" \
"v_mfma_f32_16x16x32_bf16 v[44:47], a[216:219], a[156:159], v[44:47]\n" \
"v_and_b32_e64 v193, v200, v193\n" \
"v_bfe_i32 v194, v201, 2, 1\n" \
"v_mfma_f32_16x16x32_bf16 v[76:79], a[208:211], a[168:171], v[76:79]\n" \
"ds_read_b128 v[180:183], v209 offset:2048\n" \
"v_and_b32_e64 v194, v200, v194\n" \
"v_mfma_f32_16x16x32_bf16 v[76:79], a[216:219], a[172:175], v[76:79]\n" \
"v_bfe_i32 v195, v201, 3, 1\n" \
"v_and_b32_e64 v195, v200, v195\n" \
"buffer_load_dword v216, v226, %[srdz], s68 offen offset:68 sc1\n" \
"v_mfma_f32_16x16x32_bf16 v[108:111], a[208:211], a[184:187], v[108:111]\n" \
"ds_read_b64_tr_b16 a[204:205], v227 offset:25088\n" \
"ds_read_b128 v[184:187], v209 offset:128\n" \
"v_lshrrev_b32_e64 v217, 3, v224\n" \
"s_add_u32 s51, %[sin], 4096\n" \
"v_mfma_f32_16x16x32_bf16 v[108:111], a[216:219], a[188:191], v[108:111]\n" \
"s_waitcnt vmcnt(63) lgkmcnt(12)\n" \
"s_nop 0\n" \
"v_mfma_f32_16x16x32_bf16 v[20:23], a[224:227], v[0:3], v[20:23]\n" \
"ds_read_b64_tr_b16 a[206:207], v227 offset:27136\n" \
"ds_read_b128 v[188:191], v209 offset:2176\n" \
"v_mfma_f32_16x16x32_bf16 v[20:23], a[228:231], v[8:11], v[20:23]\n" \
"ds_read_b64_tr_b16 a[220:221], v227 offset:29184\n" \
"v_bfe_i32 v196, v201, 4, 1\n" \
"v_mfma_f32_16x16x32_bf16 v[52:55], a[224:227], v[32:35], v[52:55]\n" \
"ds_read_b64_tr_b16 a[222:223], v227 offset:31232\n" \
"v_and_b32_e64 v196, v200, v196\n" \
"v_mfma_f32_16x16x32_bf16 v[52:55], a[228:231], v[40:43], v[52:55]\n" \
"v_bfe_i32 v197, v201, 5, 1\n" \
"v_and_b32_e64 v197, v200, v197\n" \
"v_mfma_f32_16x16x32_bf16 v[84:87], a[224:227], v[64:67], v[84:87]\n" \
"v_cvt_pk_bf16_f32 v4, v4, v5\n" \
"v_bfe_i32 v198, v201, 6, 1\n" \
"v_mfma_f32_16x16x32_bf16 v[84:87], a[228:231], v[72:75], v[84:87]\n" \
"v_cvt_pk_bf16_f32 v5, v6, v7\n" \
"v_and_b32_e64 v198, v200, v198\n" \
"v_mfma_f32_16x16x32_bf16 v[116:119], a[224:227], v[96:99], v[116:119]\n" \
"ds_read_b64_tr_b16 a[212:213], v215 offset:33280\n" \
"v_cvt_pk_bf16_f32 v6, v12, v13\n" \
"v_mfma_f32_16x16x32_bf16 v[116:119], a[228:231], v[104:107], v[116:119]\n" \
"ds_read_b64_tr_b16 a[214:215], v215 offset:35328\n" \
"v_cvt_pk_bf16_f32 v7, v14, v15\n" \
"v_mfma_f32_16x16x32_bf16 v[20:23], a[236:239], v[16:19], v[20:23]\n" \
"ds_read_b64_tr_b16 a[208:209], v215 offset:37376\n" \
"v_bfe_i32 v199, v201, 7, 1\n" \
"v_mfma_f32_16x16x32_bf16 v[20:23], a[232:235], v[24:27], v[20:23]\n" \
"ds_read_b64_tr_b16 a[210:211], v215 offset:39424\n" \
"v_and_b32_e64 v199, v200, v199\n" \
"v_mfma_f32_16x16x32_bf16 v[52:55], a[236:239], v[48:51], v[52:55]\n" \
"s_add_u32 s55, s46, 16384\n" \
"buffer_store_dwordx4 v[4:7], %[vxs], %[srdz], s55 offen offset:0 sc1\n" \
"v_mfma_f32_16x16x32_bf16 v[52:55], a[232:235], v[56:59], v[52:55]\n" \
"v_cvt_pk_bf16_f32 v36, v36, v37\n" \
"v_cvt_pk_bf16_f32 v37, v38, v39\n" \
"v_mfma_f32_16x16x32_bf16 v[84:87], a[236:239], v[80:83], v[84:87]\n" \
"v_cvt_pk_bf16_f32 v38, v44, v45\n" \
"s_waitcnt lgkmcnt(14)\n" \
"s_nop 0\n" \
"v_add_f32_dpp v206, v206, v206 quad_perm:[1,0,3,2] row_mask:0xf bank_mask:0xf\n" \
"v_mfma_f32_16x16x32_bf16 v[84:87], a[232:235], v[88:91], v[84:87]\n" \
"v_cvt_pk_bf16_f32 v39, v46, v47\n" \
"v_mfma_f32_16x16x32_bf16 v[116:119], a[236:239], v[112:115], v[116:119]\n" \
"ds_read_b64_tr_b16 a[216:217], v215 offset:41472\n" \
"v_add_f32_dpp v206, v206, v206 quad_perm:[2,3,0,1] row_mask:0xf bank_mask:0xf\n" \
"v_mfma_f32_16x16x32_bf16 v[116:119], a[232:235], v[120:123], v[116:119]\n" \
"s_waitcnt vmcnt(63) lgkmcnt(5)\n" \
"s_nop 0\n" \
"v_mfma_f32_16x16x32_bf16 v[28:31], a[196:199], v[0:3], v[28:31]\n" \
"ds_read_b64_tr_b16 a[218:219], v215 offset:43520\n" \
"v_cvt_pk_bf16_f32 v68, v68, v69\n" \
"v_mfma_f32_16x16x32_bf16 v[28:31], a[200:203], v[8:11], v[28:31]\n" \
"ds_read_b64_tr_b16 a[224:225], v215 offset:45568\n" \
"s_add_u32 s55, s46, 540672\n" \
"buffer_store_dwordx4 v[36:39], %[vxs], %[srdz], s55 offen offset:0 sc1\n" \
"v_mfma_f32_16x16x32_bf16 v[60:63], a[196:199], v[32:35], v[60:63]\n" \
"ds_read_b64_tr_b16 a[226:227], v215 offset:47616\n" \
"v_cvt_pk_bf16_f32 v69, v70, v71\n" \
"v_mfma_f32_16x16x32_bf16 v[60:63], a[200:203], v[40:43], v[60:63]\n" \
"v_cvt_pk_bf16_f32 v70, v76, v77\n" \
"v_cvt_pk_bf16_f32 v71, v78, v79\n" \
"v_mfma_f32_16x16x32_bf16 v[92:95], a[196:199], v[64:67], v[92:95]\n" \
"s_add_u32 s55, s46, 1064960\n" \
"buffer_store_dwordx4 v[68:71], %[vxs], %[srdz], s55 offen offset:0 sc1\n" \
"v_mfma_f32_16x16x32_bf16 v[92:95], a[200:203], v[72:75], v[92:95]\n" \
"v_mfma_f32_16x16x32_bf16 v[124:127], a[196:199], v[96:99], v[124:127]\n" \
"ds_read_b64_tr_b16 a[228:229], v227 offset:33280\n" \
"v_cvt_pk_bf16_f32 v100, v100, v101\n" \
"v_mfma_f32_16x16x32_bf16 v[124:127], a[200:203], v[104:107], v[124:127]\n" \
"ds_read_b64_tr_b16 a[230:231], v227 offset:35328\n" \
"v_cvt_pk_bf16_f32 v101, v102, v103\n" \
"v_mfma_f32_16x16x32_bf16 v[28:31], a[204:207], v[16:19], v[28:31]\n" \
"ds_read_b64_tr_b16 a[236:237], v227 offset:37376\n" \
"v_cvt_pk_bf16_f32 v102, v108, v109\n" \
"v_mfma_f32_16x16x32_bf16 v[28:31], a[220:223], v[24:27], v[28:31]\n" \
"ds_read_b64_tr_b16 a[238:239], v227 offset:39424\n" \
"v_cvt_pk_bf16_f32 v103, v110, v111\n" \
"v_mfma_f32_16x16x32_bf16 v[60:63], a[204:207], v[48:51], v[60:63]\n" \
"s_add_u32 s55, s46, 1589248\n" \
"buffer_store_dwordx4 v[100:103], %[vxs], %[srdz], s55 offen offset:0 sc1\n" \
"v_mfma_f32_16x16x32_bf16 v[60:63], a[220:223], v[56:59], v[60:63]\n" \
"v_mfma_f32_16x16x32_bf16 v[92:95], a[204:207], v[80:83], v[92:95]\n" \
"v_mov_b32_e64 v230, 1\n" \
"buffer_store_dword v230, v225, %[srdz], s62 offen offset:64 sc1\n" \
"v_mfma_f32_16x16x32_bf16 v[92:95], a[220:223], v[88:91], v[92:95]\n" \
"ds_read_b128 v[4:7], v208 offset:4096\n" \
"v_mfma_f32_16x16x32_bf16 v[124:127], a[204:207], v[112:115], v[124:127]\n" \
"ds_read_b64_tr_b16 a[232:233], v227 offset:41472\n" \
"v_mfma_f32_16x16x32_bf16 v[124:127], a[220:223], v[120:123], v[124:127]\n" \
"s_waitcnt vmcnt(63) lgkmcnt(6)\n" \
"s_nop 0\n" \
"v_mfma_f32_16x16x32_bf16 v[20:23], a[212:215], a[0:3], v[20:23]\n" \
"ds_read_b64_tr_b16 a[234:235], v227 offset:43520\n" \
"ds_read_b128 v[12:15], v208 offset:6144\n" \
"v_mfma_f32_16x16x32_bf16 v[20:23], a[208:211], a[4:7], v[20:23]\n" \
"ds_read_b64_tr_b16 a[196:197], v227 offset:45568\n" \
"v_mfma_f32_16x16x32_bf16 v[52:55], a[212:215], a[16:19], v[52:55]\n" \
"ds_read_b64_tr_b16 a[198:199], v227 offset:47616\n" \
"v_mfma_f32_16x16x32_bf16 v[52:55], a[208:211], a[20:23], v[52:55]\n" \
"ds_read_b128 v[36:39], v208 offset:4224\n" \
"v_mfma_f32_16x16x32_bf16 v[84:87], a[212:215], a[32:35], v[84:87]\n" \
"v_mfma_f32_16x16x32_bf16 v[84:87], a[208:211], a[36:39], v[84:87]\n" \
"v_mfma_f32_16x16x32_bf16 v[116:119], a[212:215], a[48:51], v[116:119]\n" \
"ds_read_b64_tr_b16 a[200:201], v215 offset:49664\n" \
"ds_read_b128 v[44:47], v208 offset:6272\n" \
"v_mfma_f32_16x16x32_bf16 v[116:119], a[208:211], a[52:55], v[116:119]\n" \
"ds_read_b64_tr_b16 a[202:203], v215 offset:51712\n" \
"v_mfma_f32_16x16x32_bf16 v[20:23], a[216:219], a[8:11], v[20:23]\n" \
"ds_read_b64_tr_b16 a[204:205], v215 offset:53760\n" \
"v_mfma_f32_16x16x32_bf16 v[20:23], a[224:227], a[12:15], v[20:23]\n" \
"ds_read_b64_tr_b16 a[206:207], v215 offset:55808\n" \
"ds_read_b128 v[68:71], v209 offset:4096\n" \
"v_mfma_f32_16x16x32_bf16 v[52:55], a[216:219], a[24:27], v[52:55]\n" \
"ds_read_b64_tr_b16 a[220:221], v215 offset:57856\n" \
"v_mfma_f32_16x16x32_bf16 v[52:55], a[224:227], a[28:31], v[52:55]\n" \
"ds_read_b64_tr_b16 a[222:223], v215 offset:59904\n" \
"v_mfma_f32_16x16x32_bf16 v[84:87], a[216:219], a[40:43], v[84:87]\n" \
"ds_read_b64_tr_b16 a[212:213], v215 offset:61952\n" \
"ds_read_b128 v[76:79], v209 offset:6144\n" \
"v_mfma_f32_16x16x32_bf16 v[84:87], a[224:227], a[44:47], v[84:87]\n" \
"ds_read_b64_tr_b16 a[214:215], v215 offset:64000\n" \
"v_mfma_f32_16x16x32_bf16 v[116:119], a[216:219], a[56:59], v[116:119]\n" \
"ds_read_b64_tr_b16 a[208:209], v227 offset:49664\n" \
"v_mfma_f32_16x16x32_bf16 v[116:119], a[224:227], a[60:63], v[116:119]\n" \
"s_waitcnt vmcnt(63) lgkmcnt(13)\n" \
"s_nop 0\n" \
"v_mfma_f32_16x16x32_bf16 v[28:31], a[228:231], a[0:3], v[28:31]\n" \
"ds_read_b64_tr_b16 a[210:211], v227 offset:51712\n" \
"ds_read_b128 v[100:103], v209 offset:4224\n" \
"v_mfma_f32_16x16x32_bf16 v[28:31], a[236:239], a[4:7], v[28:31]\n" \
"ds_read_b64_tr_b16 a[216:217], v227 offset:53760\n" \
"v_mfma_f32_16x16x32_bf16 v[60:63], a[228:231], a[16:19], v[60:63]\n" \
"ds_read_b64_tr_b16 a[218:219], v227 offset:55808\n" \
"ds_read_b128 v[108:111], v209 offset:6272\n" \
"v_mfma_f32_16x16x32_bf16 v[60:63], a[236:239], a[20:23], v[60:63]\n" \
"ds_read_b64_tr_b16 a[224:225], v227 offset:57856\n" \
"v_mfma_f32_16x16x32_bf16 v[92:95], a[228:231], a[32:35], v[92:95]\n" \
"ds_read_b64_tr_b16 a[226:227], v227 offset:59904\n" \
"v_mfma_f32_16x16x32_bf16 v[92:95], a[236:239], a[36:39], v[92:95]\n" \
"v_mfma_f32_16x16x32_bf16 v[124:127], a[228:231], a[48:51], v[124:127]\n" \
"v_mfma_f32_16x16x32_bf16 v[124:127], a[236:239], a[52:55], v[124:127]\n" \
"ds_read_b64_tr_b16 a[228:229], v227 offset:61952\n" \
"v_mfma_f32_16x16x32_bf16 v[28:31], a[232:235], a[8:11], v[28:31]\n" \
"ds_read_b64_tr_b16 a[230:231], v227 offset:64000\n" \
"s_waitcnt vmcnt(5)\n" \
"v_cmp_eq_u32_e32 vcc, 0, v216\n" \
"s_nop 0\n" \
"s_cmp_eq_u64 vcc, 0\n" \
"s_cselect_b32 %[got], 1, 0\n" \
"s_nop 0\n" \
"s_cselect_b32 s54, %[sin], 0x80000000\n" \
"s_cselect_b32 s51, s51, 0x80000000\n" \
"v_mfma_f32_16x16x32_bf16 v[28:31], a[196:199], a[12:15], v[28:31]\n" \
"v_mfma_f32_16x16x32_bf16 v[60:63], a[232:235], a[24:27], v[60:63]\n" \
"buffer_load_dwordx4 a[64:67], v217, %[srdz], s54 offen offset:0 sc1\n" \
"v_mfma_f32_16x16x32_bf16 v[60:63], a[196:199], a[28:31], v[60:63]\n" \
"v_mfma_f32_16x16x32_bf16 v[92:95], a[232:235], a[40:43], v[92:95]\n" \
"buffer_load_dwordx4 a[68:71], v217, %[srdz], s54 offen offset:1024 sc1\n" \
"v_mfma_f32_16x16x32_bf16 v[92:95], a[196:199], a[44:47], v[92:95]\n" \
"v_mfma_f32_16x16x32_bf16 v[124:127], a[232:235], a[56:59], v[124:127]\n" \
"buffer_load_dwordx4 a[72:75], v217, %[srdz], s54 offen offset:2048 sc1\n" \
"v_mfma_f32_16x16x32_bf16 v[124:127], a[196:199], a[60:63], v[124:127]\n" \
"s_waitcnt vmcnt(63) lgkmcnt(0)\n" \
"s_nop 0\n" \
"v_mfma_f32_16x16x32_bf16 v[20:23], a[200:203], a[128:131], v[20:23]\n" \
"buffer_load_dwordx4 a[76:79], v217, %[srdz], s54 offen offset:3072 sc1\n" \
"v_mfma_f32_16x16x32_bf16 v[20:23], a[204:207], a[132:135], v[20:23]\n" \
"v_mfma_f32_16x16x32_bf16 v[20:23], a[220:223], a[136:139], v[20:23]\n" \
"buffer_load_dwordx4 a[80:83], v217, %[srdz], s51 offen offset:0 sc1\n" \
"v_mfma_f32_16x16x32_bf16 v[20:23], a[212:215], a[140:143], v[20:23]\n" \
"v_mfma_f32_16x16x32_bf16 v[28:31], a[208:211], a[128:131], v[28:31]\n" \
"buffer_load_dwordx4 a[84:87], v217, %[srdz], s51 offen offset:1024 sc1\n" \
"v_mfma_f32_16x16x32_bf16 v[28:31], a[216:219], a[132:135], v[28:31]\n" \
"v_mfma_f32_16x16x32_bf16 v[28:31], a[224:227], a[136:139], v[28:31]\n" \
"buffer_load_dwordx4 a[88:91], v217, %[srdz], s51 offen offset:2048 sc1\n" \
"v_mfma_f32_16x16x32_bf16 v[28:31], a[228:231], a[140:143], v[28:31]\n" \
"v_mfma_f32_16x16x32_bf16 v[52:55], a[200:203], a[144:147], v[52:55]\n" \
"buffer_load_dwordx4 a[92:95], v217, %[srdz], s51 offen offset:3072 sc1\n" \
"v_mfma_f32_16x16x32_bf16 v[52:55], a[204:207], a[148:151], v[52:55]\n" \
"v_mfma_f32_16x16x32_bf16 v[52:55], a[220:223], a[152:155], v[52:55]\n" \
"v_mfma_f32_16x16x32_bf16 v[52:55], a[212:215], a[156:159], v[52:55]\n" \
"v_cvt_pk_bf16_f32 v20, v20, v21\n" \
"v_cvt_pk_bf16_f32 v21, v22, v23\n" \
"v_mfma_f32_16x16x32_bf16 v[60:63], a[208:211], a[144:147], v[60:63]\n" \
"buffer_load_dwordx2 v[228:229], v226, %[srdz], s68 offen offset:64 sc1\n" \
"v_mfma_f32_16x16x32_bf16 v[60:63], a[216:219], a[148:151], v[60:63]\n" \
"v_cvt_pk_bf16_f32 v22, v28, v29\n" \
"v_cvt_pk_bf16_f32 v23, v30, v31\n" \
"v_mfma_f32_16x16x32_bf16 v[60:63], a[224:227], a[152:155], v[60:63]\n" \
"s_add_u32 s55, s46, 20480\n" \
"buffer_store_dwordx4 v[20:23], %[vxs], %[srdz], s55 offen offset:0 sc1\n" \
"v_mfma_f32_16x16x32_bf16 v[60:63], a[228:231], a[156:159], v[60:63]\n" \
"v_mfma_f32_16x16x32_bf16 v[84:87], a[200:203], a[160:163], v[84:87]\n" \
"v_mfma_f32_16x16x32_bf16 v[84:87], a[204:207], a[164:167], v[84:87]\n" \
"v_mfma_f32_16x16x32_bf16 v[84:87], a[220:223], a[168:171], v[84:87]\n" \
"v_mfma_f32_16x16x32_bf16 v[84:87], a[212:215], a[172:175], v[84:87]\n" \
"v_cvt_pk_bf16_f32 v52, v52, v53\n" \
"v_cvt_pk_bf16_f32 v53, v54, v55\n" \
"v_mfma_f32_16x16x32_bf16 v[92:95], a[208:211], a[160:163], v[92:95]\n" \
"v_mfma_f32_16x16x32_bf16 v[92:95], a[216:219], a[164:167], v[92:95]\n" \
"v_cvt_pk_bf16_f32 v54, v60, v61\n" \
"v_cvt_pk_bf16_f32 v55, v62, v63\n" \
"v_mfma_f32_16x16x32_bf16 v[92:95], a[224:227], a[168:171], v[92:95]\n" \
"s_add_u32 s55, s46, 544768\n" \
"buffer_store_dwordx4 v[52:55], %[vxs], %[srdz], s55 offen offset:0 sc1\n" \
"v_mfma_f32_16x16x32_bf16 v[92:95], a[228:231], a[172:175], v[92:95]\n" \
"v_mfma_f32_16x16x32_bf16 v[116:119], a[200:203], a[176:179], v[116:119]\n" \
"v_mfma_f32_16x16x32_bf16 v[116:119], a[204:207], a[180:183], v[116:119]\n" \
"v_mfma_f32_16x16x32_bf16 v[116:119], a[220:223], a[184:187], v[116:119]\n" \
"v_mfma_f32_16x16x32_bf16 v[116:119], a[212:215], a[188:191], v[116:119]\n" \
"v_cvt_pk_bf16_f32 v84, v84, v85\n" \
"v_cvt_pk_bf16_f32 v85, v86, v87\n" \
"v_mfma_f32_16x16x32_bf16 v[124:127], a[208:211], a[176:179], v[124:127]\n" \
"v_mfma_f32_16x16x32_bf16 v[124:127], a[216:219], a[180:183], v[124:127]\n" \
"v_cvt_pk_bf16_f32 v86, v92, v93\n" \
"v_cvt_pk_bf16_f32 v87, v94, v95\n" \
"v_mfma_f32_16x16x32_bf16 v[124:127], a[224:227], a[184:187], v[124:127]\n" \
"s_add_u32 s55, s46, 1069056\n" \
"buffer_store_dwordx4 v[84:87], %[vxs], %[srdz], s55 offen offset:0 sc1\n" \
"v_mfma_f32_16x16x32_bf16 v[124:127], a[228:231], a[188:191], v[124:127]\n" \
"v_cvt_pk_bf16_f32 v116, v116, v117\n" \
"v_cvt_pk_bf16_f32 v117, v118, v119\n" \
"s_waitcnt lgkmcnt(0)\n" \
"s_nop 4\n" \
"v_cvt_pk_bf16_f32 v118, v124, v125\n" \
"v_cvt_pk_bf16_f32 v119, v126, v127\n" \
"s_add_u32 s55, s46, 1593344\n" \
"buffer_store_dwordx4 v[116:119], %[vxs], %[srdz], s55 offen offset:0 sc1\n" \
"s_branch Lend_%=\n" \
"s_nop 0\n" \
"Lsp1_%=:\n" \
"s_movk_i32 s51, 0x1000\n" \
"s_cmp_gt_u32 s48, 0\n" \
"s_cselect_b32 s61, 0, 1\n" \
"s_nop 0\n" \
"s_mul_i32 s61, s61, 262144\n" \
"Lsq1_%=:\n" \
"buffer_load_dwordx4 a[64:67], %[oq0], %[srdz], s61 offen offset:0 sc1\n" \
"buffer_load_dwordx4 a[68:71], %[oq0], %[srdz], s61 offen offset:1024 sc1\n" \
"buffer_load_dwordx4 a[72:75], %[oq0], %[srdz], s61 offen offset:2048 sc1\n" \
"buffer_load_dwordx4 a[76:79], %[oq0], %[srdz], s61 offen offset:3072 sc1\n" \
"buffer_load_dwordx4 a[80:83], v208, %[srdz], s61 offen offset:0 sc1\n" \
"buffer_load_dwordx4 a[84:87], v208, %[srdz], s61 offen offset:1024 sc1\n" \
"buffer_load_dwordx4 a[88:91], v208, %[srdz], s61 offen offset:2048 sc1\n" \
"buffer_load_dwordx4 a[92:95], v208, %[srdz], s61 offen offset:3072 sc1\n" \
"buffer_load_dwordx4 a[96:99], v209, %[srdz], s61 offen offset:0 sc1\n" \
"buffer_load_dwordx4 a[100:103], v209, %[srdz], s61 offen offset:1024 sc1\n" \
"buffer_load_dwordx4 a[104:107], v209, %[srdz], s61 offen offset:2048 sc1\n" \
"buffer_load_dwordx4 a[108:111], v209, %[srdz], s61 offen offset:3072 sc1\n" \
"buffer_load_dwordx4 a[112:115], v210, %[srdz], s61 offen offset:0 sc1\n" \
"buffer_load_dwordx4 a[116:119], v210, %[srdz], s61 offen offset:1024 sc1\n" \
"buffer_load_dwordx4 a[120:123], v210, %[srdz], s61 offen offset:2048 sc1\n" \
"buffer_load_dwordx4 a[124:127], v210, %[srdz], s61 offen offset:3072 sc1\n" \
"s_waitcnt vmcnt(0)\n" \
"s_nop 0\n" \
"v_mfma_f32_16x16x32_bf16 v[128:131], a[236:239], a[64:67], v[192:195]\n" \
"v_mfma_f32_16x16x32_bf16 v[128:131], a[196:199], a[68:71], v[128:131]\n" \
"v_mfma_f32_16x16x32_bf16 v[128:131], a[200:203], a[72:75], v[128:131]\n" \
"v_mfma_f32_16x16x32_bf16 v[128:131], a[204:207], a[76:79], v[128:131]\n" \
"v_mfma_f32_16x16x32_bf16 v[132:135], a[236:239], a[80:83], v[196:199]\n" \
"v_mfma_f32_16x16x32_bf16 v[132:135], a[196:199], a[84:87], v[132:135]\n" \
"v_mfma_f32_16x16x32_bf16 v[132:135], a[200:203], a[88:91], v[132:135]\n" \
"v_mfma_f32_16x16x32_bf16 v[132:135], a[204:207], a[92:95], v[132:135]\n" \
"v_mfma_f32_16x16x32_bf16 v[136:139], a[236:239], a[96:99], v[200:203]\n" \
"v_mfma_f32_16x16x32_bf16 v[136:139], a[196:199], a[100:103], v[136:139]\n" \
"v_mfma_f32_16x16x32_bf16 v[136:139], a[200:203], a[104:107], v[136:139]\n" \
"v_mfma_f32_16x16x32_bf16 v[136:139], a[204:207], a[108:111], v[136:139]\n" \
"v_mfma_f32_16x16x32_bf16 v[140:143], a[236:239], a[112:115], v[204:207]\n" \
"v_mfma_f32_16x16x32_bf16 v[140:143], a[196:199], a[116:119], v[140:143]\n" \
"v_mfma_f32_16x16x32_bf16 v[140:143], a[200:203], a[120:123], v[140:143]\n" \
"v_mfma_f32_16x16x32_bf16 v[140:143], a[204:207], a[124:127], v[140:143]\n" \
"s_nop 7\n" \
"s_nop 0\n" \
"v_cmp_u_f32_e64 s[44:45], v128, v132\n" \
"v_cmp_u_f32_e64 s[56:57], v136, v140\n" \
"s_or_b64 s[56:57], s[56:57], s[44:45]\n" \
"s_cbranch_scc0 Lbk1_%=\n" \
"s_sub_u32 s51, s51, 1\n" \
"s_cbranch_scc0 Lsq1_%=\n" \
"s_branch Lbk1_%=\n" \
"Lsp2_%=:\n" \
"s_movk_i32 s51, 0x1000\n" \
"s_cmp_gt_u32 s48, 1\n" \
"s_cselect_b32 s61, 1, 2\n" \
"s_mul_i32 s61, s61, 262144\n" \
"Lsq2_%=:\n" \
"buffer_load_dwordx4 a[0:3], %[oq0], %[srdz], s61 offen offset:0 sc1\n" \
"buffer_load_dwordx4 a[4:7], %[oq0], %[srdz], s61 offen offset:1024 sc1\n" \
"buffer_load_dwordx4 a[8:11], %[oq0], %[srdz], s61 offen offset:2048 sc1\n" \
"buffer_load_dwordx4 a[12:15], %[oq0], %[srdz], s61 offen offset:3072 sc1\n" \
"buffer_load_dwordx4 a[16:19], v208, %[srdz], s61 offen offset:0 sc1\n" \
"buffer_load_dwordx4 a[20:23], v208, %[srdz], s61 offen offset:1024 sc1\n" \
"buffer_load_dwordx4 a[24:27], v208, %[srdz], s61 offen offset:2048 sc1\n" \
"buffer_load_dwordx4 a[28:31], v208, %[srdz], s61 offen offset:3072 sc1\n" \
"buffer_load_dwordx4 a[32:35], v209, %[srdz], s61 offen offset:0 sc1\n" \
"buffer_load_dwordx4 a[36:39], v209, %[srdz], s61 offen offset:1024 sc1\n" \
"buffer_load_dwordx4 a[40:43], v209, %[srdz], s61 offen offset:2048 sc1\n" \
"buffer_load_dwordx4 a[44:47], v209, %[srdz], s61 offen offset:3072 sc1\n" \
"buffer_load_dwordx4 a[48:51], v210, %[srdz], s61 offen offset:0 sc1\n" \
"buffer_load_dwordx4 a[52:55], v210, %[srdz], s61 offen offset:1024 sc1\n" \
"buffer_load_dwordx4 a[56:59], v210, %[srdz], s61 offen offset:2048 sc1\n" \
"buffer_load_dwordx4 a[60:63], v210, %[srdz], s61 offen offset:3072 sc1\n" \
"s_waitcnt vmcnt(0)\n" \
"s_nop 0\n" \
"v_mfma_f32_16x16x32_bf16 v[192:195], a[196:199], a[0:3], v[128:131]\n" \
"v_mfma_f32_16x16x32_bf16 v[192:195], a[200:203], a[4:7], v[192:195]\n" \
"v_mfma_f32_16x16x32_bf16 v[192:195], a[204:207], a[8:11], v[192:195]\n" \
"v_mfma_f32_16x16x32_bf16 v[192:195], a[232:235], a[12:15], v[192:195]\n" \
"v_mfma_f32_16x16x32_bf16 v[196:199], a[196:199], a[16:19], v[132:135]\n" \
"v_mfma_f32_16x16x32_bf16 v[196:199], a[200:203], a[20:23], v[196:199]\n" \
"v_mfma_f32_16x16x32_bf16 v[196:199], a[204:207], a[24:27], v[196:199]\n" \
"v_mfma_f32_16x16x32_bf16 v[196:199], a[232:235], a[28:31], v[196:199]\n" \
"v_mfma_f32_16x16x32_bf16 v[200:203], a[196:199], a[32:35], v[136:139]\n" \
"v_mfma_f32_16x16x32_bf16 v[200:203], a[200:203], a[36:39], v[200:203]\n" \
"v_mfma_f32_16x16x32_bf16 v[200:203], a[204:207], a[40:43], v[200:203]\n" \
"v_mfma_f32_16x16x32_bf16 v[200:203], a[232:235], a[44:47], v[200:203]\n" \
"v_mfma_f32_16x16x32_bf16 v[204:207], a[196:199], a[48:51], v[140:143]\n" \
"v_mfma_f32_16x16x32_bf16 v[204:207], a[200:203], a[52:55], v[204:207]\n" \
"v_mfma_f32_16x16x32_bf16 v[204:207], a[204:207], a[56:59], v[204:207]\n" \
"v_mfma_f32_16x16x32_bf16 v[204:207], a[232:235], a[60:63], v[204:207]\n" \
"s_nop 7\n" \
"s_nop 0\n" \
"v_cmp_u_f32_e64 s[44:45], v192, v196\n" \
"v_cmp_u_f32_e64 s[56:57], v200, v204\n" \
"s_or_b64 s[56:57], s[56:57], s[44:45]\n" \
"s_cbranch_scc0 Lbk2_%=\n" \
"s_sub_u32 s51, s51, 1\n" \
"s_cbranch_scc0 Lsq2_%=\n" \
"s_branch Lbk2_%=\n" \
"Lsp3_%=:\n" \
"s_movk_i32 s51, 0x1000\n" \
"s_cmp_gt_u32 s48, 2\n" \
"s_cselect_b32 s61, 2, 3\n" \
"s_mul_i32 s61, s61, 262144\n" \
"Lsq3_%=:\n" \
"buffer_load_dwordx4 a[128:131], %[oq0], %[srdz], s61 offen offset:0 sc1\n" \
"buffer_load_dwordx4 a[132:135], %[oq0], %[srdz], s61 offen offset:1024 sc1\n" \
"buffer_load_dwordx4 a[136:139], %[oq0], %[srdz], s61 offen offset:2048 sc1\n" \
"buffer_load_dwordx4 a[140:143], %[oq0], %[srdz], s61 offen offset:3072 sc1\n" \
"buffer_load_dwordx4 a[144:147], v208, %[srdz], s61 offen offset:0 sc1\n" \
"buffer_load_dwordx4 a[148:151], v208, %[srdz], s61 offen offset:1024 sc1\n" \
"buffer_load_dwordx4 a[152:155], v208, %[srdz], s61 offen offset:2048 sc1\n" \
"buffer_load_dwordx4 a[156:159], v208, %[srdz], s61 offen offset:3072 sc1\n" \
"buffer_load_dwordx4 a[160:163], v209, %[srdz], s61 offen offset:0 sc1\n" \
"buffer_load_dwordx4 a[164:167], v209, %[srdz], s61 offen offset:1024 sc1\n" \
"buffer_load_dwordx4 a[168:171], v209, %[srdz], s61 offen offset:2048 sc1\n" \
"buffer_load_dwordx4 a[172:175], v209, %[srdz], s61 offen offset:3072 sc1\n" \
"buffer_load_dwordx4 a[176:179], v210, %[srdz], s61 offen offset:0 sc1\n" \
"buffer_load_dwordx4 a[180:183], v210, %[srdz], s61 offen offset:1024 sc1\n" \
"buffer_load_dwordx4 a[184:187], v210, %[srdz], s61 offen offset:2048 sc1\n" \
"buffer_load_dwordx4 a[188:191], v210, %[srdz], s61 offen offset:3072 sc1\n" \
"s_waitcnt vmcnt(0)\n" \
"s_nop 0\n" \
"v_mfma_f32_16x16x32_bf16 v[128:131], a[196:199], a[128:131], v[192:195]\n" \
"v_mfma_f32_16x16x32_bf16 v[128:131], a[200:203], a[132:135], v[128:131]\n" \
"v_mfma_f32_16x16x32_bf16 v[128:131], a[204:207], a[136:139], v[128:131]\n" \
"v_mfma_f32_16x16x32_bf16 v[128:131], a[224:227], a[140:143], v[128:131]\n" \
"v_mfma_f32_16x16x32_bf16 v[132:135], a[196:199], a[144:147], v[196:199]\n" \
"v_mfma_f32_16x16x32_bf16 v[132:135], a[200:203], a[148:151], v[132:135]\n" \
"v_mfma_f32_16x16x32_bf16 v[132:135], a[204:207], a[152:155], v[132:135]\n" \
"v_mfma_f32_16x16x32_bf16 v[132:135], a[224:227], a[156:159], v[132:135]\n" \
"v_mfma_f32_16x16x32_bf16 v[136:139], a[196:199], a[160:163], v[200:203]\n" \
"v_mfma_f32_16x16x32_bf16 v[136:139], a[200:203], a[164:167], v[136:139]\n" \
"v_mfma_f32_16x16x32_bf16 v[136:139], a[204:207], a[168:171], v[136:139]\n" \
"v_mfma_f32_16x16x32_bf16 v[136:139], a[224:227], a[172:175], v[136:139]\n" \
"v_mfma_f32_16x16x32_bf16 v[140:143], a[196:199], a[176:179], v[204:207]\n" \
"v_mfma_f32_16x16x32_bf16 v[140:143], a[200:203], a[180:183], v[140:143]\n" \
"v_mfma_f32_16x16x32_bf16 v[140:143], a[204:207], a[184:187], v[140:143]\n" \
"v_mfma_f32_16x16x32_bf16 v[140:143], a[224:227], a[188:191], v[140:143]\n" \
"s_nop 7\n" \
"s_nop 0\n" \
"v_cmp_u_f32_e64 s[44:45], v128, v132\n" \
"v_cmp_u_f32_e64 s[56:57], v136, v140\n" \
"s_or_b64 s[56:57], s[56:57], s[44:45]\n" \
"s_cbranch_scc0 Lbk3_%=\n" \
"s_sub_u32 s51, s51, 1\n" \
"s_cbranch_scc0 Lsq3_%=\n" \
"s_branch Lbk3_%=\n" \
"Lsp4_%=:\n" \
"s_movk_i32 s51, 0x1000\n" \
"s_cmp_gt_u32 s48, 3\n" \
"s_cselect_b32 s61, 3, 4\n" \
"s_mul_i32 s61, s61, 262144\n" \
"Lsq4_%=:\n" \
"buffer_load_dwordx4 a[64:67], %[oq0], %[srdz], s61 offen offset:0 sc1\n" \
"buffer_load_dwordx4 a[68:71], %[oq0], %[srdz], s61 offen offset:1024 sc1\n" \
"buffer_load_dwordx4 a[72:75], %[oq0], %[srdz], s61 offen offset:2048 sc1\n" \
"buffer_load_dwordx4 a[76:79], %[oq0], %[srdz], s61 offen offset:3072 sc1\n" \
"buffer_load_dwordx4 a[80:83], v208, %[srdz], s61 offen offset:0 sc1\n" \
"buffer_load_dwordx4 a[84:87], v208, %[srdz], s61 offen offset:1024 sc1\n" \
"buffer_load_dwordx4 a[88:91], v208, %[srdz], s61 offen offset:2048 sc1\n" \
"buffer_load_dwordx4 a[92:95], v208, %[srdz], s61 offen offset:3072 sc1\n" \
"buffer_load_dwordx4 a[96:99], v209, %[srdz], s61 offen offset:0 sc1\n" \
"buffer_load_dwordx4 a[100:103], v209, %[srdz], s61 offen offset:1024 sc1\n" \
"buffer_load_dwordx4 a[104:107], v209, %[srdz], s61 offen offset:2048 sc1\n" \
"buffer_load_dwordx4 a[108:111], v209, %[srdz], s61 offen offset:3072 sc1\n" \
"buffer_load_dwordx4 a[112:115], v210, %[srdz], s61 offen offset:0 sc1\n" \
"buffer_load_dwordx4 a[116:119], v210, %[srdz], s61 offen offset:1024 sc1\n" \
"buffer_load_dwordx4 a[120:123], v210, %[srdz], s61 offen offset:2048 sc1\n" \
"buffer_load_dwordx4 a[124:127], v210, %[srdz], s61 offen offset:3072 sc1\n" \
"s_waitcnt vmcnt(0)\n" \
"s_nop 0\n" \
"v_mfma_f32_16x16x32_bf16 v[192:195], a[196:199], a[64:67], v[128:131]\n" \
"v_mfma_f32_16x16x32_bf16 v[192:195], a[200:203], a[68:71], v[192:195]\n" \
"v_mfma_f32_16x16x32_bf16 v[192:195], a[204:207], a[72:75], v[192:195]\n" \
"v_mfma_f32_16x16x32_bf16 v[192:195], a[220:223], a[76:79], v[192:195]\n" \
"v_mfma_f32_16x16x32_bf16 v[196:199], a[196:199], a[80:83], v[132:135]\n" \
"v_mfma_f32_16x16x32_bf16 v[196:199], a[200:203], a[84:87], v[196:199]\n" \
"v_mfma_f32_16x16x32_bf16 v[196:199], a[204:207], a[88:91], v[196:199]\n" \
"v_mfma_f32_16x16x32_bf16 v[196:199], a[220:223], a[92:95], v[196:199]\n" \
"v_mfma_f32_16x16x32_bf16 v[200:203], a[196:199], a[96:99], v[136:139]\n" \
"v_mfma_f32_16x16x32_bf16 v[200:203], a[200:203], a[100:103], v[200:203]\n" \
"v_mfma_f32_16x16x32_bf16 v[200:203], a[204:207], a[104:107], v[200:203]\n" \
"v_mfma_f32_16x16x32_bf16 v[200:203], a[220:223], a[108:111], v[200:203]\n" \
"v_mfma_f32_16x16x32_bf16 v[204:207], a[196:199], a[112:115], v[140:143]\n" \
"v_mfma_f32_16x16x32_bf16 v[204:207], a[200:203], a[116:119], v[204:207]\n" \
"v_mfma_f32_16x16x32_bf16 v[204:207], a[204:207], a[120:123], v[204:207]\n" \
"v_mfma_f32_16x16x32_bf16 v[204:207], a[220:223], a[124:127], v[204:207]\n" \
"s_nop 7\n" \
"s_nop 0\n" \
"v_cmp_u_f32_e64 s[44:45], v192, v196\n" \
"v_cmp_u_f32_e64 s[56:57], v200, v204\n" \
"s_or_b64 s[56:57], s[56:57], s[44:45]\n" \
"s_cbranch_scc0 Lbk4_%=\n" \
"s_sub_u32 s51, s51, 1\n" \
"s_cbranch_scc0 Lsq4_%=\n" \
"s_branch Lbk4_%=\n" \
"Lsp5_%=:\n" \
"s_movk_i32 s51, 0x1000\n" \
"s_cmp_gt_u32 s48, 4\n" \
"s_cselect_b32 s61, 4, 5\n" \
"s_mul_i32 s61, s61, 262144\n" \
"Lsq5_%=:\n" \
"buffer_load_dwordx4 v[0:3], %[oq0], %[srdz], s61 offen offset:0 sc1\n" \
"buffer_load_dwordx4 v[8:11], %[oq0], %[srdz], s61 offen offset:1024 sc1\n" \
"buffer_load_dwordx4 v[16:19], %[oq0], %[srdz], s61 offen offset:2048 sc1\n" \
"buffer_load_dwordx4 v[24:27], %[oq0], %[srdz], s61 offen offset:3072 sc1\n" \
"buffer_load_dwordx4 v[32:35], v208, %[srdz], s61 offen offset:0 sc1\n" \
"buffer_load_dwordx4 v[40:43], v208, %[srdz], s61 offen offset:1024 sc1\n" \
"buffer_load_dwordx4 v[48:51], v208, %[srdz], s61 offen offset:2048 sc1\n" \
"buffer_load_dwordx4 v[56:59], v208, %[srdz], s61 offen offset:3072 sc1\n" \
"buffer_load_dwordx4 v[64:67], v209, %[srdz], s61 offen offset:0 sc1\n" \
"buffer_load_dwordx4 v[72:75], v209, %[srdz], s61 offen offset:1024 sc1\n" \
"buffer_load_dwordx4 v[80:83], v209, %[srdz], s61 offen offset:2048 sc1\n" \
"buffer_load_dwordx4 v[88:91], v209, %[srdz], s61 offen offset:3072 sc1\n" \
"buffer_load_dwordx4 v[96:99], v210, %[srdz], s61 offen offset:0 sc1\n" \
"buffer_load_dwordx4 v[104:107], v210, %[srdz], s61 offen offset:1024 sc1\n" \
"buffer_load_dwordx4 v[112:115], v210, %[srdz], s61 offen offset:2048 sc1\n" \
"buffer_load_dwordx4 v[120:123], v210, %[srdz], s61 offen offset:3072 sc1\n" \
"s_waitcnt vmcnt(0)\n" \
"s_nop 0\n" \
"v_mfma_f32_16x16x32_bf16 v[128:131], a[232:235], v[0:3], v[192:195]\n" \
"v_mfma_f32_16x16x32_bf16 v[128:131], a[236:239], v[8:11], v[128:131]\n" \
"v_mfma_f32_16x16x32_bf16 v[128:131], a[208:211], v[16:19], v[128:131]\n" \
"v_mfma_f32_16x16x32_bf16 v[128:131], a[228:231], v[24:27], v[128:131]\n" \
"v_mfma_f32_16x16x32_bf16 v[132:135], a[232:235], v[32:35], v[196:199]\n" \
"v_mfma_f32_16x16x32_bf16 v[132:135], a[236:239], v[40:43], v[132:135]\n" \
"v_mfma_f32_16x16x32_bf16 v[132:135], a[208:211], v[48:51], v[132:135]\n" \
"v_mfma_f32_16x16x32_bf16 v[132:135], a[228:231], v[56:59], v[132:135]\n" \
"v_mfma_f32_16x16x32_bf16 v[136:139], a[232:235], v[64:67], v[200:203]\n" \
"v_mfma_f32_16x16x32_bf16 v[136:139], a[236:239], v[72:75], v[136:139]\n" \
"v_mfma_f32_16x16x32_bf16 v[136:139], a[208:211], v[80:83], v[136:139]\n" \
"v_mfma_f32_16x16x32_bf16 v[136:139], a[228:231], v[88:91], v[136:139]\n" \
"v_mfma_f32_16x16x32_bf16 v[140:143], a[232:235], v[96:99], v[204:207]\n" \
"v_mfma_f32_16x16x32_bf16 v[140:143], a[236:239], v[104:107], v[140:143]\n" \
"v_mfma_f32_16x16x32_bf16 v[140:143], a[208:211], v[112:115], v[140:143]\n" \
"v_mfma_f32_16x16x32_bf16 v[140:143], a[228:231], v[120:123], v[140:143]\n" \
"s_nop 7\n" \
"s_nop 0\n" \
"v_cmp_u_f32_e64 s[44:45], v128, v132\n" \
"v_cmp_u_f32_e64 s[56:57], v136, v140\n" \
"s_or_b64 s[56:57], s[56:57], s[44:45]\n" \
"s_cbranch_scc0 Lbk5_%=\n" \
"s_sub_u32 s51, s51, 1\n" \
"s_cbranch_scc0 Lsq5_%=\n" \
"s_branch Lbk5_%=\n" \
"Lsp6_%=:\n" \
"s_movk_i32 s51, 0x1000\n" \
"s_cmp_gt_u32 s48, 5\n" \
"s_cselect_b32 s61, 5, 6\n" \
"s_mul_i32 s61, s61, 262144\n" \
"Lsq6_%=:\n" \
"buffer_load_dwordx4 a[0:3], %[oq0], %[srdz], s61 offen offset:0 sc1\n" \
"buffer_load_dwordx4 a[4:7], %[oq0], %[srdz], s61 offen offset:1024 sc1\n" \
"buffer_load_dwordx4 a[8:11], %[oq0], %[srdz], s61 offen offset:2048 sc1\n" \
"buffer_load_dwordx4 a[12:15], %[oq0], %[srdz], s61 offen offset:3072 sc1\n" \
"buffer_load_dwordx4 a[16:19], v208, %[srdz], s61 offen offset:0 sc1\n" \
"buffer_load_dwordx4 a[20:23], v208, %[srdz], s61 offen offset:1024 sc1\n" \
"buffer_load_dwordx4 a[24:27], v208, %[srdz], s61 offen offset:2048 sc1\n" \
"buffer_load_dwordx4 a[28:31], v208, %[srdz], s61 offen offset:3072 sc1\n" \
"buffer_load_dwordx4 a[32:35], v209, %[srdz], s61 offen offset:0 sc1\n" \
"buffer_load_dwordx4 a[36:39], v209, %[srdz], s61 offen offset:1024 sc1\n" \
"buffer_load_dwordx4 a[40:43], v209, %[srdz], s61 offen offset:2048 sc1\n" \
"buffer_load_dwordx4 a[44:47], v209, %[srdz], s61 offen offset:3072 sc1\n" \
"buffer_load_dwordx4 a[48:51], v210, %[srdz], s61 offen offset:0 sc1\n" \
"buffer_load_dwordx4 a[52:55], v210, %[srdz], s61 offen offset:1024 sc1\n" \
"buffer_load_dwordx4 a[56:59], v210, %[srdz], s61 offen offset:2048 sc1\n" \
"buffer_load_dwordx4 a[60:63], v210, %[srdz], s61 offen offset:3072 sc1\n" \
"s_waitcnt vmcnt(0)\n" \
"s_nop 0\n" \
"v_mfma_f32_16x16x32_bf16 v[192:195], a[196:199], a[0:3], v[128:131]\n" \
"v_mfma_f32_16x16x32_bf16 v[192:195], a[200:203], a[4:7], v[192:195]\n" \
"v_mfma_f32_16x16x32_bf16 v[192:195], a[204:207], a[8:11], v[192:195]\n" \
"v_mfma_f32_16x16x32_bf16 v[192:195], a[220:223], a[12:15], v[192:195]\n" \
"v_mfma_f32_16x16x32_bf16 v[196:199], a[196:199], a[16:19], v[132:135]\n" \
"v_mfma_f32_16x16x32_bf16 v[196:199], a[200:203], a[20:23], v[196:199]\n" \
"v_mfma_f32_16x16x32_bf16 v[196:199], a[204:207], a[24:27], v[196:199]\n" \
"v_mfma_f32_16x16x32_bf16 v[196:199], a[220:223], a[28:31], v[196:199]\n" \
"v_mfma_f32_16x16x32_bf16 v[200:203], a[196:199], a[32:35], v[136:139]\n" \
"v_mfma_f32_16x16x32_bf16 v[200:203], a[200:203], a[36:39], v[200:203]\n" \
"v_mfma_f32_16x16x32_bf16 v[200:203], a[204:207], a[40:43], v[200:203]\n" \
"v_mfma_f32_16x16x32_bf16 v[200:203], a[220:223], a[44:47], v[200:203]\n" \
"v_mfma_f32_16x16x32_bf16 v[204:207], a[196:199], a[48:51], v[140:143]\n" \
"v_mfma_f32_16x16x32_bf16 v[204:207], a[200:203], a[52:55], v[204:207]\n" \
"v_mfma_f32_16x16x32_bf16 v[204:207], a[204:207], a[56:59], v[204:207]\n" \
"v_mfma_f32_16x16x32_bf16 v[204:207], a[220:223], a[60:63], v[204:207]\n" \
"s_nop 7\n" \
"s_nop 0\n" \
"v_cmp_u_f32_e64 s[44:45], v192, v196\n" \
"v_cmp_u_f32_e64 s[56:57], v200, v204\n" \
"s_or_b64 s[56:57], s[56:57], s[44:45]\n" \
"s_cbranch_scc0 Lbk6_%=\n" \
"s_sub_u32 s51, s51, 1\n" \
"s_cbranch_scc0 Lsq6_%=\n" \
"s_branch Lbk6_%=\n" \
"Lsp7_%=:\n" \
"s_movk_i32 s51, 0x1000\n" \
"s_cmp_gt_u32 s48, 6\n" \
"s_cselect_b32 s61, 6, 7\n" \
"s_mul_i32 s61, s61, 262144\n" \
"Lsq7_%=:\n" \
"buffer_load_dwordx4 a[128:131], %[oq0], %[srdz], s61 offen offset:0 sc1\n" \
"buffer_load_dwordx4 a[132:135], %[oq0], %[srdz], s61 offen offset:1024 sc1\n" \
"buffer_load_dwordx4 a[136:139], %[oq0], %[srdz], s61 offen offset:2048 sc1\n" \
"buffer_load_dwordx4 a[140:143], %[oq0], %[srdz], s61 offen offset:3072 sc1\n" \
"buffer_load_dwordx4 a[144:147], v208, %[srdz], s61 offen offset:0 sc1\n" \
"buffer_load_dwordx4 a[148:151], v208, %[srdz], s61 offen offset:1024 sc1\n" \
"buffer_load_dwordx4 a[152:155], v208, %[srdz], s61 offen offset:2048 sc1\n" \
"buffer_load_dwordx4 a[156:159], v208, %[srdz], s61 offen offset:3072 sc1\n" \
"buffer_load_dwordx4 a[160:163], v209, %[srdz], s61 offen offset:0 sc1\n" \
"buffer_load_dwordx4 a[164:167], v209, %[srdz], s61 offen offset:1024 sc1\n" \
"buffer_load_dwordx4 a[168:171], v209, %[srdz], s61 offen offset:2048 sc1\n" \
"buffer_load_dwordx4 a[172:175], v209, %[srdz], s61 offen offset:3072 sc1\n" \
"buffer_load_dwordx4 a[176:179], v210, %[srdz], s61 offen offset:0 sc1\n" \
"buffer_load_dwordx4 a[180:183], v210, %[srdz], s61 offen offset:1024 sc1\n" \
"buffer_load_dwordx4 a[184:187], v210, %[srdz], s61 offen offset:2048 sc1\n" \
"buffer_load_dwordx4 a[188:191], v210, %[srdz], s61 offen offset:3072 sc1\n" \
"s_waitcnt vmcnt(0)\n" \
"s_nop 0\n" \
"v_mfma_f32_16x16x32_bf16 v[128:131], a[208:211], a[128:131], v[192:195]\n" \
"v_mfma_f32_16x16x32_bf16 v[128:131], a[224:227], a[132:135], v[128:131]\n" \
"v_mfma_f32_16x16x32_bf16 v[128:131], a[228:231], a[136:139], v[128:131]\n" \
"v_mfma_f32_16x16x32_bf16 v[128:131], a[236:239], a[140:143], v[128:131]\n" \
"v_mfma_f32_16x16x32_bf16 v[132:135], a[208:211], a[144:147], v[196:199]\n" \
"v_mfma_f32_16x16x32_bf16 v[132:135], a[224:227], a[148:151], v[132:135]\n" \
"v_mfma_f32_16x16x32_bf16 v[132:135], a[228:231], a[152:155], v[132:135]\n" \
"v_mfma_f32_16x16x32_bf16 v[132:135], a[236:239], a[156:159], v[132:135]\n" \
"v_mfma_f32_16x16x32_bf16 v[136:139], a[208:211], a[160:163], v[200:203]\n" \
"v_mfma_f32_16x16x32_bf16 v[136:139], a[224:227], a[164:167], v[136:139]\n" \
"v_mfma_f32_16x16x32_bf16 v[136:139], a[228:231], a[168:171], v[136:139]\n" \
"v_mfma_f32_16x16x32_bf16 v[136:139], a[236:239], a[172:175], v[136:139]\n" \
"v_mfma_f32_16x16x32_bf16 v[140:143], a[208:211], a[176:179], v[204:207]\n" \
"v_mfma_f32_16x16x32_bf16 v[140:143], a[224:227], a[180:183], v[140:143]\n" \
"v_mfma_f32_16x16x32_bf16 v[140:143], a[228:231], a[184:187], v[140:143]\n" \
"v_mfma_f32_16x16x32_bf16 v[140:143], a[236:239], a[188:191], v[140:143]\n" \
"s_nop 7\n" \
"s_nop 0\n" \
"v_cmp_u_f32_e64 s[44:45], v128, v132\n" \
"v_cmp_u_f32_e64 s[56:57], v136, v140\n" \
"s_or_b64 s[56:57], s[56:57], s[44:45]\n" \
"s_cbranch_scc0 Lbk7_%=\n" \
"s_sub_u32 s51, s51, 1\n" \
"s_cbranch_scc0 Lsq7_%=\n" \
"s_branch Lbk7_%=\n" \
"Lspk_%=:\n" \
"s_nop 0\n" \
"s_mov_b32 s51, 0x10000\n" \
"Lsqk_%=:\n" \
"buffer_load_dword v229, v226, %[srdz], s50 offen sc1\n" \
"s_waitcnt vmcnt(0)\n" \
"s_nop 0\n" \
"v_cmp_eq_u32_e64 s[56:57], 0, v229\n" \
"s_cmp_lg_u64 s[56:57], 0\n" \
"s_cbranch_scc0 Lbkk_%=\n" \
"s_sub_u32 s51, s51, 1\n" \
"s_cbranch_scc0 Lsqk_%=\n" \
"s_branch Lbkk_%=\n" \
"Lspp_%=:\n" \
"s_nop 0\n" \
"s_mov_b32 s51, 0x10000\n" \
"Lsqp_%=:\n" \
"buffer_load_dword v232, v226, %[srdz], s54 offen sc1\n" \
"s_waitcnt vmcnt(0)\n" \
"v_cmp_eq_u32_e32 vcc, 0, v232\n" \
"s_cbranch_vccz Lbkp_%=\n" \
"s_sub_u32 s51, s51, 1\n" \
"s_cbranch_scc0 Lsqp_%=\n" \
"s_branch Lbkp_%=\n" \
"Lprobe_%=:\n" \
"s_mov_b32 s62, -1\n" \
"s_mov_b32 s63, -1\n" \
"s_memtime s[56:57]\n" \
"s_waitcnt lgkmcnt(0)\n" \
"s_nop 0\n" \
"s_load_dword s61, %[pcnt], 0x29900\n" \
"s_waitcnt lgkmcnt(0)\n" \
"s_nop 0\n" \
"s_memtime s[58:59]\n" \
"s_waitcnt lgkmcnt(0)\n" \
"s_sub_u32 s58, s58, s56\n" \
"s_min_u32 s62, s62, s58\n" \
"s_nop 0\n" \
"s_memtime s[56:57]\n" \
"s_waitcnt lgkmcnt(0)\n" \
"s_nop 0\n" \
"s_load_dword s61, %[pcnt], 0x2b900\n" \
"s_waitcnt lgkmcnt(0)\n" \
"s_nop 0\n" \
"s_memtime s[58:59]\n" \
"s_waitcnt lgkmcnt(0)\n" \
"s_sub_u32 s58, s58, s56\n" \
"s_min_u32 s63, s63, s58\n" \
"s_nop 0\n" \
"s_memtime s[56:57]\n" \
"s_waitcnt lgkmcnt(0)\n" \
"s_nop 0\n" \
"s_load_dword s61, %[pcnt], 0x2dd80\n" \
"s_waitcnt lgkmcnt(0)\n" \
"s_nop 0\n" \
"s_memtime s[58:59]\n" \
"s_waitcnt lgkmcnt(0)\n" \
"s_sub_u32 s58, s58, s56\n" \
"s_min_u32 s62, s62, s58\n" \
"s_nop 0\n" \
"s_memtime s[56:57]\n" \
"s_waitcnt lgkmcnt(0)\n" \
"s_nop 0\n" \
"s_load_dword s61, %[pcnt], 0x2fd80\n" \
"s_waitcnt lgkmcnt(0)\n" \
"s_nop 0\n" \
"s_memtime s[58:59]\n" \
"s_waitcnt lgkmcnt(0)\n" \
"s_sub_u32 s58, s58, s56\n" \
"s_min_u32 s63, s63, s58\n" \
"s_nop 0\n" \
"s_memtime s[56:57]\n" \
"s_waitcnt lgkmcnt(0)\n" \
"s_nop 0\n" \
"s_load_dword s61, %[pcnt], 0x2a200\n" \
"s_waitcnt lgkmcnt(0)\n" \
"s_nop 0\n" \
"s_memtime s[58:59]\n" \
"s_waitcnt lgkmcnt(0)\n" \
"s_sub_u32 s58, s58, s56\n" \
"s_min_u32 s62, s62, s58\n" \
"s_nop 0\n" \
"s_memtime s[56:57]\n" \
"s_waitcnt lgkmcnt(0)\n" \
"s_nop 0\n" \
"s_load_dword s61, %[pcnt], 0x2c200\n" \
"s_waitcnt lgkmcnt(0)\n" \
"s_nop 0\n" \
"s_memtime s[58:59]\n" \
"s_waitcnt lgkmcnt(0)\n" \
"s_sub_u32 s58, s58, s56\n" \
"s_min_u32 s63, s63, s58\n" \
"s_cmp_lt_u32 s62, s63\n" \
"s_cselect_b32 s61, 8, 0\n" \
"s_mov_b64 exec, 1\n" \
"buffer_store_dwordx2 v[234:235], off, %[srdz], s61 offset:128 sc1\n" \
"s_mov_b64 exec, -1\n" \
"s_branch Lgo_%=\n" \
"Lnsp_%=:\n" \
"s_mov_b32 s61, 0x10000\n" \
"Lnsq_%=:\n" \
"buffer_load_dwordx4 v[230:233], off, %[srdz], 0 offset:128 sc1\n" \
"s_waitcnt vmcnt(0)\n" \
"v_cmp_eq_u64_e32 vcc, v[230:231], v[234:235]\n" \
"v_cmp_eq_u64_e64 s[56:57], v[232:233], v[234:235]\n" \
"s_nop 3\n" \
"s_or_b64 s[58:59], vcc, s[56:57]\n" \
"s_cbranch_scc1 Lnbk_%=\n" \
"s_sub_u32 s61, s61, 1\n" \
"s_cbranch_scc0 Lnsq_%=\n" \
"s_branch Lnbk_%=\n" \
"Lwalk_%=:\n" \
"s_mov_b64 exec, 0\n" \
"s_cmp_eq_u32 %[wid], 1\n" \
"s_cbranch_scc1 Lk0_%=\n" \
"s_cmp_eq_u32 %[wid], 2\n" \
"s_cbranch_scc1 Lk6_%=\n" \
"s_branch Lk12_%=\n" \
"adaln_kr:\n" \
"s_mov_b64 exec, -1\n" \
"s_branch Lgo_%=\n" \
"Lslow_%=:\n" \
"s_mov_b32 s52, 1\n" \
"s_mov_b32 s48, 0\n" \
"s_mov_b32 s51, 32\n" \
"s_nop 0\n" \
"s_mov_b32 s62, 0x80000000\n" \
"s_mov_b32 s63, 0x80000000\n" \
"s_waitcnt vmcnt(0)\n" \
"s_nop 0\n" \
"v_readlane_b32 s64, %[tlo], 9\n" \
"v_readlane_b32 s65, %[thi], 9\n" \
"s_mov_b32 s66, %[wbytes]\n" \
"v_mov_b32_e32 v229, %[vg0]\n" \
"s_mov_b32 s44, %[m0t]\n" \
"s_mov_b32 s45, 0\n" \
"v_xor_b32_e64 v208, 64, %[kb0]\n" \
"v_xor_b32_e32 v209, 0x80, %[kb0]\n" \
"v_xor_b32_e32 v210, 0xc0, %[kb0]\n" \
"v_xor_b32_e64 v212, 64, %[kd0]\n" \
"v_xor_b32_e32 v213, 0x80, %[kd0]\n" \
"v_xor_b32_e32 v214, 0xc0, %[kd0]\n" \
"v_xor_b32_e64 v211, 32, %[vvb]\n" \
"v_add_u32_e32 v215, 0xfffffc80, %[vd0]\n" \
"s_mov_b32 s66, 0\n" \
"s_add_u32 s40, %[m0b], 0\n" \
"s_add_u32 s41, %[m0b], 16384\n" \
"s_add_u32 s42, %[m0b], 32768\n" \
"s_add_u32 s43, %[m0b], 49152\n" \
"v_mov_b32_e32 v225, 0xff800000\n" \
"v_mov_b32_e32 v226, 0xff800000\n" \
"v_mov_b32_e32 v227, 0xff800000\n" \
"v_mov_b32_e32 v228, 0xff800000\n" \
"s_waitcnt vmcnt(0) lgkmcnt(0)\n" \
"s_barrier\n" \
"Lsl_%=:\n" \
"s_mov_b32 m0, s40\n" \
"s_nop 0\n" \
"buffer_load_dwordx4 %[kd0], %[srdk], s48 offen offset:0 lds\n" \
"buffer_load_dwordx4 v212, %[srdk], s48 offen offset:1024 lds\n" \
"buffer_load_dwordx4 v213, %[srdk], s48 offen offset:2048 lds\n" \
"buffer_load_dwordx4 v214, %[srdk], s48 offen offset:3072 lds\n" \
"s_waitcnt vmcnt(0)\n" \
"s_barrier\n" \
"ds_read_b128 a[64:67], %[kb0] offset:0\n" \
"ds_read_b128 a[68:71], v208 offset:0\n" \
"ds_read_b128 a[72:75], v209 offset:0\n" \
"ds_read_b128 a[76:79], v210 offset:0\n" \
"ds_read_b128 a[92:95], v210 offset:4096\n" \
"ds_read_b128 a[88:91], v209 offset:4096\n" \
"ds_read_b128 a[84:87], v208 offset:4096\n" \
"ds_read_b128 a[80:83], %[kb0] offset:4096\n" \
"ds_read_b128 a[96:99], %[kb0] offset:8192\n" \
"ds_read_b128 a[100:103], v208 offset:8192\n" \
"ds_read_b128 a[104:107], v209 offset:8192\n" \
"ds_read_b128 a[108:111], v210 offset:8192\n" \
"ds_read_b128 a[124:127], v210 offset:12288\n" \
"ds_read_b128 a[120:123], v209 offset:12288\n" \
"ds_read_b128 a[116:119], v208 offset:12288\n" \
"ds_read_b128 a[112:115], %[kb0] offset:12288\n" \
"s_waitcnt lgkmcnt(0)\n" \
"s_barrier\n" \
"v_mfma_f32_16x16x32_bf16 v[128:131], a[64:67], a[0:3], 0\n" \
"v_mfma_f32_16x16x32_bf16 v[128:131], a[68:71], a[4:7], v[128:131]\n" \
"v_mfma_f32_16x16x32_bf16 v[128:131], a[72:75], a[8:11], v[128:131]\n" \
"v_mfma_f32_16x16x32_bf16 v[128:131], a[76:79], a[12:15], v[128:131]\n" \
"v_mfma_f32_16x16x32_bf16 v[132:135], a[92:95], a[12:15], 0\n" \
"v_mfma_f32_16x16x32_bf16 v[132:135], a[88:91], a[8:11], v[132:135]\n" \
"v_mfma_f32_16x16x32_bf16 v[132:135], a[84:87], a[4:7], v[132:135]\n" \
"v_mfma_f32_16x16x32_bf16 v[132:135], a[80:83], a[0:3], v[132:135]\n" \
"v_mfma_f32_16x16x32_bf16 v[136:139], a[64:67], a[16:19], 0\n" \
"v_mfma_f32_16x16x32_bf16 v[136:139], a[68:71], a[20:23], v[136:139]\n" \
"v_mfma_f32_16x16x32_bf16 v[136:139], a[72:75], a[24:27], v[136:139]\n" \
"v_mfma_f32_16x16x32_bf16 v[136:139], a[76:79], a[28:31], v[136:139]\n" \
"v_mfma_f32_16x16x32_bf16 v[140:143], a[92:95], a[28:31], 0\n" \
"v_mfma_f32_16x16x32_bf16 v[140:143], a[88:91], a[24:27], v[140:143]\n" \
"v_mfma_f32_16x16x32_bf16 v[140:143], a[84:87], a[20:23], v[140:143]\n" \
"v_mfma_f32_16x16x32_bf16 v[140:143], a[80:83], a[16:19], v[140:143]\n" \
"v_mfma_f32_16x16x32_bf16 v[144:147], a[64:67], a[32:35], 0\n" \
"v_mfma_f32_16x16x32_bf16 v[144:147], a[68:71], a[36:39], v[144:147]\n" \
"v_mfma_f32_16x16x32_bf16 v[144:147], a[72:75], a[40:43], v[144:147]\n" \
"v_mfma_f32_16x16x32_bf16 v[144:147], a[76:79], a[44:47], v[144:147]\n" \
"v_mfma_f32_16x16x32_bf16 v[148:151], a[92:95], a[44:47], 0\n" \
"v_mfma_f32_16x16x32_bf16 v[148:151], a[88:91], a[40:43], v[148:151]\n" \
"v_mfma_f32_16x16x32_bf16 v[148:151], a[84:87], a[36:39], v[148:151]\n" \
"v_mfma_f32_16x16x32_bf16 v[148:151], a[80:83], a[32:35], v[148:151]\n" \
"v_mfma_f32_16x16x32_bf16 v[152:155], a[64:67], a[48:51], 0\n" \
"v_mfma_f32_16x16x32_bf16 v[152:155], a[68:71], a[52:55], v[152:155]\n" \
"v_mfma_f32_16x16x32_bf16 v[152:155], a[72:75], a[56:59], v[152:155]\n" \
"v_mfma_f32_16x16x32_bf16 v[152:155], a[76:79], a[60:63], v[152:155]\n" \
"v_mfma_f32_16x16x32_bf16 v[156:159], a[92:95], a[60:63], 0\n" \
"v_mfma_f32_16x16x32_bf16 v[156:159], a[88:91], a[56:59], v[156:159]\n" \
"v_mfma_f32_16x16x32_bf16 v[156:159], a[84:87], a[52:55], v[156:159]\n" \
"v_mfma_f32_16x16x32_bf16 v[156:159], a[80:83], a[48:51], v[156:159]\n" \
"s_nop 7\n" \
"s_nop 0\n" \
"v_max3_f32 v230, v128, v129, v130\n" \
"v_max3_f32 v231, v131, v132, v133\n" \
"v_max3_f32 v230, v230, v134, v135\n" \
"v_max3_f32 v225, v230, v231, v225\n" \
"v_max3_f32 v230, v136, v137, v138\n" \
"v_max3_f32 v231, v139, v140, v141\n" \
"v_max3_f32 v230, v230, v142, v143\n" \
"v_max3_f32 v226, v230, v231, v226\n" \
"v_max3_f32 v230, v144, v145, v146\n" \
"v_max3_f32 v231, v147, v148, v149\n" \
"v_max3_f32 v230, v230, v150, v151\n" \
"v_max3_f32 v227, v230, v231, v227\n" \
"v_max3_f32 v230, v152, v153, v154\n" \
"v_max3_f32 v231, v155, v156, v157\n" \
"v_max3_f32 v230, v230, v158, v159\n" \
"v_max3_f32 v228, v230, v231, v228\n" \
"s_nop 3\n" \
"s_nop 0\n" \
"v_mfma_f32_16x16x32_bf16 v[128:131], a[96:99], a[0:3], 0\n" \
"v_mfma_f32_16x16x32_bf16 v[128:131], a[100:103], a[4:7], v[128:131]\n" \
"v_mfma_f32_16x16x32_bf16 v[128:131], a[104:107], a[8:11], v[128:131]\n" \
"v_mfma_f32_16x16x32_bf16 v[128:131], a[108:111], a[12:15], v[128:131]\n" \
"v_mfma_f32_16x16x32_bf16 v[132:135], a[124:127], a[12:15], 0\n" \
"v_mfma_f32_16x16x32_bf16 v[132:135], a[120:123], a[8:11], v[132:135]\n" \
"v_mfma_f32_16x16x32_bf16 v[132:135], a[116:119], a[4:7], v[132:135]\n" \
"v_mfma_f32_16x16x32_bf16 v[132:135], a[112:115], a[0:3], v[132:135]\n" \
"v_mfma_f32_16x16x32_bf16 v[136:139], a[96:99], a[16:19], 0\n" \
"v_mfma_f32_16x16x32_bf16 v[136:139], a[100:103], a[20:23], v[136:139]\n" \
"v_mfma_f32_16x16x32_bf16 v[136:139], a[104:107], a[24:27], v[136:139]\n" \
"v_mfma_f32_16x16x32_bf16 v[136:139], a[108:111], a[28:31], v[136:139]\n" \
"v_mfma_f32_16x16x32_bf16 v[140:143], a[124:127], a[28:31], 0\n" \
"v_mfma_f32_16x16x32_bf16 v[140:143], a[120:123], a[24:27], v[140:143]\n" \
"v_mfma_f32_16x16x32_bf16 v[140:143], a[116:119], a[20:23], v[140:143]\n" \
"v_mfma_f32_16x16x32_bf16 v[140:143], a[112:115], a[16:19], v[140:143]\n" \
"v_mfma_f32_16x16x32_bf16 v[144:147], a[96:99], a[32:35], 0\n" \
"v_mfma_f32_16x16x32_bf16 v[144:147], a[100:103], a[36:39], v[144:147]\n" \
"v_mfma_f32_16x16x32_bf16 v[144:147], a[104:107], a[40:43], v[144:147]\n" \
"v_mfma_f32_16x16x32_bf16 v[144:147], a[108:111], a[44:47], v[144:147]\n" \
"v_mfma_f32_16x16x32_bf16 v[148:151], a[124:127], a[44:47], 0\n" \
"v_mfma_f32_16x16x32_bf16 v[148:151], a[120:123], a[40:43], v[148:151]\n" \
"v_mfma_f32_16x16x32_bf16 v[148:151], a[116:119], a[36:39], v[148:151]\n" \
"v_mfma_f32_16x16x32_bf16 v[148:151], a[112:115], a[32:35], v[148:151]\n" \
"v_mfma_f32_16x16x32_bf16 v[152:155], a[96:99], a[48:51], 0\n" \
"v_mfma_f32_16x16x32_bf16 v[152:155], a[100:103], a[52:55], v[152:155]\n" \
"v_mfma_f32_16x16x32_bf16 v[152:155], a[104:107], a[56:59], v[152:155]\n" \
"v_mfma_f32_16x16x32_bf16 v[152:155], a[108:111], a[60:63], v[152:155]\n" \
"v_mfma_f32_16x16x32_bf16 v[156:159], a[124:127], a[60:63], 0\n" \
"v_mfma_f32_16x16x32_bf16 v[156:159], a[120:123], a[56:59], v[156:159]\n" \
"v_mfma_f32_16x16x32_bf16 v[156:159], a[116:119], a[52:55], v[156:159]\n" \
"v_mfma_f32_16x16x32_bf16 v[156:159], a[112:115], a[48:51], v[156:159]\n" \
"s_nop 7\n" \
"s_nop 0\n" \
"v_max3_f32 v230, v128, v129, v130\n" \
"v_max3_f32 v231, v131, v132, v133\n" \
"v_max3_f32 v230, v230, v134, v135\n" \
"v_max3_f32 v225, v230, v231, v225\n" \
"v_max3_f32 v230, v136, v137, v138\n" \
"v_max3_f32 v231, v139, v140, v141\n" \
"v_max3_f32 v230, v230, v142, v143\n" \
"v_max3_f32 v226, v230, v231, v226\n" \
"v_max3_f32 v230, v144, v145, v146\n" \
"v_max3_f32 v231, v147, v148, v149\n" \
"v_max3_f32 v230, v230, v150, v151\n" \
"v_max3_f32 v227, v230, v231, v227\n" \
"v_max3_f32 v230, v152, v153, v154\n" \
"v_max3_f32 v231, v155, v156, v157\n" \
"v_max3_f32 v230, v230, v158, v159\n" \
"v_max3_f32 v228, v230, v231, v228\n" \
"s_nop 3\n" \
"s_add_u32 s48, s48, s54\n" \
"s_sub_u32 s51, s51, 1\n" \
"s_cmp_lg_u32 s51, 0\n" \
"s_cbranch_scc1 Lsl_%=\n" \
"s_nop 0\n" \
"v_mov_b32_e32 v232, v225\n" \
"v_mov_b32_e32 v233, v226\n" \
"v_mov_b32_e32 v234, v227\n" \
"v_mov_b32_e32 v235, v228\n" \
"s_nop 0\n" \
"v_permlane16_swap_b32_e32 v225, v232\n" \
"v_permlane16_swap_b32_e32 v226, v233\n" \
"v_permlane16_swap_b32_e32 v227, v234\n" \
"v_permlane16_swap_b32_e32 v228, v235\n" \
"v_max_f32_e32 v225, v225, v232\n" \
"v_max_f32_e32 v226, v226, v233\n" \
"v_max_f32_e32 v227, v227, v234\n" \
"v_max_f32_e32 v228, v228, v235\n" \
"v_mov_b32_e32 v232, v225\n" \
"v_mov_b32_e32 v233, v226\n" \
"v_mov_b32_e32 v234, v227\n" \
"v_mov_b32_e32 v235, v228\n" \
"s_nop 0\n" \
"v_permlane32_swap_b32_e32 v225, v232\n" \
"v_permlane32_swap_b32_e32 v226, v233\n" \
"v_permlane32_swap_b32_e32 v227, v234\n" \
"v_permlane32_swap_b32_e32 v228, v235\n" \
"v_max_f32_e32 v225, v225, v232\n" \
"v_max_f32_e32 v226, v226, v233\n" \
"v_max_f32_e32 v227, v227, v234\n" \
"v_max_f32_e32 v228, v228, v235\n" \
"s_cmp_lg_u32 s69, 0\n" \
"s_cbranch_scc1 Lnewm_%=\n" \
"v_sub_f32_e32 v225, 0, v160\n" \
"v_sub_f32_e32 v226, 0, v164\n" \
"v_sub_f32_e64 v227, 0, v168\n" \
"v_sub_f32_e32 v228, 0, v172\n" \
"Lnewm_%=:\n" \
"s_mov_b32 s48, 0\n" \
"s_mov_b32 m0, s40\n" \
"s_nop 0\n" \
"buffer_load_dwordx4 %[kd0], %[srdk], s48 offen offset:0 lds\n" \
"buffer_load_dwordx4 v212, %[srdk], s48 offen offset:1024 lds\n" \
"buffer_load_dwordx4 v213, %[srdk], s48 offen offset:2048 lds\n" \
"buffer_load_dwordx4 v214, %[srdk], s48 offen offset:3072 lds\n" \
"s_mov_b32 s48, 16384\n" \
"s_mov_b32 m0, s41\n" \
"s_nop 0\n" \
"buffer_load_dwordx4 %[kd0], %[srdk], s48 offen offset:0 lds\n" \
"buffer_load_dwordx4 v212, %[srdk], s48 offen offset:1024 lds\n" \
"buffer_load_dwordx4 v213, %[srdk], s48 offen offset:2048 lds\n" \
"buffer_load_dwordx4 v214, %[srdk], s48 offen offset:3072 lds\n" \
"s_add_u32 m0, %[m0b], 65536\n" \
"s_nop 0\n" \
"s_nop 0\n" \
"buffer_load_dwordx4 %[vd0], %[srdv], s48 offen offset:0 lds\n" \
"buffer_load_dwordx4 v215, %[srdv], s48 offen offset:1024 lds\n" \
"buffer_load_dwordx4 %[vd0], %[srdv], s48 offen offset:2048 lds\n" \
"buffer_load_dwordx4 v215, %[srdv], s48 offen offset:3072 lds\n" \
"s_mov_b32 s48, 32768\n" \
"s_mov_b32 m0, s42\n" \
"s_nop 0\n" \
"buffer_load_dwordx4 %[kd0], %[srdk], s48 offen offset:0 lds\n" \
"buffer_load_dwordx4 v212, %[srdk], s48 offen offset:1024 lds\n" \
"buffer_load_dwordx4 v213, %[srdk], s48 offen offset:2048 lds\n" \
"buffer_load_dwordx4 v214, %[srdk], s48 offen offset:3072 lds\n" \
"s_add_u32 m0, %[m0b], 81920\n" \
"s_nop 0\n" \
"s_nop 0\n" \
"buffer_load_dwordx4 %[vd0], %[srdv], s48 offen offset:0 lds\n" \
"buffer_load_dwordx4 v215, %[srdv], s48 offen offset:1024 lds\n" \
"buffer_load_dwordx4 %[vd0], %[srdv], s48 offen offset:2048 lds\n" \
"buffer_load_dwordx4 v215, %[srdv], s48 offen offset:3072 lds\n" \
"s_mov_b32 s48, 49152\n" \
"s_mov_b32 m0, s43\n" \
"s_nop 0\n" \
"buffer_load_dwordx4 %[kd0], %[srdk], s48 offen offset:0 lds\n" \
"buffer_load_dwordx4 v212, %[srdk], s48 offen offset:1024 lds\n" \
"buffer_load_dwordx4 v213, %[srdk], s48 offen offset:2048 lds\n" \
"buffer_load_dwordx4 v214, %[srdk], s48 offen offset:3072 lds\n" \
"s_add_u32 m0, %[m0b], 98304\n" \
"s_nop 0\n" \
"s_nop 0\n" \
"buffer_load_dwordx4 %[vd0], %[srdv], s48 offen offset:0 lds\n" \
"buffer_load_dwordx4 v215, %[srdv], s48 offen offset:1024 lds\n" \
"buffer_load_dwordx4 %[vd0], %[srdv], s48 offen offset:2048 lds\n" \
"buffer_load_dwordx4 v215, %[srdv], s48 offen offset:3072 lds\n" \
"s_waitcnt vmcnt(0)\n" \
"s_barrier\n" \
"ds_read_b128 a[64:67], %[kb0] offset:0\n" \
"ds_read_b128 a[68:71], v208 offset:0\n" \
"ds_read_b128 a[72:75], v209 offset:0\n" \
"ds_read_b128 a[76:79], v210 offset:0\n" \
"ds_read_b128 a[92:95], v210 offset:4096\n" \
"ds_read_b128 a[88:91], v209 offset:4096\n" \
"ds_read_b128 a[84:87], v208 offset:4096\n" \
"ds_read_b128 a[80:83], %[kb0] offset:4096\n" \
"s_mov_b32 s48, 65536\n" \
"s_mov_b32 s60, 0\n" \
"s_waitcnt lgkmcnt(0)\n" \
"v_mfma_f32_16x16x32_bf16 v[128:131], a[64:67], a[0:3], 0\n" \
"ds_read_b128 a[96:99], %[kb0] offset:8192\n" \
"v_mfma_f32_16x16x32_bf16 v[128:131], a[68:71], a[4:7], v[128:131]\n" \
"ds_read_b128 a[100:103], v208 offset:8192\n" \
"v_mfma_f32_16x16x32_bf16 v[128:131], a[72:75], a[8:11], v[128:131]\n" \
"ds_read_b128 a[104:107], v209 offset:8192\n" \
"v_mfma_f32_16x16x32_bf16 v[128:131], a[76:79], a[12:15], v[128:131]\n" \
"ds_read_b128 a[108:111], v210 offset:8192\n" \
"v_mfma_f32_16x16x32_bf16 v[132:135], a[92:95], a[12:15], 0\n" \
"ds_read_b128 a[124:127], v210 offset:12288\n" \
"v_mfma_f32_16x16x32_bf16 v[132:135], a[88:91], a[8:11], v[132:135]\n" \
"ds_read_b128 a[120:123], v209 offset:12288\n" \
"v_mfma_f32_16x16x32_bf16 v[132:135], a[84:87], a[4:7], v[132:135]\n" \
"ds_read_b128 a[116:119], v208 offset:12288\n" \
"v_mfma_f32_16x16x32_bf16 v[132:135], a[80:83], a[0:3], v[132:135]\n" \
"ds_read_b128 a[112:115], %[kb0] offset:12288\n" \
"v_mfma_f32_16x16x32_bf16 v[136:139], a[64:67], a[16:19], 0\n" \
"ds_read_b64_tr_b16 a[128:129], %[vvb] offset:0\n" \
"v_mfma_f32_16x16x32_bf16 v[136:139], a[68:71], a[20:23], v[136:139]\n" \
"ds_read_b64_tr_b16 a[130:131], %[vvb] offset:4096\n" \
"v_mfma_f32_16x16x32_bf16 v[136:139], a[72:75], a[24:27], v[136:139]\n" \
"ds_read_b64_tr_b16 a[132:133], v211 offset:0\n" \
"v_mfma_f32_16x16x32_bf16 v[136:139], a[76:79], a[28:31], v[136:139]\n" \
"ds_read_b64_tr_b16 a[134:135], v211 offset:4096\n" \
"v_mfma_f32_16x16x32_bf16 v[140:143], a[92:95], a[28:31], 0\n" \
"ds_read_b64_tr_b16 a[136:137], %[vvb] offset:512\n" \
"v_mfma_f32_16x16x32_bf16 v[140:143], a[88:91], a[24:27], v[140:143]\n" \
"ds_read_b64_tr_b16 a[138:139], %[vvb] offset:4608\n" \
"v_mfma_f32_16x16x32_bf16 v[140:143], a[84:87], a[20:23], v[140:143]\n" \
"ds_read_b64_tr_b16 a[140:141], v211 offset:512\n" \
"v_mfma_f32_16x16x32_bf16 v[140:143], a[80:83], a[16:19], v[140:143]\n" \
"ds_read_b64_tr_b16 a[142:143], v211 offset:4608\n" \
"v_mfma_f32_16x16x32_bf16 v[144:147], a[64:67], a[32:35], 0\n" \
"ds_read_b64_tr_b16 a[144:145], %[vvb] offset:1024\n" \
"v_mfma_f32_16x16x32_bf16 v[144:147], a[68:71], a[36:39], v[144:147]\n" \
"ds_read_b64_tr_b16 a[146:147], %[vvb] offset:5120\n" \
"v_mfma_f32_16x16x32_bf16 v[144:147], a[72:75], a[40:43], v[144:147]\n" \
"ds_read_b64_tr_b16 a[148:149], v211 offset:1024\n" \
"v_mfma_f32_16x16x32_bf16 v[144:147], a[76:79], a[44:47], v[144:147]\n" \
"ds_read_b64_tr_b16 a[150:151], v211 offset:5120\n" \
"v_mfma_f32_16x16x32_bf16 v[148:151], a[92:95], a[44:47], 0\n" \
"ds_read_b64_tr_b16 a[152:153], %[vvb] offset:1536\n" \
"v_mfma_f32_16x16x32_bf16 v[148:151], a[88:91], a[40:43], v[148:151]\n" \
"ds_read_b64_tr_b16 a[154:155], %[vvb] offset:5632\n" \
"v_mfma_f32_16x16x32_bf16 v[148:151], a[84:87], a[36:39], v[148:151]\n" \
"ds_read_b64_tr_b16 a[156:157], v211 offset:1536\n" \
"v_mfma_f32_16x16x32_bf16 v[148:151], a[80:83], a[32:35], v[148:151]\n" \
"ds_read_b64_tr_b16 a[158:159], v211 offset:5632\n" \
"v_mfma_f32_16x16x32_bf16 v[152:155], a[64:67], a[48:51], 0\n" \
"v_mfma_f32_16x16x32_bf16 v[152:155], a[68:71], a[52:55], v[152:155]\n" \
"v_mfma_f32_16x16x32_bf16 v[152:155], a[72:75], a[56:59], v[152:155]\n" \
"v_mfma_f32_16x16x32_bf16 v[152:155], a[76:79], a[60:63], v[152:155]\n" \
"v_mfma_f32_16x16x32_bf16 v[156:159], a[92:95], a[60:63], 0\n" \
"v_mfma_f32_16x16x32_bf16 v[156:159], a[88:91], a[56:59], v[156:159]\n" \
"v_mfma_f32_16x16x32_bf16 v[156:159], a[84:87], a[52:55], v[156:159]\n" \
"v_mfma_f32_16x16x32_bf16 v[156:159], a[80:83], a[48:51], v[156:159]\n" \
"s_branch Lhavem_%=\n" \
"s_nop 0\n" \
".p2align 12\n" \
"adaln_pg0:\n" \
"s_cbranch_execz adaln_pg1\n" \
"Lend_%=:\n" \
""

#define SRD_WORD3 0x00020000
DI u32x4 ld16_sc1(const void* p) { u32x4 v; asm volatile("global_load_dwordx4 %0, %1, off sc1" : "=v"(v) : "v"(p) : "memory"); return v; }
DI unsigned ord_key(float r, unsigned row) {
  const unsigned b = __float_as_uint(r), o = (b & 0x80000000u) ? ~b : (b | 0x80000000u);
  return max(o & 0xfffff800u, 0x800u) | (2047u - row);
}
DI void top2_merge(unsigned& a1, unsigned& a2, unsigned b1, unsigned b2) {
  const unsigned n1 = max(a1, b1), n2 = max(min(a1, b1), max(a2, b2)); a1 = n1; a2 = n2;
}
template <int CTRL> DI void top2_dpp(unsigned& k1, unsigned& k2) {
  const unsigned p1 = __builtin_amdgcn_update_dpp(0u, k1, CTRL, 0xf, 0xf, true), p2 = __builtin_amdgcn_update_dpp(0u, k2, CTRL, 0xf, 0xf, true); top2_merge(k1, k2, p1, p2);
}
DI float row_sum(float v) {
  v = dpp_add<DPP_XOR1>(v); v = dpp_add<DPP_XOR2>(v); v = dpp_add<DPP_ROR8>(v);
  v = swap16_add(v, v); return swap32_add(v, v);
}
DI unsigned h2_pair(float x0, float x1, float m, float rden, unsigned c1w, unsigned sbw) { return ln_pair(x0, x1, m, rden, lo16(c1w), hi16(c1w), lo16(sbw), hi16(sbw)); }
#define PAGE(n, nx) asm volatile("s_branch adaln_pe" #n "\n.p2align 12\nadaln_pg" #n ":\n\ts_cbranch_execz " nx "\nadaln_pe" #n ":")
#define STR_(x) #x
#define STR(x) STR_(x)
constexpr int MOE_WGS = DFF / 16;
constexpr unsigned RETRY_CAP = 4096;
#define ASLEEP 2
#define KLOOK 6
#define YSPLIT 8
#define FSLEEP 14
#define KSLEEP 3
#define YEARLY 8
#define FREST 0
#define KL0 1u
#define KLS 2u
#define KST 2u
#define KCHK 8
#define KPAUSE 2
#define KEY_SEL b

#define WBYTES (moe ? DM * DFF * 2 : 0)
namespace fa {
constexpr unsigned LDS_BYTES = 163840;
constexpr float QSCALE = 0.12751743082f;
struct Args { const u16 *x, *wmod, *wq, *kc, *vc, *wo, *wg, *wu, *wd, *c, *wr; char* ws; u16* out;
};
__global__ __launch_bounds__(256, 1) void fattn_k(const Args p) {
  extern __shared__ __attribute__((aligned(16))) char lds[];
  const unsigned wid = __builtin_amdgcn_readfirstlane(threadIdx.x >> 6);
  const unsigned b = blockIdx.x, idx = b >> 3, kg = b & 3u, m = idx & 7u, hd = 8u * kg + m, y = (b & 4u) + (idx >> 3);
  const unsigned ldsb = (unsigned)(size_t)lds;
  const unsigned m0b = ldsb + 4096u * wid, m0t = ldsb + 131072u + 8192u * wid;
  const __amdgpu_buffer_rsrc_t srdz = __builtin_amdgcn_make_buffer_rsrc((void*)p.ws, (short)0, (int)WS_END, SRD_WORD3);
  const bool moe = b < MOE_WGS;
  const unsigned sl = 4u * ((b & 7u) * 7u + (idx >> 2)) + (idx & 3u), yp2 = (y & 1u) * 2u;
  unsigned hx = (((y >> 2) ^ POL) & 1u) * 8192u;
  unsigned cx = 0, sin = (unsigned)WS_XIN + (((y & 3u) * 32u + hd) * 4u + wid) * BOX + (hx ^ ((~kg & 1u) * 8192u));
  unsigned sfl = (unsigned)WS_OFL + (y & 3u) * 32768u + hx + kg * 128u, spart = (unsigned)WS_PART + (b >> 1) * 16384u + ((((wid >> 1) ^ POL ^ 1u) & 1u) * 8192u) + (b & 1u) * 4096u + (wid & 1u) * 2048u;
  u32x2 xq[8]; unsigned fl, sd0, got, got2; unsigned long long tag; u16* pout;
  f32x2 xf[16]; f32x4 gaf[8], aw[8]; u32x4 alo, ahi; unsigned late; float arow;
#define FQ(i)
#define TQ(i)
  {
    const unsigned tid = threadIdx.x, lane = tid & 63u, l16 = lane & 15u, g = lane >> 4, hi = lane >> 5, r8 = (lane >> 2) & 7u;
    unsigned kb0 = ldsb + 256u * l16 + 16u * (g ^ l16);
    unsigned vvb = ldsb + 65536u + 2048u * hi + 288u * (g & 1u) + 64u * ((lane >> 2) & 3u) + 8u * (lane & 3u);
    unsigned vwb = ldsb + 65536u + 2048u * g + 64u * ((lane >> 2) & 3u) + 16u * (lane & 3u);
    unsigned kd0 = 4096u * wid + 256u * g + 16u * (l16 ^ g);
    unsigned vd0 = 4096u * wid + 256u * r8 + 64u * hi + 16u * ((lane & 3u) ^ (2u * (r8 >> 2))) + 1024u;
    unsigned wd0 = 8192u * (16u * wid + r8) + 64u * hi + 16u * (lane & 3u) + 4096u;
    unsigned hq0 = (unsigned)WS_H + (16u * y + 4u * wid) * 32768u + 16u * lane;
    unsigned oq0 = (unsigned)WS_XIN + (((y & 3u) * 32u + kg * 8u) * 4u + wid) * BOX + (hx ^ ((~kg & 1u) * 8192u)) + 32768u + 16u * lane;
    unsigned flag = ldsb + 49152u + 4u * wid, wbytes = moe ? DM * DFF * 2 : 0;
    const __amdgpu_buffer_rsrc_t srdw = __builtin_amdgcn_make_buffer_rsrc((void*)((const char*)p.wq + 256u * hd - 4096), (short)0, (int)(DM * DQ * 2 - 256u * hd + 4096u), SRD_WORD3);
    __amdgpu_buffer_rsrc_t srdk = __builtin_amdgcn_make_buffer_rsrc((void*)((const char*)p.kc + (size_t)hd * (SEQ * 256)), (short)0, SEQ * 256, SRD_WORD3);
    __amdgpu_buffer_rsrc_t srdv = __builtin_amdgcn_make_buffer_rsrc((void*)((const char*)p.vc + (size_t)hd * (SEQ * 256) - 17408), (short)0, SEQ * 256 + 17408, SRD_WORD3);
    unsigned long long pct; unsigned clen;
    asm volatile("s_getpc_b64 %0\n\ts_mov_b32 %1, adaln_text_end@rel32@lo+4" : "=s"(pct), "=s"(clen));
    const unsigned vct = min(((idx * 4u + wid) * 6u + min(lane, 5u)) * 128u, (clen - 4u) & ~3u);
      const unsigned gr = 128u * (b & 7u) + 4u * (tid >> 3), gc = 64u * (b >> 3) + 8u * (tid & 7u);
      u32x4 w[4], xs[4]; u32x2 ep, cv;
#define XV(i) ({ u32x4 v_; asm volatile("v_accvgpr_read_b32 %0, a[%4]\n\tv_accvgpr_read_b32 %1, a[%4 + 1]\n\tv_accvgpr_read_b32 %2, a[%4 + 2]\n\tv_accvgpr_read_b32 %3, a[%4 + 3]" : "=v"(v_[0]), "=v"(v_[1]), "=v"(v_[2]), "=v"(v_[3]) : "n"(200 + 4 * (i))); v_; })
      asm volatile("global_load_dwordx2 %0, %1, %2" : "=v"(cv) : "v"(gr * 2u), "s"(p.c) : "memory");
      #pragma unroll
      for (int j = 0; j < 4; ++j) asm volatile("global_load_dwordx4 %0, %1, %2 nt" : "=v"(w[j]) : "v"(((gr + j) * NMOD + gc) * 2u), "s"(p.wmod) : "memory");
      #pragma unroll
      for (int q = 0; q < 4; ++q) asm volatile("global_load_dwordx4 %0, %1, %2 offset:%3 nt" : "=v"(xs[q]) : "v"((8u * (y * 32u + hd) + 2u * wid) * 2048u + 16u * lane), "s"(p.x), "n"(1024 * q) : "memory");
      asm volatile("global_load_dwordx2 %0, %1, %2 sc1" : "=v"(ep) : "v"(0u), "s"(p.ws + WS_EP) : "memory");
      asm volatile("buffer_store_dwordx4 %0, %1, %2, 0 offen sc1\n\ts_nop 1" :: "v"(u32x4{0u, 0u, 0u, 0u}), "v"(tid < Z_SHARE / 16u ? (unsigned)WS_Z + b * Z_SHARE + tid * 16u : 0x80000000u), "s"(srdz) : "memory");
      FQ(6);
      asm volatile("global_load_dword a255, %0, %1" :: "v"(vct), "s"(pct) : "memory");
      asm volatile("s_mov_b64 exec, 0\n\ts_cmp_eq_u32 %0, 1\n\ts_cbranch_scc1 adaln_pg5\n\ts_cmp_eq_u32 %0, 2\n\ts_cbranch_scc1 adaln_pg6\n\ts_cmp_eq_u32 %0, 3\n\ts_cbranch_scc1 adaln_pg7\nadaln_fr:\n\ts_mov_b64 exec, -1" :: "s"(wid) : "scc");
      FQ(7);
      unsigned tq0 = (idx * 8u + wid * 2u + ((lane >> 3) & 1u)) * 8192u + (b & 4u) * 256u + (lane & 7u) * 128u + 4096u - 256u * m;
      const unsigned rb0 = 16u * y + 4u * (hd >> 3), ch = 16u * (hd & 7u) + (tid >> 4);
      unsigned vrw = ldsb + 135168u + wid * 256u + 32u * (tid & 7u) + 8u * g, vrr = ldsb + 135168u + 4u * lane;
      unsigned vmo = wid ? 0x80000000u : (unsigned)WS_MOD + b * 512u + lane * 8u, vst = (lane & 31u) ? 0x80000000u : (unsigned)WS_RST + (8u * (y * 32u + hd) + 2u * wid + hi) * 16u;
      unsigned vx2 = (rb0 * 16u + l16) * 2048u + ch * 16u;
      pout = p.out;
      asm volatile("s_waitcnt vmcnt(11)" : "+v"(cv), "+v"(tq0), "+v"(vrw), "+v"(vrr), "+v"(vmo), "+v"(vst), "+v"(vx2), "+s"(pout) :: "memory");
      auto silu2 = [](unsigned v) { const f32x2 f = unpk(v); return cvtpk(f[0] * __builtin_amdgcn_rcpf(1.f + __builtin_amdgcn_exp2f(f[0] * -1.44269504089f)), f[1] * __builtin_amdgcn_rcpf(1.f + __builtin_amdgcn_exp2f(f[1] * -1.44269504089f))); };
      auto dot2 = [](unsigned x, unsigned z, float c) { return __builtin_amdgcn_fdot2_f32_bf16(__builtin_bit_cast(bf16x2, x), __builtin_bit_cast(bf16x2, z), c, false); };
      unsigned s01 = silu2(cv[0]), s23 = silu2(cv[1]);
      asm volatile("s_waitcnt vmcnt(7)" : "+v"(s01), "+v"(s23), "+v"(w[0]), "+v"(w[1]), "+v"(w[2]), "+v"(w[3]) :: "memory");
      FQ(4);
      asm volatile("buffer_load_dword a255, %0, %1, 0 offen" :: "v"(tq0), "s"(srdw) : "memory");
      {
        float acc[8];
        #pragma unroll
        for (int e = 0; e < 4; ++e) {
          acc[2 * e]     = dot2(__builtin_amdgcn_perm(w[3][e], w[2][e], 0x05040100u), s23, dot2(__builtin_amdgcn_perm(w[1][e], w[0][e], 0x05040100u), s01, 0.f));
          acc[2 * e + 1] = dot2(__builtin_amdgcn_perm(w[3][e], w[2][e], 0x07060302u), s23, dot2(__builtin_amdgcn_perm(w[1][e], w[0][e], 0x07060302u), s01, 0.f));
        }
        float t[4], u[2];
        #pragma unroll
        for (int e = 0; e < 4; ++e) t[e] = swap32_add(acc[e], acc[e + 4]);
        #pragma unroll
        for (int e = 0; e < 2; ++e) u[e] = dpp_add<DPP_ROR8>(swap16_add(t[e], t[e + 2]));
        asm volatile("ds_write_b64 %5, %6\n\ts_waitcnt vmcnt(3)" : "+v"(xs[0]), "+v"(xs[1]), "+v"(xs[2]), "+v"(xs[3]), "+v"(ep) : "v"(vrw), "v"(f32x2{u[0], u[1]}) : "memory");
      }
      tag = (((unsigned long long)(unsigned)__builtin_amdgcn_readfirstlane(ep[1]) << 32) | (unsigned)__builtin_amdgcn_readfirstlane(ep[0])) + 1ull;
      const unsigned tl = (unsigned)tag & 0xffu, th = (unsigned)(tag >> 8);
      u32x4 sg;
      {
        float t4[4] = {0.f, 0.f, 0.f, 0.f};
        #pragma unroll
        for (int e = 0; e < 16; ++e) { const unsigned xw = xs[e >> 2][e & 3]; t4[2 * (e >> 3)] = dot2(xw, 0x3f803f80u, t4[2 * (e >> 3)]); t4[2 * (e >> 3) + 1] = dot2(xw, xw, t4[2 * (e >> 3) + 1]); }
        float u = swap16_add(swap32_add(t4[0], t4[2]), swap32_add(t4[1], t4[3]));
        u = dpp_add<DPP_XOR1>(u); u = dpp_add<DPP_XOR2>(u); u = dpp_add<DPP_ROR4>(u); u = dpp_add<DPP_ROR8>(u);
        const auto r16 = __builtin_amdgcn_permlane16_swap(__float_as_uint(u), __float_as_uint(u), false, false);
        const float mean = __uint_as_float(r16[0]) * (1.f / DM), m2 = mean * mean;
        float var = fmaf(__uint_as_float(r16[1]), 1.f / DM, -m2);
        if (__builtin_expect(__any(!(m2 <= var)), 0)) {
          const f32x2 mm = {__uint_as_float(__builtin_amdgcn_readlane(__float_as_uint(mean), 0)), __uint_as_float(__builtin_amdgcn_readlane(__float_as_uint(mean), 32))};
          f32x2 sq = {0.f, 0.f};
          #pragma unroll
          for (int e = 0; e < 8; ++e) {
            const f32x2 d0 = f32x2{lo16(xs[e >> 2][e & 3]), lo16(xs[2 + (e >> 2)][e & 3])} - mm, d1 = f32x2{hi16(xs[e >> 2][e & 3]), hi16(xs[2 + (e >> 2)][e & 3])} - mm;
            sq = __builtin_elementwise_fma(d1, d1, __builtin_elementwise_fma(d0, d0, sq));
          }
          const float v0 = wave_sum(sq[0]), v1 = wave_sum(sq[1]);
          var = (hi ? v1 : v0) * (1.f / DM);
        }
        sg = u32x4{cvtpk(0.f, mean) | tl, th, cvtpk(0.f, __builtin_amdgcn_sqrtf(rbh(rbh(var) + 1e-5f))) | tl, th};
      }
      {
        f32x2 ra, rc;
        asm volatile("s_waitcnt vmcnt(2) lgkmcnt(0)\n\ts_barrier\n\tds_read2st64_b32 %0, %2 offset1:1\n\tds_read2st64_b32 %1, %2 offset0:2 offset1:3\n\t"
                     "buffer_store_dwordx4 %3, %4, %5, 0 offen sc1\n\ts_waitcnt lgkmcnt(0)" : "=&v"(ra), "=&v"(rc) : "v"(vrr), "v"(sg), "v"(vst), "s"(srdz) : "memory");
        ra += rc;
        asm volatile("buffer_store_dwordx2 %0, %1, %2, 0 offen sc1" :: "v"(u32x2{((__float_as_uint(ra[0] + ra[1]) + 0x80u) & 0xffffff00u) | tl, th}), "v"(vmo), "s"(srdz) : "memory");
      }
      FQ(5);
      asm volatile("global_load_dwordx2 a[216:217], %0, %1" :: "v"(tid * 8u), "s"(p.c) : "memory");
      #pragma unroll
      for (int i = 0; i < 4; ++i) asm volatile("global_load_dwordx4 a[%2:%2 + 3], %0, %1" :: "v"(vx2 + i * 32768u), "s"(p.x), "n"(200 + 4 * i) : "memory");
      PAGE(5, "adaln_fr");
      unsigned lane2 = lane; asm volatile("" : "+v"(lane2));
#define lane lane2
    unsigned wo0 = (32u * wid + r8) * 2048u + m * 256u + 64u * hi + 16u * ((lane & 3u) ^ (2u * (r8 >> 2)));
    __amdgpu_buffer_rsrc_t srdt = __builtin_amdgcn_make_buffer_rsrc((void*)((const char*)p.wo + (size_t)kg * (DM * DM * 2)), (short)0, DM * DM * 2, SRD_WORD3);
    __amdgpu_buffer_rsrc_t srdp = __builtin_amdgcn_make_buffer_rsrc((void*)((const char*)(y < 4u ? p.kc : p.vc) + (size_t)hd * (SEQ * 256) + (y & 3u) * 4096u), (short)0, (int)(SEQ * 256 - (y & 3u) * 4096u), SRD_WORD3);
    unsigned vg0 = (256u * wid + (lane >> 1)) * (DFF * 2u) + sl * 32u + 16u * (lane & 1u);
    unsigned vdn = (16u * sl + hi) * 2048u + 512u * wid + 16u * (lane & 31u);
    const unsigned mc0 = 2u * DM + 64u * ((idx >> 2) * 8u + (b & 7u)) + 16u * (idx & 3u);
    const unsigned md0 = (256u * wid + (lane >> 1)) * (NMOD * 2u) + mc0 * 2u + 16u * (lane & 1u);
    sd0 = ldsb + 131072u + 8192u + 512u * wid + 16u * g;
    fl = md0;
    unsigned rd0 = ldsb + 131072u + 7168u + 8192u * ((lane >> 2) & 3u) + 256u * wid + 4u * (lane & 3u);
    unsigned ta0 = m0t + 256u * g + 32u * ((lane >> 2) & 3u) + 8u * (lane & 3u);
    const unsigned c8 = (b * 4u + wid) * 8192u, cd = moe ? sl * 32768u + wid * 8192u : 0u, cw = (idx * 4u + wid) * 16384u;
    const unsigned cm = moe ? ((sl & 3u) * 4u + wid) * 64u * (DFF * 2u) + (sl >> 2) * 128u : 0u;
    unsigned vxs = BOX * (l16 >> 1) + 64u * (l16 & 1u) + 16u * g;
    const unsigned sxs = (unsigned)WS_XIN + ((y & 3u) * 128u + wid * 32u) * BOX + (hx ^ ((~wid & 1u) * 8192u)) + (m >> 1) * 1024u + kg * 256u + (m & 1u) * 128u;
    unsigned vx0 = ((256u * y + 8u * hd + 2u * wid + ((lane >> 2) & 1u)) * DM + 256u * hi + 128u * (l16 >> 3) + 16u * (g & 1u) + 4u * (lane & 3u) + 32u * yp2) * 2u;
    unsigned tlo = 0, thi = 0;
#define tab(ln, lo, hi2) asm("v_writelane_b32 %0, %2, " #ln "\n\tv_writelane_b32 %1, %3, " #ln : "+v"(tlo), "+v"(thi) : "s"(lo), "s"(hi2))
#define tabp(ln, q, off) do { const unsigned long long a_ = (size_t)(q) + (off); tab(ln, (unsigned)a_, (unsigned)(a_ >> 32)); } while (0)
#define TORD 0
    tabp(1, p.x, ((y * 32u + hd) * 2u + (wid & 1u)) * 8192u); tabp(2, p.wu, moe ? c8 : 0u); tabp(3, p.wo, c8); tabp(4, p.wd, cd);
    tabp(5, p.wo, kg * 2097152u + cw); tabp(6, p.wo, kg * 2097152u + cw + 8192u); tabp(7, p.wg, cm);
    tabp(17, p.wmod, 0u); tab(18, sin + 32768u, sfl);
    tab(19, (unsigned)WS_OFL + (y & 3u) * 32768u + (hd >> 4) * 16384u + hx + (hd & 15u) * 512u + wid * 128u, 0u);
    tabp(8, p.wg, 0u); tabp(9, p.wu, 0u); tabp(10, p.wd, 0u);
    tab(11, m, m * 4u * BOX); tab(12, (y & 1u) * 128u, 128u - (y & 1u) * 128u);
    tab(13, b, (unsigned)WS_PFL + (y * 32u + hd) * 512u + kg * 128u); tab(14, sxs, 0u);
    tab(15, (unsigned)WS_MODT + (mc0 - 2u * DM + 4u * wid) * 4u, (mc0 >> 10) == 4u ? 0x3f800000u : 0u);
    tab(16, (unsigned)WS_PFL + y * 16384u + (hd * 4u + wid) * 4u, (unsigned)WS_PFL + (y * 32u + hd) * 512u + wid * 128u);
    const char* psig = p.ws + WS_CNT + y * 8192u + hd * 8u;
    const char* pcnt = p.ws + WS_CNT + y * 8192u + hd * 256u;
    tabp(20, p.wr, 0u); tab(21, spart, (unsigned)WS_ROWS + (y * 32u + hd) * 8192u + wid * 1024u);
      asm volatile("" : "+v"(tlo), "+v"(thi), "+v"(wo0), "+v"(vg0), "+v"(vdn), "+v"(ta0), "+v"(vxs), "+v"(vx0), "+v"(rd0), "+v"(fl), "+v"(sd0), "+v"(kb0), "+v"(vvb), "+v"(vwb), "+v"(kd0), "+v"(vd0), "+v"(wd0), "+v"(hq0), "+v"(oq0));
      asm volatile("" : "+s"(srdk), "+s"(srdv), "+s"(srdt), "+s"(srdp), "+s"(psig), "+s"(pcnt), "+s"(flag), "+s"(wbytes));
#undef lane
      u32x2 mp[8]; u32x4 rs;
      unsigned vlm = ((wid >> 1) * 16u + 2u * (hd & 7u) + (wid & 1u)) * 4096u + lane * 8u, vlr = (unsigned)WS_RST + ((rb0 + wid) * 16u + l16) * 16u;
#define LOOK9 "buffer_load_dwordx2 %0, %[vlm], %[srdz], %[som] offen sc1\n\tbuffer_load_dwordx2 %1, %[vlm], %[srdz], %[som] offen offset:512 sc1\n\tbuffer_load_dwordx2 %2, %[vlm], %[srdz], %[som] offen offset:1024 sc1\n\t" \
              "buffer_load_dwordx2 %3, %[vlm], %[srdz], %[som] offen offset:1536 sc1\n\tbuffer_load_dwordx2 %4, %[vlm], %[srdz], %[som] offen offset:2048 sc1\n\tbuffer_load_dwordx2 %5, %[vlm], %[srdz], %[som] offen offset:2560 sc1\n\t" \
              "buffer_load_dwordx2 %6, %[vlm], %[srdz], %[som] offen offset:3072 sc1\n\tbuffer_load_dwordx2 %7, %[vlm], %[srdz], %[som] offen offset:3584 sc1\n\tbuffer_load_dwordx4 %8, %[vlr], %[srdz], 0 offen sc1\n\t"
#define LREG(c) c "{v[0:1]}"(mp[0]), c "{v[2:3]}"(mp[1]), c "{v[4:5]}"(mp[2]), c "{v[6:7]}"(mp[3]), c "{v[8:9]}"(mp[4]), c "{v[10:11]}"(mp[5]), c "{v[12:13]}"(mp[6]), c "{v[14:15]}"(mp[7]), c "{v[16:19]}"(rs)
      asm volatile(LOOK9 : LREG("=&") : [vlm] "v"(vlm), [vlr] "v"(vlr), [srdz] "s"(srdz), [som] "s"((unsigned)WS_MOD) : "memory");
      #pragma unroll
      for (int q = 0; q < 14; ++q)
        asm volatile("s_mov_b32 m0, %0\n\ts_nop 0\n\tbuffer_load_dwordx4 %1, %2, %3 offen lds" :: "s"(m0b + 65536u + 16384u * (q >> 2) + 1024u * (q & 3)), "v"(wd0), "s"(srdw),
                     "s"(0x80000u * (q >> 2) + 65536u * ((q >> 1) & 1) + 128u * (q & 1)) : "m0", "memory");
      asm volatile("s_waitcnt vmcnt(23)" ::: "memory");
      { const u32x4 cs = XV(4); *(u32x2*)(lds + 131072 + 8192 + tid * 8u) = u32x2{silu2(cs[0]), silu2(cs[1])}; }
      f32x2 xu[16];
      #pragma unroll
      for (int i = 0; i < 4; ++i) { const u32x4 o = XV(i);
        #pragma unroll
        for (int e = 0; e < 4; ++e) { xu[4 * i + e] = unpk(o[e]); asm volatile("" : "+v"(xu[4 * i + e])); } }
      unsigned vpw = ldsb + 131072u + 4u * tid, vpr = ldsb + 131072u + 1024u + 32u * l16 + 8u * wid, vpc = ldsb + 131072u + 32u * (tid >> 4), vpq = ldsb + 131072u + 1024u + 32u * l16;
      unsigned vh = (unsigned)WS_H + rb0 * 32768u + ch * 256u + 16u * l16; float one = wid >> 1 ? 1.f : 0.f;
      {
        unsigned bl, bh, cnt = SPIN_CAP;
#define XO(d, r, t) "v_bitop3_b32 %[" #d "], v" #r ", %[" #t "], %[" #d "] bitop3:0xbe\n\t"
        asm volatile(
          "s_waitcnt vmcnt(14)\n\t"
          "s_branch Lm1_%=\n"
          "Lm0_%=:\n\t"
          "s_sleep " STR(ASLEEP) "\n\t"
          LOOK9
          "s_waitcnt vmcnt(0)\n"
          "Lm1_%=:\n\t"
          "v_bitop3_b32 %[bl], v0, v2, %[tl] bitop3:0x7e\n\t"
          "v_bitop3_b32 %[bh], v1, v3, %[th] bitop3:0x7e\n\t"
          XO(bl, 4, tl) XO(bh, 5, th) XO(bl, 6, tl) XO(bh, 7, th) XO(bl, 8, tl) XO(bh, 9, th) XO(bl, 10, tl) XO(bh, 11, th) XO(bl, 12, tl) XO(bh, 13, th) XO(bl, 14, tl) XO(bh, 15, th)
          XO(bl, 16, tl) XO(bh, 17, th) XO(bl, 18, tl) XO(bh, 19, th)
          "v_and_or_b32 %[bl], %[bl], %[ff], %[bh]\n\t"
          "v_cmp_ne_u32 vcc, 0, %[bl]\n\t"
          "s_sub_u32 %[cnt], %[cnt], 1\n\t"
          "s_cbranch_scc1 Lm2_%=\n\t"
          "s_cbranch_vccnz Lm0_%=\n"
          "Lm2_%=:"
          : LREG("+"), [bl] "=&v"(bl), [bh] "=&v"(bh), [cnt] "+s"(cnt), "+v"(vpw), "+v"(vpr), "+v"(vpc), "+v"(vpq), "+v"(vh), "+v"(one)
          : [vlm] "v"(vlm), [vlr] "v"(vlr), [srdz] "s"(srdz), [som] "s"((unsigned)WS_MOD), [tl] "s"(tl), [th] "s"(th), [ff] "v"(0xffu) : "vcc", "scc", "memory");
      }
      f32x4 a0, a1, c0, c1, t0, t1;
      {
        auto val = [&](int k) { return __uint_as_float(mp[k][0] & 0xffffff00u); };
        const float v = rbh(one + rbh(((val(0) + val(1)) + (val(2) + val(3))) + ((val(4) + val(5)) + (val(6) + val(7)))));
        const float rd = __builtin_amdgcn_rcpf(hi16(rs[2]));
        asm volatile("ds_write_b32 %0, %1\n\tds_write_b64 %2, %3" :: "v"(vpw), "v"(v * QSCALE), "v"(vpr), "v"(f32x2{-hi16(rs[0]) * rd, rd}) : "memory");
      }
      PAGE(6, "adaln_fr");
      asm volatile("s_waitcnt lgkmcnt(0)\n\ts_barrier\n\tds_read_b128 %4, %7\n\tds_read_b128 %0, %6\n\tds_read_b128 %2, %6 offset:512\n\tds_read_b128 %1, %6 offset:16\n\tds_read_b128 %3, %6 offset:528\n\tds_read_b128 %5, %7 offset:16\n\t"
                   "buffer_load_dword a255, %8, %9, %10 offen\n\ts_waitcnt lgkmcnt(0)"
                   : "=&v"(a0), "=&v"(a1), "=&v"(c0), "=&v"(c1), "=&v"(t0), "=&v"(t1) : "v"(vpc), "v"(vpq), "v"(tq0), "s"(srdw), "s"(0x200000u) : "memory");
      FQ(0);
      #pragma unroll
      for (int i = 0; i < 4; ++i) {
        const float mn = i < 2 ? t0[2 * i] : t1[2 * i - 4], rd = i < 2 ? t0[2 * i + 1] : t1[2 * i - 3];
        u32x4 o;
        #pragma unroll
        for (int e = 0; e < 4; ++e) {
          const f32x2 sc = e < 2 ? f32x2{c0[2 * e], c0[2 * e + 1]} : f32x2{c1[2 * e - 4], c1[2 * e - 3]}, sh = e < 2 ? f32x2{a0[2 * e], a0[2 * e + 1]} : f32x2{a1[2 * e - 4], a1[2 * e - 3]};
          o[e] = cvtpk2(__builtin_elementwise_fma(__builtin_elementwise_fma(xu[4 * i + e], f32x2{rd, rd}, f32x2{mn, mn}), sc, sh));
        }
        asm volatile("buffer_store_dwordx4 %0, %1, %2, %3 offen sc1\n\ts_nop 1" :: "v"(o), "v"(vh), "s"(srdz), "s"(32768u * i) : "memory");
      }
      FQ(1);
    asm volatile(FA_BODY
      : FA_OUTS, [sin] "+s"(sin), [oq0] "+v"(oq0), [tlo] "+v"(tlo), [thi] "+v"(thi), [cx] "+s"(cx)
      : [kb0] "v"(kb0), [vvb] "v"(vvb), [kd0] "v"(kd0), [vd0] "v"(vd0),
        [m0b] "s"(m0b), [flag] "s"(flag), [srdz] "s"(srdz), [srdw] "s"(srdw), [srdk] "s"(srdk), [srdv] "s"(srdv), [m0t] "s"(m0t), [srdt] "s"(srdt), [srdp] "s"(srdp),
        [tag] "s"(tag), [pcnt] "s"(pcnt), [psig] "s"(psig), [wid] "s"(wid), [rd0] "v"(rd0), [wo0] "v"(wo0), [vg0] "v"(vg0), [vx0] "v"(vx0), [px] "s"(p.x), [vct] "v"(vct), [pct] "s"(pct), [vdn] "v"(vdn), [ta0] "v"(ta0), [vxs] "v"(vxs),
        [wbytes] "s"(wbytes)
      : FA_CLOBBERS);
    cx = __builtin_amdgcn_readfirstlane(cx); hx ^= cx; sfl ^= cx; spart ^= cx;
  }
#define RDELAYN 24
#define RDELAY2 8
#define RDELAY3 16
#define RCHKN 5
#define EARLYOUT 1
  unsigned lane; asm volatile("v_mbcnt_lo_u32_b32 %0, -1, 0\n\tv_mbcnt_hi_u32_b32 %0, -1, %0" : "=v"(lane));
  const unsigned tid = wid * 64u + lane, n = lane & 15u, g = lane >> 4;
  const unsigned r0 = 256u * y + 8u * hd + 2u * wid, row = r0 + ((n >> 2) & 1u);
  const unsigned colb = 256u * (g >> 1) + 128u * (n >> 3) + 16u * (g & 1u) + 4u * (n & 3u);
  char* par = lds;
  const unsigned cq0 = colb + 32u * yp2, cq1 = colb + 32u * (2u ^ yp2);
  auto parq = [&](int o, int k) { return *(const u32x2*)(par + o + (k >> 2 ? cq1 : cq0) * 2u + 64 * ((k >> 1) & 1) + 1024 * (k & 1)); };
  auto parf = [&](int o, int k) { return *(const f32x4*)(par + o + (k >> 2 ? cq1 : cq0) * 4u + 128 * ((k >> 1) & 1) + 2048 * (k & 1)); };
  if (__builtin_expect(__any(late != 0u), 0)) {
    u32x4 mt[4]; u32x2 wr;
    asm volatile("s_barrier" ::: "memory");
    for (unsigned spins = 0; spins <= SPIN_CAP; ++spins) {
      asm volatile("s_nop 4\n\tglobal_load_dwordx2 %0, %1, %2" : "=v"(wr) : "v"(tid * 8u), "s"(p.wr) : "memory");
      #pragma unroll
      for (int k = 0; k < 4; ++k) asm volatile("buffer_load_dwordx4 %0, %1, %2, %3 offen sc1" : "=v"(mt[k]) : "v"(tid * 16u), "s"(srdz), "s"((unsigned)WS_MODT + 4096u * k) : "memory");
      asm volatile("s_waitcnt vmcnt(0)" : "+v"(mt[0]), "+v"(mt[1]), "+v"(mt[2]), "+v"(mt[3]), "+v"(wr) :: "memory");
      const u32x4 t = mt[0] & mt[1] & mt[2] & mt[3];
      if (__all((t[0] & t[1] & t[2] & t[3] & 1u) != 0u)) break;
    }
    const f32x4 a = __builtin_bit_cast(f32x4, mt[2]) * f32x4{lo16(wr[0]), hi16(wr[0]), lo16(wr[1]), hi16(wr[1])};
    *(u32x4*)(par + tid * 16u) = mt[0]; *(f32x4*)(par + 4096 + tid * 16u) = a;
    #pragma unroll
    for (int k = 1; k < 4; ++k) *(u32x2*)(par + (k == 3 ? 14336 : 6144 + 2048 * k) + tid * 8u) = u32x2{__builtin_amdgcn_perm(mt[k][1], mt[k][0], 0x07060302u), __builtin_amdgcn_perm(mt[k][3], mt[k][2], 0x07060302u)};
    *(float*)(par + 12424 + 16u * wid) = wave_sum((a[0] + a[1]) + (a[2] + a[3]));
    asm volatile("s_waitcnt lgkmcnt(0)\n\ts_barrier" ::: "memory");
    #pragma unroll
    for (int k = 0; k < 8; ++k) { gaf[k] = parf(0, k); aw[k] = parf(4096, k); }
    arow = dpp_add<DPP_XOR2>(dpp_add<DPP_XOR1>(*(const float*)(par + 12424 + 16u * (lane & 3u))));
  }
  unsigned vin = lane * 16u, vin3 = lane * 16u + 20480u, vin2 = lane * 16u + 16384u, kp = (unsigned)WS_KEYS + (b & 3u) * RSTR + hx + (lane >> 2) * KSTR + 32u * (lane & 3u);
  TQ(0);
  f32x2 x1[16], s2, d2, q2, sbf[16], gbf[16], c1f[16];
  float mrow, rdrow, drow;
  unsigned vkey, vkb, vkr, nrow, vpub, ta, tu, tb; char* pk;
  {
#define H2(v, h) f32x2{v[2 * h], v[2 * h + 1]}
#define PART(R, c) do { f32x4 t0, t1; \
      asm volatile("v_mfma_f32_16x16x32_bf16 %0, %2, a[" #R ":" #R "+3], 0\n\tv_mfma_f32_16x16x32_bf16 %1, %2, a[" #R "+8:" #R "+11], 0\n\t" \
                   "v_mfma_f32_16x16x32_bf16 %0, %3, a[" #R "+4:" #R "+7], %0\n\tv_mfma_f32_16x16x32_bf16 %1, %3, a[" #R "+12:" #R "+15], %1\n\ts_nop 6" : "=&v"(t0), "=&v"(t1) : "v"(alo), "v"(ahi)); \
      f32x2 u0 = H2(t0, 0), u1 = H2(t0, 1), u2 = H2(t1, 0), u3 = H2(t1, 1); \
      asm volatile("v_pk_fma_f32 %0, %0, %4, %8\n\tv_pk_fma_f32 %1, %1, %5, %9\n\tv_pk_fma_f32 %2, %2, %6, %10\n\tv_pk_fma_f32 %3, %3, %7, %11" : "+v"(u0), "+v"(u1), "+v"(u2), "+v"(u3) \
                   : "v"(H2(gaf[2 * c], 0)), "v"(H2(gaf[2 * c], 1)), "v"(H2(gaf[2 * c + 1], 0)), "v"(H2(gaf[2 * c + 1], 1)), "v"(xf[4 * c]), "v"(xf[4 * c + 1]), "v"(xf[4 * c + 2]), "v"(xf[4 * c + 3])); \
      x1[4 * c] = rb2(u0); x1[4 * c + 1] = rb2(u1); x1[4 * c + 2] = rb2(u2); x1[4 * c + 3] = rb2(u3); } while (0)
#define AC3(k) "v_pk_add_f32 %0, %0, %[y" #k "]\n\tv_pk_fma_f32 %1, %[y" #k "], %[w" #k "], %1\n\tv_pk_fma_f32 %2, %[y" #k "], %[y" #k "], %2\n\t"
    auto part_ = [&](int c2, f32x2& sa, f32x2& da, f32x2& qa) __attribute__((always_inline)) {
      if (c2 == 0) PART(64, 0); else if (c2 == 1) PART(80, 1); else if (c2 == 2) PART(96, 2); else PART(112, 3);
      asm volatile(AC3(0) AC3(1) AC3(2) AC3(3) : "+v"(sa), "+v"(da), "+v"(qa) : [y0] "v"(x1[4 * c2]), [y1] "v"(x1[4 * c2 + 1]), [y2] "v"(x1[4 * c2 + 2]), [y3] "v"(x1[4 * c2 + 3]),
                   [w0] "v"(H2(aw[2 * c2], 0)), [w1] "v"(H2(aw[2 * c2], 1)), [w2] "v"(H2(aw[2 * c2 + 1], 0)), [w3] "v"(H2(aw[2 * c2 + 1], 1)));
    };
#define RCV4(o, a, b, c, d) \
      "buffer_load_dwordx4 " #a ", %[vin" #o "], %[srdz], %[sin] offen sc1\n\t" \
      "buffer_load_dwordx4 " #b ", %[vin" #o "], %[srdz], %[sin] offen offset:1024 sc1\n\t" \
      "buffer_load_dwordx4 " #c ", %[vin" #o "], %[srdz], %[sin] offen offset:2048 sc1\n\t" \
      "buffer_load_dwordx4 " #d ", %[vin" #o "], %[srdz], %[sin] offen offset:3072 sc1\n\t"
#define RWAIT(lv, w) \
      "Lrq" #lv "_%=:\n\t" \
      "v_mov_b32 " w ", 0\n\t" \
      "s_mov_b32 %[sc], " STR(RCHKN) "\n\t" \
      LOOK2 \
      "Lrs" #lv "_%=:\n\t" \
      "ds_read_b32 %[tmp], %[vsh]\n\t" \
      "s_waitcnt lgkmcnt(0)\n\t" \
      "v_and_or_b32 %[tmp], %[tmp], " #lv ", " w "\n\t" \
      "v_cmp_eq_u32 vcc, 0, %[tmp]\n\t" \
      "s_cbranch_vccz Lrd" #lv "_%=\n\t" \
      "s_sub_u32 %[sc], %[sc], 1\n\t" \
      "s_cbranch_scc0 Lrs" #lv "_%=\n\t" \
      "s_sub_u32 %[cnt], %[cnt], 1\n\t" \
      "s_cselect_b32 %[cnt], 0, %[cnt]\n\t" \
      "s_waitcnt vmcnt(0)\n\t" \
      "v_cmp_eq_u32 vcc, 0, " w "\n\t" \
      "s_cbranch_scc1 Lrl" #lv "_%=\n\t" \
      "s_cbranch_vccnz Lrq" #lv "_%=\n" \
      "Lrd" #lv "_%=:\n\t" \
      "v_mov_b32 %[tmp], " #lv "\n\t" \
      "s_mov_b64 exec, 1\n\t" \
      "ds_or_b32 %[vsh], %[tmp]\n\t" \
      "s_mov_b64 exec, -1\n" \
      "Lrl" #lv "_%=:\n\t"
#define RCV8A RCV4(0, a[64:67], a[68:71], a[72:75], a[76:79]) RCV4(1, a[80:83], a[84:87], a[88:91], a[92:95])
#define RCV4C RCV4(2, a[96:99], a[100:103], a[104:107], a[108:111])
#define RCV4D RCV4(3, a[112:115], a[116:119], a[120:123], a[124:127])
#define RCV8B RCV4C RCV4D
#define LOOK2 "buffer_load_dwordx2 v[228:229], %[vfl], %[srdz], %[sfl] offen offset:64 sc1\n\t"
    unsigned cnt, tmp, sc;
    u32x2 sbq[8], gbq[8], c1q[8];
    #pragma unroll
    for (int k = 0; k < 8; ++k) { sbq[k] = parq(8192, k); c1q[k] = parq(10240, k); gbq[k] = parq(14336, k); }
    #pragma unroll
    for (int i = 0; i < 16; ++i) { sbf[i] = unpk(sbq[i >> 1][i & 1]); c1f[i] = unpk(c1q[i >> 1][i & 1]); asm volatile("" : "+v"(sbf[i]), "+v"(c1f[i])); }
    unsigned vfl = (lane & 15u) * 512u + (lane & 16u) * 1024u, vin1 = lane * 16u + 4096u;
    PAGE(1, "adaln_pg2");
    asm volatile(
      "s_mov_b32 %[go], 0\n\t"
      "s_cmp_lg_u32 %[got], 0\n\t"
      "s_waitcnt vmcnt(4)\n\t"
      "s_cbranch_scc1 Lrf_%=\n\t"
      "v_cmp_eq_u32 vcc, 0, v229\n\t"
      "s_cbranch_vccnz Lrk_%=\n\t"
      "s_mov_b32 %[got], 2\n\t"
      RCV8A
      "Lrf_%=:\n\t"
      "v_cmp_eq_u32 vcc, 0, v228\n\t"
      "s_cbranch_vccnz Lrk_%=\n\t"
      "s_mov_b32 %[go], 1\n\t"
      RCV4C
      "s_branch Lrn_%=\n"
      "Lrk_%=:\n\t"
      LOOK2
      "Lrn_%=:"
      : "+{v228}"(sd0), "+{v229}"(fl), [got] "+s"(got), [go] "=&s"(got2), [vfl] "+v"(vfl), [vin1] "+v"(vin1), "+v"(gbq[0]), "+v"(gbq[1]), "+v"(gbq[2]), "+v"(gbq[3]), "+v"(gbq[4]), "+v"(gbq[5]), "+v"(gbq[6]), "+v"(gbq[7])
      : [srdz] "s"(srdz), [sfl] "s"(sfl), [sin] "s"(sin), [vin0] "v"(vin), [vin2] "v"(vin2) : "vcc", "scc", "memory");
#define NRE
    #pragma unroll
    for (int i = 0; i < 16; ++i) { gbf[i] = unpk(gbq[i >> 1][i & 1]); asm volatile("" : "+v"(gbf[i])); }
    vkey = lane * RSTR + (b >> 4) * KSTR + (b & 15u) * 8u; vkb = ldsb + 12288u + 8u * wid + (lane & 4u); vkr = ldsb + 12288u + 4u * (lane & 7u); nrow = 2047u - row;
    vpub = (unsigned)WS_ROWS + (y * 32u + hd) * 8192u + (g >> 1) * 512u + ((n >> 3) * 8u + (g & 1u) * 4u + (n & 3u)) * 16u;
    ta = m0b + 65536u + 8u * lane; tu = m0t + 8u * lane; tb = m0b + 98304u + 16384u * (g >> 1) + 2048u * (g & 1u) + 512u * ((lane >> 2) & 3u) + 8u * (lane & 3u);
    pk = p.ws + WS_KEYS;
    s2 = f32x2{0.f, 0.f}; d2 = s2; q2 = s2;
    f32x2 s3, d3, q3;
    asm volatile(
      "s_mov_b32 %[cnt], 0x10000\n\t"
      "s_cmp_lg_u32 %[go], 0\n\t"
      "s_cbranch_scc1 Lro_%=\n\t"
      "s_cmp_lg_u32 %[got], 0\n\t"
      "s_waitcnt vmcnt(0)\n\t"
      "s_cbranch_scc1 Lrf_%=\n\t"
      "v_cmp_eq_u32 vcc, 0, v229\n\t"
      "s_cbranch_vccz Lrl2_%=\n\t"
      RWAIT(2, "v229")
      RCV8A
      "Lrf_%=:\n\t"
      "v_cmp_eq_u32 vcc, 0, v228\n\t"
      "s_cbranch_vccz Lrm_%=\n\t"
      RWAIT(4, "v228")
      RCV4C
      "s_sleep " STR(RDELAYN) "\n\t"
      "s_branch Lrp_%=\n"
      "Lrm_%=:\n\t"
      RCV4C
      "s_sleep " STR(RDELAY3) "\n\t"
      "s_branch Lrp_%=\n"
      "Lro_%=:\n\t"
      "s_sleep " STR(RDELAY2) "\n"
      "Lrp_%=:\n\t"
      RCV4D
      "s_cmp_eq_u32 %[got], 1\n\t"
      "s_cbranch_scc1 Lrz_%=\n\t"
      "s_waitcnt vmcnt(8)\n"
      "Lrz_%=:"
      : "+{v228}"(sd0), "+{v229}"(fl), [cnt] "=&s"(cnt), [tmp] "=&v"(tmp), [sc] "=&s"(sc), "+v"(s2),
        "+v"(vkey), "+v"(vkb), "+v"(vkr), "+v"(nrow), "+v"(vpub), "+v"(ta), "+v"(tu), "+v"(tb), "+s"(pk)
      : [srdz] "s"(srdz), [got] "s"(got), [go] "s"(got2), [vfl] "v"(vfl), [sfl] "s"(sfl), [sin] "s"(sin), [vin0] "v"(vin), [vin1] "v"(vin1), [vin2] "v"(vin2), [vin3] "v"(vin3), [vsh] "v"(ldsb + 12420u)
      : "vcc", "scc", "memory");
    part_(0, s2, d2, q2); part_(1, s2, d2, q2);
    TQ(2);
    s3 = s2; d3 = d2; q3 = q2;
    asm volatile("s_waitcnt vmcnt(4)" : "+{v228}"(sd0), "+{v229}"(fl), "+v"(s2), "+v"(d2), "+v"(q2), "+v"(s3), "+v"(d3), "+v"(q3) :: "memory");
    part_(2, s3, d3, q3);
    asm volatile("s_waitcnt vmcnt(0)" : "+v"(s3), "+v"(d3), "+v"(q3) :: "memory");
    TQ(3);
    part_(3, s3, d3, q3);
    if (__builtin_expect(__any(s3[0] + s3[1] != s3[0] + s3[1]), 0)) {
      unsigned spins = 0;
      do {
        asm volatile(RCV8B "s_waitcnt vmcnt(0)" :: [srdz] "s"(srdz), [sin] "s"(sin), [vin2] "v"(vin2), [vin3] "v"(vin3) : "memory"); NRE;
        s3 = s2; d3 = d2; q3 = q2;
        part_(2, s3, d3, q3); part_(3, s3, d3, q3);
      } while (__any(s3[0] + s3[1] != s3[0] + s3[1]) && ++spins < RETRY_CAP);
    }
    asm volatile("; all in");
    const float sv = s3[0] + s3[1], dv = d3[0] + d3[1], qv = q3[0] + q3[1];
    float u = swap16_add(swap32_add(sv, dv), swap32_add(qv, qv));
    u = dpp_add<DPP_XOR1>(u); u = dpp_add<DPP_XOR2>(u); u = dpp_add<DPP_ROR8>(u);
    const auto r16 = __builtin_amdgcn_permlane16_swap(__float_as_uint(u), __float_as_uint(u), false, false);
    const auto r32 = __builtin_amdgcn_permlane32_swap(r16[0], r16[0], false, false);
    const float mean = __uint_as_float(r32[0]) * (1.f / DM), m2v = mean * mean;
    drow = __uint_as_float(r32[1]);
    float var = fmaf(__uint_as_float(r16[1]), 1.f / DM, -m2v);
    if (__builtin_expect(__any(!(m2v <= var)), 0)) {
      f32x2 qq = {0.f, 0.f};
      #pragma unroll
      for (int e = 0; e < 16; ++e) { const f32x2 t = x1[e] - mean; qq = __builtin_elementwise_fma(t, t, qq); }
      var = row_sum(qq[0] + qq[1]) * (1.f / DM);
    }
    mrow = rbh(mean); rdrow = __builtin_amdgcn_rcpf(rbh(__builtin_amdgcn_sqrtf(rbh(rbh(var) + 1e-5f))));
  }
  unsigned* kb = (unsigned*)(lds + 12288);
  unsigned mykey, k1w;
  { const unsigned bb = __float_as_uint(fmaf(-mrow, arow, drow) * rdrow); mykey = max((bb ^ ((unsigned)((int)bb >> 31) | 0x80000000u)) & 0xfffff800u, 0x800u) | nrow; }
  asm volatile("ds_write_b32 %1, %2\n\ts_waitcnt lgkmcnt(0)\n\ts_barrier\n\tds_read_b32 %0, %3\n\ts_waitcnt lgkmcnt(0)" : "=&v"(k1w) : "v"(vkb), "v"(mykey), "v"(vkr) : "memory");
  bool cand;
  u32x4 ka, kc;
#define KOP "global_atomic_or_x2"
#define KSC " sc1"
  {
    const unsigned o1 = __builtin_amdgcn_update_dpp(0u, k1w, DPP_XOR1, 0xf, 0xf, true);
    unsigned k1 = max(k1w, o1), k2 = min(k1w, o1);
    top2_dpp<DPP_XOR2>(k1, k2); top2_dpp<0x141>(k1, k2);
    asm volatile("s_cmp_lg_u32 %[wid], 0\n\ts_cbranch_scc1 Lka_%=\n\ts_mov_b64 exec, %[mk]\n\t" KOP " %[va], %[kk], %[pk]" KSC "\n\ts_mov_b64 exec, -1\nLka_%=:"
                 :: [wid] "s"(wid), [mk] "n"((1u << KEY_REP) - 1u), [va] "v"(vkey), [kk] "v"(u32x2{k2, k1}), [pk] "s"(pk) : "scc", "memory");
    cand = mykey >= k2;
    vpub += mykey == k1 ? 0u : 2048u;
  }
  TQ(4);
  PAGE(2, "adaln_pg3");
#define TRA(r, a, o) asm volatile("ds_read_b64_tr_b16 a[%0:%0 + 1], %1 offset:%2" :: "n"(r), "v"(a), "n"(o) : "memory")
#define FRAGS(k) do { \
      TRA(128 + 8 * (k), ta, 2048 * ((k) >> 2) + 512 * ((k) & 1) + 16384 * (((k) >> 1) & 1)); TRA(130 + 8 * (k), ta, 2048 * ((k) >> 2) + 512 * ((k) & 1) + 16384 * (((k) >> 1) & 1) + 1024); \
      TRA(132 + 8 * (k), tu, 2048 * ((k) >> 2) + 512 * ((k) & 1) + 4096 * (((k) >> 1) & 1)); TRA(134 + 8 * (k), tu, 2048 * ((k) >> 2) + 512 * ((k) & 1) + 4096 * (((k) >> 1) & 1) + 1024); \
      TRA(192 + 4 * (k), tb, 64 * (k)); TRA(194 + 4 * (k), tb, 64 * (k) + 32); } while (0)
  f32x2 h2[16];
  {
    asm volatile("" : "+v"(mrow));
    const float nm = -mrow * rdrow;
    #pragma unroll
    for (int k = 0; k < 8; ++k) {
      #pragma unroll
      for (int e = 0; e < 2; ++e) { const int i = 2 * k + e; h2[i] = __builtin_elementwise_fma(__builtin_elementwise_fma(x1[i], f32x2{rdrow, rdrow}, f32x2{nm, nm}), c1f[i], sbf[i]); }
      if (k & 1) { asm volatile("" : "+v"(h2[2 * k]), "+v"(h2[2 * k + 1])); FRAGS(k >> 1); }
    }
  }
  const unsigned cw = __builtin_amdgcn_readfirstlane((unsigned)__ballot(cand) & 0x11u);
  auto publish = [&](const f32x2* v, unsigned off) __attribute__((always_inline)) {
    #pragma unroll
    for (int k = 0; k < 4; ++k) { const int q = 4 * (k >> 1) + (k & 1);
      asm volatile("buffer_store_dwordx4 %0, %1, %2, %3 offen sc1\n\ts_nop 1" :: "v"(u32x4{cvtpk2(v[2 * q]), cvtpk2(v[2 * q + 1]), cvtpk2(v[2 * q + 4]), cvtpk2(v[2 * q + 5])}), "v"(vpub), "s"(srdz), "s"(off + (k & 1) * 1024u + (((k >> 1) * 2u) ^ yp2) * 128u) : "memory"); }
  };
  if (cand) publish(h2, 0u);
  char* st = lds + 32768 + 4096u * wid;
  auto look1 = [&]() __attribute__((always_inline)) {
    asm volatile("s_cmp_lg_u32 %2, 0\n\ts_cbranch_scc1 Lkl_%=\n\tbuffer_load_dwordx4 %0, %3, %4, 0 offen sc1\n\tbuffer_load_dwordx4 %1, %3, %4, 0 offen offset:16 sc1\nLkl_%=:" : "=&{v[240:243]}"(ka), "=&{v[244:247]}"(kc) : "s"(wid), "v"(kp), "s"(srdz) : "scc", "memory");
  };
  if (KLOOK == 0) look1();
  #pragma unroll
  for (int k = 0; k < 8; ++k) {
    u32x2 yq;
    #pragma unroll
    for (int e = 0; e < 2; ++e) yq[e] = cvtpk2(__builtin_elementwise_fma(gbf[2 * k + e], h2[2 * k + e], x1[2 * k + e]));
    *(u32x2*)(st + (row & 1u) * 2048u + (colb + 32u * ((k >> 2) * 2u ^ yp2)) * 2u + 64 * ((k >> 1) & 1) + 1024 * (k & 1)) = yq;
    if (k & 1) FRAGS(4 + (k >> 1));
    if (k == KLOOK - 1) look1();
  }
  auto out_rows = [&](unsigned which) __attribute__((always_inline)) {
    #pragma unroll
    for (int r = 0; r < 2; ++r) {
      if ((which >> (4 * r)) & 1u) {
        const u32x4 a = *(const u32x4*)(st + 2048 * r + 16u * lane), c = *(const u32x4*)(st + 2048 * r + 1024 + 16u * lane);
        asm volatile("s_nop 4\n\tglobal_store_dwordx4 %0, %1, %3 offset:%4 sc1\n\tglobal_store_dwordx4 %0, %2, %3 offset:%5 sc1\n\ts_nop 1" :: "v"(r0 * 2048u + 16u * lane), "v"(a), "v"(c), "s"(pout), "n"(2048 * r), "n"(2048 * r + 1024) : "memory");
      }
    }
  };
  const unsigned un = tid & 127u, ca = 256u * (un >> 5) + 64u * ((un >> 4) & 1u) + 128u * ((un >> 3) & 1u) + 16u * ((un >> 2) & 1u) + 4u * (un & 3u);
  unsigned vrow = (unsigned)WS_ROWS + (moe ? wid * 512u + 16u * g : un * 16u), vsel = ldsb + 12288u + 40u + 4u * (moe ? lane & 1u : wid >> 1);
  unsigned mk[8];
  #pragma unroll
  for (int t = 0; t < 8; ++t) { mk[t] = (unsigned)__builtin_amdgcn_sbfe((int)(1u << (n >> 1)), t, 1); asm volatile("" : "+v"(mk[t])); }
  unsigned vred = ldsb + 24576u + wid * 256u + (lane & 1u) * 64u + 16u * g, vrf = ldsb + 24576u + ((lane & 1u) * 4u + g) * 16u + ((lane >> 1) & 3u) * 4u, vpt = spart + lane * 16u, vkk = ldsb + 12288u + 32u, vkw = vkk + (lane < 48u ? 64u : 0u);
  unsigned long long m2 = 0x0003000300030003ull;
  asm volatile("" : "+v"(vrow), "+v"(vsel), "+v"(vred), "+v"(vrf), "+v"(vpt), "+v"(vkk), "+v"(vkw), "+s"(m2));
  const unsigned later = __builtin_amdgcn_readfirstlane(wid && EARLYOUT ? cw : 0x11u);
  out_rows(~later);
  TQ(1);
  PAGE(3, "adaln_pg8");
  if (wid == 0) {
    unsigned cnt = SPIN_CAP, t;
    asm volatile(
      "s_waitcnt vmcnt(0)\n"
      "Lk0_%=:\n\t"
      "v_min3_u32 %[t], v240, v242, v244\n\t"
      "v_min_u32 %[t], %[t], v246\n\t"
      "v_cmp_eq_u32 vcc, 0, %[t]\n\t"
      "s_cbranch_vccz Lk1_%=\n\t"
      "s_sleep " STR(KSLEEP) "\n\t"
      "buffer_load_dwordx4 v[240:243], %[kp], %[srdz], 0 offen sc1\n\t"
      "buffer_load_dwordx4 v[244:247], %[kp], %[srdz], 0 offen offset:16 sc1\n\t"
      "s_sub_u32 %[cnt], %[cnt], 1\n\t"
      "s_waitcnt vmcnt(0)\n\t"
      "s_cbranch_scc0 Lk0_%=\n"
      "Lk1_%=:"
      : "+{v[240:243]}"(ka), "+{v[244:247]}"(kc), [t] "=&v"(t), [cnt] "+s"(cnt) : [kp] "v"(kp), [srdz] "s"(srdz) : "vcc", "scc", "memory");
    unsigned k1 = ka[1], k2 = ka[0];
    unsigned l1 = kc[1], l2 = kc[0];
    top2_merge(k1, k2, ka[3], ka[2]); top2_merge(l1, l2, kc[3], kc[2]); top2_merge(k1, k2, l1, l2);
    top2_dpp<DPP_XOR1>(k1, k2); top2_dpp<DPP_XOR2>(k1, k2); top2_dpp<0x141>(k1, k2); top2_dpp<0x140>(k1, k2);
    top2_merge(k1, k2, __builtin_amdgcn_update_dpp(0u, k1, 0x142, 0xa, 0xf, false), __builtin_amdgcn_update_dpp(0u, k2, 0x142, 0xa, 0xf, false));
    top2_merge(k1, k2, __builtin_amdgcn_update_dpp(0u, k1, 0x143, 0xc, 0xf, false), __builtin_amdgcn_update_dpp(0u, k2, 0x143, 0xc, 0xf, false));
    k1 = ~k1 & 2047u; k2 = ~k2 & 2047u;
    *(LAS u32x4*)vkw = u32x4{k1, k2, (k1 >> 3) * 8192u, (k2 >> 3) * 8192u + ((k1 ^ k2) < 8u ? 2048u : 0u)};
  }
  u32x2 kk; unsigned vo;
  asm volatile("s_waitcnt lgkmcnt(0)\n\ts_barrier\n\tds_read_b32 %0, %3\n\tds_read_b64 %1, %4\n\ts_waitcnt lgkmcnt(0)\n\tv_add_u32 %2, %2, %0" : "=&v"(vo), "=&v"(kk), "+v"(vrow) : "v"(vsel), "v"(vkk) : "memory");
  TQ(5);
  unsigned k1, k2, rest;
  auto routed = [&]() __attribute__((always_inline)) {
    asm volatile("" : "+v"(kk));
    k1 = __builtin_amdgcn_readfirstlane(kk[0]); k2 = __builtin_amdgcn_readfirstlane(kk[1]);
    rest = __builtin_amdgcn_readfirstlane(later & ((r0 != k1 && r0 != k2 ? 1u : 0u) | (r0 + 1u != k1 && r0 + 1u != k2 ? 16u : 0u)));
    if (__builtin_expect(rest != later, 0)) { if (row == k1 || row == k2) publish(x1, 4096u); }
  };
  if (__builtin_expect(!moe, 0)) {
    asm volatile("adaln_pg8:\n\ts_cbranch_execz adaln_pg4");
    PAGE(4, "adaln_kr");
    u32x4 ha; u32x2 w0, w1;
    asm volatile("s_nop 4\n\tglobal_load_dwordx2 %0, %2, %3\n\tglobal_load_dwordx2 %1, %2, %3 offset:64" : "=&v"(w0), "=&v"(w1) : "v"(ca * 2u), "s"(p.wr) : "memory");
    asm volatile("buffer_load_dwordx4 %0, %3, %4, 0 offen sc1\n\ts_waitcnt vmcnt(0)" : "=&v"(ha), "+v"(w0), "+v"(w1) : "v"(vrow), "s"(srdz) : "memory");
    for (unsigned spins = 0; __builtin_expect(__any(max(max(ha[0], ha[1]), max(ha[2], ha[3])) == 0xffffffffu), 0) && spins < RETRY_CAP; ++spins)
      asm volatile("buffer_load_dwordx4 %0, %1, %2, 0 offen sc1\n\ts_waitcnt vmcnt(0)" : "=&v"(ha) : "v"(vrow), "s"(srdz) : "memory");
    routed();
    if (b == 255u && tid == 0u) __hip_atomic_store((gu64*)(p.ws + WS_EP), tag, RLX_AGENT);
    char* rows = lds + 16384 + (tid >> 7) * 2048u + ca * 2u;
    *(u32x2*)rows = u32x2{ha[0], ha[1]}; *(u32x2*)(rows + 64) = u32x2{ha[2], ha[3]};
    {
      float dot = 0.f;
      #pragma unroll
      for (int e = 0; e < 4; ++e) { const unsigned w = e < 2 ? w0[e] : w1[e - 2]; dot = fmaf(hi16(ha[e]), hi16(w), fmaf(lo16(ha[e]), lo16(w), dot)); }
      dot = wave_sum(dot);
      if (lane == 0) kb[16u + wid] = __float_as_uint(dot);
    }
    asm volatile("s_waitcnt lgkmcnt(0)\n\ts_barrier" ::: "memory");
    TQ(6);
    const unsigned zj = (b & 4u) * 4u + (idx & 3u) * 4u + (b & 3u), pp = zj * 64u + lane, sq = pp & 1u, nn = 128u * (pp >> 8) + 16u * (2u * ((pp >> 6) & 3u) + ((pp >> 1) & 1u)) + ((pp >> 2) & 15u);
    float gbv = bf2f(*(const u16*)(par + 14336 + nn * 2u)), hv = bf2f(*(const u16*)(lds + 16384 + sq * 2048u + nn * 2u));
    float rsel = rbf(__builtin_amdgcn_rcpf(1.f + __builtin_amdgcn_exp2f(rbf(__uint_as_float(kb[16u + 2u * sq]) + __uint_as_float(kb[17u + 2u * sq])) * -1.44269504089f)));
    u16* vout = pout + (sq ? k2 : k1) * DM + nn;
    unsigned vp = (unsigned)WS_PART + (tid >> 5) * 16384u + (hx ^ 8192u) + ((tid >> 4) & 1u) * 4096u + (zj & 15u) * 256u + (tid & 15u) * 16u;
    unsigned vw = ldsb + 24576u + wid * 256u + (lane & 15u) * 16u + 4u * g, vr = ldsb + 24576u + 4u * lane;
    unsigned vx = (unsigned)WS_ROWS + 4096u + kb[10u + sq] + (nn >> 9) * 1024u + (32u * ((nn >> 8) & 1u) + 16u * ((nn >> 6) & 1u) + 8u * ((nn >> 7) & 1u) + 4u * ((nn >> 4) & 1u) + ((nn >> 2) & 3u)) * 16u + ((nn >> 5) & 1u) * 8u + (nn & 3u) * 2u;
    asm volatile("" : "+v"(gbv), "+v"(hv), "+v"(rsel), "+v"(vout), "+v"(vp), "+v"(vw), "+v"(vr), "+v"(vx));
    out_rows(rest);
    TQ(7);
    __builtin_amdgcn_s_sleep(FSLEEP);
    float u; unsigned xw;
    for (unsigned spins = 0;; ++spins) {
      f32x4 v[14];
      asm volatile("buffer_load_ushort %0, %1, %2, 0 offen sc1" : "=v"(xw) : "v"(vx), "s"(srdz) : "memory");
      #pragma unroll
      for (int k = 0; k < 14; ++k) asm volatile("buffer_load_dwordx4 %0, %1, %2, %3 offen sc1" : "=v"(v[k]) : "v"(vp), "s"(srdz), "s"(131072u * k) : "memory");
      asm volatile("s_waitcnt vmcnt(12)" : "+v"(v[0]), "+v"(v[1]), "+v"(xw) :: "memory");
      f32x4 sum = v[0] + v[1];
#define NEXT2(k) asm volatile("s_waitcnt vmcnt(%3)" : "+v"(v[k]), "+v"(v[k + 1]), "+v"(sum) : "n"(12 - k) : "memory"); sum += v[k]; sum += v[k + 1]
      NEXT2(2); NEXT2(4); NEXT2(6); NEXT2(8); NEXT2(10); NEXT2(12);
      u = swap16_add(swap32_add(sum[0], sum[2]), swap32_add(sum[1], sum[3]));
      if (!__any(u != u || xw == 0xffffu) || spins > RETRY_CAP) break;
    }
    TQ(1);
    f32x2 ra, rc;
    asm volatile("ds_write_b32 %0, %1\n\ts_waitcnt lgkmcnt(0)\n\ts_barrier" :: "v"(vw), "v"(u) : "memory");
    if (wid == 0) {
      asm volatile("ds_read2st64_b32 %0, %2 offset1:1\n\tds_read2st64_b32 %1, %2 offset0:2 offset1:3\n\ts_waitcnt lgkmcnt(0)" : "=&v"(ra), "=&v"(rc) : "v"(vr) : "memory");
      ra += rc;
      const unsigned ov = cvtpk(fmaf(gbv, fmaf(ra[0] + ra[1], rsel, hv), lo16(xw)), 0.f);
      asm volatile("global_store_short %0, %1, off sc1" :: "v"(vout), "v"(ov) : "memory");
    }
  } else {
    f32x4 dg, du;
    bf16x8 hb[8];
    auto fetch = [&]() __attribute__((always_inline)) {
      asm volatile("s_mov_b64 exec, %8\n\t"
                   "buffer_load_dwordx4 %0, %9, %10, 0 offen sc1\n\tbuffer_load_dwordx4 %1, %9, %10, 0 offen offset:64 sc1\n\tbuffer_load_dwordx4 %2, %9, %10, 0 offen offset:128 sc1\n\tbuffer_load_dwordx4 %3, %9, %10, 0 offen offset:192 sc1\n\t"
                   "buffer_load_dwordx4 %4, %9, %10, 0 offen offset:256 sc1\n\tbuffer_load_dwordx4 %5, %9, %10, 0 offen offset:320 sc1\n\tbuffer_load_dwordx4 %6, %9, %10, 0 offen offset:384 sc1\n\tbuffer_load_dwordx4 %7, %9, %10, 0 offen offset:448 sc1\n\t"
                   "s_mov_b64 exec, -1"
                   : "=&v"(hb[0]), "=&v"(hb[1]), "=&v"(hb[2]), "=&v"(hb[3]), "=&v"(hb[4]), "=&v"(hb[5]), "=&v"(hb[6]), "=&v"(hb[7]) : "s"(m2), "v"(vrow), "s"(srdz) : "memory");
    };
#define MM2(a, h, c0, c1) "v_mfma_f32_16x16x32_bf16 %0, a[" #a ":" #a "+3], %" #h ", " c0 "\n\tv_mfma_f32_16x16x32_bf16 %1, a[" #a "+4:" #a "+7], %" #h ", " c1 "\n\t"
#define MM16 MM2(128, 2, "0", "0") MM2(136, 3, "%0", "%1") MM2(144, 4, "%0", "%1") MM2(152, 5, "%0", "%1") MM2(160, 6, "%0", "%1") MM2(168, 7, "%0", "%1") MM2(176, 8, "%0", "%1") MM2(184, 9, "%0", "%1") "s_nop 7"
#define HB "+v"(hb[0]), "+v"(hb[1]), "+v"(hb[2]), "+v"(hb[3]), "+v"(hb[4]), "+v"(hb[5]), "+v"(hb[6]), "+v"(hb[7])
    fetch();
    routed();
    asm volatile("s_cmp_lg_u32 %10, %11\n\ts_waitcnt vmcnt(4)\n\ts_cbranch_scc1 Lh_%=\n\ts_waitcnt vmcnt(0)\nLh_%=:\n\t" MM16 : "=&v"(dg), "=&v"(du), HB : "s"(rest), "s"(later) : "scc", "memory");
    TQ(6);
    for (unsigned spins = 0; __builtin_expect((__ballot(dg[0] != dg[0]) & m2) != 0ull, 0) && spins < RETRY_CAP; ++spins) {
      fetch();
      asm volatile("s_waitcnt vmcnt(0)\n\t" MM16 : "=&v"(dg), "=&v"(du), HB :: "memory");
    }
    f32x2 ra, rc, rd, re;
    asm volatile("s_mov_b64 exec, %7\n\tds_write_b128 %4, %5\n\tds_write_b128 %4, %6 offset:128\n\ts_mov_b64 exec, -1\n\ts_waitcnt lgkmcnt(0)\n\ts_barrier\n\t"
                 "ds_read2_b32 %0, %8 offset1:32\n\tds_read2_b32 %1, %8 offset0:64 offset1:96\n\tds_read2_b32 %2, %8 offset0:128 offset1:160\n\tds_read2_b32 %3, %8 offset0:192 offset1:224\n\ts_waitcnt lgkmcnt(0)"
                 : "=&v"(ra), "=&v"(rc), "=&v"(rd), "=&v"(re) : "v"(vred), "v"(dg), "v"(du), "s"(m2), "v"(vrf) : "memory");
    u32x2 actp;
    {
      const f32x2 gu = rb2((ra + rc) + (rd + re));
      const float sg = __uint_as_float(cvtpk(0.f, gu[0] * __builtin_amdgcn_rcpf(1.f + __builtin_amdgcn_exp2f(gu[0] * -1.44269504089f))));
      const unsigned a = cvtpk(0.f, sg * gu[1]);
      const unsigned o2 = __builtin_amdgcn_update_dpp(0u, a, DPP_XOR2, 0xf, 0xf, true);
      const unsigned pr = (lane & 2u) ? ((o2 >> 16) | a) : ((a >> 16) | o2);
      const unsigned o4 = __builtin_amdgcn_update_dpp(0u, pr, DPP_ROR4, 0xf, 0xf, true);
      actp = (lane & 4u) ? u32x2{o4, pr} : u32x2{pr, o4};
    }
    f32x4 yv[2];
#define ABD(t) "v"(u32x4{actp[0] & mk[t], actp[1] & mk[t], actp[0] & mk[t + 1], actp[1] & mk[t + 1]})
#define MM3(a, w, c0, c1) "v_mfma_f32_16x16x32_bf16 %0, %" #a ", a[" #w ":" #w "+3], " c0 "\n\tv_mfma_f32_16x16x32_bf16 %1, %" #a ", a[" #w "+16:" #w "+19], " c1 "\n\t"
    asm volatile("s_nop 1\n\t" MM3(2, 192, "0", "0") MM3(3, 196, "%0", "%1") MM3(4, 200, "%0", "%1") MM3(5, 204, "%0", "%1") "s_nop 6\n\t"
                 "buffer_store_dwordx4 %0, %6, %7, 0 offen sc1\n\tbuffer_store_dwordx4 %1, %6, %7, 0 offen offset:1024 sc1\n\ts_nop 1"
                 : "=&v"(yv[0]), "=&v"(yv[1]) : ABD(0), ABD(2), ABD(4), ABD(6), "v"(vpt), "s"(srdz) : "memory");
    TQ(7);
    out_rows(rest);
  }
  asm volatile("adaln_text_end:");
}
}

extern "C" void kernel_launch(void* const* d_in, const int* in_sizes, int n_in, void* d_out, int out_size, void* d_ws, size_t ws_size, hipStream_t stream) {
  (void)in_sizes; (void)n_in; (void)out_size;
  if (ws_size < WS_END) return;
  const u16* x    = (const u16*)d_in[0];
  const u16* kc   = (const u16*)d_in[1];
  const u16* vc   = (const u16*)d_in[2];
  const u16* wq   = (const u16*)d_in[3];
  const u16* wo   = (const u16*)d_in[4];
  const u16* wr   = (const u16*)d_in[6];
  const u16* wg   = (const u16*)d_in[7];
  const u16* wu   = (const u16*)d_in[8];
  const u16* wd   = (const u16*)d_in[9];
  const u16* c    = (const u16*)d_in[10];
  const u16* wmod = (const u16*)d_in[11];
  u16* out = (u16*)d_out;
  char* ws = (char*)d_ws;
  fa::Args fr;
  fr.x = x; fr.wmod = wmod; fr.wq = wq; fr.kc = kc; fr.vc = vc; fr.wo = wo; fr.wg = wg; fr.wu = wu; fr.wd = wd; fr.ws = ws; fr.out = out; fr.c = c; fr.wr = wr;
  hipFuncSetAttribute((const void*)fa::fattn_k, hipFuncAttributeMaxDynamicSharedMemorySize, (int)fa::LDS_BYTES);
  hipLaunchKernelGGL(fa::fattn_k, dim3(256), dim3(256), fa::LDS_BYTES, stream, fr);
}
```

```cpp
#include <hip/hip_runtime.h>
#include <hip/hip_bf16.h>
#include <utility>
#include <cstdint>

#pragma clang fp contract(off)
typedef float    f32x16 __attribute__((ext_vector_type(16)));
typedef float    f32x2  __attribute__((ext_vector_type(2)));
typedef float    f32x4  __attribute__((ext_vector_type(4)));
typedef __bf16   bf16x8 __attribute__((ext_vector_type(8)));
typedef __bf16   bf16x4 __attribute__((ext_vector_type(4)));
typedef __bf16   bf16x2 __attribute__((ext_vector_type(2)));
typedef unsigned u32x4  __attribute__((ext_vector_type(4)));
typedef unsigned u32x2  __attribute__((ext_vector_type(2)));
typedef short    s16x4  __attribute__((ext_vector_type(4)));
typedef unsigned short u16;
template <int... Is> using iseq = std::integer_sequence<int, Is...>;
template <int N> using make_iseq = std::make_integer_sequence<int, N>;
#define DI __device__ __forceinline__
#define TAILB
#define PACE2
#define SELB

constexpr int SEQ = 2048, DM = 1024, NHEAD = 32, DHEAD = 128, DQ = NHEAD * DHEAD, DFF = 3584, NMOD = 6 * DM;
constexpr size_t WS_EP    = 0;
constexpr size_t WS_RST   = 32768;
constexpr size_t WS_CNT   = 65536;
constexpr size_t WS_MOD   = 131072;
constexpr size_t WS_Z     = 262144;
constexpr size_t WS_MODT  = WS_Z;
#define KEY_REP 8u
#define KSTR 128u
#define RSTR (16u * KSTR)
constexpr size_t WS_KEYS  = WS_Z + 16384;
constexpr unsigned Z_SHARE = 1280;
static_assert(KEY_REP <= 8 && KSTR <= 384 && Z_SHARE * 256 == 327680, "zeroed area");
constexpr size_t WS_PFL   = WS_Z + 65536;
constexpr size_t WS_OFL   = WS_Z + 196608;
constexpr size_t WS_H     = 1048576;
constexpr size_t WS_PART  = 6u << 20;
constexpr size_t WS_ROWS  = 8u << 20;
constexpr size_t WS_XIN   = 10u << 20;
constexpr unsigned BOX    = 65536;
constexpr size_t WS_END   = WS_XIN + (size_t)BOX * (SEQ / 4);
#define POL 0u

DI float bf2f(u16 b) { return __uint_as_float((unsigned)b << 16); }
DI float rbf(float x) { return (float)(__bf16)x; }
DI u16   f2bf(float x) { return __builtin_bit_cast(u16, (__bf16)x); }
DI float lo16(unsigned w) { return __uint_as_float(w << 16); }
DI float hi16(unsigned w) { return __uint_as_float(w & 0xffff0000u); }
DI unsigned cvtpk(float lo, float hi) {
  f32x2 v = {lo, hi}; bf16x2 b = __builtin_convertvector(v, bf16x2); return __builtin_bit_cast(unsigned, b);
}
template <int CTRL> DI float dpp_add(float v) { return v + __uint_as_float(__builtin_amdgcn_update_dpp(0u, __float_as_uint(v), CTRL, 0xf, 0xf, true)); }
DI float swap32_add(float a, float b) {
  auto r = __builtin_amdgcn_permlane32_swap(__float_as_uint(a), __float_as_uint(b), false, false); return __uint_as_float(r[0]) + __uint_as_float(r[1]);
}
DI float swap16_add(float a, float b) {
  auto r = __builtin_amdgcn_permlane16_swap(__float_as_uint(a), __float_as_uint(b), false, false); return __uint_as_float(r[0]) + __uint_as_float(r[1]);
}
constexpr int DPP_XOR1 = 0xB1, DPP_XOR2 = 0x4E, DPP_ROR4 = 0x124, DPP_ROR8 = 0x128;
DI float wave_sum(float v) {
  v = dpp_add<DPP_XOR1>(v); v = dpp_add<DPP_XOR2>(v); v = dpp_add<DPP_ROR4>(v); v = dpp_add<DPP_ROR8>(v);
  v = swap16_add(v, v); return swap32_add(v, v);
}

DI void st16_wt(void* p, u32x4 v) { asm volatile("global_store_dwordx4 %0, %1, off sc1\n\ts_nop 1" :: "v"(p), "v"(v) : "memory"); }

DI f32x2 unpk(unsigned w) { return f32x2{lo16(w), hi16(w)}; }
DI unsigned cvtpk2(f32x2 v) { return __builtin_bit_cast(unsigned, __builtin_convertvector(v, bf16x2)); }
DI float rbh(float v) { return __uint_as_float(cvtpk(0.f, v)); }
DI f32x2 rb2(f32x2 v) { return f32x2{__uint_as_float(cvtpk(0.f, v[0])), __uint_as_float(cvtpk(0.f, v[1]))}; }
DI unsigned ln_pair(float x0, float x1, float m, float rden, float c0, float c1, float s0, float s1) {
  const unsigned d = cvtpk2(f32x2{x0, x1} - m), l = cvtpk2(unpk(d) * rden), t = cvtpk2(unpk(l) * f32x2{c0, c1});
  return cvtpk2(unpk(t) + f32x2{s0, s1});
}
#define GAS __attribute__((address_space(1)))
#define LAS __attribute__((address_space(3)))
typedef GAS unsigned long long gu64;
typedef short v4i16_t __attribute__((ext_vector_type(4)));
DI bf16x4 lds_tr(unsigned a) { return __builtin_bit_cast(bf16x4, __builtin_amdgcn_ds_read_tr16_b64_v4i16((LAS v4i16_t*)a)); }
#define RLX_AGENT __ATOMIC_RELAXED, __HIP_MEMORY_SCOPE_AGENT
constexpr unsigned SPIN_CAP = 1u << 16;

static_assert(BOX == 65536, "box size");
#define NP_FILL 8
#define GOT_WAIT "8"
#define FA_CLOBBERS "v0", "v1", "v2", "v3", "v8", "v9", "v10", "v11", "v16", "v17", "v18", "v19", "v20", "v21", "v22", "v23", "v24", "v25", "v26", "v27", "v28", "v29", "v30", "v31", "v32", "v33", "v34", "v35", "v40", "v41", "v42", "v43", "v48", "v49", "v50", "v51", "v52", "v53", "v54", "v55", "v56", "v57", "v58", "v59", "v60", "v61", "v62", "v63", "v64", "v65", "v66", "v67", "v72", "v73", "v74", "v75", "v80", "v81", "v82", "v83", "v84", "v85", "v86", "v87", "v88", "v89", "v90", "v91", "v92", "v93", "v94", "v95", "v96", "v97", "v98", "v99", "v104", "v105", "v106", "v107", "v112", "v113", "v114", "v115", "v116", "v117", "v118", "v119", "v120", "v121", "v122", "v123", "v124", "v125", "v126", "v127", "v200", "v201", "v202", "v203", "v205", "v207", "v208", "v209", "v210", "v211", "v212", "v213", "v214", "v215", "v216", "v217", "v218", "v219", "v220", "v221", "v222", "v223", "v224", "v230", "v231", "v232", "v233", "v234", "v235", "v237", "a0", "a1", "a2", "a3", "a4", "a5", "a6", "a7", "a8", "a9", "a10", "a11", "a12", "a13", "a14", "a15", "a16", "a17", "a18", "a19", "a20", "a21", "a22", "a23", "a24", "a25", "a26", "a27", "a28", "a29", "a30", "a31", "a32", "a33", "a34", "a35", "a36", "a37", "a38", "a39", "a40", "a41", "a42", "a43", "a44", "a45", "a46", "a47", "a48", "a49", "a50", "a51", "a52", "a53", "a54", "a55", "a56", "a57", "a58", "a59", "a60", "a61", "a62", "a63", "a64", "a65", "a66", "a67", "a68", "a69", "a70", "a71", "a72", "a73", "a74", "a75", "a76", "a77", "a78", "a79", "a80", "a81", "a82", "a83", "a84", "a85", "a86", "a87", "a88", "a89", "a90", "a91", "a92", "a93", "a94", "a95", "a96", "a97", "a98", "a99", "a100", "a101", "a102", "a103", "a104", "a105", "a106", "a107", "a108", "a109", "a110", "a111", "a112", "a113", "a114", "a115", "a116", "a117", "a118", "a119", "a120", "a121", "a122", "a123", "a124", "a125", "a126", "a127", "a128", "a129", "a130", "a131", "a132", "a133", "a134", "a135", "a136", "a137", "a138", "a139", "a140", "a141", "a142", "a143", "a144", "a145", "a146", "a147", "a148", "a149", "a150", "a151", "a152", "a153", "a154", "a155", "a156", "a157", "a158", "a159", "a160", "a161", "a162", "a163", "a164", "a165", "a166", "a167", "a168", "a169", "a170", "a171", "a172", "a173", "a174", "a175", "a176", "a177", "a178", "a179", "a180", "a181", "a182", "a183", "a184", "a185", "a186", "a187", "a188", "a189", "a190", "a191", "a192", "a193", "a194", "a195", "a196", "a197", "a198", "a199", "a200", "a201", "a202", "a203", "a204", "a205", "a206", "a207", "a208", "a209", "a210", "a211", "a212", "a213", "a214", "a215", "a216", "a217", "a218", "a219", "a220", "a221", "a222", "a223", "a224", "a225", "a226", "a227", "a228", "a229", "a230", "a231", "a232", "a233", "a234", "a235", "a236", "a237", "a238", "a239", "a240", "a241", "a242", "a243", "a244", "a245", "a246", "a247", "a248", "a249", "a250", "a251", "a252", "a253", "a254", "a255", "s40", "s41", "s42", "s43", "s44", "s45", "s46", "s47", "s48", "s49", "s50", "s51", "s52", "s53", "s54", "s55", "s56", "s57", "s58", "s59", "s60", "s61", "s62", "s63", "s64", "s65", "s66", "s67", "s68", "s69", "vcc", "m0", "scc", "memory"
#define EARLY2 0
#define SHW 1
#define QROT 0
#define SKV 0
#define KVPAR 0
#define QT2 0
#define QSTAG 0
#define TLATE 0
#define NTV 5
#define BPERM 0
#define NEAR 3
#define PPAR 1
#define FA_OUTS [got] "=&s"(got), "=&{v[128:129]}"(xf[0]), "=&{v[130:131]}"(xf[1]), "=&{v[132:133]}"(xf[2]), "=&{v[134:135]}"(xf[3]), "=&{v[136:137]}"(xf[4]), "=&{v[138:139]}"(xf[5]), "=&{v[140:141]}"(xf[6]), "=&{v[142:143]}"(xf[7]), "=&{v[144:145]}"(xf[8]), "=&{v[146:147]}"(xf[9]), "=&{v[148:149]}"(xf[10]), "=&{v[150:151]}"(xf[11]), "=&{v[152:153]}"(xf[12]), "=&{v[154:155]}"(xf[13]), "=&{v[156:157]}"(xf[14]), "=&{v[158:159]}"(xf[15]), "=&{v[160:163]}"(gaf[0]), "=&{v[164:167]}"(gaf[1]), "=&{v[168:171]}"(gaf[2]), "=&{v[172:175]}"(gaf[3]), "=&{v[176:179]}"(gaf[4]), "=&{v[180:183]}"(gaf[5]), "=&{v[184:187]}"(gaf[6]), "=&{v[188:191]}"(gaf[7]), "=&{v[4:7]}"(aw[0]), "=&{v[12:15]}"(aw[1]), "=&{v[36:39]}"(aw[2]), "=&{v[44:47]}"(aw[3]), "=&{v[68:71]}"(aw[4]), "=&{v[76:79]}"(aw[5]), "=&{v[100:103]}"(aw[6]), "=&{v[108:111]}"(aw[7]), "=&{v[192:195]}"(alo), "=&{v[196:199]}"(ahi), "=&{v204}"(late), "=&{v206}"(arow), "+{v229}"(fl), "+{v225}"(vwb), "+{v226}"(wd0), "+{v227}"(hq0), "+{v228}"(sd0), "+{v236}"(tq0)
#define FA_BODY \
"s_branch adaln_body\n" \
".p2align 12\n" \
"adaln_pg7:\n" \
"adaln_body:\n" \
"s_cbranch_execz adaln_fr\n" \
"s_nop 0\n" \
"s_mov_b32 s55, 0x80000\n" \
"v_add_u32_e32 v219, 0x8000, v227\n" \
"v_add_u32_e32 v220, 0x10000, v227\n" \
"v_add_u32_e32 v221, 0x18000, v227\n" \
"s_mov_b32 s50, 0\n" \
"s_movk_i32 s53, 0x1000\n" \
"s_mov_b32 s52, 0\n" \
"s_nop 0\n" \
"s_mov_b32 s49, 1572864\n" \
"v_add_u32_e32 v216, 0xfffffc80, v226\n" \
"v_add_u32_e32 v217, 0xf800, v226\n" \
"v_add_u32_e32 v218, 0xf480, v226\n" \
"s_add_u32 s40, %[m0b], 0\n" \
"s_nop 0\n" \
"s_add_u32 s44, %[m0b], 65536\n" \
"s_add_u32 s41, %[m0b], 16384\n" \
"s_add_u32 s45, %[m0b], 81920\n" \
"s_add_u32 s42, %[m0b], 32768\n" \
"s_add_u32 s46, %[m0b], 98304\n" \
"s_add_u32 s43, %[m0b], 49152\n" \
"s_add_u32 s47, %[m0b], 114688\n" \
"v_xor_b32_e64 v208, 64, %[kb0]\n" \
"v_xor_b32_e32 v209, 0x80, %[kb0]\n" \
"v_xor_b32_e32 v210, 0xc0, %[kb0]\n" \
"v_xor_b32_e64 v212, 64, %[kd0]\n" \
"v_xor_b32_e32 v213, 0x80, %[kd0]\n" \
"v_xor_b32_e32 v214, 0xc0, %[kd0]\n" \
"v_xor_b32_e64 v211, 32, %[vvb]\n" \
"v_add_u32_e32 v215, 0xfffffc80, %[vd0]\n" \
"v_readlane_b32 s64, %[tlo], 17\n" \
"v_readlane_b32 s65, %[thi], 17\n" \
"s_mov_b32 s66, 12582912\n" \
"s_mov_b32 s67, 0x20000\n" \
"v_mov_b64_e32 v[0:1], 0\n" \
"v_mov_b64_e32 v[2:3], 0\n" \
"v_mov_b64_e32 v[4:5], 0\n" \
"v_mov_b64_e32 v[6:7], 0\n" \
"v_mov_b64_e32 v[8:9], 0\n" \
"v_mov_b64_e32 v[10:11], 0\n" \
"v_mov_b64_e32 v[12:13], 0\n" \
"v_mov_b64_e32 v[14:15], 0\n" \
"v_mov_b64_e32 v[16:17], 0\n" \
"v_mov_b64_e32 v[18:19], 0\n" \
"v_mov_b64_e32 v[20:21], 0\n" \
"v_mov_b64_e32 v[22:23], 0\n" \
"v_mov_b64_e32 v[24:25], 0\n" \
"v_mov_b64_e32 v[26:27], 0\n" \
"v_mov_b64_e32 v[28:29], 0\n" \
"v_mov_b64_e32 v[30:31], 0\n" \
"v_mov_b64_e32 v[32:33], 0\n" \
"v_mov_b64_e32 v[34:35], 0\n" \
"v_mov_b64_e32 v[36:37], 0\n" \
"v_mov_b64_e32 v[38:39], 0\n" \
"v_mov_b64_e32 v[40:41], 0\n" \
"v_mov_b64_e32 v[42:43], 0\n" \
"v_mov_b64_e32 v[44:45], 0\n" \
"v_mov_b64_e32 v[46:47], 0\n" \
"v_mov_b64_e32 v[48:49], 0\n" \
"v_mov_b64_e32 v[50:51], 0\n" \
"v_mov_b64_e32 v[52:53], 0\n" \
"v_mov_b64_e32 v[54:55], 0\n" \
"v_mov_b64_e32 v[56:57], 0\n" \
"v_mov_b64_e32 v[58:59], 0\n" \
"v_mov_b64_e32 v[60:61], 0\n" \
"v_mov_b64_e32 v[62:63], 0\n" \
"v_mov_b64_e32 v[64:65], 0\n" \
"v_mov_b64_e32 v[66:67], 0\n" \
"v_mov_b64_e32 v[68:69], 0\n" \
"v_mov_b64_e32 v[70:71], 0\n" \
"v_mov_b64_e32 v[72:73], 0\n" \
"v_mov_b64_e32 v[74:75], 0\n" \
"v_mov_b64_e32 v[76:77], 0\n" \
"v_mov_b64_e32 v[78:79], 0\n" \
"v_mov_b64_e32 v[80:81], 0\n" \
"v_mov_b64_e32 v[82:83], 0\n" \
"v_mov_b64_e32 v[84:85], 0\n" \
"v_mov_b64_e32 v[86:87], 0\n" \
"v_mov_b64_e32 v[88:89], 0\n" \
"v_mov_b64_e32 v[90:91], 0\n" \
"v_mov_b64_e32 v[92:93], 0\n" \
"v_mov_b64_e32 v[94:95], 0\n" \
"v_mov_b64_e32 v[96:97], 0\n" \
"v_mov_b64_e32 v[98:99], 0\n" \
"v_mov_b64_e32 v[100:101], 0\n" \
"v_mov_b64_e32 v[102:103], 0\n" \
"v_mov_b64_e32 v[104:105], 0\n" \
"v_mov_b64_e32 v[106:107], 0\n" \
"v_mov_b64_e32 v[108:109], 0\n" \
"v_mov_b64_e32 v[110:111], 0\n" \
"v_mov_b64_e32 v[112:113], 0\n" \
"v_mov_b64_e32 v[114:115], 0\n" \
"v_mov_b64_e32 v[116:117], 0\n" \
"v_mov_b64_e32 v[118:119], 0\n" \
"v_mov_b64_e32 v[120:121], 0\n" \
"v_mov_b64_e32 v[122:123], 0\n" \
"v_mov_b64_e32 v[124:125], 0\n" \
"v_mov_b64_e32 v[126:127], 0\n" \
"s_mov_b32 s51, 0x10000\n" \
"v_mbcnt_lo_u32_b32 v232, -1, 0\n" \
"v_mov_b64_e32 v[234:235], %[tag]\n" \
"v_lshlrev_b32_e32 v233, 3, v232\n" \
"v_lshlrev_b32_e32 v232, 8, v232\n" \
"s_waitcnt vmcnt(0)\n" \
"s_barrier\n" \
"s_cmp_lg_u32 %[wid], 0\n" \
"s_cbranch_scc1 Lwalk_%=\n" \
"s_mov_b32 exec_hi, 0\n" \
"global_store_dwordx2 v232, v[234:235], %[psig] sc1\n" \
"s_mov_b32 exec_hi, -1\n" \
"s_nop 0\n" \
"v_readlane_b32 s62, %[tlo], 13\n" \
"s_nop 3\n" \
"s_cmpk_eq_u32 s62, 252\n" \
"s_cbranch_scc1 Lprobe_%=\n" \
"Lgo_%=:\n" \
"s_nop 0\n" \
"ds_read_b64_tr_b16 v[128:129], v225 offset:0\n" \
"ds_read_b64_tr_b16 v[130:131], v225 offset:256\n" \
"ds_read_b64_tr_b16 v[132:133], v225 offset:8192\n" \
"ds_read_b64_tr_b16 v[134:135], v225 offset:8448\n" \
"ds_read_b64_tr_b16 v[136:137], v225 offset:8\n" \
"ds_read_b64_tr_b16 v[138:139], v225 offset:264\n" \
"ds_read_b64_tr_b16 v[140:141], v225 offset:8200\n" \
"ds_read_b64_tr_b16 v[142:143], v225 offset:8456\n" \
"ds_read_b64_tr_b16 v[144:145], v225 offset:512\n" \
"ds_read_b64_tr_b16 v[146:147], v225 offset:768\n" \
"ds_read_b64_tr_b16 v[148:149], v225 offset:8704\n" \
"ds_read_b64_tr_b16 v[150:151], v225 offset:8960\n" \
"ds_read_b64_tr_b16 v[152:153], v225 offset:520\n" \
"ds_read_b64_tr_b16 v[154:155], v225 offset:776\n" \
"ds_read_b64_tr_b16 v[156:157], v225 offset:8712\n" \
"ds_read_b64_tr_b16 v[158:159], v225 offset:8968\n" \
"s_mov_b32 s60, 4194304\n" \
"v_and_b32_e32 v233, 56, v233\n" \
"s_lshl_b32 s61, %[wid], 6\n" \
"v_add_u32_e64 v233, s61, v233\n" \
"Lpoll_%=:\n" \
"global_load_dwordx2 v[230:231], v233, %[pcnt] sc1\n" \
"s_waitcnt vmcnt(0)\n" \
"v_cmp_ne_u64_e32 vcc, v[230:231], v[234:235]\n" \
"s_sub_u32 s51, s51, 1\n" \
"s_cbranch_scc1 Lpd_%=\n" \
"s_nop 3\n" \
"s_cbranch_vccnz Lpoll_%=\n" \
"Lpd_%=:\n" \
"buffer_load_dwordx4 a[128:131], v227, %[srdz], s50 offen offset:0 sc1\n" \
"buffer_load_dwordx4 a[132:135], v219, %[srdz], s50 offen offset:0 sc1\n" \
"buffer_load_dwordx4 a[136:139], v220, %[srdz], s50 offen offset:0 sc1\n" \
"buffer_load_dwordx4 a[140:143], v221, %[srdz], s50 offen offset:0 sc1\n" \
"buffer_load_dwordx4 a[144:147], v227, %[srdz], s50 offen offset:1024 sc1\n" \
"buffer_load_dwordx4 a[148:151], v219, %[srdz], s50 offen offset:1024 sc1\n" \
"buffer_load_dwordx4 a[152:155], v220, %[srdz], s50 offen offset:1024 sc1\n" \
"buffer_load_dwordx4 a[156:159], v221, %[srdz], s50 offen offset:1024 sc1\n" \
"buffer_load_dwordx4 a[160:163], v227, %[srdz], s50 offen offset:2048 sc1\n" \
"buffer_load_dwordx4 a[164:167], v219, %[srdz], s50 offen offset:2048 sc1\n" \
"buffer_load_dwordx4 a[168:171], v220, %[srdz], s50 offen offset:2048 sc1\n" \
"buffer_load_dwordx4 a[172:175], v221, %[srdz], s50 offen offset:2048 sc1\n" \
"buffer_load_dwordx4 a[176:179], v227, %[srdz], s50 offen offset:3072 sc1\n" \
"buffer_load_dwordx4 a[180:183], v219, %[srdz], s50 offen offset:3072 sc1\n" \
"buffer_load_dwordx4 a[184:187], v220, %[srdz], s50 offen offset:3072 sc1\n" \
"buffer_load_dwordx4 a[188:191], v221, %[srdz], s50 offen offset:3072 sc1\n" \
"s_add_u32 s50, s50, s53\n" \
"s_nop 0\n" \
"buffer_load_dwordx4 a[192:195], v227, %[srdz], s50 offen offset:0 sc1\n" \
"buffer_load_dwordx4 a[196:199], v219, %[srdz], s50 offen offset:0 sc1\n" \
"buffer_load_dwordx4 a[200:203], v220, %[srdz], s50 offen offset:0 sc1\n" \
"buffer_load_dwordx4 a[204:207], v221, %[srdz], s50 offen offset:0 sc1\n" \
"buffer_load_dwordx4 a[208:211], v227, %[srdz], s50 offen offset:1024 sc1\n" \
"buffer_load_dwordx4 a[212:215], v219, %[srdz], s50 offen offset:1024 sc1\n" \
"buffer_load_dwordx4 a[216:219], v220, %[srdz], s50 offen offset:1024 sc1\n" \
"buffer_load_dwordx4 a[220:223], v221, %[srdz], s50 offen offset:1024 sc1\n" \
"s_mov_b32 s51, 3\n" \
"s_waitcnt vmcnt(16) lgkmcnt(0)\n" \
"Lq_%=:\n" \
"v_mfma_f32_16x16x32_bf16 v[0:3], v[128:131], a[128:131], v[0:3]\n" \
"ds_read_b64_tr_b16 v[160:161], v225 offset:1024\n" \
"v_mfma_f32_16x16x32_bf16 v[0:3], v[132:135], a[144:147], v[0:3]\n" \
"ds_read_b64_tr_b16 v[162:163], v225 offset:1280\n" \
"v_mfma_f32_16x16x32_bf16 v[32:35], v[128:131], a[132:135], v[32:35]\n" \
"s_mov_b32 m0, s47\n" \
"s_nop 0\n" \
"buffer_load_dwordx4 v217, %[srdw], s49 offen offset:2048 lds\n" \
"v_mfma_f32_16x16x32_bf16 v[32:35], v[132:135], a[148:151], v[32:35]\n" \
"ds_read_b64_tr_b16 v[164:165], v225 offset:9216\n" \
"v_mfma_f32_16x16x32_bf16 v[64:67], v[128:131], a[136:139], v[64:67]\n" \
"ds_read_b64_tr_b16 v[166:167], v225 offset:9472\n" \
"v_mfma_f32_16x16x32_bf16 v[64:67], v[132:135], a[152:155], v[64:67]\n" \
"v_mfma_f32_16x16x32_bf16 v[96:99], v[128:131], a[140:143], v[96:99]\n" \
"ds_read_b64_tr_b16 v[168:169], v225 offset:1032\n" \
"v_mfma_f32_16x16x32_bf16 v[96:99], v[132:135], a[156:159], v[96:99]\n" \
"ds_read_b64_tr_b16 v[170:171], v225 offset:1288\n" \
"v_mfma_f32_16x16x32_bf16 v[4:7], v[136:139], a[128:131], v[4:7]\n" \
"buffer_load_dwordx4 v218, %[srdw], s49 offen offset:3072 lds\n" \
"v_mfma_f32_16x16x32_bf16 v[4:7], v[140:143], a[144:147], v[4:7]\n" \
"ds_read_b64_tr_b16 v[172:173], v225 offset:9224\n" \
"v_mfma_f32_16x16x32_bf16 v[36:39], v[136:139], a[132:135], v[36:39]\n" \
"ds_read_b64_tr_b16 v[174:175], v225 offset:9480\n" \
"v_mfma_f32_16x16x32_bf16 v[36:39], v[140:143], a[148:151], v[36:39]\n" \
"v_mfma_f32_16x16x32_bf16 v[68:71], v[136:139], a[136:139], v[68:71]\n" \
"ds_read_b64_tr_b16 v[176:177], v225 offset:1536\n" \
"v_mfma_f32_16x16x32_bf16 v[68:71], v[140:143], a[152:155], v[68:71]\n" \
"ds_read_b64_tr_b16 v[178:179], v225 offset:1792\n" \
"v_mfma_f32_16x16x32_bf16 v[100:103], v[136:139], a[140:143], v[100:103]\n" \
"buffer_load_dwordx4 a[224:227], v227, %[srdz], s50 offen offset:2048 sc1\n" \
"v_mfma_f32_16x16x32_bf16 v[100:103], v[140:143], a[156:159], v[100:103]\n" \
"ds_read_b64_tr_b16 v[180:181], v225 offset:9728\n" \
"v_mfma_f32_16x16x32_bf16 v[8:11], v[144:147], a[128:131], v[8:11]\n" \
"ds_read_b64_tr_b16 v[182:183], v225 offset:9984\n" \
"v_mfma_f32_16x16x32_bf16 v[8:11], v[148:151], a[144:147], v[8:11]\n" \
"v_mfma_f32_16x16x32_bf16 v[40:43], v[144:147], a[132:135], v[40:43]\n" \
"ds_read_b64_tr_b16 v[184:185], v225 offset:1544\n" \
"v_mfma_f32_16x16x32_bf16 v[40:43], v[148:151], a[148:151], v[40:43]\n" \
"ds_read_b64_tr_b16 v[186:187], v225 offset:1800\n" \
"v_mfma_f32_16x16x32_bf16 v[72:75], v[144:147], a[136:139], v[72:75]\n" \
"buffer_load_dwordx4 a[228:231], v219, %[srdz], s50 offen offset:2048 sc1\n" \
"v_mfma_f32_16x16x32_bf16 v[72:75], v[148:151], a[152:155], v[72:75]\n" \
"ds_read_b64_tr_b16 v[188:189], v225 offset:9736\n" \
"v_mfma_f32_16x16x32_bf16 v[104:107], v[144:147], a[140:143], v[104:107]\n" \
"ds_read_b64_tr_b16 v[190:191], v225 offset:9992\n" \
"v_mfma_f32_16x16x32_bf16 v[104:107], v[148:151], a[156:159], v[104:107]\n" \
"v_mfma_f32_16x16x32_bf16 v[12:15], v[152:155], a[128:131], v[12:15]\n" \
"buffer_load_dwordx4 a[232:235], v220, %[srdz], s50 offen offset:2048 sc1\n" \
"v_mfma_f32_16x16x32_bf16 v[12:15], v[156:159], a[144:147], v[12:15]\n" \
"v_mfma_f32_16x16x32_bf16 v[44:47], v[152:155], a[132:135], v[44:47]\n" \
"v_mfma_f32_16x16x32_bf16 v[44:47], v[156:159], a[148:151], v[44:47]\n" \
"v_mfma_f32_16x16x32_bf16 v[76:79], v[152:155], a[136:139], v[76:79]\n" \
"buffer_load_dwordx4 a[236:239], v221, %[srdz], s50 offen offset:2048 sc1\n" \
"v_mfma_f32_16x16x32_bf16 v[76:79], v[156:159], a[152:155], v[76:79]\n" \
"v_mfma_f32_16x16x32_bf16 v[108:111], v[152:155], a[140:143], v[108:111]\n" \
"v_mfma_f32_16x16x32_bf16 v[108:111], v[156:159], a[156:159], v[108:111]\n" \
"s_waitcnt vmcnt(29) lgkmcnt(0)\n" \
"s_barrier\n" \
"v_mfma_f32_16x16x32_bf16 v[16:19], v[160:163], a[128:131], v[16:19]\n" \
"ds_read_b64_tr_b16 v[128:129], v225 offset:16384\n" \
"v_mfma_f32_16x16x32_bf16 v[16:19], v[164:167], a[144:147], v[16:19]\n" \
"ds_read_b64_tr_b16 v[130:131], v225 offset:16640\n" \
"v_mfma_f32_16x16x32_bf16 v[48:51], v[160:163], a[132:135], v[48:51]\n" \
"s_add_u32 s49, s49, s55\n" \
"s_mov_b32 m0, s44\n" \
"s_nop 0\n" \
"buffer_load_dwordx4 v226, %[srdw], s49 offen offset:0 lds\n" \
"v_mfma_f32_16x16x32_bf16 v[48:51], v[164:167], a[148:151], v[48:51]\n" \
"ds_read_b64_tr_b16 v[132:133], v225 offset:24576\n" \
"v_mfma_f32_16x16x32_bf16 v[80:83], v[160:163], a[136:139], v[80:83]\n" \
"ds_read_b64_tr_b16 v[134:135], v225 offset:24832\n" \
"v_mfma_f32_16x16x32_bf16 v[80:83], v[164:167], a[152:155], v[80:83]\n" \
"v_mfma_f32_16x16x32_bf16 v[112:115], v[160:163], a[140:143], v[112:115]\n" \
"ds_read_b64_tr_b16 v[136:137], v225 offset:16392\n" \
"v_mfma_f32_16x16x32_bf16 v[112:115], v[164:167], a[156:159], v[112:115]\n" \
"ds_read_b64_tr_b16 v[138:139], v225 offset:16648\n" \
"v_mfma_f32_16x16x32_bf16 v[20:23], v[168:171], a[128:131], v[20:23]\n" \
"buffer_load_dwordx4 v216, %[srdw], s49 offen offset:1024 lds\n" \
"v_mfma_f32_16x16x32_bf16 v[20:23], v[172:175], a[144:147], v[20:23]\n" \
"ds_read_b64_tr_b16 v[140:141], v225 offset:24584\n" \
"v_mfma_f32_16x16x32_bf16 v[52:55], v[168:171], a[132:135], v[52:55]\n" \
"ds_read_b64_tr_b16 v[142:143], v225 offset:24840\n" \
"v_mfma_f32_16x16x32_bf16 v[52:55], v[172:175], a[148:151], v[52:55]\n" \
"v_mfma_f32_16x16x32_bf16 v[84:87], v[168:171], a[136:139], v[84:87]\n" \
"ds_read_b64_tr_b16 v[144:145], v225 offset:16896\n" \
"v_mfma_f32_16x16x32_bf16 v[84:87], v[172:175], a[152:155], v[84:87]\n" \
"ds_read_b64_tr_b16 v[146:147], v225 offset:17152\n" \
"v_mfma_f32_16x16x32_bf16 v[116:119], v[168:171], a[140:143], v[116:119]\n" \
"buffer_load_dwordx4 a[240:243], v227, %[srdz], s50 offen offset:3072 sc1\n" \
"v_mfma_f32_16x16x32_bf16 v[116:119], v[172:175], a[156:159], v[116:119]\n" \
"ds_read_b64_tr_b16 v[148:149], v225 offset:25088\n" \
"v_mfma_f32_16x16x32_bf16 v[24:27], v[176:179], a[128:131], v[24:27]\n" \
"ds_read_b64_tr_b16 v[150:151], v225 offset:25344\n" \
"v_mfma_f32_16x16x32_bf16 v[24:27], v[180:183], a[144:147], v[24:27]\n" \
"v_mfma_f32_16x16x32_bf16 v[56:59], v[176:179], a[132:135], v[56:59]\n" \
"ds_read_b64_tr_b16 v[152:153], v225 offset:16904\n" \
"v_mfma_f32_16x16x32_bf16 v[56:59], v[180:183], a[148:151], v[56:59]\n" \
"ds_read_b64_tr_b16 v[154:155], v225 offset:17160\n" \
"v_mfma_f32_16x16x32_bf16 v[88:91], v[176:179], a[136:139], v[88:91]\n" \
"buffer_load_dwordx4 a[244:247], v219, %[srdz], s50 offen offset:3072 sc1\n" \
"v_mfma_f32_16x16x32_bf16 v[88:91], v[180:183], a[152:155], v[88:91]\n" \
"ds_read_b64_tr_b16 v[156:157], v225 offset:25096\n" \
"v_mfma_f32_16x16x32_bf16 v[120:123], v[176:179], a[140:143], v[120:123]\n" \
"ds_read_b64_tr_b16 v[158:159], v225 offset:25352\n" \
"v_mfma_f32_16x16x32_bf16 v[120:123], v[180:183], a[156:159], v[120:123]\n" \
"v_mfma_f32_16x16x32_bf16 v[28:31], v[184:187], a[128:131], v[28:31]\n" \
"buffer_load_dwordx4 a[248:251], v220, %[srdz], s50 offen offset:3072 sc1\n" \
"v_mfma_f32_16x16x32_bf16 v[28:31], v[188:191], a[144:147], v[28:31]\n" \
"v_mfma_f32_16x16x32_bf16 v[60:63], v[184:187], a[132:135], v[60:63]\n" \
"v_mfma_f32_16x16x32_bf16 v[60:63], v[188:191], a[148:151], v[60:63]\n" \
"v_mfma_f32_16x16x32_bf16 v[92:95], v[184:187], a[136:139], v[92:95]\n" \
"buffer_load_dwordx4 a[252:255], v221, %[srdz], s50 offen offset:3072 sc1\n" \
"v_mfma_f32_16x16x32_bf16 v[92:95], v[188:191], a[152:155], v[92:95]\n" \
"v_mfma_f32_16x16x32_bf16 v[124:127], v[184:187], a[140:143], v[124:127]\n" \
"v_mfma_f32_16x16x32_bf16 v[124:127], v[188:191], a[156:159], v[124:127]\n" \
"s_waitcnt vmcnt(20) lgkmcnt(0)\n" \
"v_mfma_f32_16x16x32_bf16 v[0:3], v[128:131], a[160:163], v[0:3]\n" \
"ds_read_b64_tr_b16 v[160:161], v225 offset:17408\n" \
"v_mfma_f32_16x16x32_bf16 v[0:3], v[132:135], a[176:179], v[0:3]\n" \
"ds_read_b64_tr_b16 v[162:163], v225 offset:17664\n" \
"v_mfma_f32_16x16x32_bf16 v[32:35], v[128:131], a[164:167], v[32:35]\n" \
"s_mov_b32 m0, s44\n" \
"s_nop 0\n" \
"buffer_load_dwordx4 v217, %[srdw], s49 offen offset:2048 lds\n" \
"v_mfma_f32_16x16x32_bf16 v[32:35], v[132:135], a[180:183], v[32:35]\n" \
"ds_read_b64_tr_b16 v[164:165], v225 offset:25600\n" \
"v_mfma_f32_16x16x32_bf16 v[64:67], v[128:131], a[168:171], v[64:67]\n" \
"ds_read_b64_tr_b16 v[166:167], v225 offset:25856\n" \
"v_mfma_f32_16x16x32_bf16 v[64:67], v[132:135], a[184:187], v[64:67]\n" \
"v_mfma_f32_16x16x32_bf16 v[96:99], v[128:131], a[172:175], v[96:99]\n" \
"ds_read_b64_tr_b16 v[168:169], v225 offset:17416\n" \
"v_mfma_f32_16x16x32_bf16 v[96:99], v[132:135], a[188:191], v[96:99]\n" \
"ds_read_b64_tr_b16 v[170:171], v225 offset:17672\n" \
"v_mfma_f32_16x16x32_bf16 v[4:7], v[136:139], a[160:163], v[4:7]\n" \
"buffer_load_dwordx4 v218, %[srdw], s49 offen offset:3072 lds\n" \
"v_mfma_f32_16x16x32_bf16 v[4:7], v[140:143], a[176:179], v[4:7]\n" \
"ds_read_b64_tr_b16 v[172:173], v225 offset:25608\n" \
"v_mfma_f32_16x16x32_bf16 v[36:39], v[136:139], a[164:167], v[36:39]\n" \
"ds_read_b64_tr_b16 v[174:175], v225 offset:25864\n" \
"v_mfma_f32_16x16x32_bf16 v[36:39], v[140:143], a[180:183], v[36:39]\n" \
"v_mfma_f32_16x16x32_bf16 v[68:71], v[136:139], a[168:171], v[68:71]\n" \
"ds_read_b64_tr_b16 v[176:177], v225 offset:17920\n" \
"v_mfma_f32_16x16x32_bf16 v[68:71], v[140:143], a[184:187], v[68:71]\n" \
"ds_read_b64_tr_b16 v[178:179], v225 offset:18176\n" \
"v_mfma_f32_16x16x32_bf16 v[100:103], v[136:139], a[172:175], v[100:103]\n" \
"s_add_u32 s50, s50, s53\n" \
"buffer_load_dwordx4 a[128:131], v227, %[srdz], s50 offen offset:0 sc1\n" \
"v_mfma_f32_16x16x32_bf16 v[100:103], v[140:143], a[188:191], v[100:103]\n" \
"ds_read_b64_tr_b16 v[180:181], v225 offset:26112\n" \
"v_mfma_f32_16x16x32_bf16 v[8:11], v[144:147], a[160:163], v[8:11]\n" \
"ds_read_b64_tr_b16 v[182:183], v225 offset:26368\n" \
"v_mfma_f32_16x16x32_bf16 v[8:11], v[148:151], a[176:179], v[8:11]\n" \
"v_mfma_f32_16x16x32_bf16 v[40:43], v[144:147], a[164:167], v[40:43]\n" \
"ds_read_b64_tr_b16 v[184:185], v225 offset:17928\n" \
"v_mfma_f32_16x16x32_bf16 v[40:43], v[148:151], a[180:183], v[40:43]\n" \
"ds_read_b64_tr_b16 v[186:187], v225 offset:18184\n" \
"v_mfma_f32_16x16x32_bf16 v[72:75], v[144:147], a[168:171], v[72:75]\n" \
"buffer_load_dwordx4 a[132:135], v219, %[srdz], s50 offen offset:0 sc1\n" \
"v_mfma_f32_16x16x32_bf16 v[72:75], v[148:151], a[184:187], v[72:75]\n" \
"ds_read_b64_tr_b16 v[188:189], v225 offset:26120\n" \
"v_mfma_f32_16x16x32_bf16 v[104:107], v[144:147], a[172:175], v[104:107]\n" \
"ds_read_b64_tr_b16 v[190:191], v225 offset:26376\n" \
"v_mfma_f32_16x16x32_bf16 v[104:107], v[148:151], a[188:191], v[104:107]\n" \
"v_mfma_f32_16x16x32_bf16 v[12:15], v[152:155], a[160:163], v[12:15]\n" \
"buffer_load_dwordx4 a[136:139], v220, %[srdz], s50 offen offset:0 sc1\n" \
"v_mfma_f32_16x16x32_bf16 v[12:15], v[156:159], a[176:179], v[12:15]\n" \
"v_mfma_f32_16x16x32_bf16 v[44:47], v[152:155], a[164:167], v[44:47]\n" \
"v_mfma_f32_16x16x32_bf16 v[44:47], v[156:159], a[180:183], v[44:47]\n" \
"v_mfma_f32_16x16x32_bf16 v[76:79], v[152:155], a[168:171], v[76:79]\n" \
"buffer_load_dwordx4 a[140:143], v221, %[srdz], s50 offen offset:0 sc1\n" \
"v_mfma_f32_16x16x32_bf16 v[76:79], v[156:159], a[184:187], v[76:79]\n" \
"v_mfma_f32_16x16x32_bf16 v[108:111], v[152:155], a[172:175], v[108:111]\n" \
"v_mfma_f32_16x16x32_bf16 v[108:111], v[156:159], a[188:191], v[108:111]\n" \
"s_waitcnt vmcnt(29) lgkmcnt(0)\n" \
"s_barrier\n" \
"v_mfma_f32_16x16x32_bf16 v[16:19], v[160:163], a[160:163], v[16:19]\n" \
"ds_read_b64_tr_b16 v[128:129], v225 offset:32768\n" \
"v_mfma_f32_16x16x32_bf16 v[16:19], v[164:167], a[176:179], v[16:19]\n" \
"ds_read_b64_tr_b16 v[130:131], v225 offset:33024\n" \
"v_mfma_f32_16x16x32_bf16 v[48:51], v[160:163], a[164:167], v[48:51]\n" \
"s_add_u32 s49, s49, s55\n" \
"s_mov_b32 m0, s45\n" \
"s_nop 0\n" \
"buffer_load_dwordx4 v226, %[srdw], s49 offen offset:0 lds\n" \
"v_mfma_f32_16x16x32_bf16 v[48:51], v[164:167], a[180:183], v[48:51]\n" \
"ds_read_b64_tr_b16 v[132:133], v225 offset:40960\n" \
"v_mfma_f32_16x16x32_bf16 v[80:83], v[160:163], a[168:171], v[80:83]\n" \
"ds_read_b64_tr_b16 v[134:135], v225 offset:41216\n" \
"v_mfma_f32_16x16x32_bf16 v[80:83], v[164:167], a[184:187], v[80:83]\n" \
"v_mfma_f32_16x16x32_bf16 v[112:115], v[160:163], a[172:175], v[112:115]\n" \
"ds_read_b64_tr_b16 v[136:137], v225 offset:32776\n" \
"v_mfma_f32_16x16x32_bf16 v[112:115], v[164:167], a[188:191], v[112:115]\n" \
"ds_read_b64_tr_b16 v[138:139], v225 offset:33032\n" \
"v_mfma_f32_16x16x32_bf16 v[20:23], v[168:171], a[160:163], v[20:23]\n" \
"buffer_load_dwordx4 v216, %[srdw], s49 offen offset:1024 lds\n" \
"v_mfma_f32_16x16x32_bf16 v[20:23], v[172:175], a[176:179], v[20:23]\n" \
"ds_read_b64_tr_b16 v[140:141], v225 offset:40968\n" \
"v_mfma_f32_16x16x32_bf16 v[52:55], v[168:171], a[164:167], v[52:55]\n" \
"ds_read_b64_tr_b16 v[142:143], v225 offset:41224\n" \
"v_mfma_f32_16x16x32_bf16 v[52:55], v[172:175], a[180:183], v[52:55]\n" \
"v_mfma_f32_16x16x32_bf16 v[84:87], v[168:171], a[168:171], v[84:87]\n" \
"ds_read_b64_tr_b16 v[144:145], v225 offset:33280\n" \
"v_mfma_f32_16x16x32_bf16 v[84:87], v[172:175], a[184:187], v[84:87]\n" \
"ds_read_b64_tr_b16 v[146:147], v225 offset:33536\n" \
"v_mfma_f32_16x16x32_bf16 v[116:119], v[168:171], a[172:175], v[116:119]\n" \
"buffer_load_dwordx4 a[144:147], v227, %[srdz], s50 offen offset:1024 sc1\n" \
"v_mfma_f32_16x16x32_bf16 v[116:119], v[172:175], a[188:191], v[116:119]\n" \
"ds_read_b64_tr_b16 v[148:149], v225 offset:41472\n" \
"v_mfma_f32_16x16x32_bf16 v[24:27], v[176:179], a[160:163], v[24:27]\n" \
"ds_read_b64_tr_b16 v[150:151], v225 offset:41728\n" \
"v_mfma_f32_16x16x32_bf16 v[24:27], v[180:183], a[176:179], v[24:27]\n" \
"v_mfma_f32_16x16x32_bf16 v[56:59], v[176:179], a[164:167], v[56:59]\n" \
"ds_read_b64_tr_b16 v[152:153], v225 offset:33288\n" \
"v_mfma_f32_16x16x32_bf16 v[56:59], v[180:183], a[180:183], v[56:59]\n" \
"ds_read_b64_tr_b16 v[154:155], v225 offset:33544\n" \
"v_mfma_f32_16x16x32_bf16 v[88:91], v[176:179], a[168:171], v[88:91]\n" \
"buffer_load_dwordx4 a[148:151], v219, %[srdz], s50 offen offset:1024 sc1\n" \
"v_mfma_f32_16x16x32_bf16 v[88:91], v[180:183], a[184:187], v[88:91]\n" \
"ds_read_b64_tr_b16 v[156:157], v225 offset:41480\n" \
"v_mfma_f32_16x16x32_bf16 v[120:123], v[176:179], a[172:175], v[120:123]\n" \
"ds_read_b64_tr_b16 v[158:159], v225 offset:41736\n" \
"v_mfma_f32_16x16x32_bf16 v[120:123], v[180:183], a[188:191], v[120:123]\n" \
"v_mfma_f32_16x16x32_bf16 v[28:31], v[184:187], a[160:163], v[28:31]\n" \
"buffer_load_dwordx4 a[152:155], v220, %[srdz], s50 offen offset:1024 sc1\n" \
"v_mfma_f32_16x16x32_bf16 v[28:31], v[188:191], a[176:179], v[28:31]\n" \
"v_mfma_f32_16x16x32_bf16 v[60:63], v[184:187], a[164:167], v[60:63]\n" \
"v_mfma_f32_16x16x32_bf16 v[60:63], v[188:191], a[180:183], v[60:63]\n" \
"v_mfma_f32_16x16x32_bf16 v[92:95], v[184:187], a[168:171], v[92:95]\n" \
"buffer_load_dwordx4 a[156:159], v221, %[srdz], s50 offen offset:1024 sc1\n" \
"v_mfma_f32_16x16x32_bf16 v[92:95], v[188:191], a[184:187], v[92:95]\n" \
"v_mfma_f32_16x16x32_bf16 v[124:127], v[184:187], a[172:175], v[124:127]\n" \
"v_mfma_f32_16x16x32_bf16 v[124:127], v[188:191], a[188:191], v[124:127]\n" \
"s_waitcnt vmcnt(24) lgkmcnt(0)\n" \
"v_mfma_f32_16x16x32_bf16 v[0:3], v[128:131], a[192:195], v[0:3]\n" \
"ds_read_b64_tr_b16 v[160:161], v225 offset:33792\n" \
"v_mfma_f32_16x16x32_bf16 v[0:3], v[132:135], a[208:211], v[0:3]\n" \
"ds_read_b64_tr_b16 v[162:163], v225 offset:34048\n" \
"v_mfma_f32_16x16x32_bf16 v[32:35], v[128:131], a[196:199], v[32:35]\n" \
"s_mov_b32 m0, s45\n" \
"s_nop 0\n" \
"buffer_load_dwordx4 v217, %[srdw], s49 offen offset:2048 lds\n" \
"v_mfma_f32_16x16x32_bf16 v[32:35], v[132:135], a[212:215], v[32:35]\n" \
"ds_read_b64_tr_b16 v[164:165], v225 offset:41984\n" \
"v_mfma_f32_16x16x32_bf16 v[64:67], v[128:131], a[200:203], v[64:67]\n" \
"ds_read_b64_tr_b16 v[166:167], v225 offset:42240\n" \
"v_mfma_f32_16x16x32_bf16 v[64:67], v[132:135], a[216:219], v[64:67]\n" \
"v_mfma_f32_16x16x32_bf16 v[96:99], v[128:131], a[204:207], v[96:99]\n" \
"ds_read_b64_tr_b16 v[168:169], v225 offset:33800\n" \
"v_mfma_f32_16x16x32_bf16 v[96:99], v[132:135], a[220:223], v[96:99]\n" \
"ds_read_b64_tr_b16 v[170:171], v225 offset:34056\n" \
"v_mfma_f32_16x16x32_bf16 v[4:7], v[136:139], a[192:195], v[4:7]\n" \
"buffer_load_dwordx4 v218, %[srdw], s49 offen offset:3072 lds\n" \
"v_mfma_f32_16x16x32_bf16 v[4:7], v[140:143], a[208:211], v[4:7]\n" \
"ds_read_b64_tr_b16 v[172:173], v225 offset:41992\n" \
"v_mfma_f32_16x16x32_bf16 v[36:39], v[136:139], a[196:199], v[36:39]\n" \
"ds_read_b64_tr_b16 v[174:175], v225 offset:42248\n" \
"v_mfma_f32_16x16x32_bf16 v[36:39], v[140:143], a[212:215], v[36:39]\n" \
"v_mfma_f32_16x16x32_bf16 v[68:71], v[136:139], a[200:203], v[68:71]\n" \
"ds_read_b64_tr_b16 v[176:177], v225 offset:34304\n" \
"v_mfma_f32_16x16x32_bf16 v[68:71], v[140:143], a[216:219], v[68:71]\n" \
"ds_read_b64_tr_b16 v[178:179], v225 offset:34560\n" \
"v_mfma_f32_16x16x32_bf16 v[100:103], v[136:139], a[204:207], v[100:103]\n" \
"buffer_load_dwordx4 a[160:163], v227, %[srdz], s50 offen offset:2048 sc1\n" \
"v_mfma_f32_16x16x32_bf16 v[100:103], v[140:143], a[220:223], v[100:103]\n" \
"ds_read_b64_tr_b16 v[180:181], v225 offset:42496\n" \
"v_mfma_f32_16x16x32_bf16 v[8:11], v[144:147], a[192:195], v[8:11]\n" \
"ds_read_b64_tr_b16 v[182:183], v225 offset:42752\n" \
"v_mfma_f32_16x16x32_bf16 v[8:11], v[148:151], a[208:211], v[8:11]\n" \
"v_mfma_f32_16x16x32_bf16 v[40:43], v[144:147], a[196:199], v[40:43]\n" \
"ds_read_b64_tr_b16 v[184:185], v225 offset:34312\n" \
"v_mfma_f32_16x16x32_bf16 v[40:43], v[148:151], a[212:215], v[40:43]\n" \
"ds_read_b64_tr_b16 v[186:187], v225 offset:34568\n" \
"v_mfma_f32_16x16x32_bf16 v[72:75], v[144:147], a[200:203], v[72:75]\n" \
"buffer_load_dwordx4 a[164:167], v219, %[srdz], s50 offen offset:2048 sc1\n" \
"v_mfma_f32_16x16x32_bf16 v[72:75], v[148:151], a[216:219], v[72:75]\n" \
"ds_read_b64_tr_b16 v[188:189], v225 offset:42504\n" \
"v_mfma_f32_16x16x32_bf16 v[104:107], v[144:147], a[204:207], v[104:107]\n" \
"ds_read_b64_tr_b16 v[190:191], v225 offset:42760\n" \
"v_mfma_f32_16x16x32_bf16 v[104:107], v[148:151], a[220:223], v[104:107]\n" \
"v_mfma_f32_16x16x32_bf16 v[12:15], v[152:155], a[192:195], v[12:15]\n" \
"buffer_load_dwordx4 a[168:171], v220, %[srdz], s50 offen offset:2048 sc1\n" \
"v_mfma_f32_16x16x32_bf16 v[12:15], v[156:159], a[208:211], v[12:15]\n" \
"v_mfma_f32_16x16x32_bf16 v[44:47], v[152:155], a[196:199], v[44:47]\n" \
"v_mfma_f32_16x16x32_bf16 v[44:47], v[156:159], a[212:215], v[44:47]\n" \
"v_mfma_f32_16x16x32_bf16 v[76:79], v[152:155], a[200:203], v[76:79]\n" \
"buffer_load_dwordx4 a[172:175], v221, %[srdz], s50 offen offset:2048 sc1\n" \
"v_mfma_f32_16x16x32_bf16 v[76:79], v[156:159], a[216:219], v[76:79]\n" \
"v_mfma_f32_16x16x32_bf16 v[108:111], v[152:155], a[204:207], v[108:111]\n" \
"v_mfma_f32_16x16x32_bf16 v[108:111], v[156:159], a[220:223], v[108:111]\n" \
"s_waitcnt vmcnt(28) lgkmcnt(0)\n" \
"s_barrier\n" \
"v_mfma_f32_16x16x32_bf16 v[16:19], v[160:163], a[192:195], v[16:19]\n" \
"ds_read_b64_tr_b16 v[128:129], v225 offset:49152\n" \
"v_mfma_f32_16x16x32_bf16 v[16:19], v[164:167], a[208:211], v[16:19]\n" \
"ds_read_b64_tr_b16 v[130:131], v225 offset:49408\n" \
"v_mfma_f32_16x16x32_bf16 v[48:51], v[160:163], a[196:199], v[48:51]\n" \
"s_add_u32 s49, s49, s55\n" \
"s_mov_b32 m0, s46\n" \
"s_nop 0\n" \
"buffer_load_dwordx4 v226, %[srdw], s49 offen offset:0 lds\n" \
"v_mfma_f32_16x16x32_bf16 v[48:51], v[164:167], a[212:215], v[48:51]\n" \
"ds_read_b64_tr_b16 v[132:133], v225 offset:57344\n" \
"v_mfma_f32_16x16x32_bf16 v[80:83], v[160:163], a[200:203], v[80:83]\n" \
"ds_read_b64_tr_b16 v[134:135], v225 offset:57600\n" \
"v_mfma_f32_16x16x32_bf16 v[80:83], v[164:167], a[216:219], v[80:83]\n" \
"v_mfma_f32_16x16x32_bf16 v[112:115], v[160:163], a[204:207], v[112:115]\n" \
"ds_read_b64_tr_b16 v[136:137], v225 offset:49160\n" \
"v_mfma_f32_16x16x32_bf16 v[112:115], v[164:167], a[220:223], v[112:115]\n" \
"ds_read_b64_tr_b16 v[138:139], v225 offset:49416\n" \
"v_mfma_f32_16x16x32_bf16 v[20:23], v[168:171], a[192:195], v[20:23]\n" \
"buffer_load_dwordx4 v216, %[srdw], s49 offen offset:1024 lds\n" \
"v_mfma_f32_16x16x32_bf16 v[20:23], v[172:175], a[208:211], v[20:23]\n" \
"ds_read_b64_tr_b16 v[140:141], v225 offset:57352\n" \
"v_mfma_f32_16x16x32_bf16 v[52:55], v[168:171], a[196:199], v[52:55]\n" \
"ds_read_b64_tr_b16 v[142:143], v225 offset:57608\n" \
"v_mfma_f32_16x16x32_bf16 v[52:55], v[172:175], a[212:215], v[52:55]\n" \
"v_mfma_f32_16x16x32_bf16 v[84:87], v[168:171], a[200:203], v[84:87]\n" \
"ds_read_b64_tr_b16 v[144:145], v225 offset:49664\n" \
"v_mfma_f32_16x16x32_bf16 v[84:87], v[172:175], a[216:219], v[84:87]\n" \
"ds_read_b64_tr_b16 v[146:147], v225 offset:49920\n" \
"v_mfma_f32_16x16x32_bf16 v[116:119], v[168:171], a[204:207], v[116:119]\n" \
"buffer_load_dwordx4 a[176:179], v227, %[srdz], s50 offen offset:3072 sc1\n" \
"v_mfma_f32_16x16x32_bf16 v[116:119], v[172:175], a[220:223], v[116:119]\n" \
"ds_read_b64_tr_b16 v[148:149], v225 offset:57856\n" \
"v_mfma_f32_16x16x32_bf16 v[24:27], v[176:179], a[192:195], v[24:27]\n" \
"ds_read_b64_tr_b16 v[150:151], v225 offset:58112\n" \
"v_mfma_f32_16x16x32_bf16 v[24:27], v[180:183], a[208:211], v[24:27]\n" \
"v_mfma_f32_16x16x32_bf16 v[56:59], v[176:179], a[196:199], v[56:59]\n" \
"ds_read_b64_tr_b16 v[152:153], v225 offset:49672\n" \
"v_mfma_f32_16x16x32_bf16 v[56:59], v[180:183], a[212:215], v[56:59]\n" \
"ds_read_b64_tr_b16 v[154:155], v225 offset:49928\n" \
"v_mfma_f32_16x16x32_bf16 v[88:91], v[176:179], a[200:203], v[88:91]\n" \
"buffer_load_dwordx4 a[180:183], v219, %[srdz], s50 offen offset:3072 sc1\n" \
"v_mfma_f32_16x16x32_bf16 v[88:91], v[180:183], a[216:219], v[88:91]\n" \
"ds_read_b64_tr_b16 v[156:157], v225 offset:57864\n" \
"v_mfma_f32_16x16x32_bf16 v[120:123], v[176:179], a[204:207], v[120:123]\n" \
"ds_read_b64_tr_b16 v[158:159], v225 offset:58120\n" \
"v_mfma_f32_16x16x32_bf16 v[120:123], v[180:183], a[220:223], v[120:123]\n" \
"v_mfma_f32_16x16x32_bf16 v[28:31], v[184:187], a[192:195], v[28:31]\n" \
"buffer_load_dwordx4 a[184:187], v220, %[srdz], s50 offen offset:3072 sc1\n" \
"v_mfma_f32_16x16x32_bf16 v[28:31], v[188:191], a[208:211], v[28:31]\n" \
"v_mfma_f32_16x16x32_bf16 v[60:63], v[184:187], a[196:199], v[60:63]\n" \
"v_mfma_f32_16x16x32_bf16 v[60:63], v[188:191], a[212:215], v[60:63]\n" \
"v_mfma_f32_16x16x32_bf16 v[92:95], v[184:187], a[200:203], v[92:95]\n" \
"buffer_load_dwordx4 a[188:191], v221, %[srdz], s50 offen offset:3072 sc1\n" \
"v_mfma_f32_16x16x32_bf16 v[92:95], v[188:191], a[216:219], v[92:95]\n" \
"v_mfma_f32_16x16x32_bf16 v[124:127], v[184:187], a[204:207], v[124:127]\n" \
"v_mfma_f32_16x16x32_bf16 v[124:127], v[188:191], a[220:223], v[124:127]\n" \
"s_waitcnt vmcnt(24) lgkmcnt(0)\n" \
"v_mfma_f32_16x16x32_bf16 v[0:3], v[128:131], a[224:227], v[0:3]\n" \
"ds_read_b64_tr_b16 v[160:161], v225 offset:50176\n" \
"v_mfma_f32_16x16x32_bf16 v[0:3], v[132:135], a[240:243], v[0:3]\n" \
"ds_read_b64_tr_b16 v[162:163], v225 offset:50432\n" \
"v_mfma_f32_16x16x32_bf16 v[32:35], v[128:131], a[228:231], v[32:35]\n" \
"s_mov_b32 m0, s46\n" \
"s_nop 0\n" \
"buffer_load_dwordx4 v217, %[srdw], s49 offen offset:2048 lds\n" \
"v_mfma_f32_16x16x32_bf16 v[32:35], v[132:135], a[244:247], v[32:35]\n" \
"ds_read_b64_tr_b16 v[164:165], v225 offset:58368\n" \
"v_mfma_f32_16x16x32_bf16 v[64:67], v[128:131], a[232:235], v[64:67]\n" \
"ds_read_b64_tr_b16 v[166:167], v225 offset:58624\n" \
"v_mfma_f32_16x16x32_bf16 v[64:67], v[132:135], a[248:251], v[64:67]\n" \
"v_mfma_f32_16x16x32_bf16 v[96:99], v[128:131], a[236:239], v[96:99]\n" \
"ds_read_b64_tr_b16 v[168:169], v225 offset:50184\n" \
"v_mfma_f32_16x16x32_bf16 v[96:99], v[132:135], a[252:255], v[96:99]\n" \
"ds_read_b64_tr_b16 v[170:171], v225 offset:50440\n" \
"v_mfma_f32_16x16x32_bf16 v[4:7], v[136:139], a[224:227], v[4:7]\n" \
"buffer_load_dwordx4 v218, %[srdw], s49 offen offset:3072 lds\n" \
"v_mfma_f32_16x16x32_bf16 v[4:7], v[140:143], a[240:243], v[4:7]\n" \
"ds_read_b64_tr_b16 v[172:173], v225 offset:58376\n" \
"v_mfma_f32_16x16x32_bf16 v[36:39], v[136:139], a[228:231], v[36:39]\n" \
"ds_read_b64_tr_b16 v[174:175], v225 offset:58632\n" \
"v_mfma_f32_16x16x32_bf16 v[36:39], v[140:143], a[244:247], v[36:39]\n" \
"v_mfma_f32_16x16x32_bf16 v[68:71], v[136:139], a[232:235], v[68:71]\n" \
"ds_read_b64_tr_b16 v[176:177], v225 offset:50688\n" \
"v_mfma_f32_16x16x32_bf16 v[68:71], v[140:143], a[248:251], v[68:71]\n" \
"ds_read_b64_tr_b16 v[178:179], v225 offset:50944\n" \
"v_mfma_f32_16x16x32_bf16 v[100:103], v[136:139], a[236:239], v[100:103]\n" \
"s_add_u32 s50, s50, s53\n" \
"buffer_load_dwordx4 a[192:195], v227, %[srdz], s50 offen offset:0 sc1\n" \
"v_mfma_f32_16x16x32_bf16 v[100:103], v[140:143], a[252:255], v[100:103]\n" \
"ds_read_b64_tr_b16 v[180:181], v225 offset:58880\n" \
"v_mfma_f32_16x16x32_bf16 v[8:11], v[144:147], a[224:227], v[8:11]\n" \
"ds_read_b64_tr_b16 v[182:183], v225 offset:59136\n" \
"v_mfma_f32_16x16x32_bf16 v[8:11], v[148:151], a[240:243], v[8:11]\n" \
"v_mfma_f32_16x16x32_bf16 v[40:43], v[144:147], a[228:231], v[40:43]\n" \
"ds_read_b64_tr_b16 v[184:185], v225 offset:50696\n" \
"v_mfma_f32_16x16x32_bf16 v[40:43], v[148:151], a[244:247], v[40:43]\n" \
"ds_read_b64_tr_b16 v[186:187], v225 offset:50952\n" \
"v_mfma_f32_16x16x32_bf16 v[72:75], v[144:147], a[232:235], v[72:75]\n" \
"buffer_load_dwordx4 a[196:199], v219, %[srdz], s50 offen offset:0 sc1\n" \
"v_mfma_f32_16x16x32_bf16 v[72:75], v[148:151], a[248:251], v[72:75]\n" \
"ds_read_b64_tr_b16 v[188:189], v225 offset:58888\n" \
"v_mfma_f32_16x16x32_bf16 v[104:107], v[144:147], a[236:239], v[104:107]\n" \
"ds_read_b64_tr_b16 v[190:191], v225 offset:59144\n" \
"v_mfma_f32_16x16x32_bf16 v[104:107], v[148:151], a[252:255], v[104:107]\n" \
"v_mfma_f32_16x16x32_bf16 v[12:15], v[152:155], a[224:227], v[12:15]\n" \
"buffer_load_dwordx4 a[200:203], v220, %[srdz], s50 offen offset:0 sc1\n" \
"v_mfma_f32_16x16x32_bf16 v[12:15], v[156:159], a[240:243], v[12:15]\n" \
"v_mfma_f32_16x16x32_bf16 v[44:47], v[152:155], a[228:231], v[44:47]\n" \
"buffer_load_dword v237, v236, %[srdw], s60 offen\n" \
"s_add_u32 s60, s60, 0x200000\n" \
"v_mfma_f32_16x16x32_bf16 v[44:47], v[156:159], a[244:247], v[44:47]\n" \
".if . - adaln_body != 4096\n" \
".error \"size model\"\n" \
".endif\n" \
"Lk0_%=:\n" \
"s_cbranch_execz Lk1_%=\n" \
"s_nop 0\n" \
"v_mfma_f32_16x16x32_bf16 v[76:79], v[152:155], a[232:235], v[76:79]\n" \
"buffer_load_dwordx4 a[204:207], v221, %[srdz], s50 offen offset:0 sc1\n" \
"v_mfma_f32_16x16x32_bf16 v[76:79], v[156:159], a[248:251], v[76:79]\n" \
"v_mfma_f32_16x16x32_bf16 v[108:111], v[152:155], a[236:239], v[108:111]\n" \
"v_mfma_f32_16x16x32_bf16 v[108:111], v[156:159], a[252:255], v[108:111]\n" \
"s_waitcnt vmcnt(29) lgkmcnt(0)\n" \
"s_barrier\n" \
"v_mfma_f32_16x16x32_bf16 v[16:19], v[160:163], a[224:227], v[16:19]\n" \
"ds_read_b64_tr_b16 v[128:129], v225 offset:0\n" \
"v_mfma_f32_16x16x32_bf16 v[16:19], v[164:167], a[240:243], v[16:19]\n" \
"ds_read_b64_tr_b16 v[130:131], v225 offset:256\n" \
"v_mfma_f32_16x16x32_bf16 v[48:51], v[160:163], a[228:231], v[48:51]\n" \
"s_add_u32 s49, s49, s55\n" \
"s_mov_b32 m0, s47\n" \
"s_nop 0\n" \
"buffer_load_dwordx4 v226, %[srdw], s49 offen offset:0 lds\n" \
"v_mfma_f32_16x16x32_bf16 v[48:51], v[164:167], a[244:247], v[48:51]\n" \
"ds_read_b64_tr_b16 v[132:133], v225 offset:8192\n" \
"v_mfma_f32_16x16x32_bf16 v[80:83], v[160:163], a[232:235], v[80:83]\n" \
"ds_read_b64_tr_b16 v[134:135], v225 offset:8448\n" \
"v_mfma_f32_16x16x32_bf16 v[80:83], v[164:167], a[248:251], v[80:83]\n" \
"v_mfma_f32_16x16x32_bf16 v[112:115], v[160:163], a[236:239], v[112:115]\n" \
"ds_read_b64_tr_b16 v[136:137], v225 offset:8\n" \
"v_mfma_f32_16x16x32_bf16 v[112:115], v[164:167], a[252:255], v[112:115]\n" \
"ds_read_b64_tr_b16 v[138:139], v225 offset:264\n" \
"v_mfma_f32_16x16x32_bf16 v[20:23], v[168:171], a[224:227], v[20:23]\n" \
"buffer_load_dwordx4 v216, %[srdw], s49 offen offset:1024 lds\n" \
"v_mfma_f32_16x16x32_bf16 v[20:23], v[172:175], a[240:243], v[20:23]\n" \
"ds_read_b64_tr_b16 v[140:141], v225 offset:8200\n" \
"v_mfma_f32_16x16x32_bf16 v[52:55], v[168:171], a[228:231], v[52:55]\n" \
"ds_read_b64_tr_b16 v[142:143], v225 offset:8456\n" \
"v_mfma_f32_16x16x32_bf16 v[52:55], v[172:175], a[244:247], v[52:55]\n" \
"v_mfma_f32_16x16x32_bf16 v[84:87], v[168:171], a[232:235], v[84:87]\n" \
"ds_read_b64_tr_b16 v[144:145], v225 offset:512\n" \
"v_mfma_f32_16x16x32_bf16 v[84:87], v[172:175], a[248:251], v[84:87]\n" \
"ds_read_b64_tr_b16 v[146:147], v225 offset:768\n" \
"v_mfma_f32_16x16x32_bf16 v[116:119], v[168:171], a[236:239], v[116:119]\n" \
"buffer_load_dwordx4 a[208:211], v227, %[srdz], s50 offen offset:1024 sc1\n" \
"v_mfma_f32_16x16x32_bf16 v[116:119], v[172:175], a[252:255], v[116:119]\n" \
"ds_read_b64_tr_b16 v[148:149], v225 offset:8704\n" \
"v_mfma_f32_16x16x32_bf16 v[24:27], v[176:179], a[224:227], v[24:27]\n" \
"ds_read_b64_tr_b16 v[150:151], v225 offset:8960\n" \
"v_mfma_f32_16x16x32_bf16 v[24:27], v[180:183], a[240:243], v[24:27]\n" \
"v_mfma_f32_16x16x32_bf16 v[56:59], v[176:179], a[228:231], v[56:59]\n" \
"ds_read_b64_tr_b16 v[152:153], v225 offset:520\n" \
"v_mfma_f32_16x16x32_bf16 v[56:59], v[180:183], a[244:247], v[56:59]\n" \
"ds_read_b64_tr_b16 v[154:155], v225 offset:776\n" \
"v_mfma_f32_16x16x32_bf16 v[88:91], v[176:179], a[232:235], v[88:91]\n" \
"buffer_load_dwordx4 a[212:215], v219, %[srdz], s50 offen offset:1024 sc1\n" \
"v_mfma_f32_16x16x32_bf16 v[88:91], v[180:183], a[248:251], v[88:91]\n" \
"ds_read_b64_tr_b16 v[156:157], v225 offset:8712\n" \
"v_mfma_f32_16x16x32_bf16 v[120:123], v[176:179], a[236:239], v[120:123]\n" \
"ds_read_b64_tr_b16 v[158:159], v225 offset:8968\n" \
"v_mfma_f32_16x16x32_bf16 v[120:123], v[180:183], a[252:255], v[120:123]\n" \
"v_mfma_f32_16x16x32_bf16 v[28:31], v[184:187], a[224:227], v[28:31]\n" \
"buffer_load_dwordx4 a[216:219], v220, %[srdz], s50 offen offset:1024 sc1\n" \
"v_mfma_f32_16x16x32_bf16 v[28:31], v[188:191], a[240:243], v[28:31]\n" \
"v_mfma_f32_16x16x32_bf16 v[60:63], v[184:187], a[228:231], v[60:63]\n" \
"v_mfma_f32_16x16x32_bf16 v[60:63], v[188:191], a[244:247], v[60:63]\n" \
"v_mfma_f32_16x16x32_bf16 v[92:95], v[184:187], a[232:235], v[92:95]\n" \
"buffer_load_dwordx4 a[220:223], v221, %[srdz], s50 offen offset:1024 sc1\n" \
"v_mfma_f32_16x16x32_bf16 v[92:95], v[188:191], a[248:251], v[92:95]\n" \
"v_mfma_f32_16x16x32_bf16 v[124:127], v[184:187], a[236:239], v[124:127]\n" \
"v_mfma_f32_16x16x32_bf16 v[124:127], v[188:191], a[252:255], v[124:127]\n" \
"s_waitcnt vmcnt(25) lgkmcnt(0)\n" \
"s_sub_u32 s51, s51, 1\n" \
"s_cmp_lg_u32 s51, 0\n" \
"s_cbranch_scc1 Lq_%=\n" \
"s_nop 0\n" \
"v_mfma_f32_16x16x32_bf16 v[0:3], v[128:131], a[128:131], v[0:3]\n" \
"ds_read_b64_tr_b16 v[160:161], v225 offset:1024\n" \
"v_mfma_f32_16x16x32_bf16 v[0:3], v[132:135], a[144:147], v[0:3]\n" \
"ds_read_b64_tr_b16 v[162:163], v225 offset:1280\n" \
"v_mfma_f32_16x16x32_bf16 v[32:35], v[128:131], a[132:135], v[32:35]\n" \
"s_mov_b32 m0, s47\n" \
"s_nop 0\n" \
"buffer_load_dwordx4 v217, %[srdw], s49 offen offset:2048 lds\n" \
"v_mfma_f32_16x16x32_bf16 v[32:35], v[132:135], a[148:151], v[32:35]\n" \
"ds_read_b64_tr_b16 v[164:165], v225 offset:9216\n" \
"v_mfma_f32_16x16x32_bf16 v[64:67], v[128:131], a[136:139], v[64:67]\n" \
"ds_read_b64_tr_b16 v[166:167], v225 offset:9472\n" \
"v_mfma_f32_16x16x32_bf16 v[64:67], v[132:135], a[152:155], v[64:67]\n" \
"v_mfma_f32_16x16x32_bf16 v[96:99], v[128:131], a[140:143], v[96:99]\n" \
"ds_read_b64_tr_b16 v[168:169], v225 offset:1032\n" \
"v_mfma_f32_16x16x32_bf16 v[96:99], v[132:135], a[156:159], v[96:99]\n" \
"ds_read_b64_tr_b16 v[170:171], v225 offset:1288\n" \
"v_mfma_f32_16x16x32_bf16 v[4:7], v[136:139], a[128:131], v[4:7]\n" \
"buffer_load_dwordx4 v218, %[srdw], s49 offen offset:3072 lds\n" \
"v_mfma_f32_16x16x32_bf16 v[4:7], v[140:143], a[144:147], v[4:7]\n" \
"ds_read_b64_tr_b16 v[172:173], v225 offset:9224\n" \
"v_mfma_f32_16x16x32_bf16 v[36:39], v[136:139], a[132:135], v[36:39]\n" \
"ds_read_b64_tr_b16 v[174:175], v225 offset:9480\n" \
"v_mfma_f32_16x16x32_bf16 v[36:39], v[140:143], a[148:151], v[36:39]\n" \
"v_mfma_f32_16x16x32_bf16 v[68:71], v[136:139], a[136:139], v[68:71]\n" \
"ds_read_b64_tr_b16 v[176:177], v225 offset:1536\n" \
"v_mfma_f32_16x16x32_bf16 v[68:71], v[140:143], a[152:155], v[68:71]\n" \
"ds_read_b64_tr_b16 v[178:179], v225 offset:1792\n" \
"v_mfma_f32_16x16x32_bf16 v[100:103], v[136:139], a[140:143], v[100:103]\n" \
"buffer_load_dwordx4 a[224:227], v227, %[srdz], s50 offen offset:2048 sc1\n" \
"v_mfma_f32_16x16x32_bf16 v[100:103], v[140:143], a[156:159], v[100:103]\n" \
"ds_read_b64_tr_b16 v[180:181], v225 offset:9728\n" \
"v_mfma_f32_16x16x32_bf16 v[8:11], v[144:147], a[128:131], v[8:11]\n" \
"ds_read_b64_tr_b16 v[182:183], v225 offset:9984\n" \
"v_mfma_f32_16x16x32_bf16 v[8:11], v[148:151], a[144:147], v[8:11]\n" \
"buffer_load_dwordx4 v[230:233], off, %[srdz], 0 offset:128 sc1\n" \
"v_mfma_f32_16x16x32_bf16 v[40:43], v[144:147], a[132:135], v[40:43]\n" \
"ds_read_b64_tr_b16 v[184:185], v225 offset:1544\n" \
"v_mfma_f32_16x16x32_bf16 v[40:43], v[148:151], a[148:151], v[40:43]\n" \
"ds_read_b64_tr_b16 v[186:187], v225 offset:1800\n" \
"v_mfma_f32_16x16x32_bf16 v[72:75], v[144:147], a[136:139], v[72:75]\n" \
"buffer_load_dwordx4 a[228:231], v219, %[srdz], s50 offen offset:2048 sc1\n" \
"v_mfma_f32_16x16x32_bf16 v[72:75], v[148:151], a[152:155], v[72:75]\n" \
"ds_read_b64_tr_b16 v[188:189], v225 offset:9736\n" \
"v_mfma_f32_16x16x32_bf16 v[104:107], v[144:147], a[140:143], v[104:107]\n" \
"ds_read_b64_tr_b16 v[190:191], v225 offset:9992\n" \
"v_mfma_f32_16x16x32_bf16 v[104:107], v[148:151], a[156:159], v[104:107]\n" \
"v_mfma_f32_16x16x32_bf16 v[12:15], v[152:155], a[128:131], v[12:15]\n" \
"buffer_load_dwordx4 a[232:235], v220, %[srdz], s50 offen offset:2048 sc1\n" \
"v_mfma_f32_16x16x32_bf16 v[12:15], v[156:159], a[144:147], v[12:15]\n" \
"v_mfma_f32_16x16x32_bf16 v[44:47], v[152:155], a[132:135], v[44:47]\n" \
"v_mfma_f32_16x16x32_bf16 v[44:47], v[156:159], a[148:151], v[44:47]\n" \
"v_mfma_f32_16x16x32_bf16 v[76:79], v[152:155], a[136:139], v[76:79]\n" \
"buffer_load_dwordx4 a[236:239], v221, %[srdz], s50 offen offset:2048 sc1\n" \
"v_mfma_f32_16x16x32_bf16 v[76:79], v[156:159], a[152:155], v[76:79]\n" \
"v_mfma_f32_16x16x32_bf16 v[108:111], v[152:155], a[140:143], v[108:111]\n" \
"v_mfma_f32_16x16x32_bf16 v[108:111], v[156:159], a[156:159], v[108:111]\n" \
"s_waitcnt vmcnt(30) lgkmcnt(0)\n" \
"s_barrier\n" \
"v_mfma_f32_16x16x32_bf16 v[16:19], v[160:163], a[128:131], v[16:19]\n" \
"ds_read_b64_tr_b16 v[128:129], v225 offset:16384\n" \
"v_mfma_f32_16x16x32_bf16 v[16:19], v[164:167], a[144:147], v[16:19]\n" \
"ds_read_b64_tr_b16 v[130:131], v225 offset:16640\n" \
"v_mfma_f32_16x16x32_bf16 v[48:51], v[160:163], a[132:135], v[48:51]\n" \
"s_mov_b32 s48, 16384\n" \
"s_mov_b32 m0, s44\n" \
"s_nop 0\n" \
"buffer_load_dwordx4 %[vd0], %[srdv], s48 offen offset:0 lds\n" \
"v_mfma_f32_16x16x32_bf16 v[48:51], v[164:167], a[148:151], v[48:51]\n" \
"ds_read_b64_tr_b16 v[132:133], v225 offset:24576\n" \
"v_mfma_f32_16x16x32_bf16 v[80:83], v[160:163], a[136:139], v[80:83]\n" \
"ds_read_b64_tr_b16 v[134:135], v225 offset:24832\n" \
"v_mfma_f32_16x16x32_bf16 v[80:83], v[164:167], a[152:155], v[80:83]\n" \
"v_mfma_f32_16x16x32_bf16 v[112:115], v[160:163], a[140:143], v[112:115]\n" \
"ds_read_b64_tr_b16 v[136:137], v225 offset:16392\n" \
"v_mfma_f32_16x16x32_bf16 v[112:115], v[164:167], a[156:159], v[112:115]\n" \
"ds_read_b64_tr_b16 v[138:139], v225 offset:16648\n" \
"v_mfma_f32_16x16x32_bf16 v[20:23], v[168:171], a[128:131], v[20:23]\n" \
"buffer_load_dwordx4 v215, %[srdv], s48 offen offset:1024 lds\n" \
"v_mfma_f32_16x16x32_bf16 v[20:23], v[172:175], a[144:147], v[20:23]\n" \
"ds_read_b64_tr_b16 v[140:141], v225 offset:24584\n" \
"v_mfma_f32_16x16x32_bf16 v[52:55], v[168:171], a[132:135], v[52:55]\n" \
"ds_read_b64_tr_b16 v[142:143], v225 offset:24840\n" \
"v_mfma_f32_16x16x32_bf16 v[52:55], v[172:175], a[148:151], v[52:55]\n" \
"v_mfma_f32_16x16x32_bf16 v[84:87], v[168:171], a[136:139], v[84:87]\n" \
"ds_read_b64_tr_b16 v[144:145], v225 offset:16896\n" \
"v_mfma_f32_16x16x32_bf16 v[84:87], v[172:175], a[152:155], v[84:87]\n" \
"ds_read_b64_tr_b16 v[146:147], v225 offset:17152\n" \
"v_mfma_f32_16x16x32_bf16 v[116:119], v[168:171], a[140:143], v[116:119]\n" \
"buffer_load_dwordx4 a[240:243], v227, %[srdz], s50 offen offset:3072 sc1\n" \
"v_mfma_f32_16x16x32_bf16 v[116:119], v[172:175], a[156:159], v[116:119]\n" \
"ds_read_b64_tr_b16 v[148:149], v225 offset:25088\n" \
"v_mfma_f32_16x16x32_bf16 v[24:27], v[176:179], a[128:131], v[24:27]\n" \
"ds_read_b64_tr_b16 v[150:151], v225 offset:25344\n" \
"v_mfma_f32_16x16x32_bf16 v[24:27], v[180:183], a[144:147], v[24:27]\n" \
"v_mfma_f32_16x16x32_bf16 v[56:59], v[176:179], a[132:135], v[56:59]\n" \
"ds_read_b64_tr_b16 v[152:153], v225 offset:16904\n" \
"v_mfma_f32_16x16x32_bf16 v[56:59], v[180:183], a[148:151], v[56:59]\n" \
"ds_read_b64_tr_b16 v[154:155], v225 offset:17160\n" \
"v_mfma_f32_16x16x32_bf16 v[88:91], v[176:179], a[136:139], v[88:91]\n" \
"buffer_load_dwordx4 a[244:247], v219, %[srdz], s50 offen offset:3072 sc1\n" \
"v_mfma_f32_16x16x32_bf16 v[88:91], v[180:183], a[152:155], v[88:91]\n" \
"ds_read_b64_tr_b16 v[156:157], v225 offset:25096\n" \
"v_mfma_f32_16x16x32_bf16 v[120:123], v[176:179], a[140:143], v[120:123]\n" \
"ds_read_b64_tr_b16 v[158:159], v225 offset:25352\n" \
"v_mfma_f32_16x16x32_bf16 v[120:123], v[180:183], a[156:159], v[120:123]\n" \
"v_mfma_f32_16x16x32_bf16 v[28:31], v[184:187], a[128:131], v[28:31]\n" \
"buffer_load_dwordx4 a[248:251], v220, %[srdz], s50 offen offset:3072 sc1\n" \
"v_mfma_f32_16x16x32_bf16 v[28:31], v[188:191], a[144:147], v[28:31]\n" \
"v_mfma_f32_16x16x32_bf16 v[60:63], v[184:187], a[132:135], v[60:63]\n" \
"v_mfma_f32_16x16x32_bf16 v[60:63], v[188:191], a[148:151], v[60:63]\n" \
"v_mfma_f32_16x16x32_bf16 v[92:95], v[184:187], a[136:139], v[92:95]\n" \
"buffer_load_dwordx4 a[252:255], v221, %[srdz], s50 offen offset:3072 sc1\n" \
"v_mfma_f32_16x16x32_bf16 v[92:95], v[188:191], a[152:155], v[92:95]\n" \
"v_mfma_f32_16x16x32_bf16 v[124:127], v[184:187], a[140:143], v[124:127]\n" \
"v_mfma_f32_16x16x32_bf16 v[124:127], v[188:191], a[156:159], v[124:127]\n" \
"s_waitcnt vmcnt(26) lgkmcnt(0)\n" \
"v_mfma_f32_16x16x32_bf16 v[0:3], v[128:131], a[160:163], v[0:3]\n" \
"ds_read_b64_tr_b16 v[160:161], v225 offset:17408\n" \
"v_mfma_f32_16x16x32_bf16 v[0:3], v[132:135], a[176:179], v[0:3]\n" \
"ds_read_b64_tr_b16 v[162:163], v225 offset:17664\n" \
"v_mfma_f32_16x16x32_bf16 v[32:35], v[128:131], a[164:167], v[32:35]\n" \
"s_mov_b32 s48, 16384\n" \
"s_mov_b32 m0, s44\n" \
"s_nop 0\n" \
"buffer_load_dwordx4 %[vd0], %[srdv], s48 offen offset:2048 lds\n" \
"v_mfma_f32_16x16x32_bf16 v[32:35], v[132:135], a[180:183], v[32:35]\n" \
"ds_read_b64_tr_b16 v[164:165], v225 offset:25600\n" \
"v_mfma_f32_16x16x32_bf16 v[64:67], v[128:131], a[168:171], v[64:67]\n" \
"ds_read_b64_tr_b16 v[166:167], v225 offset:25856\n" \
"v_mfma_f32_16x16x32_bf16 v[64:67], v[132:135], a[184:187], v[64:67]\n" \
"v_mfma_f32_16x16x32_bf16 v[96:99], v[128:131], a[172:175], v[96:99]\n" \
"ds_read_b64_tr_b16 v[168:169], v225 offset:17416\n" \
"v_mfma_f32_16x16x32_bf16 v[96:99], v[132:135], a[188:191], v[96:99]\n" \
"ds_read_b64_tr_b16 v[170:171], v225 offset:17672\n" \
"v_mfma_f32_16x16x32_bf16 v[4:7], v[136:139], a[160:163], v[4:7]\n" \
"buffer_load_dwordx4 v215, %[srdv], s48 offen offset:3072 lds\n" \
"v_mfma_f32_16x16x32_bf16 v[4:7], v[140:143], a[176:179], v[4:7]\n" \
"ds_read_b64_tr_b16 v[172:173], v225 offset:25608\n" \
"v_mfma_f32_16x16x32_bf16 v[36:39], v[136:139], a[164:167], v[36:39]\n" \
"ds_read_b64_tr_b16 v[174:175], v225 offset:25864\n" \
"v_mfma_f32_16x16x32_bf16 v[36:39], v[140:143], a[180:183], v[36:39]\n" \
"v_mfma_f32_16x16x32_bf16 v[68:71], v[136:139], a[168:171], v[68:71]\n" \
"ds_read_b64_tr_b16 v[176:177], v225 offset:17920\n" \
"v_mfma_f32_16x16x32_bf16 v[68:71], v[140:143], a[184:187], v[68:71]\n" \
"ds_read_b64_tr_b16 v[178:179], v225 offset:18176\n" \
"v_mfma_f32_16x16x32_bf16 v[100:103], v[136:139], a[172:175], v[100:103]\n" \
"s_mov_b32 s48, 0\n" \
"s_mov_b32 m0, s40\n" \
"s_nop 0\n" \
"buffer_load_dwordx4 %[kd0], %[srdk], s48 offen offset:0 lds\n" \
"v_mfma_f32_16x16x32_bf16 v[100:103], v[140:143], a[188:191], v[100:103]\n" \
"ds_read_b64_tr_b16 v[180:181], v225 offset:26112\n" \
"v_mfma_f32_16x16x32_bf16 v[8:11], v[144:147], a[160:163], v[8:11]\n" \
"ds_read_b64_tr_b16 v[182:183], v225 offset:26368\n" \
"v_mfma_f32_16x16x32_bf16 v[8:11], v[148:151], a[176:179], v[8:11]\n" \
"v_mfma_f32_16x16x32_bf16 v[40:43], v[144:147], a[164:167], v[40:43]\n" \
"ds_read_b64_tr_b16 v[184:185], v225 offset:17928\n" \
"v_mfma_f32_16x16x32_bf16 v[40:43], v[148:151], a[180:183], v[40:43]\n" \
"ds_read_b64_tr_b16 v[186:187], v225 offset:18184\n" \
"v_mfma_f32_16x16x32_bf16 v[72:75], v[144:147], a[168:171], v[72:75]\n" \
"buffer_load_dwordx4 v212, %[srdk], s48 offen offset:1024 lds\n" \
"v_mfma_f32_16x16x32_bf16 v[72:75], v[148:151], a[184:187], v[72:75]\n" \
"ds_read_b64_tr_b16 v[188:189], v225 offset:26120\n" \
"v_mfma_f32_16x16x32_bf16 v[104:107], v[144:147], a[172:175], v[104:107]\n" \
"ds_read_b64_tr_b16 v[190:191], v225 offset:26376\n" \
"v_mfma_f32_16x16x32_bf16 v[104:107], v[148:151], a[188:191], v[104:107]\n" \
"v_mfma_f32_16x16x32_bf16 v[12:15], v[152:155], a[160:163], v[12:15]\n" \
"buffer_load_dwordx4 v213, %[srdk], s48 offen offset:2048 lds\n" \
"v_mfma_f32_16x16x32_bf16 v[12:15], v[156:159], a[176:179], v[12:15]\n" \
"v_mfma_f32_16x16x32_bf16 v[44:47], v[152:155], a[164:167], v[44:47]\n" \
"v_mfma_f32_16x16x32_bf16 v[44:47], v[156:159], a[180:183], v[44:47]\n" \
"v_mfma_f32_16x16x32_bf16 v[76:79], v[152:155], a[168:171], v[76:79]\n" \
"buffer_load_dwordx4 v214, %[srdk], s48 offen offset:3072 lds\n" \
"v_mfma_f32_16x16x32_bf16 v[76:79], v[156:159], a[184:187], v[76:79]\n" \
"v_mfma_f32_16x16x32_bf16 v[108:111], v[152:155], a[172:175], v[108:111]\n" \
"v_mfma_f32_16x16x32_bf16 v[108:111], v[156:159], a[188:191], v[108:111]\n" \
"s_waitcnt vmcnt(30) lgkmcnt(0)\n" \
"s_barrier\n" \
"v_mfma_f32_16x16x32_bf16 v[16:19], v[160:163], a[160:163], v[16:19]\n" \
"ds_read_b64_tr_b16 v[128:129], v225 offset:32768\n" \
"v_mfma_f32_16x16x32_bf16 v[16:19], v[164:167], a[176:179], v[16:19]\n" \
"ds_read_b64_tr_b16 v[130:131], v225 offset:33024\n" \
"v_mfma_f32_16x16x32_bf16 v[48:51], v[160:163], a[164:167], v[48:51]\n" \
"s_mov_b32 s48, 32768\n" \
"s_mov_b32 m0, s45\n" \
"s_nop 0\n" \
"buffer_load_dwordx4 %[vd0], %[srdv], s48 offen offset:0 lds\n" \
"v_mfma_f32_16x16x32_bf16 v[48:51], v[164:167], a[180:183], v[48:51]\n" \
"ds_read_b64_tr_b16 v[132:133], v225 offset:40960\n" \
"v_mfma_f32_16x16x32_bf16 v[80:83], v[160:163], a[168:171], v[80:83]\n" \
"ds_read_b64_tr_b16 v[134:135], v225 offset:41216\n" \
"v_mfma_f32_16x16x32_bf16 v[80:83], v[164:167], a[184:187], v[80:83]\n" \
"v_mfma_f32_16x16x32_bf16 v[112:115], v[160:163], a[172:175], v[112:115]\n" \
"ds_read_b64_tr_b16 v[136:137], v225 offset:32776\n" \
"v_mfma_f32_16x16x32_bf16 v[112:115], v[164:167], a[188:191], v[112:115]\n" \
"ds_read_b64_tr_b16 v[138:139], v225 offset:33032\n" \
"v_mfma_f32_16x16x32_bf16 v[20:23], v[168:171], a[160:163], v[20:23]\n" \
"buffer_load_dwordx4 v215, %[srdv], s48 offen offset:1024 lds\n" \
"v_mfma_f32_16x16x32_bf16 v[20:23], v[172:175], a[176:179], v[20:23]\n" \
"ds_read_b64_tr_b16 v[140:141], v225 offset:40968\n" \
"v_mfma_f32_16x16x32_bf16 v[52:55], v[168:171], a[164:167], v[52:55]\n" \
"ds_read_b64_tr_b16 v[142:143], v225 offset:41224\n" \
"v_mfma_f32_16x16x32_bf16 v[52:55], v[172:175], a[180:183], v[52:55]\n" \
"v_mfma_f32_16x16x32_bf16 v[84:87], v[168:171], a[168:171], v[84:87]\n" \
"ds_read_b64_tr_b16 v[144:145], v225 offset:33280\n" \
"v_mfma_f32_16x16x32_bf16 v[84:87], v[172:175], a[184:187], v[84:87]\n" \
"ds_read_b64_tr_b16 v[146:147], v225 offset:33536\n" \
"v_mfma_f32_16x16x32_bf16 v[116:119], v[168:171], a[172:175], v[116:119]\n" \
"s_mov_b32 s48, 16384\n" \
"s_mov_b32 m0, s41\n" \
"s_nop 0\n" \
"buffer_load_dwordx4 %[kd0], %[srdk], s48 offen offset:0 lds\n" \
"v_mfma_f32_16x16x32_bf16 v[116:119], v[172:175], a[188:191], v[116:119]\n" \
"ds_read_b64_tr_b16 v[148:149], v225 offset:41472\n" \
"v_mfma_f32_16x16x32_bf16 v[24:27], v[176:179], a[160:163], v[24:27]\n" \
"ds_read_b64_tr_b16 v[150:151], v225 offset:41728\n" \
"v_mfma_f32_16x16x32_bf16 v[24:27], v[180:183], a[176:179], v[24:27]\n" \
"s_waitcnt vmcnt(18)\n" \
"v_cmp_eq_u64_e32 vcc, v[230:231], v[234:235]\n" \
"v_cmp_eq_u64_e64 s[56:57], v[232:233], v[234:235]\n" \
"s_nop 3\n" \
"s_or_b64 s[58:59], vcc, s[56:57]\n" \
"s_cbranch_scc0 Lnsp_%=\n" \
"Lnbk_%=:\n" \
"v_mfma_f32_16x16x32_bf16 v[56:59], v[176:179], a[164:167], v[56:59]\n" \
"ds_read_b64_tr_b16 v[152:153], v225 offset:33288\n" \
"v_mfma_f32_16x16x32_bf16 v[56:59], v[180:183], a[180:183], v[56:59]\n" \
"ds_read_b64_tr_b16 v[154:155], v225 offset:33544\n" \
"s_cmp_lg_u64 s[56:57], 0\n" \
"s_cselect_b32 %[cx], 0x2000, 0\n" \
"s_xor_b32 %[sin], %[sin], %[cx]\n" \
"v_mfma_f32_16x16x32_bf16 v[88:91], v[176:179], a[168:171], v[88:91]\n" \
"buffer_load_dwordx4 v212, %[srdk], s48 offen offset:1024 lds\n" \
"v_mfma_f32_16x16x32_bf16 v[88:91], v[180:183], a[184:187], v[88:91]\n" \
"ds_read_b64_tr_b16 v[156:157], v225 offset:41480\n" \
"v_mfma_f32_16x16x32_bf16 v[120:123], v[176:179], a[172:175], v[120:123]\n" \
"ds_read_b64_tr_b16 v[158:159], v225 offset:41736\n" \
"v_mfma_f32_16x16x32_bf16 v[120:123], v[180:183], a[188:191], v[120:123]\n" \
"s_mov_b64 exec, 0x2c4000\n" \
"v_xor_b32_e32 %[tlo], %[cx], %[tlo]\n" \
"s_mov_b64 exec, 0x40000\n" \
"v_xor_b32_e32 %[thi], %[cx], %[thi]\n" \
"s_mov_b64 exec, -1\n" \
"v_mfma_f32_16x16x32_bf16 v[28:31], v[184:187], a[160:163], v[28:31]\n" \
"buffer_load_dwordx4 v213, %[srdk], s48 offen offset:2048 lds\n" \
"v_mfma_f32_16x16x32_bf16 v[28:31], v[188:191], a[176:179], v[28:31]\n" \
"v_xor_b32_e32 %[oq0], %[cx], %[oq0]\n" \
"v_mfma_f32_16x16x32_bf16 v[60:63], v[184:187], a[164:167], v[60:63]\n" \
"v_mfma_f32_16x16x32_bf16 v[60:63], v[188:191], a[180:183], v[60:63]\n" \
"v_mfma_f32_16x16x32_bf16 v[92:95], v[184:187], a[168:171], v[92:95]\n" \
"buffer_load_dwordx4 v214, %[srdk], s48 offen offset:3072 lds\n" \
"v_mfma_f32_16x16x32_bf16 v[92:95], v[188:191], a[184:187], v[92:95]\n" \
"v_mfma_f32_16x16x32_bf16 v[124:127], v[184:187], a[172:175], v[124:127]\n" \
"v_mfma_f32_16x16x32_bf16 v[124:127], v[188:191], a[188:191], v[124:127]\n" \
"s_waitcnt vmcnt(25) lgkmcnt(0)\n" \
"v_mfma_f32_16x16x32_bf16 v[0:3], v[128:131], a[192:195], v[0:3]\n" \
"ds_read_b64_tr_b16 v[160:161], v225 offset:33792\n" \
"v_mfma_f32_16x16x32_bf16 v[0:3], v[132:135], a[208:211], v[0:3]\n" \
"ds_read_b64_tr_b16 v[162:163], v225 offset:34048\n" \
"v_mfma_f32_16x16x32_bf16 v[32:35], v[128:131], a[196:199], v[32:35]\n" \
"s_mov_b32 s48, 32768\n" \
"s_mov_b32 m0, s45\n" \
"s_nop 0\n" \
"buffer_load_dwordx4 %[vd0], %[srdv], s48 offen offset:2048 lds\n" \
"v_mfma_f32_16x16x32_bf16 v[32:35], v[132:135], a[212:215], v[32:35]\n" \
"ds_read_b64_tr_b16 v[164:165], v225 offset:41984\n" \
"v_mfma_f32_16x16x32_bf16 v[64:67], v[128:131], a[200:203], v[64:67]\n" \
"ds_read_b64_tr_b16 v[166:167], v225 offset:42240\n" \
"v_mfma_f32_16x16x32_bf16 v[64:67], v[132:135], a[216:219], v[64:67]\n" \
"v_mfma_f32_16x16x32_bf16 v[96:99], v[128:131], a[204:207], v[96:99]\n" \
"ds_read_b64_tr_b16 v[168:169], v225 offset:33800\n" \
"v_mfma_f32_16x16x32_bf16 v[96:99], v[132:135], a[220:223], v[96:99]\n" \
"ds_read_b64_tr_b16 v[170:171], v225 offset:34056\n" \
"v_mfma_f32_16x16x32_bf16 v[4:7], v[136:139], a[192:195], v[4:7]\n" \
"buffer_load_dwordx4 v215, %[srdv], s48 offen offset:3072 lds\n" \
"v_mfma_f32_16x16x32_bf16 v[4:7], v[140:143], a[208:211], v[4:7]\n" \
"ds_read_b64_tr_b16 v[172:173], v225 offset:41992\n" \
"v_mfma_f32_16x16x32_bf16 v[36:39], v[136:139], a[196:199], v[36:39]\n" \
"ds_read_b64_tr_b16 v[174:175], v225 offset:42248\n" \
"v_mfma_f32_16x16x32_bf16 v[36:39], v[140:143], a[212:215], v[36:39]\n" \
"v_mfma_f32_16x16x32_bf16 v[68:71], v[136:139], a[200:203], v[68:71]\n" \
"ds_read_b64_tr_b16 v[176:177], v225 offset:34304\n" \
"v_mfma_f32_16x16x32_bf16 v[68:71], v[140:143], a[216:219], v[68:71]\n" \
"ds_read_b64_tr_b16 v[178:179], v225 offset:34560\n" \
"v_mfma_f32_16x16x32_bf16 v[100:103], v[136:139], a[204:207], v[100:103]\n" \
"s_mov_b32 s48, 32768\n" \
"s_mov_b32 m0, s42\n" \
"s_nop 0\n" \
"buffer_load_dwordx4 %[kd0], %[srdk], s48 offen offset:0 lds\n" \
"v_mfma_f32_16x16x32_bf16 v[100:103], v[140:143], a[220:223], v[100:103]\n" \
"ds_read_b64_tr_b16 v[180:181], v225 offset:42496\n" \
"v_mfma_f32_16x16x32_bf16 v[8:11], v[144:147], a[192:195], v[8:11]\n" \
"ds_read_b64_tr_b16 v[182:183], v225 offset:42752\n" \
"v_mfma_f32_16x16x32_bf16 v[8:11], v[148:151], a[208:211], v[8:11]\n" \
"v_mfma_f32_16x16x32_bf16 v[40:43], v[144:147], a[196:199], v[40:43]\n" \
"ds_read_b64_tr_b16 v[184:185], v225 offset:34312\n" \
"v_mfma_f32_16x16x32_bf16 v[40:43], v[148:151], a[212:215], v[40:43]\n" \
"ds_read_b64_tr_b16 v[186:187], v225 offset:34568\n" \
"v_mfma_f32_16x16x32_bf16 v[72:75], v[144:147], a[200:203], v[72:75]\n" \
"buffer_load_dwordx4 v212, %[srdk], s48 offen offset:1024 lds\n" \
"v_mfma_f32_16x16x32_bf16 v[72:75], v[148:151], a[216:219], v[72:75]\n" \
"ds_read_b64_tr_b16 v[188:189], v225 offset:42504\n" \
"v_mfma_f32_16x16x32_bf16 v[104:107], v[144:147], a[204:207], v[104:107]\n" \
"ds_read_b64_tr_b16 v[190:191], v225 offset:42760\n" \
"v_mfma_f32_16x16x32_bf16 v[104:107], v[148:151], a[220:223], v[104:107]\n" \
"v_mfma_f32_16x16x32_bf16 v[12:15], v[152:155], a[192:195], v[12:15]\n" \
"buffer_load_dwordx4 v213, %[srdk], s48 offen offset:2048 lds\n" \
"v_mfma_f32_16x16x32_bf16 v[12:15], v[156:159], a[208:211], v[12:15]\n" \
"v_mfma_f32_16x16x32_bf16 v[44:47], v[152:155], a[196:199], v[44:47]\n" \
"v_mfma_f32_16x16x32_bf16 v[44:47], v[156:159], a[212:215], v[44:47]\n" \
"v_mfma_f32_16x16x32_bf16 v[76:79], v[152:155], a[200:203], v[76:79]\n" \
"buffer_load_dwordx4 v214, %[srdk], s48 offen offset:3072 lds\n" \
"v_mfma_f32_16x16x32_bf16 v[76:79], v[156:159], a[216:219], v[76:79]\n" \
"v_mfma_f32_16x16x32_bf16 v[108:111], v[152:155], a[204:207], v[108:111]\n" \
"v_mfma_f32_16x16x32_bf16 v[108:111], v[156:159], a[220:223], v[108:111]\n" \
"s_waitcnt vmcnt(29) lgkmcnt(0)\n" \
"s_barrier\n" \
"v_mfma_f32_16x16x32_bf16 v[16:19], v[160:163], a[192:195], v[16:19]\n" \
"ds_read_b64_tr_b16 v[128:129], v225 offset:49152\n" \
"s_mov_b32 s54, 0x4000\n" \
"v_mfma_f32_16x16x32_bf16 v[16:19], v[164:167], a[208:211], v[16:19]\n" \
"ds_read_b64_tr_b16 v[130:131], v225 offset:49408\n" \
"v_mfma_f32_16x16x32_bf16 v[48:51], v[160:163], a[196:199], v[48:51]\n" \
"s_mov_b32 s48, 49152\n" \
"s_mov_b32 m0, s46\n" \
"s_nop 0\n" \
"buffer_load_dwordx4 %[vd0], %[srdv], s48 offen offset:0 lds\n" \
"v_mbcnt_lo_u32_b32 v231, -1, 0\n" \
"v_mfma_f32_16x16x32_bf16 v[48:51], v[164:167], a[212:215], v[48:51]\n" \
"ds_read_b64_tr_b16 v[132:133], v225 offset:57344\n" \
"v_mfma_f32_16x16x32_bf16 v[80:83], v[160:163], a[200:203], v[80:83]\n" \
"ds_read_b64_tr_b16 v[134:135], v225 offset:57600\n" \
"v_mfma_f32_16x16x32_bf16 v[80:83], v[164:167], a[216:219], v[80:83]\n" \
"v_mbcnt_hi_u32_b32 v231, -1, v231\n" \
"v_mfma_f32_16x16x32_bf16 v[112:115], v[160:163], a[204:207], v[112:115]\n" \
"ds_read_b64_tr_b16 v[136:137], v225 offset:49160\n" \
"v_mfma_f32_16x16x32_bf16 v[112:115], v[164:167], a[220:223], v[112:115]\n" \
"ds_read_b64_tr_b16 v[138:139], v225 offset:49416\n" \
"v_mfma_f32_16x16x32_bf16 v[20:23], v[168:171], a[192:195], v[20:23]\n" \
"buffer_load_dwordx4 v215, %[srdv], s48 offen offset:1024 lds\n" \
"v_lshrrev_b32_e64 v232, 4, v231\n" \
"v_mfma_f32_16x16x32_bf16 v[20:23], v[172:175], a[208:211], v[20:23]\n" \
"ds_read_b64_tr_b16 v[140:141], v225 offset:57352\n" \
"v_mfma_f32_16x16x32_bf16 v[52:55], v[168:171], a[196:199], v[52:55]\n" \
"ds_read_b64_tr_b16 v[142:143], v225 offset:57608\n" \
"v_xor_b32_e64 v232, v232, v231\n" \
"v_mfma_f32_16x16x32_bf16 v[52:55], v[172:175], a[212:215], v[52:55]\n" \
"v_mfma_f32_16x16x32_bf16 v[84:87], v[168:171], a[200:203], v[84:87]\n" \
"ds_read_b64_tr_b16 v[144:145], v225 offset:49664\n" \
"v_mfma_f32_16x16x32_bf16 v[84:87], v[172:175], a[216:219], v[84:87]\n" \
"ds_read_b64_tr_b16 v[146:147], v225 offset:49920\n" \
"v_bfe_i32 v232, v232, 1, 1\n" \
"v_mfma_f32_16x16x32_bf16 v[116:119], v[168:171], a[204:207], v[116:119]\n" \
"s_mov_b32 s48, 49152\n" \
"s_mov_b32 m0, s43\n" \
"s_nop 0\n" \
"buffer_load_dwordx4 %[kd0], %[srdk], s48 offen offset:0 lds\n" \
"v_mfma_f32_16x16x32_bf16 v[116:119], v[172:175], a[220:223], v[116:119]\n" \
"ds_read_b64_tr_b16 v[148:149], v225 offset:57856\n" \
"v_mfma_f32_16x16x32_bf16 v[24:27], v[176:179], a[192:195], v[24:27]\n" \
"ds_read_b64_tr_b16 v[150:151], v225 offset:58112\n" \
"v_not_b32_e64 v232, v232\n" \
"v_mfma_f32_16x16x32_bf16 v[24:27], v[180:183], a[208:211], v[24:27]\n" \
"v_mfma_f32_16x16x32_bf16 v[56:59], v[176:179], a[196:199], v[56:59]\n" \
"ds_read_b64_tr_b16 v[152:153], v225 offset:49672\n" \
"v_mfma_f32_16x16x32_bf16 v[56:59], v[180:183], a[212:215], v[56:59]\n" \
"ds_read_b64_tr_b16 v[154:155], v225 offset:49928\n" \
"v_and_b32_e32 v230, 0x3f803f80, v232\n" \
"v_mfma_f32_16x16x32_bf16 v[88:91], v[176:179], a[200:203], v[88:91]\n" \
"buffer_load_dwordx4 v212, %[srdk], s48 offen offset:1024 lds\n" \
"v_mfma_f32_16x16x32_bf16 v[88:91], v[180:183], a[216:219], v[88:91]\n" \
"ds_read_b64_tr_b16 v[156:157], v225 offset:57864\n" \
"v_readlane_b32 s62, %[tlo], 18\n" \
"v_mfma_f32_16x16x32_bf16 v[120:123], v[176:179], a[204:207], v[120:123]\n" \
"ds_read_b64_tr_b16 v[158:159], v225 offset:58120\n" \
"v_mfma_f32_16x16x32_bf16 v[120:123], v[180:183], a[220:223], v[120:123]\n" \
"v_mfma_f32_16x16x32_bf16 v[28:31], v[184:187], a[192:195], v[28:31]\n" \
"buffer_load_dwordx4 v213, %[srdk], s48 offen offset:2048 lds\n" \
"v_readlane_b32 s61, %[tlo], 21\n" \
"v_mfma_f32_16x16x32_bf16 v[28:31], v[188:191], a[208:211], v[28:31]\n" \
"v_mfma_f32_16x16x32_bf16 v[60:63], v[184:187], a[196:199], v[60:63]\n" \
"v_mfma_f32_16x16x32_bf16 v[60:63], v[188:191], a[212:215], v[60:63]\n" \
"v_lshlrev_b32_e64 v224, 7, v231\n" \
"v_mfma_f32_16x16x32_bf16 v[92:95], v[184:187], a[200:203], v[92:95]\n" \
"buffer_load_dwordx4 v214, %[srdk], s48 offen offset:3072 lds\n" \
"v_mfma_f32_16x16x32_bf16 v[92:95], v[188:191], a[216:219], v[92:95]\n" \
"v_mfma_f32_16x16x32_bf16 v[124:127], v[184:187], a[204:207], v[124:127]\n" \
"v_mfma_f32_16x16x32_bf16 v[124:127], v[188:191], a[220:223], v[124:127]\n" \
"s_waitcnt vmcnt(24) lgkmcnt(0)\n" \
"v_mfma_f32_16x16x32_bf16 v[0:3], v[128:131], a[224:227], v[0:3]\n" \
"ds_read_b64_tr_b16 v[160:161], v225 offset:50176\n" \
"v_accvgpr_write_b32 a192, v230\n" \
"v_mfma_f32_16x16x32_bf16 v[0:3], v[132:135], a[240:243], v[0:3]\n" \
"ds_read_b64_tr_b16 v[162:163], v225 offset:50432\n" \
"v_mfma_f32_16x16x32_bf16 v[32:35], v[128:131], a[228:231], v[32:35]\n" \
"s_mov_b32 s48, 49152\n" \
"s_mov_b32 m0, s46\n" \
"s_nop 0\n" \
"buffer_load_dwordx4 %[vd0], %[srdv], s48 offen offset:2048 lds\n" \
"v_accvgpr_write_b32 a193, v230\n" \
"v_mfma_f32_16x16x32_bf16 v[32:35], v[132:135], a[244:247], v[32:35]\n" \
"ds_read_b64_tr_b16 v[164:165], v225 offset:58368\n" \
"v_mfma_f32_16x16x32_bf16 v[64:67], v[128:131], a[232:235], v[64:67]\n" \
"ds_read_b64_tr_b16 v[166:167], v225 offset:58624\n" \
"v_accvgpr_write_b32 a194, v230\n" \
"v_mfma_f32_16x16x32_bf16 v[64:67], v[132:135], a[248:251], v[64:67]\n" \
"v_mfma_f32_16x16x32_bf16 v[96:99], v[128:131], a[236:239], v[96:99]\n" \
"ds_read_b64_tr_b16 v[168:169], v225 offset:50184\n" \
"v_accvgpr_write_b32 a195, v230\n" \
"v_mfma_f32_16x16x32_bf16 v[96:99], v[132:135], a[252:255], v[96:99]\n" \
"ds_read_b64_tr_b16 v[170:171], v225 offset:50440\n" \
"v_mfma_f32_16x16x32_bf16 v[4:7], v[136:139], a[224:227], v[4:7]\n" \
"buffer_load_dwordx4 v215, %[srdv], s48 offen offset:3072 lds\n" \
"v_accvgpr_write_b32 a196, -1\n" \
"v_mfma_f32_16x16x32_bf16 v[4:7], v[140:143], a[240:243], v[4:7]\n" \
"ds_read_b64_tr_b16 v[172:173], v225 offset:58376\n" \
"v_mfma_f32_16x16x32_bf16 v[36:39], v[136:139], a[228:231], v[36:39]\n" \
"ds_read_b64_tr_b16 v[174:175], v225 offset:58632\n" \
"v_accvgpr_write_b32 a197, -1\n" \
"v_mfma_f32_16x16x32_bf16 v[36:39], v[140:143], a[244:247], v[36:39]\n" \
"v_mfma_f32_16x16x32_bf16 v[68:71], v[136:139], a[232:235], v[68:71]\n" \
"ds_read_b64_tr_b16 v[176:177], v225 offset:50688\n" \
"v_accvgpr_write_b32 a198, -1\n" \
"v_mfma_f32_16x16x32_bf16 v[68:71], v[140:143], a[248:251], v[68:71]\n" \
"ds_read_b64_tr_b16 v[178:179], v225 offset:50944\n" \
"v_mfma_f32_16x16x32_bf16 v[100:103], v[136:139], a[236:239], v[100:103]\n" \
"v_accvgpr_write_b32 a199, -1\n" \
"v_mfma_f32_16x16x32_bf16 v[100:103], v[140:143], a[252:255], v[100:103]\n" \
"ds_read_b64_tr_b16 v[180:181], v225 offset:58880\n" \
"v_mfma_f32_16x16x32_bf16 v[8:11], v[144:147], a[224:227], v[8:11]\n" \
"ds_read_b64_tr_b16 v[182:183], v225 offset:59136\n" \
"v_lshrrev_b32_e32 v231, 3, v224\n" \
"v_mfma_f32_16x16x32_bf16 v[8:11], v[148:151], a[240:243], v[8:11]\n" \
"v_mfma_f32_16x16x32_bf16 v[40:43], v[144:147], a[228:231], v[40:43]\n" \
"ds_read_b64_tr_b16 v[184:185], v225 offset:50696\n" \
"v_mov_b32_e64 v232, -1\n" \
"v_mfma_f32_16x16x32_bf16 v[40:43], v[148:151], a[244:247], v[40:43]\n" \
"ds_read_b64_tr_b16 v[186:187], v225 offset:50952\n" \
"v_mfma_f32_16x16x32_bf16 v[72:75], v[144:147], a[232:235], v[72:75]\n" \
"v_mov_b32_e64 v233, -1\n" \
"v_mfma_f32_16x16x32_bf16 v[72:75], v[148:151], a[248:251], v[72:75]\n" \
"ds_read_b64_tr_b16 v[188:189], v225 offset:58888\n" \
"v_mfma_f32_16x16x32_bf16 v[104:107], v[144:147], a[236:239], v[104:107]\n" \
"ds_read_b64_tr_b16 v[190:191], v225 offset:59144\n" \
"v_mov_b32_e64 v234, -1\n" \
"v_mfma_f32_16x16x32_bf16 v[104:107], v[148:151], a[252:255], v[104:107]\n" \
"v_mfma_f32_16x16x32_bf16 v[12:15], v[152:155], a[224:227], v[12:15]\n" \
"v_mov_b32_e64 v235, -1\n" \
"v_mfma_f32_16x16x32_bf16 v[12:15], v[156:159], a[240:243], v[12:15]\n" \
"v_mfma_f32_16x16x32_bf16 v[44:47], v[152:155], a[228:231], v[44:47]\n" \
"buffer_store_dwordx4 v[232:235], v231, %[srdz], s61 offen offset:0 sc1\n" \
"v_mfma_f32_16x16x32_bf16 v[44:47], v[156:159], a[244:247], v[44:47]\n" \
"v_mfma_f32_16x16x32_bf16 v[76:79], v[152:155], a[232:235], v[76:79]\n" \
"buffer_store_dwordx4 v[232:235], v231, %[srdz], s61 offen offset:1024 sc1\n" \
"v_mfma_f32_16x16x32_bf16 v[76:79], v[156:159], a[248:251], v[76:79]\n" \
"v_mfma_f32_16x16x32_bf16 v[108:111], v[152:155], a[236:239], v[108:111]\n" \
"v_mfma_f32_16x16x32_bf16 v[108:111], v[156:159], a[252:255], v[108:111]\n" \
"s_waitcnt vmcnt(22) lgkmcnt(0)\n" \
"s_barrier\n" \
"v_mfma_f32_16x16x32_bf16 v[16:19], v[160:163], a[224:227], v[16:19]\n" \
"ds_read_b128 a[64:67], %[kb0] offset:0\n" \
"v_mfma_f32_16x16x32_bf16 v[16:19], v[164:167], a[240:243], v[16:19]\n" \
"v_mfma_f32_16x16x32_bf16 v[20:23], v[168:171], a[224:227], v[20:23]\n" \
"v_cvt_pk_bf16_f32 v230, v0, v1\n" \
"v_cvt_pk_bf16_f32 v231, v2, v3\n" \
"v_cvt_pk_bf16_f32 v232, v4, v5\n" \
"ds_read_b128 a[68:71], v208 offset:0\n" \
"v_mfma_f32_16x16x32_bf16 v[20:23], v[172:175], a[240:243], v[20:23]\n" \
"v_cvt_pk_bf16_f32 v233, v6, v7\n" \
"v_accvgpr_write_b32 a0, v230\n" \
".if . - adaln_body != 8192\n" \
".error \"size model\"\n" \
".endif\n" \
"Lk1_%=:\n" \
"s_cbranch_execz Lk2_%=\n" \
"s_nop 0\n" \
"v_accvgpr_write_b32 a1, v231\n" \
"v_mfma_f32_16x16x32_bf16 v[24:27], v[176:179], a[224:227], v[24:27]\n" \
"v_accvgpr_write_b32 a2, v232\n" \
"v_accvgpr_write_b32 a3, v233\n" \
"v_cvt_pk_bf16_f32 v234, v8, v9\n" \
"ds_read_b128 a[72:75], v209 offset:0\n" \
"v_mfma_f32_16x16x32_bf16 v[24:27], v[180:183], a[240:243], v[24:27]\n" \
"v_cvt_pk_bf16_f32 v235, v10, v11\n" \
"v_cvt_pk_bf16_f32 v236, v12, v13\n" \
"v_cvt_pk_bf16_f32 v237, v14, v15\n" \
"v_mfma_f32_16x16x32_bf16 v[28:31], v[184:187], a[224:227], v[28:31]\n" \
"v_accvgpr_write_b32 a4, v234\n" \
"v_accvgpr_write_b32 a5, v235\n" \
"v_accvgpr_write_b32 a6, v236\n" \
"ds_read_b128 a[76:79], v210 offset:0\n" \
"v_mfma_f32_16x16x32_bf16 v[28:31], v[188:191], a[240:243], v[28:31]\n" \
"v_accvgpr_write_b32 a7, v237\n" \
"v_cvt_pk_bf16_f32 v230, v32, v33\n" \
"v_cvt_pk_bf16_f32 v231, v34, v35\n" \
"v_mfma_f32_16x16x32_bf16 v[48:51], v[160:163], a[228:231], v[48:51]\n" \
"v_cvt_pk_bf16_f32 v232, v36, v37\n" \
"v_cvt_pk_bf16_f32 v233, v38, v39\n" \
"v_accvgpr_write_b32 a16, v230\n" \
"ds_read_b128 a[92:95], v210 offset:4096\n" \
"v_mfma_f32_16x16x32_bf16 v[48:51], v[164:167], a[244:247], v[48:51]\n" \
"v_accvgpr_write_b32 a17, v231\n" \
"v_accvgpr_write_b32 a18, v232\n" \
"v_accvgpr_write_b32 a19, v233\n" \
"v_mfma_f32_16x16x32_bf16 v[52:55], v[168:171], a[228:231], v[52:55]\n" \
"v_cvt_pk_bf16_f32 v234, v40, v41\n" \
"v_cvt_pk_bf16_f32 v235, v42, v43\n" \
"v_cvt_pk_bf16_f32 v236, v44, v45\n" \
"ds_read_b128 a[88:91], v209 offset:4096\n" \
"v_mfma_f32_16x16x32_bf16 v[52:55], v[172:175], a[244:247], v[52:55]\n" \
"v_cvt_pk_bf16_f32 v237, v46, v47\n" \
"v_accvgpr_write_b32 a20, v234\n" \
"v_accvgpr_write_b32 a21, v235\n" \
"v_mfma_f32_16x16x32_bf16 v[56:59], v[176:179], a[228:231], v[56:59]\n" \
"v_accvgpr_write_b32 a22, v236\n" \
"v_accvgpr_write_b32 a23, v237\n" \
"v_cvt_pk_bf16_f32 v230, v16, v17\n" \
"ds_read_b128 a[84:87], v208 offset:4096\n" \
"v_mfma_f32_16x16x32_bf16 v[56:59], v[180:183], a[244:247], v[56:59]\n" \
"v_cvt_pk_bf16_f32 v231, v18, v19\n" \
"v_cvt_pk_bf16_f32 v232, v20, v21\n" \
"v_cvt_pk_bf16_f32 v233, v22, v23\n" \
"v_mfma_f32_16x16x32_bf16 v[60:63], v[184:187], a[228:231], v[60:63]\n" \
"v_accvgpr_write_b32 a8, v230\n" \
"v_accvgpr_write_b32 a9, v231\n" \
"v_accvgpr_write_b32 a10, v232\n" \
"ds_read_b128 a[80:83], %[kb0] offset:4096\n" \
"v_mfma_f32_16x16x32_bf16 v[60:63], v[188:191], a[244:247], v[60:63]\n" \
"v_accvgpr_write_b32 a11, v233\n" \
"v_cvt_pk_bf16_f32 v234, v24, v25\n" \
"v_cvt_pk_bf16_f32 v235, v26, v27\n" \
"v_mfma_f32_16x16x32_bf16 v[80:83], v[160:163], a[232:235], v[80:83]\n" \
"v_cvt_pk_bf16_f32 v236, v28, v29\n" \
"v_cvt_pk_bf16_f32 v237, v30, v31\n" \
"v_accvgpr_write_b32 a12, v234\n" \
"v_mfma_f32_16x16x32_bf16 v[80:83], v[164:167], a[248:251], v[80:83]\n" \
"v_accvgpr_write_b32 a13, v235\n" \
"v_accvgpr_write_b32 a14, v236\n" \
"v_accvgpr_write_b32 a15, v237\n" \
"v_mfma_f32_16x16x32_bf16 v[84:87], v[168:171], a[232:235], v[84:87]\n" \
"v_cvt_pk_bf16_f32 v230, v64, v65\n" \
"v_cvt_pk_bf16_f32 v231, v66, v67\n" \
"v_cvt_pk_bf16_f32 v232, v68, v69\n" \
"v_mfma_f32_16x16x32_bf16 v[84:87], v[172:175], a[248:251], v[84:87]\n" \
"v_cvt_pk_bf16_f32 v233, v70, v71\n" \
"v_accvgpr_write_b32 a32, v230\n" \
"v_accvgpr_write_b32 a33, v231\n" \
"v_mfma_f32_16x16x32_bf16 v[88:91], v[176:179], a[232:235], v[88:91]\n" \
"v_accvgpr_write_b32 a34, v232\n" \
"v_accvgpr_write_b32 a35, v233\n" \
"v_cvt_pk_bf16_f32 v234, v72, v73\n" \
"v_mfma_f32_16x16x32_bf16 v[88:91], v[180:183], a[248:251], v[88:91]\n" \
"v_cvt_pk_bf16_f32 v235, v74, v75\n" \
"v_cvt_pk_bf16_f32 v236, v76, v77\n" \
"v_cvt_pk_bf16_f32 v237, v78, v79\n" \
"v_mfma_f32_16x16x32_bf16 v[92:95], v[184:187], a[232:235], v[92:95]\n" \
"v_accvgpr_write_b32 a36, v234\n" \
"v_accvgpr_write_b32 a37, v235\n" \
"v_accvgpr_write_b32 a38, v236\n" \
"v_mfma_f32_16x16x32_bf16 v[92:95], v[188:191], a[248:251], v[92:95]\n" \
"v_accvgpr_write_b32 a39, v237\n" \
"v_cvt_pk_bf16_f32 v230, v48, v49\n" \
"v_cvt_pk_bf16_f32 v231, v50, v51\n" \
"v_mfma_f32_16x16x32_bf16 v[112:115], v[160:163], a[236:239], v[112:115]\n" \
"v_cvt_pk_bf16_f32 v232, v52, v53\n" \
"v_cvt_pk_bf16_f32 v233, v54, v55\n" \
"v_accvgpr_write_b32 a24, v230\n" \
"v_mfma_f32_16x16x32_bf16 v[112:115], v[164:167], a[252:255], v[112:115]\n" \
"v_accvgpr_write_b32 a25, v231\n" \
"v_accvgpr_write_b32 a26, v232\n" \
"v_accvgpr_write_b32 a27, v233\n" \
"v_mfma_f32_16x16x32_bf16 v[116:119], v[168:171], a[236:239], v[116:119]\n" \
"v_cvt_pk_bf16_f32 v234, v56, v57\n" \
"v_cvt_pk_bf16_f32 v235, v58, v59\n" \
"v_cvt_pk_bf16_f32 v236, v60, v61\n" \
"v_mfma_f32_16x16x32_bf16 v[116:119], v[172:175], a[252:255], v[116:119]\n" \
"v_cvt_pk_bf16_f32 v237, v62, v63\n" \
"v_accvgpr_write_b32 a28, v234\n" \
"v_accvgpr_write_b32 a29, v235\n" \
"v_mfma_f32_16x16x32_bf16 v[120:123], v[176:179], a[236:239], v[120:123]\n" \
"v_accvgpr_write_b32 a30, v236\n" \
"v_accvgpr_write_b32 a31, v237\n" \
"v_cvt_pk_bf16_f32 v230, v96, v97\n" \
"v_mfma_f32_16x16x32_bf16 v[120:123], v[180:183], a[252:255], v[120:123]\n" \
"v_cvt_pk_bf16_f32 v231, v98, v99\n" \
"v_cvt_pk_bf16_f32 v232, v100, v101\n" \
"v_cvt_pk_bf16_f32 v233, v102, v103\n" \
"v_mfma_f32_16x16x32_bf16 v[124:127], v[184:187], a[236:239], v[124:127]\n" \
"v_accvgpr_write_b32 a48, v230\n" \
"v_accvgpr_write_b32 a49, v231\n" \
"v_accvgpr_write_b32 a50, v232\n" \
"v_mfma_f32_16x16x32_bf16 v[124:127], v[188:191], a[252:255], v[124:127]\n" \
"v_accvgpr_write_b32 a51, v233\n" \
"s_waitcnt vmcnt(63) lgkmcnt(0)\n" \
"s_nop 0\n" \
"s_mov_b32 s48, 65536\n" \
"s_mov_b32 s60, 0\n" \
"s_waitcnt lgkmcnt(0)\n" \
"v_mfma_f32_16x16x32_bf16 v[128:131], a[64:67], a[0:3], 0\n" \
"ds_read_b128 a[96:99], %[kb0] offset:8192\n" \
"v_cvt_pk_bf16_f32 v234, v80, v81\n" \
"v_cvt_pk_bf16_f32 v235, v82, v83\n" \
"v_mfma_f32_16x16x32_bf16 v[128:131], a[68:71], a[4:7], v[128:131]\n" \
"ds_read_b128 a[100:103], v208 offset:8192\n" \
"v_cvt_pk_bf16_f32 v236, v84, v85\n" \
"v_cvt_pk_bf16_f32 v237, v86, v87\n" \
"v_mfma_f32_16x16x32_bf16 v[128:131], a[72:75], a[8:11], v[128:131]\n" \
"ds_read_b128 a[104:107], v209 offset:8192\n" \
"v_accvgpr_write_b32 a40, v234\n" \
"v_accvgpr_write_b32 a41, v235\n" \
"v_mfma_f32_16x16x32_bf16 v[128:131], a[76:79], a[12:15], v[128:131]\n" \
"ds_read_b128 a[108:111], v210 offset:8192\n" \
"v_accvgpr_write_b32 a42, v236\n" \
"v_accvgpr_write_b32 a43, v237\n" \
"v_mfma_f32_16x16x32_bf16 v[132:135], a[92:95], a[12:15], 0\n" \
"ds_read_b128 a[124:127], v210 offset:12288\n" \
"v_cvt_pk_bf16_f32 v230, v88, v89\n" \
"v_cvt_pk_bf16_f32 v231, v90, v91\n" \
"v_mfma_f32_16x16x32_bf16 v[132:135], a[88:91], a[8:11], v[132:135]\n" \
"ds_read_b128 a[120:123], v209 offset:12288\n" \
"v_cvt_pk_bf16_f32 v232, v92, v93\n" \
"v_cvt_pk_bf16_f32 v233, v94, v95\n" \
"v_mfma_f32_16x16x32_bf16 v[132:135], a[84:87], a[4:7], v[132:135]\n" \
"ds_read_b128 a[116:119], v208 offset:12288\n" \
"v_accvgpr_write_b32 a44, v230\n" \
"v_accvgpr_write_b32 a45, v231\n" \
"v_mfma_f32_16x16x32_bf16 v[132:135], a[80:83], a[0:3], v[132:135]\n" \
"ds_read_b128 a[112:115], %[kb0] offset:12288\n" \
"v_accvgpr_write_b32 a46, v232\n" \
"v_accvgpr_write_b32 a47, v233\n" \
"v_mfma_f32_16x16x32_bf16 v[136:139], a[64:67], a[16:19], 0\n" \
"ds_read_b64_tr_b16 a[128:129], %[vvb] offset:0\n" \
"v_cvt_pk_bf16_f32 v234, v104, v105\n" \
"v_cvt_pk_bf16_f32 v235, v106, v107\n" \
"v_mfma_f32_16x16x32_bf16 v[136:139], a[68:71], a[20:23], v[136:139]\n" \
"ds_read_b64_tr_b16 a[130:131], %[vvb] offset:4096\n" \
"v_cvt_pk_bf16_f32 v236, v108, v109\n" \
"v_cvt_pk_bf16_f32 v237, v110, v111\n" \
"v_mfma_f32_16x16x32_bf16 v[136:139], a[72:75], a[24:27], v[136:139]\n" \
"ds_read_b64_tr_b16 a[132:133], v211 offset:0\n" \
"v_accvgpr_write_b32 a52, v234\n" \
"v_accvgpr_write_b32 a53, v235\n" \
"v_mfma_f32_16x16x32_bf16 v[136:139], a[76:79], a[28:31], v[136:139]\n" \
"ds_read_b64_tr_b16 a[134:135], v211 offset:4096\n" \
"v_accvgpr_write_b32 a54, v236\n" \
"v_accvgpr_write_b32 a55, v237\n" \
"v_mfma_f32_16x16x32_bf16 v[140:143], a[92:95], a[28:31], 0\n" \
"ds_read_b64_tr_b16 a[136:137], %[vvb] offset:512\n" \
"v_cvt_pk_bf16_f32 v230, v112, v113\n" \
"v_cvt_pk_bf16_f32 v231, v114, v115\n" \
"v_mfma_f32_16x16x32_bf16 v[140:143], a[88:91], a[24:27], v[140:143]\n" \
"ds_read_b64_tr_b16 a[138:139], %[vvb] offset:4608\n" \
"v_cvt_pk_bf16_f32 v232, v116, v117\n" \
"v_cvt_pk_bf16_f32 v233, v118, v119\n" \
"v_mfma_f32_16x16x32_bf16 v[140:143], a[84:87], a[20:23], v[140:143]\n" \
"ds_read_b64_tr_b16 a[140:141], v211 offset:512\n" \
"v_accvgpr_write_b32 a56, v230\n" \
"v_accvgpr_write_b32 a57, v231\n" \
"v_mfma_f32_16x16x32_bf16 v[140:143], a[80:83], a[16:19], v[140:143]\n" \
"ds_read_b64_tr_b16 a[142:143], v211 offset:4608\n" \
"v_accvgpr_write_b32 a58, v232\n" \
"v_accvgpr_write_b32 a59, v233\n" \
"v_mfma_f32_16x16x32_bf16 v[144:147], a[64:67], a[32:35], 0\n" \
"ds_read_b64_tr_b16 a[144:145], %[vvb] offset:1024\n" \
"v_cvt_pk_bf16_f32 v234, v120, v121\n" \
"v_cvt_pk_bf16_f32 v235, v122, v123\n" \
"v_mfma_f32_16x16x32_bf16 v[144:147], a[68:71], a[36:39], v[144:147]\n" \
"ds_read_b64_tr_b16 a[146:147], %[vvb] offset:5120\n" \
"v_cvt_pk_bf16_f32 v236, v124, v125\n" \
"v_cvt_pk_bf16_f32 v237, v126, v127\n" \
"v_mfma_f32_16x16x32_bf16 v[144:147], a[72:75], a[40:43], v[144:147]\n" \
"ds_read_b64_tr_b16 a[148:149], v211 offset:1024\n" \
"v_accvgpr_write_b32 a60, v234\n" \
"v_accvgpr_write_b32 a61, v235\n" \
"v_mfma_f32_16x16x32_bf16 v[144:147], a[76:79], a[44:47], v[144:147]\n" \
"ds_read_b64_tr_b16 a[150:151], v211 offset:5120\n" \
"v_accvgpr_write_b32 a62, v236\n" \
"v_accvgpr_write_b32 a63, v237\n" \
"v_mfma_f32_16x16x32_bf16 v[148:151], a[92:95], a[44:47], 0\n" \
"ds_read_b64_tr_b16 a[152:153], %[vvb] offset:1536\n" \
"v_max3_f32 v192, v128, v129, v130\n" \
"v_max3_f32 v193, v131, v132, v133\n" \
"v_mfma_f32_16x16x32_bf16 v[148:151], a[88:91], a[40:43], v[148:151]\n" \
"ds_read_b64_tr_b16 a[154:155], %[vvb] offset:5632\n" \
"v_max3_f32 v192, v192, v134, v135\n" \
"v_max_f32_e64 v225, v192, v193\n" \
"v_mfma_f32_16x16x32_bf16 v[148:151], a[84:87], a[36:39], v[148:151]\n" \
"ds_read_b64_tr_b16 a[156:157], v211 offset:1536\n" \
"v_max3_f32 v192, v136, v137, v138\n" \
"v_max3_f32 v193, v139, v140, v141\n" \
"v_mfma_f32_16x16x32_bf16 v[148:151], a[80:83], a[32:35], v[148:151]\n" \
"ds_read_b64_tr_b16 a[158:159], v211 offset:5632\n" \
"v_max3_f32 v192, v192, v142, v143\n" \
"v_max_f32_e64 v226, v192, v193\n" \
"v_mfma_f32_16x16x32_bf16 v[152:155], a[64:67], a[48:51], 0\n" \
"ds_read_b128 a[200:203], v228 offset:0\n" \
"s_mov_b32 s44, %[m0t]\n" \
"s_mov_b32 s45, 0\n" \
"v_mfma_f32_16x16x32_bf16 v[152:155], a[68:71], a[52:55], v[152:155]\n" \
"ds_read_b128 a[204:207], v228 offset:64\n" \
"v_mfma_f32_16x16x32_bf16 v[152:155], a[72:75], a[56:59], v[152:155]\n" \
"ds_read_b128 a[208:211], v228 offset:128\n" \
"v_mfma_f32_16x16x32_bf16 v[152:155], a[76:79], a[60:63], v[152:155]\n" \
"ds_read_b128 a[212:215], v228 offset:192\n" \
"v_mfma_f32_16x16x32_bf16 v[156:159], a[92:95], a[60:63], 0\n" \
"ds_read_b128 a[216:219], v228 offset:256\n" \
"v_mfma_f32_16x16x32_bf16 v[156:159], a[88:91], a[56:59], v[156:159]\n" \
"ds_read_b128 a[220:223], v228 offset:320\n" \
"v_mfma_f32_16x16x32_bf16 v[156:159], a[84:87], a[52:55], v[156:159]\n" \
"ds_read_b128 a[224:227], v228 offset:384\n" \
"v_max3_f32 v192, v144, v145, v146\n" \
"v_max3_f32 v193, v147, v148, v149\n" \
"v_mfma_f32_16x16x32_bf16 v[156:159], a[80:83], a[48:51], v[156:159]\n" \
"ds_read_b128 a[228:231], v228 offset:448\n" \
"v_max3_f32 v192, v192, v150, v151\n" \
"v_max_f32_e64 v227, v192, v193\n" \
"s_nop 3\n" \
"s_nop 0\n" \
"v_max3_f32 v230, v152, v153, v154\n" \
"v_max3_f32 v231, v155, v156, v157\n" \
"v_max3_f32 v230, v230, v158, v159\n" \
"v_max_f32_e32 v228, v230, v231\n" \
"v_mov_b32_e32 v232, v225\n" \
"v_mov_b32_e32 v233, v226\n" \
"v_mov_b32_e32 v234, v227\n" \
"v_mov_b32_e32 v235, v228\n" \
"s_nop 0\n" \
"v_permlane16_swap_b32_e32 v225, v232\n" \
"v_permlane16_swap_b32_e32 v226, v233\n" \
"v_permlane16_swap_b32_e32 v227, v234\n" \
"v_permlane16_swap_b32_e32 v228, v235\n" \
"v_max_f32_e32 v225, v225, v232\n" \
"v_max_f32_e32 v226, v226, v233\n" \
"v_max_f32_e32 v227, v227, v234\n" \
"v_max_f32_e32 v228, v228, v235\n" \
"v_mov_b32_e32 v232, v225\n" \
"v_mov_b32_e32 v233, v226\n" \
"v_mov_b32_e32 v234, v227\n" \
"v_mov_b32_e32 v235, v228\n" \
"s_nop 0\n" \
"v_permlane32_swap_b32_e32 v225, v232\n" \
"v_permlane32_swap_b32_e32 v226, v233\n" \
"v_permlane32_swap_b32_e32 v227, v234\n" \
"v_permlane32_swap_b32_e32 v228, v235\n" \
"v_max_f32_e32 v225, v225, v232\n" \
"v_max_f32_e32 v226, v226, v233\n" \
"v_max_f32_e32 v227, v227, v234\n" \
"v_max_f32_e64 v228, v228, v235\n" \
"v_add_f32_e32 v225, 0x41800000, v225\n" \
"v_add_f32_e32 v226, 0x41800000, v226\n" \
"v_add_f32_e32 v227, 0x41800000, v227\n" \
"v_add_f32_e32 v228, 0x41800000, v228\n" \
"Lhavem_%=:\n" \
"s_nop 3\n" \
"v_mov_b64_e32 v[176:177], 0\n" \
"v_mov_b64_e32 v[178:179], 0\n" \
"v_mov_b64_e32 v[180:181], 0\n" \
"v_mov_b64_e32 v[182:183], 0\n" \
"s_and_b32 s68, s52, s69\n" \
"s_cselect_b32 s68, 0, 0x39400000\n" \
"s_waitcnt lgkmcnt(14)\n" \
"v_mfma_f32_16x16x32_bf16 v[230:233], a[128:131], a[128:131], 0\n" \
"v_sub_f32_e32 v160, 0, v225\n" \
"v_sub_f32_e32 v161, 0, v225\n" \
"v_sub_f32_e32 v162, 0, v225\n" \
"v_sub_f32_e32 v163, 0, v225\n" \
"v_sub_f32_e32 v164, 0, v226\n" \
"v_sub_f32_e32 v165, 0, v226\n" \
"v_sub_f32_e32 v166, 0, v226\n" \
"v_sub_f32_e32 v167, 0, v226\n" \
"v_sub_f32_e32 v168, 0, v227\n" \
"v_sub_f32_e32 v169, 0, v227\n" \
"v_sub_f32_e32 v170, 0, v227\n" \
"v_sub_f32_e32 v171, 0, v227\n" \
"v_sub_f32_e32 v172, 0, v228\n" \
"v_sub_f32_e32 v173, 0, v228\n" \
"v_sub_f32_e32 v174, 0, v228\n" \
"v_sub_f32_e32 v175, 0, v228\n" \
"v_sub_f32_e32 v128, v128, v225\n" \
"v_sub_f32_e32 v129, v129, v225\n" \
"v_sub_f32_e32 v130, v130, v225\n" \
"v_sub_f32_e32 v131, v131, v225\n" \
"v_sub_f32_e32 v132, v132, v225\n" \
"v_sub_f32_e32 v133, v133, v225\n" \
"v_sub_f32_e32 v134, v134, v225\n" \
"v_sub_f32_e32 v135, v135, v225\n" \
"v_sub_f32_e32 v136, v136, v226\n" \
"v_sub_f32_e32 v137, v137, v226\n" \
"v_sub_f32_e32 v138, v138, v226\n" \
"v_sub_f32_e32 v139, v139, v226\n" \
"v_sub_f32_e32 v140, v140, v226\n" \
"v_sub_f32_e32 v141, v141, v226\n" \
"v_sub_f32_e32 v142, v142, v226\n" \
"v_sub_f32_e32 v143, v143, v226\n" \
"v_sub_f32_e32 v144, v144, v227\n" \
"v_sub_f32_e32 v145, v145, v227\n" \
"v_sub_f32_e32 v146, v146, v227\n" \
"v_sub_f32_e32 v147, v147, v227\n" \
"v_sub_f32_e32 v148, v148, v227\n" \
"v_sub_f32_e32 v149, v149, v227\n" \
"v_sub_f32_e32 v150, v150, v227\n" \
"v_sub_f32_e32 v151, v151, v227\n" \
"v_sub_f32_e32 v152, v152, v228\n" \
"v_sub_f32_e32 v153, v153, v228\n" \
"v_sub_f32_e32 v154, v154, v228\n" \
"v_sub_f32_e32 v155, v155, v228\n" \
"v_sub_f32_e32 v156, v156, v228\n" \
"v_sub_f32_e32 v157, v157, v228\n" \
"v_sub_f32_e32 v158, v158, v228\n" \
"v_sub_f32_e32 v159, v159, v228\n" \
"v_exp_f32_e32 v128, v128\n" \
"v_exp_f32_e32 v129, v129\n" \
"v_exp_f32_e32 v130, v130\n" \
"v_exp_f32_e32 v131, v131\n" \
"v_exp_f32_e32 v132, v132\n" \
"v_exp_f32_e32 v133, v133\n" \
"v_exp_f32_e32 v134, v134\n" \
"v_exp_f32_e32 v135, v135\n" \
"v_exp_f32_e32 v136, v136\n" \
"v_exp_f32_e32 v137, v137\n" \
"v_exp_f32_e32 v138, v138\n" \
"v_exp_f32_e32 v139, v139\n" \
"v_exp_f32_e32 v140, v140\n" \
"v_exp_f32_e32 v141, v141\n" \
"v_exp_f32_e32 v142, v142\n" \
"v_exp_f32_e32 v143, v143\n" \
"v_exp_f32_e32 v144, v144\n" \
"v_exp_f32_e32 v145, v145\n" \
"v_exp_f32_e32 v146, v146\n" \
"v_exp_f32_e32 v147, v147\n" \
"v_exp_f32_e32 v148, v148\n" \
"v_exp_f32_e32 v149, v149\n" \
"v_exp_f32_e32 v150, v150\n" \
"v_exp_f32_e32 v151, v151\n" \
"v_exp_f32_e32 v152, v152\n" \
"v_exp_f32_e32 v153, v153\n" \
"v_exp_f32_e32 v154, v154\n" \
"v_exp_f32_e64 v155, v155\n" \
"v_bfe_i32 v228, v224, 7, 1\n" \
"v_and_b32_e32 v228, 0xaaaa, v228\n" \
"v_or_b32_e32 v228, 0x4444, v228\n" \
"v_cvt_pk_bf16_f32 v184, v128, v129\n" \
"v_cvt_pk_bf16_f32 v185, v130, v131\n" \
"v_cvt_pk_bf16_f32 v186, v132, v133\n" \
"v_cvt_pk_bf16_f32 v187, v134, v135\n" \
"v_cvt_pk_bf16_f32 v188, v136, v137\n" \
"v_cvt_pk_bf16_f32 v189, v138, v139\n" \
"v_cvt_pk_bf16_f32 v190, v140, v141\n" \
"v_cvt_pk_bf16_f32 v191, v142, v143\n" \
"v_cvt_pk_bf16_f32 v216, v144, v145\n" \
"v_cvt_pk_bf16_f32 v217, v146, v147\n" \
"v_cvt_pk_bf16_f32 v218, v148, v149\n" \
"v_cvt_pk_bf16_f32 v219, v150, v151\n" \
"v_max3_f32 v230, |v230|, |v231|, |v232|\n" \
"v_max_f32_e64 v230, v230, |v233|\n" \
"v_sqrt_f32_e32 v230, v230\n" \
"s_nop 0\n" \
"v_mul_legacy_f32 v225, s68, v230\n" \
"v_mov_b32_e32 v230, v225\n" \
"v_mov_b32_e32 v231, v225\n" \
"v_mov_b32_e32 v232, v225\n" \
"v_mov_b32_e32 v233, v225\n" \
"v_lshrrev_b32_e64 v235, 3, v224\n" \
"v_readlane_b32 s68, %[tlo], 13\n" \
"s_xor_b32 s61, %[sin], 0x2000\n" \
"s_sub_u32 s61, s61, %[sin]\n" \
"s_add_u32 s61, s61, 0x200000\n" \
"v_add_u32_e32 v226, s61, v235\n" \
"s_bitcmp0_b32 s68, 0\n" \
"s_cselect_b64 vcc, -1, 0\n" \
"s_waitcnt vmcnt(2) lgkmcnt(0)\n" \
"s_barrier\n" \
"v_mfma_f32_16x16x32_bf16 v[128:131], a[96:99], a[0:3], v[160:163]\n" \
"v_exp_f32_e64 v156, v156\n" \
"v_mfma_f32_16x16x32_bf16 v[128:131], a[100:103], a[4:7], v[128:131]\n" \
"s_add_u32 m0, %[m0b], 114688\n" \
"v_mfma_f32_16x16x32_bf16 v[128:131], a[104:107], a[8:11], v[128:131]\n" \
"v_exp_f32_e64 v157, v157\n" \
"v_mfma_f32_16x16x32_bf16 v[128:131], a[108:111], a[12:15], v[128:131]\n" \
"v_cvt_pk_bf16_f32 v220, v152, v153\n" \
"v_cvt_pk_bf16_f32 v221, v154, v155\n" \
"v_mfma_f32_16x16x32_bf16 v[132:135], a[124:127], a[12:15], v[160:163]\n" \
"v_exp_f32_e64 v158, v158\n" \
"v_mfma_f32_16x16x32_bf16 v[132:135], a[120:123], a[8:11], v[132:135]\n" \
"v_mfma_f32_16x16x32_bf16 v[132:135], a[116:119], a[4:7], v[132:135]\n" \
"buffer_load_dwordx4 %[vd0], %[srdv], s48 offen offset:0 lds\n" \
"v_mfma_f32_16x16x32_bf16 v[132:135], a[112:115], a[0:3], v[132:135]\n" \
"v_exp_f32_e64 v159, v159\n" \
"v_mfma_f32_16x16x32_bf16 v[0:3], a[128:131], v[184:187], v[230:233]\n" \
"ds_read_b128 a[64:67], %[kb0] offset:16384\n" \
"v_mfma_f32_16x16x32_bf16 v[32:35], a[128:131], v[188:191], v[230:233]\n" \
"v_exp_f32_e64 v128, v128\n" \
"v_mfma_f32_16x16x32_bf16 v[4:7], a[132:135], v[184:187], v[230:233]\n" \
"v_cvt_pk_bf16_f32 v222, v156, v157\n" \
"v_cvt_pk_bf16_f32 v223, v158, v159\n" \
"v_mfma_f32_16x16x32_bf16 v[36:39], a[132:135], v[188:191], v[230:233]\n" \
"v_exp_f32_e64 v129, v129\n" \
"v_mfma_f32_16x16x32_bf16 v[8:11], a[136:139], v[184:187], v[230:233]\n" \
"ds_read_b128 a[68:71], v208 offset:16384\n" \
"v_mfma_f32_16x16x32_bf16 v[40:43], a[136:139], v[188:191], v[230:233]\n" \
"v_exp_f32_e64 v130, v130\n" \
"v_mfma_f32_16x16x32_bf16 v[12:15], a[140:143], v[184:187], v[230:233]\n" \
"ds_read_b128 a[72:75], v209 offset:16384\n" \
"v_mfma_f32_16x16x32_bf16 v[44:47], a[140:143], v[188:191], v[230:233]\n" \
"v_exp_f32_e64 v131, v131\n" \
"v_smfmac_f32_16x16x64_bf16 v[176:179], a[192:195], v[184:191], v228\n" \
"v_mfma_f32_16x16x32_bf16 v[136:139], a[96:99], a[16:19], v[164:167]\n" \
"v_exp_f32_e64 v132, v132\n" \
"v_mfma_f32_16x16x32_bf16 v[136:139], a[100:103], a[20:23], v[136:139]\n" \
"ds_read_b128 a[76:79], v210 offset:16384\n" \
"v_mfma_f32_16x16x32_bf16 v[136:139], a[104:107], a[24:27], v[136:139]\n" \
"v_exp_f32_e64 v133, v133\n" \
"v_mfma_f32_16x16x32_bf16 v[136:139], a[108:111], a[28:31], v[136:139]\n" \
"v_cvt_pk_bf16_f32 v192, v128, v129\n" \
"v_cvt_pk_bf16_f32 v193, v130, v131\n" \
"v_mfma_f32_16x16x32_bf16 v[140:143], a[124:127], a[28:31], v[164:167]\n" \
"v_exp_f32_e64 v134, v134\n" \
"v_mfma_f32_16x16x32_bf16 v[140:143], a[120:123], a[24:27], v[140:143]\n" \
"ds_read_b128 a[92:95], v210 offset:20480\n" \
"v_mfma_f32_16x16x32_bf16 v[140:143], a[116:119], a[20:23], v[140:143]\n" \
"buffer_load_dwordx4 v215, %[srdv], s48 offen offset:1024 lds\n" \
"v_mfma_f32_16x16x32_bf16 v[140:143], a[112:115], a[16:19], v[140:143]\n" \
"v_exp_f32_e64 v135, v135\n" \
"v_mfma_f32_16x16x32_bf16 v[16:19], a[144:147], v[184:187], v[230:233]\n" \
"ds_read_b128 a[88:91], v209 offset:20480\n" \
"v_mfma_f32_16x16x32_bf16 v[48:51], a[144:147], v[188:191], v[230:233]\n" \
"v_exp_f32_e64 v136, v136\n" \
"v_mfma_f32_16x16x32_bf16 v[20:23], a[148:151], v[184:187], v[230:233]\n" \
"v_cvt_pk_bf16_f32 v194, v132, v133\n" \
"v_cvt_pk_bf16_f32 v195, v134, v135\n" \
"v_mfma_f32_16x16x32_bf16 v[52:55], a[148:151], v[188:191], v[230:233]\n" \
"v_exp_f32_e64 v137, v137\n" \
"v_mfma_f32_16x16x32_bf16 v[24:27], a[152:155], v[184:187], v[230:233]\n" \
"v_exp_f32_e64 v138, v138\n" \
"v_mfma_f32_16x16x32_bf16 v[56:59], a[152:155], v[188:191], v[230:233]\n" \
"ds_read_b128 a[84:87], v208 offset:20480\n" \
"v_mfma_f32_16x16x32_bf16 v[28:31], a[156:159], v[184:187], v[230:233]\n" \
"v_exp_f32_e64 v139, v139\n" \
"v_mfma_f32_16x16x32_bf16 v[60:63], a[156:159], v[188:191], v[230:233]\n" \
"v_mfma_f32_16x16x32_bf16 v[144:147], a[96:99], a[32:35], v[168:171]\n" \
"v_exp_f32_e64 v140, v140\n" \
"v_mfma_f32_16x16x32_bf16 v[144:147], a[100:103], a[36:39], v[144:147]\n" \
"ds_read_b128 a[80:83], %[kb0] offset:20480\n" \
"v_mfma_f32_16x16x32_bf16 v[144:147], a[104:107], a[40:43], v[144:147]\n" \
"v_exp_f32_e64 v141, v141\n" \
"v_mfma_f32_16x16x32_bf16 v[144:147], a[108:111], a[44:47], v[144:147]\n" \
"v_cvt_pk_bf16_f32 v196, v136, v137\n" \
"v_cvt_pk_bf16_f32 v197, v138, v139\n" \
"v_mfma_f32_16x16x32_bf16 v[148:151], a[124:127], a[44:47], v[168:171]\n" \
"v_exp_f32_e64 v142, v142\n" \
"v_mfma_f32_16x16x32_bf16 v[148:151], a[120:123], a[40:43], v[148:151]\n" \
"v_mfma_f32_16x16x32_bf16 v[148:151], a[116:119], a[36:39], v[148:151]\n" \
"buffer_load_dwordx4 %[vd0], %[srdv], s48 offen offset:2048 lds\n" \
"v_mfma_f32_16x16x32_bf16 v[148:151], a[112:115], a[32:35], v[148:151]\n" \
"v_exp_f32_e64 v143, v143\n" \
"v_mfma_f32_16x16x32_bf16 v[64:67], a[128:131], v[216:219], v[230:233]\n" \
"ds_read_b64_tr_b16 a[160:161], %[vvb] offset:8192\n" \
"v_mfma_f32_16x16x32_bf16 v[96:99], a[128:131], v[220:223], v[230:233]\n" \
"v_exp_f32_e64 v144, v144\n" \
"v_mfma_f32_16x16x32_bf16 v[68:71], a[132:135], v[216:219], v[230:233]\n" \
"v_cvt_pk_bf16_f32 v198, v140, v141\n" \
"v_cvt_pk_bf16_f32 v199, v142, v143\n" \
"v_mfma_f32_16x16x32_bf16 v[100:103], a[132:135], v[220:223], v[230:233]\n" \
"v_exp_f32_e64 v145, v145\n" \
"v_mfma_f32_16x16x32_bf16 v[72:75], a[136:139], v[216:219], v[230:233]\n" \
"ds_read_b64_tr_b16 a[162:163], %[vvb] offset:12288\n" \
"ds_read_b64_tr_b16 a[164:165], v211 offset:8192\n" \
"v_mfma_f32_16x16x32_bf16 v[104:107], a[136:139], v[220:223], v[230:233]\n" \
"v_exp_f32_e64 v146, v146\n" \
"v_mfma_f32_16x16x32_bf16 v[76:79], a[140:143], v[216:219], v[230:233]\n" \
"ds_read_b64_tr_b16 a[166:167], v211 offset:12288\n" \
"v_mfma_f32_16x16x32_bf16 v[108:111], a[140:143], v[220:223], v[230:233]\n" \
"v_exp_f32_e64 v147, v147\n" \
"v_smfmac_f32_16x16x64_bf16 v[180:183], a[192:195], v[216:223], v228\n" \
"ds_read_b64_tr_b16 a[168:169], %[vvb] offset:8704\n" \
"v_mfma_f32_16x16x32_bf16 v[152:155], a[96:99], a[48:51], v[172:175]\n" \
"v_exp_f32_e64 v148, v148\n" \
"v_mfma_f32_16x16x32_bf16 v[152:155], a[100:103], a[52:55], v[152:155]\n" \
"ds_read_b64_tr_b16 a[170:171], %[vvb] offset:12800\n" \
"v_mfma_f32_16x16x32_bf16 v[152:155], a[104:107], a[56:59], v[152:155]\n" \
"v_exp_f32_e64 v149, v149\n" \
"v_mfma_f32_16x16x32_bf16 v[152:155], a[108:111], a[60:63], v[152:155]\n" \
"v_cvt_pk_bf16_f32 v200, v144, v145\n" \
"v_cvt_pk_bf16_f32 v201, v146, v147\n" \
"v_mfma_f32_16x16x32_bf16 v[156:159], a[124:127], a[60:63], v[172:175]\n" \
"v_exp_f32_e64 v150, v150\n" \
"v_mfma_f32_16x16x32_bf16 v[156:159], a[120:123], a[56:59], v[156:159]\n" \
"ds_read_b64_tr_b16 a[172:173], v211 offset:8704\n" \
"v_mfma_f32_16x16x32_bf16 v[156:159], a[116:119], a[52:55], v[156:159]\n" \
"buffer_load_dwordx4 v215, %[srdv], s48 offen offset:3072 lds\n" \
"v_mfma_f32_16x16x32_bf16 v[156:159], a[112:115], a[48:51], v[156:159]\n" \
"v_exp_f32_e64 v151, v151\n" \
"v_mfma_f32_16x16x32_bf16 v[80:83], a[144:147], v[216:219], v[230:233]\n" \
"ds_read_b64_tr_b16 a[174:175], v211 offset:12800\n" \
"ds_read_b64_tr_b16 a[176:177], %[vvb] offset:9216\n" \
"v_mfma_f32_16x16x32_bf16 v[112:115], a[144:147], v[220:223], v[230:233]\n" \
"v_exp_f32_e64 v152, v152\n" \
"v_mfma_f32_16x16x32_bf16 v[84:87], a[148:151], v[216:219], v[230:233]\n" \
"v_cvt_pk_bf16_f32 v202, v148, v149\n" \
"v_cvt_pk_bf16_f32 v203, v150, v151\n" \
"v_mfma_f32_16x16x32_bf16 v[116:119], a[148:151], v[220:223], v[230:233]\n" \
"v_exp_f32_e64 v153, v153\n" \
"v_mfma_f32_16x16x32_bf16 v[88:91], a[152:155], v[216:219], v[230:233]\n" \
"v_exp_f32_e64 v154, v154\n" \
"v_mfma_f32_16x16x32_bf16 v[120:123], a[152:155], v[220:223], v[230:233]\n" \
"ds_read_b64_tr_b16 a[178:179], %[vvb] offset:13312\n" \
"v_mfma_f32_16x16x32_bf16 v[92:95], a[156:159], v[216:219], v[230:233]\n" \
"v_exp_f32_e64 v155, v155\n" \
"v_mov_b64_e32 v[216:217], 0\n" \
"v_mov_b64_e32 v[218:219], 0\n" \
"v_mfma_f32_16x16x32_bf16 v[124:127], a[156:159], v[220:223], v[230:233]\n" \
"ds_read_b64_tr_b16 a[180:181], v211 offset:9216\n" \
"s_waitcnt lgkmcnt(13)\n" \
"s_nop 0\n" \
"LmA_%=:\n" \
"v_mfma_f32_16x16x32_bf16 v[128:131], a[64:67], a[0:3], v[160:163]\n" \
"v_exp_f32_e64 v156, v156\n" \
"v_mfma_f32_16x16x32_bf16 v[128:131], a[68:71], a[4:7], v[128:131]\n" \
"ds_read_b64_tr_b16 a[182:183], v211 offset:13312\n" \
"s_add_u32 m0, %[m0b], 0\n" \
"v_mfma_f32_16x16x32_bf16 v[128:131], a[72:75], a[8:11], v[128:131]\n" \
"v_exp_f32_e32 v157, v157\n" \
"v_mfma_f32_16x16x32_bf16 v[128:131], a[76:79], a[12:15], v[128:131]\n" \
"v_cvt_pk_bf16_f32 v204, v152, v153\n" \
"v_cvt_pk_bf16_f32 v205, v154, v155\n" \
"v_mfma_f32_16x16x32_bf16 v[132:135], a[92:95], a[12:15], v[160:163]\n" \
"v_exp_f32_e64 v158, v158\n" \
"v_mfma_f32_16x16x32_bf16 v[132:135], a[88:91], a[8:11], v[132:135]\n" \
"ds_read_b64_tr_b16 a[184:185], %[vvb] offset:9728\n" \
"s_waitcnt lgkmcnt(5)\n" \
"v_mfma_f32_16x16x32_bf16 v[132:135], a[84:87], a[4:7], v[132:135]\n" \
"buffer_load_dwordx4 %[kd0], %[srdk], s48 offen offset:0 lds\n" \
"v_mfma_f32_16x16x32_bf16 v[132:135], a[80:83], a[0:3], v[132:135]\n" \
"v_exp_f32_e32 v159, v159\n" \
"v_mfma_f32_16x16x32_bf16 v[64:67], a[128:131], v[216:219], v[64:67]\n" \
"ds_read_b64_tr_b16 a[186:187], %[vvb] offset:13824\n" \
"ds_read_b64_tr_b16 a[188:189], v211 offset:9728\n" \
"v_mfma_f32_16x16x32_bf16 v[64:67], a[160:163], v[200:203], v[64:67]\n" \
"v_exp_f32_e64 v128, v128\n" \
"v_mfma_f32_16x16x32_bf16 v[68:71], a[132:135], v[216:219], v[68:71]\n" \
"v_cvt_pk_bf16_f32 v206, v156, v157\n" \
"v_cvt_pk_bf16_f32 v207, v158, v159\n" \
"v_mfma_f32_16x16x32_bf16 v[68:71], a[164:167], v[200:203], v[68:71]\n" \
"v_exp_f32_e64 v129, v129\n" \
"v_mfma_f32_16x16x32_bf16 v[72:75], a[136:139], v[216:219], v[72:75]\n" \
"ds_read_b64_tr_b16 a[190:191], v211 offset:13824\n" \
"ds_read_b64_tr_b16 a[128:129], %[vvb] offset:16384\n" \
"s_add_u32 s60, s60, 1\n" \
"s_nop 0\n" \
"v_readlane_b32 s58, %[tlo], s60\n" \
"v_mfma_f32_16x16x32_bf16 v[72:75], a[168:171], v[200:203], v[72:75]\n" \
"v_exp_f32_e64 v130, v130\n" \
"v_mfma_f32_16x16x32_bf16 v[76:79], a[140:143], v[216:219], v[76:79]\n" \
"ds_read_b64_tr_b16 a[130:131], %[vvb] offset:20480\n" \
"ds_read_b64_tr_b16 a[132:133], v211 offset:16384\n" \
"v_readlane_b32 s59, %[thi], s60\n" \
"v_mfma_f32_16x16x32_bf16 v[76:79], a[172:175], v[200:203], v[76:79]\n" \
"v_exp_f32_e64 v131, v131\n" \
".if . - adaln_body != 12288\n" \
".error \"size model\"\n" \
".endif\n" \
"Lk2_%=:\n" \
"s_cbranch_execz Lk3_%=\n" \
"s_nop 0\n" \
"v_smfmac_f32_16x16x64_bf16 v[176:179], a[192:195], v[192:199], v228\n" \
"ds_read_b64_tr_b16 a[134:135], v211 offset:20480\n" \
"ds_read_b64_tr_b16 a[136:137], %[vvb] offset:16896\n" \
"v_mfma_f32_16x16x32_bf16 v[136:139], a[64:67], a[16:19], v[164:167]\n" \
"v_exp_f32_e64 v132, v132\n" \
"v_mfma_f32_16x16x32_bf16 v[136:139], a[68:71], a[20:23], v[136:139]\n" \
"ds_read_b64_tr_b16 a[138:139], %[vvb] offset:20992\n" \
"ds_read_b64_tr_b16 a[140:141], v211 offset:16896\n" \
"v_mfma_f32_16x16x32_bf16 v[136:139], a[72:75], a[24:27], v[136:139]\n" \
"v_exp_f32_e64 v133, v133\n" \
"v_mfma_f32_16x16x32_bf16 v[136:139], a[76:79], a[28:31], v[136:139]\n" \
"v_cvt_pk_bf16_f32 v184, v128, v129\n" \
"v_cvt_pk_bf16_f32 v185, v130, v131\n" \
"v_mfma_f32_16x16x32_bf16 v[140:143], a[92:95], a[28:31], v[164:167]\n" \
"v_exp_f32_e64 v134, v134\n" \
"v_mfma_f32_16x16x32_bf16 v[140:143], a[88:91], a[24:27], v[140:143]\n" \
"ds_read_b64_tr_b16 a[142:143], v211 offset:20992\n" \
"s_waitcnt lgkmcnt(8)\n" \
"v_mfma_f32_16x16x32_bf16 v[140:143], a[84:87], a[20:23], v[140:143]\n" \
"buffer_load_dwordx4 v212, %[srdk], s48 offen offset:1024 lds\n" \
"v_mfma_f32_16x16x32_bf16 v[140:143], a[80:83], a[16:19], v[140:143]\n" \
"v_exp_f32_e32 v135, v135\n" \
"v_mfma_f32_16x16x32_bf16 v[80:83], a[144:147], v[216:219], v[80:83]\n" \
"ds_read_b64_tr_b16 a[144:145], %[vvb] offset:17408\n" \
"ds_read_b64_tr_b16 a[146:147], %[vvb] offset:21504\n" \
"v_mfma_f32_16x16x32_bf16 v[80:83], a[176:179], v[200:203], v[80:83]\n" \
"v_exp_f32_e64 v136, v136\n" \
"v_mfma_f32_16x16x32_bf16 v[84:87], a[148:151], v[216:219], v[84:87]\n" \
"v_cvt_pk_bf16_f32 v186, v132, v133\n" \
"v_cvt_pk_bf16_f32 v187, v134, v135\n" \
"v_mfma_f32_16x16x32_bf16 v[84:87], a[180:183], v[200:203], v[84:87]\n" \
"v_exp_f32_e64 v137, v137\n" \
"v_mfma_f32_16x16x32_bf16 v[88:91], a[152:155], v[216:219], v[88:91]\n" \
"v_exp_f32_e64 v138, v138\n" \
"v_mfma_f32_16x16x32_bf16 v[88:91], a[184:187], v[200:203], v[88:91]\n" \
"ds_read_b64_tr_b16 a[148:149], v211 offset:17408\n" \
"ds_read_b64_tr_b16 a[150:151], v211 offset:21504\n" \
"v_mfma_f32_16x16x32_bf16 v[92:95], a[156:159], v[216:219], v[92:95]\n" \
"v_exp_f32_e64 v139, v139\n" \
"v_mfma_f32_16x16x32_bf16 v[92:95], a[188:191], v[200:203], v[92:95]\n" \
"ds_read_b64_tr_b16 a[152:153], %[vvb] offset:17920\n" \
"v_mfma_f32_16x16x32_bf16 v[144:147], a[64:67], a[32:35], v[168:171]\n" \
"v_exp_f32_e64 v140, v140\n" \
"v_mfma_f32_16x16x32_bf16 v[144:147], a[68:71], a[36:39], v[144:147]\n" \
"ds_read_b64_tr_b16 a[154:155], %[vvb] offset:22016\n" \
"ds_read_b64_tr_b16 a[156:157], v211 offset:17920\n" \
"v_mfma_f32_16x16x32_bf16 v[144:147], a[72:75], a[40:43], v[144:147]\n" \
"v_exp_f32_e64 v141, v141\n" \
"v_mfma_f32_16x16x32_bf16 v[144:147], a[76:79], a[44:47], v[144:147]\n" \
"v_cvt_pk_bf16_f32 v188, v136, v137\n" \
"v_cvt_pk_bf16_f32 v189, v138, v139\n" \
"v_mfma_f32_16x16x32_bf16 v[148:151], a[92:95], a[44:47], v[168:171]\n" \
"v_exp_f32_e64 v142, v142\n" \
"v_mfma_f32_16x16x32_bf16 v[148:151], a[88:91], a[40:43], v[148:151]\n" \
"ds_read_b64_tr_b16 a[158:159], v211 offset:22016\n" \
"s_waitcnt lgkmcnt(8)\n" \
"v_mfma_f32_16x16x32_bf16 v[148:151], a[84:87], a[36:39], v[148:151]\n" \
"buffer_load_dwordx4 v213, %[srdk], s48 offen offset:2048 lds\n" \
"v_mfma_f32_16x16x32_bf16 v[148:151], a[80:83], a[32:35], v[148:151]\n" \
"v_exp_f32_e32 v143, v143\n" \
"v_mfma_f32_16x16x32_bf16 v[0:3], a[160:163], v[192:195], v[0:3]\n" \
"ds_read_b128 a[96:99], %[kb0] offset:24576\n" \
"v_mfma_f32_16x16x32_bf16 v[0:3], a[128:131], v[184:187], v[0:3]\n" \
"v_exp_f32_e64 v144, v144\n" \
"v_mfma_f32_16x16x32_bf16 v[4:7], a[164:167], v[192:195], v[4:7]\n" \
"v_cvt_pk_bf16_f32 v190, v140, v141\n" \
"v_cvt_pk_bf16_f32 v191, v142, v143\n" \
"v_mfma_f32_16x16x32_bf16 v[4:7], a[132:135], v[184:187], v[4:7]\n" \
"v_exp_f32_e64 v145, v145\n" \
"v_mfma_f32_16x16x32_bf16 v[8:11], a[168:171], v[192:195], v[8:11]\n" \
"ds_read_b128 a[100:103], v208 offset:24576\n" \
"v_mfma_f32_16x16x32_bf16 v[8:11], a[136:139], v[184:187], v[8:11]\n" \
"v_exp_f32_e64 v146, v146\n" \
"v_mfma_f32_16x16x32_bf16 v[12:15], a[172:175], v[192:195], v[12:15]\n" \
"ds_read_b128 a[104:107], v209 offset:24576\n" \
"v_mfma_f32_16x16x32_bf16 v[12:15], a[140:143], v[184:187], v[12:15]\n" \
"v_exp_f32_e64 v147, v147\n" \
"v_smfmac_f32_16x16x64_bf16 v[180:183], a[192:195], v[200:207], v228\n" \
"ds_read_b128 a[108:111], v210 offset:24576\n" \
"v_mfma_f32_16x16x32_bf16 v[152:155], a[64:67], a[48:51], v[172:175]\n" \
"v_exp_f32_e64 v148, v148\n" \
"v_mfma_f32_16x16x32_bf16 v[152:155], a[68:71], a[52:55], v[152:155]\n" \
"ds_read_b128 a[124:127], v210 offset:28672\n" \
"v_mfma_f32_16x16x32_bf16 v[152:155], a[72:75], a[56:59], v[152:155]\n" \
"v_exp_f32_e64 v149, v149\n" \
"v_mfma_f32_16x16x32_bf16 v[152:155], a[76:79], a[60:63], v[152:155]\n" \
"v_cvt_pk_bf16_f32 v216, v144, v145\n" \
"v_cvt_pk_bf16_f32 v217, v146, v147\n" \
"v_mfma_f32_16x16x32_bf16 v[156:159], a[92:95], a[60:63], v[172:175]\n" \
"v_exp_f32_e64 v150, v150\n" \
"v_mfma_f32_16x16x32_bf16 v[156:159], a[88:91], a[56:59], v[156:159]\n" \
"ds_read_b128 a[120:123], v209 offset:28672\n" \
"s_waitcnt lgkmcnt(6)\n" \
"v_mfma_f32_16x16x32_bf16 v[156:159], a[84:87], a[52:55], v[156:159]\n" \
"buffer_load_dwordx4 v214, %[srdk], s48 offen offset:3072 lds\n" \
"v_mfma_f32_16x16x32_bf16 v[156:159], a[80:83], a[48:51], v[156:159]\n" \
"v_exp_f32_e32 v151, v151\n" \
"v_mfma_f32_16x16x32_bf16 v[16:19], a[176:179], v[192:195], v[16:19]\n" \
"ds_read_b128 a[116:119], v208 offset:28672\n" \
"v_mfma_f32_16x16x32_bf16 v[16:19], a[144:147], v[184:187], v[16:19]\n" \
"v_exp_f32_e64 v152, v152\n" \
"v_mfma_f32_16x16x32_bf16 v[20:23], a[180:183], v[192:195], v[20:23]\n" \
"v_cvt_pk_bf16_f32 v218, v148, v149\n" \
"v_cvt_pk_bf16_f32 v219, v150, v151\n" \
"v_mfma_f32_16x16x32_bf16 v[20:23], a[148:151], v[184:187], v[20:23]\n" \
"v_exp_f32_e64 v153, v153\n" \
"v_mfma_f32_16x16x32_bf16 v[24:27], a[184:187], v[192:195], v[24:27]\n" \
"v_exp_f32_e64 v154, v154\n" \
"v_mfma_f32_16x16x32_bf16 v[24:27], a[152:155], v[184:187], v[24:27]\n" \
"ds_read_b128 a[112:115], %[kb0] offset:28672\n" \
"s_add_u32 s48, s48, 0x4000\n" \
"s_mov_b32 m0, s44\n" \
"s_addk_i32 s44, 0x400\n" \
"buffer_load_dwordx4 v229, s[64:67], s45 offen offset:0 nt lds\n" \
"s_add_u32 s45, s45, 393216\n" \
"v_mfma_f32_16x16x32_bf16 v[28:31], a[188:191], v[192:195], v[28:31]\n" \
"v_exp_f32_e64 v155, v155\n" \
"v_mfma_f32_16x16x32_bf16 v[28:31], a[156:159], v[184:187], v[28:31]\n" \
"s_waitcnt vmcnt(13) lgkmcnt(2)\n" \
"s_barrier\n" \
"v_mfma_f32_16x16x32_bf16 v[128:131], a[96:99], a[0:3], v[160:163]\n" \
"v_exp_f32_e64 v156, v156\n" \
"v_mfma_f32_16x16x32_bf16 v[128:131], a[100:103], a[4:7], v[128:131]\n" \
"s_add_u32 m0, %[m0b], 65536\n" \
"v_mfma_f32_16x16x32_bf16 v[128:131], a[104:107], a[8:11], v[128:131]\n" \
"v_exp_f32_e64 v157, v157\n" \
"v_mfma_f32_16x16x32_bf16 v[128:131], a[108:111], a[12:15], v[128:131]\n" \
"v_cvt_pk_bf16_f32 v220, v152, v153\n" \
"v_cvt_pk_bf16_f32 v221, v154, v155\n" \
"v_mfma_f32_16x16x32_bf16 v[132:135], a[124:127], a[12:15], v[160:163]\n" \
"v_exp_f32_e64 v158, v158\n" \
"v_mfma_f32_16x16x32_bf16 v[132:135], a[120:123], a[8:11], v[132:135]\n" \
"s_waitcnt lgkmcnt(0)\n" \
"v_mfma_f32_16x16x32_bf16 v[132:135], a[116:119], a[4:7], v[132:135]\n" \
"buffer_load_dwordx4 %[vd0], %[srdv], s48 offen offset:0 lds\n" \
"v_mfma_f32_16x16x32_bf16 v[132:135], a[112:115], a[0:3], v[132:135]\n" \
"v_exp_f32_e32 v159, v159\n" \
"v_mfma_f32_16x16x32_bf16 v[32:35], a[160:163], v[196:199], v[32:35]\n" \
"ds_read_b128 a[64:67], %[kb0] offset:32768\n" \
"v_mfma_f32_16x16x32_bf16 v[32:35], a[128:131], v[188:191], v[32:35]\n" \
"v_exp_f32_e64 v128, v128\n" \
"v_mfma_f32_16x16x32_bf16 v[36:39], a[164:167], v[196:199], v[36:39]\n" \
"v_cvt_pk_bf16_f32 v222, v156, v157\n" \
"v_cvt_pk_bf16_f32 v223, v158, v159\n" \
"v_mfma_f32_16x16x32_bf16 v[36:39], a[132:135], v[188:191], v[36:39]\n" \
"v_exp_f32_e64 v129, v129\n" \
"v_mfma_f32_16x16x32_bf16 v[40:43], a[168:171], v[196:199], v[40:43]\n" \
"ds_read_b128 a[68:71], v208 offset:32768\n" \
"v_mfma_f32_16x16x32_bf16 v[40:43], a[136:139], v[188:191], v[40:43]\n" \
"v_exp_f32_e64 v130, v130\n" \
"v_mfma_f32_16x16x32_bf16 v[44:47], a[172:175], v[196:199], v[44:47]\n" \
"ds_read_b128 a[72:75], v209 offset:32768\n" \
"v_mfma_f32_16x16x32_bf16 v[44:47], a[140:143], v[188:191], v[44:47]\n" \
"v_exp_f32_e64 v131, v131\n" \
"v_smfmac_f32_16x16x64_bf16 v[176:179], a[192:195], v[184:191], v228\n" \
"v_mfma_f32_16x16x32_bf16 v[136:139], a[96:99], a[16:19], v[164:167]\n" \
"v_exp_f32_e64 v132, v132\n" \
"v_mfma_f32_16x16x32_bf16 v[136:139], a[100:103], a[20:23], v[136:139]\n" \
"ds_read_b128 a[76:79], v210 offset:32768\n" \
"v_mfma_f32_16x16x32_bf16 v[136:139], a[104:107], a[24:27], v[136:139]\n" \
"v_exp_f32_e64 v133, v133\n" \
"v_mfma_f32_16x16x32_bf16 v[136:139], a[108:111], a[28:31], v[136:139]\n" \
"v_cvt_pk_bf16_f32 v192, v128, v129\n" \
"v_cvt_pk_bf16_f32 v193, v130, v131\n" \
"v_mfma_f32_16x16x32_bf16 v[140:143], a[124:127], a[28:31], v[164:167]\n" \
"v_exp_f32_e64 v134, v134\n" \
"v_mfma_f32_16x16x32_bf16 v[140:143], a[120:123], a[24:27], v[140:143]\n" \
"ds_read_b128 a[92:95], v210 offset:36864\n" \
"v_mfma_f32_16x16x32_bf16 v[140:143], a[116:119], a[20:23], v[140:143]\n" \
"buffer_load_dwordx4 v215, %[srdv], s48 offen offset:1024 lds\n" \
"v_mfma_f32_16x16x32_bf16 v[140:143], a[112:115], a[16:19], v[140:143]\n" \
"v_exp_f32_e64 v135, v135\n" \
"v_mfma_f32_16x16x32_bf16 v[48:51], a[176:179], v[196:199], v[48:51]\n" \
"ds_read_b128 a[88:91], v209 offset:36864\n" \
"v_mfma_f32_16x16x32_bf16 v[48:51], a[144:147], v[188:191], v[48:51]\n" \
"v_exp_f32_e64 v136, v136\n" \
"v_mfma_f32_16x16x32_bf16 v[52:55], a[180:183], v[196:199], v[52:55]\n" \
"v_cvt_pk_bf16_f32 v194, v132, v133\n" \
"v_cvt_pk_bf16_f32 v195, v134, v135\n" \
"v_mfma_f32_16x16x32_bf16 v[52:55], a[148:151], v[188:191], v[52:55]\n" \
"v_exp_f32_e64 v137, v137\n" \
"v_mfma_f32_16x16x32_bf16 v[56:59], a[184:187], v[196:199], v[56:59]\n" \
"v_exp_f32_e64 v138, v138\n" \
"v_mfma_f32_16x16x32_bf16 v[56:59], a[152:155], v[188:191], v[56:59]\n" \
"ds_read_b128 a[84:87], v208 offset:36864\n" \
"v_mfma_f32_16x16x32_bf16 v[60:63], a[188:191], v[196:199], v[60:63]\n" \
"v_exp_f32_e64 v139, v139\n" \
"v_mfma_f32_16x16x32_bf16 v[60:63], a[156:159], v[188:191], v[60:63]\n" \
"s_cmp_lg_u32 %[wid], 0\n" \
"s_cbranch_scc1 LtA3_%=\n" \
"global_load_dword v237, v224, s[58:59]\n" \
"s_add_u32 s61, s48, 49152\n" \
"s_mov_b32 exec_hi, 0\n" \
"s_nop 0\n" \
"buffer_load_dword v237, v224, %[srdp], s61 offen\n" \
"s_mov_b32 exec_hi, -1\n" \
"s_nop 0\n" \
"LtA3_%=:\n" \
"v_mfma_f32_16x16x32_bf16 v[144:147], a[96:99], a[32:35], v[168:171]\n" \
"v_exp_f32_e64 v140, v140\n" \
"v_mfma_f32_16x16x32_bf16 v[144:147], a[100:103], a[36:39], v[144:147]\n" \
"ds_read_b128 a[80:83], %[kb0] offset:36864\n" \
"v_mfma_f32_16x16x32_bf16 v[144:147], a[104:107], a[40:43], v[144:147]\n" \
"v_exp_f32_e64 v141, v141\n" \
"v_mfma_f32_16x16x32_bf16 v[144:147], a[108:111], a[44:47], v[144:147]\n" \
"v_cvt_pk_bf16_f32 v196, v136, v137\n" \
"v_cvt_pk_bf16_f32 v197, v138, v139\n" \
"v_mfma_f32_16x16x32_bf16 v[148:151], a[124:127], a[44:47], v[168:171]\n" \
"v_exp_f32_e64 v142, v142\n" \
"v_mfma_f32_16x16x32_bf16 v[148:151], a[120:123], a[40:43], v[148:151]\n" \
"v_mfma_f32_16x16x32_bf16 v[148:151], a[116:119], a[36:39], v[148:151]\n" \
"buffer_load_dwordx4 %[vd0], %[srdv], s48 offen offset:2048 lds\n" \
"v_mfma_f32_16x16x32_bf16 v[148:151], a[112:115], a[32:35], v[148:151]\n" \
"v_exp_f32_e64 v143, v143\n" \
"v_mfma_f32_16x16x32_bf16 v[96:99], a[160:163], v[204:207], v[96:99]\n" \
"ds_read_b64_tr_b16 a[160:161], %[vvb] offset:24576\n" \
"v_mfma_f32_16x16x32_bf16 v[96:99], a[128:131], v[220:223], v[96:99]\n" \
"v_exp_f32_e64 v144, v144\n" \
"v_mfma_f32_16x16x32_bf16 v[100:103], a[164:167], v[204:207], v[100:103]\n" \
"v_cvt_pk_bf16_f32 v198, v140, v141\n" \
"v_cvt_pk_bf16_f32 v199, v142, v143\n" \
"v_mfma_f32_16x16x32_bf16 v[100:103], a[132:135], v[220:223], v[100:103]\n" \
"v_exp_f32_e64 v145, v145\n" \
"v_mfma_f32_16x16x32_bf16 v[104:107], a[168:171], v[204:207], v[104:107]\n" \
"ds_read_b64_tr_b16 a[162:163], %[vvb] offset:28672\n" \
"ds_read_b64_tr_b16 a[164:165], v211 offset:24576\n" \
"v_mfma_f32_16x16x32_bf16 v[104:107], a[136:139], v[220:223], v[104:107]\n" \
"v_exp_f32_e64 v146, v146\n" \
"v_mfma_f32_16x16x32_bf16 v[108:111], a[172:175], v[204:207], v[108:111]\n" \
"ds_read_b64_tr_b16 a[166:167], v211 offset:28672\n" \
"v_mfma_f32_16x16x32_bf16 v[108:111], a[140:143], v[220:223], v[108:111]\n" \
"v_exp_f32_e64 v147, v147\n" \
"v_smfmac_f32_16x16x64_bf16 v[180:183], a[192:195], v[216:223], v228\n" \
"ds_read_b64_tr_b16 a[168:169], %[vvb] offset:25088\n" \
"v_mfma_f32_16x16x32_bf16 v[152:155], a[96:99], a[48:51], v[172:175]\n" \
"v_exp_f32_e64 v148, v148\n" \
"v_mfma_f32_16x16x32_bf16 v[152:155], a[100:103], a[52:55], v[152:155]\n" \
"ds_read_b64_tr_b16 a[170:171], %[vvb] offset:29184\n" \
"v_mfma_f32_16x16x32_bf16 v[152:155], a[104:107], a[56:59], v[152:155]\n" \
"v_exp_f32_e64 v149, v149\n" \
"v_mfma_f32_16x16x32_bf16 v[152:155], a[108:111], a[60:63], v[152:155]\n" \
"v_cvt_pk_bf16_f32 v200, v144, v145\n" \
"v_cvt_pk_bf16_f32 v201, v146, v147\n" \
"v_mfma_f32_16x16x32_bf16 v[156:159], a[124:127], a[60:63], v[172:175]\n" \
"v_exp_f32_e64 v150, v150\n" \
"v_mfma_f32_16x16x32_bf16 v[156:159], a[120:123], a[56:59], v[156:159]\n" \
"ds_read_b64_tr_b16 a[172:173], v211 offset:25088\n" \
"v_mfma_f32_16x16x32_bf16 v[156:159], a[116:119], a[52:55], v[156:159]\n" \
"buffer_load_dwordx4 v215, %[srdv], s48 offen offset:3072 lds\n" \
"v_mfma_f32_16x16x32_bf16 v[156:159], a[112:115], a[48:51], v[156:159]\n" \
"v_exp_f32_e64 v151, v151\n" \
"v_mfma_f32_16x16x32_bf16 v[112:115], a[176:179], v[204:207], v[112:115]\n" \
"ds_read_b64_tr_b16 a[174:175], v211 offset:29184\n" \
"ds_read_b64_tr_b16 a[176:177], %[vvb] offset:25600\n" \
"v_mfma_f32_16x16x32_bf16 v[112:115], a[144:147], v[220:223], v[112:115]\n" \
"v_exp_f32_e64 v152, v152\n" \
"v_mfma_f32_16x16x32_bf16 v[116:119], a[180:183], v[204:207], v[116:119]\n" \
"v_cvt_pk_bf16_f32 v202, v148, v149\n" \
"v_cvt_pk_bf16_f32 v203, v150, v151\n" \
"v_mfma_f32_16x16x32_bf16 v[116:119], a[148:151], v[220:223], v[116:119]\n" \
"v_exp_f32_e64 v153, v153\n" \
"v_mfma_f32_16x16x32_bf16 v[120:123], a[184:187], v[204:207], v[120:123]\n" \
"v_exp_f32_e64 v154, v154\n" \
"v_mfma_f32_16x16x32_bf16 v[120:123], a[152:155], v[220:223], v[120:123]\n" \
"ds_read_b64_tr_b16 a[178:179], %[vvb] offset:29696\n" \
"v_mfma_f32_16x16x32_bf16 v[124:127], a[188:191], v[204:207], v[124:127]\n" \
"v_exp_f32_e64 v155, v155\n" \
"v_mfma_f32_16x16x32_bf16 v[124:127], a[156:159], v[220:223], v[124:127]\n" \
"ds_read_b64_tr_b16 a[180:181], v211 offset:25600\n" \
"s_waitcnt lgkmcnt(13)\n" \
"v_mfma_f32_16x16x32_bf16 v[128:131], a[64:67], a[0:3], v[160:163]\n" \
"v_exp_f32_e32 v156, v156\n" \
"v_mfma_f32_16x16x32_bf16 v[128:131], a[68:71], a[4:7], v[128:131]\n" \
"ds_read_b64_tr_b16 a[182:183], v211 offset:29696\n" \
"s_add_u32 m0, %[m0b], 16384\n" \
"v_mfma_f32_16x16x32_bf16 v[128:131], a[72:75], a[8:11], v[128:131]\n" \
"v_exp_f32_e64 v157, v157\n" \
"v_mfma_f32_16x16x32_bf16 v[128:131], a[76:79], a[12:15], v[128:131]\n" \
"v_cvt_pk_bf16_f32 v204, v152, v153\n" \
"v_cvt_pk_bf16_f32 v205, v154, v155\n" \
"v_mfma_f32_16x16x32_bf16 v[132:135], a[92:95], a[12:15], v[160:163]\n" \
"v_exp_f32_e64 v158, v158\n" \
"v_mfma_f32_16x16x32_bf16 v[132:135], a[88:91], a[8:11], v[132:135]\n" \
"ds_read_b64_tr_b16 a[184:185], %[vvb] offset:26112\n" \
"s_waitcnt lgkmcnt(5)\n" \
"v_mfma_f32_16x16x32_bf16 v[132:135], a[84:87], a[4:7], v[132:135]\n" \
"buffer_load_dwordx4 %[kd0], %[srdk], s48 offen offset:0 lds\n" \
"v_mfma_f32_16x16x32_bf16 v[132:135], a[80:83], a[0:3], v[132:135]\n" \
"v_exp_f32_e32 v159, v159\n" \
"v_mfma_f32_16x16x32_bf16 v[64:67], a[128:131], v[216:219], v[64:67]\n" \
"ds_read_b64_tr_b16 a[186:187], %[vvb] offset:30208\n" \
"ds_read_b64_tr_b16 a[188:189], v211 offset:26112\n" \
"v_mfma_f32_16x16x32_bf16 v[64:67], a[160:163], v[200:203], v[64:67]\n" \
"v_exp_f32_e64 v128, v128\n" \
"v_mfma_f32_16x16x32_bf16 v[68:71], a[132:135], v[216:219], v[68:71]\n" \
"v_cvt_pk_bf16_f32 v206, v156, v157\n" \
"v_cvt_pk_bf16_f32 v207, v158, v159\n" \
"v_mfma_f32_16x16x32_bf16 v[68:71], a[164:167], v[200:203], v[68:71]\n" \
"v_exp_f32_e64 v129, v129\n" \
"v_mfma_f32_16x16x32_bf16 v[72:75], a[136:139], v[216:219], v[72:75]\n" \
"ds_read_b64_tr_b16 a[190:191], v211 offset:30208\n" \
"ds_read_b64_tr_b16 a[128:129], %[vvb] offset:32768\n" \
"v_mfma_f32_16x16x32_bf16 v[72:75], a[168:171], v[200:203], v[72:75]\n" \
"v_exp_f32_e64 v130, v130\n" \
"v_mfma_f32_16x16x32_bf16 v[76:79], a[140:143], v[216:219], v[76:79]\n" \
"ds_read_b64_tr_b16 a[130:131], %[vvb] offset:36864\n" \
"ds_read_b64_tr_b16 a[132:133], v211 offset:32768\n" \
"v_mfma_f32_16x16x32_bf16 v[76:79], a[172:175], v[200:203], v[76:79]\n" \
"v_exp_f32_e64 v131, v131\n" \
"v_smfmac_f32_16x16x64_bf16 v[176:179], a[192:195], v[192:199], v228\n" \
"ds_read_b64_tr_b16 a[134:135], v211 offset:36864\n" \
"ds_read_b64_tr_b16 a[136:137], %[vvb] offset:33280\n" \
"v_mfma_f32_16x16x32_bf16 v[136:139], a[64:67], a[16:19], v[164:167]\n" \
"v_exp_f32_e64 v132, v132\n" \
"v_mfma_f32_16x16x32_bf16 v[136:139], a[68:71], a[20:23], v[136:139]\n" \
"ds_read_b64_tr_b16 a[138:139], %[vvb] offset:37376\n" \
"ds_read_b64_tr_b16 a[140:141], v211 offset:33280\n" \
"v_mfma_f32_16x16x32_bf16 v[136:139], a[72:75], a[24:27], v[136:139]\n" \
"v_exp_f32_e64 v133, v133\n" \
"v_mfma_f32_16x16x32_bf16 v[136:139], a[76:79], a[28:31], v[136:139]\n" \
"v_cvt_pk_bf16_f32 v184, v128, v129\n" \
"v_cvt_pk_bf16_f32 v185, v130, v131\n" \
"v_mfma_f32_16x16x32_bf16 v[140:143], a[92:95], a[28:31], v[164:167]\n" \
"v_exp_f32_e64 v134, v134\n" \
"v_mfma_f32_16x16x32_bf16 v[140:143], a[88:91], a[24:27], v[140:143]\n" \
"ds_read_b64_tr_b16 a[142:143], v211 offset:37376\n" \
"s_waitcnt lgkmcnt(8)\n" \
"v_mfma_f32_16x16x32_bf16 v[140:143], a[84:87], a[20:23], v[140:143]\n" \
"buffer_load_dwordx4 v212, %[srdk], s48 offen offset:1024 lds\n" \
"v_mfma_f32_16x16x32_bf16 v[140:143], a[80:83], a[16:19], v[140:143]\n" \
"v_exp_f32_e32 v135, v135\n" \
"v_mfma_f32_16x16x32_bf16 v[80:83], a[144:147], v[216:219], v[80:83]\n" \
"ds_read_b64_tr_b16 a[144:145], %[vvb] offset:33792\n" \
"ds_read_b64_tr_b16 a[146:147], %[vvb] offset:37888\n" \
"v_mfma_f32_16x16x32_bf16 v[80:83], a[176:179], v[200:203], v[80:83]\n" \
"v_exp_f32_e64 v136, v136\n" \
"v_mfma_f32_16x16x32_bf16 v[84:87], a[148:151], v[216:219], v[84:87]\n" \
"v_cvt_pk_bf16_f32 v186, v132, v133\n" \
"v_cvt_pk_bf16_f32 v187, v134, v135\n" \
"v_mfma_f32_16x16x32_bf16 v[84:87], a[180:183], v[200:203], v[84:87]\n" \
"v_exp_f32_e64 v137, v137\n" \
"v_mfma_f32_16x16x32_bf16 v[88:91], a[152:155], v[216:219], v[88:91]\n" \
"v_exp_f32_e64 v138, v138\n" \
"v_mfma_f32_16x16x32_bf16 v[88:91], a[184:187], v[200:203], v[88:91]\n" \
"ds_read_b64_tr_b16 a[148:149], v211 offset:33792\n" \
"ds_read_b64_tr_b16 a[150:151], v211 offset:37888\n" \
"v_mfma_f32_16x16x32_bf16 v[92:95], a[156:159], v[216:219], v[92:95]\n" \
"v_exp_f32_e64 v139, v139\n" \
"v_mfma_f32_16x16x32_bf16 v[92:95], a[188:191], v[200:203], v[92:95]\n" \
"ds_read_b64_tr_b16 a[152:153], %[vvb] offset:34304\n" \
"s_cbranch_vccz Lpp1_%=\n" \
"buffer_store_dwordx4 a[196:199], v235, %[srdz], s62 offen offset:0 sc1\n" \
"buffer_store_dwordx4 a[196:199], v226, %[srdz], s62 offen offset:0 sc1\n" \
"Lpp1_%=:\n" \
"v_mfma_f32_16x16x32_bf16 v[144:147], a[64:67], a[32:35], v[168:171]\n" \
"v_exp_f32_e32 v140, v140\n" \
"v_mfma_f32_16x16x32_bf16 v[144:147], a[68:71], a[36:39], v[144:147]\n" \
"ds_read_b64_tr_b16 a[154:155], %[vvb] offset:38400\n" \
"ds_read_b64_tr_b16 a[156:157], v211 offset:34304\n" \
"v_mfma_f32_16x16x32_bf16 v[144:147], a[72:75], a[40:43], v[144:147]\n" \
"v_exp_f32_e64 v141, v141\n" \
"v_mfma_f32_16x16x32_bf16 v[144:147], a[76:79], a[44:47], v[144:147]\n" \
"v_cvt_pk_bf16_f32 v188, v136, v137\n" \
"v_cvt_pk_bf16_f32 v189, v138, v139\n" \
"v_mfma_f32_16x16x32_bf16 v[148:151], a[92:95], a[44:47], v[168:171]\n" \
"v_exp_f32_e64 v142, v142\n" \
"v_mfma_f32_16x16x32_bf16 v[148:151], a[88:91], a[40:43], v[148:151]\n" \
"ds_read_b64_tr_b16 a[158:159], v211 offset:38400\n" \
"s_waitcnt lgkmcnt(8)\n" \
"v_mfma_f32_16x16x32_bf16 v[148:151], a[84:87], a[36:39], v[148:151]\n" \
"buffer_load_dwordx4 v213, %[srdk], s48 offen offset:2048 lds\n" \
"v_mfma_f32_16x16x32_bf16 v[148:151], a[80:83], a[32:35], v[148:151]\n" \
"v_exp_f32_e32 v143, v143\n" \
"v_mfma_f32_16x16x32_bf16 v[0:3], a[160:163], v[192:195], v[0:3]\n" \
"ds_read_b128 a[96:99], %[kb0] offset:40960\n" \
"v_mfma_f32_16x16x32_bf16 v[0:3], a[128:131], v[184:187], v[0:3]\n" \
"v_exp_f32_e64 v144, v144\n" \
"v_mfma_f32_16x16x32_bf16 v[4:7], a[164:167], v[192:195], v[4:7]\n" \
"v_cvt_pk_bf16_f32 v190, v140, v141\n" \
"v_cvt_pk_bf16_f32 v191, v142, v143\n" \
"v_mfma_f32_16x16x32_bf16 v[4:7], a[132:135], v[184:187], v[4:7]\n" \
"v_exp_f32_e64 v145, v145\n" \
"v_mfma_f32_16x16x32_bf16 v[8:11], a[168:171], v[192:195], v[8:11]\n" \
"ds_read_b128 a[100:103], v208 offset:40960\n" \
"v_mfma_f32_16x16x32_bf16 v[8:11], a[136:139], v[184:187], v[8:11]\n" \
"v_exp_f32_e64 v146, v146\n" \
"v_mfma_f32_16x16x32_bf16 v[12:15], a[172:175], v[192:195], v[12:15]\n" \
"ds_read_b128 a[104:107], v209 offset:40960\n" \
"v_mfma_f32_16x16x32_bf16 v[12:15], a[140:143], v[184:187], v[12:15]\n" \
"v_exp_f32_e64 v147, v147\n" \
"v_smfmac_f32_16x16x64_bf16 v[180:183], a[192:195], v[200:207], v228\n" \
"ds_read_b128 a[108:111], v210 offset:40960\n" \
"v_mfma_f32_16x16x32_bf16 v[152:155], a[64:67], a[48:51], v[172:175]\n" \
"v_exp_f32_e64 v148, v148\n" \
"v_mfma_f32_16x16x32_bf16 v[152:155], a[68:71], a[52:55], v[152:155]\n" \
"ds_read_b128 a[124:127], v210 offset:45056\n" \
"v_mfma_f32_16x16x32_bf16 v[152:155], a[72:75], a[56:59], v[152:155]\n" \
"v_exp_f32_e64 v149, v149\n" \
"v_mfma_f32_16x16x32_bf16 v[152:155], a[76:79], a[60:63], v[152:155]\n" \
"v_cvt_pk_bf16_f32 v216, v144, v145\n" \
"v_cvt_pk_bf16_f32 v217, v146, v147\n" \
"v_mfma_f32_16x16x32_bf16 v[156:159], a[92:95], a[60:63], v[172:175]\n" \
"v_exp_f32_e64 v150, v150\n" \
"v_mfma_f32_16x16x32_bf16 v[156:159], a[88:91], a[56:59], v[156:159]\n" \
"ds_read_b128 a[120:123], v209 offset:45056\n" \
"s_waitcnt lgkmcnt(6)\n" \
"v_mfma_f32_16x16x32_bf16 v[156:159], a[84:87], a[52:55], v[156:159]\n" \
"buffer_load_dwordx4 v214, %[srdk], s48 offen offset:3072 lds\n" \
"v_mfma_f32_16x16x32_bf16 v[156:159], a[80:83], a[48:51], v[156:159]\n" \
"v_exp_f32_e32 v151, v151\n" \
"v_mfma_f32_16x16x32_bf16 v[16:19], a[176:179], v[192:195], v[16:19]\n" \
"ds_read_b128 a[116:119], v208 offset:45056\n" \
"v_mfma_f32_16x16x32_bf16 v[16:19], a[144:147], v[184:187], v[16:19]\n" \
"v_exp_f32_e64 v152, v152\n" \
"v_mfma_f32_16x16x32_bf16 v[20:23], a[180:183], v[192:195], v[20:23]\n" \
"v_cvt_pk_bf16_f32 v218, v148, v149\n" \
"v_cvt_pk_bf16_f32 v219, v150, v151\n" \
"v_mfma_f32_16x16x32_bf16 v[20:23], a[148:151], v[184:187], v[20:23]\n" \
"v_exp_f32_e64 v153, v153\n" \
"v_mfma_f32_16x16x32_bf16 v[24:27], a[184:187], v[192:195], v[24:27]\n" \
"v_exp_f32_e64 v154, v154\n" \
"v_mfma_f32_16x16x32_bf16 v[24:27], a[152:155], v[184:187], v[24:27]\n" \
"ds_read_b128 a[112:115], %[kb0] offset:45056\n" \
"s_add_u32 s48, s48, 0x4000\n" \
"v_mfma_f32_16x16x32_bf16 v[28:31], a[188:191], v[192:195], v[28:31]\n" \
"v_exp_f32_e64 v155, v155\n" \
"v_mfma_f32_16x16x32_bf16 v[28:31], a[156:159], v[184:187], v[28:31]\n" \
"s_waitcnt vmcnt(13) lgkmcnt(2)\n" \
"s_barrier\n" \
"v_mfma_f32_16x16x32_bf16 v[128:131], a[96:99], a[0:3], v[160:163]\n" \
"v_exp_f32_e64 v156, v156\n" \
"v_mfma_f32_16x16x32_bf16 v[128:131], a[100:103], a[4:7], v[128:131]\n" \
"s_add_u32 m0, %[m0b], 81920\n" \
"v_mfma_f32_16x16x32_bf16 v[128:131], a[104:107], a[8:11], v[128:131]\n" \
"v_exp_f32_e64 v157, v157\n" \
"v_mfma_f32_16x16x32_bf16 v[128:131], a[108:111], a[12:15], v[128:131]\n" \
"v_cvt_pk_bf16_f32 v220, v152, v153\n" \
"v_cvt_pk_bf16_f32 v221, v154, v155\n" \
"v_mfma_f32_16x16x32_bf16 v[132:135], a[124:127], a[12:15], v[160:163]\n" \
"v_exp_f32_e64 v158, v158\n" \
"v_mfma_f32_16x16x32_bf16 v[132:135], a[120:123], a[8:11], v[132:135]\n" \
"s_waitcnt lgkmcnt(0)\n" \
"v_mfma_f32_16x16x32_bf16 v[132:135], a[116:119], a[4:7], v[132:135]\n" \
"buffer_load_dwordx4 %[vd0], %[srdv], s48 offen offset:0 lds\n" \
"v_mfma_f32_16x16x32_bf16 v[132:135], a[112:115], a[0:3], v[132:135]\n" \
"v_exp_f32_e32 v159, v159\n" \
"v_mfma_f32_16x16x32_bf16 v[32:35], a[160:163], v[196:199], v[32:35]\n" \
"ds_read_b128 a[64:67], %[kb0] offset:49152\n" \
"v_mfma_f32_16x16x32_bf16 v[32:35], a[128:131], v[188:191], v[32:35]\n" \
"v_exp_f32_e64 v128, v128\n" \
"v_mfma_f32_16x16x32_bf16 v[36:39], a[164:167], v[196:199], v[36:39]\n" \
"v_cvt_pk_bf16_f32 v222, v156, v157\n" \
"v_cvt_pk_bf16_f32 v223, v158, v159\n" \
"v_mfma_f32_16x16x32_bf16 v[36:39], a[132:135], v[188:191], v[36:39]\n" \
"v_exp_f32_e64 v129, v129\n" \
"v_mfma_f32_16x16x32_bf16 v[40:43], a[168:171], v[196:199], v[40:43]\n" \
"ds_read_b128 a[68:71], v208 offset:49152\n" \
"v_mfma_f32_16x16x32_bf16 v[40:43], a[136:139], v[188:191], v[40:43]\n" \
"v_exp_f32_e64 v130, v130\n" \
"v_mfma_f32_16x16x32_bf16 v[44:47], a[172:175], v[196:199], v[44:47]\n" \
"ds_read_b128 a[72:75], v209 offset:49152\n" \
"v_mfma_f32_16x16x32_bf16 v[44:47], a[140:143], v[188:191], v[44:47]\n" \
"v_exp_f32_e64 v131, v131\n" \
"v_smfmac_f32_16x16x64_bf16 v[176:179], a[192:195], v[184:191], v228\n" \
"v_mfma_f32_16x16x32_bf16 v[136:139], a[96:99], a[16:19], v[164:167]\n" \
"v_exp_f32_e64 v132, v132\n" \
"v_mfma_f32_16x16x32_bf16 v[136:139], a[100:103], a[20:23], v[136:139]\n" \
"ds_read_b128 a[76:79], v210 offset:49152\n" \
"v_mfma_f32_16x16x32_bf16 v[136:139], a[104:107], a[24:27], v[136:139]\n" \
"v_exp_f32_e64 v133, v133\n" \
"v_mfma_f32_16x16x32_bf16 v[136:139], a[108:111], a[28:31], v[136:139]\n" \
"v_cvt_pk_bf16_f32 v192, v128, v129\n" \
"v_cvt_pk_bf16_f32 v193, v130, v131\n" \
"v_mfma_f32_16x16x32_bf16 v[140:143], a[124:127], a[28:31], v[164:167]\n" \
"v_exp_f32_e64 v134, v134\n" \
"v_mfma_f32_16x16x32_bf16 v[140:143], a[120:123], a[24:27], v[140:143]\n" \
"ds_read_b128 a[92:95], v210 offset:53248\n" \
"v_mfma_f32_16x16x32_bf16 v[140:143], a[116:119], a[20:23], v[140:143]\n" \
"buffer_load_dwordx4 v215, %[srdv], s48 offen offset:1024 lds\n" \
"v_mfma_f32_16x16x32_bf16 v[140:143], a[112:115], a[16:19], v[140:143]\n" \
"v_exp_f32_e64 v135, v135\n" \
"v_mfma_f32_16x16x32_bf16 v[48:51], a[176:179], v[196:199], v[48:51]\n" \
"ds_read_b128 a[88:91], v209 offset:53248\n" \
"v_mfma_f32_16x16x32_bf16 v[48:51], a[144:147], v[188:191], v[48:51]\n" \
"v_exp_f32_e64 v136, v136\n" \
"v_mfma_f32_16x16x32_bf16 v[52:55], a[180:183], v[196:199], v[52:55]\n" \
"v_cvt_pk_bf16_f32 v194, v132, v133\n" \
"v_cvt_pk_bf16_f32 v195, v134, v135\n" \
"v_mfma_f32_16x16x32_bf16 v[52:55], a[148:151], v[188:191], v[52:55]\n" \
"v_exp_f32_e64 v137, v137\n" \
"v_mfma_f32_16x16x32_bf16 v[56:59], a[184:187], v[196:199], v[56:59]\n" \
"v_exp_f32_e64 v138, v138\n" \
"v_mfma_f32_16x16x32_bf16 v[56:59], a[152:155], v[188:191], v[56:59]\n" \
"ds_read_b128 a[84:87], v208 offset:53248\n" \
"v_mfma_f32_16x16x32_bf16 v[60:63], a[188:191], v[196:199], v[60:63]\n" \
"v_exp_f32_e64 v139, v139\n" \
"v_mfma_f32_16x16x32_bf16 v[60:63], a[156:159], v[188:191], v[60:63]\n" \
"s_cmp_lg_u32 %[wid], 1\n" \
"s_cbranch_scc1 LtA5_%=\n" \
"global_load_dword v237, v224, s[58:59]\n" \
"s_add_u32 s61, s48, 49152\n" \
"s_mov_b32 exec_hi, 0\n" \
"s_nop 0\n" \
"buffer_load_dword v237, v224, %[srdp], s61 offen\n" \
"s_mov_b32 exec_hi, -1\n" \
"s_nop 0\n" \
"LtA5_%=:\n" \
"v_mfma_f32_16x16x32_bf16 v[144:147], a[96:99], a[32:35], v[168:171]\n" \
"v_exp_f32_e64 v140, v140\n" \
"v_mfma_f32_16x16x32_bf16 v[144:147], a[100:103], a[36:39], v[144:147]\n" \
"ds_read_b128 a[80:83], %[kb0] offset:53248\n" \
"v_mfma_f32_16x16x32_bf16 v[144:147], a[104:107], a[40:43], v[144:147]\n" \
"v_exp_f32_e64 v141, v141\n" \
"v_mfma_f32_16x16x32_bf16 v[144:147], a[108:111], a[44:47], v[144:147]\n" \
"v_cvt_pk_bf16_f32 v196, v136, v137\n" \
"v_cvt_pk_bf16_f32 v197, v138, v139\n" \
"v_mfma_f32_16x16x32_bf16 v[148:151], a[124:127], a[44:47], v[168:171]\n" \
"v_exp_f32_e64 v142, v142\n" \
"v_mfma_f32_16x16x32_bf16 v[148:151], a[120:123], a[40:43], v[148:151]\n" \
"v_mfma_f32_16x16x32_bf16 v[148:151], a[116:119], a[36:39], v[148:151]\n" \
"buffer_load_dwordx4 %[vd0], %[srdv], s48 offen offset:2048 lds\n" \
"v_mfma_f32_16x16x32_bf16 v[148:151], a[112:115], a[32:35], v[148:151]\n" \
"v_exp_f32_e64 v143, v143\n" \
"v_mfma_f32_16x16x32_bf16 v[96:99], a[160:163], v[204:207], v[96:99]\n" \
"ds_read_b64_tr_b16 a[160:161], %[vvb] offset:40960\n" \
"v_mfma_f32_16x16x32_bf16 v[96:99], a[128:131], v[220:223], v[96:99]\n" \
"v_exp_f32_e64 v144, v144\n" \
"v_mfma_f32_16x16x32_bf16 v[100:103], a[164:167], v[204:207], v[100:103]\n" \
"v_cvt_pk_bf16_f32 v198, v140, v141\n" \
"v_cvt_pk_bf16_f32 v199, v142, v143\n" \
"v_mfma_f32_16x16x32_bf16 v[100:103], a[132:135], v[220:223], v[100:103]\n" \
"v_exp_f32_e64 v145, v145\n" \
"v_mfma_f32_16x16x32_bf16 v[104:107], a[168:171], v[204:207], v[104:107]\n" \
"ds_read_b64_tr_b16 a[162:163], %[vvb] offset:45056\n" \
".if . - adaln_body != 16384\n" \
".error \"size model\"\n" \
".endif\n" \
"Lk3_%=:\n" \
"s_cbranch_execz Lk4_%=\n" \
"s_nop 0\n" \
"ds_read_b64_tr_b16 a[164:165], v211 offset:40960\n" \
"v_mfma_f32_16x16x32_bf16 v[104:107], a[136:139], v[220:223], v[104:107]\n" \
"v_exp_f32_e64 v146, v146\n" \
"v_mfma_f32_16x16x32_bf16 v[108:111], a[172:175], v[204:207], v[108:111]\n" \
"ds_read_b64_tr_b16 a[166:167], v211 offset:45056\n" \
"v_mfma_f32_16x16x32_bf16 v[108:111], a[140:143], v[220:223], v[108:111]\n" \
"v_exp_f32_e64 v147, v147\n" \
"v_smfmac_f32_16x16x64_bf16 v[180:183], a[192:195], v[216:223], v228\n" \
"ds_read_b64_tr_b16 a[168:169], %[vvb] offset:41472\n" \
"v_mfma_f32_16x16x32_bf16 v[152:155], a[96:99], a[48:51], v[172:175]\n" \
"v_exp_f32_e64 v148, v148\n" \
"v_mfma_f32_16x16x32_bf16 v[152:155], a[100:103], a[52:55], v[152:155]\n" \
"ds_read_b64_tr_b16 a[170:171], %[vvb] offset:45568\n" \
"v_mfma_f32_16x16x32_bf16 v[152:155], a[104:107], a[56:59], v[152:155]\n" \
"v_exp_f32_e64 v149, v149\n" \
"v_mfma_f32_16x16x32_bf16 v[152:155], a[108:111], a[60:63], v[152:155]\n" \
"v_cvt_pk_bf16_f32 v200, v144, v145\n" \
"v_cvt_pk_bf16_f32 v201, v146, v147\n" \
"v_mfma_f32_16x16x32_bf16 v[156:159], a[124:127], a[60:63], v[172:175]\n" \
"v_exp_f32_e64 v150, v150\n" \
"v_mfma_f32_16x16x32_bf16 v[156:159], a[120:123], a[56:59], v[156:159]\n" \
"ds_read_b64_tr_b16 a[172:173], v211 offset:41472\n" \
"v_mfma_f32_16x16x32_bf16 v[156:159], a[116:119], a[52:55], v[156:159]\n" \
"buffer_load_dwordx4 v215, %[srdv], s48 offen offset:3072 lds\n" \
"v_mfma_f32_16x16x32_bf16 v[156:159], a[112:115], a[48:51], v[156:159]\n" \
"v_exp_f32_e64 v151, v151\n" \
"v_mfma_f32_16x16x32_bf16 v[112:115], a[176:179], v[204:207], v[112:115]\n" \
"ds_read_b64_tr_b16 a[174:175], v211 offset:45568\n" \
"ds_read_b64_tr_b16 a[176:177], %[vvb] offset:41984\n" \
"v_mfma_f32_16x16x32_bf16 v[112:115], a[144:147], v[220:223], v[112:115]\n" \
"v_exp_f32_e64 v152, v152\n" \
"v_mfma_f32_16x16x32_bf16 v[116:119], a[180:183], v[204:207], v[116:119]\n" \
"v_cvt_pk_bf16_f32 v202, v148, v149\n" \
"v_cvt_pk_bf16_f32 v203, v150, v151\n" \
"v_mfma_f32_16x16x32_bf16 v[116:119], a[148:151], v[220:223], v[116:119]\n" \
"v_exp_f32_e64 v153, v153\n" \
"v_mfma_f32_16x16x32_bf16 v[120:123], a[184:187], v[204:207], v[120:123]\n" \
"v_exp_f32_e64 v154, v154\n" \
"v_mfma_f32_16x16x32_bf16 v[120:123], a[152:155], v[220:223], v[120:123]\n" \
"ds_read_b64_tr_b16 a[178:179], %[vvb] offset:46080\n" \
"v_mfma_f32_16x16x32_bf16 v[124:127], a[188:191], v[204:207], v[124:127]\n" \
"v_exp_f32_e64 v155, v155\n" \
"v_mfma_f32_16x16x32_bf16 v[124:127], a[156:159], v[220:223], v[124:127]\n" \
"ds_read_b64_tr_b16 a[180:181], v211 offset:41984\n" \
"s_waitcnt lgkmcnt(13)\n" \
"v_mfma_f32_16x16x32_bf16 v[128:131], a[64:67], a[0:3], v[160:163]\n" \
"v_exp_f32_e32 v156, v156\n" \
"v_mfma_f32_16x16x32_bf16 v[128:131], a[68:71], a[4:7], v[128:131]\n" \
"ds_read_b64_tr_b16 a[182:183], v211 offset:46080\n" \
"s_add_u32 m0, %[m0b], 32768\n" \
"v_mfma_f32_16x16x32_bf16 v[128:131], a[72:75], a[8:11], v[128:131]\n" \
"v_exp_f32_e64 v157, v157\n" \
"v_mfma_f32_16x16x32_bf16 v[128:131], a[76:79], a[12:15], v[128:131]\n" \
"v_cvt_pk_bf16_f32 v204, v152, v153\n" \
"v_cvt_pk_bf16_f32 v205, v154, v155\n" \
"v_mfma_f32_16x16x32_bf16 v[132:135], a[92:95], a[12:15], v[160:163]\n" \
"v_exp_f32_e64 v158, v158\n" \
"v_mfma_f32_16x16x32_bf16 v[132:135], a[88:91], a[8:11], v[132:135]\n" \
"ds_read_b64_tr_b16 a[184:185], %[vvb] offset:42496\n" \
"s_waitcnt lgkmcnt(5)\n" \
"v_mfma_f32_16x16x32_bf16 v[132:135], a[84:87], a[4:7], v[132:135]\n" \
"buffer_load_dwordx4 %[kd0], %[srdk], s48 offen offset:0 lds\n" \
"v_mfma_f32_16x16x32_bf16 v[132:135], a[80:83], a[0:3], v[132:135]\n" \
"v_exp_f32_e32 v159, v159\n" \
"v_mfma_f32_16x16x32_bf16 v[64:67], a[128:131], v[216:219], v[64:67]\n" \
"ds_read_b64_tr_b16 a[186:187], %[vvb] offset:46592\n" \
"ds_read_b64_tr_b16 a[188:189], v211 offset:42496\n" \
"v_mfma_f32_16x16x32_bf16 v[64:67], a[160:163], v[200:203], v[64:67]\n" \
"v_exp_f32_e64 v128, v128\n" \
"v_mfma_f32_16x16x32_bf16 v[68:71], a[132:135], v[216:219], v[68:71]\n" \
"v_cvt_pk_bf16_f32 v206, v156, v157\n" \
"v_cvt_pk_bf16_f32 v207, v158, v159\n" \
"v_mfma_f32_16x16x32_bf16 v[68:71], a[164:167], v[200:203], v[68:71]\n" \
"v_exp_f32_e64 v129, v129\n" \
"v_mfma_f32_16x16x32_bf16 v[72:75], a[136:139], v[216:219], v[72:75]\n" \
"ds_read_b64_tr_b16 a[190:191], v211 offset:46592\n" \
"ds_read_b64_tr_b16 a[128:129], %[vvb] offset:49152\n" \
"v_mfma_f32_16x16x32_bf16 v[72:75], a[168:171], v[200:203], v[72:75]\n" \
"v_exp_f32_e64 v130, v130\n" \
"v_mfma_f32_16x16x32_bf16 v[76:79], a[140:143], v[216:219], v[76:79]\n" \
"ds_read_b64_tr_b16 a[130:131], %[vvb] offset:53248\n" \
"ds_read_b64_tr_b16 a[132:133], v211 offset:49152\n" \
"v_mfma_f32_16x16x32_bf16 v[76:79], a[172:175], v[200:203], v[76:79]\n" \
"v_exp_f32_e64 v131, v131\n" \
"v_smfmac_f32_16x16x64_bf16 v[176:179], a[192:195], v[192:199], v228\n" \
"ds_read_b64_tr_b16 a[134:135], v211 offset:53248\n" \
"ds_read_b64_tr_b16 a[136:137], %[vvb] offset:49664\n" \
"v_mfma_f32_16x16x32_bf16 v[136:139], a[64:67], a[16:19], v[164:167]\n" \
"v_exp_f32_e64 v132, v132\n" \
"v_mfma_f32_16x16x32_bf16 v[136:139], a[68:71], a[20:23], v[136:139]\n" \
"ds_read_b64_tr_b16 a[138:139], %[vvb] offset:53760\n" \
"ds_read_b64_tr_b16 a[140:141], v211 offset:49664\n" \
"v_mfma_f32_16x16x32_bf16 v[136:139], a[72:75], a[24:27], v[136:139]\n" \
"v_exp_f32_e64 v133, v133\n" \
"v_mfma_f32_16x16x32_bf16 v[136:139], a[76:79], a[28:31], v[136:139]\n" \
"v_cvt_pk_bf16_f32 v184, v128, v129\n" \
"v_cvt_pk_bf16_f32 v185, v130, v131\n" \
"v_mfma_f32_16x16x32_bf16 v[140:143], a[92:95], a[28:31], v[164:167]\n" \
"v_exp_f32_e64 v134, v134\n" \
"v_mfma_f32_16x16x32_bf16 v[140:143], a[88:91], a[24:27], v[140:143]\n" \
"ds_read_b64_tr_b16 a[142:143], v211 offset:53760\n" \
"s_waitcnt lgkmcnt(8)\n" \
"v_mfma_f32_16x16x32_bf16 v[140:143], a[84:87], a[20:23], v[140:143]\n" \
"buffer_load_dwordx4 v212, %[srdk], s48 offen offset:1024 lds\n" \
"v_mfma_f32_16x16x32_bf16 v[140:143], a[80:83], a[16:19], v[140:143]\n" \
"v_exp_f32_e32 v135, v135\n" \
"v_mfma_f32_16x16x32_bf16 v[80:83], a[144:147], v[216:219], v[80:83]\n" \
"ds_read_b64_tr_b16 a[144:145], %[vvb] offset:50176\n" \
"ds_read_b64_tr_b16 a[146:147], %[vvb] offset:54272\n" \
"v_mfma_f32_16x16x32_bf16 v[80:83], a[176:179], v[200:203], v[80:83]\n" \
"v_exp_f32_e64 v136, v136\n" \
"v_mfma_f32_16x16x32_bf16 v[84:87], a[148:151], v[216:219], v[84:87]\n" \
"v_cvt_pk_bf16_f32 v186, v132, v133\n" \
"v_cvt_pk_bf16_f32 v187, v134, v135\n" \
"v_mfma_f32_16x16x32_bf16 v[84:87], a[180:183], v[200:203], v[84:87]\n" \
"v_exp_f32_e64 v137, v137\n" \
"v_mfma_f32_16x16x32_bf16 v[88:91], a[152:155], v[216:219], v[88:91]\n" \
"v_exp_f32_e64 v138, v138\n" \
"v_mfma_f32_16x16x32_bf16 v[88:91], a[184:187], v[200:203], v[88:91]\n" \
"ds_read_b64_tr_b16 a[148:149], v211 offset:50176\n" \
"ds_read_b64_tr_b16 a[150:151], v211 offset:54272\n" \
"v_mfma_f32_16x16x32_bf16 v[92:95], a[156:159], v[216:219], v[92:95]\n" \
"v_exp_f32_e64 v139, v139\n" \
"v_mfma_f32_16x16x32_bf16 v[92:95], a[188:191], v[200:203], v[92:95]\n" \
"ds_read_b64_tr_b16 a[152:153], %[vvb] offset:50688\n" \
"global_load_dword v237, %[vct], %[pct]\n" \
"v_mfma_f32_16x16x32_bf16 v[144:147], a[64:67], a[32:35], v[168:171]\n" \
"v_exp_f32_e64 v140, v140\n" \
"v_mfma_f32_16x16x32_bf16 v[144:147], a[68:71], a[36:39], v[144:147]\n" \
"ds_read_b64_tr_b16 a[154:155], %[vvb] offset:54784\n" \
"ds_read_b64_tr_b16 a[156:157], v211 offset:50688\n" \
"v_mfma_f32_16x16x32_bf16 v[144:147], a[72:75], a[40:43], v[144:147]\n" \
"v_exp_f32_e64 v141, v141\n" \
"v_mfma_f32_16x16x32_bf16 v[144:147], a[76:79], a[44:47], v[144:147]\n" \
"v_cvt_pk_bf16_f32 v188, v136, v137\n" \
"v_cvt_pk_bf16_f32 v189, v138, v139\n" \
"v_mfma_f32_16x16x32_bf16 v[148:151], a[92:95], a[44:47], v[168:171]\n" \
"v_exp_f32_e64 v142, v142\n" \
"v_mfma_f32_16x16x32_bf16 v[148:151], a[88:91], a[40:43], v[148:151]\n" \
"ds_read_b64_tr_b16 a[158:159], v211 offset:54784\n" \
"s_waitcnt lgkmcnt(8)\n" \
"v_mfma_f32_16x16x32_bf16 v[148:151], a[84:87], a[36:39], v[148:151]\n" \
"buffer_load_dwordx4 v213, %[srdk], s48 offen offset:2048 lds\n" \
"v_mfma_f32_16x16x32_bf16 v[148:151], a[80:83], a[32:35], v[148:151]\n" \
"v_exp_f32_e32 v143, v143\n" \
"v_mfma_f32_16x16x32_bf16 v[0:3], a[160:163], v[192:195], v[0:3]\n" \
"ds_read_b128 a[96:99], %[kb0] offset:57344\n" \
"v_mfma_f32_16x16x32_bf16 v[0:3], a[128:131], v[184:187], v[0:3]\n" \
"v_exp_f32_e64 v144, v144\n" \
"v_mfma_f32_16x16x32_bf16 v[4:7], a[164:167], v[192:195], v[4:7]\n" \
"v_cvt_pk_bf16_f32 v190, v140, v141\n" \
"v_cvt_pk_bf16_f32 v191, v142, v143\n" \
"v_mfma_f32_16x16x32_bf16 v[4:7], a[132:135], v[184:187], v[4:7]\n" \
"v_exp_f32_e64 v145, v145\n" \
"v_mfma_f32_16x16x32_bf16 v[8:11], a[168:171], v[192:195], v[8:11]\n" \
"ds_read_b128 a[100:103], v208 offset:57344\n" \
"v_mfma_f32_16x16x32_bf16 v[8:11], a[136:139], v[184:187], v[8:11]\n" \
"v_exp_f32_e64 v146, v146\n" \
"v_mfma_f32_16x16x32_bf16 v[12:15], a[172:175], v[192:195], v[12:15]\n" \
"ds_read_b128 a[104:107], v209 offset:57344\n" \
"v_mfma_f32_16x16x32_bf16 v[12:15], a[140:143], v[184:187], v[12:15]\n" \
"v_exp_f32_e64 v147, v147\n" \
"v_smfmac_f32_16x16x64_bf16 v[180:183], a[192:195], v[200:207], v228\n" \
"ds_read_b128 a[108:111], v210 offset:57344\n" \
"v_mfma_f32_16x16x32_bf16 v[152:155], a[64:67], a[48:51], v[172:175]\n" \
"v_exp_f32_e64 v148, v148\n" \
"v_mfma_f32_16x16x32_bf16 v[152:155], a[68:71], a[52:55], v[152:155]\n" \
"ds_read_b128 a[124:127], v210 offset:61440\n" \
"v_mfma_f32_16x16x32_bf16 v[152:155], a[72:75], a[56:59], v[152:155]\n" \
"v_exp_f32_e64 v149, v149\n" \
"v_mfma_f32_16x16x32_bf16 v[152:155], a[76:79], a[60:63], v[152:155]\n" \
"v_cvt_pk_bf16_f32 v216, v144, v145\n" \
"v_cvt_pk_bf16_f32 v217, v146, v147\n" \
"v_mfma_f32_16x16x32_bf16 v[156:159], a[92:95], a[60:63], v[172:175]\n" \
"v_exp_f32_e64 v150, v150\n" \
"v_mfma_f32_16x16x32_bf16 v[156:159], a[88:91], a[56:59], v[156:159]\n" \
"ds_read_b128 a[120:123], v209 offset:61440\n" \
"s_waitcnt lgkmcnt(6)\n" \
"v_mfma_f32_16x16x32_bf16 v[156:159], a[84:87], a[52:55], v[156:159]\n" \
"buffer_load_dwordx4 v214, %[srdk], s48 offen offset:3072 lds\n" \
"v_mfma_f32_16x16x32_bf16 v[156:159], a[80:83], a[48:51], v[156:159]\n" \
"v_exp_f32_e32 v151, v151\n" \
"v_mfma_f32_16x16x32_bf16 v[16:19], a[176:179], v[192:195], v[16:19]\n" \
"ds_read_b128 a[116:119], v208 offset:61440\n" \
"v_mfma_f32_16x16x32_bf16 v[16:19], a[144:147], v[184:187], v[16:19]\n" \
"v_exp_f32_e64 v152, v152\n" \
"v_mfma_f32_16x16x32_bf16 v[20:23], a[180:183], v[192:195], v[20:23]\n" \
"v_cvt_pk_bf16_f32 v218, v148, v149\n" \
"v_cvt_pk_bf16_f32 v219, v150, v151\n" \
"v_mfma_f32_16x16x32_bf16 v[20:23], a[148:151], v[184:187], v[20:23]\n" \
"v_exp_f32_e64 v153, v153\n" \
"v_mfma_f32_16x16x32_bf16 v[24:27], a[184:187], v[192:195], v[24:27]\n" \
"v_exp_f32_e64 v154, v154\n" \
"v_mfma_f32_16x16x32_bf16 v[24:27], a[152:155], v[184:187], v[24:27]\n" \
"ds_read_b128 a[112:115], %[kb0] offset:61440\n" \
"s_add_u32 s48, s48, 0x4000\n" \
"s_mov_b32 m0, s44\n" \
"s_addk_i32 s44, 0x400\n" \
"buffer_load_dwordx4 v229, s[64:67], s45 offen offset:0 nt lds\n" \
"s_add_u32 s45, s45, 393216\n" \
"v_mfma_f32_16x16x32_bf16 v[28:31], a[188:191], v[192:195], v[28:31]\n" \
"v_exp_f32_e64 v155, v155\n" \
"v_mfma_f32_16x16x32_bf16 v[28:31], a[156:159], v[184:187], v[28:31]\n" \
"s_waitcnt vmcnt(14) lgkmcnt(2)\n" \
"s_barrier\n" \
"v_mfma_f32_16x16x32_bf16 v[128:131], a[96:99], a[0:3], v[160:163]\n" \
"v_exp_f32_e64 v156, v156\n" \
"v_mfma_f32_16x16x32_bf16 v[128:131], a[100:103], a[4:7], v[128:131]\n" \
"s_add_u32 m0, %[m0b], 98304\n" \
"v_mfma_f32_16x16x32_bf16 v[128:131], a[104:107], a[8:11], v[128:131]\n" \
"v_exp_f32_e64 v157, v157\n" \
"v_mfma_f32_16x16x32_bf16 v[128:131], a[108:111], a[12:15], v[128:131]\n" \
"v_cvt_pk_bf16_f32 v220, v152, v153\n" \
"v_cvt_pk_bf16_f32 v221, v154, v155\n" \
"v_mfma_f32_16x16x32_bf16 v[132:135], a[124:127], a[12:15], v[160:163]\n" \
"v_exp_f32_e64 v158, v158\n" \
"v_mfma_f32_16x16x32_bf16 v[132:135], a[120:123], a[8:11], v[132:135]\n" \
"s_waitcnt lgkmcnt(0)\n" \
"v_mfma_f32_16x16x32_bf16 v[132:135], a[116:119], a[4:7], v[132:135]\n" \
"buffer_load_dwordx4 %[vd0], %[srdv], s48 offen offset:0 lds\n" \
"v_mfma_f32_16x16x32_bf16 v[132:135], a[112:115], a[0:3], v[132:135]\n" \
"v_exp_f32_e32 v159, v159\n" \
"v_mfma_f32_16x16x32_bf16 v[32:35], a[160:163], v[196:199], v[32:35]\n" \
"ds_read_b128 a[64:67], %[kb0] offset:0\n" \
"v_mfma_f32_16x16x32_bf16 v[32:35], a[128:131], v[188:191], v[32:35]\n" \
"v_exp_f32_e64 v128, v128\n" \
"v_mfma_f32_16x16x32_bf16 v[36:39], a[164:167], v[196:199], v[36:39]\n" \
"v_cvt_pk_bf16_f32 v222, v156, v157\n" \
"v_cvt_pk_bf16_f32 v223, v158, v159\n" \
"v_mfma_f32_16x16x32_bf16 v[36:39], a[132:135], v[188:191], v[36:39]\n" \
"v_exp_f32_e64 v129, v129\n" \
"v_mfma_f32_16x16x32_bf16 v[40:43], a[168:171], v[196:199], v[40:43]\n" \
"ds_read_b128 a[68:71], v208 offset:0\n" \
"v_mfma_f32_16x16x32_bf16 v[40:43], a[136:139], v[188:191], v[40:43]\n" \
"v_exp_f32_e64 v130, v130\n" \
"v_mfma_f32_16x16x32_bf16 v[44:47], a[172:175], v[196:199], v[44:47]\n" \
"ds_read_b128 a[72:75], v209 offset:0\n" \
"v_mfma_f32_16x16x32_bf16 v[44:47], a[140:143], v[188:191], v[44:47]\n" \
"v_exp_f32_e64 v131, v131\n" \
"v_smfmac_f32_16x16x64_bf16 v[176:179], a[192:195], v[184:191], v228\n" \
"v_mfma_f32_16x16x32_bf16 v[136:139], a[96:99], a[16:19], v[164:167]\n" \
"v_exp_f32_e64 v132, v132\n" \
"v_mfma_f32_16x16x32_bf16 v[136:139], a[100:103], a[20:23], v[136:139]\n" \
"ds_read_b128 a[76:79], v210 offset:0\n" \
"v_mfma_f32_16x16x32_bf16 v[136:139], a[104:107], a[24:27], v[136:139]\n" \
"v_exp_f32_e64 v133, v133\n" \
"v_mfma_f32_16x16x32_bf16 v[136:139], a[108:111], a[28:31], v[136:139]\n" \
"v_cvt_pk_bf16_f32 v192, v128, v129\n" \
"v_cvt_pk_bf16_f32 v193, v130, v131\n" \
"v_mfma_f32_16x16x32_bf16 v[140:143], a[124:127], a[28:31], v[164:167]\n" \
"v_exp_f32_e64 v134, v134\n" \
"v_mfma_f32_16x16x32_bf16 v[140:143], a[120:123], a[24:27], v[140:143]\n" \
"ds_read_b128 a[92:95], v210 offset:4096\n" \
"v_mfma_f32_16x16x32_bf16 v[140:143], a[116:119], a[20:23], v[140:143]\n" \
"buffer_load_dwordx4 v215, %[srdv], s48 offen offset:1024 lds\n" \
"v_mfma_f32_16x16x32_bf16 v[140:143], a[112:115], a[16:19], v[140:143]\n" \
"v_exp_f32_e64 v135, v135\n" \
"v_mfma_f32_16x16x32_bf16 v[48:51], a[176:179], v[196:199], v[48:51]\n" \
"ds_read_b128 a[88:91], v209 offset:4096\n" \
"v_mfma_f32_16x16x32_bf16 v[48:51], a[144:147], v[188:191], v[48:51]\n" \
"v_exp_f32_e64 v136, v136\n" \
"v_mfma_f32_16x16x32_bf16 v[52:55], a[180:183], v[196:199], v[52:55]\n" \
"v_cvt_pk_bf16_f32 v194, v132, v133\n" \
"v_cvt_pk_bf16_f32 v195, v134, v135\n" \
"v_mfma_f32_16x16x32_bf16 v[52:55], a[148:151], v[188:191], v[52:55]\n" \
"v_exp_f32_e64 v137, v137\n" \
"v_mfma_f32_16x16x32_bf16 v[56:59], a[184:187], v[196:199], v[56:59]\n" \
"v_exp_f32_e64 v138, v138\n" \
"v_mfma_f32_16x16x32_bf16 v[56:59], a[152:155], v[188:191], v[56:59]\n" \
"ds_read_b128 a[84:87], v208 offset:4096\n" \
"v_mfma_f32_16x16x32_bf16 v[60:63], a[188:191], v[196:199], v[60:63]\n" \
"v_exp_f32_e64 v139, v139\n" \
"v_mfma_f32_16x16x32_bf16 v[60:63], a[156:159], v[188:191], v[60:63]\n" \
"s_cmp_lg_u32 %[wid], 2\n" \
"s_cbranch_scc1 LtA7_%=\n" \
"global_load_dword v237, v224, s[58:59]\n" \
"s_add_u32 s61, s48, 49152\n" \
"s_mov_b32 exec_hi, 0\n" \
"s_nop 0\n" \
"buffer_load_dword v237, v224, %[srdp], s61 offen\n" \
"s_mov_b32 exec_hi, -1\n" \
"s_nop 0\n" \
"LtA7_%=:\n" \
"v_mfma_f32_16x16x32_bf16 v[144:147], a[96:99], a[32:35], v[168:171]\n" \
"v_exp_f32_e64 v140, v140\n" \
"v_mfma_f32_16x16x32_bf16 v[144:147], a[100:103], a[36:39], v[144:147]\n" \
"ds_read_b128 a[80:83], %[kb0] offset:4096\n" \
"v_mfma_f32_16x16x32_bf16 v[144:147], a[104:107], a[40:43], v[144:147]\n" \
"v_exp_f32_e64 v141, v141\n" \
"v_mfma_f32_16x16x32_bf16 v[144:147], a[108:111], a[44:47], v[144:147]\n" \
"v_cvt_pk_bf16_f32 v196, v136, v137\n" \
"v_cvt_pk_bf16_f32 v197, v138, v139\n" \
"v_mfma_f32_16x16x32_bf16 v[148:151], a[124:127], a[44:47], v[168:171]\n" \
"v_exp_f32_e64 v142, v142\n" \
"v_mfma_f32_16x16x32_bf16 v[148:151], a[120:123], a[40:43], v[148:151]\n" \
"v_mfma_f32_16x16x32_bf16 v[148:151], a[116:119], a[36:39], v[148:151]\n" \
"buffer_load_dwordx4 %[vd0], %[srdv], s48 offen offset:2048 lds\n" \
"v_mfma_f32_16x16x32_bf16 v[148:151], a[112:115], a[32:35], v[148:151]\n" \
"v_exp_f32_e64 v143, v143\n" \
"v_mfma_f32_16x16x32_bf16 v[96:99], a[160:163], v[204:207], v[96:99]\n" \
"ds_read_b64_tr_b16 a[160:161], %[vvb] offset:57344\n" \
"v_mfma_f32_16x16x32_bf16 v[96:99], a[128:131], v[220:223], v[96:99]\n" \
"v_exp_f32_e64 v144, v144\n" \
"v_mfma_f32_16x16x32_bf16 v[100:103], a[164:167], v[204:207], v[100:103]\n" \
"v_cvt_pk_bf16_f32 v198, v140, v141\n" \
"v_cvt_pk_bf16_f32 v199, v142, v143\n" \
"v_mfma_f32_16x16x32_bf16 v[100:103], a[132:135], v[220:223], v[100:103]\n" \
"v_exp_f32_e64 v145, v145\n" \
"v_mfma_f32_16x16x32_bf16 v[104:107], a[168:171], v[204:207], v[104:107]\n" \
"ds_read_b64_tr_b16 a[162:163], %[vvb] offset:61440\n" \
"ds_read_b64_tr_b16 a[164:165], v211 offset:57344\n" \
"v_mfma_f32_16x16x32_bf16 v[104:107], a[136:139], v[220:223], v[104:107]\n" \
"v_exp_f32_e64 v146, v146\n" \
"v_mfma_f32_16x16x32_bf16 v[108:111], a[172:175], v[204:207], v[108:111]\n" \
"ds_read_b64_tr_b16 a[166:167], v211 offset:61440\n" \
"v_mfma_f32_16x16x32_bf16 v[108:111], a[140:143], v[220:223], v[108:111]\n" \
"v_exp_f32_e64 v147, v147\n" \
"v_smfmac_f32_16x16x64_bf16 v[180:183], a[192:195], v[216:223], v228\n" \
"ds_read_b64_tr_b16 a[168:169], %[vvb] offset:57856\n" \
"v_mfma_f32_16x16x32_bf16 v[152:155], a[96:99], a[48:51], v[172:175]\n" \
"v_exp_f32_e64 v148, v148\n" \
"v_mfma_f32_16x16x32_bf16 v[152:155], a[100:103], a[52:55], v[152:155]\n" \
"ds_read_b64_tr_b16 a[170:171], %[vvb] offset:61952\n" \
"v_mfma_f32_16x16x32_bf16 v[152:155], a[104:107], a[56:59], v[152:155]\n" \
"v_exp_f32_e64 v149, v149\n" \
"v_mfma_f32_16x16x32_bf16 v[152:155], a[108:111], a[60:63], v[152:155]\n" \
"v_cvt_pk_bf16_f32 v200, v144, v145\n" \
"v_cvt_pk_bf16_f32 v201, v146, v147\n" \
"v_mfma_f32_16x16x32_bf16 v[156:159], a[124:127], a[60:63], v[172:175]\n" \
"v_exp_f32_e64 v150, v150\n" \
"v_mfma_f32_16x16x32_bf16 v[156:159], a[120:123], a[56:59], v[156:159]\n" \
"ds_read_b64_tr_b16 a[172:173], v211 offset:57856\n" \
"v_mfma_f32_16x16x32_bf16 v[156:159], a[116:119], a[52:55], v[156:159]\n" \
"buffer_load_dwordx4 v215, %[srdv], s48 offen offset:3072 lds\n" \
"v_mfma_f32_16x16x32_bf16 v[156:159], a[112:115], a[48:51], v[156:159]\n" \
"v_exp_f32_e64 v151, v151\n" \
"v_mfma_f32_16x16x32_bf16 v[112:115], a[176:179], v[204:207], v[112:115]\n" \
"ds_read_b64_tr_b16 a[174:175], v211 offset:61952\n" \
"ds_read_b64_tr_b16 a[176:177], %[vvb] offset:58368\n" \
"v_mfma_f32_16x16x32_bf16 v[112:115], a[144:147], v[220:223], v[112:115]\n" \
"v_exp_f32_e64 v152, v152\n" \
"v_mfma_f32_16x16x32_bf16 v[116:119], a[180:183], v[204:207], v[116:119]\n" \
"v_cvt_pk_bf16_f32 v202, v148, v149\n" \
"v_cvt_pk_bf16_f32 v203, v150, v151\n" \
"v_mfma_f32_16x16x32_bf16 v[116:119], a[148:151], v[220:223], v[116:119]\n" \
"v_exp_f32_e64 v153, v153\n" \
"v_mfma_f32_16x16x32_bf16 v[120:123], a[184:187], v[204:207], v[120:123]\n" \
"v_exp_f32_e64 v154, v154\n" \
"v_mfma_f32_16x16x32_bf16 v[120:123], a[152:155], v[220:223], v[120:123]\n" \
"ds_read_b64_tr_b16 a[178:179], %[vvb] offset:62464\n" \
"v_mfma_f32_16x16x32_bf16 v[124:127], a[188:191], v[204:207], v[124:127]\n" \
"v_exp_f32_e64 v155, v155\n" \
"v_mfma_f32_16x16x32_bf16 v[124:127], a[156:159], v[220:223], v[124:127]\n" \
"ds_read_b64_tr_b16 a[180:181], v211 offset:58368\n" \
"s_waitcnt lgkmcnt(13)\n" \
"v_mfma_f32_16x16x32_bf16 v[128:131], a[64:67], a[0:3], v[160:163]\n" \
"v_exp_f32_e32 v156, v156\n" \
"v_mfma_f32_16x16x32_bf16 v[128:131], a[68:71], a[4:7], v[128:131]\n" \
"ds_read_b64_tr_b16 a[182:183], v211 offset:62464\n" \
"s_add_u32 m0, %[m0b], 49152\n" \
"v_mfma_f32_16x16x32_bf16 v[128:131], a[72:75], a[8:11], v[128:131]\n" \
"v_exp_f32_e64 v157, v157\n" \
"v_mfma_f32_16x16x32_bf16 v[128:131], a[76:79], a[12:15], v[128:131]\n" \
"v_cvt_pk_bf16_f32 v204, v152, v153\n" \
"v_cvt_pk_bf16_f32 v205, v154, v155\n" \
"v_mfma_f32_16x16x32_bf16 v[132:135], a[92:95], a[12:15], v[160:163]\n" \
"v_exp_f32_e64 v158, v158\n" \
"v_mfma_f32_16x16x32_bf16 v[132:135], a[88:91], a[8:11], v[132:135]\n" \
"ds_read_b64_tr_b16 a[184:185], %[vvb] offset:58880\n" \
"s_waitcnt lgkmcnt(5)\n" \
"v_mfma_f32_16x16x32_bf16 v[132:135], a[84:87], a[4:7], v[132:135]\n" \
"buffer_load_dwordx4 %[kd0], %[srdk], s48 offen offset:0 lds\n" \
"v_mfma_f32_16x16x32_bf16 v[132:135], a[80:83], a[0:3], v[132:135]\n" \
"v_exp_f32_e32 v159, v159\n" \
"v_mfma_f32_16x16x32_bf16 v[64:67], a[128:131], v[216:219], v[64:67]\n" \
"ds_read_b64_tr_b16 a[186:187], %[vvb] offset:62976\n" \
"ds_read_b64_tr_b16 a[188:189], v211 offset:58880\n" \
"v_mfma_f32_16x16x32_bf16 v[64:67], a[160:163], v[200:203], v[64:67]\n" \
"v_exp_f32_e64 v128, v128\n" \
"v_mfma_f32_16x16x32_bf16 v[68:71], a[132:135], v[216:219], v[68:71]\n" \
"v_cvt_pk_bf16_f32 v206, v156, v157\n" \
"v_cvt_pk_bf16_f32 v207, v158, v159\n" \
"v_mfma_f32_16x16x32_bf16 v[68:71], a[164:167], v[200:203], v[68:71]\n" \
"v_exp_f32_e64 v129, v129\n" \
"v_mfma_f32_16x16x32_bf16 v[72:75], a[136:139], v[216:219], v[72:75]\n" \
"ds_read_b64_tr_b16 a[190:191], v211 offset:62976\n" \
"ds_read_b64_tr_b16 a[128:129], %[vvb] offset:0\n" \
"v_mfma_f32_16x16x32_bf16 v[72:75], a[168:171], v[200:203], v[72:75]\n" \
"v_exp_f32_e64 v130, v130\n" \
"v_mfma_f32_16x16x32_bf16 v[76:79], a[140:143], v[216:219], v[76:79]\n" \
"ds_read_b64_tr_b16 a[130:131], %[vvb] offset:4096\n" \
"ds_read_b64_tr_b16 a[132:133], v211 offset:0\n" \
"v_mfma_f32_16x16x32_bf16 v[76:79], a[172:175], v[200:203], v[76:79]\n" \
"v_exp_f32_e64 v131, v131\n" \
"v_smfmac_f32_16x16x64_bf16 v[176:179], a[192:195], v[192:199], v228\n" \
"ds_read_b64_tr_b16 a[134:135], v211 offset:4096\n" \
"ds_read_b64_tr_b16 a[136:137], %[vvb] offset:512\n" \
"v_mfma_f32_16x16x32_bf16 v[136:139], a[64:67], a[16:19], v[164:167]\n" \
"v_exp_f32_e64 v132, v132\n" \
"v_mfma_f32_16x16x32_bf16 v[136:139], a[68:71], a[20:23], v[136:139]\n" \
"ds_read_b64_tr_b16 a[138:139], %[vvb] offset:4608\n" \
"ds_read_b64_tr_b16 a[140:141], v211 offset:512\n" \
"v_mfma_f32_16x16x32_bf16 v[136:139], a[72:75], a[24:27], v[136:139]\n" \
"v_exp_f32_e64 v133, v133\n" \
"v_mfma_f32_16x16x32_bf16 v[136:139], a[76:79], a[28:31], v[136:139]\n" \
"v_cvt_pk_bf16_f32 v184, v128, v129\n" \
"v_cvt_pk_bf16_f32 v185, v130, v131\n" \
"v_mfma_f32_16x16x32_bf16 v[140:143], a[92:95], a[28:31], v[164:167]\n" \
"v_exp_f32_e64 v134, v134\n" \
"v_mfma_f32_16x16x32_bf16 v[140:143], a[88:91], a[24:27], v[140:143]\n" \
"ds_read_b64_tr_b16 a[142:143], v211 offset:4608\n" \
"s_waitcnt lgkmcnt(8)\n" \
"v_mfma_f32_16x16x32_bf16 v[140:143], a[84:87], a[20:23], v[140:143]\n" \
"buffer_load_dwordx4 v212, %[srdk], s48 offen offset:1024 lds\n" \
"v_mfma_f32_16x16x32_bf16 v[140:143], a[80:83], a[16:19], v[140:143]\n" \
"v_exp_f32_e32 v135, v135\n" \
"v_mfma_f32_16x16x32_bf16 v[80:83], a[144:147], v[216:219], v[80:83]\n" \
"ds_read_b64_tr_b16 a[144:145], %[vvb] offset:1024\n" \
"ds_read_b64_tr_b16 a[146:147], %[vvb] offset:5120\n" \
"v_mfma_f32_16x16x32_bf16 v[80:83], a[176:179], v[200:203], v[80:83]\n" \
"v_exp_f32_e64 v136, v136\n" \
"v_mfma_f32_16x16x32_bf16 v[84:87], a[148:151], v[216:219], v[84:87]\n" \
"v_cvt_pk_bf16_f32 v186, v132, v133\n" \
"v_cvt_pk_bf16_f32 v187, v134, v135\n" \
"v_mfma_f32_16x16x32_bf16 v[84:87], a[180:183], v[200:203], v[84:87]\n" \
"v_exp_f32_e64 v137, v137\n" \
"v_mfma_f32_16x16x32_bf16 v[88:91], a[152:155], v[216:219], v[88:91]\n" \
"v_exp_f32_e64 v138, v138\n" \
"v_mfma_f32_16x16x32_bf16 v[88:91], a[184:187], v[200:203], v[88:91]\n" \
"ds_read_b64_tr_b16 a[148:149], v211 offset:1024\n" \
"ds_read_b64_tr_b16 a[150:151], v211 offset:5120\n" \
"v_mfma_f32_16x16x32_bf16 v[92:95], a[156:159], v[216:219], v[92:95]\n" \
"v_exp_f32_e64 v139, v139\n" \
"v_mfma_f32_16x16x32_bf16 v[92:95], a[188:191], v[200:203], v[92:95]\n" \
"ds_read_b64_tr_b16 a[152:153], %[vvb] offset:1536\n" \
"s_cbranch_vccz Lpp2_%=\n" \
"buffer_store_dwordx4 a[196:199], v235, %[srdz], s62 offen offset:1024 sc1\n" \
"buffer_store_dwordx4 a[196:199], v226, %[srdz], s62 offen offset:1024 sc1\n" \
"Lpp2_%=:\n" \
"s_add_u32 s62, s62, 2048\n" \
"v_mfma_f32_16x16x32_bf16 v[144:147], a[64:67], a[32:35], v[168:171]\n" \
"v_exp_f32_e32 v140, v140\n" \
"v_mfma_f32_16x16x32_bf16 v[144:147], a[68:71], a[36:39], v[144:147]\n" \
"ds_read_b64_tr_b16 a[154:155], %[vvb] offset:5632\n" \
"ds_read_b64_tr_b16 a[156:157], v211 offset:1536\n" \
"v_mfma_f32_16x16x32_bf16 v[144:147], a[72:75], a[40:43], v[144:147]\n" \
"v_exp_f32_e64 v141, v141\n" \
"v_mfma_f32_16x16x32_bf16 v[144:147], a[76:79], a[44:47], v[144:147]\n" \
"v_cvt_pk_bf16_f32 v188, v136, v137\n" \
"v_cvt_pk_bf16_f32 v189, v138, v139\n" \
"v_mfma_f32_16x16x32_bf16 v[148:151], a[92:95], a[44:47], v[168:171]\n" \
"v_exp_f32_e64 v142, v142\n" \
"v_mfma_f32_16x16x32_bf16 v[148:151], a[88:91], a[40:43], v[148:151]\n" \
"ds_read_b64_tr_b16 a[158:159], v211 offset:5632\n" \
"s_waitcnt lgkmcnt(8)\n" \
"v_mfma_f32_16x16x32_bf16 v[148:151], a[84:87], a[36:39], v[148:151]\n" \
"buffer_load_dwordx4 v213, %[srdk], s48 offen offset:2048 lds\n" \
"v_mfma_f32_16x16x32_bf16 v[148:151], a[80:83], a[32:35], v[148:151]\n" \
"v_exp_f32_e32 v143, v143\n" \
"v_mfma_f32_16x16x32_bf16 v[0:3], a[160:163], v[192:195], v[0:3]\n" \
"ds_read_b128 a[96:99], %[kb0] offset:8192\n" \
"v_mfma_f32_16x16x32_bf16 v[0:3], a[128:131], v[184:187], v[0:3]\n" \
"v_exp_f32_e64 v144, v144\n" \
"v_mfma_f32_16x16x32_bf16 v[4:7], a[164:167], v[192:195], v[4:7]\n" \
"v_cvt_pk_bf16_f32 v190, v140, v141\n" \
"v_cvt_pk_bf16_f32 v191, v142, v143\n" \
"v_mfma_f32_16x16x32_bf16 v[4:7], a[132:135], v[184:187], v[4:7]\n" \
"v_exp_f32_e64 v145, v145\n" \
"v_mfma_f32_16x16x32_bf16 v[8:11], a[168:171], v[192:195], v[8:11]\n" \
"ds_read_b128 a[100:103], v208 offset:8192\n" \
"v_mfma_f32_16x16x32_bf16 v[8:11], a[136:139], v[184:187], v[8:11]\n" \
"v_exp_f32_e64 v146, v146\n" \
"v_mfma_f32_16x16x32_bf16 v[12:15], a[172:175], v[192:195], v[12:15]\n" \
"ds_read_b128 a[104:107], v209 offset:8192\n" \
"v_mfma_f32_16x16x32_bf16 v[12:15], a[140:143], v[184:187], v[12:15]\n" \
"v_exp_f32_e64 v147, v147\n" \
"v_smfmac_f32_16x16x64_bf16 v[180:183], a[192:195], v[200:207], v228\n" \
"ds_read_b128 a[108:111], v210 offset:8192\n" \
"v_mfma_f32_16x16x32_bf16 v[152:155], a[64:67], a[48:51], v[172:175]\n" \
"v_exp_f32_e64 v148, v148\n" \
"v_mfma_f32_16x16x32_bf16 v[152:155], a[68:71], a[52:55], v[152:155]\n" \
"ds_read_b128 a[124:127], v210 offset:12288\n" \
"v_mfma_f32_16x16x32_bf16 v[152:155], a[72:75], a[56:59], v[152:155]\n" \
"v_exp_f32_e64 v149, v149\n" \
"v_mfma_f32_16x16x32_bf16 v[152:155], a[76:79], a[60:63], v[152:155]\n" \
"v_cvt_pk_bf16_f32 v216, v144, v145\n" \
"v_cvt_pk_bf16_f32 v217, v146, v147\n" \
"v_mfma_f32_16x16x32_bf16 v[156:159], a[92:95], a[60:63], v[172:175]\n" \
"v_exp_f32_e64 v150, v150\n" \
"v_mfma_f32_16x16x32_bf16 v[156:159], a[88:91], a[56:59], v[156:159]\n" \
"ds_read_b128 a[120:123], v209 offset:12288\n" \
"s_waitcnt lgkmcnt(6)\n" \
"v_mfma_f32_16x16x32_bf16 v[156:159], a[84:87], a[52:55], v[156:159]\n" \
"buffer_load_dwordx4 v214, %[srdk], s48 offen offset:3072 lds\n" \
"v_mfma_f32_16x16x32_bf16 v[156:159], a[80:83], a[48:51], v[156:159]\n" \
"v_exp_f32_e32 v151, v151\n" \
"v_mfma_f32_16x16x32_bf16 v[16:19], a[176:179], v[192:195], v[16:19]\n" \
"ds_read_b128 a[116:119], v208 offset:12288\n" \
"v_mfma_f32_16x16x32_bf16 v[16:19], a[144:147], v[184:187], v[16:19]\n" \
"v_exp_f32_e64 v152, v152\n" \
"v_mfma_f32_16x16x32_bf16 v[20:23], a[180:183], v[192:195], v[20:23]\n" \
"v_cvt_pk_bf16_f32 v218, v148, v149\n" \
"v_cvt_pk_bf16_f32 v219, v150, v151\n" \
"v_mfma_f32_16x16x32_bf16 v[20:23], a[148:151], v[184:187], v[20:23]\n" \
"v_exp_f32_e64 v153, v153\n" \
"v_mfma_f32_16x16x32_bf16 v[24:27], a[184:187], v[192:195], v[24:27]\n" \
"v_exp_f32_e64 v154, v154\n" \
"v_mfma_f32_16x16x32_bf16 v[24:27], a[152:155], v[184:187], v[24:27]\n" \
"ds_read_b128 a[112:115], %[kb0] offset:12288\n" \
"s_add_u32 s48, s48, 0x4000\n" \
"v_mfma_f32_16x16x32_bf16 v[28:31], a[188:191], v[192:195], v[28:31]\n" \
"v_exp_f32_e64 v155, v155\n" \
"v_mfma_f32_16x16x32_bf16 v[28:31], a[156:159], v[184:187], v[28:31]\n" \
"s_waitcnt vmcnt(14) lgkmcnt(2)\n" \
"s_barrier\n" \
"v_mfma_f32_16x16x32_bf16 v[128:131], a[96:99], a[0:3], v[160:163]\n" \
"v_exp_f32_e64 v156, v156\n" \
"v_mfma_f32_16x16x32_bf16 v[128:131], a[100:103], a[4:7], v[128:131]\n" \
"s_add_u32 m0, %[m0b], 114688\n" \
"v_mfma_f32_16x16x32_bf16 v[128:131], a[104:107], a[8:11], v[128:131]\n" \
"v_exp_f32_e64 v157, v157\n" \
"v_mfma_f32_16x16x32_bf16 v[128:131], a[108:111], a[12:15], v[128:131]\n" \
"v_cvt_pk_bf16_f32 v220, v152, v153\n" \
"v_cvt_pk_bf16_f32 v221, v154, v155\n" \
"v_mfma_f32_16x16x32_bf16 v[132:135], a[124:127], a[12:15], v[160:163]\n" \
"v_exp_f32_e64 v158, v158\n" \
"v_mfma_f32_16x16x32_bf16 v[132:135], a[120:123], a[8:11], v[132:135]\n" \
"s_waitcnt lgkmcnt(0)\n" \
"v_mfma_f32_16x16x32_bf16 v[132:135], a[116:119], a[4:7], v[132:135]\n" \
"buffer_load_dwordx4 %[vd0], %[srdv], s48 offen offset:0 lds\n" \
"v_mfma_f32_16x16x32_bf16 v[132:135], a[112:115], a[0:3], v[132:135]\n" \
"v_exp_f32_e32 v159, v159\n" \
".if . - adaln_body != 20480\n" \
".error \"size model\"\n" \
".endif\n" \
"Lk4_%=:\n" \
"s_cbranch_execz Lk5_%=\n" \
"s_nop 0\n" \
"v_mfma_f32_16x16x32_bf16 v[32:35], a[160:163], v[196:199], v[32:35]\n" \
"ds_read_b128 a[64:67], %[kb0] offset:16384\n" \
"v_mfma_f32_16x16x32_bf16 v[32:35], a[128:131], v[188:191], v[32:35]\n" \
"v_exp_f32_e64 v128, v128\n" \
"v_mfma_f32_16x16x32_bf16 v[36:39], a[164:167], v[196:199], v[36:39]\n" \
"v_cvt_pk_bf16_f32 v222, v156, v157\n" \
"v_cvt_pk_bf16_f32 v223, v158, v159\n" \
"v_mfma_f32_16x16x32_bf16 v[36:39], a[132:135], v[188:191], v[36:39]\n" \
"v_exp_f32_e64 v129, v129\n" \
"v_mfma_f32_16x16x32_bf16 v[40:43], a[168:171], v[196:199], v[40:43]\n" \
"ds_read_b128 a[68:71], v208 offset:16384\n" \
"v_mfma_f32_16x16x32_bf16 v[40:43], a[136:139], v[188:191], v[40:43]\n" \
"v_exp_f32_e64 v130, v130\n" \
"v_mfma_f32_16x16x32_bf16 v[44:47], a[172:175], v[196:199], v[44:47]\n" \
"ds_read_b128 a[72:75], v209 offset:16384\n" \
"v_mfma_f32_16x16x32_bf16 v[44:47], a[140:143], v[188:191], v[44:47]\n" \
"v_exp_f32_e64 v131, v131\n" \
"v_smfmac_f32_16x16x64_bf16 v[176:179], a[192:195], v[184:191], v228\n" \
"v_mfma_f32_16x16x32_bf16 v[136:139], a[96:99], a[16:19], v[164:167]\n" \
"v_exp_f32_e64 v132, v132\n" \
"v_mfma_f32_16x16x32_bf16 v[136:139], a[100:103], a[20:23], v[136:139]\n" \
"ds_read_b128 a[76:79], v210 offset:16384\n" \
"v_mfma_f32_16x16x32_bf16 v[136:139], a[104:107], a[24:27], v[136:139]\n" \
"v_exp_f32_e64 v133, v133\n" \
"v_mfma_f32_16x16x32_bf16 v[136:139], a[108:111], a[28:31], v[136:139]\n" \
"v_cvt_pk_bf16_f32 v192, v128, v129\n" \
"v_cvt_pk_bf16_f32 v193, v130, v131\n" \
"v_mfma_f32_16x16x32_bf16 v[140:143], a[124:127], a[28:31], v[164:167]\n" \
"v_exp_f32_e64 v134, v134\n" \
"v_mfma_f32_16x16x32_bf16 v[140:143], a[120:123], a[24:27], v[140:143]\n" \
"ds_read_b128 a[92:95], v210 offset:20480\n" \
"v_mfma_f32_16x16x32_bf16 v[140:143], a[116:119], a[20:23], v[140:143]\n" \
"buffer_load_dwordx4 v215, %[srdv], s48 offen offset:1024 lds\n" \
"v_mfma_f32_16x16x32_bf16 v[140:143], a[112:115], a[16:19], v[140:143]\n" \
"v_exp_f32_e64 v135, v135\n" \
"v_mfma_f32_16x16x32_bf16 v[48:51], a[176:179], v[196:199], v[48:51]\n" \
"ds_read_b128 a[88:91], v209 offset:20480\n" \
"v_mfma_f32_16x16x32_bf16 v[48:51], a[144:147], v[188:191], v[48:51]\n" \
"v_exp_f32_e64 v136, v136\n" \
"v_mfma_f32_16x16x32_bf16 v[52:55], a[180:183], v[196:199], v[52:55]\n" \
"v_cvt_pk_bf16_f32 v194, v132, v133\n" \
"v_cvt_pk_bf16_f32 v195, v134, v135\n" \
"v_mfma_f32_16x16x32_bf16 v[52:55], a[148:151], v[188:191], v[52:55]\n" \
"v_exp_f32_e64 v137, v137\n" \
"v_mfma_f32_16x16x32_bf16 v[56:59], a[184:187], v[196:199], v[56:59]\n" \
"v_exp_f32_e64 v138, v138\n" \
"v_mfma_f32_16x16x32_bf16 v[56:59], a[152:155], v[188:191], v[56:59]\n" \
"ds_read_b128 a[84:87], v208 offset:20480\n" \
"v_mfma_f32_16x16x32_bf16 v[60:63], a[188:191], v[196:199], v[60:63]\n" \
"v_exp_f32_e64 v139, v139\n" \
"v_mfma_f32_16x16x32_bf16 v[60:63], a[156:159], v[188:191], v[60:63]\n" \
"s_cmp_lg_u32 %[wid], 3\n" \
"s_cbranch_scc1 LtA1_%=\n" \
"global_load_dword v237, v224, s[58:59]\n" \
"s_add_u32 s61, s48, 49152\n" \
"s_mov_b32 exec_hi, 0\n" \
"s_nop 0\n" \
"buffer_load_dword v237, v224, %[srdp], s61 offen\n" \
"s_mov_b32 exec_hi, -1\n" \
"s_nop 0\n" \
"LtA1_%=:\n" \
"v_mfma_f32_16x16x32_bf16 v[144:147], a[96:99], a[32:35], v[168:171]\n" \
"v_exp_f32_e64 v140, v140\n" \
"v_mfma_f32_16x16x32_bf16 v[144:147], a[100:103], a[36:39], v[144:147]\n" \
"ds_read_b128 a[80:83], %[kb0] offset:20480\n" \
"v_mfma_f32_16x16x32_bf16 v[144:147], a[104:107], a[40:43], v[144:147]\n" \
"v_exp_f32_e64 v141, v141\n" \
"v_mfma_f32_16x16x32_bf16 v[144:147], a[108:111], a[44:47], v[144:147]\n" \
"v_cvt_pk_bf16_f32 v196, v136, v137\n" \
"v_cvt_pk_bf16_f32 v197, v138, v139\n" \
"v_mfma_f32_16x16x32_bf16 v[148:151], a[124:127], a[44:47], v[168:171]\n" \
"v_exp_f32_e64 v142, v142\n" \
"v_mfma_f32_16x16x32_bf16 v[148:151], a[120:123], a[40:43], v[148:151]\n" \
"v_mfma_f32_16x16x32_bf16 v[148:151], a[116:119], a[36:39], v[148:151]\n" \
"buffer_load_dwordx4 %[vd0], %[srdv], s48 offen offset:2048 lds\n" \
"v_mfma_f32_16x16x32_bf16 v[148:151], a[112:115], a[32:35], v[148:151]\n" \
"v_exp_f32_e64 v143, v143\n" \
"v_mfma_f32_16x16x32_bf16 v[96:99], a[160:163], v[204:207], v[96:99]\n" \
"ds_read_b64_tr_b16 a[160:161], %[vvb] offset:8192\n" \
"v_mfma_f32_16x16x32_bf16 v[96:99], a[128:131], v[220:223], v[96:99]\n" \
"v_exp_f32_e64 v144, v144\n" \
"v_mfma_f32_16x16x32_bf16 v[100:103], a[164:167], v[204:207], v[100:103]\n" \
"v_cvt_pk_bf16_f32 v198, v140, v141\n" \
"v_cvt_pk_bf16_f32 v199, v142, v143\n" \
"v_mfma_f32_16x16x32_bf16 v[100:103], a[132:135], v[220:223], v[100:103]\n" \
"v_exp_f32_e64 v145, v145\n" \
"v_mfma_f32_16x16x32_bf16 v[104:107], a[168:171], v[204:207], v[104:107]\n" \
"ds_read_b64_tr_b16 a[162:163], %[vvb] offset:12288\n" \
"ds_read_b64_tr_b16 a[164:165], v211 offset:8192\n" \
"v_mfma_f32_16x16x32_bf16 v[104:107], a[136:139], v[220:223], v[104:107]\n" \
"v_exp_f32_e64 v146, v146\n" \
"v_mfma_f32_16x16x32_bf16 v[108:111], a[172:175], v[204:207], v[108:111]\n" \
"ds_read_b64_tr_b16 a[166:167], v211 offset:12288\n" \
"v_mfma_f32_16x16x32_bf16 v[108:111], a[140:143], v[220:223], v[108:111]\n" \
"v_exp_f32_e64 v147, v147\n" \
"v_smfmac_f32_16x16x64_bf16 v[180:183], a[192:195], v[216:223], v228\n" \
"ds_read_b64_tr_b16 a[168:169], %[vvb] offset:8704\n" \
"v_mfma_f32_16x16x32_bf16 v[152:155], a[96:99], a[48:51], v[172:175]\n" \
"v_exp_f32_e64 v148, v148\n" \
"v_mfma_f32_16x16x32_bf16 v[152:155], a[100:103], a[52:55], v[152:155]\n" \
"ds_read_b64_tr_b16 a[170:171], %[vvb] offset:12800\n" \
"v_mfma_f32_16x16x32_bf16 v[152:155], a[104:107], a[56:59], v[152:155]\n" \
"v_exp_f32_e64 v149, v149\n" \
"v_mfma_f32_16x16x32_bf16 v[152:155], a[108:111], a[60:63], v[152:155]\n" \
"v_cvt_pk_bf16_f32 v200, v144, v145\n" \
"v_cvt_pk_bf16_f32 v201, v146, v147\n" \
"v_mfma_f32_16x16x32_bf16 v[156:159], a[124:127], a[60:63], v[172:175]\n" \
"v_exp_f32_e64 v150, v150\n" \
"v_mfma_f32_16x16x32_bf16 v[156:159], a[120:123], a[56:59], v[156:159]\n" \
"ds_read_b64_tr_b16 a[172:173], v211 offset:8704\n" \
"v_mfma_f32_16x16x32_bf16 v[156:159], a[116:119], a[52:55], v[156:159]\n" \
"buffer_load_dwordx4 v215, %[srdv], s48 offen offset:3072 lds\n" \
"v_mfma_f32_16x16x32_bf16 v[156:159], a[112:115], a[48:51], v[156:159]\n" \
"v_exp_f32_e64 v151, v151\n" \
"v_mfma_f32_16x16x32_bf16 v[112:115], a[176:179], v[204:207], v[112:115]\n" \
"ds_read_b64_tr_b16 a[174:175], v211 offset:12800\n" \
"ds_read_b64_tr_b16 a[176:177], %[vvb] offset:9216\n" \
"v_mfma_f32_16x16x32_bf16 v[112:115], a[144:147], v[220:223], v[112:115]\n" \
"v_exp_f32_e64 v152, v152\n" \
"v_mfma_f32_16x16x32_bf16 v[116:119], a[180:183], v[204:207], v[116:119]\n" \
"v_cvt_pk_bf16_f32 v202, v148, v149\n" \
"v_cvt_pk_bf16_f32 v203, v150, v151\n" \
"v_mfma_f32_16x16x32_bf16 v[116:119], a[148:151], v[220:223], v[116:119]\n" \
"v_exp_f32_e64 v153, v153\n" \
"v_mfma_f32_16x16x32_bf16 v[120:123], a[184:187], v[204:207], v[120:123]\n" \
"v_exp_f32_e64 v154, v154\n" \
"v_mfma_f32_16x16x32_bf16 v[120:123], a[152:155], v[220:223], v[120:123]\n" \
"ds_read_b64_tr_b16 a[178:179], %[vvb] offset:13312\n" \
"v_mfma_f32_16x16x32_bf16 v[124:127], a[188:191], v[204:207], v[124:127]\n" \
"v_exp_f32_e64 v155, v155\n" \
"v_mfma_f32_16x16x32_bf16 v[124:127], a[156:159], v[220:223], v[124:127]\n" \
"ds_read_b64_tr_b16 a[180:181], v211 offset:9216\n" \
"s_waitcnt lgkmcnt(13)\n" \
"s_cmp_lg_u32 s60, 4\n" \
"s_cbranch_scc1 LmA_%=\n" \
"s_nop 0\n" \
"v_readlane_b32 s64, %[tlo], 9\n" \
"v_readlane_b32 s65, %[thi], 9\n" \
"s_mov_b32 s66, %[wbytes]\n" \
"v_mov_b32_e32 v229, %[vg0]\n" \
"s_mov_b32 s44, %[m0t]\n" \
"s_mov_b32 s45, 0\n" \
"s_cmp_lg_u32 s52, 0\n" \
"s_cbranch_scc1 LmB_%=\n" \
"v_readlane_b32 s61, %[thi], 21\n" \
"s_add_u32 s62, %[sin], 16384\n" \
"s_add_u32 s63, %[sin], 49152\n" \
"s_add_u32 s55, s61, 4096\n" \
"s_nop 1\n" \
"s_nop 0\n" \
"buffer_store_dwordx4 a[196:199], v235, %[srdz], s61 offen offset:0 sc1\n" \
"buffer_store_dwordx4 a[196:199], v235, %[srdz], s55 offen offset:0 sc1\n" \
"s_waitcnt vmcnt(14)\n" \
"s_nop 0\n" \
"ds_read_b64_tr_b16 a[232:233], %[ta0] offset:0\n" \
"ds_read_b64_tr_b16 a[234:235], %[ta0] offset:128\n" \
"ds_read_b64_tr_b16 a[236:237], %[ta0] offset:1024\n" \
"ds_read_b64_tr_b16 a[238:239], %[ta0] offset:1152\n" \
"ds_read_b64_tr_b16 a[240:241], %[ta0] offset:2048\n" \
"ds_read_b64_tr_b16 a[242:243], %[ta0] offset:2176\n" \
"ds_read_b64_tr_b16 a[244:245], %[ta0] offset:3072\n" \
"ds_read_b64_tr_b16 a[246:247], %[ta0] offset:3200\n" \
"ds_read_b64_tr_b16 a[248:249], %[ta0] offset:4096\n" \
"ds_read_b64_tr_b16 a[250:251], %[ta0] offset:4224\n" \
"ds_read_b64_tr_b16 a[252:253], %[ta0] offset:5120\n" \
"ds_read_b64_tr_b16 a[254:255], %[ta0] offset:5248\n" \
"v_add_u32_e32 v234, %[m0t], v235\n" \
"s_waitcnt lgkmcnt(8)\n" \
"v_mfma_f32_16x16x32_bf16 v[230:233], a[232:235], a[200:203], 0\n" \
"v_mfma_f32_16x16x32_bf16 v[230:233], a[236:239], a[204:207], v[230:233]\n" \
"ds_read_b64_tr_b16 a[232:233], %[ta0] offset:6144\n" \
"ds_read_b64_tr_b16 a[234:235], %[ta0] offset:6272\n" \
"ds_read_b64_tr_b16 a[236:237], %[ta0] offset:7168\n" \
"ds_read_b64_tr_b16 a[238:239], %[ta0] offset:7296\n" \
"s_waitcnt lgkmcnt(4)\n" \
"s_nop 0\n" \
"v_mfma_f32_16x16x32_bf16 v[230:233], a[240:243], a[208:211], v[230:233]\n" \
"v_mfma_f32_16x16x32_bf16 v[230:233], a[244:247], a[212:215], v[230:233]\n" \
"v_mfma_f32_16x16x32_bf16 v[230:233], a[248:251], a[216:219], v[230:233]\n" \
"v_mfma_f32_16x16x32_bf16 v[230:233], a[252:255], a[220:223], v[230:233]\n" \
"s_waitcnt lgkmcnt(0)\n" \
"s_nop 0\n" \
"v_mfma_f32_16x16x32_bf16 v[230:233], a[232:235], a[224:227], v[230:233]\n" \
"v_mfma_f32_16x16x32_bf16 v[230:233], a[236:239], a[228:231], v[230:233]\n" \
"s_nop 7\n" \
"s_nop 0\n" \
"ds_write_b128 v234, v[230:233] offset:7168\n" \
"LmB_%=:\n" \
"v_mfma_f32_16x16x32_bf16 v[128:131], a[64:67], a[0:3], v[160:163]\n" \
"v_exp_f32_e64 v156, v156\n" \
"v_mfma_f32_16x16x32_bf16 v[128:131], a[68:71], a[4:7], v[128:131]\n" \
"ds_read_b64_tr_b16 a[182:183], v211 offset:13312\n" \
"s_add_u32 m0, %[m0b], 0\n" \
"v_mfma_f32_16x16x32_bf16 v[128:131], a[72:75], a[8:11], v[128:131]\n" \
"v_exp_f32_e32 v157, v157\n" \
"v_mfma_f32_16x16x32_bf16 v[128:131], a[76:79], a[12:15], v[128:131]\n" \
"v_cvt_pk_bf16_f32 v204, v152, v153\n" \
"v_cvt_pk_bf16_f32 v205, v154, v155\n" \
"v_mfma_f32_16x16x32_bf16 v[132:135], a[92:95], a[12:15], v[160:163]\n" \
"v_exp_f32_e64 v158, v158\n" \
"v_mfma_f32_16x16x32_bf16 v[132:135], a[88:91], a[8:11], v[132:135]\n" \
"ds_read_b64_tr_b16 a[184:185], %[vvb] offset:9728\n" \
"s_waitcnt lgkmcnt(5)\n" \
"v_mfma_f32_16x16x32_bf16 v[132:135], a[84:87], a[4:7], v[132:135]\n" \
"buffer_load_dwordx4 %[kd0], %[srdk], s48 offen offset:0 lds\n" \
"v_mfma_f32_16x16x32_bf16 v[132:135], a[80:83], a[0:3], v[132:135]\n" \
"v_exp_f32_e32 v159, v159\n" \
"v_mfma_f32_16x16x32_bf16 v[64:67], a[128:131], v[216:219], v[64:67]\n" \
"ds_read_b64_tr_b16 a[186:187], %[vvb] offset:13824\n" \
"ds_read_b64_tr_b16 a[188:189], v211 offset:9728\n" \
"v_mfma_f32_16x16x32_bf16 v[64:67], a[160:163], v[200:203], v[64:67]\n" \
"v_exp_f32_e64 v128, v128\n" \
"v_mfma_f32_16x16x32_bf16 v[68:71], a[132:135], v[216:219], v[68:71]\n" \
"v_cvt_pk_bf16_f32 v206, v156, v157\n" \
"v_cvt_pk_bf16_f32 v207, v158, v159\n" \
"v_mfma_f32_16x16x32_bf16 v[68:71], a[164:167], v[200:203], v[68:71]\n" \
"v_exp_f32_e64 v129, v129\n" \
"v_mfma_f32_16x16x32_bf16 v[72:75], a[136:139], v[216:219], v[72:75]\n" \
"ds_read_b64_tr_b16 a[190:191], v211 offset:13824\n" \
"ds_read_b64_tr_b16 a[128:129], %[vvb] offset:16384\n" \
"s_add_u32 s60, s60, 1\n" \
"s_nop 0\n" \
"v_readlane_b32 s58, %[tlo], s60\n" \
"v_mfma_f32_16x16x32_bf16 v[72:75], a[168:171], v[200:203], v[72:75]\n" \
"v_exp_f32_e64 v130, v130\n" \
"v_mfma_f32_16x16x32_bf16 v[76:79], a[140:143], v[216:219], v[76:79]\n" \
"ds_read_b64_tr_b16 a[130:131], %[vvb] offset:20480\n" \
"ds_read_b64_tr_b16 a[132:133], v211 offset:16384\n" \
"v_readlane_b32 s59, %[thi], s60\n" \
"v_mfma_f32_16x16x32_bf16 v[76:79], a[172:175], v[200:203], v[76:79]\n" \
"v_exp_f32_e64 v131, v131\n" \
"v_smfmac_f32_16x16x64_bf16 v[176:179], a[192:195], v[192:199], v228\n" \
"ds_read_b64_tr_b16 a[134:135], v211 offset:20480\n" \
"ds_read_b64_tr_b16 a[136:137], %[vvb] offset:16896\n" \
"s_cmp_eq_u32 s60, 7\n" \
"s_cselect_b32 s61, 56, 1\n" \
"v_mul_u32_u24_e64 v236, s61, v224\n" \
"v_mfma_f32_16x16x32_bf16 v[136:139], a[64:67], a[16:19], v[164:167]\n" \
"v_exp_f32_e64 v132, v132\n" \
"v_mfma_f32_16x16x32_bf16 v[136:139], a[68:71], a[20:23], v[136:139]\n" \
"ds_read_b64_tr_b16 a[138:139], %[vvb] offset:20992\n" \
"ds_read_b64_tr_b16 a[140:141], v211 offset:16896\n" \
"v_mfma_f32_16x16x32_bf16 v[136:139], a[72:75], a[24:27], v[136:139]\n" \
"v_exp_f32_e64 v133, v133\n" \
"v_mfma_f32_16x16x32_bf16 v[136:139], a[76:79], a[28:31], v[136:139]\n" \
"v_cvt_pk_bf16_f32 v184, v128, v129\n" \
"v_cvt_pk_bf16_f32 v185, v130, v131\n" \
"v_mfma_f32_16x16x32_bf16 v[140:143], a[92:95], a[28:31], v[164:167]\n" \
"v_exp_f32_e64 v134, v134\n" \
"v_mfma_f32_16x16x32_bf16 v[140:143], a[88:91], a[24:27], v[140:143]\n" \
"ds_read_b64_tr_b16 a[142:143], v211 offset:20992\n" \
"s_waitcnt lgkmcnt(8)\n" \
"v_mfma_f32_16x16x32_bf16 v[140:143], a[84:87], a[20:23], v[140:143]\n" \
"buffer_load_dwordx4 v212, %[srdk], s48 offen offset:1024 lds\n" \
"v_mfma_f32_16x16x32_bf16 v[140:143], a[80:83], a[16:19], v[140:143]\n" \
"v_exp_f32_e32 v135, v135\n" \
"v_mfma_f32_16x16x32_bf16 v[80:83], a[144:147], v[216:219], v[80:83]\n" \
"ds_read_b64_tr_b16 a[144:145], %[vvb] offset:17408\n" \
"ds_read_b64_tr_b16 a[146:147], %[vvb] offset:21504\n" \
"v_mfma_f32_16x16x32_bf16 v[80:83], a[176:179], v[200:203], v[80:83]\n" \
"v_exp_f32_e64 v136, v136\n" \
"v_mfma_f32_16x16x32_bf16 v[84:87], a[148:151], v[216:219], v[84:87]\n" \
"v_cvt_pk_bf16_f32 v186, v132, v133\n" \
"v_cvt_pk_bf16_f32 v187, v134, v135\n" \
"v_mfma_f32_16x16x32_bf16 v[84:87], a[180:183], v[200:203], v[84:87]\n" \
"v_exp_f32_e64 v137, v137\n" \
"v_mfma_f32_16x16x32_bf16 v[88:91], a[152:155], v[216:219], v[88:91]\n" \
"v_exp_f32_e64 v138, v138\n" \
"v_mfma_f32_16x16x32_bf16 v[88:91], a[184:187], v[200:203], v[88:91]\n" \
"ds_read_b64_tr_b16 a[148:149], v211 offset:17408\n" \
"ds_read_b64_tr_b16 a[150:151], v211 offset:21504\n" \
"v_mfma_f32_16x16x32_bf16 v[92:95], a[156:159], v[216:219], v[92:95]\n" \
"v_exp_f32_e64 v139, v139\n" \
"v_mfma_f32_16x16x32_bf16 v[92:95], a[188:191], v[200:203], v[92:95]\n" \
"ds_read_b64_tr_b16 a[152:153], %[vvb] offset:17920\n" \
"s_cbranch_vccz Lpp3_%=\n" \
"buffer_store_dwordx4 a[196:199], v235, %[srdz], s63 offen offset:0 sc1\n" \
"buffer_store_dwordx4 a[196:199], v226, %[srdz], s63 offen offset:0 sc1\n" \
"Lpp3_%=:\n" \
"v_mfma_f32_16x16x32_bf16 v[144:147], a[64:67], a[32:35], v[168:171]\n" \
"v_exp_f32_e32 v140, v140\n" \
"v_mfma_f32_16x16x32_bf16 v[144:147], a[68:71], a[36:39], v[144:147]\n" \
"ds_read_b64_tr_b16 a[154:155], %[vvb] offset:22016\n" \
"ds_read_b64_tr_b16 a[156:157], v211 offset:17920\n" \
"v_mfma_f32_16x16x32_bf16 v[144:147], a[72:75], a[40:43], v[144:147]\n" \
"v_exp_f32_e64 v141, v141\n" \
"v_mfma_f32_16x16x32_bf16 v[144:147], a[76:79], a[44:47], v[144:147]\n" \
"v_cvt_pk_bf16_f32 v188, v136, v137\n" \
"v_cvt_pk_bf16_f32 v189, v138, v139\n" \
"v_mfma_f32_16x16x32_bf16 v[148:151], a[92:95], a[44:47], v[168:171]\n" \
"v_exp_f32_e64 v142, v142\n" \
"v_mfma_f32_16x16x32_bf16 v[148:151], a[88:91], a[40:43], v[148:151]\n" \
"ds_read_b64_tr_b16 a[158:159], v211 offset:22016\n" \
"s_waitcnt lgkmcnt(8)\n" \
"v_mfma_f32_16x16x32_bf16 v[148:151], a[84:87], a[36:39], v[148:151]\n" \
"buffer_load_dwordx4 v213, %[srdk], s48 offen offset:2048 lds\n" \
"v_mfma_f32_16x16x32_bf16 v[148:151], a[80:83], a[32:35], v[148:151]\n" \
"v_exp_f32_e32 v143, v143\n" \
"v_mfma_f32_16x16x32_bf16 v[0:3], a[160:163], v[192:195], v[0:3]\n" \
"ds_read_b128 a[96:99], %[kb0] offset:24576\n" \
"v_mfma_f32_16x16x32_bf16 v[0:3], a[128:131], v[184:187], v[0:3]\n" \
"v_exp_f32_e64 v144, v144\n" \
"v_mfma_f32_16x16x32_bf16 v[4:7], a[164:167], v[192:195], v[4:7]\n" \
"v_cvt_pk_bf16_f32 v190, v140, v141\n" \
"v_cvt_pk_bf16_f32 v191, v142, v143\n" \
"v_mfma_f32_16x16x32_bf16 v[4:7], a[132:135], v[184:187], v[4:7]\n" \
"v_exp_f32_e64 v145, v145\n" \
"v_mfma_f32_16x16x32_bf16 v[8:11], a[168:171], v[192:195], v[8:11]\n" \
"ds_read_b128 a[100:103], v208 offset:24576\n" \
"v_mfma_f32_16x16x32_bf16 v[8:11], a[136:139], v[184:187], v[8:11]\n" \
"v_exp_f32_e64 v146, v146\n" \
"v_mfma_f32_16x16x32_bf16 v[12:15], a[172:175], v[192:195], v[12:15]\n" \
"ds_read_b128 a[104:107], v209 offset:24576\n" \
"v_mfma_f32_16x16x32_bf16 v[12:15], a[140:143], v[184:187], v[12:15]\n" \
"v_exp_f32_e64 v147, v147\n" \
"v_smfmac_f32_16x16x64_bf16 v[180:183], a[192:195], v[200:207], v228\n" \
"ds_read_b128 a[108:111], v210 offset:24576\n" \
"v_mfma_f32_16x16x32_bf16 v[152:155], a[64:67], a[48:51], v[172:175]\n" \
"v_exp_f32_e64 v148, v148\n" \
"v_mfma_f32_16x16x32_bf16 v[152:155], a[68:71], a[52:55], v[152:155]\n" \
"ds_read_b128 a[124:127], v210 offset:28672\n" \
"v_mfma_f32_16x16x32_bf16 v[152:155], a[72:75], a[56:59], v[152:155]\n" \
"v_exp_f32_e64 v149, v149\n" \
"v_mfma_f32_16x16x32_bf16 v[152:155], a[76:79], a[60:63], v[152:155]\n" \
"v_cvt_pk_bf16_f32 v216, v144, v145\n" \
"v_cvt_pk_bf16_f32 v217, v146, v147\n" \
"v_mfma_f32_16x16x32_bf16 v[156:159], a[92:95], a[60:63], v[172:175]\n" \
"v_exp_f32_e64 v150, v150\n" \
"v_mfma_f32_16x16x32_bf16 v[156:159], a[88:91], a[56:59], v[156:159]\n" \
"ds_read_b128 a[120:123], v209 offset:28672\n" \
"s_waitcnt lgkmcnt(6)\n" \
"v_mfma_f32_16x16x32_bf16 v[156:159], a[84:87], a[52:55], v[156:159]\n" \
"buffer_load_dwordx4 v214, %[srdk], s48 offen offset:3072 lds\n" \
"v_mfma_f32_16x16x32_bf16 v[156:159], a[80:83], a[48:51], v[156:159]\n" \
"v_exp_f32_e32 v151, v151\n" \
"v_mfma_f32_16x16x32_bf16 v[16:19], a[176:179], v[192:195], v[16:19]\n" \
"ds_read_b128 a[116:119], v208 offset:28672\n" \
"v_mfma_f32_16x16x32_bf16 v[16:19], a[144:147], v[184:187], v[16:19]\n" \
"v_exp_f32_e64 v152, v152\n" \
"v_mfma_f32_16x16x32_bf16 v[20:23], a[180:183], v[192:195], v[20:23]\n" \
"v_cvt_pk_bf16_f32 v218, v148, v149\n" \
"v_cvt_pk_bf16_f32 v219, v150, v151\n" \
"v_mfma_f32_16x16x32_bf16 v[20:23], a[148:151], v[184:187], v[20:23]\n" \
"v_exp_f32_e64 v153, v153\n" \
"v_mfma_f32_16x16x32_bf16 v[24:27], a[184:187], v[192:195], v[24:27]\n" \
"v_exp_f32_e64 v154, v154\n" \
"v_mfma_f32_16x16x32_bf16 v[24:27], a[152:155], v[184:187], v[24:27]\n" \
"ds_read_b128 a[112:115], %[kb0] offset:28672\n" \
"s_add_u32 s48, s48, 0x4000\n" \
"s_mov_b32 m0, s44\n" \
"s_addk_i32 s44, 0x400\n" \
"buffer_load_dwordx4 v229, s[64:67], s45 offen offset:0 lds\n" \
"s_add_u32 s45, s45, 229376\n" \
"v_mfma_f32_16x16x32_bf16 v[28:31], a[188:191], v[192:195], v[28:31]\n" \
"v_exp_f32_e64 v155, v155\n" \
"v_mfma_f32_16x16x32_bf16 v[28:31], a[156:159], v[184:187], v[28:31]\n" \
"s_waitcnt vmcnt(13) lgkmcnt(2)\n" \
"s_barrier\n" \
"v_mfma_f32_16x16x32_bf16 v[128:131], a[96:99], a[0:3], v[160:163]\n" \
"v_exp_f32_e64 v156, v156\n" \
"v_mfma_f32_16x16x32_bf16 v[128:131], a[100:103], a[4:7], v[128:131]\n" \
"s_add_u32 m0, %[m0b], 65536\n" \
"v_mfma_f32_16x16x32_bf16 v[128:131], a[104:107], a[8:11], v[128:131]\n" \
"v_exp_f32_e64 v157, v157\n" \
"v_mfma_f32_16x16x32_bf16 v[128:131], a[108:111], a[12:15], v[128:131]\n" \
"v_cvt_pk_bf16_f32 v220, v152, v153\n" \
"v_cvt_pk_bf16_f32 v221, v154, v155\n" \
"v_mfma_f32_16x16x32_bf16 v[132:135], a[124:127], a[12:15], v[160:163]\n" \
"v_exp_f32_e64 v158, v158\n" \
"v_mfma_f32_16x16x32_bf16 v[132:135], a[120:123], a[8:11], v[132:135]\n" \
"s_waitcnt lgkmcnt(0)\n" \
"v_mfma_f32_16x16x32_bf16 v[132:135], a[116:119], a[4:7], v[132:135]\n" \
"buffer_load_dwordx4 %[vd0], %[srdv], s48 offen offset:0 lds\n" \
"v_mfma_f32_16x16x32_bf16 v[132:135], a[112:115], a[0:3], v[132:135]\n" \
"v_exp_f32_e32 v159, v159\n" \
"v_mfma_f32_16x16x32_bf16 v[32:35], a[160:163], v[196:199], v[32:35]\n" \
"ds_read_b128 a[64:67], %[kb0] offset:32768\n" \
"v_mfma_f32_16x16x32_bf16 v[32:35], a[128:131], v[188:191], v[32:35]\n" \
"v_exp_f32_e64 v128, v128\n" \
"v_mfma_f32_16x16x32_bf16 v[36:39], a[164:167], v[196:199], v[36:39]\n" \
"v_cvt_pk_bf16_f32 v222, v156, v157\n" \
"v_cvt_pk_bf16_f32 v223, v158, v159\n" \
"v_mfma_f32_16x16x32_bf16 v[36:39], a[132:135], v[188:191], v[36:39]\n" \
"v_exp_f32_e64 v129, v129\n" \
"v_mfma_f32_16x16x32_bf16 v[40:43], a[168:171], v[196:199], v[40:43]\n" \
"ds_read_b128 a[68:71], v208 offset:32768\n" \
"v_mfma_f32_16x16x32_bf16 v[40:43], a[136:139], v[188:191], v[40:43]\n" \
"v_exp_f32_e64 v130, v130\n" \
"v_mfma_f32_16x16x32_bf16 v[44:47], a[172:175], v[196:199], v[44:47]\n" \
"ds_read_b128 a[72:75], v209 offset:32768\n" \
"v_mfma_f32_16x16x32_bf16 v[44:47], a[140:143], v[188:191], v[44:47]\n" \
"v_exp_f32_e64 v131, v131\n" \
"v_smfmac_f32_16x16x64_bf16 v[176:179], a[192:195], v[184:191], v228\n" \
"v_mfma_f32_16x16x32_bf16 v[136:139], a[96:99], a[16:19], v[164:167]\n" \
"v_exp_f32_e64 v132, v132\n" \
"v_mfma_f32_16x16x32_bf16 v[136:139], a[100:103], a[20:23], v[136:139]\n" \
"ds_read_b128 a[76:79], v210 offset:32768\n" \
"v_mfma_f32_16x16x32_bf16 v[136:139], a[104:107], a[24:27], v[136:139]\n" \
"v_exp_f32_e64 v133, v133\n" \
"v_mfma_f32_16x16x32_bf16 v[136:139], a[108:111], a[28:31], v[136:139]\n" \
"v_cvt_pk_bf16_f32 v192, v128, v129\n" \
"v_cvt_pk_bf16_f32 v193, v130, v131\n" \
"v_mfma_f32_16x16x32_bf16 v[140:143], a[124:127], a[28:31], v[164:167]\n" \
"v_exp_f32_e64 v134, v134\n" \
"v_mfma_f32_16x16x32_bf16 v[140:143], a[120:123], a[24:27], v[140:143]\n" \
"ds_read_b128 a[92:95], v210 offset:36864\n" \
"v_mfma_f32_16x16x32_bf16 v[140:143], a[116:119], a[20:23], v[140:143]\n" \
"buffer_load_dwordx4 v215, %[srdv], s48 offen offset:1024 lds\n" \
"v_mfma_f32_16x16x32_bf16 v[140:143], a[112:115], a[16:19], v[140:143]\n" \
"v_exp_f32_e64 v135, v135\n" \
"v_mfma_f32_16x16x32_bf16 v[48:51], a[176:179], v[196:199], v[48:51]\n" \
"ds_read_b128 a[88:91], v209 offset:36864\n" \
"v_mfma_f32_16x16x32_bf16 v[48:51], a[144:147], v[188:191], v[48:51]\n" \
"v_exp_f32_e64 v136, v136\n" \
"v_mfma_f32_16x16x32_bf16 v[52:55], a[180:183], v[196:199], v[52:55]\n" \
"v_cvt_pk_bf16_f32 v194, v132, v133\n" \
"v_cvt_pk_bf16_f32 v195, v134, v135\n" \
"v_mfma_f32_16x16x32_bf16 v[52:55], a[148:151], v[188:191], v[52:55]\n" \
"v_exp_f32_e64 v137, v137\n" \
"v_mfma_f32_16x16x32_bf16 v[56:59], a[184:187], v[196:199], v[56:59]\n" \
"v_exp_f32_e64 v138, v138\n" \
"v_mfma_f32_16x16x32_bf16 v[56:59], a[152:155], v[188:191], v[56:59]\n" \
"ds_read_b128 a[84:87], v208 offset:36864\n" \
"v_mfma_f32_16x16x32_bf16 v[60:63], a[188:191], v[196:199], v[60:63]\n" \
"v_exp_f32_e64 v139, v139\n" \
"v_mfma_f32_16x16x32_bf16 v[60:63], a[156:159], v[188:191], v[60:63]\n" \
"s_cmp_lg_u32 %[wid], 0\n" \
"s_cbranch_scc1 LtB3_%=\n" \
"global_load_dword v237, v236, s[58:59]\n" \
"s_add_u32 s61, s48, 49152\n" \
"s_mov_b32 exec_hi, 0\n" \
"s_nop 0\n" \
"buffer_load_dword v237, v224, %[srdp], s61 offen\n" \
"s_mov_b32 exec_hi, -1\n" \
"s_nop 0\n" \
"LtB3_%=:\n" \
"v_mfma_f32_16x16x32_bf16 v[144:147], a[96:99], a[32:35], v[168:171]\n" \
"v_exp_f32_e64 v140, v140\n" \
"v_mfma_f32_16x16x32_bf16 v[144:147], a[100:103], a[36:39], v[144:147]\n" \
"ds_read_b128 a[80:83], %[kb0] offset:36864\n" \
"v_mfma_f32_16x16x32_bf16 v[144:147], a[104:107], a[40:43], v[144:147]\n" \
"v_exp_f32_e64 v141, v141\n" \
"v_mfma_f32_16x16x32_bf16 v[144:147], a[108:111], a[44:47], v[144:147]\n" \
"v_cvt_pk_bf16_f32 v196, v136, v137\n" \
"v_cvt_pk_bf16_f32 v197, v138, v139\n" \
"v_mfma_f32_16x16x32_bf16 v[148:151], a[124:127], a[44:47], v[168:171]\n" \
"v_exp_f32_e64 v142, v142\n" \
"v_mfma_f32_16x16x32_bf16 v[148:151], a[120:123], a[40:43], v[148:151]\n" \
"v_mfma_f32_16x16x32_bf16 v[148:151], a[116:119], a[36:39], v[148:151]\n" \
"buffer_load_dwordx4 %[vd0], %[srdv], s48 offen offset:2048 lds\n" \
"v_mfma_f32_16x16x32_bf16 v[148:151], a[112:115], a[32:35], v[148:151]\n" \
"v_exp_f32_e64 v143, v143\n" \
"v_mfma_f32_16x16x32_bf16 v[96:99], a[160:163], v[204:207], v[96:99]\n" \
"ds_read_b64_tr_b16 a[160:161], %[vvb] offset:24576\n" \
"v_mfma_f32_16x16x32_bf16 v[96:99], a[128:131], v[220:223], v[96:99]\n" \
"v_exp_f32_e64 v144, v144\n" \
"v_mfma_f32_16x16x32_bf16 v[100:103], a[164:167], v[204:207], v[100:103]\n" \
"v_cvt_pk_bf16_f32 v198, v140, v141\n" \
"v_cvt_pk_bf16_f32 v199, v142, v143\n" \
"v_mfma_f32_16x16x32_bf16 v[100:103], a[132:135], v[220:223], v[100:103]\n" \
"v_exp_f32_e64 v145, v145\n" \
"v_mfma_f32_16x16x32_bf16 v[104:107], a[168:171], v[204:207], v[104:107]\n" \
"ds_read_b64_tr_b16 a[162:163], %[vvb] offset:28672\n" \
"ds_read_b64_tr_b16 a[164:165], v211 offset:24576\n" \
"v_mfma_f32_16x16x32_bf16 v[104:107], a[136:139], v[220:223], v[104:107]\n" \
"v_exp_f32_e64 v146, v146\n" \
"v_mfma_f32_16x16x32_bf16 v[108:111], a[172:175], v[204:207], v[108:111]\n" \
"ds_read_b64_tr_b16 a[166:167], v211 offset:28672\n" \
"v_mfma_f32_16x16x32_bf16 v[108:111], a[140:143], v[220:223], v[108:111]\n" \
"v_exp_f32_e64 v147, v147\n" \
"v_smfmac_f32_16x16x64_bf16 v[180:183], a[192:195], v[216:223], v228\n" \
"ds_read_b64_tr_b16 a[168:169], %[vvb] offset:25088\n" \
"s_cbranch_vccz Lpp4_%=\n" \
"buffer_store_dwordx4 a[196:199], v235, %[srdz], s62 offen offset:0 sc1\n" \
"buffer_store_dwordx4 a[196:199], v226, %[srdz], s62 offen offset:0 sc1\n" \
"Lpp4_%=:\n" \
"v_mfma_f32_16x16x32_bf16 v[152:155], a[96:99], a[48:51], v[172:175]\n" \
"v_exp_f32_e32 v148, v148\n" \
"v_mfma_f32_16x16x32_bf16 v[152:155], a[100:103], a[52:55], v[152:155]\n" \
"ds_read_b64_tr_b16 a[170:171], %[vvb] offset:29184\n" \
"v_mfma_f32_16x16x32_bf16 v[152:155], a[104:107], a[56:59], v[152:155]\n" \
"v_exp_f32_e64 v149, v149\n" \
"v_mfma_f32_16x16x32_bf16 v[152:155], a[108:111], a[60:63], v[152:155]\n" \
"v_cvt_pk_bf16_f32 v200, v144, v145\n" \
"v_cvt_pk_bf16_f32 v201, v146, v147\n" \
"v_mfma_f32_16x16x32_bf16 v[156:159], a[124:127], a[60:63], v[172:175]\n" \
"v_exp_f32_e64 v150, v150\n" \
"v_mfma_f32_16x16x32_bf16 v[156:159], a[120:123], a[56:59], v[156:159]\n" \
"ds_read_b64_tr_b16 a[172:173], v211 offset:25088\n" \
"v_mfma_f32_16x16x32_bf16 v[156:159], a[116:119], a[52:55], v[156:159]\n" \
"buffer_load_dwordx4 v215, %[srdv], s48 offen offset:3072 lds\n" \
"v_mfma_f32_16x16x32_bf16 v[156:159], a[112:115], a[48:51], v[156:159]\n" \
"v_exp_f32_e64 v151, v151\n" \
"v_mfma_f32_16x16x32_bf16 v[112:115], a[176:179], v[204:207], v[112:115]\n" \
"ds_read_b64_tr_b16 a[174:175], v211 offset:29184\n" \
"ds_read_b64_tr_b16 a[176:177], %[vvb] offset:25600\n" \
"v_mfma_f32_16x16x32_bf16 v[112:115], a[144:147], v[220:223], v[112:115]\n" \
"v_exp_f32_e64 v152, v152\n" \
"v_mfma_f32_16x16x32_bf16 v[116:119], a[180:183], v[204:207], v[116:119]\n" \
"v_cvt_pk_bf16_f32 v202, v148, v149\n" \
"v_cvt_pk_bf16_f32 v203, v150, v151\n" \
"v_mfma_f32_16x16x32_bf16 v[116:119], a[148:151], v[220:223], v[116:119]\n" \
"v_exp_f32_e64 v153, v153\n" \
"v_mfma_f32_16x16x32_bf16 v[120:123], a[184:187], v[204:207], v[120:123]\n" \
"v_exp_f32_e64 v154, v154\n" \
"v_mfma_f32_16x16x32_bf16 v[120:123], a[152:155], v[220:223], v[120:123]\n" \
"ds_read_b64_tr_b16 a[178:179], %[vvb] offset:29696\n" \
"v_mfma_f32_16x16x32_bf16 v[124:127], a[188:191], v[204:207], v[124:127]\n" \
"v_exp_f32_e64 v155, v155\n" \
"v_mfma_f32_16x16x32_bf16 v[124:127], a[156:159], v[220:223], v[124:127]\n" \
"ds_read_b64_tr_b16 a[180:181], v211 offset:25600\n" \
"s_waitcnt lgkmcnt(13)\n" \
"v_mfma_f32_16x16x32_bf16 v[128:131], a[64:67], a[0:3], v[160:163]\n" \
"v_exp_f32_e32 v156, v156\n" \
"v_mfma_f32_16x16x32_bf16 v[128:131], a[68:71], a[4:7], v[128:131]\n" \
"ds_read_b64_tr_b16 a[182:183], v211 offset:29696\n" \
"s_add_u32 m0, %[m0b], 16384\n" \
"v_mfma_f32_16x16x32_bf16 v[128:131], a[72:75], a[8:11], v[128:131]\n" \
"v_exp_f32_e64 v157, v157\n" \
"v_mfma_f32_16x16x32_bf16 v[128:131], a[76:79], a[12:15], v[128:131]\n" \
"v_cvt_pk_bf16_f32 v204, v152, v153\n" \
"v_cvt_pk_bf16_f32 v205, v154, v155\n" \
"v_mfma_f32_16x16x32_bf16 v[132:135], a[92:95], a[12:15], v[160:163]\n" \
"v_exp_f32_e64 v158, v158\n" \
"v_mfma_f32_16x16x32_bf16 v[132:135], a[88:91], a[8:11], v[132:135]\n" \
"ds_read_b64_tr_b16 a[184:185], %[vvb] offset:26112\n" \
"s_waitcnt lgkmcnt(5)\n" \
"v_mfma_f32_16x16x32_bf16 v[132:135], a[84:87], a[4:7], v[132:135]\n" \
"buffer_load_dwordx4 %[kd0], %[srdk], s48 offen offset:0 lds\n" \
"v_mfma_f32_16x16x32_bf16 v[132:135], a[80:83], a[0:3], v[132:135]\n" \
"v_exp_f32_e32 v159, v159\n" \
"v_mfma_f32_16x16x32_bf16 v[64:67], a[128:131], v[216:219], v[64:67]\n" \
"ds_read_b64_tr_b16 a[186:187], %[vvb] offset:30208\n" \
"ds_read_b64_tr_b16 a[188:189], v211 offset:26112\n" \
"v_mfma_f32_16x16x32_bf16 v[64:67], a[160:163], v[200:203], v[64:67]\n" \
"v_exp_f32_e64 v128, v128\n" \
"v_mfma_f32_16x16x32_bf16 v[68:71], a[132:135], v[216:219], v[68:71]\n" \
"v_cvt_pk_bf16_f32 v206, v156, v157\n" \
"v_cvt_pk_bf16_f32 v207, v158, v159\n" \
"v_mfma_f32_16x16x32_bf16 v[68:71], a[164:167], v[200:203], v[68:71]\n" \
"v_exp_f32_e64 v129, v129\n" \
"v_mfma_f32_16x16x32_bf16 v[72:75], a[136:139], v[216:219], v[72:75]\n" \
"ds_read_b64_tr_b16 a[190:191], v211 offset:30208\n" \
"ds_read_b64_tr_b16 a[128:129], %[vvb] offset:32768\n" \
"v_mfma_f32_16x16x32_bf16 v[72:75], a[168:171], v[200:203], v[72:75]\n" \
"v_exp_f32_e64 v130, v130\n" \
"v_mfma_f32_16x16x32_bf16 v[76:79], a[140:143], v[216:219], v[76:79]\n" \
".if . - adaln_body != 24576\n" \
".error \"size model\"\n" \
".endif\n" \
"Lk5_%=:\n" \
"s_cbranch_execz adaln_kr\n" \
"s_nop 0\n" \
"ds_read_b64_tr_b16 a[130:131], %[vvb] offset:36864\n" \
"ds_read_b64_tr_b16 a[132:133], v211 offset:32768\n" \
"v_mfma_f32_16x16x32_bf16 v[76:79], a[172:175], v[200:203], v[76:79]\n" \
"v_exp_f32_e64 v131, v131\n" \
"v_smfmac_f32_16x16x64_bf16 v[176:179], a[192:195], v[192:199], v228\n" \
"ds_read_b64_tr_b16 a[134:135], v211 offset:36864\n" \
"ds_read_b64_tr_b16 a[136:137], %[vvb] offset:33280\n" \
"v_mfma_f32_16x16x32_bf16 v[136:139], a[64:67], a[16:19], v[164:167]\n" \
"v_exp_f32_e64 v132, v132\n" \
"v_mfma_f32_16x16x32_bf16 v[136:139], a[68:71], a[20:23], v[136:139]\n" \
"ds_read_b64_tr_b16 a[138:139], %[vvb] offset:37376\n" \
"ds_read_b64_tr_b16 a[140:141], v211 offset:33280\n" \
"v_mfma_f32_16x16x32_bf16 v[136:139], a[72:75], a[24:27], v[136:139]\n" \
"v_exp_f32_e64 v133, v133\n" \
"v_mfma_f32_16x16x32_bf16 v[136:139], a[76:79], a[28:31], v[136:139]\n" \
"v_cvt_pk_bf16_f32 v184, v128, v129\n" \
"v_cvt_pk_bf16_f32 v185, v130, v131\n" \
"v_mfma_f32_16x16x32_bf16 v[140:143], a[92:95], a[28:31], v[164:167]\n" \
"v_exp_f32_e64 v134, v134\n" \
"v_mfma_f32_16x16x32_bf16 v[140:143], a[88:91], a[24:27], v[140:143]\n" \
"ds_read_b64_tr_b16 a[142:143], v211 offset:37376\n" \
"s_waitcnt lgkmcnt(8)\n" \
"v_mfma_f32_16x16x32_bf16 v[140:143], a[84:87], a[20:23], v[140:143]\n" \
"buffer_load_dwordx4 v212, %[srdk], s48 offen offset:1024 lds\n" \
"v_mfma_f32_16x16x32_bf16 v[140:143], a[80:83], a[16:19], v[140:143]\n" \
"v_exp_f32_e32 v135, v135\n" \
"v_mfma_f32_16x16x32_bf16 v[80:83], a[144:147], v[216:219], v[80:83]\n" \
"ds_read_b64_tr_b16 a[144:145], %[vvb] offset:33792\n" \
"ds_read_b64_tr_b16 a[146:147], %[vvb] offset:37888\n" \
"v_mfma_f32_16x16x32_bf16 v[80:83], a[176:179], v[200:203], v[80:83]\n" \
"v_exp_f32_e64 v136, v136\n" \
"v_mfma_f32_16x16x32_bf16 v[84:87], a[148:151], v[216:219], v[84:87]\n" \
"v_cvt_pk_bf16_f32 v186, v132, v133\n" \
"v_cvt_pk_bf16_f32 v187, v134, v135\n" \
"v_mfma_f32_16x16x32_bf16 v[84:87], a[180:183], v[200:203], v[84:87]\n" \
"v_exp_f32_e64 v137, v137\n" \
"v_mfma_f32_16x16x32_bf16 v[88:91], a[152:155], v[216:219], v[88:91]\n" \
"v_exp_f32_e64 v138, v138\n" \
"v_mfma_f32_16x16x32_bf16 v[88:91], a[184:187], v[200:203], v[88:91]\n" \
"ds_read_b64_tr_b16 a[148:149], v211 offset:33792\n" \
"ds_read_b64_tr_b16 a[150:151], v211 offset:37888\n" \
"v_mfma_f32_16x16x32_bf16 v[92:95], a[156:159], v[216:219], v[92:95]\n" \
"v_exp_f32_e64 v139, v139\n" \
"v_mfma_f32_16x16x32_bf16 v[92:95], a[188:191], v[200:203], v[92:95]\n" \
"ds_read_b64_tr_b16 a[152:153], %[vvb] offset:34304\n" \
"s_cbranch_vccz Lpp5_%=\n" \
"buffer_store_dwordx4 a[196:199], v235, %[srdz], s63 offen offset:1024 sc1\n" \
"buffer_store_dwordx4 a[196:199], v226, %[srdz], s63 offen offset:1024 sc1\n" \
"Lpp5_%=:\n" \
"v_mfma_f32_16x16x32_bf16 v[144:147], a[64:67], a[32:35], v[168:171]\n" \
"v_exp_f32_e32 v140, v140\n" \
"v_mfma_f32_16x16x32_bf16 v[144:147], a[68:71], a[36:39], v[144:147]\n" \
"ds_read_b64_tr_b16 a[154:155], %[vvb] offset:38400\n" \
"ds_read_b64_tr_b16 a[156:157], v211 offset:34304\n" \
"v_mfma_f32_16x16x32_bf16 v[144:147], a[72:75], a[40:43], v[144:147]\n" \
"v_exp_f32_e64 v141, v141\n" \
"v_mfma_f32_16x16x32_bf16 v[144:147], a[76:79], a[44:47], v[144:147]\n" \
"v_cvt_pk_bf16_f32 v188, v136, v137\n" \
"v_cvt_pk_bf16_f32 v189, v138, v139\n" \
"v_mfma_f32_16x16x32_bf16 v[148:151], a[92:95], a[44:47], v[168:171]\n" \
"v_exp_f32_e64 v142, v142\n" \
"v_mfma_f32_16x16x32_bf16 v[148:151], a[88:91], a[40:43], v[148:151]\n" \
"ds_read_b64_tr_b16 a[158:159], v211 offset:38400\n" \
"s_waitcnt lgkmcnt(8)\n" \
"v_mfma_f32_16x16x32_bf16 v[148:151], a[84:87], a[36:39], v[148:151]\n" \
"buffer_load_dwordx4 v213, %[srdk], s48 offen offset:2048 lds\n" \
"v_mfma_f32_16x16x32_bf16 v[148:151], a[80:83], a[32:35], v[148:151]\n" \
"v_exp_f32_e32 v143, v143\n" \
"v_mfma_f32_16x16x32_bf16 v[0:3], a[160:163], v[192:195], v[0:3]\n" \
"ds_read_b128 a[96:99], %[kb0] offset:40960\n" \
"v_mfma_f32_16x16x32_bf16 v[0:3], a[128:131], v[184:187], v[0:3]\n" \
"v_exp_f32_e64 v144, v144\n" \
"v_mfma_f32_16x16x32_bf16 v[4:7], a[164:167], v[192:195], v[4:7]\n" \
"v_cvt_pk_bf16_f32 v190, v140, v141\n" \
"v_cvt_pk_bf16_f32 v191, v142, v143\n" \
"v_mfma_f32_16x16x32_bf16 v[4:7], a[132:135], v[184:187], v[4:7]\n" \
"v_exp_f32_e64 v145, v145\n" \
"v_mfma_f32_16x16x32_bf16 v[8:11], a[168:171], v[192:195], v[8:11]\n" \
"ds_read_b128 a[100:103], v208 offset:40960\n" \
"v_mfma_f32_16x16x32_bf16 v[8:11], a[136:139], v[184:187], v[8:11]\n" \
"v_exp_f32_e64 v146, v146\n" \
"v_mfma_f32_16x16x32_bf16 v[12:15], a[172:175], v[192:195], v[12:15]\n" \
"ds_read_b128 a[104:107], v209 offset:40960\n" \
"v_mfma_f32_16x16x32_bf16 v[12:15], a[140:143], v[184:187], v[12:15]\n" \
"v_exp_f32_e64 v147, v147\n" \
"v_smfmac_f32_16x16x64_bf16 v[180:183], a[192:195], v[200:207], v228\n" \
"ds_read_b128 a[108:111], v210 offset:40960\n" \
"v_mfma_f32_16x16x32_bf16 v[152:155], a[64:67], a[48:51], v[172:175]\n" \
"v_exp_f32_e64 v148, v148\n" \
"v_mfma_f32_16x16x32_bf16 v[152:155], a[68:71], a[52:55], v[152:155]\n" \
"ds_read_b128 a[124:127], v210 offset:45056\n" \
"v_mfma_f32_16x16x32_bf16 v[152:155], a[72:75], a[56:59], v[152:155]\n" \
"v_exp_f32_e64 v149, v149\n" \
"v_mfma_f32_16x16x32_bf16 v[152:155], a[76:79], a[60:63], v[152:155]\n" \
"v_cvt_pk_bf16_f32 v216, v144, v145\n" \
"v_cvt_pk_bf16_f32 v217, v146, v147\n" \
"v_mfma_f32_16x16x32_bf16 v[156:159], a[92:95], a[60:63], v[172:175]\n" \
"v_exp_f32_e64 v150, v150\n" \
"v_mfma_f32_16x16x32_bf16 v[156:159], a[88:91], a[56:59], v[156:159]\n" \
"ds_read_b128 a[120:123], v209 offset:45056\n" \
"s_waitcnt lgkmcnt(6)\n" \
"v_mfma_f32_16x16x32_bf16 v[156:159], a[84:87], a[52:55], v[156:159]\n" \
"buffer_load_dwordx4 v214, %[srdk], s48 offen offset:3072 lds\n" \
"v_mfma_f32_16x16x32_bf16 v[156:159], a[80:83], a[48:51], v[156:159]\n" \
"v_exp_f32_e32 v151, v151\n" \
"v_mfma_f32_16x16x32_bf16 v[16:19], a[176:179], v[192:195], v[16:19]\n" \
"ds_read_b128 a[116:119], v208 offset:45056\n" \
"v_mfma_f32_16x16x32_bf16 v[16:19], a[144:147], v[184:187], v[16:19]\n" \
"v_exp_f32_e64 v152, v152\n" \
"v_mfma_f32_16x16x32_bf16 v[20:23], a[180:183], v[192:195], v[20:23]\n" \
"v_cvt_pk_bf16_f32 v218, v148, v149\n" \
"v_cvt_pk_bf16_f32 v219, v150, v151\n" \
"v_mfma_f32_16x16x32_bf16 v[20:23], a[148:151], v[184:187], v[20:23]\n" \
"v_exp_f32_e64 v153, v153\n" \
"v_mfma_f32_16x16x32_bf16 v[24:27], a[184:187], v[192:195], v[24:27]\n" \
"v_exp_f32_e64 v154, v154\n" \
"v_mfma_f32_16x16x32_bf16 v[24:27], a[152:155], v[184:187], v[24:27]\n" \
"ds_read_b128 a[112:115], %[kb0] offset:45056\n" \
"s_add_u32 s48, s48, 0x4000\n" \
"v_mfma_f32_16x16x32_bf16 v[28:31], a[188:191], v[192:195], v[28:31]\n" \
"v_exp_f32_e64 v155, v155\n" \
"v_mfma_f32_16x16x32_bf16 v[28:31], a[156:159], v[184:187], v[28:31]\n" \
"s_waitcnt vmcnt(13) lgkmcnt(2)\n" \
"s_barrier\n" \
"v_mfma_f32_16x16x32_bf16 v[128:131], a[96:99], a[0:3], v[160:163]\n" \
"v_exp_f32_e64 v156, v156\n" \
"v_mfma_f32_16x16x32_bf16 v[128:131], a[100:103], a[4:7], v[128:131]\n" \
"s_add_u32 m0, %[m0b], 81920\n" \
"v_mfma_f32_16x16x32_bf16 v[128:131], a[104:107], a[8:11], v[128:131]\n" \
"v_exp_f32_e64 v157, v157\n" \
"v_mfma_f32_16x16x32_bf16 v[128:131], a[108:111], a[12:15], v[128:131]\n" \
"v_cvt_pk_bf16_f32 v220, v152, v153\n" \
"v_cvt_pk_bf16_f32 v221, v154, v155\n" \
"v_mfma_f32_16x16x32_bf16 v[132:135], a[124:127], a[12:15], v[160:163]\n" \
"v_exp_f32_e64 v158, v158\n" \
"v_mfma_f32_16x16x32_bf16 v[132:135], a[120:123], a[8:11], v[132:135]\n" \
"s_waitcnt lgkmcnt(0)\n" \
"v_mfma_f32_16x16x32_bf16 v[132:135], a[116:119], a[4:7], v[132:135]\n" \
"buffer_load_dwordx4 %[vd0], %[srdv], s48 offen offset:0 lds\n" \
"v_mfma_f32_16x16x32_bf16 v[132:135], a[112:115], a[0:3], v[132:135]\n" \
"v_exp_f32_e32 v159, v159\n" \
"v_mfma_f32_16x16x32_bf16 v[32:35], a[160:163], v[196:199], v[32:35]\n" \
"ds_read_b128 a[64:67], %[kb0] offset:49152\n" \
"v_mfma_f32_16x16x32_bf16 v[32:35], a[128:131], v[188:191], v[32:35]\n" \
"v_exp_f32_e64 v128, v128\n" \
"v_mfma_f32_16x16x32_bf16 v[36:39], a[164:167], v[196:199], v[36:39]\n" \
"v_cvt_pk_bf16_f32 v222, v156, v157\n" \
"v_cvt_pk_bf16_f32 v223, v158, v159\n" \
"v_mfma_f32_16x16x32_bf16 v[36:39], a[132:135], v[188:191], v[36:39]\n" \
"v_exp_f32_e64 v129, v129\n" \
"v_mfma_f32_16x16x32_bf16 v[40:43], a[168:171], v[196:199], v[40:43]\n" \
"ds_read_b128 a[68:71], v208 offset:49152\n" \
"v_mfma_f32_16x16x32_bf16 v[40:43], a[136:139], v[188:191], v[40:43]\n" \
"v_exp_f32_e64 v130, v130\n" \
"v_mfma_f32_16x16x32_bf16 v[44:47], a[172:175], v[196:199], v[44:47]\n" \
"ds_read_b128 a[72:75], v209 offset:49152\n" \
"v_mfma_f32_16x16x32_bf16 v[44:47], a[140:143], v[188:191], v[44:47]\n" \
"v_exp_f32_e64 v131, v131\n" \
"v_smfmac_f32_16x16x64_bf16 v[176:179], a[192:195], v[184:191], v228\n" \
"v_mfma_f32_16x16x32_bf16 v[136:139], a[96:99], a[16:19], v[164:167]\n" \
"v_exp_f32_e64 v132, v132\n" \
"v_mfma_f32_16x16x32_bf16 v[136:139], a[100:103], a[20:23], v[136:139]\n" \
"ds_read_b128 a[76:79], v210 offset:49152\n" \
"v_mfma_f32_16x16x32_bf16 v[136:139], a[104:107], a[24:27], v[136:139]\n" \
"v_exp_f32_e64 v133, v133\n" \
"v_mfma_f32_16x16x32_bf16 v[136:139], a[108:111], a[28:31], v[136:139]\n" \
"v_cvt_pk_bf16_f32 v192, v128, v129\n" \
"v_cvt_pk_bf16_f32 v193, v130, v131\n" \
"v_mfma_f32_16x16x32_bf16 v[140:143], a[124:127], a[28:31], v[164:167]\n" \
"v_exp_f32_e64 v134, v134\n" \
"v_mfma_f32_16x16x32_bf16 v[140:143], a[120:123], a[24:27], v[140:143]\n" \
"ds_read_b128 a[92:95], v210 offset:53248\n" \
"v_mfma_f32_16x16x32_bf16 v[140:143], a[116:119], a[20:23], v[140:143]\n" \
"buffer_load_dwordx4 v215, %[srdv], s48 offen offset:1024 lds\n" \
"v_mfma_f32_16x16x32_bf16 v[140:143], a[112:115], a[16:19], v[140:143]\n" \
"v_exp_f32_e64 v135, v135\n" \
"v_mfma_f32_16x16x32_bf16 v[48:51], a[176:179], v[196:199], v[48:51]\n" \
"ds_read_b128 a[88:91], v209 offset:53248\n" \
"v_mfma_f32_16x16x32_bf16 v[48:51], a[144:147], v[188:191], v[48:51]\n" \
"v_exp_f32_e64 v136, v136\n" \
"v_mfma_f32_16x16x32_bf16 v[52:55], a[180:183], v[196:199], v[52:55]\n" \
"v_cvt_pk_bf16_f32 v194, v132, v133\n" \
"v_cvt_pk_bf16_f32 v195, v134, v135\n" \
"v_mfma_f32_16x16x32_bf16 v[52:55], a[148:151], v[188:191], v[52:55]\n" \
"v_exp_f32_e64 v137, v137\n" \
"v_mfma_f32_16x16x32_bf16 v[56:59], a[184:187], v[196:199], v[56:59]\n" \
"v_exp_f32_e64 v138, v138\n" \
"v_mfma_f32_16x16x32_bf16 v[56:59], a[152:155], v[188:191], v[56:59]\n" \
"ds_read_b128 a[84:87], v208 offset:53248\n" \
"v_mfma_f32_16x16x32_bf16 v[60:63], a[188:191], v[196:199], v[60:63]\n" \
"v_exp_f32_e64 v139, v139\n" \
"v_mfma_f32_16x16x32_bf16 v[60:63], a[156:159], v[188:191], v[60:63]\n" \
"s_cmp_lg_u32 %[wid], 1\n" \
"s_cbranch_scc1 LtB5_%=\n" \
"global_load_dword v237, v236, s[58:59]\n" \
"s_add_u32 s61, s48, 49152\n" \
"s_mov_b32 exec_hi, 0\n" \
"s_nop 0\n" \
"buffer_load_dword v237, v224, %[srdp], s61 offen\n" \
"s_mov_b32 exec_hi, -1\n" \
"s_nop 0\n" \
"LtB5_%=:\n" \
"v_mfma_f32_16x16x32_bf16 v[144:147], a[96:99], a[32:35], v[168:171]\n" \
"v_exp_f32_e64 v140, v140\n" \
"v_mfma_f32_16x16x32_bf16 v[144:147], a[100:103], a[36:39], v[144:147]\n" \
"ds_read_b128 a[80:83], %[kb0] offset:53248\n" \
"v_mfma_f32_16x16x32_bf16 v[144:147], a[104:107], a[40:43], v[144:147]\n" \
"v_exp_f32_e64 v141, v141\n" \
"v_mfma_f32_16x16x32_bf16 v[144:147], a[108:111], a[44:47], v[144:147]\n" \
"v_cvt_pk_bf16_f32 v196, v136, v137\n" \
"v_cvt_pk_bf16_f32 v197, v138, v139\n" \
"v_mfma_f32_16x16x32_bf16 v[148:151], a[124:127], a[44:47], v[168:171]\n" \
"v_exp_f32_e64 v142, v142\n" \
"v_mfma_f32_16x16x32_bf16 v[148:151], a[120:123], a[40:43], v[148:151]\n" \
"v_mfma_f32_16x16x32_bf16 v[148:151], a[116:119], a[36:39], v[148:151]\n" \
"buffer_load_dwordx4 %[vd0], %[srdv], s48 offen offset:2048 lds\n" \
"v_mfma_f32_16x16x32_bf16 v[148:151], a[112:115], a[32:35], v[148:151]\n" \
"v_exp_f32_e64 v143, v143\n" \
"v_mfma_f32_16x16x32_bf16 v[96:99], a[160:163], v[204:207], v[96:99]\n" \
"ds_read_b64_tr_b16 a[160:161], %[vvb] offset:40960\n" \
"v_mfma_f32_16x16x32_bf16 v[96:99], a[128:131], v[220:223], v[96:99]\n" \
"v_exp_f32_e64 v144, v144\n" \
"v_mfma_f32_16x16x32_bf16 v[100:103], a[164:167], v[204:207], v[100:103]\n" \
"v_cvt_pk_bf16_f32 v198, v140, v141\n" \
"v_cvt_pk_bf16_f32 v199, v142, v143\n" \
"v_mfma_f32_16x16x32_bf16 v[100:103], a[132:135], v[220:223], v[100:103]\n" \
"v_exp_f32_e64 v145, v145\n" \
"v_mfma_f32_16x16x32_bf16 v[104:107], a[168:171], v[204:207], v[104:107]\n" \
"ds_read_b64_tr_b16 a[162:163], %[vvb] offset:45056\n" \
"ds_read_b64_tr_b16 a[164:165], v211 offset:40960\n" \
"v_mfma_f32_16x16x32_bf16 v[104:107], a[136:139], v[220:223], v[104:107]\n" \
"v_exp_f32_e64 v146, v146\n" \
"v_mfma_f32_16x16x32_bf16 v[108:111], a[172:175], v[204:207], v[108:111]\n" \
"ds_read_b64_tr_b16 a[166:167], v211 offset:45056\n" \
"v_mfma_f32_16x16x32_bf16 v[108:111], a[140:143], v[220:223], v[108:111]\n" \
"v_exp_f32_e64 v147, v147\n" \
"v_smfmac_f32_16x16x64_bf16 v[180:183], a[192:195], v[216:223], v228\n" \
"ds_read_b64_tr_b16 a[168:169], %[vvb] offset:41472\n" \
"v_mfma_f32_16x16x32_bf16 v[152:155], a[96:99], a[48:51], v[172:175]\n" \
"v_exp_f32_e64 v148, v148\n" \
"v_mfma_f32_16x16x32_bf16 v[152:155], a[100:103], a[52:55], v[152:155]\n" \
"ds_read_b64_tr_b16 a[170:171], %[vvb] offset:45568\n" \
"v_mfma_f32_16x16x32_bf16 v[152:155], a[104:107], a[56:59], v[152:155]\n" \
"v_exp_f32_e64 v149, v149\n" \
"v_mfma_f32_16x16x32_bf16 v[152:155], a[108:111], a[60:63], v[152:155]\n" \
"v_cvt_pk_bf16_f32 v200, v144, v145\n" \
"v_cvt_pk_bf16_f32 v201, v146, v147\n" \
"v_mfma_f32_16x16x32_bf16 v[156:159], a[124:127], a[60:63], v[172:175]\n" \
"v_exp_f32_e64 v150, v150\n" \
"v_mfma_f32_16x16x32_bf16 v[156:159], a[120:123], a[56:59], v[156:159]\n" \
"ds_read_b64_tr_b16 a[172:173], v211 offset:41472\n" \
"v_mfma_f32_16x16x32_bf16 v[156:159], a[116:119], a[52:55], v[156:159]\n" \
"buffer_load_dwordx4 v215, %[srdv], s48 offen offset:3072 lds\n" \
"v_mfma_f32_16x16x32_bf16 v[156:159], a[112:115], a[48:51], v[156:159]\n" \
"v_exp_f32_e64 v151, v151\n" \
"v_mfma_f32_16x16x32_bf16 v[112:115], a[176:179], v[204:207], v[112:115]\n" \
"ds_read_b64_tr_b16 a[174:175], v211 offset:45568\n" \
"ds_read_b64_tr_b16 a[176:177], %[vvb] offset:41984\n" \
"v_mfma_f32_16x16x32_bf16 v[112:115], a[144:147], v[220:223], v[112:115]\n" \
"v_exp_f32_e64 v152, v152\n" \
"v_mfma_f32_16x16x32_bf16 v[116:119], a[180:183], v[204:207], v[116:119]\n" \
"v_cvt_pk_bf16_f32 v202, v148, v149\n" \
"v_cvt_pk_bf16_f32 v203, v150, v151\n" \
"v_mfma_f32_16x16x32_bf16 v[116:119], a[148:151], v[220:223], v[116:119]\n" \
"v_exp_f32_e64 v153, v153\n" \
"v_mfma_f32_16x16x32_bf16 v[120:123], a[184:187], v[204:207], v[120:123]\n" \
"v_exp_f32_e64 v154, v154\n" \
"v_mfma_f32_16x16x32_bf16 v[120:123], a[152:155], v[220:223], v[120:123]\n" \
"ds_read_b64_tr_b16 a[178:179], %[vvb] offset:46080\n" \
"v_mfma_f32_16x16x32_bf16 v[124:127], a[188:191], v[204:207], v[124:127]\n" \
"v_exp_f32_e64 v155, v155\n" \
"v_mfma_f32_16x16x32_bf16 v[124:127], a[156:159], v[220:223], v[124:127]\n" \
"ds_read_b64_tr_b16 a[180:181], v211 offset:41984\n" \
"s_waitcnt lgkmcnt(13)\n" \
"v_mfma_f32_16x16x32_bf16 v[128:131], a[64:67], a[0:3], v[160:163]\n" \
"v_exp_f32_e32 v156, v156\n" \
"v_mfma_f32_16x16x32_bf16 v[128:131], a[68:71], a[4:7], v[128:131]\n" \
"ds_read_b64_tr_b16 a[182:183], v211 offset:46080\n" \
"s_add_u32 m0, %[m0b], 32768\n" \
"v_mfma_f32_16x16x32_bf16 v[128:131], a[72:75], a[8:11], v[128:131]\n" \
"v_exp_f32_e64 v157, v157\n" \
"v_mfma_f32_16x16x32_bf16 v[128:131], a[76:79], a[12:15], v[128:131]\n" \
"v_cvt_pk_bf16_f32 v204, v152, v153\n" \
"v_cvt_pk_bf16_f32 v205, v154, v155\n" \
"v_mfma_f32_16x16x32_bf16 v[132:135], a[92:95], a[12:15], v[160:163]\n" \
"v_exp_f32_e64 v158, v158\n" \
"v_mfma_f32_16x16x32_bf16 v[132:135], a[88:91], a[8:11], v[132:135]\n" \
"ds_read_b64_tr_b16 a[184:185], %[vvb] offset:42496\n" \
"s_waitcnt lgkmcnt(5)\n" \
"v_mfma_f32_16x16x32_bf16 v[132:135], a[84:87], a[4:7], v[132:135]\n" \
"buffer_load_dwordx4 %[kd0], %[srdk], s48 offen offset:0 lds\n" \
"v_mfma_f32_16x16x32_bf16 v[132:135], a[80:83], a[0:3], v[132:135]\n" \
"v_exp_f32_e32 v159, v159\n" \
"v_mfma_f32_16x16x32_bf16 v[64:67], a[128:131], v[216:219], v[64:67]\n" \
"ds_read_b64_tr_b16 a[186:187], %[vvb] offset:46592\n" \
"ds_read_b64_tr_b16 a[188:189], v211 offset:42496\n" \
"v_mfma_f32_16x16x32_bf16 v[64:67], a[160:163], v[200:203], v[64:67]\n" \
"v_exp_f32_e64 v128, v128\n" \
"v_mfma_f32_16x16x32_bf16 v[68:71], a[132:135], v[216:219], v[68:71]\n" \
"v_cvt_pk_bf16_f32 v206, v156, v157\n" \
"v_cvt_pk_bf16_f32 v207, v158, v159\n" \
"v_mfma_f32_16x16x32_bf16 v[68:71], a[164:167], v[200:203], v[68:71]\n" \
"v_exp_f32_e64 v129, v129\n" \
"v_mfma_f32_16x16x32_bf16 v[72:75], a[136:139], v[216:219], v[72:75]\n" \
"ds_read_b64_tr_b16 a[190:191], v211 offset:46592\n" \
"ds_read_b64_tr_b16 a[128:129], %[vvb] offset:49152\n" \
"v_mfma_f32_16x16x32_bf16 v[72:75], a[168:171], v[200:203], v[72:75]\n" \
"v_exp_f32_e64 v130, v130\n" \
"v_mfma_f32_16x16x32_bf16 v[76:79], a[140:143], v[216:219], v[76:79]\n" \
"ds_read_b64_tr_b16 a[130:131], %[vvb] offset:53248\n" \
"ds_read_b64_tr_b16 a[132:133], v211 offset:49152\n" \
"v_mfma_f32_16x16x32_bf16 v[76:79], a[172:175], v[200:203], v[76:79]\n" \
"v_exp_f32_e64 v131, v131\n" \
"v_smfmac_f32_16x16x64_bf16 v[176:179], a[192:195], v[192:199], v228\n" \
"ds_read_b64_tr_b16 a[134:135], v211 offset:53248\n" \
"ds_read_b64_tr_b16 a[136:137], %[vvb] offset:49664\n" \
"s_cbranch_vccz Lpp6_%=\n" \
"buffer_store_dwordx4 a[196:199], v235, %[srdz], s62 offen offset:1024 sc1\n" \
"buffer_store_dwordx4 a[196:199], v226, %[srdz], s62 offen offset:1024 sc1\n" \
"Lpp6_%=:\n" \
"v_mfma_f32_16x16x32_bf16 v[136:139], a[64:67], a[16:19], v[164:167]\n" \
"v_exp_f32_e32 v132, v132\n" \
"v_mfma_f32_16x16x32_bf16 v[136:139], a[68:71], a[20:23], v[136:139]\n" \
"ds_read_b64_tr_b16 a[138:139], %[vvb] offset:53760\n" \
"ds_read_b64_tr_b16 a[140:141], v211 offset:49664\n" \
"v_mfma_f32_16x16x32_bf16 v[136:139], a[72:75], a[24:27], v[136:139]\n" \
"v_exp_f32_e64 v133, v133\n" \
"v_mfma_f32_16x16x32_bf16 v[136:139], a[76:79], a[28:31], v[136:139]\n" \
"v_cvt_pk_bf16_f32 v184, v128, v129\n" \
"v_cvt_pk_bf16_f32 v185, v130, v131\n" \
"v_mfma_f32_16x16x32_bf16 v[140:143], a[92:95], a[28:31], v[164:167]\n" \
"v_exp_f32_e64 v134, v134\n" \
"v_mfma_f32_16x16x32_bf16 v[140:143], a[88:91], a[24:27], v[140:143]\n" \
"ds_read_b64_tr_b16 a[142:143], v211 offset:53760\n" \
"s_waitcnt lgkmcnt(8)\n" \
"v_mfma_f32_16x16x32_bf16 v[140:143], a[84:87], a[20:23], v[140:143]\n" \
"buffer_load_dwordx4 v212, %[srdk], s48 offen offset:1024 lds\n" \
"v_mfma_f32_16x16x32_bf16 v[140:143], a[80:83], a[16:19], v[140:143]\n" \
"v_exp_f32_e32 v135, v135\n" \
"v_mfma_f32_16x16x32_bf16 v[80:83], a[144:147], v[216:219], v[80:83]\n" \
"ds_read_b64_tr_b16 a[144:145], %[vvb] offset:50176\n" \
"ds_read_b64_tr_b16 a[146:147], %[vvb] offset:54272\n" \
"v_mfma_f32_16x16x32_bf16 v[80:83], a[176:179], v[200:203], v[80:83]\n" \
"v_exp_f32_e64 v136, v136\n" \
"v_mfma_f32_16x16x32_bf16 v[84:87], a[148:151], v[216:219], v[84:87]\n" \
"v_cvt_pk_bf16_f32 v186, v132, v133\n" \
"v_cvt_pk_bf16_f32 v187, v134, v135\n" \
"v_mfma_f32_16x16x32_bf16 v[84:87], a[180:183], v[200:203], v[84:87]\n" \
"v_exp_f32_e64 v137, v137\n" \
"v_mfma_f32_16x16x32_bf16 v[88:91], a[152:155], v[216:219], v[88:91]\n" \
"v_exp_f32_e64 v138, v138\n" \
"v_mfma_f32_16x16x32_bf16 v[88:91], a[184:187], v[200:203], v[88:91]\n" \
"ds_read_b64_tr_b16 a[148:149], v211 offset:50176\n" \
"ds_read_b64_tr_b16 a[150:151], v211 offset:54272\n" \
"v_mfma_f32_16x16x32_bf16 v[92:95], a[156:159], v[216:219], v[92:95]\n" \
"v_exp_f32_e64 v139, v139\n" \
"v_mfma_f32_16x16x32_bf16 v[92:95], a[188:191], v[200:203], v[92:95]\n" \
"ds_read_b64_tr_b16 a[152:153], %[vvb] offset:50688\n" \
"global_load_dword v237, %[vct], %[pct]\n" \
"v_mfma_f32_16x16x32_bf16 v[144:147], a[64:67], a[32:35], v[168:171]\n" \
"v_exp_f32_e64 v140, v140\n" \
"v_mfma_f32_16x16x32_bf16 v[144:147], a[68:71], a[36:39], v[144:147]\n" \
"ds_read_b64_tr_b16 a[154:155], %[vvb] offset:54784\n" \
"ds_read_b64_tr_b16 a[156:157], v211 offset:50688\n" \
"v_mfma_f32_16x16x32_bf16 v[144:147], a[72:75], a[40:43], v[144:147]\n" \
"v_exp_f32_e64 v141, v141\n" \
"v_mfma_f32_16x16x32_bf16 v[144:147], a[76:79], a[44:47], v[144:147]\n" \
"v_cvt_pk_bf16_f32 v188, v136, v137\n" \
"v_cvt_pk_bf16_f32 v189, v138, v139\n" \
"v_mfma_f32_16x16x32_bf16 v[148:151], a[92:95], a[44:47], v[168:171]\n" \
"v_exp_f32_e64 v142, v142\n" \
"v_mfma_f32_16x16x32_bf16 v[148:151], a[88:91], a[40:43], v[148:151]\n" \
"ds_read_b64_tr_b16 a[158:159], v211 offset:54784\n" \
"s_waitcnt lgkmcnt(8)\n" \
"v_mfma_f32_16x16x32_bf16 v[148:151], a[84:87], a[36:39], v[148:151]\n" \
"buffer_load_dwordx4 v213, %[srdk], s48 offen offset:2048 lds\n" \
"v_mfma_f32_16x16x32_bf16 v[148:151], a[80:83], a[32:35], v[148:151]\n" \
"v_exp_f32_e32 v143, v143\n" \
"v_mfma_f32_16x16x32_bf16 v[0:3], a[160:163], v[192:195], v[0:3]\n" \
"ds_read_b128 a[96:99], %[kb0] offset:57344\n" \
"v_mfma_f32_16x16x32_bf16 v[0:3], a[128:131], v[184:187], v[0:3]\n" \
"v_exp_f32_e64 v144, v144\n" \
"v_mfma_f32_16x16x32_bf16 v[4:7], a[164:167], v[192:195], v[4:7]\n" \
"v_cvt_pk_bf16_f32 v190, v140, v141\n" \
"v_cvt_pk_bf16_f32 v191, v142, v143\n" \
"v_mfma_f32_16x16x32_bf16 v[4:7], a[132:135], v[184:187], v[4:7]\n" \
"v_exp_f32_e64 v145, v145\n" \
"v_mfma_f32_16x16x32_bf16 v[8:11], a[168:171], v[192:195], v[8:11]\n" \
"ds_read_b128 a[100:103], v208 offset:57344\n" \
"v_mfma_f32_16x16x32_bf16 v[8:11], a[136:139], v[184:187], v[8:11]\n" \
"v_exp_f32_e64 v146, v146\n" \
"v_mfma_f32_16x16x32_bf16 v[12:15], a[172:175], v[192:195], v[12:15]\n" \
"ds_read_b128 a[104:107], v209 offset:57344\n" \
"v_mfma_f32_16x16x32_bf16 v[12:15], a[140:143], v[184:187], v[12:15]\n" \
"v_exp_f32_e64 v147, v147\n" \
"v_smfmac_f32_16x16x64_bf16 v[180:183], a[192:195], v[200:207], v228\n" \
"ds_read_b128 a[108:111], v210 offset:57344\n" \
"v_mfma_f32_16x16x32_bf16 v[152:155], a[64:67], a[48:51], v[172:175]\n" \
"v_exp_f32_e64 v148, v148\n" \
"v_mfma_f32_16x16x32_bf16 v[152:155], a[68:71], a[52:55], v[152:155]\n" \
"ds_read_b128 a[124:127], v210 offset:61440\n" \
"v_mfma_f32_16x16x32_bf16 v[152:155], a[72:75], a[56:59], v[152:155]\n" \
"v_exp_f32_e64 v149, v149\n" \
"v_mfma_f32_16x16x32_bf16 v[152:155], a[76:79], a[60:63], v[152:155]\n" \
"v_cvt_pk_bf16_f32 v216, v144, v145\n" \
"v_cvt_pk_bf16_f32 v217, v146, v147\n" \
"v_mfma_f32_16x16x32_bf16 v[156:159], a[92:95], a[60:63], v[172:175]\n" \
"v_exp_f32_e64 v150, v150\n" \
"v_mfma_f32_16x16x32_bf16 v[156:159], a[88:91], a[56:59], v[156:159]\n" \
"ds_read_b128 a[120:123], v209 offset:61440\n" \
"s_waitcnt lgkmcnt(6)\n" \
"v_mfma_f32_16x16x32_bf16 v[156:159], a[84:87], a[52:55], v[156:159]\n" \
"buffer_load_dwordx4 v214, %[srdk], s48 offen offset:3072 lds\n" \
"v_mfma_f32_16x16x32_bf16 v[156:159], a[80:83], a[48:51], v[156:159]\n" \
"v_exp_f32_e32 v151, v151\n" \
"v_mfma_f32_16x16x32_bf16 v[16:19], a[176:179], v[192:195], v[16:19]\n" \
"ds_read_b128 a[116:119], v208 offset:61440\n" \
"v_mfma_f32_16x16x32_bf16 v[16:19], a[144:147], v[184:187], v[16:19]\n" \
"v_exp_f32_e64 v152, v152\n" \
"v_mfma_f32_16x16x32_bf16 v[20:23], a[180:183], v[192:195], v[20:23]\n" \
"v_cvt_pk_bf16_f32 v218, v148, v149\n" \
"v_cvt_pk_bf16_f32 v219, v150, v151\n" \
"v_mfma_f32_16x16x32_bf16 v[20:23], a[148:151], v[184:187], v[20:23]\n" \
"v_exp_f32_e64 v153, v153\n" \
"v_mfma_f32_16x16x32_bf16 v[24:27], a[184:187], v[192:195], v[24:27]\n" \
"v_exp_f32_e64 v154, v154\n" \
"v_mfma_f32_16x16x32_bf16 v[24:27], a[152:155], v[184:187], v[24:27]\n" \
"ds_read_b128 a[112:115], %[kb0] offset:61440\n" \
"s_add_u32 s48, s48, 0x4000\n" \
"s_mov_b32 m0, s44\n" \
"s_addk_i32 s44, 0x400\n" \
"buffer_load_dwordx4 v229, s[64:67], s45 offen offset:0 lds\n" \
"s_add_u32 s45, s45, 229376\n" \
"v_mfma_f32_16x16x32_bf16 v[28:31], a[188:191], v[192:195], v[28:31]\n" \
"v_exp_f32_e64 v155, v155\n" \
"v_mfma_f32_16x16x32_bf16 v[28:31], a[156:159], v[184:187], v[28:31]\n" \
"s_waitcnt vmcnt(14) lgkmcnt(2)\n" \
"s_barrier\n" \
"v_mfma_f32_16x16x32_bf16 v[128:131], a[96:99], a[0:3], v[160:163]\n" \
"v_exp_f32_e64 v156, v156\n" \
"v_mfma_f32_16x16x32_bf16 v[128:131], a[100:103], a[4:7], v[128:131]\n" \
"s_add_u32 m0, %[m0b], 98304\n" \
"v_mfma_f32_16x16x32_bf16 v[128:131], a[104:107], a[8:11], v[128:131]\n" \
"v_exp_f32_e64 v157, v157\n" \
"v_mfma_f32_16x16x32_bf16 v[128:131], a[108:111], a[12:15], v[128:131]\n" \
"v_cvt_pk_bf16_f32 v220, v152, v153\n" \
"v_cvt_pk_bf16_f32 v221, v154, v155\n" \
"v_mfma_f32_16x16x32_bf16 v[132:135], a[124:127], a[12:15], v[160:163]\n" \
"v_exp_f32_e64 v158, v158\n" \
"v_mfma_f32_16x16x32_bf16 v[132:135], a[120:123], a[8:11], v[132:135]\n" \
"s_waitcnt lgkmcnt(0)\n" \
"v_mfma_f32_16x16x32_bf16 v[132:135], a[116:119], a[4:7], v[132:135]\n" \
"buffer_load_dwordx4 %[vd0], %[srdv], s48 offen offset:0 lds\n" \
"v_mfma_f32_16x16x32_bf16 v[132:135], a[112:115], a[0:3], v[132:135]\n" \
"v_exp_f32_e32 v159, v159\n" \
"v_mfma_f32_16x16x32_bf16 v[32:35], a[160:163], v[196:199], v[32:35]\n" \
"ds_read_b128 a[64:67], %[kb0] offset:0\n" \
"v_mfma_f32_16x16x32_bf16 v[32:35], a[128:131], v[188:191], v[32:35]\n" \
"v_exp_f32_e64 v128, v128\n" \
"v_mfma_f32_16x16x32_bf16 v[36:39], a[164:167], v[196:199], v[36:39]\n" \
"v_cvt_pk_bf16_f32 v222, v156, v157\n" \
"v_cvt_pk_bf16_f32 v223, v158, v159\n" \
"v_mfma_f32_16x16x32_bf16 v[36:39], a[132:135], v[188:191], v[36:39]\n" \
"v_exp_f32_e64 v129, v129\n" \
"v_mfma_f32_16x16x32_bf16 v[40:43], a[168:171], v[196:199], v[40:43]\n" \
"ds_read_b128 a[68:71], v208 offset:0\n" \
"v_mfma_f32_16x16x32_bf16 v[40:43], a[136:139], v[188:191], v[40:43]\n" \
"v_exp_f32_e64 v130, v130\n" \
"v_mfma_f32_16x16x32_bf16 v[44:47], a[172:175], v[196:199], v[44:47]\n" \
"ds_read_b128 a[72:75], v209 offset:0\n" \
"v_mfma_f32_16x16x32_bf16 v[44:47], a[140:143], v[188:191], v[44:47]\n" \
"v_exp_f32_e64 v131, v131\n" \
"v_smfmac_f32_16x16x64_bf16 v[176:179], a[192:195], v[184:191], v228\n" \
"s_cbranch_vccz Lpp7_%=\n" \
"buffer_store_dwordx4 a[196:199], v235, %[srdz], s63 offen offset:2048 sc1\n" \
"buffer_store_dwordx4 a[196:199], v226, %[srdz], s63 offen offset:2048 sc1\n" \
"Lpp7_%=:\n" \
"s_add_u32 s63, s63, 2560\n" \
"v_mfma_f32_16x16x32_bf16 v[136:139], a[96:99], a[16:19], v[164:167]\n" \
"v_exp_f32_e32 v132, v132\n" \
"v_mfma_f32_16x16x32_bf16 v[136:139], a[100:103], a[20:23], v[136:139]\n" \
"ds_read_b128 a[76:79], v210 offset:0\n" \
"v_mfma_f32_16x16x32_bf16 v[136:139], a[104:107], a[24:27], v[136:139]\n" \
"v_exp_f32_e64 v133, v133\n" \
"v_mfma_f32_16x16x32_bf16 v[136:139], a[108:111], a[28:31], v[136:139]\n" \
"v_cvt_pk_bf16_f32 v192, v128, v129\n" \
"v_cvt_pk_bf16_f32 v193, v130, v131\n" \
"v_mfma_f32_16x16x32_bf16 v[140:143], a[124:127], a[28:31], v[164:167]\n" \
"v_exp_f32_e64 v134, v134\n" \
"v_mfma_f32_16x16x32_bf16 v[140:143], a[120:123], a[24:27], v[140:143]\n" \
"ds_read_b128 a[92:95], v210 offset:4096\n" \
"v_mfma_f32_16x16x32_bf16 v[140:143], a[116:119], a[20:23], v[140:143]\n" \
"buffer_load_dwordx4 v215, %[srdv], s48 offen offset:1024 lds\n" \
"v_mfma_f32_16x16x32_bf16 v[140:143], a[112:115], a[16:19], v[140:143]\n" \
"v_exp_f32_e64 v135, v135\n" \
"v_mfma_f32_16x16x32_bf16 v[48:51], a[176:179], v[196:199], v[48:51]\n" \
"ds_read_b128 a[88:91], v209 offset:4096\n" \
"v_mfma_f32_16x16x32_bf16 v[48:51], a[144:147], v[188:191], v[48:51]\n" \
"v_exp_f32_e64 v136, v136\n" \
"v_mfma_f32_16x16x32_bf16 v[52:55], a[180:183], v[196:199], v[52:55]\n" \
"v_cvt_pk_bf16_f32 v194, v132, v133\n" \
"v_cvt_pk_bf16_f32 v195, v134, v135\n" \
"v_mfma_f32_16x16x32_bf16 v[52:55], a[148:151], v[188:191], v[52:55]\n" \
"v_exp_f32_e64 v137, v137\n" \
"v_mfma_f32_16x16x32_bf16 v[56:59], a[184:187], v[196:199], v[56:59]\n" \
"v_exp_f32_e64 v138, v138\n" \
"v_mfma_f32_16x16x32_bf16 v[56:59], a[152:155], v[188:191], v[56:59]\n" \
"ds_read_b128 a[84:87], v208 offset:4096\n" \
"v_mfma_f32_16x16x32_bf16 v[60:63], a[188:191], v[196:199], v[60:63]\n" \
"v_exp_f32_e64 v139, v139\n" \
"v_mfma_f32_16x16x32_bf16 v[60:63], a[156:159], v[188:191], v[60:63]\n" \
"s_cmp_lg_u32 %[wid], 2\n" \
"s_cbranch_scc1 LtB7_%=\n" \
"global_load_dword v237, v236, s[58:59]\n" \
"s_add_u32 s61, s48, 49152\n" \
"s_mov_b32 exec_hi, 0\n" \
"s_nop 0\n" \
"buffer_load_dword v237, v224, %[srdp], s61 offen\n" \
"s_mov_b32 exec_hi, -1\n" \
"s_nop 0\n" \
"LtB7_%=:\n" \
"v_mfma_f32_16x16x32_bf16 v[144:147], a[96:99], a[32:35], v[168:171]\n" \
"v_exp_f32_e64 v140, v140\n" \
"v_mfma_f32_16x16x32_bf16 v[144:147], a[100:103], a[36:39], v[144:147]\n" \
"ds_read_b128 a[80:83], %[kb0] offset:4096\n" \
"v_mfma_f32_16x16x32_bf16 v[144:147], a[104:107], a[40:43], v[144:147]\n" \
"v_exp_f32_e64 v141, v141\n" \
"v_mfma_f32_16x16x32_bf16 v[144:147], a[108:111], a[44:47], v[144:147]\n" \
"v_cvt_pk_bf16_f32 v196, v136, v137\n" \
"v_cvt_pk_bf16_f32 v197, v138, v139\n" \
"v_mfma_f32_16x16x32_bf16 v[148:151], a[124:127], a[44:47], v[168:171]\n" \
"v_exp_f32_e64 v142, v142\n" \
"v_mfma_f32_16x16x32_bf16 v[148:151], a[120:123], a[40:43], v[148:151]\n" \
"v_mfma_f32_16x16x32_bf16 v[148:151], a[116:119], a[36:39], v[148:151]\n" \
"buffer_load_dwordx4 %[vd0], %[srdv], s48 offen offset:2048 lds\n" \
"v_mfma_f32_16x16x32_bf16 v[148:151], a[112:115], a[32:35], v[148:151]\n" \
"v_exp_f32_e64 v143, v143\n" \
"v_mfma_f32_16x16x32_bf16 v[96:99], a[160:163], v[204:207], v[96:99]\n" \
".if . - adaln_body != 28672\n" \
".error \"size model\"\n" \
".endif\n" \
"Lk6_%=:\n" \
"s_cbranch_execz Lk7_%=\n" \
"s_nop 0\n" \
"ds_read_b64_tr_b16 a[160:161], %[vvb] offset:57344\n" \
"v_mfma_f32_16x16x32_bf16 v[96:99], a[128:131], v[220:223], v[96:99]\n" \
"v_exp_f32_e64 v144, v144\n" \
"v_mfma_f32_16x16x32_bf16 v[100:103], a[164:167], v[204:207], v[100:103]\n" \
"v_cvt_pk_bf16_f32 v198, v140, v141\n" \
"v_cvt_pk_bf16_f32 v199, v142, v143\n" \
"v_mfma_f32_16x16x32_bf16 v[100:103], a[132:135], v[220:223], v[100:103]\n" \
"v_exp_f32_e64 v145, v145\n" \
"v_mfma_f32_16x16x32_bf16 v[104:107], a[168:171], v[204:207], v[104:107]\n" \
"ds_read_b64_tr_b16 a[162:163], %[vvb] offset:61440\n" \
"ds_read_b64_tr_b16 a[164:165], v211 offset:57344\n" \
"v_mfma_f32_16x16x32_bf16 v[104:107], a[136:139], v[220:223], v[104:107]\n" \
"v_exp_f32_e64 v146, v146\n" \
"v_mfma_f32_16x16x32_bf16 v[108:111], a[172:175], v[204:207], v[108:111]\n" \
"ds_read_b64_tr_b16 a[166:167], v211 offset:61440\n" \
"v_mfma_f32_16x16x32_bf16 v[108:111], a[140:143], v[220:223], v[108:111]\n" \
"v_exp_f32_e64 v147, v147\n" \
"v_smfmac_f32_16x16x64_bf16 v[180:183], a[192:195], v[216:223], v228\n" \
"ds_read_b64_tr_b16 a[168:169], %[vvb] offset:57856\n" \
"v_mfma_f32_16x16x32_bf16 v[152:155], a[96:99], a[48:51], v[172:175]\n" \
"v_exp_f32_e64 v148, v148\n" \
"v_mfma_f32_16x16x32_bf16 v[152:155], a[100:103], a[52:55], v[152:155]\n" \
"ds_read_b64_tr_b16 a[170:171], %[vvb] offset:61952\n" \
"v_mfma_f32_16x16x32_bf16 v[152:155], a[104:107], a[56:59], v[152:155]\n" \
"v_exp_f32_e64 v149, v149\n" \
"v_mfma_f32_16x16x32_bf16 v[152:155], a[108:111], a[60:63], v[152:155]\n" \
"v_cvt_pk_bf16_f32 v200, v144, v145\n" \
"v_cvt_pk_bf16_f32 v201, v146, v147\n" \
"v_mfma_f32_16x16x32_bf16 v[156:159], a[124:127], a[60:63], v[172:175]\n" \
"v_exp_f32_e64 v150, v150\n" \
"v_mfma_f32_16x16x32_bf16 v[156:159], a[120:123], a[56:59], v[156:159]\n" \
"ds_read_b64_tr_b16 a[172:173], v211 offset:57856\n" \
"v_mfma_f32_16x16x32_bf16 v[156:159], a[116:119], a[52:55], v[156:159]\n" \
"buffer_load_dwordx4 v215, %[srdv], s48 offen offset:3072 lds\n" \
"v_mfma_f32_16x16x32_bf16 v[156:159], a[112:115], a[48:51], v[156:159]\n" \
"v_exp_f32_e64 v151, v151\n" \
"v_mfma_f32_16x16x32_bf16 v[112:115], a[176:179], v[204:207], v[112:115]\n" \
"ds_read_b64_tr_b16 a[174:175], v211 offset:61952\n" \
"ds_read_b64_tr_b16 a[176:177], %[vvb] offset:58368\n" \
"v_mfma_f32_16x16x32_bf16 v[112:115], a[144:147], v[220:223], v[112:115]\n" \
"v_exp_f32_e64 v152, v152\n" \
"v_mfma_f32_16x16x32_bf16 v[116:119], a[180:183], v[204:207], v[116:119]\n" \
"v_cvt_pk_bf16_f32 v202, v148, v149\n" \
"v_cvt_pk_bf16_f32 v203, v150, v151\n" \
"v_mfma_f32_16x16x32_bf16 v[116:119], a[148:151], v[220:223], v[116:119]\n" \
"v_exp_f32_e64 v153, v153\n" \
"v_mfma_f32_16x16x32_bf16 v[120:123], a[184:187], v[204:207], v[120:123]\n" \
"v_exp_f32_e64 v154, v154\n" \
"v_mfma_f32_16x16x32_bf16 v[120:123], a[152:155], v[220:223], v[120:123]\n" \
"ds_read_b64_tr_b16 a[178:179], %[vvb] offset:62464\n" \
"v_mfma_f32_16x16x32_bf16 v[124:127], a[188:191], v[204:207], v[124:127]\n" \
"v_exp_f32_e64 v155, v155\n" \
"v_mfma_f32_16x16x32_bf16 v[124:127], a[156:159], v[220:223], v[124:127]\n" \
"ds_read_b64_tr_b16 a[180:181], v211 offset:58368\n" \
"s_waitcnt lgkmcnt(13)\n" \
"v_mfma_f32_16x16x32_bf16 v[128:131], a[64:67], a[0:3], v[160:163]\n" \
"v_exp_f32_e32 v156, v156\n" \
"v_mfma_f32_16x16x32_bf16 v[128:131], a[68:71], a[4:7], v[128:131]\n" \
"ds_read_b64_tr_b16 a[182:183], v211 offset:62464\n" \
"s_add_u32 m0, %[m0b], 49152\n" \
"v_mfma_f32_16x16x32_bf16 v[128:131], a[72:75], a[8:11], v[128:131]\n" \
"v_exp_f32_e64 v157, v157\n" \
"v_mfma_f32_16x16x32_bf16 v[128:131], a[76:79], a[12:15], v[128:131]\n" \
"v_cvt_pk_bf16_f32 v204, v152, v153\n" \
"v_cvt_pk_bf16_f32 v205, v154, v155\n" \
"v_mfma_f32_16x16x32_bf16 v[132:135], a[92:95], a[12:15], v[160:163]\n" \
"v_exp_f32_e64 v158, v158\n" \
"v_mfma_f32_16x16x32_bf16 v[132:135], a[88:91], a[8:11], v[132:135]\n" \
"ds_read_b64_tr_b16 a[184:185], %[vvb] offset:58880\n" \
"s_waitcnt lgkmcnt(5)\n" \
"v_mfma_f32_16x16x32_bf16 v[132:135], a[84:87], a[4:7], v[132:135]\n" \
"buffer_load_dwordx4 %[kd0], %[srdk], s48 offen offset:0 lds\n" \
"v_mfma_f32_16x16x32_bf16 v[132:135], a[80:83], a[0:3], v[132:135]\n" \
"v_exp_f32_e32 v159, v159\n" \
"v_mfma_f32_16x16x32_bf16 v[64:67], a[128:131], v[216:219], v[64:67]\n" \
"ds_read_b64_tr_b16 a[186:187], %[vvb] offset:62976\n" \
"ds_read_b64_tr_b16 a[188:189], v211 offset:58880\n" \
"v_mfma_f32_16x16x32_bf16 v[64:67], a[160:163], v[200:203], v[64:67]\n" \
"v_exp_f32_e64 v128, v128\n" \
"v_mfma_f32_16x16x32_bf16 v[68:71], a[132:135], v[216:219], v[68:71]\n" \
"v_cvt_pk_bf16_f32 v206, v156, v157\n" \
"v_cvt_pk_bf16_f32 v207, v158, v159\n" \
"v_mfma_f32_16x16x32_bf16 v[68:71], a[164:167], v[200:203], v[68:71]\n" \
"v_exp_f32_e64 v129, v129\n" \
"v_mfma_f32_16x16x32_bf16 v[72:75], a[136:139], v[216:219], v[72:75]\n" \
"ds_read_b64_tr_b16 a[190:191], v211 offset:62976\n" \
"ds_read_b64_tr_b16 a[128:129], %[vvb] offset:0\n" \
"v_mfma_f32_16x16x32_bf16 v[72:75], a[168:171], v[200:203], v[72:75]\n" \
"v_exp_f32_e64 v130, v130\n" \
"v_mfma_f32_16x16x32_bf16 v[76:79], a[140:143], v[216:219], v[76:79]\n" \
"ds_read_b64_tr_b16 a[130:131], %[vvb] offset:4096\n" \
"ds_read_b64_tr_b16 a[132:133], v211 offset:0\n" \
"v_mfma_f32_16x16x32_bf16 v[76:79], a[172:175], v[200:203], v[76:79]\n" \
"v_exp_f32_e64 v131, v131\n" \
"v_smfmac_f32_16x16x64_bf16 v[176:179], a[192:195], v[192:199], v228\n" \
"ds_read_b64_tr_b16 a[134:135], v211 offset:4096\n" \
"ds_read_b64_tr_b16 a[136:137], %[vvb] offset:512\n" \
"v_mfma_f32_16x16x32_bf16 v[136:139], a[64:67], a[16:19], v[164:167]\n" \
"v_exp_f32_e64 v132, v132\n" \
"v_mfma_f32_16x16x32_bf16 v[136:139], a[68:71], a[20:23], v[136:139]\n" \
"ds_read_b64_tr_b16 a[138:139], %[vvb] offset:4608\n" \
"ds_read_b64_tr_b16 a[140:141], v211 offset:512\n" \
"v_mfma_f32_16x16x32_bf16 v[136:139], a[72:75], a[24:27], v[136:139]\n" \
"v_exp_f32_e64 v133, v133\n" \
"v_mfma_f32_16x16x32_bf16 v[136:139], a[76:79], a[28:31], v[136:139]\n" \
"v_cvt_pk_bf16_f32 v184, v128, v129\n" \
"v_cvt_pk_bf16_f32 v185, v130, v131\n" \
"v_mfma_f32_16x16x32_bf16 v[140:143], a[92:95], a[28:31], v[164:167]\n" \
"v_exp_f32_e64 v134, v134\n" \
"v_mfma_f32_16x16x32_bf16 v[140:143], a[88:91], a[24:27], v[140:143]\n" \
"ds_read_b64_tr_b16 a[142:143], v211 offset:4608\n" \
"s_waitcnt lgkmcnt(8)\n" \
"v_mfma_f32_16x16x32_bf16 v[140:143], a[84:87], a[20:23], v[140:143]\n" \
"buffer_load_dwordx4 v212, %[srdk], s48 offen offset:1024 lds\n" \
"v_mfma_f32_16x16x32_bf16 v[140:143], a[80:83], a[16:19], v[140:143]\n" \
"v_exp_f32_e32 v135, v135\n" \
"v_mfma_f32_16x16x32_bf16 v[80:83], a[144:147], v[216:219], v[80:83]\n" \
"ds_read_b64_tr_b16 a[144:145], %[vvb] offset:1024\n" \
"ds_read_b64_tr_b16 a[146:147], %[vvb] offset:5120\n" \
"v_mfma_f32_16x16x32_bf16 v[80:83], a[176:179], v[200:203], v[80:83]\n" \
"v_exp_f32_e64 v136, v136\n" \
"v_mfma_f32_16x16x32_bf16 v[84:87], a[148:151], v[216:219], v[84:87]\n" \
"v_cvt_pk_bf16_f32 v186, v132, v133\n" \
"v_cvt_pk_bf16_f32 v187, v134, v135\n" \
"v_mfma_f32_16x16x32_bf16 v[84:87], a[180:183], v[200:203], v[84:87]\n" \
"v_exp_f32_e64 v137, v137\n" \
"v_mfma_f32_16x16x32_bf16 v[88:91], a[152:155], v[216:219], v[88:91]\n" \
"v_exp_f32_e64 v138, v138\n" \
"v_mfma_f32_16x16x32_bf16 v[88:91], a[184:187], v[200:203], v[88:91]\n" \
"ds_read_b64_tr_b16 a[148:149], v211 offset:1024\n" \
"ds_read_b64_tr_b16 a[150:151], v211 offset:5120\n" \
"v_mfma_f32_16x16x32_bf16 v[92:95], a[156:159], v[216:219], v[92:95]\n" \
"v_exp_f32_e64 v139, v139\n" \
"v_mfma_f32_16x16x32_bf16 v[92:95], a[188:191], v[200:203], v[92:95]\n" \
"ds_read_b64_tr_b16 a[152:153], %[vvb] offset:1536\n" \
"s_cbranch_vccz Lpp8_%=\n" \
"buffer_store_dwordx4 a[196:199], v235, %[srdz], s62 offen offset:2048 sc1\n" \
"buffer_store_dwordx4 a[196:199], v226, %[srdz], s62 offen offset:2048 sc1\n" \
"Lpp8_%=:\n" \
"s_add_u32 s62, s62, 2560\n" \
"v_mfma_f32_16x16x32_bf16 v[144:147], a[64:67], a[32:35], v[168:171]\n" \
"v_exp_f32_e32 v140, v140\n" \
"v_mfma_f32_16x16x32_bf16 v[144:147], a[68:71], a[36:39], v[144:147]\n" \
"ds_read_b64_tr_b16 a[154:155], %[vvb] offset:5632\n" \
"ds_read_b64_tr_b16 a[156:157], v211 offset:1536\n" \
"v_mfma_f32_16x16x32_bf16 v[144:147], a[72:75], a[40:43], v[144:147]\n" \
"v_exp_f32_e64 v141, v141\n" \
"v_mfma_f32_16x16x32_bf16 v[144:147], a[76:79], a[44:47], v[144:147]\n" \
"v_cvt_pk_bf16_f32 v188, v136, v137\n" \
"v_cvt_pk_bf16_f32 v189, v138, v139\n" \
"v_mfma_f32_16x16x32_bf16 v[148:151], a[92:95], a[44:47], v[168:171]\n" \
"v_exp_f32_e64 v142, v142\n" \
"v_mfma_f32_16x16x32_bf16 v[148:151], a[88:91], a[40:43], v[148:151]\n" \
"ds_read_b64_tr_b16 a[158:159], v211 offset:5632\n" \
"s_waitcnt lgkmcnt(8)\n" \
"v_mfma_f32_16x16x32_bf16 v[148:151], a[84:87], a[36:39], v[148:151]\n" \
"buffer_load_dwordx4 v213, %[srdk], s48 offen offset:2048 lds\n" \
"v_mfma_f32_16x16x32_bf16 v[148:151], a[80:83], a[32:35], v[148:151]\n" \
"v_exp_f32_e32 v143, v143\n" \
"v_mfma_f32_16x16x32_bf16 v[0:3], a[160:163], v[192:195], v[0:3]\n" \
"ds_read_b128 a[96:99], %[kb0] offset:8192\n" \
"v_mfma_f32_16x16x32_bf16 v[0:3], a[128:131], v[184:187], v[0:3]\n" \
"v_exp_f32_e64 v144, v144\n" \
"v_mfma_f32_16x16x32_bf16 v[4:7], a[164:167], v[192:195], v[4:7]\n" \
"v_cvt_pk_bf16_f32 v190, v140, v141\n" \
"v_cvt_pk_bf16_f32 v191, v142, v143\n" \
"v_mfma_f32_16x16x32_bf16 v[4:7], a[132:135], v[184:187], v[4:7]\n" \
"v_exp_f32_e64 v145, v145\n" \
"v_mfma_f32_16x16x32_bf16 v[8:11], a[168:171], v[192:195], v[8:11]\n" \
"ds_read_b128 a[100:103], v208 offset:8192\n" \
"v_mfma_f32_16x16x32_bf16 v[8:11], a[136:139], v[184:187], v[8:11]\n" \
"v_exp_f32_e64 v146, v146\n" \
"v_mfma_f32_16x16x32_bf16 v[12:15], a[172:175], v[192:195], v[12:15]\n" \
"ds_read_b128 a[104:107], v209 offset:8192\n" \
"v_mfma_f32_16x16x32_bf16 v[12:15], a[140:143], v[184:187], v[12:15]\n" \
"v_exp_f32_e64 v147, v147\n" \
"v_smfmac_f32_16x16x64_bf16 v[180:183], a[192:195], v[200:207], v228\n" \
"ds_read_b128 a[108:111], v210 offset:8192\n" \
"v_mfma_f32_16x16x32_bf16 v[152:155], a[64:67], a[48:51], v[172:175]\n" \
"v_exp_f32_e64 v148, v148\n" \
"v_mfma_f32_16x16x32_bf16 v[152:155], a[68:71], a[52:55], v[152:155]\n" \
"ds_read_b128 a[124:127], v210 offset:12288\n" \
"v_mfma_f32_16x16x32_bf16 v[152:155], a[72:75], a[56:59], v[152:155]\n" \
"v_exp_f32_e64 v149, v149\n" \
"v_mfma_f32_16x16x32_bf16 v[152:155], a[76:79], a[60:63], v[152:155]\n" \
"v_cvt_pk_bf16_f32 v216, v144, v145\n" \
"v_cvt_pk_bf16_f32 v217, v146, v147\n" \
"v_mfma_f32_16x16x32_bf16 v[156:159], a[92:95], a[60:63], v[172:175]\n" \
"v_exp_f32_e64 v150, v150\n" \
"v_mfma_f32_16x16x32_bf16 v[156:159], a[88:91], a[56:59], v[156:159]\n" \
"ds_read_b128 a[120:123], v209 offset:12288\n" \
"s_waitcnt lgkmcnt(6)\n" \
"v_mfma_f32_16x16x32_bf16 v[156:159], a[84:87], a[52:55], v[156:159]\n" \
"buffer_load_dwordx4 v214, %[srdk], s48 offen offset:3072 lds\n" \
"v_mfma_f32_16x16x32_bf16 v[156:159], a[80:83], a[48:51], v[156:159]\n" \
"v_exp_f32_e32 v151, v151\n" \
"v_mfma_f32_16x16x32_bf16 v[16:19], a[176:179], v[192:195], v[16:19]\n" \
"ds_read_b128 a[116:119], v208 offset:12288\n" \
"v_mfma_f32_16x16x32_bf16 v[16:19], a[144:147], v[184:187], v[16:19]\n" \
"v_exp_f32_e64 v152, v152\n" \
"v_mfma_f32_16x16x32_bf16 v[20:23], a[180:183], v[192:195], v[20:23]\n" \
"v_cvt_pk_bf16_f32 v218, v148, v149\n" \
"v_cvt_pk_bf16_f32 v219, v150, v151\n" \
"v_mfma_f32_16x16x32_bf16 v[20:23], a[148:151], v[184:187], v[20:23]\n" \
"v_exp_f32_e64 v153, v153\n" \
"v_mfma_f32_16x16x32_bf16 v[24:27], a[184:187], v[192:195], v[24:27]\n" \
"v_exp_f32_e64 v154, v154\n" \
"v_mfma_f32_16x16x32_bf16 v[24:27], a[152:155], v[184:187], v[24:27]\n" \
"ds_read_b128 a[112:115], %[kb0] offset:12288\n" \
"s_add_u32 s48, s48, 0x4000\n" \
"v_mfma_f32_16x16x32_bf16 v[28:31], a[188:191], v[192:195], v[28:31]\n" \
"v_exp_f32_e64 v155, v155\n" \
"v_mfma_f32_16x16x32_bf16 v[28:31], a[156:159], v[184:187], v[28:31]\n" \
"s_waitcnt vmcnt(14) lgkmcnt(2)\n" \
"s_barrier\n" \
"v_mfma_f32_16x16x32_bf16 v[128:131], a[96:99], a[0:3], v[160:163]\n" \
"v_exp_f32_e64 v156, v156\n" \
"v_mfma_f32_16x16x32_bf16 v[128:131], a[100:103], a[4:7], v[128:131]\n" \
"s_add_u32 m0, %[m0b], 114688\n" \
"v_mfma_f32_16x16x32_bf16 v[128:131], a[104:107], a[8:11], v[128:131]\n" \
"v_exp_f32_e64 v157, v157\n" \
"v_mfma_f32_16x16x32_bf16 v[128:131], a[108:111], a[12:15], v[128:131]\n" \
"v_cvt_pk_bf16_f32 v220, v152, v153\n" \
"v_cvt_pk_bf16_f32 v221, v154, v155\n" \
"v_mfma_f32_16x16x32_bf16 v[132:135], a[124:127], a[12:15], v[160:163]\n" \
"v_exp_f32_e64 v158, v158\n" \
"v_mfma_f32_16x16x32_bf16 v[132:135], a[120:123], a[8:11], v[132:135]\n" \
"s_waitcnt lgkmcnt(0)\n" \
"v_mfma_f32_16x16x32_bf16 v[132:135], a[116:119], a[4:7], v[132:135]\n" \
"buffer_load_dwordx4 %[vd0], %[srdv], s48 offen offset:0 lds\n" \
"v_mfma_f32_16x16x32_bf16 v[132:135], a[112:115], a[0:3], v[132:135]\n" \
"v_exp_f32_e32 v159, v159\n" \
"v_mfma_f32_16x16x32_bf16 v[32:35], a[160:163], v[196:199], v[32:35]\n" \
"ds_read_b128 a[64:67], %[kb0] offset:16384\n" \
"v_mfma_f32_16x16x32_bf16 v[32:35], a[128:131], v[188:191], v[32:35]\n" \
"v_exp_f32_e64 v128, v128\n" \
"v_mfma_f32_16x16x32_bf16 v[36:39], a[164:167], v[196:199], v[36:39]\n" \
"v_cvt_pk_bf16_f32 v222, v156, v157\n" \
"v_cvt_pk_bf16_f32 v223, v158, v159\n" \
"v_mfma_f32_16x16x32_bf16 v[36:39], a[132:135], v[188:191], v[36:39]\n" \
"v_exp_f32_e64 v129, v129\n" \
"v_mfma_f32_16x16x32_bf16 v[40:43], a[168:171], v[196:199], v[40:43]\n" \
"ds_read_b128 a[68:71], v208 offset:16384\n" \
"v_mfma_f32_16x16x32_bf16 v[40:43], a[136:139], v[188:191], v[40:43]\n" \
"v_exp_f32_e64 v130, v130\n" \
"v_mfma_f32_16x16x32_bf16 v[44:47], a[172:175], v[196:199], v[44:47]\n" \
"ds_read_b128 a[72:75], v209 offset:16384\n" \
"v_mfma_f32_16x16x32_bf16 v[44:47], a[140:143], v[188:191], v[44:47]\n" \
"v_exp_f32_e64 v131, v131\n" \
"v_smfmac_f32_16x16x64_bf16 v[176:179], a[192:195], v[184:191], v228\n" \
"v_mfma_f32_16x16x32_bf16 v[136:139], a[96:99], a[16:19], v[164:167]\n" \
"v_exp_f32_e64 v132, v132\n" \
"v_mfma_f32_16x16x32_bf16 v[136:139], a[100:103], a[20:23], v[136:139]\n" \
"ds_read_b128 a[76:79], v210 offset:16384\n" \
"v_mfma_f32_16x16x32_bf16 v[136:139], a[104:107], a[24:27], v[136:139]\n" \
"v_exp_f32_e64 v133, v133\n" \
"v_mfma_f32_16x16x32_bf16 v[136:139], a[108:111], a[28:31], v[136:139]\n" \
"v_cvt_pk_bf16_f32 v192, v128, v129\n" \
"v_cvt_pk_bf16_f32 v193, v130, v131\n" \
"v_mfma_f32_16x16x32_bf16 v[140:143], a[124:127], a[28:31], v[164:167]\n" \
"v_exp_f32_e64 v134, v134\n" \
"v_mfma_f32_16x16x32_bf16 v[140:143], a[120:123], a[24:27], v[140:143]\n" \
"ds_read_b128 a[92:95], v210 offset:20480\n" \
"v_mfma_f32_16x16x32_bf16 v[140:143], a[116:119], a[20:23], v[140:143]\n" \
"buffer_load_dwordx4 v215, %[srdv], s48 offen offset:1024 lds\n" \
"v_mfma_f32_16x16x32_bf16 v[140:143], a[112:115], a[16:19], v[140:143]\n" \
"v_exp_f32_e64 v135, v135\n" \
"v_mfma_f32_16x16x32_bf16 v[48:51], a[176:179], v[196:199], v[48:51]\n" \
"ds_read_b128 a[88:91], v209 offset:20480\n" \
"v_mfma_f32_16x16x32_bf16 v[48:51], a[144:147], v[188:191], v[48:51]\n" \
"v_exp_f32_e64 v136, v136\n" \
"v_mfma_f32_16x16x32_bf16 v[52:55], a[180:183], v[196:199], v[52:55]\n" \
"v_cvt_pk_bf16_f32 v194, v132, v133\n" \
"v_cvt_pk_bf16_f32 v195, v134, v135\n" \
"v_mfma_f32_16x16x32_bf16 v[52:55], a[148:151], v[188:191], v[52:55]\n" \
"v_exp_f32_e64 v137, v137\n" \
"v_mfma_f32_16x16x32_bf16 v[56:59], a[184:187], v[196:199], v[56:59]\n" \
"v_exp_f32_e64 v138, v138\n" \
"v_mfma_f32_16x16x32_bf16 v[56:59], a[152:155], v[188:191], v[56:59]\n" \
"ds_read_b128 a[84:87], v208 offset:20480\n" \
"v_mfma_f32_16x16x32_bf16 v[60:63], a[188:191], v[196:199], v[60:63]\n" \
"v_exp_f32_e64 v139, v139\n" \
"v_mfma_f32_16x16x32_bf16 v[60:63], a[156:159], v[188:191], v[60:63]\n" \
"s_cmp_lg_u32 %[wid], 3\n" \
"s_cbranch_scc1 LtB1_%=\n" \
"global_load_dword v237, v236, s[58:59]\n" \
"s_add_u32 s61, s48, 49152\n" \
"s_mov_b32 exec_hi, 0\n" \
"s_nop 0\n" \
"buffer_load_dword v237, v224, %[srdp], s61 offen\n" \
"s_mov_b32 exec_hi, -1\n" \
"s_nop 0\n" \
"LtB1_%=:\n" \
"v_mfma_f32_16x16x32_bf16 v[144:147], a[96:99], a[32:35], v[168:171]\n" \
"v_exp_f32_e64 v140, v140\n" \
"v_mfma_f32_16x16x32_bf16 v[144:147], a[100:103], a[36:39], v[144:147]\n" \
"ds_read_b128 a[80:83], %[kb0] offset:20480\n" \
"v_mfma_f32_16x16x32_bf16 v[144:147], a[104:107], a[40:43], v[144:147]\n" \
"v_exp_f32_e64 v141, v141\n" \
"v_mfma_f32_16x16x32_bf16 v[144:147], a[108:111], a[44:47], v[144:147]\n" \
"v_cvt_pk_bf16_f32 v196, v136, v137\n" \
"v_cvt_pk_bf16_f32 v197, v138, v139\n" \
"v_mfma_f32_16x16x32_bf16 v[148:151], a[124:127], a[44:47], v[168:171]\n" \
"v_exp_f32_e64 v142, v142\n" \
"v_mfma_f32_16x16x32_bf16 v[148:151], a[120:123], a[40:43], v[148:151]\n" \
"v_mfma_f32_16x16x32_bf16 v[148:151], a[116:119], a[36:39], v[148:151]\n" \
"buffer_load_dwordx4 %[vd0], %[srdv], s48 offen offset:2048 lds\n" \
"v_mfma_f32_16x16x32_bf16 v[148:151], a[112:115], a[32:35], v[148:151]\n" \
"v_exp_f32_e64 v143, v143\n" \
"v_mfma_f32_16x16x32_bf16 v[96:99], a[160:163], v[204:207], v[96:99]\n" \
"ds_read_b64_tr_b16 a[160:161], %[vvb] offset:8192\n" \
"v_mfma_f32_16x16x32_bf16 v[96:99], a[128:131], v[220:223], v[96:99]\n" \
"v_exp_f32_e64 v144, v144\n" \
"v_mfma_f32_16x16x32_bf16 v[100:103], a[164:167], v[204:207], v[100:103]\n" \
"v_cvt_pk_bf16_f32 v198, v140, v141\n" \
"v_cvt_pk_bf16_f32 v199, v142, v143\n" \
"v_mfma_f32_16x16x32_bf16 v[100:103], a[132:135], v[220:223], v[100:103]\n" \
"v_exp_f32_e64 v145, v145\n" \
"v_mfma_f32_16x16x32_bf16 v[104:107], a[168:171], v[204:207], v[104:107]\n" \
"ds_read_b64_tr_b16 a[162:163], %[vvb] offset:12288\n" \
"ds_read_b64_tr_b16 a[164:165], v211 offset:8192\n" \
"v_mfma_f32_16x16x32_bf16 v[104:107], a[136:139], v[220:223], v[104:107]\n" \
"v_exp_f32_e64 v146, v146\n" \
"v_mfma_f32_16x16x32_bf16 v[108:111], a[172:175], v[204:207], v[108:111]\n" \
"ds_read_b64_tr_b16 a[166:167], v211 offset:12288\n" \
"v_mfma_f32_16x16x32_bf16 v[108:111], a[140:143], v[220:223], v[108:111]\n" \
"v_exp_f32_e64 v147, v147\n" \
"v_smfmac_f32_16x16x64_bf16 v[180:183], a[192:195], v[216:223], v228\n" \
"ds_read_b64_tr_b16 a[168:169], %[vvb] offset:8704\n" \
"v_mfma_f32_16x16x32_bf16 v[152:155], a[96:99], a[48:51], v[172:175]\n" \
"v_exp_f32_e64 v148, v148\n" \
"v_mfma_f32_16x16x32_bf16 v[152:155], a[100:103], a[52:55], v[152:155]\n" \
"ds_read_b64_tr_b16 a[170:171], %[vvb] offset:12800\n" \
"v_mfma_f32_16x16x32_bf16 v[152:155], a[104:107], a[56:59], v[152:155]\n" \
"v_exp_f32_e64 v149, v149\n" \
"v_mfma_f32_16x16x32_bf16 v[152:155], a[108:111], a[60:63], v[152:155]\n" \
"v_cvt_pk_bf16_f32 v200, v144, v145\n" \
"v_cvt_pk_bf16_f32 v201, v146, v147\n" \
"v_mfma_f32_16x16x32_bf16 v[156:159], a[124:127], a[60:63], v[172:175]\n" \
"v_exp_f32_e64 v150, v150\n" \
"v_mfma_f32_16x16x32_bf16 v[156:159], a[120:123], a[56:59], v[156:159]\n" \
"ds_read_b64_tr_b16 a[172:173], v211 offset:8704\n" \
"v_mfma_f32_16x16x32_bf16 v[156:159], a[116:119], a[52:55], v[156:159]\n" \
"buffer_load_dwordx4 v215, %[srdv], s48 offen offset:3072 lds\n" \
"v_mfma_f32_16x16x32_bf16 v[156:159], a[112:115], a[48:51], v[156:159]\n" \
"v_exp_f32_e64 v151, v151\n" \
"v_mfma_f32_16x16x32_bf16 v[112:115], a[176:179], v[204:207], v[112:115]\n" \
"ds_read_b64_tr_b16 a[174:175], v211 offset:12800\n" \
"ds_read_b64_tr_b16 a[176:177], %[vvb] offset:9216\n" \
"v_mfma_f32_16x16x32_bf16 v[112:115], a[144:147], v[220:223], v[112:115]\n" \
"v_exp_f32_e64 v152, v152\n" \
"v_mfma_f32_16x16x32_bf16 v[116:119], a[180:183], v[204:207], v[116:119]\n" \
"v_cvt_pk_bf16_f32 v202, v148, v149\n" \
"v_cvt_pk_bf16_f32 v203, v150, v151\n" \
"v_mfma_f32_16x16x32_bf16 v[116:119], a[148:151], v[220:223], v[116:119]\n" \
"v_exp_f32_e64 v153, v153\n" \
"v_mfma_f32_16x16x32_bf16 v[120:123], a[184:187], v[204:207], v[120:123]\n" \
"v_exp_f32_e64 v154, v154\n" \
"v_mfma_f32_16x16x32_bf16 v[120:123], a[152:155], v[220:223], v[120:123]\n" \
"ds_read_b64_tr_b16 a[178:179], %[vvb] offset:13312\n" \
"v_mfma_f32_16x16x32_bf16 v[124:127], a[188:191], v[204:207], v[124:127]\n" \
"v_exp_f32_e64 v155, v155\n" \
"v_mfma_f32_16x16x32_bf16 v[124:127], a[156:159], v[220:223], v[124:127]\n" \
"ds_read_b64_tr_b16 a[180:181], v211 offset:9216\n" \
"s_waitcnt lgkmcnt(13)\n" \
"s_cmp_lg_u32 s60, 7\n" \
"s_cbranch_scc1 LmB_%=\n" \
"s_nop 0\n" \
"v_readlane_b32 s48, %[tlo], 11\n" \
"v_readlane_b32 s58, %[tlo], 12\n" \
"v_mfma_f32_16x16x32_bf16 v[128:131], a[64:67], a[0:3], v[160:163]\n" \
"v_exp_f32_e64 v156, v156\n" \
"v_mfma_f32_16x16x32_bf16 v[128:131], a[68:71], a[4:7], v[128:131]\n" \
"ds_read_b64_tr_b16 a[182:183], v211 offset:13312\n" \
"s_add_u32 m0, %[m0b], 0\n" \
"s_mov_b32 s49, s48\n" \
"v_mfma_f32_16x16x32_bf16 v[128:131], a[72:75], a[8:11], v[128:131]\n" \
"v_exp_f32_e64 v157, v157\n" \
"v_mfma_f32_16x16x32_bf16 v[128:131], a[76:79], a[12:15], v[128:131]\n" \
"v_cvt_pk_bf16_f32 v204, v152, v153\n" \
"v_cvt_pk_bf16_f32 v205, v154, v155\n" \
"v_mfma_f32_16x16x32_bf16 v[132:135], a[92:95], a[12:15], v[160:163]\n" \
"v_exp_f32_e64 v158, v158\n" \
"v_mfma_f32_16x16x32_bf16 v[132:135], a[88:91], a[8:11], v[132:135]\n" \
"ds_read_b64_tr_b16 a[184:185], %[vvb] offset:9728\n" \
"s_lshl_b32 s49, s49, 18\n" \
"s_or_b32 s49, s49, s58\n" \
"v_accvgpr_write_b32 a228, v225\n" \
"s_waitcnt lgkmcnt(5)\n" \
"v_mfma_f32_16x16x32_bf16 v[132:135], a[84:87], a[4:7], v[132:135]\n" \
"buffer_load_dwordx4 %[wo0], %[srdt], s49 offen offset:0 lds\n" \
"v_mfma_f32_16x16x32_bf16 v[132:135], a[80:83], a[0:3], v[132:135]\n" \
"v_exp_f32_e32 v159, v159\n" \
"v_mfma_f32_16x16x32_bf16 v[64:67], a[128:131], v[216:219], v[64:67]\n" \
"ds_read_b64_tr_b16 a[186:187], %[vvb] offset:13824\n" \
"ds_read_b64_tr_b16 a[188:189], v211 offset:9728\n" \
"v_readlane_b32 s60, %[thi], 11\n" \
"v_mfma_f32_16x16x32_bf16 v[64:67], a[160:163], v[200:203], v[64:67]\n" \
"v_exp_f32_e64 v128, v128\n" \
"v_mfma_f32_16x16x32_bf16 v[68:71], a[132:135], v[216:219], v[68:71]\n" \
"v_cvt_pk_bf16_f32 v206, v156, v157\n" \
"v_cvt_pk_bf16_f32 v207, v158, v159\n" \
"v_mfma_f32_16x16x32_bf16 v[68:71], a[164:167], v[200:203], v[68:71]\n" \
"v_exp_f32_e64 v129, v129\n" \
"v_mfma_f32_16x16x32_bf16 v[72:75], a[136:139], v[216:219], v[72:75]\n" \
"ds_read_b64_tr_b16 a[190:191], v211 offset:13824\n" \
"ds_read_b64_tr_b16 a[128:129], %[vvb] offset:16384\n" \
"v_readlane_b32 s59, %[thi], 12\n" \
"v_mfma_f32_16x16x32_bf16 v[72:75], a[168:171], v[200:203], v[72:75]\n" \
"v_exp_f32_e64 v130, v130\n" \
"v_mfma_f32_16x16x32_bf16 v[76:79], a[140:143], v[216:219], v[76:79]\n" \
"ds_read_b64_tr_b16 a[130:131], %[vvb] offset:20480\n" \
"ds_read_b64_tr_b16 a[132:133], v211 offset:16384\n" \
"v_readlane_b32 s53, %[tlo], 13\n" \
"v_mfma_f32_16x16x32_bf16 v[76:79], a[172:175], v[200:203], v[76:79]\n" \
"v_exp_f32_e64 v131, v131\n" \
"v_smfmac_f32_16x16x64_bf16 v[176:179], a[192:195], v[192:199], v228\n" \
"ds_read_b64_tr_b16 a[134:135], v211 offset:20480\n" \
"ds_read_b64_tr_b16 a[136:137], %[vvb] offset:16896\n" \
"v_readlane_b32 s50, %[thi], 13\n" \
"v_mfma_f32_16x16x32_bf16 v[136:139], a[64:67], a[16:19], v[164:167]\n" \
"v_exp_f32_e64 v132, v132\n" \
"v_mfma_f32_16x16x32_bf16 v[136:139], a[68:71], a[20:23], v[136:139]\n" \
"ds_read_b64_tr_b16 a[138:139], %[vvb] offset:20992\n" \
"ds_read_b64_tr_b16 a[140:141], v211 offset:16896\n" \
"v_readlane_b32 s46, %[tlo], 14\n" \
"v_add_u32_e32 v212, 0x3c00, %[wo0]\n" \
"v_mfma_f32_16x16x32_bf16 v[136:139], a[72:75], a[24:27], v[136:139]\n" \
"v_exp_f32_e64 v133, v133\n" \
"v_mfma_f32_16x16x32_bf16 v[136:139], a[76:79], a[28:31], v[136:139]\n" \
"v_cvt_pk_bf16_f32 v184, v128, v129\n" \
"v_cvt_pk_bf16_f32 v185, v130, v131\n" \
"v_mfma_f32_16x16x32_bf16 v[140:143], a[92:95], a[28:31], v[164:167]\n" \
"v_exp_f32_e64 v134, v134\n" \
"v_mfma_f32_16x16x32_bf16 v[140:143], a[88:91], a[24:27], v[140:143]\n" \
"ds_read_b64_tr_b16 a[142:143], v211 offset:20992\n" \
"v_readlane_b32 s47, %[thi], 14\n" \
"v_add_u32_e32 v213, 0x7800, %[wo0]\n" \
"s_waitcnt lgkmcnt(8)\n" \
"v_mfma_f32_16x16x32_bf16 v[140:143], a[84:87], a[20:23], v[140:143]\n" \
"buffer_load_dwordx4 v212, %[srdt], s49 offen offset:1024 lds\n" \
"v_mfma_f32_16x16x32_bf16 v[140:143], a[80:83], a[16:19], v[140:143]\n" \
"v_exp_f32_e32 v135, v135\n" \
"v_mfma_f32_16x16x32_bf16 v[80:83], a[144:147], v[216:219], v[80:83]\n" \
"ds_read_b64_tr_b16 a[144:145], %[vvb] offset:17408\n" \
"ds_read_b64_tr_b16 a[146:147], %[vvb] offset:21504\n" \
"ds_read_b32 v230, %[rd0]\n" \
"v_mfma_f32_16x16x32_bf16 v[80:83], a[176:179], v[200:203], v[80:83]\n" \
"v_exp_f32_e64 v136, v136\n" \
"v_mfma_f32_16x16x32_bf16 v[84:87], a[148:151], v[216:219], v[84:87]\n" \
"v_cvt_pk_bf16_f32 v186, v132, v133\n" \
"v_cvt_pk_bf16_f32 v187, v134, v135\n" \
"v_mfma_f32_16x16x32_bf16 v[84:87], a[180:183], v[200:203], v[84:87]\n" \
"v_exp_f32_e64 v137, v137\n" \
"v_mfma_f32_16x16x32_bf16 v[88:91], a[152:155], v[216:219], v[88:91]\n" \
"v_exp_f32_e64 v138, v138\n" \
"v_mfma_f32_16x16x32_bf16 v[88:91], a[184:187], v[200:203], v[88:91]\n" \
"ds_read_b64_tr_b16 a[148:149], v211 offset:17408\n" \
"v_readlane_b32 s55, %[thi], 15\n" \
"ds_read_b64_tr_b16 a[150:151], v211 offset:21504\n" \
"v_lshrrev_b32_e64 v234, 5, v224\n" \
"v_mfma_f32_16x16x32_bf16 v[92:95], a[156:159], v[216:219], v[92:95]\n" \
"v_exp_f32_e64 v139, v139\n" \
"v_mfma_f32_16x16x32_bf16 v[92:95], a[188:191], v[200:203], v[92:95]\n" \
"ds_read_b64_tr_b16 a[152:153], %[vvb] offset:17920\n" \
"v_mfma_f32_16x16x32_bf16 v[144:147], a[64:67], a[32:35], v[168:171]\n" \
"v_exp_f32_e64 v140, v140\n" \
"v_mfma_f32_16x16x32_bf16 v[144:147], a[68:71], a[36:39], v[144:147]\n" \
"ds_read_b64_tr_b16 a[154:155], %[vvb] offset:22016\n" \
"ds_read_b64_tr_b16 a[156:157], v211 offset:17920\n" \
"s_waitcnt lgkmcnt(5)\n" \
"v_add_f32_dpp v230, v230, v230 row_ror:4 row_mask:0xf bank_mask:0xf\n" \
"v_mfma_f32_16x16x32_bf16 v[144:147], a[72:75], a[40:43], v[144:147]\n" \
"v_exp_f32_e32 v141, v141\n" \
"v_mfma_f32_16x16x32_bf16 v[144:147], a[76:79], a[44:47], v[144:147]\n" \
"v_cvt_pk_bf16_f32 v188, v136, v137\n" \
"v_cvt_pk_bf16_f32 v189, v138, v139\n" \
"v_mfma_f32_16x16x32_bf16 v[148:151], a[92:95], a[44:47], v[168:171]\n" \
"v_exp_f32_e64 v142, v142\n" \
"v_mfma_f32_16x16x32_bf16 v[148:151], a[88:91], a[40:43], v[148:151]\n" \
"ds_read_b64_tr_b16 a[158:159], v211 offset:22016\n" \
"v_add_f32_dpp v230, v230, v230 row_ror:8 row_mask:0xf bank_mask:0xf\n" \
"s_waitcnt lgkmcnt(9)\n" \
"v_mfma_f32_16x16x32_bf16 v[148:151], a[84:87], a[36:39], v[148:151]\n" \
"buffer_load_dwordx4 v213, %[srdt], s49 offen offset:2048 lds\n" \
"v_mfma_f32_16x16x32_bf16 v[148:151], a[80:83], a[32:35], v[148:151]\n" \
"v_exp_f32_e32 v143, v143\n" \
"v_mfma_f32_16x16x32_bf16 v[0:3], a[160:163], v[192:195], v[0:3]\n" \
"ds_read_b128 a[96:99], %[kb0] offset:24576\n" \
"v_cvt_pk_bf16_f32 v230, v230, v230\n" \
"v_mfma_f32_16x16x32_bf16 v[0:3], a[128:131], v[184:187], v[0:3]\n" \
"v_exp_f32_e64 v144, v144\n" \
"v_mfma_f32_16x16x32_bf16 v[4:7], a[164:167], v[192:195], v[4:7]\n" \
"v_cvt_pk_bf16_f32 v190, v140, v141\n" \
"v_cvt_pk_bf16_f32 v191, v142, v143\n" \
"v_mfma_f32_16x16x32_bf16 v[4:7], a[132:135], v[184:187], v[4:7]\n" \
"v_exp_f32_e64 v145, v145\n" \
"v_mfma_f32_16x16x32_bf16 v[8:11], a[168:171], v[192:195], v[8:11]\n" \
"ds_read_b128 a[100:103], v208 offset:24576\n" \
"v_lshlrev_b32_e64 v230, 16, v230\n" \
"v_mfma_f32_16x16x32_bf16 v[8:11], a[136:139], v[184:187], v[8:11]\n" \
"v_exp_f32_e64 v146, v146\n" \
"v_mfma_f32_16x16x32_bf16 v[12:15], a[172:175], v[192:195], v[12:15]\n" \
"ds_read_b128 a[104:107], v209 offset:24576\n" \
"v_add_f32_e64 v230, s55, v230\n" \
"v_add_u32_e32 v214, 0xb400, %[wo0]\n" \
"v_mfma_f32_16x16x32_bf16 v[12:15], a[140:143], v[184:187], v[12:15]\n" \
"v_exp_f32_e64 v147, v147\n" \
"v_smfmac_f32_16x16x64_bf16 v[180:183], a[192:195], v[200:207], v228\n" \
"ds_read_b128 a[108:111], v210 offset:24576\n" \
"v_cvt_pk_bf16_f32 v230, v230, v230\n" \
"v_readlane_b32 s55, %[tlo], 15\n" \
"v_mfma_f32_16x16x32_bf16 v[152:155], a[64:67], a[48:51], v[172:175]\n" \
"v_exp_f32_e64 v148, v148\n" \
"v_mfma_f32_16x16x32_bf16 v[152:155], a[68:71], a[52:55], v[152:155]\n" \
"ds_read_b128 a[124:127], v210 offset:28672\n" \
"v_lshl_or_b32 v230, v230, 16, 1\n" \
"v_mfma_f32_16x16x32_bf16 v[152:155], a[72:75], a[56:59], v[152:155]\n" \
"v_exp_f32_e64 v149, v149\n" \
"v_mfma_f32_16x16x32_bf16 v[152:155], a[76:79], a[60:63], v[152:155]\n" \
"v_cvt_pk_bf16_f32 v216, v144, v145\n" \
"v_cvt_pk_bf16_f32 v217, v146, v147\n" \
"v_mfma_f32_16x16x32_bf16 v[156:159], a[92:95], a[60:63], v[172:175]\n" \
"v_exp_f32_e64 v150, v150\n" \
"v_mfma_f32_16x16x32_bf16 v[156:159], a[88:91], a[56:59], v[156:159]\n" \
"ds_read_b128 a[120:123], v209 offset:28672\n" \
"s_cmp_lg_u32 s52, 0\n" \
"s_cselect_b32 s55, 0x80000000, s55\n" \
"s_mov_b64 exec, 15\n" \
"buffer_store_dword v230, v234, %[srdz], s55 offen sc1\n" \
"s_mov_b64 exec, -1\n" \
"s_waitcnt lgkmcnt(6)\n" \
"v_mfma_f32_16x16x32_bf16 v[156:159], a[84:87], a[52:55], v[156:159]\n" \
"buffer_load_dwordx4 v214, %[srdt], s49 offen offset:3072 lds\n" \
".if . - adaln_body != 32768\n" \
".error \"size model\"\n" \
".endif\n" \
"Lk7_%=:\n" \
"s_cbranch_execz Lk8_%=\n" \
"s_nop 0\n" \
"v_mfma_f32_16x16x32_bf16 v[156:159], a[80:83], a[48:51], v[156:159]\n" \
"v_exp_f32_e64 v151, v151\n" \
"v_mfma_f32_16x16x32_bf16 v[16:19], a[176:179], v[192:195], v[16:19]\n" \
"ds_read_b128 a[116:119], v208 offset:28672\n" \
"v_mfma_f32_16x16x32_bf16 v[16:19], a[144:147], v[184:187], v[16:19]\n" \
"v_exp_f32_e64 v152, v152\n" \
"v_mfma_f32_16x16x32_bf16 v[20:23], a[180:183], v[192:195], v[20:23]\n" \
"v_cvt_pk_bf16_f32 v218, v148, v149\n" \
"v_cvt_pk_bf16_f32 v219, v150, v151\n" \
"v_mfma_f32_16x16x32_bf16 v[20:23], a[148:151], v[184:187], v[20:23]\n" \
"v_exp_f32_e64 v153, v153\n" \
"v_mfma_f32_16x16x32_bf16 v[24:27], a[184:187], v[192:195], v[24:27]\n" \
"v_exp_f32_e64 v154, v154\n" \
"v_mfma_f32_16x16x32_bf16 v[24:27], a[152:155], v[184:187], v[24:27]\n" \
"ds_read_b128 a[112:115], %[kb0] offset:28672\n" \
"s_mov_b32 m0, s44\n" \
"s_addk_i32 s44, 0x400\n" \
"buffer_load_dwordx4 v229, s[64:67], s45 offen offset:0 lds\n" \
"s_add_u32 s45, s45, 229376\n" \
"v_mfma_f32_16x16x32_bf16 v[28:31], a[188:191], v[192:195], v[28:31]\n" \
"v_exp_f32_e64 v155, v155\n" \
"v_mfma_f32_16x16x32_bf16 v[28:31], a[156:159], v[184:187], v[28:31]\n" \
"s_waitcnt vmcnt(6) lgkmcnt(2)\n" \
"s_barrier\n" \
"v_mfma_f32_16x16x32_bf16 v[128:131], a[96:99], a[0:3], v[160:163]\n" \
"v_exp_f32_e64 v156, v156\n" \
"s_mov_b32 m0, s44\n" \
"s_addk_i32 s44, 0x400\n" \
"buffer_load_dwordx4 v229, s[64:67], s45 offen offset:0 lds\n" \
"s_add_u32 s45, s45, 229376\n" \
"v_readlane_b32 s64, %[tlo], 8\n" \
"v_readlane_b32 s65, %[thi], 8\n" \
"v_mfma_f32_16x16x32_bf16 v[128:131], a[100:103], a[4:7], v[128:131]\n" \
"v_mbcnt_lo_u32_b32 v231, -1, 0\n" \
"v_mfma_f32_16x16x32_bf16 v[128:131], a[104:107], a[8:11], v[128:131]\n" \
"v_exp_f32_e64 v157, v157\n" \
"v_mfma_f32_16x16x32_bf16 v[128:131], a[108:111], a[12:15], v[128:131]\n" \
"v_cvt_pk_bf16_f32 v220, v152, v153\n" \
"v_cvt_pk_bf16_f32 v221, v154, v155\n" \
"v_mfma_f32_16x16x32_bf16 v[132:135], a[124:127], a[12:15], v[160:163]\n" \
"v_exp_f32_e64 v158, v158\n" \
"v_mfma_f32_16x16x32_bf16 v[132:135], a[120:123], a[8:11], v[132:135]\n" \
"v_mbcnt_hi_u32_b32 v231, -1, v231\n" \
"s_waitcnt lgkmcnt(0)\n" \
"v_mfma_f32_16x16x32_bf16 v[132:135], a[116:119], a[4:7], v[132:135]\n" \
"s_add_u32 m0, %[m0b], 65536\n" \
"s_mov_b32 s61, 0\n" \
"buffer_load_dwordx4 %[vg0], s[64:67], s61 offen offset:0 lds\n" \
"v_mfma_f32_16x16x32_bf16 v[132:135], a[112:115], a[0:3], v[132:135]\n" \
"v_exp_f32_e64 v159, v159\n" \
"v_mfma_f32_16x16x32_bf16 v[32:35], a[160:163], v[196:199], v[32:35]\n" \
"ds_read_b128 a[64:67], %[kb0] offset:32768\n" \
"v_and_b32_e64 v226, 31, v231\n" \
"v_mfma_f32_16x16x32_bf16 v[32:35], a[128:131], v[188:191], v[32:35]\n" \
"v_exp_f32_e64 v128, v128\n" \
"v_mfma_f32_16x16x32_bf16 v[36:39], a[164:167], v[196:199], v[36:39]\n" \
"v_cvt_pk_bf16_f32 v222, v156, v157\n" \
"v_cvt_pk_bf16_f32 v223, v158, v159\n" \
"v_mfma_f32_16x16x32_bf16 v[36:39], a[132:135], v[188:191], v[36:39]\n" \
"v_exp_f32_e64 v129, v129\n" \
"v_mfma_f32_16x16x32_bf16 v[40:43], a[168:171], v[196:199], v[40:43]\n" \
"ds_read_b128 a[68:71], v208 offset:32768\n" \
"v_lshlrev_b32_e64 v226, 2, v226\n" \
"v_mfma_f32_16x16x32_bf16 v[40:43], a[136:139], v[188:191], v[40:43]\n" \
"v_exp_f32_e64 v130, v130\n" \
"v_mfma_f32_16x16x32_bf16 v[44:47], a[172:175], v[196:199], v[44:47]\n" \
"ds_read_b128 a[72:75], v209 offset:32768\n" \
"v_cmp_gt_u32_e32 vcc, 1, v231\n" \
"v_mfma_f32_16x16x32_bf16 v[44:47], a[140:143], v[188:191], v[44:47]\n" \
"v_exp_f32_e32 v131, v131\n" \
"v_smfmac_f32_16x16x64_bf16 v[176:179], a[192:195], v[184:191], v228\n" \
"v_mov_b32_e32 v225, 0x80000000\n" \
"v_mfma_f32_16x16x32_bf16 v[136:139], a[96:99], a[16:19], v[164:167]\n" \
"v_exp_f32_e64 v132, v132\n" \
"v_mfma_f32_16x16x32_bf16 v[136:139], a[100:103], a[20:23], v[136:139]\n" \
"ds_read_b128 a[76:79], v210 offset:32768\n" \
"v_mfma_f32_16x16x32_bf16 v[136:139], a[104:107], a[24:27], v[136:139]\n" \
"v_exp_f32_e64 v133, v133\n" \
"v_mfma_f32_16x16x32_bf16 v[136:139], a[108:111], a[28:31], v[136:139]\n" \
"v_cvt_pk_bf16_f32 v192, v128, v129\n" \
"v_cvt_pk_bf16_f32 v193, v130, v131\n" \
"v_mfma_f32_16x16x32_bf16 v[140:143], a[124:127], a[28:31], v[164:167]\n" \
"v_exp_f32_e64 v134, v134\n" \
"v_mfma_f32_16x16x32_bf16 v[140:143], a[120:123], a[24:27], v[140:143]\n" \
"ds_read_b128 a[92:95], v210 offset:36864\n" \
"v_cndmask_b32_e32 v225, v225, v224, vcc\n" \
"v_mfma_f32_16x16x32_bf16 v[140:143], a[116:119], a[20:23], v[140:143]\n" \
"s_add_u32 m0, %[m0b], 66560\n" \
"s_mov_b32 s61, 229376\n" \
"buffer_load_dwordx4 %[vg0], s[64:67], s61 offen offset:0 lds\n" \
"v_mfma_f32_16x16x32_bf16 v[140:143], a[112:115], a[16:19], v[140:143]\n" \
"v_exp_f32_e32 v135, v135\n" \
"v_mfma_f32_16x16x32_bf16 v[48:51], a[176:179], v[196:199], v[48:51]\n" \
"ds_read_b128 a[88:91], v209 offset:36864\n" \
"v_mfma_f32_16x16x32_bf16 v[48:51], a[144:147], v[188:191], v[48:51]\n" \
"v_exp_f32_e64 v136, v136\n" \
"v_mfma_f32_16x16x32_bf16 v[52:55], a[180:183], v[196:199], v[52:55]\n" \
"v_cvt_pk_bf16_f32 v194, v132, v133\n" \
"v_cvt_pk_bf16_f32 v195, v134, v135\n" \
"v_mfma_f32_16x16x32_bf16 v[52:55], a[148:151], v[188:191], v[52:55]\n" \
"v_exp_f32_e64 v137, v137\n" \
"v_mfma_f32_16x16x32_bf16 v[56:59], a[184:187], v[196:199], v[56:59]\n" \
"v_exp_f32_e64 v138, v138\n" \
"v_mfma_f32_16x16x32_bf16 v[56:59], a[152:155], v[188:191], v[56:59]\n" \
"ds_read_b128 a[84:87], v208 offset:36864\n" \
"v_mfma_f32_16x16x32_bf16 v[60:63], a[188:191], v[196:199], v[60:63]\n" \
"v_exp_f32_e64 v139, v139\n" \
"v_mfma_f32_16x16x32_bf16 v[60:63], a[156:159], v[188:191], v[60:63]\n" \
"v_mfma_f32_16x16x32_bf16 v[144:147], a[96:99], a[32:35], v[168:171]\n" \
"v_exp_f32_e64 v140, v140\n" \
"v_mfma_f32_16x16x32_bf16 v[144:147], a[100:103], a[36:39], v[144:147]\n" \
"ds_read_b128 a[80:83], %[kb0] offset:36864\n" \
"s_sub_u32 s61, s48, 0\n" \
"s_cmp_lt_u32 s61, 5\n" \
"s_cselect_b64 vcc, -1, 0\n" \
"v_mfma_f32_16x16x32_bf16 v[144:147], a[104:107], a[40:43], v[144:147]\n" \
"v_exp_f32_e32 v141, v141\n" \
"v_mfma_f32_16x16x32_bf16 v[144:147], a[108:111], a[44:47], v[144:147]\n" \
"v_cvt_pk_bf16_f32 v196, v136, v137\n" \
"v_cvt_pk_bf16_f32 v197, v138, v139\n" \
"v_mfma_f32_16x16x32_bf16 v[148:151], a[124:127], a[44:47], v[168:171]\n" \
"v_exp_f32_e64 v142, v142\n" \
"v_mfma_f32_16x16x32_bf16 v[148:151], a[120:123], a[40:43], v[148:151]\n" \
"v_readlane_b32 s55, %[tlo], 16\n" \
"v_mfma_f32_16x16x32_bf16 v[148:151], a[116:119], a[36:39], v[148:151]\n" \
"s_add_u32 m0, %[m0b], 67584\n" \
"s_mov_b32 s61, 458752\n" \
"buffer_load_dwordx4 %[vg0], s[64:67], s61 offen offset:0 lds\n" \
"v_mfma_f32_16x16x32_bf16 v[148:151], a[112:115], a[32:35], v[148:151]\n" \
"v_exp_f32_e64 v143, v143\n" \
"v_mfma_f32_16x16x32_bf16 v[96:99], a[160:163], v[204:207], v[96:99]\n" \
"ds_read_b64_tr_b16 a[160:161], %[vvb] offset:24576\n" \
"v_and_b32_e64 v232, 31, v231\n" \
"v_mfma_f32_16x16x32_bf16 v[96:99], a[128:131], v[220:223], v[96:99]\n" \
"v_exp_f32_e64 v144, v144\n" \
"v_mfma_f32_16x16x32_bf16 v[100:103], a[164:167], v[204:207], v[100:103]\n" \
"v_cvt_pk_bf16_f32 v198, v140, v141\n" \
"v_cvt_pk_bf16_f32 v199, v142, v143\n" \
"v_mfma_f32_16x16x32_bf16 v[100:103], a[132:135], v[220:223], v[100:103]\n" \
"v_exp_f32_e64 v145, v145\n" \
"v_mfma_f32_16x16x32_bf16 v[104:107], a[168:171], v[204:207], v[104:107]\n" \
"ds_read_b64_tr_b16 a[162:163], %[vvb] offset:28672\n" \
"ds_read_b64_tr_b16 a[164:165], v211 offset:24576\n" \
"v_lshlrev_b32_e64 v232, 9, v232\n" \
"v_mfma_f32_16x16x32_bf16 v[104:107], a[136:139], v[220:223], v[104:107]\n" \
"v_exp_f32_e64 v146, v146\n" \
"v_mfma_f32_16x16x32_bf16 v[108:111], a[172:175], v[204:207], v[108:111]\n" \
"ds_read_b64_tr_b16 a[166:167], v211 offset:28672\n" \
"v_and_b32_e64 v233, 32, v231\n" \
"v_mfma_f32_16x16x32_bf16 v[108:111], a[140:143], v[220:223], v[108:111]\n" \
"v_exp_f32_e64 v147, v147\n" \
"v_smfmac_f32_16x16x64_bf16 v[180:183], a[192:195], v[216:223], v228\n" \
"ds_read_b64_tr_b16 a[168:169], %[vvb] offset:25088\n" \
"v_mfma_f32_16x16x32_bf16 v[152:155], a[96:99], a[48:51], v[172:175]\n" \
"v_exp_f32_e64 v148, v148\n" \
"v_mfma_f32_16x16x32_bf16 v[152:155], a[100:103], a[52:55], v[152:155]\n" \
"ds_read_b64_tr_b16 a[170:171], %[vvb] offset:29184\n" \
"v_lshl_add_u32 v232, v233, 2, v232\n" \
"v_mfma_f32_16x16x32_bf16 v[152:155], a[104:107], a[56:59], v[152:155]\n" \
"v_exp_f32_e64 v149, v149\n" \
"v_mfma_f32_16x16x32_bf16 v[152:155], a[108:111], a[60:63], v[152:155]\n" \
"v_cvt_pk_bf16_f32 v200, v144, v145\n" \
"v_cvt_pk_bf16_f32 v201, v146, v147\n" \
"v_mfma_f32_16x16x32_bf16 v[156:159], a[124:127], a[60:63], v[172:175]\n" \
"v_exp_f32_e64 v150, v150\n" \
"v_mfma_f32_16x16x32_bf16 v[156:159], a[120:123], a[56:59], v[156:159]\n" \
"ds_read_b64_tr_b16 a[172:173], v211 offset:25088\n" \
"v_mov_b32_e64 v230, 1\n" \
"v_mfma_f32_16x16x32_bf16 v[156:159], a[116:119], a[52:55], v[156:159]\n" \
"s_add_u32 m0, %[m0b], 68608\n" \
"s_mov_b32 s61, 688128\n" \
"buffer_load_dwordx4 %[vg0], s[64:67], s61 offen offset:0 lds\n" \
"v_mfma_f32_16x16x32_bf16 v[156:159], a[112:115], a[48:51], v[156:159]\n" \
"v_exp_f32_e64 v151, v151\n" \
"v_mfma_f32_16x16x32_bf16 v[112:115], a[176:179], v[204:207], v[112:115]\n" \
"ds_read_b64_tr_b16 a[174:175], v211 offset:29184\n" \
"ds_read_b64_tr_b16 a[176:177], %[vvb] offset:25600\n" \
"v_mfma_f32_16x16x32_bf16 v[112:115], a[144:147], v[220:223], v[112:115]\n" \
"v_exp_f32_e64 v152, v152\n" \
"v_mfma_f32_16x16x32_bf16 v[116:119], a[180:183], v[204:207], v[116:119]\n" \
"v_cvt_pk_bf16_f32 v202, v148, v149\n" \
"v_cvt_pk_bf16_f32 v203, v150, v151\n" \
"v_mfma_f32_16x16x32_bf16 v[116:119], a[148:151], v[220:223], v[116:119]\n" \
"v_exp_f32_e64 v153, v153\n" \
"v_mfma_f32_16x16x32_bf16 v[120:123], a[184:187], v[204:207], v[120:123]\n" \
"v_exp_f32_e64 v154, v154\n" \
"v_mfma_f32_16x16x32_bf16 v[120:123], a[152:155], v[220:223], v[120:123]\n" \
"ds_read_b64_tr_b16 a[178:179], %[vvb] offset:29696\n" \
"buffer_store_dword v230, v232, %[srdz], s55 offen sc1\n" \
"v_mfma_f32_16x16x32_bf16 v[124:127], a[188:191], v[204:207], v[124:127]\n" \
"v_exp_f32_e64 v155, v155\n" \
"v_mfma_f32_16x16x32_bf16 v[124:127], a[156:159], v[220:223], v[124:127]\n" \
"ds_read_b64_tr_b16 a[180:181], v211 offset:25600\n" \
"s_waitcnt lgkmcnt(13)\n" \
"v_mfma_f32_16x16x32_bf16 v[128:131], a[64:67], a[0:3], v[160:163]\n" \
"v_exp_f32_e32 v156, v156\n" \
"v_mfma_f32_16x16x32_bf16 v[128:131], a[68:71], a[4:7], v[128:131]\n" \
"ds_read_b64_tr_b16 a[182:183], v211 offset:29696\n" \
"s_add_u32 m0, %[m0b], 16384\n" \
"s_mov_b32 s49, s48\n" \
"v_mfma_f32_16x16x32_bf16 v[128:131], a[72:75], a[8:11], v[128:131]\n" \
"v_exp_f32_e32 v157, v157\n" \
"v_mfma_f32_16x16x32_bf16 v[128:131], a[76:79], a[12:15], v[128:131]\n" \
"v_cvt_pk_bf16_f32 v204, v152, v153\n" \
"v_cvt_pk_bf16_f32 v205, v154, v155\n" \
"v_mfma_f32_16x16x32_bf16 v[132:135], a[92:95], a[12:15], v[160:163]\n" \
"v_exp_f32_e64 v158, v158\n" \
"v_mfma_f32_16x16x32_bf16 v[132:135], a[88:91], a[8:11], v[132:135]\n" \
"ds_read_b64_tr_b16 a[184:185], %[vvb] offset:26112\n" \
"s_lshl_b32 s49, s49, 18\n" \
"s_or_b32 s49, s49, s59\n" \
"s_waitcnt lgkmcnt(5)\n" \
"v_mfma_f32_16x16x32_bf16 v[132:135], a[84:87], a[4:7], v[132:135]\n" \
"buffer_load_dwordx4 %[wo0], %[srdt], s49 offen offset:0 lds\n" \
"v_mfma_f32_16x16x32_bf16 v[132:135], a[80:83], a[0:3], v[132:135]\n" \
"v_exp_f32_e32 v159, v159\n" \
"v_mfma_f32_16x16x32_bf16 v[64:67], a[128:131], v[216:219], v[64:67]\n" \
"ds_read_b64_tr_b16 a[186:187], %[vvb] offset:30208\n" \
"ds_read_b64_tr_b16 a[188:189], v211 offset:26112\n" \
"v_xor_b32_e32 v232, 0x80, %[vx0]\n" \
"v_mfma_f32_16x16x32_bf16 v[64:67], a[160:163], v[200:203], v[64:67]\n" \
"v_exp_f32_e64 v128, v128\n" \
"v_mfma_f32_16x16x32_bf16 v[68:71], a[132:135], v[216:219], v[68:71]\n" \
"v_cvt_pk_bf16_f32 v206, v156, v157\n" \
"v_cvt_pk_bf16_f32 v207, v158, v159\n" \
"v_mfma_f32_16x16x32_bf16 v[68:71], a[164:167], v[200:203], v[68:71]\n" \
"v_exp_f32_e64 v129, v129\n" \
"v_mfma_f32_16x16x32_bf16 v[72:75], a[136:139], v[216:219], v[72:75]\n" \
"ds_read_b64_tr_b16 a[190:191], v211 offset:30208\n" \
"ds_read_b64_tr_b16 a[128:129], %[vvb] offset:32768\n" \
"v_mfma_f32_16x16x32_bf16 v[72:75], a[168:171], v[200:203], v[72:75]\n" \
"v_exp_f32_e64 v130, v130\n" \
"v_mfma_f32_16x16x32_bf16 v[76:79], a[140:143], v[216:219], v[76:79]\n" \
"ds_read_b64_tr_b16 a[130:131], %[vvb] offset:36864\n" \
"ds_read_b64_tr_b16 a[132:133], v211 offset:32768\n" \
"v_mfma_f32_16x16x32_bf16 v[76:79], a[172:175], v[200:203], v[76:79]\n" \
"v_exp_f32_e64 v131, v131\n" \
"v_smfmac_f32_16x16x64_bf16 v[176:179], a[192:195], v[192:199], v228\n" \
"ds_read_b64_tr_b16 a[134:135], v211 offset:36864\n" \
"ds_read_b64_tr_b16 a[136:137], %[vvb] offset:33280\n" \
"global_load_dwordx2 a[240:241], %[vx0], %[px] offset:0\n" \
"v_mfma_f32_16x16x32_bf16 v[136:139], a[64:67], a[16:19], v[164:167]\n" \
"v_exp_f32_e64 v132, v132\n" \
"v_mfma_f32_16x16x32_bf16 v[136:139], a[68:71], a[20:23], v[136:139]\n" \
"ds_read_b64_tr_b16 a[138:139], %[vvb] offset:37376\n" \
"ds_read_b64_tr_b16 a[140:141], v211 offset:33280\n" \
"v_mfma_f32_16x16x32_bf16 v[136:139], a[72:75], a[24:27], v[136:139]\n" \
"v_exp_f32_e64 v133, v133\n" \
"v_mfma_f32_16x16x32_bf16 v[136:139], a[76:79], a[28:31], v[136:139]\n" \
"v_cvt_pk_bf16_f32 v184, v128, v129\n" \
"v_cvt_pk_bf16_f32 v185, v130, v131\n" \
"v_mfma_f32_16x16x32_bf16 v[140:143], a[92:95], a[28:31], v[164:167]\n" \
"v_exp_f32_e64 v134, v134\n" \
"v_mfma_f32_16x16x32_bf16 v[140:143], a[88:91], a[24:27], v[140:143]\n" \
"ds_read_b64_tr_b16 a[142:143], v211 offset:37376\n" \
"s_waitcnt lgkmcnt(8)\n" \
"v_mfma_f32_16x16x32_bf16 v[140:143], a[84:87], a[20:23], v[140:143]\n" \
"buffer_load_dwordx4 v212, %[srdt], s49 offen offset:1024 lds\n" \
"v_mfma_f32_16x16x32_bf16 v[140:143], a[80:83], a[16:19], v[140:143]\n" \
"v_exp_f32_e32 v135, v135\n" \
"v_mfma_f32_16x16x32_bf16 v[80:83], a[144:147], v[216:219], v[80:83]\n" \
"ds_read_b64_tr_b16 a[144:145], %[vvb] offset:33792\n" \
"ds_read_b64_tr_b16 a[146:147], %[vvb] offset:37888\n" \
"v_mfma_f32_16x16x32_bf16 v[80:83], a[176:179], v[200:203], v[80:83]\n" \
"v_exp_f32_e64 v136, v136\n" \
"v_mfma_f32_16x16x32_bf16 v[84:87], a[148:151], v[216:219], v[84:87]\n" \
"v_cvt_pk_bf16_f32 v186, v132, v133\n" \
"v_cvt_pk_bf16_f32 v187, v134, v135\n" \
"v_mfma_f32_16x16x32_bf16 v[84:87], a[180:183], v[200:203], v[84:87]\n" \
"v_exp_f32_e64 v137, v137\n" \
"v_mfma_f32_16x16x32_bf16 v[88:91], a[152:155], v[216:219], v[88:91]\n" \
"v_exp_f32_e64 v138, v138\n" \
"v_mfma_f32_16x16x32_bf16 v[88:91], a[184:187], v[200:203], v[88:91]\n" \
"ds_read_b64_tr_b16 a[148:149], v211 offset:33792\n" \
"ds_read_b64_tr_b16 a[150:151], v211 offset:37888\n" \
"v_mfma_f32_16x16x32_bf16 v[92:95], a[156:159], v[216:219], v[92:95]\n" \
"v_exp_f32_e64 v139, v139\n" \
"v_mfma_f32_16x16x32_bf16 v[92:95], a[188:191], v[200:203], v[92:95]\n" \
"ds_read_b64_tr_b16 a[152:153], %[vvb] offset:34304\n" \
"global_load_dwordx2 a[242:243], %[vx0], %[px] offset:1024\n" \
"v_mfma_f32_16x16x32_bf16 v[144:147], a[64:67], a[32:35], v[168:171]\n" \
"v_exp_f32_e64 v140, v140\n" \
"v_mfma_f32_16x16x32_bf16 v[144:147], a[68:71], a[36:39], v[144:147]\n" \
"ds_read_b64_tr_b16 a[154:155], %[vvb] offset:38400\n" \
"ds_read_b64_tr_b16 a[156:157], v211 offset:34304\n" \
"v_mfma_f32_16x16x32_bf16 v[144:147], a[72:75], a[40:43], v[144:147]\n" \
"v_exp_f32_e64 v141, v141\n" \
"v_mfma_f32_16x16x32_bf16 v[144:147], a[76:79], a[44:47], v[144:147]\n" \
"v_cvt_pk_bf16_f32 v188, v136, v137\n" \
"v_cvt_pk_bf16_f32 v189, v138, v139\n" \
"v_mfma_f32_16x16x32_bf16 v[148:151], a[92:95], a[44:47], v[168:171]\n" \
"v_exp_f32_e64 v142, v142\n" \
"v_mfma_f32_16x16x32_bf16 v[148:151], a[88:91], a[40:43], v[148:151]\n" \
"ds_read_b64_tr_b16 a[158:159], v211 offset:38400\n" \
"s_waitcnt lgkmcnt(8)\n" \
"v_mfma_f32_16x16x32_bf16 v[148:151], a[84:87], a[36:39], v[148:151]\n" \
"buffer_load_dwordx4 v213, %[srdt], s49 offen offset:2048 lds\n" \
"v_mfma_f32_16x16x32_bf16 v[148:151], a[80:83], a[32:35], v[148:151]\n" \
"v_exp_f32_e32 v143, v143\n" \
"v_mfma_f32_16x16x32_bf16 v[0:3], a[160:163], v[192:195], v[0:3]\n" \
"ds_read_b128 a[96:99], %[kb0] offset:40960\n" \
"v_mfma_f32_16x16x32_bf16 v[0:3], a[128:131], v[184:187], v[0:3]\n" \
"v_exp_f32_e64 v144, v144\n" \
"v_mfma_f32_16x16x32_bf16 v[4:7], a[164:167], v[192:195], v[4:7]\n" \
"v_cvt_pk_bf16_f32 v190, v140, v141\n" \
"v_cvt_pk_bf16_f32 v191, v142, v143\n" \
"v_mfma_f32_16x16x32_bf16 v[4:7], a[132:135], v[184:187], v[4:7]\n" \
"v_exp_f32_e64 v145, v145\n" \
"v_mfma_f32_16x16x32_bf16 v[8:11], a[168:171], v[192:195], v[8:11]\n" \
"ds_read_b128 a[100:103], v208 offset:40960\n" \
"v_mfma_f32_16x16x32_bf16 v[8:11], a[136:139], v[184:187], v[8:11]\n" \
"v_exp_f32_e64 v146, v146\n" \
"v_mfma_f32_16x16x32_bf16 v[12:15], a[172:175], v[192:195], v[12:15]\n" \
"ds_read_b128 a[104:107], v209 offset:40960\n" \
"v_mfma_f32_16x16x32_bf16 v[12:15], a[140:143], v[184:187], v[12:15]\n" \
"v_exp_f32_e64 v147, v147\n" \
"v_smfmac_f32_16x16x64_bf16 v[180:183], a[192:195], v[200:207], v228\n" \
"ds_read_b128 a[108:111], v210 offset:40960\n" \
"global_load_dwordx2 a[248:249], v232, %[px] offset:0\n" \
"v_mfma_f32_16x16x32_bf16 v[152:155], a[64:67], a[48:51], v[172:175]\n" \
"v_exp_f32_e64 v148, v148\n" \
"v_mfma_f32_16x16x32_bf16 v[152:155], a[68:71], a[52:55], v[152:155]\n" \
"ds_read_b128 a[124:127], v210 offset:45056\n" \
"v_mfma_f32_16x16x32_bf16 v[152:155], a[72:75], a[56:59], v[152:155]\n" \
"v_exp_f32_e64 v149, v149\n" \
"v_mfma_f32_16x16x32_bf16 v[152:155], a[76:79], a[60:63], v[152:155]\n" \
"v_cvt_pk_bf16_f32 v216, v144, v145\n" \
"v_cvt_pk_bf16_f32 v217, v146, v147\n" \
"v_mfma_f32_16x16x32_bf16 v[156:159], a[92:95], a[60:63], v[172:175]\n" \
"v_exp_f32_e64 v150, v150\n" \
"v_mfma_f32_16x16x32_bf16 v[156:159], a[88:91], a[56:59], v[156:159]\n" \
"ds_read_b128 a[120:123], v209 offset:45056\n" \
"s_waitcnt lgkmcnt(6)\n" \
"v_mfma_f32_16x16x32_bf16 v[156:159], a[84:87], a[52:55], v[156:159]\n" \
"buffer_load_dwordx4 v214, %[srdt], s49 offen offset:3072 lds\n" \
"v_mfma_f32_16x16x32_bf16 v[156:159], a[80:83], a[48:51], v[156:159]\n" \
"v_exp_f32_e32 v151, v151\n" \
"v_mfma_f32_16x16x32_bf16 v[16:19], a[176:179], v[192:195], v[16:19]\n" \
"ds_read_b128 a[116:119], v208 offset:45056\n" \
"v_mfma_f32_16x16x32_bf16 v[16:19], a[144:147], v[184:187], v[16:19]\n" \
"v_exp_f32_e64 v152, v152\n" \
"v_mfma_f32_16x16x32_bf16 v[20:23], a[180:183], v[192:195], v[20:23]\n" \
"v_cvt_pk_bf16_f32 v218, v148, v149\n" \
"v_cvt_pk_bf16_f32 v219, v150, v151\n" \
"v_mfma_f32_16x16x32_bf16 v[20:23], a[148:151], v[184:187], v[20:23]\n" \
"v_exp_f32_e64 v153, v153\n" \
"v_mfma_f32_16x16x32_bf16 v[24:27], a[184:187], v[192:195], v[24:27]\n" \
"v_exp_f32_e64 v154, v154\n" \
"v_mfma_f32_16x16x32_bf16 v[24:27], a[152:155], v[184:187], v[24:27]\n" \
"ds_read_b128 a[112:115], %[kb0] offset:45056\n" \
"global_load_dwordx2 a[250:251], v232, %[px] offset:1024\n" \
"v_mfma_f32_16x16x32_bf16 v[28:31], a[188:191], v[192:195], v[28:31]\n" \
"v_exp_f32_e64 v155, v155\n" \
"v_mfma_f32_16x16x32_bf16 v[28:31], a[156:159], v[184:187], v[28:31]\n" \
"s_waitcnt vmcnt(63) lgkmcnt(2)\n" \
"s_barrier\n" \
"v_mfma_f32_16x16x32_bf16 v[128:131], a[96:99], a[0:3], v[160:163]\n" \
"v_exp_f32_e64 v156, v156\n" \
"v_mfma_f32_16x16x32_bf16 v[128:131], a[100:103], a[4:7], v[128:131]\n" \
"v_lshrrev_b32_e64 v227, 5, v231\n" \
"v_mfma_f32_16x16x32_bf16 v[128:131], a[104:107], a[8:11], v[128:131]\n" \
"v_exp_f32_e64 v157, v157\n" \
"v_mfma_f32_16x16x32_bf16 v[128:131], a[108:111], a[12:15], v[128:131]\n" \
"v_cvt_pk_bf16_f32 v220, v152, v153\n" \
"v_cvt_pk_bf16_f32 v221, v154, v155\n" \
"v_mfma_f32_16x16x32_bf16 v[132:135], a[124:127], a[12:15], v[160:163]\n" \
"v_exp_f32_e64 v158, v158\n" \
"v_mfma_f32_16x16x32_bf16 v[132:135], a[120:123], a[8:11], v[132:135]\n" \
"v_lshlrev_b32_e32 v227, 10, v227\n" \
"s_waitcnt lgkmcnt(0)\n" \
"v_mfma_f32_16x16x32_bf16 v[132:135], a[116:119], a[4:7], v[132:135]\n" \
"s_add_u32 m0, %[m0b], 81920\n" \
"s_mov_b32 s61, 917504\n" \
"buffer_load_dwordx4 %[vg0], s[64:67], s61 offen offset:0 lds\n" \
"v_mfma_f32_16x16x32_bf16 v[132:135], a[112:115], a[0:3], v[132:135]\n" \
"v_exp_f32_e64 v159, v159\n" \
"v_mfma_f32_16x16x32_bf16 v[32:35], a[160:163], v[196:199], v[32:35]\n" \
"ds_read_b128 a[64:67], %[kb0] offset:49152\n" \
"v_add_u32_e32 v227, 0x10000, v227\n" \
"v_mfma_f32_16x16x32_bf16 v[32:35], a[128:131], v[188:191], v[32:35]\n" \
"v_exp_f32_e64 v128, v128\n" \
"v_mfma_f32_16x16x32_bf16 v[36:39], a[164:167], v[196:199], v[36:39]\n" \
"v_cvt_pk_bf16_f32 v222, v156, v157\n" \
"v_cvt_pk_bf16_f32 v223, v158, v159\n" \
"v_mfma_f32_16x16x32_bf16 v[36:39], a[132:135], v[188:191], v[36:39]\n" \
"v_exp_f32_e64 v129, v129\n" \
"v_mfma_f32_16x16x32_bf16 v[40:43], a[168:171], v[196:199], v[40:43]\n" \
"ds_read_b128 a[68:71], v208 offset:49152\n" \
"v_sub_u32_e64 v215, %[vvb], v227\n" \
"v_mfma_f32_16x16x32_bf16 v[40:43], a[136:139], v[188:191], v[40:43]\n" \
"v_exp_f32_e64 v130, v130\n" \
"v_mfma_f32_16x16x32_bf16 v[44:47], a[172:175], v[196:199], v[44:47]\n" \
"ds_read_b128 a[72:75], v209 offset:49152\n" \
"v_xor_b32_e64 v227, 32, v215\n" \
"v_mfma_f32_16x16x32_bf16 v[44:47], a[140:143], v[188:191], v[44:47]\n" \
"v_exp_f32_e64 v131, v131\n" \
"v_smfmac_f32_16x16x64_bf16 v[176:179], a[192:195], v[184:191], v228\n" \
"global_load_dwordx2 a[244:245], %[vx0], %[px] offset:64\n" \
"v_mfma_f32_16x16x32_bf16 v[136:139], a[96:99], a[16:19], v[164:167]\n" \
"v_exp_f32_e64 v132, v132\n" \
"v_mfma_f32_16x16x32_bf16 v[136:139], a[100:103], a[20:23], v[136:139]\n" \
"ds_read_b128 a[76:79], v210 offset:49152\n" \
"v_mfma_f32_16x16x32_bf16 v[136:139], a[104:107], a[24:27], v[136:139]\n" \
"v_exp_f32_e64 v133, v133\n" \
"v_mfma_f32_16x16x32_bf16 v[136:139], a[108:111], a[28:31], v[136:139]\n" \
"v_cvt_pk_bf16_f32 v192, v128, v129\n" \
"v_cvt_pk_bf16_f32 v193, v130, v131\n" \
"v_mfma_f32_16x16x32_bf16 v[140:143], a[124:127], a[28:31], v[164:167]\n" \
"v_exp_f32_e64 v134, v134\n" \
"v_mfma_f32_16x16x32_bf16 v[140:143], a[120:123], a[24:27], v[140:143]\n" \
"ds_read_b128 a[92:95], v210 offset:53248\n" \
"v_mfma_f32_16x16x32_bf16 v[140:143], a[116:119], a[20:23], v[140:143]\n" \
"s_add_u32 m0, %[m0b], 82944\n" \
"s_mov_b32 s61, 1146880\n" \
"buffer_load_dwordx4 %[vg0], s[64:67], s61 offen offset:0 lds\n" \
"v_mfma_f32_16x16x32_bf16 v[140:143], a[112:115], a[16:19], v[140:143]\n" \
"v_exp_f32_e64 v135, v135\n" \
"v_mfma_f32_16x16x32_bf16 v[48:51], a[176:179], v[196:199], v[48:51]\n" \
"ds_read_b128 a[88:91], v209 offset:53248\n" \
"v_mfma_f32_16x16x32_bf16 v[48:51], a[144:147], v[188:191], v[48:51]\n" \
"v_exp_f32_e64 v136, v136\n" \
"v_mfma_f32_16x16x32_bf16 v[52:55], a[180:183], v[196:199], v[52:55]\n" \
"v_cvt_pk_bf16_f32 v194, v132, v133\n" \
"v_cvt_pk_bf16_f32 v195, v134, v135\n" \
"v_mfma_f32_16x16x32_bf16 v[52:55], a[148:151], v[188:191], v[52:55]\n" \
"v_exp_f32_e64 v137, v137\n" \
"v_mfma_f32_16x16x32_bf16 v[56:59], a[184:187], v[196:199], v[56:59]\n" \
"v_exp_f32_e64 v138, v138\n" \
"v_mfma_f32_16x16x32_bf16 v[56:59], a[152:155], v[188:191], v[56:59]\n" \
"ds_read_b128 a[84:87], v208 offset:53248\n" \
"v_mfma_f32_16x16x32_bf16 v[60:63], a[188:191], v[196:199], v[60:63]\n" \
"v_exp_f32_e64 v139, v139\n" \
"v_mfma_f32_16x16x32_bf16 v[60:63], a[156:159], v[188:191], v[60:63]\n" \
"global_load_dwordx2 a[246:247], %[vx0], %[px] offset:1088\n" \
"v_mfma_f32_16x16x32_bf16 v[144:147], a[96:99], a[32:35], v[168:171]\n" \
"v_exp_f32_e64 v140, v140\n" \
"v_mfma_f32_16x16x32_bf16 v[144:147], a[100:103], a[36:39], v[144:147]\n" \
"ds_read_b128 a[80:83], %[kb0] offset:53248\n" \
"v_mfma_f32_16x16x32_bf16 v[144:147], a[104:107], a[40:43], v[144:147]\n" \
"v_exp_f32_e64 v141, v141\n" \
"v_mfma_f32_16x16x32_bf16 v[144:147], a[108:111], a[44:47], v[144:147]\n" \
"v_cvt_pk_bf16_f32 v196, v136, v137\n" \
"v_cvt_pk_bf16_f32 v197, v138, v139\n" \
"v_mfma_f32_16x16x32_bf16 v[148:151], a[124:127], a[44:47], v[168:171]\n" \
"v_exp_f32_e64 v142, v142\n" \
"v_mfma_f32_16x16x32_bf16 v[148:151], a[120:123], a[40:43], v[148:151]\n" \
"v_mfma_f32_16x16x32_bf16 v[148:151], a[116:119], a[36:39], v[148:151]\n" \
"s_add_u32 m0, %[m0b], 83968\n" \
"s_mov_b32 s61, 1376256\n" \
"buffer_load_dwordx4 %[vg0], s[64:67], s61 offen offset:0 lds\n" \
"v_mfma_f32_16x16x32_bf16 v[148:151], a[112:115], a[32:35], v[148:151]\n" \
"v_exp_f32_e64 v143, v143\n" \
"v_mfma_f32_16x16x32_bf16 v[96:99], a[160:163], v[204:207], v[96:99]\n" \
"ds_read_b64_tr_b16 a[160:161], %[vvb] offset:40960\n" \
"v_mfma_f32_16x16x32_bf16 v[96:99], a[128:131], v[220:223], v[96:99]\n" \
"v_exp_f32_e64 v144, v144\n" \
"v_mfma_f32_16x16x32_bf16 v[100:103], a[164:167], v[204:207], v[100:103]\n" \
"v_cvt_pk_bf16_f32 v198, v140, v141\n" \
"v_cvt_pk_bf16_f32 v199, v142, v143\n" \
"v_mfma_f32_16x16x32_bf16 v[100:103], a[132:135], v[220:223], v[100:103]\n" \
"v_exp_f32_e64 v145, v145\n" \
"v_mfma_f32_16x16x32_bf16 v[104:107], a[168:171], v[204:207], v[104:107]\n" \
"ds_read_b64_tr_b16 a[162:163], %[vvb] offset:45056\n" \
"ds_read_b64_tr_b16 a[164:165], v211 offset:40960\n" \
"v_mfma_f32_16x16x32_bf16 v[104:107], a[136:139], v[220:223], v[104:107]\n" \
"v_exp_f32_e64 v146, v146\n" \
"v_mfma_f32_16x16x32_bf16 v[108:111], a[172:175], v[204:207], v[108:111]\n" \
"ds_read_b64_tr_b16 a[166:167], v211 offset:45056\n" \
"v_mfma_f32_16x16x32_bf16 v[108:111], a[140:143], v[220:223], v[108:111]\n" \
"v_exp_f32_e64 v147, v147\n" \
"v_smfmac_f32_16x16x64_bf16 v[180:183], a[192:195], v[216:223], v228\n" \
"ds_read_b64_tr_b16 a[168:169], %[vvb] offset:41472\n" \
"global_load_dwordx2 a[252:253], v232, %[px] offset:64\n" \
"v_mfma_f32_16x16x32_bf16 v[152:155], a[96:99], a[48:51], v[172:175]\n" \
"v_exp_f32_e64 v148, v148\n" \
"v_mfma_f32_16x16x32_bf16 v[152:155], a[100:103], a[52:55], v[152:155]\n" \
"ds_read_b64_tr_b16 a[170:171], %[vvb] offset:45568\n" \
"v_mfma_f32_16x16x32_bf16 v[152:155], a[104:107], a[56:59], v[152:155]\n" \
"v_exp_f32_e64 v149, v149\n" \
"v_mfma_f32_16x16x32_bf16 v[152:155], a[108:111], a[60:63], v[152:155]\n" \
"v_cvt_pk_bf16_f32 v200, v144, v145\n" \
"v_cvt_pk_bf16_f32 v201, v146, v147\n" \
"v_mfma_f32_16x16x32_bf16 v[156:159], a[124:127], a[60:63], v[172:175]\n" \
"v_exp_f32_e64 v150, v150\n" \
"v_mfma_f32_16x16x32_bf16 v[156:159], a[120:123], a[56:59], v[156:159]\n" \
"ds_read_b64_tr_b16 a[172:173], v211 offset:41472\n" \
"v_mfma_f32_16x16x32_bf16 v[156:159], a[116:119], a[52:55], v[156:159]\n" \
"s_add_u32 m0, %[m0b], 84992\n" \
"s_mov_b32 s61, 1605632\n" \
"buffer_load_dwordx4 %[vg0], s[64:67], s61 offen offset:0 lds\n" \
"v_mfma_f32_16x16x32_bf16 v[156:159], a[112:115], a[48:51], v[156:159]\n" \
"v_exp_f32_e64 v151, v151\n" \
"v_mfma_f32_16x16x32_bf16 v[112:115], a[176:179], v[204:207], v[112:115]\n" \
"ds_read_b64_tr_b16 a[174:175], v211 offset:45568\n" \
"ds_read_b64_tr_b16 a[176:177], %[vvb] offset:41984\n" \
"v_mfma_f32_16x16x32_bf16 v[112:115], a[144:147], v[220:223], v[112:115]\n" \
"v_exp_f32_e64 v152, v152\n" \
"v_mfma_f32_16x16x32_bf16 v[116:119], a[180:183], v[204:207], v[116:119]\n" \
"v_cvt_pk_bf16_f32 v202, v148, v149\n" \
"v_cvt_pk_bf16_f32 v203, v150, v151\n" \
"v_mfma_f32_16x16x32_bf16 v[116:119], a[148:151], v[220:223], v[116:119]\n" \
"v_exp_f32_e64 v153, v153\n" \
"v_mfma_f32_16x16x32_bf16 v[120:123], a[184:187], v[204:207], v[120:123]\n" \
"v_exp_f32_e64 v154, v154\n" \
"v_mfma_f32_16x16x32_bf16 v[120:123], a[152:155], v[220:223], v[120:123]\n" \
"ds_read_b64_tr_b16 a[178:179], %[vvb] offset:46080\n" \
"global_load_dwordx2 a[254:255], v232, %[px] offset:1088\n" \
"v_mfma_f32_16x16x32_bf16 v[124:127], a[188:191], v[204:207], v[124:127]\n" \
"v_exp_f32_e64 v155, v155\n" \
"v_mfma_f32_16x16x32_bf16 v[124:127], a[156:159], v[220:223], v[124:127]\n" \
"ds_read_b64_tr_b16 a[180:181], v211 offset:41984\n" \
"s_waitcnt lgkmcnt(13)\n" \
"v_mfma_f32_16x16x32_bf16 v[128:131], a[64:67], a[0:3], v[160:163]\n" \
"v_exp_f32_e32 v156, v156\n" \
"v_mfma_f32_16x16x32_bf16 v[128:131], a[68:71], a[4:7], v[128:131]\n" \
"ds_read_b64_tr_b16 a[182:183], v211 offset:46080\n" \
"s_add_u32 m0, %[m0b], 32768\n" \
"s_cmp_gt_u32 s48, 0\n" \
"v_mfma_f32_16x16x32_bf16 v[128:131], a[72:75], a[8:11], v[128:131]\n" \
"v_exp_f32_e32 v157, v157\n" \
"v_mfma_f32_16x16x32_bf16 v[128:131], a[76:79], a[12:15], v[128:131]\n" \
"v_cvt_pk_bf16_f32 v204, v152, v153\n" \
"v_cvt_pk_bf16_f32 v205, v154, v155\n" \
"v_mfma_f32_16x16x32_bf16 v[132:135], a[92:95], a[12:15], v[160:163]\n" \
"v_exp_f32_e64 v158, v158\n" \
"v_mfma_f32_16x16x32_bf16 v[132:135], a[88:91], a[8:11], v[132:135]\n" \
"ds_read_b64_tr_b16 a[184:185], %[vvb] offset:42496\n" \
"s_cselect_b32 s49, 0, 1\n" \
"s_lshl_b32 s49, s49, 18\n" \
"s_or_b32 s49, s49, s58\n" \
"s_waitcnt lgkmcnt(5)\n" \
"v_mfma_f32_16x16x32_bf16 v[132:135], a[84:87], a[4:7], v[132:135]\n" \
".if . - adaln_body != 36864\n" \
".error \"size model\"\n" \
".endif\n" \
"Lk8_%=:\n" \
"s_cbranch_execz Lk9_%=\n" \
"s_nop 0\n" \
"buffer_load_dwordx4 %[wo0], %[srdt], s49 offen offset:0 lds\n" \
"v_mfma_f32_16x16x32_bf16 v[132:135], a[80:83], a[0:3], v[132:135]\n" \
"v_exp_f32_e64 v159, v159\n" \
"v_mfma_f32_16x16x32_bf16 v[64:67], a[128:131], v[216:219], v[64:67]\n" \
"ds_read_b64_tr_b16 a[186:187], %[vvb] offset:46592\n" \
"ds_read_b64_tr_b16 a[188:189], v211 offset:42496\n" \
"v_mfma_f32_16x16x32_bf16 v[64:67], a[160:163], v[200:203], v[64:67]\n" \
"v_exp_f32_e64 v128, v128\n" \
"v_mfma_f32_16x16x32_bf16 v[68:71], a[132:135], v[216:219], v[68:71]\n" \
"v_cvt_pk_bf16_f32 v206, v156, v157\n" \
"v_cvt_pk_bf16_f32 v207, v158, v159\n" \
"v_mfma_f32_16x16x32_bf16 v[68:71], a[164:167], v[200:203], v[68:71]\n" \
"v_exp_f32_e64 v129, v129\n" \
"v_mfma_f32_16x16x32_bf16 v[72:75], a[136:139], v[216:219], v[72:75]\n" \
"ds_read_b64_tr_b16 a[190:191], v211 offset:46592\n" \
"ds_read_b64_tr_b16 a[128:129], %[vvb] offset:49152\n" \
"v_mfma_f32_16x16x32_bf16 v[72:75], a[168:171], v[200:203], v[72:75]\n" \
"v_exp_f32_e64 v130, v130\n" \
"v_mfma_f32_16x16x32_bf16 v[76:79], a[140:143], v[216:219], v[76:79]\n" \
"ds_read_b64_tr_b16 a[130:131], %[vvb] offset:53248\n" \
"ds_read_b64_tr_b16 a[132:133], v211 offset:49152\n" \
"v_mfma_f32_16x16x32_bf16 v[76:79], a[172:175], v[200:203], v[76:79]\n" \
"v_exp_f32_e64 v131, v131\n" \
"v_smfmac_f32_16x16x64_bf16 v[176:179], a[192:195], v[192:199], v228\n" \
"ds_read_b64_tr_b16 a[134:135], v211 offset:53248\n" \
"ds_read_b64_tr_b16 a[136:137], %[vvb] offset:49664\n" \
"v_mfma_f32_16x16x32_bf16 v[136:139], a[64:67], a[16:19], v[164:167]\n" \
"v_exp_f32_e64 v132, v132\n" \
"v_mfma_f32_16x16x32_bf16 v[136:139], a[68:71], a[20:23], v[136:139]\n" \
"ds_read_b64_tr_b16 a[138:139], %[vvb] offset:53760\n" \
"ds_read_b64_tr_b16 a[140:141], v211 offset:49664\n" \
"v_mfma_f32_16x16x32_bf16 v[136:139], a[72:75], a[24:27], v[136:139]\n" \
"v_exp_f32_e64 v133, v133\n" \
"v_mfma_f32_16x16x32_bf16 v[136:139], a[76:79], a[28:31], v[136:139]\n" \
"v_cvt_pk_bf16_f32 v184, v128, v129\n" \
"v_cvt_pk_bf16_f32 v185, v130, v131\n" \
"v_mfma_f32_16x16x32_bf16 v[140:143], a[92:95], a[28:31], v[164:167]\n" \
"v_exp_f32_e64 v134, v134\n" \
"v_mfma_f32_16x16x32_bf16 v[140:143], a[88:91], a[24:27], v[140:143]\n" \
"ds_read_b64_tr_b16 a[142:143], v211 offset:53760\n" \
"s_waitcnt lgkmcnt(8)\n" \
"v_mfma_f32_16x16x32_bf16 v[140:143], a[84:87], a[20:23], v[140:143]\n" \
"buffer_load_dwordx4 v212, %[srdt], s49 offen offset:1024 lds\n" \
"v_mfma_f32_16x16x32_bf16 v[140:143], a[80:83], a[16:19], v[140:143]\n" \
"v_exp_f32_e32 v135, v135\n" \
"v_mfma_f32_16x16x32_bf16 v[80:83], a[144:147], v[216:219], v[80:83]\n" \
"ds_read_b64_tr_b16 a[144:145], %[vvb] offset:50176\n" \
"ds_read_b64_tr_b16 a[146:147], %[vvb] offset:54272\n" \
"v_mfma_f32_16x16x32_bf16 v[80:83], a[176:179], v[200:203], v[80:83]\n" \
"v_exp_f32_e64 v136, v136\n" \
"v_mfma_f32_16x16x32_bf16 v[84:87], a[148:151], v[216:219], v[84:87]\n" \
"v_cvt_pk_bf16_f32 v186, v132, v133\n" \
"v_cvt_pk_bf16_f32 v187, v134, v135\n" \
"v_mfma_f32_16x16x32_bf16 v[84:87], a[180:183], v[200:203], v[84:87]\n" \
"v_exp_f32_e64 v137, v137\n" \
"v_mfma_f32_16x16x32_bf16 v[88:91], a[152:155], v[216:219], v[88:91]\n" \
"v_exp_f32_e64 v138, v138\n" \
"v_mfma_f32_16x16x32_bf16 v[88:91], a[184:187], v[200:203], v[88:91]\n" \
"ds_read_b64_tr_b16 a[148:149], v211 offset:50176\n" \
"ds_read_b64_tr_b16 a[150:151], v211 offset:54272\n" \
"v_readlane_b32 s64, %[tlo], 10\n" \
"v_mfma_f32_16x16x32_bf16 v[92:95], a[156:159], v[216:219], v[92:95]\n" \
"v_exp_f32_e64 v139, v139\n" \
"v_mfma_f32_16x16x32_bf16 v[92:95], a[188:191], v[200:203], v[92:95]\n" \
"ds_read_b64_tr_b16 a[152:153], %[vvb] offset:50688\n" \
"v_readlane_b32 s65, %[thi], 10\n" \
"v_mfma_f32_16x16x32_bf16 v[144:147], a[64:67], a[32:35], v[168:171]\n" \
"v_exp_f32_e64 v140, v140\n" \
"v_mfma_f32_16x16x32_bf16 v[144:147], a[68:71], a[36:39], v[144:147]\n" \
"ds_read_b64_tr_b16 a[154:155], %[vvb] offset:54784\n" \
"ds_read_b64_tr_b16 a[156:157], v211 offset:50688\n" \
"v_mfma_f32_16x16x32_bf16 v[144:147], a[72:75], a[40:43], v[144:147]\n" \
"v_exp_f32_e64 v141, v141\n" \
"v_mfma_f32_16x16x32_bf16 v[144:147], a[76:79], a[44:47], v[144:147]\n" \
"v_cvt_pk_bf16_f32 v188, v136, v137\n" \
"v_cvt_pk_bf16_f32 v189, v138, v139\n" \
"v_mfma_f32_16x16x32_bf16 v[148:151], a[92:95], a[44:47], v[168:171]\n" \
"v_exp_f32_e64 v142, v142\n" \
"v_mfma_f32_16x16x32_bf16 v[148:151], a[88:91], a[40:43], v[148:151]\n" \
"ds_read_b64_tr_b16 a[158:159], v211 offset:54784\n" \
"s_waitcnt lgkmcnt(8)\n" \
"v_mfma_f32_16x16x32_bf16 v[148:151], a[84:87], a[36:39], v[148:151]\n" \
"buffer_load_dwordx4 v213, %[srdt], s49 offen offset:2048 lds\n" \
"v_max3_f32 v232, v33, v38, v43\n" \
"v_mfma_f32_16x16x32_bf16 v[148:151], a[80:83], a[32:35], v[148:151]\n" \
"v_exp_f32_e32 v143, v143\n" \
"v_mfma_f32_16x16x32_bf16 v[0:3], a[160:163], v[192:195], v[0:3]\n" \
"ds_read_b128 a[96:99], %[kb0] offset:57344\n" \
"v_min3_f32 v233, v33, v38, v43\n" \
"v_mfma_f32_16x16x32_bf16 v[0:3], a[128:131], v[184:187], v[0:3]\n" \
"v_exp_f32_e64 v144, v144\n" \
"v_mfma_f32_16x16x32_bf16 v[4:7], a[164:167], v[192:195], v[4:7]\n" \
"v_cvt_pk_bf16_f32 v190, v140, v141\n" \
"v_cvt_pk_bf16_f32 v191, v142, v143\n" \
"v_mfma_f32_16x16x32_bf16 v[4:7], a[132:135], v[184:187], v[4:7]\n" \
"v_exp_f32_e64 v145, v145\n" \
"v_mfma_f32_16x16x32_bf16 v[8:11], a[168:171], v[192:195], v[8:11]\n" \
"ds_read_b128 a[100:103], v208 offset:57344\n" \
"v_max3_f32 v232, v232, v48, v53\n" \
"v_mfma_f32_16x16x32_bf16 v[8:11], a[136:139], v[184:187], v[8:11]\n" \
"v_exp_f32_e64 v146, v146\n" \
"v_mfma_f32_16x16x32_bf16 v[12:15], a[172:175], v[192:195], v[12:15]\n" \
"ds_read_b128 a[104:107], v209 offset:57344\n" \
"v_min3_f32 v233, v233, v48, v53\n" \
"v_mfma_f32_16x16x32_bf16 v[12:15], a[140:143], v[184:187], v[12:15]\n" \
"v_exp_f32_e64 v147, v147\n" \
"v_smfmac_f32_16x16x64_bf16 v[180:183], a[192:195], v[200:207], v228\n" \
"ds_read_b128 a[108:111], v210 offset:57344\n" \
"buffer_load_dword v229, v226, %[srdz], s50 offen sc1\n" \
"v_mfma_f32_16x16x32_bf16 v[152:155], a[64:67], a[48:51], v[172:175]\n" \
"v_exp_f32_e64 v148, v148\n" \
"v_mfma_f32_16x16x32_bf16 v[152:155], a[68:71], a[52:55], v[152:155]\n" \
"ds_read_b128 a[124:127], v210 offset:61440\n" \
"v_sub_f32_e64 v234, v232, v233\n" \
"v_mfma_f32_16x16x32_bf16 v[152:155], a[72:75], a[56:59], v[152:155]\n" \
"v_exp_f32_e64 v149, v149\n" \
"v_mfma_f32_16x16x32_bf16 v[152:155], a[76:79], a[60:63], v[152:155]\n" \
"v_cvt_pk_bf16_f32 v216, v144, v145\n" \
"v_cvt_pk_bf16_f32 v217, v146, v147\n" \
"v_mfma_f32_16x16x32_bf16 v[156:159], a[92:95], a[60:63], v[172:175]\n" \
"v_exp_f32_e64 v150, v150\n" \
"v_mfma_f32_16x16x32_bf16 v[156:159], a[88:91], a[56:59], v[156:159]\n" \
"ds_read_b128 a[120:123], v209 offset:61440\n" \
"s_waitcnt lgkmcnt(6)\n" \
"v_mfma_f32_16x16x32_bf16 v[156:159], a[84:87], a[52:55], v[156:159]\n" \
"buffer_load_dwordx4 v214, %[srdt], s49 offen offset:3072 lds\n" \
"v_mfma_f32_16x16x32_bf16 v[156:159], a[80:83], a[48:51], v[156:159]\n" \
"v_exp_f32_e32 v151, v151\n" \
"v_mfma_f32_16x16x32_bf16 v[16:19], a[176:179], v[192:195], v[16:19]\n" \
"ds_read_b128 a[116:119], v208 offset:61440\n" \
"v_mfma_f32_16x16x32_bf16 v[16:19], a[144:147], v[184:187], v[16:19]\n" \
"v_exp_f32_e64 v152, v152\n" \
"v_mfma_f32_16x16x32_bf16 v[20:23], a[180:183], v[192:195], v[20:23]\n" \
"v_cvt_pk_bf16_f32 v218, v148, v149\n" \
"v_cvt_pk_bf16_f32 v219, v150, v151\n" \
"v_mfma_f32_16x16x32_bf16 v[20:23], a[148:151], v[184:187], v[20:23]\n" \
"v_exp_f32_e64 v153, v153\n" \
"v_mfma_f32_16x16x32_bf16 v[24:27], a[184:187], v[192:195], v[24:27]\n" \
"v_exp_f32_e64 v154, v154\n" \
"v_mfma_f32_16x16x32_bf16 v[24:27], a[152:155], v[184:187], v[24:27]\n" \
"ds_read_b128 a[112:115], %[kb0] offset:61440\n" \
"v_mfma_f32_16x16x32_bf16 v[28:31], a[188:191], v[192:195], v[28:31]\n" \
"v_exp_f32_e64 v155, v155\n" \
"v_mfma_f32_16x16x32_bf16 v[28:31], a[156:159], v[184:187], v[28:31]\n" \
"s_waitcnt vmcnt(28) lgkmcnt(2)\n" \
"s_barrier\n" \
"v_mfma_f32_16x16x32_bf16 v[128:131], a[96:99], a[0:3], v[160:163]\n" \
"v_exp_f32_e64 v156, v156\n" \
"v_mfma_f32_16x16x32_bf16 v[128:131], a[100:103], a[4:7], v[128:131]\n" \
"s_add_u32 m0, %[m0b], 98304\n" \
"s_cmp_gt_u32 s48, 0\n" \
"v_mfma_f32_16x16x32_bf16 v[128:131], a[104:107], a[8:11], v[128:131]\n" \
"v_exp_f32_e32 v157, v157\n" \
"v_mfma_f32_16x16x32_bf16 v[128:131], a[108:111], a[12:15], v[128:131]\n" \
"v_cvt_pk_bf16_f32 v220, v152, v153\n" \
"v_cvt_pk_bf16_f32 v221, v154, v155\n" \
"v_mfma_f32_16x16x32_bf16 v[132:135], a[124:127], a[12:15], v[160:163]\n" \
"v_exp_f32_e64 v158, v158\n" \
"v_mfma_f32_16x16x32_bf16 v[132:135], a[120:123], a[8:11], v[132:135]\n" \
"s_cselect_b32 s49, 0, 1\n" \
"s_lshl_b32 s49, s49, 18\n" \
"s_or_b32 s49, s49, s59\n" \
"v_max3_f32 v232, v97, v102, v107\n" \
"s_waitcnt lgkmcnt(0)\n" \
"v_mfma_f32_16x16x32_bf16 v[132:135], a[116:119], a[4:7], v[132:135]\n" \
"buffer_load_dwordx4 %[wo0], %[srdt], s49 offen offset:0 lds\n" \
"v_mfma_f32_16x16x32_bf16 v[132:135], a[112:115], a[0:3], v[132:135]\n" \
"v_exp_f32_e64 v159, v159\n" \
"v_mfma_f32_16x16x32_bf16 v[32:35], a[160:163], v[196:199], v[32:35]\n" \
"ds_read_b64_tr_b16 a[196:197], v215 offset:0\n" \
"v_min3_f32 v233, v97, v102, v107\n" \
"v_mfma_f32_16x16x32_bf16 v[32:35], a[128:131], v[188:191], v[32:35]\n" \
"v_exp_f32_e64 v128, v128\n" \
"v_mfma_f32_16x16x32_bf16 v[36:39], a[164:167], v[196:199], v[36:39]\n" \
"v_cvt_pk_bf16_f32 v222, v156, v157\n" \
"v_cvt_pk_bf16_f32 v223, v158, v159\n" \
"v_mfma_f32_16x16x32_bf16 v[36:39], a[132:135], v[188:191], v[36:39]\n" \
"v_exp_f32_e64 v129, v129\n" \
"v_mfma_f32_16x16x32_bf16 v[40:43], a[168:171], v[196:199], v[40:43]\n" \
"ds_read_b64_tr_b16 a[198:199], v215 offset:2048\n" \
"v_max3_f32 v232, v232, v112, v117\n" \
"v_mfma_f32_16x16x32_bf16 v[40:43], a[136:139], v[188:191], v[40:43]\n" \
"v_exp_f32_e64 v130, v130\n" \
"v_mfma_f32_16x16x32_bf16 v[44:47], a[172:175], v[196:199], v[44:47]\n" \
"ds_read_b64_tr_b16 a[200:201], v215 offset:4096\n" \
"v_min3_f32 v233, v233, v112, v117\n" \
"v_mfma_f32_16x16x32_bf16 v[44:47], a[140:143], v[188:191], v[44:47]\n" \
"v_exp_f32_e64 v131, v131\n" \
"v_smfmac_f32_16x16x64_bf16 v[176:179], a[192:195], v[184:191], v228\n" \
"v_sub_f32_e32 v232, v232, v233\n" \
"v_min_f32_e32 v234, v234, v232\n" \
"v_mfma_f32_16x16x32_bf16 v[136:139], a[96:99], a[16:19], v[164:167]\n" \
"v_exp_f32_e64 v132, v132\n" \
"v_mfma_f32_16x16x32_bf16 v[136:139], a[100:103], a[20:23], v[136:139]\n" \
"ds_read_b64_tr_b16 a[202:203], v215 offset:6144\n" \
"v_max3_f32 v232, v1, v6, v11\n" \
"v_mfma_f32_16x16x32_bf16 v[136:139], a[104:107], a[24:27], v[136:139]\n" \
"v_exp_f32_e64 v133, v133\n" \
"v_mfma_f32_16x16x32_bf16 v[136:139], a[108:111], a[28:31], v[136:139]\n" \
"v_cvt_pk_bf16_f32 v192, v128, v129\n" \
"v_cvt_pk_bf16_f32 v193, v130, v131\n" \
"v_mfma_f32_16x16x32_bf16 v[140:143], a[124:127], a[28:31], v[164:167]\n" \
"v_exp_f32_e64 v134, v134\n" \
"v_mfma_f32_16x16x32_bf16 v[140:143], a[120:123], a[24:27], v[140:143]\n" \
"ds_read_b64_tr_b16 a[204:205], v215 offset:8192\n" \
"v_min3_f32 v233, v1, v6, v11\n" \
"v_mfma_f32_16x16x32_bf16 v[140:143], a[116:119], a[20:23], v[140:143]\n" \
"buffer_load_dwordx4 v212, %[srdt], s49 offen offset:1024 lds\n" \
"v_max3_f32 v232, v232, v16, v21\n" \
"v_mfma_f32_16x16x32_bf16 v[140:143], a[112:115], a[16:19], v[140:143]\n" \
"v_exp_f32_e64 v135, v135\n" \
"v_mfma_f32_16x16x32_bf16 v[48:51], a[176:179], v[196:199], v[48:51]\n" \
"ds_read_b64_tr_b16 a[206:207], v215 offset:10240\n" \
"v_add_u32_e32 v208, 0x1000, %[oq0]\n" \
"v_mfma_f32_16x16x32_bf16 v[48:51], a[144:147], v[188:191], v[48:51]\n" \
"v_exp_f32_e64 v136, v136\n" \
"v_mfma_f32_16x16x32_bf16 v[52:55], a[180:183], v[196:199], v[52:55]\n" \
"v_cvt_pk_bf16_f32 v194, v132, v133\n" \
"v_cvt_pk_bf16_f32 v195, v134, v135\n" \
"v_mfma_f32_16x16x32_bf16 v[52:55], a[148:151], v[188:191], v[52:55]\n" \
"v_exp_f32_e64 v137, v137\n" \
"v_mfma_f32_16x16x32_bf16 v[56:59], a[184:187], v[196:199], v[56:59]\n" \
"v_exp_f32_e64 v138, v138\n" \
"v_mfma_f32_16x16x32_bf16 v[56:59], a[152:155], v[188:191], v[56:59]\n" \
"ds_read_b64_tr_b16 a[208:209], v215 offset:12288\n" \
"v_add_u32_e32 v209, 0x4000, %[oq0]\n" \
"v_mfma_f32_16x16x32_bf16 v[60:63], a[188:191], v[196:199], v[60:63]\n" \
"v_exp_f32_e64 v139, v139\n" \
"v_mfma_f32_16x16x32_bf16 v[60:63], a[156:159], v[188:191], v[60:63]\n" \
"v_add_u32_e32 v210, 0x5000, %[oq0]\n" \
"v_mfma_f32_16x16x32_bf16 v[144:147], a[96:99], a[32:35], v[168:171]\n" \
"v_exp_f32_e64 v140, v140\n" \
"v_mfma_f32_16x16x32_bf16 v[144:147], a[100:103], a[36:39], v[144:147]\n" \
"ds_read_b64_tr_b16 a[210:211], v215 offset:14336\n" \
"v_min3_f32 v233, v233, v16, v21\n" \
"v_mfma_f32_16x16x32_bf16 v[144:147], a[104:107], a[40:43], v[144:147]\n" \
"v_exp_f32_e64 v141, v141\n" \
"v_mfma_f32_16x16x32_bf16 v[144:147], a[108:111], a[44:47], v[144:147]\n" \
"v_cvt_pk_bf16_f32 v196, v136, v137\n" \
"v_cvt_pk_bf16_f32 v197, v138, v139\n" \
"v_mfma_f32_16x16x32_bf16 v[148:151], a[124:127], a[44:47], v[168:171]\n" \
"v_exp_f32_e64 v142, v142\n" \
"v_mfma_f32_16x16x32_bf16 v[148:151], a[120:123], a[40:43], v[148:151]\n" \
"v_sub_f32_e32 v232, v232, v233\n" \
"v_min_f32_e32 v234, v234, v232\n" \
"v_mfma_f32_16x16x32_bf16 v[148:151], a[116:119], a[36:39], v[148:151]\n" \
"buffer_load_dwordx4 v213, %[srdt], s49 offen offset:2048 lds\n" \
"v_max3_f32 v232, v65, v70, v75\n" \
"v_mfma_f32_16x16x32_bf16 v[148:151], a[112:115], a[32:35], v[148:151]\n" \
"v_exp_f32_e64 v143, v143\n" \
"v_mfma_f32_16x16x32_bf16 v[96:99], a[160:163], v[204:207], v[96:99]\n" \
"ds_read_b64_tr_b16 a[160:161], %[vvb] offset:57344\n" \
"v_min3_f32 v233, v65, v70, v75\n" \
"v_mfma_f32_16x16x32_bf16 v[96:99], a[128:131], v[220:223], v[96:99]\n" \
"v_exp_f32_e64 v144, v144\n" \
"v_mfma_f32_16x16x32_bf16 v[100:103], a[164:167], v[204:207], v[100:103]\n" \
"v_cvt_pk_bf16_f32 v198, v140, v141\n" \
"v_cvt_pk_bf16_f32 v199, v142, v143\n" \
"v_mfma_f32_16x16x32_bf16 v[100:103], a[132:135], v[220:223], v[100:103]\n" \
"v_exp_f32_e64 v145, v145\n" \
"v_mfma_f32_16x16x32_bf16 v[104:107], a[168:171], v[204:207], v[104:107]\n" \
"ds_read_b64_tr_b16 a[162:163], %[vvb] offset:61440\n" \
"ds_read_b64_tr_b16 a[164:165], v211 offset:57344\n" \
"v_mfma_f32_16x16x32_bf16 v[104:107], a[136:139], v[220:223], v[104:107]\n" \
"v_exp_f32_e64 v146, v146\n" \
"v_mfma_f32_16x16x32_bf16 v[108:111], a[172:175], v[204:207], v[108:111]\n" \
"ds_read_b64_tr_b16 a[166:167], v211 offset:61440\n" \
"v_max3_f32 v232, v232, v80, v85\n" \
"v_mfma_f32_16x16x32_bf16 v[108:111], a[140:143], v[220:223], v[108:111]\n" \
"v_exp_f32_e64 v147, v147\n" \
"v_smfmac_f32_16x16x64_bf16 v[180:183], a[192:195], v[216:223], v228\n" \
"ds_read_b64_tr_b16 a[168:169], %[vvb] offset:57856\n" \
"v_min3_f32 v233, v233, v80, v85\n" \
"v_mfma_f32_16x16x32_bf16 v[152:155], a[96:99], a[48:51], v[172:175]\n" \
"v_exp_f32_e64 v148, v148\n" \
"v_mfma_f32_16x16x32_bf16 v[152:155], a[100:103], a[52:55], v[152:155]\n" \
"ds_read_b64_tr_b16 a[170:171], %[vvb] offset:61952\n" \
"v_sub_f32_e64 v232, v232, v233\n" \
"v_mfma_f32_16x16x32_bf16 v[152:155], a[104:107], a[56:59], v[152:155]\n" \
"v_exp_f32_e64 v149, v149\n" \
"v_mfma_f32_16x16x32_bf16 v[152:155], a[108:111], a[60:63], v[152:155]\n" \
"v_cvt_pk_bf16_f32 v200, v144, v145\n" \
"v_cvt_pk_bf16_f32 v201, v146, v147\n" \
"v_mfma_f32_16x16x32_bf16 v[156:159], a[124:127], a[60:63], v[172:175]\n" \
"v_exp_f32_e64 v150, v150\n" \
"v_mfma_f32_16x16x32_bf16 v[156:159], a[120:123], a[56:59], v[156:159]\n" \
"ds_read_b64_tr_b16 a[172:173], v211 offset:57856\n" \
"v_min_f32_e64 v234, v234, v232\n" \
"v_mfma_f32_16x16x32_bf16 v[156:159], a[116:119], a[52:55], v[156:159]\n" \
"buffer_load_dwordx4 v214, %[srdt], s49 offen offset:3072 lds\n" \
"v_accvgpr_read_b32 v233, a228\n" \
"v_mul_f32_e32 v233, 0x3a000000, v233\n" \
"v_mfma_f32_16x16x32_bf16 v[156:159], a[112:115], a[48:51], v[156:159]\n" \
"v_exp_f32_e64 v151, v151\n" \
"v_mfma_f32_16x16x32_bf16 v[112:115], a[176:179], v[204:207], v[112:115]\n" \
"ds_read_b64_tr_b16 a[174:175], v211 offset:61952\n" \
"ds_read_b64_tr_b16 a[176:177], %[vvb] offset:58368\n" \
"v_mfma_f32_16x16x32_bf16 v[112:115], a[144:147], v[220:223], v[112:115]\n" \
"v_exp_f32_e64 v152, v152\n" \
"v_mfma_f32_16x16x32_bf16 v[116:119], a[180:183], v[204:207], v[116:119]\n" \
"v_cvt_pk_bf16_f32 v202, v148, v149\n" \
"v_cvt_pk_bf16_f32 v203, v150, v151\n" \
"v_mfma_f32_16x16x32_bf16 v[116:119], a[148:151], v[220:223], v[116:119]\n" \
"v_exp_f32_e64 v153, v153\n" \
"v_mfma_f32_16x16x32_bf16 v[120:123], a[184:187], v[204:207], v[120:123]\n" \
"v_exp_f32_e64 v154, v154\n" \
"v_mfma_f32_16x16x32_bf16 v[120:123], a[152:155], v[220:223], v[120:123]\n" \
"ds_read_b64_tr_b16 a[178:179], %[vvb] offset:62464\n" \
"s_waitcnt vmcnt(5)\n" \
"v_cmp_eq_u32_e64 s[56:57], 0, v229\n" \
"s_cmp_lg_u64 s[56:57], 0\n" \
"s_cbranch_scc1 Lspk_%=\n" \
"Lbkk_%=:\n" \
"v_mfma_f32_16x16x32_bf16 v[124:127], a[188:191], v[204:207], v[124:127]\n" \
"v_exp_f32_e32 v155, v155\n" \
"v_mfma_f32_16x16x32_bf16 v[124:127], a[156:159], v[220:223], v[124:127]\n" \
"ds_read_b64_tr_b16 a[180:181], v211 offset:58368\n" \
"ds_read_b64_tr_b16 a[182:183], v211 offset:62464\n" \
"ds_read_b64_tr_b16 a[184:185], %[vvb] offset:58880\n" \
"ds_read_b64_tr_b16 a[186:187], %[vvb] offset:62976\n" \
"ds_read_b64_tr_b16 a[188:189], v211 offset:58880\n" \
"ds_read_b64_tr_b16 a[190:191], v211 offset:62976\n" \
"s_waitcnt lgkmcnt(8)\n" \
"s_nop 0\n" \
"v_smfmac_f32_16x16x64_bf16 v[176:179], a[192:195], v[192:199], v228\n" \
"v_exp_f32_e64 v156, v156\n" \
"ds_read_b64_tr_b16 a[212:213], v227 offset:0\n" \
"v_mfma_f32_16x16x32_bf16 v[0:3], a[160:163], v[192:195], v[0:3]\n" \
"v_exp_f32_e64 v157, v157\n" \
"ds_read_b64_tr_b16 a[214:215], v227 offset:2048\n" \
"v_mfma_f32_16x16x32_bf16 v[4:7], a[164:167], v[192:195], v[4:7]\n" \
"v_cvt_pk_bf16_f32 v204, v152, v153\n" \
"ds_read_b64_tr_b16 a[216:217], v227 offset:4096\n" \
"v_mfma_f32_16x16x32_bf16 v[8:11], a[168:171], v[192:195], v[8:11]\n" \
"v_cvt_pk_bf16_f32 v205, v154, v155\n" \
"ds_read_b64_tr_b16 a[218:219], v227 offset:6144\n" \
"v_mfma_f32_16x16x32_bf16 v[12:15], a[172:175], v[192:195], v[12:15]\n" \
"v_exp_f32_e64 v158, v158\n" \
"ds_read_b64_tr_b16 a[220:221], v227 offset:8192\n" \
"s_waitcnt lgkmcnt(5)\n" \
"s_nop 0\n" \
"v_mfma_f32_16x16x32_bf16 v[16:19], a[176:179], v[192:195], v[16:19]\n" \
"v_exp_f32_e64 v159, v159\n" \
"ds_read_b64_tr_b16 a[222:223], v227 offset:10240\n" \
"v_mfma_f32_16x16x32_bf16 v[20:23], a[180:183], v[192:195], v[20:23]\n" \
"v_cvt_pk_bf16_f32 v206, v156, v157\n" \
"ds_read_b64_tr_b16 a[224:225], v227 offset:12288\n" \
"v_accvgpr_read_b32 v229, a228\n" \
"v_mfma_f32_16x16x32_bf16 v[24:27], a[184:187], v[192:195], v[24:27]\n" \
"v_cvt_pk_bf16_f32 v207, v158, v159\n" \
"ds_read_b64_tr_b16 a[226:227], v227 offset:14336\n" \
"v_mfma_f32_16x16x32_bf16 v[28:31], a[188:191], v[192:195], v[28:31]\n" \
"v_add_f32_e64 v176, v176, v177\n" \
"v_smfmac_f32_16x16x64_bf16 v[180:183], a[192:195], v[200:207], v228\n" \
"v_add_f32_e32 v178, v178, v179\n" \
"v_add_f32_e32 v231, v176, v178\n" \
"v_rcp_f32_e32 v176, v176\n" \
"v_rcp_f32_e32 v178, v178\n" \
"v_mul_f32_e64 v177, -v229, v176\n" \
"v_mul_f32_e64 v179, -v229, v178\n" \
"v_mfma_f32_16x16x32_bf16 v[32:35], a[160:163], v[196:199], v[32:35]\n" \
"v_pk_fma_f32 v[0:1], v[0:1], v[176:177], v[176:177] op_sel:[0,0,1] op_sel_hi:[1,0,1]\n" \
"v_pk_fma_f32 v[2:3], v[2:3], v[176:177], v[176:177] op_sel:[0,0,1] op_sel_hi:[1,0,1]\n" \
"v_pk_fma_f32 v[4:5], v[4:5], v[176:177], v[176:177] op_sel:[0,0,1] op_sel_hi:[1,0,1]\n" \
"v_pk_fma_f32 v[6:7], v[6:7], v[176:177], v[176:177] op_sel:[0,0,1] op_sel_hi:[1,0,1]\n" \
"v_cvt_pk_bf16_f32 v0, v0, v1\n" \
"v_cvt_pk_bf16_f32 v1, v2, v3\n" \
"v_cvt_pk_bf16_f32 v2, v4, v5\n" \
"v_cvt_pk_bf16_f32 v3, v6, v7\n" \
"v_mfma_f32_16x16x32_bf16 v[36:39], a[164:167], v[196:199], v[36:39]\n" \
"s_mov_b32 s55, 0x4e800000\n" \
"v_mov_b32_e32 v236, %[flag]\n" \
"v_add_f32_e32 v180, v180, v181\n" \
"v_add_f32_e32 v182, v182, v183\n" \
"v_add_f32_e32 v231, v231, v180\n" \
"v_add_f32_e32 v231, v231, v182\n" \
"v_rcp_f32_e32 v180, v180\n" \
"v_cmp_nle_f32_e64 s[56:57], v231, s55\n" \
"v_mfma_f32_16x16x32_bf16 v[40:43], a[168:171], v[196:199], v[40:43]\n" \
"v_rcp_f32_e64 v182, v182\n" \
"v_cmp_nle_f32_e64 s[44:45], v233, v234\n" \
"v_mul_f32_e64 v181, -v229, v180\n" \
"v_mul_f32_e64 v183, -v229, v182\n" \
"s_nop 1\n" \
"s_or_b64 s[56:57], s[56:57], s[44:45]\n" \
"s_cselect_b32 s55, 1, 0\n" \
"v_mov_b32_e32 v230, s55\n" \
"v_mfma_f32_16x16x32_bf16 v[44:47], a[172:175], v[196:199], v[44:47]\n" \
"s_mov_b32 s69, s55\n" \
"s_andn2_b32 s55, s55, s52\n" \
"s_cselect_b64 vcc, 0, vcc\n" \
"s_nop 0\n" \
"ds_write_b32 v236, v230\n" \
"v_and_b32_e32 v236, -16, v236\n" \
"s_cbranch_vccz Lg21_%=\n" \
"buffer_store_dwordx4 v[0:3], %[oq0], %[srdz], s60 offen offset:0 sc1\n" \
"Lg21_%=:\n" \
"v_pk_fma_f32 v[8:9], v[8:9], v[176:177], v[176:177] op_sel:[0,0,1] op_sel_hi:[1,0,1]\n" \
"v_pk_fma_f32 v[10:11], v[10:11], v[176:177], v[176:177] op_sel:[0,0,1] op_sel_hi:[1,0,1]\n" \
"v_mfma_f32_16x16x32_bf16 v[48:51], a[176:179], v[196:199], v[48:51]\n" \
"v_pk_fma_f32 v[12:13], v[12:13], v[176:177], v[176:177] op_sel:[0,0,1] op_sel_hi:[1,0,1]\n" \
"v_pk_fma_f32 v[14:15], v[14:15], v[176:177], v[176:177] op_sel:[0,0,1] op_sel_hi:[1,0,1]\n" \
"v_cvt_pk_bf16_f32 v8, v8, v9\n" \
"v_cvt_pk_bf16_f32 v9, v10, v11\n" \
"v_cvt_pk_bf16_f32 v10, v12, v13\n" \
"v_cvt_pk_bf16_f32 v11, v14, v15\n" \
"s_cbranch_vccz Lg22_%=\n" \
"s_nop 0\n" \
"buffer_store_dwordx4 v[8:11], %[oq0], %[srdz], s60 offen offset:1024 sc1\n" \
"Lg22_%=:\n" \
"v_pk_fma_f32 v[16:17], v[16:17], v[176:177], v[176:177] op_sel:[0,0,1] op_sel_hi:[1,0,1]\n" \
"v_mfma_f32_16x16x32_bf16 v[52:55], a[180:183], v[196:199], v[52:55]\n" \
"v_pk_fma_f32 v[18:19], v[18:19], v[176:177], v[176:177] op_sel:[0,0,1] op_sel_hi:[1,0,1]\n" \
"v_pk_fma_f32 v[20:21], v[20:21], v[176:177], v[176:177] op_sel:[0,0,1] op_sel_hi:[1,0,1]\n" \
"v_pk_fma_f32 v[22:23], v[22:23], v[176:177], v[176:177] op_sel:[0,0,1] op_sel_hi:[1,0,1]\n" \
"v_cvt_pk_bf16_f32 v16, v16, v17\n" \
"v_cvt_pk_bf16_f32 v17, v18, v19\n" \
"v_cvt_pk_bf16_f32 v18, v20, v21\n" \
"v_cvt_pk_bf16_f32 v19, v22, v23\n" \
"s_cbranch_vccz Lg23_%=\n" \
"s_nop 0\n" \
"buffer_store_dwordx4 v[16:19], %[oq0], %[srdz], s60 offen offset:2048 sc1\n" \
"Lg23_%=:\n" \
"v_mfma_f32_16x16x32_bf16 v[56:59], a[184:187], v[196:199], v[56:59]\n" \
"v_pk_fma_f32 v[24:25], v[24:25], v[176:177], v[176:177] op_sel:[0,0,1] op_sel_hi:[1,0,1]\n" \
"v_pk_fma_f32 v[26:27], v[26:27], v[176:177], v[176:177] op_sel:[0,0,1] op_sel_hi:[1,0,1]\n" \
"v_pk_fma_f32 v[28:29], v[28:29], v[176:177], v[176:177] op_sel:[0,0,1] op_sel_hi:[1,0,1]\n" \
"v_pk_fma_f32 v[30:31], v[30:31], v[176:177], v[176:177] op_sel:[0,0,1] op_sel_hi:[1,0,1]\n" \
"v_cvt_pk_bf16_f32 v24, v24, v25\n" \
"v_cvt_pk_bf16_f32 v25, v26, v27\n" \
"v_cvt_pk_bf16_f32 v26, v28, v29\n" \
"v_cvt_pk_bf16_f32 v27, v30, v31\n" \
"v_mfma_f32_16x16x32_bf16 v[60:63], a[188:191], v[196:199], v[60:63]\n" \
"s_cbranch_vccz Lg24_%=\n" \
"s_nop 0\n" \
"buffer_store_dwordx4 v[24:27], %[oq0], %[srdz], s60 offen offset:3072 sc1\n" \
"Lg24_%=:\n" \
"v_mfma_f32_16x16x32_bf16 v[64:67], a[128:131], v[216:219], v[64:67]\n" \
"v_fma_f32 v32, v32, v178, v179\n" \
"v_fma_f32 v33, v33, v178, v179\n" \
"v_fma_f32 v34, v34, v178, v179\n" \
"v_fma_f32 v35, v35, v178, v179\n" \
"v_fma_f32 v36, v36, v178, v179\n" \
"v_fma_f32 v37, v37, v178, v179\n" \
"v_mfma_f32_16x16x32_bf16 v[64:67], a[160:163], v[200:203], v[64:67]\n" \
"v_fma_f32 v38, v38, v178, v179\n" \
"v_fma_f32 v39, v39, v178, v179\n" \
"v_cvt_pk_bf16_f32 v32, v32, v33\n" \
"v_cvt_pk_bf16_f32 v33, v34, v35\n" \
"v_mfma_f32_16x16x32_bf16 v[68:71], a[132:135], v[216:219], v[68:71]\n" \
"v_cvt_pk_bf16_f32 v34, v36, v37\n" \
"v_cvt_pk_bf16_f32 v35, v38, v39\n" \
"s_cbranch_vccz Lg25_%=\n" \
"s_nop 0\n" \
"buffer_store_dwordx4 v[32:35], v208, %[srdz], s60 offen offset:0 sc1\n" \
"Lg25_%=:\n" \
"v_mfma_f32_16x16x32_bf16 v[68:71], a[164:167], v[200:203], v[68:71]\n" \
"v_fma_f32 v40, v40, v178, v179\n" \
"v_fma_f32 v41, v41, v178, v179\n" \
"v_fma_f32 v42, v42, v178, v179\n" \
"v_fma_f32 v43, v43, v178, v179\n" \
"v_fma_f32 v44, v44, v178, v179\n" \
"v_fma_f32 v45, v45, v178, v179\n" \
"v_mfma_f32_16x16x32_bf16 v[72:75], a[136:139], v[216:219], v[72:75]\n" \
"v_fma_f32 v46, v46, v178, v179\n" \
"v_fma_f32 v47, v47, v178, v179\n" \
"v_cvt_pk_bf16_f32 v40, v40, v41\n" \
"v_cvt_pk_bf16_f32 v41, v42, v43\n" \
"v_mfma_f32_16x16x32_bf16 v[72:75], a[168:171], v[200:203], v[72:75]\n" \
"v_cvt_pk_bf16_f32 v42, v44, v45\n" \
"v_cvt_pk_bf16_f32 v43, v46, v47\n" \
"s_cbranch_vccz Lg26_%=\n" \
"s_nop 0\n" \
"buffer_store_dwordx4 v[40:43], v208, %[srdz], s60 offen offset:1024 sc1\n" \
"Lg26_%=:\n" \
"v_mfma_f32_16x16x32_bf16 v[76:79], a[140:143], v[216:219], v[76:79]\n" \
"v_fma_f32 v48, v48, v178, v179\n" \
"v_fma_f32 v49, v49, v178, v179\n" \
"v_fma_f32 v50, v50, v178, v179\n" \
"v_fma_f32 v51, v51, v178, v179\n" \
"v_fma_f32 v52, v52, v178, v179\n" \
"v_fma_f32 v53, v53, v178, v179\n" \
"v_mfma_f32_16x16x32_bf16 v[76:79], a[172:175], v[200:203], v[76:79]\n" \
"v_fma_f32 v54, v54, v178, v179\n" \
"v_fma_f32 v55, v55, v178, v179\n" \
"v_cvt_pk_bf16_f32 v48, v48, v49\n" \
"v_cvt_pk_bf16_f32 v49, v50, v51\n" \
"v_mfma_f32_16x16x32_bf16 v[80:83], a[144:147], v[216:219], v[80:83]\n" \
"v_cvt_pk_bf16_f32 v50, v52, v53\n" \
"v_cvt_pk_bf16_f32 v51, v54, v55\n" \
"s_cbranch_vccz Lg27_%=\n" \
"s_nop 0\n" \
"buffer_store_dwordx4 v[48:51], v208, %[srdz], s60 offen offset:2048 sc1\n" \
"Lg27_%=:\n" \
"v_mfma_f32_16x16x32_bf16 v[80:83], a[176:179], v[200:203], v[80:83]\n" \
"v_fma_f32 v56, v56, v178, v179\n" \
"v_fma_f32 v57, v57, v178, v179\n" \
"v_fma_f32 v58, v58, v178, v179\n" \
"v_fma_f32 v59, v59, v178, v179\n" \
"v_fma_f32 v60, v60, v178, v179\n" \
"v_fma_f32 v61, v61, v178, v179\n" \
"v_mfma_f32_16x16x32_bf16 v[84:87], a[148:151], v[216:219], v[84:87]\n" \
"v_fma_f32 v62, v62, v178, v179\n" \
"v_fma_f32 v63, v63, v178, v179\n" \
"v_cvt_pk_bf16_f32 v56, v56, v57\n" \
"v_cvt_pk_bf16_f32 v57, v58, v59\n" \
"v_mfma_f32_16x16x32_bf16 v[84:87], a[180:183], v[200:203], v[84:87]\n" \
"v_cvt_pk_bf16_f32 v58, v60, v61\n" \
"v_cvt_pk_bf16_f32 v59, v62, v63\n" \
"s_cbranch_vccz Lg28_%=\n" \
"s_nop 0\n" \
"buffer_store_dwordx4 v[56:59], v208, %[srdz], s60 offen offset:3072 sc1\n" \
"Lg28_%=:\n" \
"v_mfma_f32_16x16x32_bf16 v[88:91], a[152:155], v[216:219], v[88:91]\n" \
"v_mfma_f32_16x16x32_bf16 v[88:91], a[184:187], v[200:203], v[88:91]\n" \
"v_mfma_f32_16x16x32_bf16 v[92:95], a[156:159], v[216:219], v[92:95]\n" \
"v_mfma_f32_16x16x32_bf16 v[92:95], a[188:191], v[200:203], v[92:95]\n" \
"s_waitcnt vmcnt(12) lgkmcnt(0)\n" \
"s_bitcmp1_b32 0x1f, s48\n" \
"s_cbranch_scc1 Lcw1_%=\n" \
"s_waitcnt vmcnt(4)\n" \
"Lcw1_%=:\n" \
"s_barrier\n" \
"s_nop 0\n" \
"ds_read_b128 v[232:235], v236\n" \
"v_mfma_f32_16x16x32_bf16 v[96:99], a[160:163], v[204:207], v[96:99]\n" \
"v_pk_fma_f32 v[64:65], v[64:65], v[180:181], v[180:181] op_sel:[0,0,1] op_sel_hi:[1,0,1]\n" \
"v_pk_fma_f32 v[66:67], v[66:67], v[180:181], v[180:181] op_sel:[0,0,1] op_sel_hi:[1,0,1]\n" \
"v_pk_fma_f32 v[68:69], v[68:69], v[180:181], v[180:181] op_sel:[0,0,1] op_sel_hi:[1,0,1]\n" \
"v_pk_fma_f32 v[70:71], v[70:71], v[180:181], v[180:181] op_sel:[0,0,1] op_sel_hi:[1,0,1]\n" \
"v_cvt_pk_bf16_f32 v64, v64, v65\n" \
"v_mfma_f32_16x16x32_bf16 v[100:103], a[164:167], v[204:207], v[100:103]\n" \
"v_cvt_pk_bf16_f32 v65, v66, v67\n" \
"v_cvt_pk_bf16_f32 v66, v68, v69\n" \
"v_cvt_pk_bf16_f32 v67, v70, v71\n" \
"s_cbranch_vccz Lg29_%=\n" \
"s_nop 0\n" \
"buffer_store_dwordx4 v[64:67], v209, %[srdz], s60 offen offset:0 sc1\n" \
"Lg29_%=:\n" \
"v_pk_fma_f32 v[72:73], v[72:73], v[180:181], v[180:181] op_sel:[0,0,1] op_sel_hi:[1,0,1]\n" \
"v_mfma_f32_16x16x32_bf16 v[104:107], a[168:171], v[204:207], v[104:107]\n" \
"v_pk_fma_f32 v[74:75], v[74:75], v[180:181], v[180:181] op_sel:[0,0,1] op_sel_hi:[1,0,1]\n" \
"v_pk_fma_f32 v[76:77], v[76:77], v[180:181], v[180:181] op_sel:[0,0,1] op_sel_hi:[1,0,1]\n" \
"v_pk_fma_f32 v[78:79], v[78:79], v[180:181], v[180:181] op_sel:[0,0,1] op_sel_hi:[1,0,1]\n" \
"v_cvt_pk_bf16_f32 v72, v72, v73\n" \
"v_cvt_pk_bf16_f32 v73, v74, v75\n" \
"v_mfma_f32_16x16x32_bf16 v[108:111], a[172:175], v[204:207], v[108:111]\n" \
"v_cvt_pk_bf16_f32 v74, v76, v77\n" \
"v_cvt_pk_bf16_f32 v75, v78, v79\n" \
"s_cbranch_vccz Lg30_%=\n" \
"s_nop 0\n" \
"buffer_store_dwordx4 v[72:75], v209, %[srdz], s60 offen offset:1024 sc1\n" \
"Lg30_%=:\n" \
"v_pk_fma_f32 v[80:81], v[80:81], v[180:181], v[180:181] op_sel:[0,0,1] op_sel_hi:[1,0,1]\n" \
"v_pk_fma_f32 v[82:83], v[82:83], v[180:181], v[180:181] op_sel:[0,0,1] op_sel_hi:[1,0,1]\n" \
"v_mfma_f32_16x16x32_bf16 v[112:115], a[176:179], v[204:207], v[112:115]\n" \
"v_pk_fma_f32 v[84:85], v[84:85], v[180:181], v[180:181] op_sel:[0,0,1] op_sel_hi:[1,0,1]\n" \
"v_pk_fma_f32 v[86:87], v[86:87], v[180:181], v[180:181] op_sel:[0,0,1] op_sel_hi:[1,0,1]\n" \
".if . - adaln_body != 40960\n" \
".error \"size model\"\n" \
".endif\n" \
"Lk9_%=:\n" \
"s_cbranch_execz Lk10_%=\n" \
"s_nop 0\n" \
"v_cvt_pk_bf16_f32 v80, v80, v81\n" \
"v_cvt_pk_bf16_f32 v81, v82, v83\n" \
"v_cvt_pk_bf16_f32 v82, v84, v85\n" \
"v_mfma_f32_16x16x32_bf16 v[116:119], a[180:183], v[204:207], v[116:119]\n" \
"v_cvt_pk_bf16_f32 v83, v86, v87\n" \
"s_cbranch_vccz Lg31_%=\n" \
"s_nop 0\n" \
"buffer_store_dwordx4 v[80:83], v209, %[srdz], s60 offen offset:2048 sc1\n" \
"Lg31_%=:\n" \
"v_pk_fma_f32 v[88:89], v[88:89], v[180:181], v[180:181] op_sel:[0,0,1] op_sel_hi:[1,0,1]\n" \
"v_pk_fma_f32 v[90:91], v[90:91], v[180:181], v[180:181] op_sel:[0,0,1] op_sel_hi:[1,0,1]\n" \
"v_pk_fma_f32 v[92:93], v[92:93], v[180:181], v[180:181] op_sel:[0,0,1] op_sel_hi:[1,0,1]\n" \
"v_mfma_f32_16x16x32_bf16 v[120:123], a[184:187], v[204:207], v[120:123]\n" \
"v_pk_fma_f32 v[94:95], v[94:95], v[180:181], v[180:181] op_sel:[0,0,1] op_sel_hi:[1,0,1]\n" \
"v_cvt_pk_bf16_f32 v88, v88, v89\n" \
"v_cvt_pk_bf16_f32 v89, v90, v91\n" \
"v_cvt_pk_bf16_f32 v90, v92, v93\n" \
"v_cvt_pk_bf16_f32 v91, v94, v95\n" \
"v_mfma_f32_16x16x32_bf16 v[124:127], a[188:191], v[204:207], v[124:127]\n" \
"s_cbranch_vccz Lg32_%=\n" \
"s_nop 0\n" \
"buffer_store_dwordx4 v[88:91], v209, %[srdz], s60 offen offset:3072 sc1\n" \
"Lg32_%=:\n" \
"s_waitcnt lgkmcnt(0)\n" \
"s_nop 0\n" \
"v_or3_b32 v232, v232, v233, v234\n" \
"v_or_b32_e32 v232, v232, v235\n" \
"s_nop 0\n" \
"v_readfirstlane_b32 s55, v232\n" \
"s_nop 3\n" \
"s_andn2_b32 s55, s55, s52\n" \
"s_cbranch_scc1 Lslow_%=\n" \
"v_mfma_f32_16x16x32_bf16 v[192:195], a[196:199], v[0:3], 0\n" \
"v_mfma_f32_16x16x32_bf16 v[192:195], a[200:203], v[8:11], v[192:195]\n" \
"v_fma_f32 v96, v96, v182, v183\n" \
"v_fma_f32 v97, v97, v182, v183\n" \
"v_fma_f32 v98, v98, v182, v183\n" \
"v_fma_f32 v99, v99, v182, v183\n" \
"v_mfma_f32_16x16x32_bf16 v[192:195], a[204:207], v[16:19], v[192:195]\n" \
"v_fma_f32 v100, v100, v182, v183\n" \
"v_fma_f32 v101, v101, v182, v183\n" \
"v_fma_f32 v102, v102, v182, v183\n" \
"v_fma_f32 v103, v103, v182, v183\n" \
"s_add_u32 m0, %[m0b], 114688\n" \
"s_cmp_gt_u32 s48, 1\n" \
"s_cselect_b32 s49, 1, 2\n" \
"s_lshl_b32 s49, s49, 18\n" \
"s_or_b32 s49, s49, s58\n" \
"buffer_load_dwordx4 %[wo0], %[srdt], s49 offen offset:0 lds\n" \
"v_mfma_f32_16x16x32_bf16 v[192:195], a[208:211], v[24:27], v[192:195]\n" \
"v_cvt_pk_bf16_f32 v96, v96, v97\n" \
"v_cvt_pk_bf16_f32 v97, v98, v99\n" \
"v_mfma_f32_16x16x32_bf16 v[144:147], a[212:215], v[0:3], 0\n" \
"v_cvt_pk_bf16_f32 v98, v100, v101\n" \
"v_mfma_f32_16x16x32_bf16 v[144:147], a[216:219], v[8:11], v[144:147]\n" \
"v_cvt_pk_bf16_f32 v99, v102, v103\n" \
"s_cbranch_vccz Lg1_%=\n" \
"s_nop 0\n" \
"buffer_store_dwordx4 v[96:99], v210, %[srdz], s60 offen offset:0 sc1\n" \
"Lg1_%=:\n" \
"v_mfma_f32_16x16x32_bf16 v[144:147], a[220:223], v[16:19], v[144:147]\n" \
"ds_read_b64_tr_b16 a[228:229], v215 offset:512\n" \
"v_fma_f32 v104, v104, v182, v183\n" \
"v_fma_f32 v105, v105, v182, v183\n" \
"v_fma_f32 v106, v106, v182, v183\n" \
"v_fma_f32 v107, v107, v182, v183\n" \
"v_mfma_f32_16x16x32_bf16 v[144:147], a[224:227], v[24:27], v[144:147]\n" \
"ds_read_b64_tr_b16 a[230:231], v215 offset:2560\n" \
"v_fma_f32 v108, v108, v182, v183\n" \
"v_fma_f32 v109, v109, v182, v183\n" \
"v_mfma_f32_16x16x32_bf16 v[196:199], a[196:199], v[32:35], 0\n" \
"ds_read_b64_tr_b16 a[232:233], v215 offset:4608\n" \
"v_fma_f32 v110, v110, v182, v183\n" \
"v_fma_f32 v111, v111, v182, v183\n" \
"v_cvt_pk_bf16_f32 v104, v104, v105\n" \
"v_mfma_f32_16x16x32_bf16 v[196:199], a[200:203], v[40:43], v[196:199]\n" \
"ds_read_b64_tr_b16 a[234:235], v215 offset:6656\n" \
"v_cvt_pk_bf16_f32 v105, v106, v107\n" \
"v_cvt_pk_bf16_f32 v106, v108, v109\n" \
"v_mfma_f32_16x16x32_bf16 v[196:199], a[204:207], v[48:51], v[196:199]\n" \
"v_cvt_pk_bf16_f32 v107, v110, v111\n" \
"s_cbranch_vccz Lg2_%=\n" \
"s_nop 0\n" \
"buffer_store_dwordx4 v[104:107], v210, %[srdz], s60 offen offset:1024 sc1\n" \
"Lg2_%=:\n" \
"v_mfma_f32_16x16x32_bf16 v[196:199], a[208:211], v[56:59], v[196:199]\n" \
"v_fma_f32 v112, v112, v182, v183\n" \
"v_fma_f32 v113, v113, v182, v183\n" \
"v_mfma_f32_16x16x32_bf16 v[148:151], a[212:215], v[32:35], 0\n" \
"v_fma_f32 v114, v114, v182, v183\n" \
"v_fma_f32 v115, v115, v182, v183\n" \
"v_fma_f32 v116, v116, v182, v183\n" \
"v_fma_f32 v117, v117, v182, v183\n" \
"buffer_load_dwordx4 v212, %[srdt], s49 offen offset:1024 lds\n" \
"v_mfma_f32_16x16x32_bf16 v[148:151], a[216:219], v[40:43], v[148:151]\n" \
"v_fma_f32 v118, v118, v182, v183\n" \
"v_fma_f32 v119, v119, v182, v183\n" \
"v_cvt_pk_bf16_f32 v112, v112, v113\n" \
"v_mfma_f32_16x16x32_bf16 v[148:151], a[220:223], v[48:51], v[148:151]\n" \
"ds_read_b64_tr_b16 a[236:237], v215 offset:8704\n" \
"v_cvt_pk_bf16_f32 v113, v114, v115\n" \
"v_mfma_f32_16x16x32_bf16 v[148:151], a[224:227], v[56:59], v[148:151]\n" \
"v_cvt_pk_bf16_f32 v114, v116, v117\n" \
"v_cvt_pk_bf16_f32 v115, v118, v119\n" \
"v_mfma_f32_16x16x32_bf16 v[200:203], a[196:199], v[64:67], 0\n" \
"ds_read_b64_tr_b16 a[238:239], v215 offset:10752\n" \
"s_cbranch_vccz Lg3_%=\n" \
"s_nop 0\n" \
"buffer_store_dwordx4 v[112:115], v210, %[srdz], s60 offen offset:2048 sc1\n" \
"Lg3_%=:\n" \
"v_fma_f32 v120, v120, v182, v183\n" \
"v_fma_f32 v121, v121, v182, v183\n" \
"v_mfma_f32_16x16x32_bf16 v[200:203], a[200:203], v[72:75], v[200:203]\n" \
"v_fma_f32 v122, v122, v182, v183\n" \
"v_fma_f32 v123, v123, v182, v183\n" \
"v_fma_f32 v124, v124, v182, v183\n" \
"v_fma_f32 v125, v125, v182, v183\n" \
"v_mfma_f32_16x16x32_bf16 v[200:203], a[204:207], v[80:83], v[200:203]\n" \
"v_fma_f32 v126, v126, v182, v183\n" \
"v_fma_f32 v127, v127, v182, v183\n" \
"v_mfma_f32_16x16x32_bf16 v[200:203], a[208:211], v[88:91], v[200:203]\n" \
"v_cvt_pk_bf16_f32 v120, v120, v121\n" \
"v_cvt_pk_bf16_f32 v121, v122, v123\n" \
"v_mfma_f32_16x16x32_bf16 v[152:155], a[212:215], v[64:67], 0\n" \
"v_cvt_pk_bf16_f32 v122, v124, v125\n" \
"v_cvt_pk_bf16_f32 v123, v126, v127\n" \
"v_mfma_f32_16x16x32_bf16 v[152:155], a[216:219], v[72:75], v[152:155]\n" \
"s_cbranch_vccz Lg4_%=\n" \
"s_nop 0\n" \
"buffer_store_dwordx4 v[120:123], v210, %[srdz], s60 offen offset:3072 sc1\n" \
"Lg4_%=:\n" \
"v_mfma_f32_16x16x32_bf16 v[152:155], a[220:223], v[80:83], v[152:155]\n" \
"v_add_u32_e32 v220, 0x10000, v215\n" \
"v_mfma_f32_16x16x32_bf16 v[152:155], a[224:227], v[88:91], v[152:155]\n" \
"v_mov_b32_e64 v230, 1\n" \
"v_readlane_b32 s62, %[tlo], 19\n" \
"v_readlane_b32 s63, %[thi], 19\n" \
"v_and_b32_e32 v233, 0x380, v224\n" \
"v_lshlrev_b32_e64 v233, 2, v233\n" \
"buffer_load_dwordx4 v213, %[srdt], s49 offen offset:2048 lds\n" \
"v_mfma_f32_16x16x32_bf16 v[204:207], a[196:199], v[96:99], 0\n" \
"v_add_u32_e32 v221, 0x10000, v227\n" \
"v_mfma_f32_16x16x32_bf16 v[204:207], a[200:203], v[104:107], v[204:207]\n" \
"ds_read_b64_tr_b16 a[196:197], v215 offset:12800\n" \
"v_mfma_f32_16x16x32_bf16 v[204:207], a[204:207], v[112:115], v[204:207]\n" \
"ds_read_b64_tr_b16 a[198:199], v215 offset:14848\n" \
"v_mfma_f32_16x16x32_bf16 v[204:207], a[208:211], v[120:123], v[204:207]\n" \
"ds_read_b64_tr_b16 a[200:201], v227 offset:512\n" \
"v_mfma_f32_16x16x32_bf16 v[156:159], a[212:215], v[96:99], 0\n" \
"ds_read_b64_tr_b16 a[202:203], v227 offset:2560\n" \
"v_mfma_f32_16x16x32_bf16 v[156:159], a[216:219], v[104:107], v[156:159]\n" \
"ds_read_b64_tr_b16 a[204:205], v227 offset:4608\n" \
"v_mfma_f32_16x16x32_bf16 v[156:159], a[220:223], v[112:115], v[156:159]\n" \
"ds_read_b64_tr_b16 a[206:207], v227 offset:6656\n" \
"v_mfma_f32_16x16x32_bf16 v[156:159], a[224:227], v[120:123], v[156:159]\n" \
"s_waitcnt vmcnt(63) lgkmcnt(4)\n" \
"s_nop 0\n" \
"v_mfma_f32_16x16x32_bf16 v[160:163], a[228:231], v[0:3], 0\n" \
"ds_read_b64_tr_b16 a[208:209], v227 offset:8704\n" \
"v_mfma_f32_16x16x32_bf16 v[160:163], a[232:235], v[8:11], v[160:163]\n" \
"ds_read_b64_tr_b16 a[210:211], v227 offset:10752\n" \
"buffer_load_dwordx4 v214, %[srdt], s49 offen offset:3072 lds\n" \
"v_mfma_f32_16x16x32_bf16 v[164:167], a[228:231], v[32:35], 0\n" \
"ds_read_b64_tr_b16 a[212:213], v227 offset:12800\n" \
"v_mfma_f32_16x16x32_bf16 v[164:167], a[232:235], v[40:43], v[164:167]\n" \
"ds_read_b64_tr_b16 a[214:215], v227 offset:14848\n" \
"v_mfma_f32_16x16x32_bf16 v[168:171], a[228:231], v[64:67], 0\n" \
"v_mfma_f32_16x16x32_bf16 v[168:171], a[232:235], v[72:75], v[168:171]\n" \
"v_mfma_f32_16x16x32_bf16 v[172:175], a[228:231], v[96:99], 0\n" \
"ds_read_b64_tr_b16 a[216:217], v215 offset:16384\n" \
"v_mfma_f32_16x16x32_bf16 v[172:175], a[232:235], v[104:107], v[172:175]\n" \
"ds_read_b64_tr_b16 a[218:219], v215 offset:18432\n" \
"v_mfma_f32_16x16x32_bf16 v[160:163], a[236:239], v[16:19], v[160:163]\n" \
"ds_read_b64_tr_b16 a[220:221], v215 offset:20480\n" \
"v_mfma_f32_16x16x32_bf16 v[160:163], a[196:199], v[24:27], v[160:163]\n" \
"ds_read_b64_tr_b16 a[222:223], v215 offset:22528\n" \
"v_mfma_f32_16x16x32_bf16 v[164:167], a[236:239], v[48:51], v[164:167]\n" \
"v_mfma_f32_16x16x32_bf16 v[164:167], a[196:199], v[56:59], v[164:167]\n" \
"v_mfma_f32_16x16x32_bf16 v[168:171], a[236:239], v[80:83], v[168:171]\n" \
"s_waitcnt vmcnt(63) lgkmcnt(0)\n" \
"s_barrier\n" \
"v_mfma_f32_16x16x32_bf16 v[168:171], a[196:199], v[88:91], v[168:171]\n" \
"v_mfma_f32_16x16x32_bf16 v[172:175], a[236:239], v[112:115], v[172:175]\n" \
"ds_read_b64_tr_b16 a[224:225], v215 offset:24576\n" \
"v_mfma_f32_16x16x32_bf16 v[172:175], a[196:199], v[120:123], v[172:175]\n" \
"s_waitcnt vmcnt(63) lgkmcnt(5)\n" \
"s_nop 0\n" \
"v_mfma_f32_16x16x32_bf16 v[176:179], a[200:203], v[0:3], 0\n" \
"ds_read_b64_tr_b16 a[226:227], v215 offset:26624\n" \
"v_mfma_f32_16x16x32_bf16 v[176:179], a[204:207], v[8:11], v[176:179]\n" \
"ds_read_b64_tr_b16 a[228:229], v215 offset:28672\n" \
"s_add_u32 m0, %[m0b], 49152\n" \
"s_cmp_gt_u32 s48, 1\n" \
"s_cselect_b32 s49, 1, 2\n" \
"s_lshl_b32 s49, s49, 18\n" \
"s_or_b32 s49, s49, s59\n" \
"buffer_load_dwordx4 %[wo0], %[srdt], s49 offen offset:0 lds\n" \
"v_mfma_f32_16x16x32_bf16 v[180:183], a[200:203], v[32:35], 0\n" \
"ds_read_b64_tr_b16 a[230:231], v215 offset:30720\n" \
"v_mfma_f32_16x16x32_bf16 v[180:183], a[204:207], v[40:43], v[180:183]\n" \
"v_mfma_f32_16x16x32_bf16 v[184:187], a[200:203], v[64:67], 0\n" \
"v_mfma_f32_16x16x32_bf16 v[184:187], a[204:207], v[72:75], v[184:187]\n" \
"buffer_load_dwordx4 v212, %[srdt], s49 offen offset:1024 lds\n" \
"v_mfma_f32_16x16x32_bf16 v[188:191], a[200:203], v[96:99], 0\n" \
"ds_read_b64_tr_b16 a[232:233], v227 offset:16384\n" \
"v_mfma_f32_16x16x32_bf16 v[188:191], a[204:207], v[104:107], v[188:191]\n" \
"ds_read_b64_tr_b16 a[234:235], v227 offset:18432\n" \
"v_mfma_f32_16x16x32_bf16 v[176:179], a[208:211], v[16:19], v[176:179]\n" \
"ds_read_b64_tr_b16 a[236:237], v227 offset:20480\n" \
"v_mfma_f32_16x16x32_bf16 v[176:179], a[212:215], v[24:27], v[176:179]\n" \
"ds_read_b64_tr_b16 a[238:239], v227 offset:22528\n" \
"v_mfma_f32_16x16x32_bf16 v[180:183], a[208:211], v[48:51], v[180:183]\n" \
"v_mfma_f32_16x16x32_bf16 v[180:183], a[212:215], v[56:59], v[180:183]\n" \
"v_mfma_f32_16x16x32_bf16 v[184:187], a[208:211], v[80:83], v[184:187]\n" \
"v_mfma_f32_16x16x32_bf16 v[184:187], a[212:215], v[88:91], v[184:187]\n" \
"buffer_load_dwordx4 v213, %[srdt], s49 offen offset:2048 lds\n" \
"v_mfma_f32_16x16x32_bf16 v[188:191], a[208:211], v[112:115], v[188:191]\n" \
"ds_read_b64_tr_b16 a[196:197], v227 offset:24576\n" \
"v_mfma_f32_16x16x32_bf16 v[188:191], a[212:215], v[120:123], v[188:191]\n" \
"s_waitcnt vmcnt(63) lgkmcnt(5)\n" \
"s_nop 0\n" \
"v_mfma_f32_16x16x32_bf16 v[4:7], a[216:219], v[0:3], 0\n" \
"ds_read_b64_tr_b16 a[198:199], v227 offset:26624\n" \
"v_mfma_f32_16x16x32_bf16 v[4:7], a[220:223], v[8:11], v[4:7]\n" \
"ds_read_b64_tr_b16 a[200:201], v227 offset:28672\n" \
"v_mfma_f32_16x16x32_bf16 v[36:39], a[216:219], v[32:35], 0\n" \
"ds_read_b64_tr_b16 a[202:203], v227 offset:30720\n" \
"v_mfma_f32_16x16x32_bf16 v[36:39], a[220:223], v[40:43], v[36:39]\n" \
"v_mfma_f32_16x16x32_bf16 v[68:71], a[216:219], v[64:67], 0\n" \
"buffer_load_dwordx4 v214, %[srdt], s49 offen offset:3072 lds\n" \
"v_mfma_f32_16x16x32_bf16 v[68:71], a[220:223], v[72:75], v[68:71]\n" \
"v_mfma_f32_16x16x32_bf16 v[100:103], a[216:219], v[96:99], 0\n" \
"ds_read_b64_tr_b16 a[204:205], v215 offset:16896\n" \
"v_mfma_f32_16x16x32_bf16 v[100:103], a[220:223], v[104:107], v[100:103]\n" \
"ds_read_b64_tr_b16 a[206:207], v215 offset:18944\n" \
"v_mfma_f32_16x16x32_bf16 v[4:7], a[224:227], v[16:19], v[4:7]\n" \
"ds_read_b64_tr_b16 a[208:209], v215 offset:20992\n" \
"v_mfma_f32_16x16x32_bf16 v[4:7], a[228:231], v[24:27], v[4:7]\n" \
"ds_read_b64_tr_b16 a[210:211], v215 offset:23040\n" \
"v_mfma_f32_16x16x32_bf16 v[36:39], a[224:227], v[48:51], v[36:39]\n" \
"v_mfma_f32_16x16x32_bf16 v[36:39], a[228:231], v[56:59], v[36:39]\n" \
"v_mfma_f32_16x16x32_bf16 v[68:71], a[224:227], v[80:83], v[68:71]\n" \
"v_mfma_f32_16x16x32_bf16 v[68:71], a[228:231], v[88:91], v[68:71]\n" \
"v_mfma_f32_16x16x32_bf16 v[100:103], a[224:227], v[112:115], v[100:103]\n" \
"ds_read_b64_tr_b16 a[212:213], v215 offset:25088\n" \
"v_mfma_f32_16x16x32_bf16 v[100:103], a[228:231], v[120:123], v[100:103]\n" \
"s_waitcnt vmcnt(63) lgkmcnt(5)\n" \
"s_nop 0\n" \
"v_mfma_f32_16x16x32_bf16 v[12:15], a[232:235], v[0:3], 0\n" \
"ds_read_b64_tr_b16 a[214:215], v215 offset:27136\n" \
"s_cmp_gt_u32 s48, 0\n" \
"s_cselect_b32 s50, 0, 1\n" \
"s_mul_i32 s50, s50, 262144\n" \
"buffer_load_dwordx4 a[64:67], %[oq0], %[srdz], s50 offen offset:0 sc1\n" \
"v_mfma_f32_16x16x32_bf16 v[12:15], a[236:239], v[8:11], v[12:15]\n" \
"ds_read_b64_tr_b16 a[216:217], v215 offset:29184\n" \
"v_mfma_f32_16x16x32_bf16 v[44:47], a[232:235], v[32:35], 0\n" \
"ds_read_b64_tr_b16 a[218:219], v215 offset:31232\n" \
"buffer_load_dwordx4 a[68:71], %[oq0], %[srdz], s50 offen offset:1024 sc1\n" \
"v_mfma_f32_16x16x32_bf16 v[44:47], a[236:239], v[40:43], v[44:47]\n" \
"v_mfma_f32_16x16x32_bf16 v[76:79], a[232:235], v[64:67], 0\n" \
"buffer_load_dwordx4 a[80:83], v208, %[srdz], s50 offen offset:0 sc1\n" \
"v_mfma_f32_16x16x32_bf16 v[76:79], a[236:239], v[72:75], v[76:79]\n" \
"v_mfma_f32_16x16x32_bf16 v[108:111], a[232:235], v[96:99], 0\n" \
"ds_read_b64_tr_b16 a[220:221], v227 offset:16896\n" \
"buffer_load_dwordx4 a[84:87], v208, %[srdz], s50 offen offset:1024 sc1\n" \
"v_mfma_f32_16x16x32_bf16 v[108:111], a[236:239], v[104:107], v[108:111]\n" \
"ds_read_b64_tr_b16 a[222:223], v227 offset:18944\n" \
"v_mfma_f32_16x16x32_bf16 v[12:15], a[196:199], v[16:19], v[12:15]\n" \
"ds_read_b64_tr_b16 a[224:225], v227 offset:25088\n" \
"buffer_load_dwordx4 a[96:99], v209, %[srdz], s50 offen offset:0 sc1\n" \
"v_mfma_f32_16x16x32_bf16 v[12:15], a[200:203], v[24:27], v[12:15]\n" \
"ds_read_b64_tr_b16 a[226:227], v227 offset:27136\n" \
"v_mfma_f32_16x16x32_bf16 v[44:47], a[196:199], v[48:51], v[44:47]\n" \
"ds_read_b64_tr_b16 a[228:229], v227 offset:20992\n" \
"buffer_load_dwordx4 a[100:103], v209, %[srdz], s50 offen offset:1024 sc1\n" \
"v_mfma_f32_16x16x32_bf16 v[44:47], a[200:203], v[56:59], v[44:47]\n" \
"ds_read_b64_tr_b16 a[230:231], v227 offset:23040\n" \
"v_mfma_f32_16x16x32_bf16 v[76:79], a[196:199], v[80:83], v[76:79]\n" \
"ds_read_b64_tr_b16 a[232:233], v227 offset:29184\n" \
"buffer_load_dwordx4 a[112:115], v210, %[srdz], s50 offen offset:0 sc1\n" \
"v_mfma_f32_16x16x32_bf16 v[76:79], a[200:203], v[88:91], v[76:79]\n" \
"ds_read_b64_tr_b16 a[234:235], v227 offset:31232\n" \
"v_mfma_f32_16x16x32_bf16 v[108:111], a[196:199], v[112:115], v[108:111]\n" \
"ds_read_b64_tr_b16 a[236:237], v215 offset:32768\n" \
"buffer_load_dwordx4 a[116:119], v210, %[srdz], s50 offen offset:1024 sc1\n" \
"v_mfma_f32_16x16x32_bf16 v[108:111], a[200:203], v[120:123], v[108:111]\n" \
"s_waitcnt vmcnt(63) lgkmcnt(13)\n" \
"s_nop 0\n" \
"v_mfma_f32_16x16x32_bf16 v[20:23], a[204:207], v[0:3], 0\n" \
"ds_read_b64_tr_b16 a[238:239], v215 offset:34816\n" \
"buffer_load_dwordx4 a[72:75], %[oq0], %[srdz], s50 offen offset:2048 sc1\n" \
"v_mfma_f32_16x16x32_bf16 v[20:23], a[208:211], v[8:11], v[20:23]\n" \
"ds_read_b64_tr_b16 a[196:197], v215 offset:36864\n" \
"v_mfma_f32_16x16x32_bf16 v[52:55], a[204:207], v[32:35], 0\n" \
"ds_read_b64_tr_b16 a[198:199], v215 offset:38912\n" \
"buffer_load_dwordx4 a[76:79], %[oq0], %[srdz], s50 offen offset:3072 sc1\n" \
"v_mfma_f32_16x16x32_bf16 v[52:55], a[208:211], v[40:43], v[52:55]\n" \
"v_mfma_f32_16x16x32_bf16 v[84:87], a[204:207], v[64:67], 0\n" \
"s_waitcnt vmcnt(63) lgkmcnt(4)\n" \
"s_barrier\n" \
"v_mfma_f32_16x16x32_bf16 v[84:87], a[208:211], v[72:75], v[84:87]\n" \
"buffer_load_dwordx4 a[88:91], v208, %[srdz], s50 offen offset:2048 sc1\n" \
"v_mfma_f32_16x16x32_bf16 v[116:119], a[204:207], v[96:99], 0\n" \
"ds_read_b64_tr_b16 a[200:201], v215 offset:40960\n" \
"v_mfma_f32_16x16x32_bf16 v[116:119], a[208:211], v[104:107], v[116:119]\n" \
"ds_read_b64_tr_b16 a[202:203], v215 offset:43008\n" \
"buffer_load_dwordx4 a[92:95], v208, %[srdz], s50 offen offset:3072 sc1\n" \
"v_mfma_f32_16x16x32_bf16 v[20:23], a[212:215], v[16:19], v[20:23]\n" \
"ds_read_b64_tr_b16 a[204:205], v215 offset:45056\n" \
"v_mfma_f32_16x16x32_bf16 v[20:23], a[216:219], v[24:27], v[20:23]\n" \
"ds_read_b64_tr_b16 a[206:207], v215 offset:47104\n" \
"buffer_load_dwordx4 a[104:107], v209, %[srdz], s50 offen offset:2048 sc1\n" \
"v_mfma_f32_16x16x32_bf16 v[52:55], a[212:215], v[48:51], v[52:55]\n" \
"v_mfma_f32_16x16x32_bf16 v[52:55], a[216:219], v[56:59], v[52:55]\n" \
"buffer_load_dwordx4 a[108:111], v209, %[srdz], s50 offen offset:3072 sc1\n" \
"v_mfma_f32_16x16x32_bf16 v[84:87], a[212:215], v[80:83], v[84:87]\n" \
"v_mfma_f32_16x16x32_bf16 v[84:87], a[216:219], v[88:91], v[84:87]\n" \
"buffer_load_dwordx4 a[120:123], v210, %[srdz], s50 offen offset:2048 sc1\n" \
"v_mfma_f32_16x16x32_bf16 v[116:119], a[212:215], v[112:115], v[116:119]\n" \
"v_mfma_f32_16x16x32_bf16 v[116:119], a[216:219], v[120:123], v[116:119]\n" \
"s_waitcnt vmcnt(63) lgkmcnt(10)\n" \
"s_nop 0\n" \
"v_mfma_f32_16x16x32_bf16 v[28:31], a[220:223], v[0:3], 0\n" \
"buffer_load_dwordx4 a[124:127], v210, %[srdz], s50 offen offset:3072 sc1\n" \
"v_mfma_f32_16x16x32_bf16 v[28:31], a[228:231], v[8:11], v[28:31]\n" \
"s_sub_u32 s61, s48, 5\n" \
"s_cmp_lt_u32 s61, 3\n" \
"s_cselect_b64 vcc, -1, 0\n" \
"s_nop 0\n" \
"v_mfma_f32_16x16x32_bf16 v[60:63], a[220:223], v[32:35], 0\n" \
"s_cbranch_vccz Lg5_%=\n" \
"s_nop 0\n" \
"buffer_store_dwordx4 v[0:3], %[oq0], %[srdz], s60 offen offset:0 sc1\n" \
"Lg5_%=:\n" \
"v_mfma_f32_16x16x32_bf16 v[60:63], a[228:231], v[40:43], v[60:63]\n" \
"v_mfma_f32_16x16x32_bf16 v[92:95], a[220:223], v[64:67], 0\n" \
"s_cmp_gt_u32 s48, 1\n" \
"s_cselect_b32 s50, 1, 2\n" \
"s_mul_i32 s50, s50, 262144\n" \
"buffer_load_dwordx4 a[0:3], %[oq0], %[srdz], s50 offen offset:0 sc1\n" \
"v_mfma_f32_16x16x32_bf16 v[92:95], a[228:231], v[72:75], v[92:95]\n" \
"v_mfma_f32_16x16x32_bf16 v[124:127], a[220:223], v[96:99], 0\n" \
"ds_read_b64_tr_b16 a[208:209], v227 offset:32768\n" \
"v_mfma_f32_16x16x32_bf16 v[124:127], a[228:231], v[104:107], v[124:127]\n" \
"ds_read_b64_tr_b16 a[210:211], v227 offset:34816\n" \
"s_waitcnt vmcnt(15) lgkmcnt(6)\n" \
"s_nop 0\n" \
"v_mfma_f32_16x16x32_bf16 v[128:131], a[236:239], a[64:67], v[192:195]\n" \
"ds_read_b64_tr_b16 a[212:213], v227 offset:36864\n" \
"v_mfma_f32_16x16x32_bf16 v[128:131], a[196:199], a[68:71], v[128:131]\n" \
"ds_read_b64_tr_b16 a[214:215], v227 offset:38912\n" \
"buffer_load_dwordx4 a[4:7], %[oq0], %[srdz], s50 offen offset:1024 sc1\n" \
"s_waitcnt vmcnt(14) lgkmcnt(15)\n" \
"s_nop 0\n" \
"v_mfma_f32_16x16x32_bf16 v[132:135], a[236:239], a[80:83], v[196:199]\n" \
"v_mfma_f32_16x16x32_bf16 v[132:135], a[196:199], a[84:87], v[132:135]\n" \
"buffer_load_dwordx4 a[8:11], %[oq0], %[srdz], s50 offen offset:2048 sc1\n" \
"s_waitcnt vmcnt(13) lgkmcnt(15)\n" \
"s_nop 0\n" \
"v_mfma_f32_16x16x32_bf16 v[136:139], a[236:239], a[96:99], v[200:203]\n" \
"v_mfma_f32_16x16x32_bf16 v[136:139], a[196:199], a[100:103], v[136:139]\n" \
"s_cbranch_vccz Lg6_%=\n" \
"s_nop 0\n" \
"buffer_store_dwordx4 v[8:11], %[oq0], %[srdz], s60 offen offset:1024 sc1\n" \
"Lg6_%=:\n" \
"s_waitcnt vmcnt(13) lgkmcnt(15)\n" \
"s_nop 0\n" \
"s_bitcmp1_b32 0xe0, s48\n" \
"s_cbranch_scc1 Lcw2_%=\n" \
"s_waitcnt vmcnt(11)\n" \
"Lcw2_%=:\n" \
"v_mfma_f32_16x16x32_bf16 v[140:143], a[236:239], a[112:115], v[204:207]\n" \
"ds_read_b64_tr_b16 a[216:217], v227 offset:40960\n" \
"v_mfma_f32_16x16x32_bf16 v[140:143], a[196:199], a[116:119], v[140:143]\n" \
"s_waitcnt vmcnt(11) lgkmcnt(5)\n" \
"s_nop 0\n" \
"s_bitcmp1_b32 0xe0, s48\n" \
"s_cbranch_scc1 Lcw3_%=\n" \
"s_waitcnt vmcnt(9)\n" \
"Lcw3_%=:\n" \
"v_mfma_f32_16x16x32_bf16 v[128:131], a[200:203], a[72:75], v[128:131]\n" \
"ds_read_b64_tr_b16 a[218:219], v227 offset:43008\n" \
"v_mfma_f32_16x16x32_bf16 v[128:131], a[204:207], a[76:79], v[128:131]\n" \
"ds_read_b64_tr_b16 a[220:221], v227 offset:45056\n" \
"buffer_load_dwordx4 a[12:15], %[oq0], %[srdz], s50 offen offset:3072 sc1\n" \
"s_waitcnt vmcnt(10) lgkmcnt(15)\n" \
"s_nop 0\n" \
"s_bitcmp1_b32 0xe0, s48\n" \
"s_cbranch_scc1 Lcw4_%=\n" \
"s_waitcnt vmcnt(8)\n" \
"Lcw4_%=:\n" \
"v_mfma_f32_16x16x32_bf16 v[132:135], a[200:203], a[88:91], v[132:135]\n" \
"ds_read_b64_tr_b16 a[222:223], v227 offset:47104\n" \
"v_mfma_f32_16x16x32_bf16 v[132:135], a[204:207], a[92:95], v[132:135]\n" \
"buffer_load_dwordx4 a[16:19], v208, %[srdz], s50 offen offset:0 sc1\n" \
"s_waitcnt vmcnt(7) lgkmcnt(14)\n" \
"s_nop 0\n" \
"s_bitcmp1_b32 0xe0, s48\n" \
"s_cbranch_scc1 Lcw5_%=\n" \
"s_waitcnt vmcnt(5)\n" \
"Lcw5_%=:\n" \
"v_mfma_f32_16x16x32_bf16 v[136:139], a[200:203], a[104:107], v[136:139]\n" \
"v_mfma_f32_16x16x32_bf16 v[136:139], a[204:207], a[108:111], v[136:139]\n" \
"v_mfma_f32_16x16x32_bf16 v[140:143], a[200:203], a[120:123], v[140:143]\n" \
"ds_read_b64_tr_b16 a[228:229], v215 offset:33280\n" \
"v_mfma_f32_16x16x32_bf16 v[140:143], a[204:207], a[124:127], v[140:143]\n" \
"ds_read_b64_tr_b16 a[230:231], v215 offset:35328\n" \
"v_mfma_f32_16x16x32_bf16 v[28:31], a[224:227], v[16:19], v[28:31]\n" \
"v_mfma_f32_16x16x32_bf16 v[28:31], a[232:235], v[24:27], v[28:31]\n" \
"s_cbranch_vccz Lg7_%=\n" \
"s_nop 0\n" \
"buffer_store_dwordx4 v[16:19], %[oq0], %[srdz], s60 offen offset:2048 sc1\n" \
"Lg7_%=:\n" \
"v_mfma_f32_16x16x32_bf16 v[60:63], a[224:227], v[48:51], v[60:63]\n" \
"v_mfma_f32_16x16x32_bf16 v[60:63], a[232:235], v[56:59], v[60:63]\n" \
"buffer_load_dwordx4 a[20:23], v208, %[srdz], s50 offen offset:1024 sc1\n" \
"v_mfma_f32_16x16x32_bf16 v[92:95], a[224:227], v[80:83], v[92:95]\n" \
"v_mfma_f32_16x16x32_bf16 v[92:95], a[232:235], v[88:91], v[92:95]\n" \
"v_cmp_u_f32_e64 s[44:45], v128, v132\n" \
"buffer_load_dwordx4 a[24:27], v208, %[srdz], s50 offen offset:2048 sc1\n" \
"v_mfma_f32_16x16x32_bf16 v[124:127], a[224:227], v[112:115], v[124:127]\n" \
"v_mfma_f32_16x16x32_bf16 v[124:127], a[232:235], v[120:123], v[124:127]\n" \
"v_cmp_u_f32_e64 s[56:57], v136, v140\n" \
"s_or_b64 s[56:57], s[56:57], s[44:45]\n" \
"s_cbranch_scc1 Lsp1_%=\n" \
"Lbk1_%=:\n" \
"s_waitcnt vmcnt(63) lgkmcnt(2)\n" \
"s_nop 0\n" \
"v_mfma_f32_16x16x32_bf16 v[144:147], a[208:211], a[64:67], v[144:147]\n" \
"ds_read_b64_tr_b16 a[224:225], v215 offset:37376\n" \
"v_mfma_f32_16x16x32_bf16 v[144:147], a[212:215], a[68:71], v[144:147]\n" \
"ds_read_b64_tr_b16 a[226:227], v215 offset:39424\n" \
"buffer_load_dwordx4 a[28:31], v208, %[srdz], s50 offen offset:3072 sc1\n" \
"v_mfma_f32_16x16x32_bf16 v[148:151], a[208:211], a[80:83], v[148:151]\n" \
"ds_read_b64_tr_b16 a[196:197], v215 offset:41472\n" \
"v_mfma_f32_16x16x32_bf16 v[148:151], a[212:215], a[84:87], v[148:151]\n" \
"ds_read_b64_tr_b16 a[198:199], v215 offset:43520\n" \
"v_mfma_f32_16x16x32_bf16 v[152:155], a[208:211], a[96:99], v[152:155]\n" \
"ds_read_b64_tr_b16 a[200:201], v215 offset:45568\n" \
"s_cbranch_vccz Lg8_%=\n" \
"s_nop 0\n" \
"buffer_store_dwordx4 v[24:27], %[oq0], %[srdz], s60 offen offset:3072 sc1\n" \
"Lg8_%=:\n" \
"v_mfma_f32_16x16x32_bf16 v[152:155], a[212:215], a[100:103], v[152:155]\n" \
"ds_read_b64_tr_b16 a[202:203], v215 offset:47616\n" \
"v_mfma_f32_16x16x32_bf16 v[156:159], a[208:211], a[112:115], v[156:159]\n" \
"ds_read_b64_tr_b16 a[204:205], v227 offset:33280\n" \
"buffer_load_dwordx4 a[32:35], v209, %[srdz], s50 offen offset:0 sc1\n" \
"v_mfma_f32_16x16x32_bf16 v[156:159], a[212:215], a[116:119], v[156:159]\n" \
"ds_read_b64_tr_b16 a[206:207], v227 offset:35328\n" \
"v_mfma_f32_16x16x32_bf16 v[144:147], a[216:219], a[72:75], v[144:147]\n" \
"ds_read_b64_tr_b16 a[232:233], v227 offset:37376\n" \
"v_mfma_f32_16x16x32_bf16 v[144:147], a[220:223], a[76:79], v[144:147]\n" \
"ds_read_b64_tr_b16 a[234:235], v227 offset:39424\n" \
"v_mfma_f32_16x16x32_bf16 v[148:151], a[216:219], a[88:91], v[148:151]\n" \
"buffer_load_dwordx4 a[36:39], v209, %[srdz], s50 offen offset:1024 sc1\n" \
"v_mfma_f32_16x16x32_bf16 v[148:151], a[220:223], a[92:95], v[148:151]\n" \
"v_mfma_f32_16x16x32_bf16 v[152:155], a[216:219], a[104:107], v[152:155]\n" \
"buffer_load_dwordx4 a[40:43], v209, %[srdz], s50 offen offset:2048 sc1\n" \
"v_mfma_f32_16x16x32_bf16 v[152:155], a[220:223], a[108:111], v[152:155]\n" \
"v_mfma_f32_16x16x32_bf16 v[156:159], a[216:219], a[120:123], v[156:159]\n" \
"ds_read_b64_tr_b16 a[236:237], v227 offset:41472\n" \
"v_mfma_f32_16x16x32_bf16 v[156:159], a[220:223], a[124:127], v[156:159]\n" \
"s_waitcnt vmcnt(63) lgkmcnt(5)\n" \
"s_nop 0\n" \
"v_mfma_f32_16x16x32_bf16 v[160:163], a[228:231], a[64:67], v[160:163]\n" \
"ds_read_b64_tr_b16 a[238:239], v227 offset:43520\n" \
"s_cbranch_vccz Lg9_%=\n" \
"s_nop 0\n" \
"buffer_store_dwordx4 v[32:35], v208, %[srdz], s60 offen offset:0 sc1\n" \
"Lg9_%=:\n" \
"v_mfma_f32_16x16x32_bf16 v[160:163], a[224:227], a[68:71], v[160:163]\n" \
"ds_read_b64_tr_b16 a[208:209], v227 offset:45568\n" \
"v_mfma_f32_16x16x32_bf16 v[164:167], a[228:231], a[80:83], v[164:167]\n" \
"ds_read_b64_tr_b16 a[210:211], v227 offset:47616\n" \
"buffer_load_dwordx4 a[44:47], v209, %[srdz], s50 offen offset:3072 sc1\n" \
"v_mfma_f32_16x16x32_bf16 v[164:167], a[224:227], a[84:87], v[164:167]\n" \
"v_mfma_f32_16x16x32_bf16 v[168:171], a[228:231], a[96:99], v[168:171]\n" \
"buffer_load_dwordx4 a[48:51], v210, %[srdz], s50 offen offset:0 sc1\n" \
"v_mfma_f32_16x16x32_bf16 v[168:171], a[224:227], a[100:103], v[168:171]\n" \
"v_mfma_f32_16x16x32_bf16 v[172:175], a[228:231], a[112:115], v[172:175]\n" \
"v_mfma_f32_16x16x32_bf16 v[172:175], a[224:227], a[116:119], v[172:175]\n" \
"buffer_load_dwordx4 a[52:55], v210, %[srdz], s50 offen offset:1024 sc1\n" \
"v_mfma_f32_16x16x32_bf16 v[160:163], a[196:199], a[72:75], v[160:163]\n" \
"v_mfma_f32_16x16x32_bf16 v[160:163], a[200:203], a[76:79], v[160:163]\n" \
"v_mfma_f32_16x16x32_bf16 v[164:167], a[196:199], a[88:91], v[164:167]\n" \
"v_mfma_f32_16x16x32_bf16 v[164:167], a[200:203], a[92:95], v[164:167]\n" \
"buffer_load_dwordx4 a[56:59], v210, %[srdz], s50 offen offset:2048 sc1\n" \
"v_mfma_f32_16x16x32_bf16 v[168:171], a[196:199], a[104:107], v[168:171]\n" \
"s_waitcnt vmcnt(55) lgkmcnt(0)\n" \
"s_bitcmp1_b32 0x1f, s48\n" \
"s_cbranch_scc1 Lcw6_%=\n" \
"s_waitcnt vmcnt(44)\n" \
"s_bitcmp1_b32 0xe0, s48\n" \
"s_cbranch_scc1 Lcw6_%=\n" \
"s_waitcnt vmcnt(39)\n" \
"Lcw6_%=:\n" \
"s_barrier\n" \
"s_nop 0\n" \
"v_mfma_f32_16x16x32_bf16 v[168:171], a[200:203], a[108:111], v[168:171]\n" \
"ds_read_b64_tr_b16 a[212:213], v220 offset:32768\n" \
"s_cbranch_vccz Lg10_%=\n" \
"s_nop 0\n" \
"buffer_store_dwordx4 v[40:43], v208, %[srdz], s60 offen offset:1024 sc1\n" \
"Lg10_%=:\n" \
"v_mfma_f32_16x16x32_bf16 v[172:175], a[196:199], a[120:123], v[172:175]\n" \
"ds_read_b64_tr_b16 a[214:215], v220 offset:34816\n" \
"v_mfma_f32_16x16x32_bf16 v[172:175], a[200:203], a[124:127], v[172:175]\n" \
"s_waitcnt vmcnt(63) lgkmcnt(2)\n" \
"s_nop 0\n" \
"v_mfma_f32_16x16x32_bf16 v[176:179], a[204:207], a[64:67], v[176:179]\n" \
"ds_read_b64_tr_b16 a[216:217], v220 offset:36864\n" \
"v_mfma_f32_16x16x32_bf16 v[176:179], a[232:235], a[68:71], v[176:179]\n" \
"ds_read_b64_tr_b16 a[218:219], v220 offset:38912\n" \
"v_mfma_f32_16x16x32_bf16 v[180:183], a[204:207], a[80:83], v[180:183]\n" \
"ds_read_b64_tr_b16 a[220:221], v220 offset:40960\n" \
"buffer_load_dwordx4 a[60:63], v210, %[srdz], s50 offen offset:3072 sc1\n" \
"v_mfma_f32_16x16x32_bf16 v[180:183], a[232:235], a[84:87], v[180:183]\n" \
"ds_read_b64_tr_b16 a[222:223], v220 offset:43008\n" \
"v_mfma_f32_16x16x32_bf16 v[184:187], a[204:207], a[96:99], v[184:187]\n" \
"ds_read_b64_tr_b16 a[228:229], v220 offset:45056\n" \
"v_mfma_f32_16x16x32_bf16 v[184:187], a[232:235], a[100:103], v[184:187]\n" \
"ds_read_b64_tr_b16 a[230:231], v220 offset:47104\n" \
"v_mfma_f32_16x16x32_bf16 v[188:191], a[204:207], a[112:115], v[188:191]\n" \
"ds_read_b64_tr_b16 a[224:225], v221 offset:32768\n" \
"v_mfma_f32_16x16x32_bf16 v[188:191], a[232:235], a[116:119], v[188:191]\n" \
"ds_read_b64_tr_b16 a[226:227], v221 offset:34816\n" \
"s_cbranch_vccz Lg11_%=\n" \
"s_nop 0\n" \
"buffer_store_dwordx4 v[48:51], v208, %[srdz], s60 offen offset:2048 sc1\n" \
"Lg11_%=:\n" \
"v_mfma_f32_16x16x32_bf16 v[176:179], a[236:239], a[72:75], v[176:179]\n" \
"ds_read_b64_tr_b16 a[196:197], v221 offset:36864\n" \
"v_mfma_f32_16x16x32_bf16 v[176:179], a[208:211], a[76:79], v[176:179]\n" \
"ds_read_b64_tr_b16 a[198:199], v221 offset:38912\n" \
"v_mfma_f32_16x16x32_bf16 v[180:183], a[236:239], a[88:91], v[180:183]\n" \
"s_cmp_gt_u32 s48, 2\n" \
"s_cselect_b32 s50, 2, 3\n" \
"s_mul_i32 s50, s50, 262144\n" \
"buffer_load_dwordx4 a[128:131], %[oq0], %[srdz], s50 offen offset:0 sc1\n" \
"v_mfma_f32_16x16x32_bf16 v[180:183], a[208:211], a[92:95], v[180:183]\n" \
"v_mfma_f32_16x16x32_bf16 v[184:187], a[236:239], a[104:107], v[184:187]\n" \
"v_mfma_f32_16x16x32_bf16 v[184:187], a[208:211], a[108:111], v[184:187]\n" \
"v_mfma_f32_16x16x32_bf16 v[188:191], a[236:239], a[120:123], v[188:191]\n" \
"ds_read_b64_tr_b16 a[200:201], v221 offset:40960\n" \
"v_mfma_f32_16x16x32_bf16 v[188:191], a[208:211], a[124:127], v[188:191]\n" \
"s_waitcnt vmcnt(63) lgkmcnt(5)\n" \
"s_nop 0\n" \
"v_mfma_f32_16x16x32_bf16 v[4:7], a[212:215], a[64:67], v[4:7]\n" \
"ds_read_b64_tr_b16 a[202:203], v221 offset:43008\n" \
"v_mfma_f32_16x16x32_bf16 v[4:7], a[216:219], a[68:71], v[4:7]\n" \
"ds_read_b64_tr_b16 a[204:205], v221 offset:45056\n" \
"s_add_u32 m0, %[m0b], 0\n" \
"s_cmp_gt_u32 s48, 2\n" \
"s_cselect_b32 s49, 2, 3\n" \
"s_lshl_b32 s49, s49, 18\n" \
"s_or_b32 s49, s49, s58\n" \
"s_nop 0\n" \
"buffer_load_dwordx4 %[wo0], %[srdt], s49 offen offset:0 lds\n" \
"v_mfma_f32_16x16x32_bf16 v[36:39], a[212:215], a[80:83], v[36:39]\n" \
"ds_read_b64_tr_b16 a[206:207], v221 offset:47104\n" \
"v_mfma_f32_16x16x32_bf16 v[36:39], a[216:219], a[84:87], v[36:39]\n" \
"s_cbranch_vccz Lg12_%=\n" \
"s_nop 0\n" \
"buffer_store_dwordx4 v[56:59], v208, %[srdz], s60 offen offset:3072 sc1\n" \
"Lg12_%=:\n" \
"v_mfma_f32_16x16x32_bf16 v[68:71], a[212:215], a[96:99], v[68:71]\n" \
"v_mfma_f32_16x16x32_bf16 v[68:71], a[216:219], a[100:103], v[68:71]\n" \
"buffer_load_dwordx4 a[132:135], %[oq0], %[srdz], s50 offen offset:1024 sc1\n" \
"v_mfma_f32_16x16x32_bf16 v[100:103], a[212:215], a[112:115], v[100:103]\n" \
"ds_read_b64_tr_b16 a[232:233], v220 offset:33280\n" \
"v_mfma_f32_16x16x32_bf16 v[100:103], a[216:219], a[116:119], v[100:103]\n" \
"ds_read_b64_tr_b16 a[234:235], v220 offset:35328\n" \
"v_mfma_f32_16x16x32_bf16 v[4:7], a[220:223], a[72:75], v[4:7]\n" \
"ds_read_b64_tr_b16 a[236:237], v220 offset:37376\n" \
"v_mfma_f32_16x16x32_bf16 v[4:7], a[228:231], a[76:79], v[4:7]\n" \
"ds_read_b64_tr_b16 a[238:239], v220 offset:39424\n" \
"buffer_load_dwordx4 a[136:139], %[oq0], %[srdz], s50 offen offset:2048 sc1\n" \
"v_mfma_f32_16x16x32_bf16 v[36:39], a[220:223], a[88:91], v[36:39]\n" \
"v_mfma_f32_16x16x32_bf16 v[36:39], a[228:231], a[92:95], v[36:39]\n" \
"v_mfma_f32_16x16x32_bf16 v[68:71], a[220:223], a[104:107], v[68:71]\n" \
"v_mfma_f32_16x16x32_bf16 v[68:71], a[228:231], a[108:111], v[68:71]\n" \
"buffer_load_dwordx4 a[140:143], %[oq0], %[srdz], s50 offen offset:3072 sc1\n" \
"v_mfma_f32_16x16x32_bf16 v[100:103], a[220:223], a[120:123], v[100:103]\n" \
"ds_read_b64_tr_b16 a[208:209], v220 offset:41472\n" \
"v_mfma_f32_16x16x32_bf16 v[100:103], a[228:231], a[124:127], v[100:103]\n" \
"s_waitcnt vmcnt(63) lgkmcnt(5)\n" \
"s_nop 0\n" \
"v_mfma_f32_16x16x32_bf16 v[12:15], a[224:227], a[64:67], v[12:15]\n" \
".if . - adaln_body != 45056\n" \
".error \"size model\"\n" \
".endif\n" \
"Lk10_%=:\n" \
"s_cbranch_execz Lk11_%=\n" \
"s_nop 0\n" \
"ds_read_b64_tr_b16 a[210:211], v220 offset:43520\n" \
"s_cbranch_vccz Lg13_%=\n" \
"s_nop 0\n" \
"buffer_store_dwordx4 v[64:67], v209, %[srdz], s60 offen offset:0 sc1\n" \
"Lg13_%=:\n" \
"v_mfma_f32_16x16x32_bf16 v[12:15], a[196:199], a[68:71], v[12:15]\n" \
"ds_read_b64_tr_b16 a[212:213], v220 offset:45568\n" \
"v_mfma_f32_16x16x32_bf16 v[44:47], a[224:227], a[80:83], v[44:47]\n" \
"ds_read_b64_tr_b16 a[214:215], v220 offset:47616\n" \
"buffer_load_dwordx4 v212, %[srdt], s49 offen offset:1024 lds\n" \
"v_mfma_f32_16x16x32_bf16 v[44:47], a[196:199], a[84:87], v[44:47]\n" \
"v_mfma_f32_16x16x32_bf16 v[76:79], a[224:227], a[96:99], v[76:79]\n" \
"v_mfma_f32_16x16x32_bf16 v[76:79], a[196:199], a[100:103], v[76:79]\n" \
"buffer_load_dwordx4 a[144:147], v208, %[srdz], s50 offen offset:0 sc1\n" \
"v_mfma_f32_16x16x32_bf16 v[108:111], a[224:227], a[112:115], v[108:111]\n" \
"ds_read_b64_tr_b16 a[216:217], v221 offset:33280\n" \
"v_mfma_f32_16x16x32_bf16 v[108:111], a[196:199], a[116:119], v[108:111]\n" \
"ds_read_b64_tr_b16 a[218:219], v221 offset:35328\n" \
"v_mfma_f32_16x16x32_bf16 v[12:15], a[200:203], a[72:75], v[12:15]\n" \
"ds_read_b64_tr_b16 a[220:221], v221 offset:41472\n" \
"v_mfma_f32_16x16x32_bf16 v[12:15], a[204:207], a[76:79], v[12:15]\n" \
"ds_read_b64_tr_b16 a[222:223], v221 offset:43520\n" \
"v_mfma_f32_16x16x32_bf16 v[44:47], a[200:203], a[88:91], v[44:47]\n" \
"ds_read_b64_tr_b16 a[228:229], v221 offset:37376\n" \
"s_cbranch_vccz Lg14_%=\n" \
"s_nop 0\n" \
"buffer_store_dwordx4 v[72:75], v209, %[srdz], s60 offen offset:1024 sc1\n" \
"Lg14_%=:\n" \
"v_mfma_f32_16x16x32_bf16 v[44:47], a[204:207], a[92:95], v[44:47]\n" \
"ds_read_b64_tr_b16 a[230:231], v221 offset:39424\n" \
"v_mfma_f32_16x16x32_bf16 v[76:79], a[200:203], a[104:107], v[76:79]\n" \
"ds_read_b64_tr_b16 a[224:225], v221 offset:45568\n" \
"v_mfma_f32_16x16x32_bf16 v[76:79], a[204:207], a[108:111], v[76:79]\n" \
"ds_read_b64_tr_b16 a[226:227], v221 offset:47616\n" \
"v_mfma_f32_16x16x32_bf16 v[108:111], a[200:203], a[120:123], v[108:111]\n" \
"buffer_load_dwordx4 a[148:151], v208, %[srdz], s50 offen offset:1024 sc1\n" \
"v_mfma_f32_16x16x32_bf16 v[108:111], a[204:207], a[124:127], v[108:111]\n" \
"s_waitcnt vmcnt(63) lgkmcnt(12)\n" \
"s_nop 0\n" \
"v_mfma_f32_16x16x32_bf16 v[20:23], a[232:235], a[64:67], v[20:23]\n" \
"v_mfma_f32_16x16x32_bf16 v[20:23], a[236:239], a[68:71], v[20:23]\n" \
"buffer_load_dwordx4 v213, %[srdt], s49 offen offset:2048 lds\n" \
"v_mfma_f32_16x16x32_bf16 v[52:55], a[232:235], a[80:83], v[52:55]\n" \
"v_mfma_f32_16x16x32_bf16 v[52:55], a[236:239], a[84:87], v[52:55]\n" \
"buffer_load_dwordx4 a[152:155], v208, %[srdz], s50 offen offset:2048 sc1\n" \
"v_mfma_f32_16x16x32_bf16 v[84:87], a[232:235], a[96:99], v[84:87]\n" \
"s_waitcnt vmcnt(56) lgkmcnt(0)\n" \
"s_nop 0\n" \
"s_bitcmp1_b32 0xe0, s48\n" \
"s_cbranch_scc1 Lcw7_%=\n" \
"s_waitcnt vmcnt(46)\n" \
"Lcw7_%=:\n" \
"s_barrier\n" \
"s_nop 0\n" \
"v_mfma_f32_16x16x32_bf16 v[84:87], a[236:239], a[100:103], v[84:87]\n" \
"ds_read_b64_tr_b16 a[196:197], v220 offset:49152\n" \
"v_mfma_f32_16x16x32_bf16 v[116:119], a[232:235], a[112:115], v[116:119]\n" \
"ds_read_b64_tr_b16 a[198:199], v220 offset:51200\n" \
"s_cbranch_vccz Lg15_%=\n" \
"s_nop 0\n" \
"buffer_store_dwordx4 v[80:83], v209, %[srdz], s60 offen offset:2048 sc1\n" \
"Lg15_%=:\n" \
"v_mfma_f32_16x16x32_bf16 v[116:119], a[236:239], a[116:119], v[116:119]\n" \
"ds_read_b64_tr_b16 a[200:201], v220 offset:53248\n" \
"v_mfma_f32_16x16x32_bf16 v[20:23], a[208:211], a[72:75], v[20:23]\n" \
"ds_read_b64_tr_b16 a[202:203], v220 offset:55296\n" \
"v_mfma_f32_16x16x32_bf16 v[20:23], a[212:215], a[76:79], v[20:23]\n" \
"ds_read_b64_tr_b16 a[204:205], v220 offset:57344\n" \
"v_mfma_f32_16x16x32_bf16 v[52:55], a[208:211], a[88:91], v[52:55]\n" \
"ds_read_b64_tr_b16 a[206:207], v220 offset:59392\n" \
"v_mfma_f32_16x16x32_bf16 v[52:55], a[212:215], a[92:95], v[52:55]\n" \
"ds_read_b64_tr_b16 a[232:233], v220 offset:61440\n" \
"v_mfma_f32_16x16x32_bf16 v[84:87], a[208:211], a[104:107], v[84:87]\n" \
"ds_read_b64_tr_b16 a[234:235], v220 offset:63488\n" \
"v_mfma_f32_16x16x32_bf16 v[84:87], a[212:215], a[108:111], v[84:87]\n" \
"buffer_load_dwordx4 a[156:159], v208, %[srdz], s50 offen offset:3072 sc1\n" \
"v_mfma_f32_16x16x32_bf16 v[116:119], a[208:211], a[120:123], v[116:119]\n" \
"v_mfma_f32_16x16x32_bf16 v[116:119], a[212:215], a[124:127], v[116:119]\n" \
"s_waitcnt vmcnt(19) lgkmcnt(4)\n" \
"s_nop 0\n" \
"s_bitcmp1_b32 0xe0, s48\n" \
"s_cbranch_scc1 Lcw8_%=\n" \
"s_waitcnt vmcnt(13)\n" \
"Lcw8_%=:\n" \
"v_mfma_f32_16x16x32_bf16 v[28:31], a[216:219], a[64:67], v[28:31]\n" \
"v_mfma_f32_16x16x32_bf16 v[28:31], a[228:231], a[68:71], v[28:31]\n" \
"s_cbranch_vccz Lg16_%=\n" \
"s_nop 0\n" \
"buffer_store_dwordx4 v[88:91], v209, %[srdz], s60 offen offset:3072 sc1\n" \
"Lg16_%=:\n" \
"v_mfma_f32_16x16x32_bf16 v[60:63], a[216:219], a[80:83], v[60:63]\n" \
"v_mfma_f32_16x16x32_bf16 v[60:63], a[228:231], a[84:87], v[60:63]\n" \
"buffer_load_dwordx4 v214, %[srdt], s49 offen offset:3072 lds\n" \
"v_mfma_f32_16x16x32_bf16 v[92:95], a[216:219], a[96:99], v[92:95]\n" \
"v_mfma_f32_16x16x32_bf16 v[92:95], a[228:231], a[100:103], v[92:95]\n" \
"buffer_load_dwordx4 a[160:163], v209, %[srdz], s50 offen offset:0 sc1\n" \
"v_mfma_f32_16x16x32_bf16 v[124:127], a[216:219], a[112:115], v[124:127]\n" \
"ds_read_b64_tr_b16 a[236:237], v221 offset:49152\n" \
"v_mfma_f32_16x16x32_bf16 v[124:127], a[228:231], a[116:119], v[124:127]\n" \
"ds_read_b64_tr_b16 a[238:239], v221 offset:51200\n" \
"v_mfma_f32_16x16x32_bf16 v[192:195], a[196:199], a[0:3], v[128:131]\n" \
"ds_read_b64_tr_b16 a[208:209], v221 offset:53248\n" \
"v_mfma_f32_16x16x32_bf16 v[192:195], a[200:203], a[4:7], v[192:195]\n" \
"ds_read_b64_tr_b16 a[210:211], v221 offset:55296\n" \
"v_mfma_f32_16x16x32_bf16 v[196:199], a[196:199], a[16:19], v[132:135]\n" \
"buffer_load_dwordx4 a[164:167], v209, %[srdz], s50 offen offset:1024 sc1\n" \
"v_mfma_f32_16x16x32_bf16 v[196:199], a[200:203], a[20:23], v[196:199]\n" \
"v_mfma_f32_16x16x32_bf16 v[200:203], a[196:199], a[32:35], v[136:139]\n" \
"v_mfma_f32_16x16x32_bf16 v[200:203], a[200:203], a[36:39], v[200:203]\n" \
"s_cbranch_vccz Lg17_%=\n" \
"s_nop 0\n" \
"buffer_store_dwordx4 v[96:99], v210, %[srdz], s60 offen offset:0 sc1\n" \
"Lg17_%=:\n" \
"v_mfma_f32_16x16x32_bf16 v[204:207], a[196:199], a[48:51], v[140:143]\n" \
"ds_read_b64_tr_b16 a[212:213], v221 offset:57344\n" \
"v_mfma_f32_16x16x32_bf16 v[204:207], a[200:203], a[52:55], v[204:207]\n" \
"s_waitcnt vmcnt(21) lgkmcnt(5)\n" \
"s_nop 0\n" \
"s_bitcmp1_b32 0xe0, s48\n" \
"s_cbranch_scc1 Lcw9_%=\n" \
"s_waitcnt vmcnt(14)\n" \
"Lcw9_%=:\n" \
"v_mfma_f32_16x16x32_bf16 v[192:195], a[204:207], a[8:11], v[192:195]\n" \
"ds_read_b64_tr_b16 a[214:215], v221 offset:59392\n" \
"v_mfma_f32_16x16x32_bf16 v[192:195], a[232:235], a[12:15], v[192:195]\n" \
"ds_read_b64_tr_b16 a[216:217], v221 offset:61440\n" \
"v_mfma_f32_16x16x32_bf16 v[196:199], a[204:207], a[24:27], v[196:199]\n" \
"ds_read_b64_tr_b16 a[218:219], v221 offset:63488\n" \
"v_mfma_f32_16x16x32_bf16 v[196:199], a[232:235], a[28:31], v[196:199]\n" \
"buffer_load_dwordx4 a[168:171], v209, %[srdz], s50 offen offset:2048 sc1\n" \
"v_mfma_f32_16x16x32_bf16 v[200:203], a[204:207], a[40:43], v[200:203]\n" \
"v_mfma_f32_16x16x32_bf16 v[200:203], a[232:235], a[44:47], v[200:203]\n" \
"v_mfma_f32_16x16x32_bf16 v[204:207], a[204:207], a[56:59], v[204:207]\n" \
"ds_read_b64_tr_b16 a[228:229], v220 offset:49664\n" \
"v_mfma_f32_16x16x32_bf16 v[204:207], a[232:235], a[60:63], v[204:207]\n" \
"ds_read_b64_tr_b16 a[230:231], v220 offset:51712\n" \
"v_mfma_f32_16x16x32_bf16 v[28:31], a[220:223], a[72:75], v[28:31]\n" \
"v_mfma_f32_16x16x32_bf16 v[28:31], a[224:227], a[76:79], v[28:31]\n" \
"s_cbranch_vccz Lg18_%=\n" \
"s_nop 0\n" \
"buffer_store_dwordx4 v[104:107], v210, %[srdz], s60 offen offset:1024 sc1\n" \
"Lg18_%=:\n" \
"v_mfma_f32_16x16x32_bf16 v[60:63], a[220:223], a[88:91], v[60:63]\n" \
"v_mfma_f32_16x16x32_bf16 v[60:63], a[224:227], a[92:95], v[60:63]\n" \
"buffer_load_dwordx4 a[172:175], v209, %[srdz], s50 offen offset:3072 sc1\n" \
"v_mfma_f32_16x16x32_bf16 v[92:95], a[220:223], a[104:107], v[92:95]\n" \
"v_mfma_f32_16x16x32_bf16 v[92:95], a[224:227], a[108:111], v[92:95]\n" \
"v_cmp_u_f32_e64 s[44:45], v192, v196\n" \
"v_mfma_f32_16x16x32_bf16 v[124:127], a[220:223], a[120:123], v[124:127]\n" \
"s_add_u32 m0, %[m0b], 16384\n" \
"s_cmp_gt_u32 s48, 2\n" \
"s_cselect_b32 s49, 2, 3\n" \
"s_lshl_b32 s49, s49, 18\n" \
"s_or_b32 s49, s49, s59\n" \
"buffer_load_dwordx4 %[wo0], %[srdt], s49 offen offset:0 lds\n" \
"v_mfma_f32_16x16x32_bf16 v[124:127], a[224:227], a[124:127], v[124:127]\n" \
"v_cmp_u_f32_e64 s[56:57], v200, v204\n" \
"s_or_b64 s[56:57], s[56:57], s[44:45]\n" \
"s_cbranch_scc1 Lsp2_%=\n" \
"Lbk2_%=:\n" \
"s_waitcnt vmcnt(63) lgkmcnt(2)\n" \
"s_nop 0\n" \
"v_mfma_f32_16x16x32_bf16 v[144:147], a[236:239], a[0:3], v[144:147]\n" \
"ds_read_b64_tr_b16 a[220:221], v220 offset:53760\n" \
"v_mfma_f32_16x16x32_bf16 v[144:147], a[208:211], a[4:7], v[144:147]\n" \
"ds_read_b64_tr_b16 a[222:223], v220 offset:55808\n" \
"v_mfma_f32_16x16x32_bf16 v[148:151], a[236:239], a[16:19], v[148:151]\n" \
"ds_read_b64_tr_b16 a[196:197], v220 offset:57856\n" \
"buffer_load_dwordx4 a[176:179], v210, %[srdz], s50 offen offset:0 sc1\n" \
"v_mfma_f32_16x16x32_bf16 v[148:151], a[208:211], a[20:23], v[148:151]\n" \
"ds_read_b64_tr_b16 a[198:199], v220 offset:59904\n" \
"v_mfma_f32_16x16x32_bf16 v[152:155], a[236:239], a[32:35], v[152:155]\n" \
"ds_read_b64_tr_b16 a[200:201], v220 offset:61952\n" \
"s_cbranch_vccz Lg19_%=\n" \
"s_nop 0\n" \
"buffer_store_dwordx4 v[112:115], v210, %[srdz], s60 offen offset:2048 sc1\n" \
"Lg19_%=:\n" \
"v_mfma_f32_16x16x32_bf16 v[152:155], a[208:211], a[36:39], v[152:155]\n" \
"ds_read_b64_tr_b16 a[202:203], v220 offset:64000\n" \
"v_mfma_f32_16x16x32_bf16 v[156:159], a[236:239], a[48:51], v[156:159]\n" \
"ds_read_b64_tr_b16 a[204:205], v221 offset:49664\n" \
"v_mfma_f32_16x16x32_bf16 v[156:159], a[208:211], a[52:55], v[156:159]\n" \
"ds_read_b64_tr_b16 a[206:207], v221 offset:51712\n" \
"v_mfma_f32_16x16x32_bf16 v[144:147], a[212:215], a[8:11], v[144:147]\n" \
"ds_read_b64_tr_b16 a[224:225], v221 offset:53760\n" \
"v_mfma_f32_16x16x32_bf16 v[144:147], a[216:219], a[12:15], v[144:147]\n" \
"ds_read_b64_tr_b16 a[226:227], v221 offset:55808\n" \
"buffer_load_dwordx4 a[180:183], v210, %[srdz], s50 offen offset:1024 sc1\n" \
"v_mfma_f32_16x16x32_bf16 v[148:151], a[212:215], a[24:27], v[148:151]\n" \
"v_mfma_f32_16x16x32_bf16 v[148:151], a[216:219], a[28:31], v[148:151]\n" \
"v_mfma_f32_16x16x32_bf16 v[152:155], a[212:215], a[40:43], v[152:155]\n" \
"v_mfma_f32_16x16x32_bf16 v[152:155], a[216:219], a[44:47], v[152:155]\n" \
"buffer_load_dwordx4 v212, %[srdt], s49 offen offset:1024 lds\n" \
"v_mfma_f32_16x16x32_bf16 v[156:159], a[212:215], a[56:59], v[156:159]\n" \
"ds_read_b64_tr_b16 a[232:233], v221 offset:57856\n" \
"v_mfma_f32_16x16x32_bf16 v[156:159], a[216:219], a[60:63], v[156:159]\n" \
"s_waitcnt vmcnt(63) lgkmcnt(5)\n" \
"s_nop 0\n" \
"v_mfma_f32_16x16x32_bf16 v[160:163], a[228:231], a[0:3], v[160:163]\n" \
"ds_read_b64_tr_b16 a[234:235], v221 offset:59904\n" \
"s_cbranch_vccz Lg20_%=\n" \
"s_nop 0\n" \
"buffer_store_dwordx4 v[120:123], v210, %[srdz], s60 offen offset:3072 sc1\n" \
"Lg20_%=:\n" \
"v_mfma_f32_16x16x32_bf16 v[160:163], a[220:223], a[4:7], v[160:163]\n" \
"ds_read_b64_tr_b16 a[236:237], v221 offset:61952\n" \
"v_mfma_f32_16x16x32_bf16 v[164:167], a[228:231], a[16:19], v[164:167]\n" \
"ds_read_b64_tr_b16 a[238:239], v221 offset:64000\n" \
"buffer_load_dwordx4 a[184:187], v210, %[srdz], s50 offen offset:2048 sc1\n" \
"v_mfma_f32_16x16x32_bf16 v[164:167], a[220:223], a[20:23], v[164:167]\n" \
"v_mfma_f32_16x16x32_bf16 v[168:171], a[228:231], a[32:35], v[168:171]\n" \
"buffer_load_dwordx4 a[188:191], v210, %[srdz], s50 offen offset:3072 sc1\n" \
"v_mfma_f32_16x16x32_bf16 v[168:171], a[220:223], a[36:39], v[168:171]\n" \
"v_mfma_f32_16x16x32_bf16 v[172:175], a[228:231], a[48:51], v[172:175]\n" \
"v_mfma_f32_16x16x32_bf16 v[172:175], a[220:223], a[52:55], v[172:175]\n" \
"v_mfma_f32_16x16x32_bf16 v[160:163], a[196:199], a[8:11], v[160:163]\n" \
"v_mfma_f32_16x16x32_bf16 v[160:163], a[200:203], a[12:15], v[160:163]\n" \
"v_mfma_f32_16x16x32_bf16 v[164:167], a[196:199], a[24:27], v[164:167]\n" \
"v_mfma_f32_16x16x32_bf16 v[164:167], a[200:203], a[28:31], v[164:167]\n" \
"v_mfma_f32_16x16x32_bf16 v[168:171], a[196:199], a[40:43], v[168:171]\n" \
"s_waitcnt vmcnt(63) lgkmcnt(0)\n" \
"s_nop 0\n" \
"s_bitcmp1_b32 0xe0, s48\n" \
"s_cbranch_scc1 Lcw10_%=\n" \
"s_waitcnt vmcnt(54)\n" \
"Lcw10_%=:\n" \
"s_barrier\n" \
"s_nop 0\n" \
"v_mfma_f32_16x16x32_bf16 v[168:171], a[200:203], a[44:47], v[168:171]\n" \
"ds_read_b64_tr_b16 a[208:209], v215 offset:49152\n" \
"v_mfma_f32_16x16x32_bf16 v[172:175], a[196:199], a[56:59], v[172:175]\n" \
"ds_read_b64_tr_b16 a[210:211], v215 offset:51200\n" \
"v_mfma_f32_16x16x32_bf16 v[172:175], a[200:203], a[60:63], v[172:175]\n" \
"s_waitcnt vmcnt(63) lgkmcnt(2)\n" \
"s_nop 0\n" \
"v_mfma_f32_16x16x32_bf16 v[176:179], a[204:207], a[0:3], v[176:179]\n" \
"ds_read_b64_tr_b16 a[212:213], v215 offset:53248\n" \
"global_load_dword v237, %[vct], %[pct]\n" \
"v_mfma_f32_16x16x32_bf16 v[176:179], a[224:227], a[4:7], v[176:179]\n" \
"ds_read_b64_tr_b16 a[214:215], v215 offset:55296\n" \
"v_mfma_f32_16x16x32_bf16 v[180:183], a[204:207], a[16:19], v[180:183]\n" \
"ds_read_b64_tr_b16 a[216:217], v215 offset:57344\n" \
"buffer_load_dwordx4 v213, %[srdt], s49 offen offset:2048 lds\n" \
"v_mfma_f32_16x16x32_bf16 v[180:183], a[224:227], a[20:23], v[180:183]\n" \
"ds_read_b64_tr_b16 a[218:219], v215 offset:59392\n" \
"v_mfma_f32_16x16x32_bf16 v[184:187], a[204:207], a[32:35], v[184:187]\n" \
"ds_read_b64_tr_b16 a[228:229], v215 offset:61440\n" \
"v_mfma_f32_16x16x32_bf16 v[184:187], a[224:227], a[36:39], v[184:187]\n" \
"ds_read_b64_tr_b16 a[230:231], v215 offset:63488\n" \
"v_mfma_f32_16x16x32_bf16 v[188:191], a[204:207], a[48:51], v[188:191]\n" \
"ds_read_b64_tr_b16 a[220:221], v227 offset:49152\n" \
"v_mfma_f32_16x16x32_bf16 v[188:191], a[224:227], a[52:55], v[188:191]\n" \
"ds_read_b64_tr_b16 a[222:223], v227 offset:51200\n" \
"v_mfma_f32_16x16x32_bf16 v[176:179], a[232:235], a[8:11], v[176:179]\n" \
"ds_read_b64_tr_b16 a[196:197], v227 offset:53248\n" \
"v_mfma_f32_16x16x32_bf16 v[176:179], a[236:239], a[12:15], v[176:179]\n" \
"ds_read_b64_tr_b16 a[198:199], v227 offset:55296\n" \
"s_cmp_gt_u32 s48, 3\n" \
"s_cselect_b32 s50, 3, 4\n" \
"s_mul_i32 s50, s50, 262144\n" \
"buffer_load_dwordx4 a[64:67], %[oq0], %[srdz], s50 offen offset:0 sc1\n" \
"v_mfma_f32_16x16x32_bf16 v[180:183], a[232:235], a[24:27], v[180:183]\n" \
"v_mfma_f32_16x16x32_bf16 v[180:183], a[236:239], a[28:31], v[180:183]\n" \
"buffer_load_dwordx4 v214, %[srdt], s49 offen offset:3072 lds\n" \
"v_mfma_f32_16x16x32_bf16 v[184:187], a[232:235], a[40:43], v[184:187]\n" \
"v_mfma_f32_16x16x32_bf16 v[184:187], a[236:239], a[44:47], v[184:187]\n" \
"buffer_load_dwordx4 a[68:71], %[oq0], %[srdz], s50 offen offset:1024 sc1\n" \
"v_mfma_f32_16x16x32_bf16 v[188:191], a[232:235], a[56:59], v[188:191]\n" \
"ds_read_b64_tr_b16 a[200:201], v227 offset:57344\n" \
"v_mfma_f32_16x16x32_bf16 v[188:191], a[236:239], a[60:63], v[188:191]\n" \
"s_waitcnt vmcnt(63) lgkmcnt(5)\n" \
"s_nop 0\n" \
"v_mfma_f32_16x16x32_bf16 v[4:7], a[208:211], a[0:3], v[4:7]\n" \
"ds_read_b64_tr_b16 a[202:203], v227 offset:59392\n" \
"v_mfma_f32_16x16x32_bf16 v[4:7], a[212:215], a[4:7], v[4:7]\n" \
"ds_read_b64_tr_b16 a[204:205], v227 offset:61440\n" \
"v_mfma_f32_16x16x32_bf16 v[36:39], a[208:211], a[16:19], v[36:39]\n" \
"ds_read_b64_tr_b16 a[206:207], v227 offset:63488\n" \
"v_mfma_f32_16x16x32_bf16 v[36:39], a[212:215], a[20:23], v[36:39]\n" \
"v_mfma_f32_16x16x32_bf16 v[68:71], a[208:211], a[32:35], v[68:71]\n" \
"buffer_load_dwordx4 a[72:75], %[oq0], %[srdz], s50 offen offset:2048 sc1\n" \
"v_mfma_f32_16x16x32_bf16 v[68:71], a[212:215], a[36:39], v[68:71]\n" \
"v_mfma_f32_16x16x32_bf16 v[100:103], a[208:211], a[48:51], v[100:103]\n" \
"ds_read_b64_tr_b16 a[224:225], v215 offset:49664\n" \
"v_mfma_f32_16x16x32_bf16 v[100:103], a[212:215], a[52:55], v[100:103]\n" \
"ds_read_b64_tr_b16 a[226:227], v215 offset:51712\n" \
"v_mfma_f32_16x16x32_bf16 v[4:7], a[216:219], a[8:11], v[4:7]\n" \
"ds_read_b64_tr_b16 a[232:233], v215 offset:53760\n" \
"v_mfma_f32_16x16x32_bf16 v[4:7], a[228:231], a[12:15], v[4:7]\n" \
"ds_read_b64_tr_b16 a[234:235], v215 offset:55808\n" \
"v_mfma_f32_16x16x32_bf16 v[36:39], a[216:219], a[24:27], v[36:39]\n" \
"v_mfma_f32_16x16x32_bf16 v[36:39], a[228:231], a[28:31], v[36:39]\n" \
"buffer_load_dwordx4 a[76:79], %[oq0], %[srdz], s50 offen offset:3072 sc1\n" \
"v_mfma_f32_16x16x32_bf16 v[68:71], a[216:219], a[40:43], v[68:71]\n" \
"v_mfma_f32_16x16x32_bf16 v[68:71], a[228:231], a[44:47], v[68:71]\n" \
"v_mfma_f32_16x16x32_bf16 v[100:103], a[216:219], a[56:59], v[100:103]\n" \
"ds_read_b64_tr_b16 a[236:237], v215 offset:57856\n" \
"v_mfma_f32_16x16x32_bf16 v[100:103], a[228:231], a[60:63], v[100:103]\n" \
"s_waitcnt vmcnt(63) lgkmcnt(5)\n" \
"s_nop 0\n" \
"v_mfma_f32_16x16x32_bf16 v[12:15], a[220:223], a[0:3], v[12:15]\n" \
"ds_read_b64_tr_b16 a[238:239], v215 offset:59904\n" \
"v_mfma_f32_16x16x32_bf16 v[12:15], a[196:199], a[4:7], v[12:15]\n" \
"ds_read_b64_tr_b16 a[208:209], v215 offset:61952\n" \
"v_mfma_f32_16x16x32_bf16 v[44:47], a[220:223], a[16:19], v[44:47]\n" \
"ds_read_b64_tr_b16 a[210:211], v215 offset:64000\n" \
"v_mfma_f32_16x16x32_bf16 v[44:47], a[196:199], a[20:23], v[44:47]\n" \
"buffer_load_dwordx4 a[80:83], v208, %[srdz], s50 offen offset:0 sc1\n" \
"v_mfma_f32_16x16x32_bf16 v[76:79], a[220:223], a[32:35], v[76:79]\n" \
"v_mfma_f32_16x16x32_bf16 v[76:79], a[196:199], a[36:39], v[76:79]\n" \
"s_add_u32 m0, %[m0b], 32768\n" \
"s_cmp_gt_u32 s48, 3\n" \
"s_cselect_b32 s49, 3, 4\n" \
"s_lshl_b32 s49, s49, 18\n" \
"s_or_b32 s49, s49, s58\n" \
"buffer_load_dwordx4 %[wo0], %[srdt], s49 offen offset:0 lds\n" \
"v_mfma_f32_16x16x32_bf16 v[108:111], a[220:223], a[48:51], v[108:111]\n" \
"ds_read_b64_tr_b16 a[212:213], v227 offset:49664\n" \
"v_mfma_f32_16x16x32_bf16 v[108:111], a[196:199], a[52:55], v[108:111]\n" \
"ds_read_b64_tr_b16 a[214:215], v227 offset:51712\n" \
"v_mfma_f32_16x16x32_bf16 v[12:15], a[200:203], a[8:11], v[12:15]\n" \
"ds_read_b64_tr_b16 a[216:217], v227 offset:57856\n" \
"v_mfma_f32_16x16x32_bf16 v[12:15], a[204:207], a[12:15], v[12:15]\n" \
"ds_read_b64_tr_b16 a[218:219], v227 offset:59904\n" \
"v_mfma_f32_16x16x32_bf16 v[44:47], a[200:203], a[24:27], v[44:47]\n" \
"ds_read_b64_tr_b16 a[228:229], v227 offset:53760\n" \
"v_mfma_f32_16x16x32_bf16 v[44:47], a[204:207], a[28:31], v[44:47]\n" \
"ds_read_b64_tr_b16 a[230:231], v227 offset:55808\n" \
"v_mfma_f32_16x16x32_bf16 v[76:79], a[200:203], a[40:43], v[76:79]\n" \
"ds_read_b64_tr_b16 a[220:221], v227 offset:61952\n" \
"buffer_load_dwordx4 a[84:87], v208, %[srdz], s50 offen offset:1024 sc1\n" \
"v_mfma_f32_16x16x32_bf16 v[76:79], a[204:207], a[44:47], v[76:79]\n" \
"ds_read_b64_tr_b16 a[222:223], v227 offset:64000\n" \
"v_mfma_f32_16x16x32_bf16 v[108:111], a[200:203], a[56:59], v[108:111]\n" \
"v_mfma_f32_16x16x32_bf16 v[108:111], a[204:207], a[60:63], v[108:111]\n" \
"s_waitcnt vmcnt(63) lgkmcnt(12)\n" \
"s_nop 0\n" \
"v_mfma_f32_16x16x32_bf16 v[20:23], a[224:227], a[0:3], v[20:23]\n" \
"v_mfma_f32_16x16x32_bf16 v[20:23], a[232:235], a[4:7], v[20:23]\n" \
"buffer_load_dwordx4 a[88:91], v208, %[srdz], s50 offen offset:2048 sc1\n" \
"v_mfma_f32_16x16x32_bf16 v[52:55], a[224:227], a[16:19], v[52:55]\n" \
"v_mfma_f32_16x16x32_bf16 v[52:55], a[232:235], a[20:23], v[52:55]\n" \
"buffer_load_dwordx4 v212, %[srdt], s49 offen offset:1024 lds\n" \
"v_mfma_f32_16x16x32_bf16 v[84:87], a[224:227], a[32:35], v[84:87]\n" \
"s_waitcnt vmcnt(26) lgkmcnt(0)\n" \
"s_nop 0\n" \
"s_bitcmp1_b32 0xe0, s48\n" \
"s_cbranch_scc1 Lcw11_%=\n" \
"s_waitcnt vmcnt(22)\n" \
"Lcw11_%=:\n" \
"s_barrier\n" \
"s_nop 0\n" \
"v_mfma_f32_16x16x32_bf16 v[84:87], a[232:235], a[36:39], v[84:87]\n" \
"ds_read_b64_tr_b16 a[196:197], v215 offset:0\n" \
"v_mfma_f32_16x16x32_bf16 v[116:119], a[224:227], a[48:51], v[116:119]\n" \
"ds_read_b64_tr_b16 a[198:199], v215 offset:2048\n" \
"v_mfma_f32_16x16x32_bf16 v[116:119], a[232:235], a[52:55], v[116:119]\n" \
"ds_read_b64_tr_b16 a[200:201], v215 offset:4096\n" \
"v_mfma_f32_16x16x32_bf16 v[20:23], a[236:239], a[8:11], v[20:23]\n" \
"ds_read_b64_tr_b16 a[202:203], v215 offset:6144\n" \
"v_mfma_f32_16x16x32_bf16 v[20:23], a[208:211], a[12:15], v[20:23]\n" \
"ds_read_b64_tr_b16 a[204:205], v215 offset:8192\n" \
"v_mfma_f32_16x16x32_bf16 v[52:55], a[236:239], a[24:27], v[52:55]\n" \
"ds_read_b64_tr_b16 a[206:207], v215 offset:10240\n" \
"v_mfma_f32_16x16x32_bf16 v[52:55], a[208:211], a[28:31], v[52:55]\n" \
"ds_read_b64_tr_b16 a[224:225], v215 offset:12288\n" \
"buffer_load_dwordx4 a[92:95], v208, %[srdz], s50 offen offset:3072 sc1\n" \
"v_mfma_f32_16x16x32_bf16 v[84:87], a[236:239], a[40:43], v[84:87]\n" \
"ds_read_b64_tr_b16 a[226:227], v215 offset:14336\n" \
"v_mfma_f32_16x16x32_bf16 v[84:87], a[208:211], a[44:47], v[84:87]\n" \
"v_mfma_f32_16x16x32_bf16 v[116:119], a[236:239], a[56:59], v[116:119]\n" \
"v_mfma_f32_16x16x32_bf16 v[116:119], a[208:211], a[60:63], v[116:119]\n" \
"s_waitcnt vmcnt(16) lgkmcnt(4)\n" \
"s_nop 0\n" \
"v_mfma_f32_16x16x32_bf16 v[28:31], a[212:215], a[0:3], v[28:31]\n" \
"buffer_load_dwordx4 a[96:99], v209, %[srdz], s50 offen offset:0 sc1\n" \
"v_mfma_f32_16x16x32_bf16 v[28:31], a[228:231], a[4:7], v[28:31]\n" \
"v_mfma_f32_16x16x32_bf16 v[60:63], a[212:215], a[16:19], v[60:63]\n" \
"buffer_load_dwordx4 v213, %[srdt], s49 offen offset:2048 lds\n" \
"v_mfma_f32_16x16x32_bf16 v[60:63], a[228:231], a[20:23], v[60:63]\n" \
"v_mfma_f32_16x16x32_bf16 v[92:95], a[212:215], a[32:35], v[92:95]\n" \
"v_mfma_f32_16x16x32_bf16 v[92:95], a[228:231], a[36:39], v[92:95]\n" \
"v_mfma_f32_16x16x32_bf16 v[124:127], a[212:215], a[48:51], v[124:127]\n" \
"ds_read_b64_tr_b16 a[232:233], v227 offset:0\n" \
"v_mfma_f32_16x16x32_bf16 v[124:127], a[228:231], a[52:55], v[124:127]\n" \
"ds_read_b64_tr_b16 a[234:235], v227 offset:2048\n" \
"v_mfma_f32_16x16x32_bf16 v[128:131], a[196:199], a[128:131], v[192:195]\n" \
"ds_read_b64_tr_b16 a[236:237], v227 offset:4096\n" \
"v_mfma_f32_16x16x32_bf16 v[128:131], a[200:203], a[132:135], v[128:131]\n" \
"ds_read_b64_tr_b16 a[238:239], v227 offset:6144\n" \
"buffer_load_dwordx4 a[100:103], v209, %[srdz], s50 offen offset:1024 sc1\n" \
"v_mfma_f32_16x16x32_bf16 v[132:135], a[196:199], a[144:147], v[196:199]\n" \
"v_mfma_f32_16x16x32_bf16 v[132:135], a[200:203], a[148:151], v[132:135]\n" \
"v_mfma_f32_16x16x32_bf16 v[136:139], a[196:199], a[160:163], v[200:203]\n" \
"v_mfma_f32_16x16x32_bf16 v[136:139], a[200:203], a[164:167], v[136:139]\n" \
"buffer_load_dwordx4 a[104:107], v209, %[srdz], s50 offen offset:2048 sc1\n" \
"v_mfma_f32_16x16x32_bf16 v[140:143], a[196:199], a[176:179], v[204:207]\n" \
"ds_read_b64_tr_b16 a[208:209], v227 offset:8192\n" \
"v_mfma_f32_16x16x32_bf16 v[140:143], a[200:203], a[180:183], v[140:143]\n" \
"s_waitcnt vmcnt(17) lgkmcnt(5)\n" \
"s_nop 0\n" \
"v_mfma_f32_16x16x32_bf16 v[128:131], a[204:207], a[136:139], v[128:131]\n" \
"ds_read_b64_tr_b16 a[210:211], v227 offset:10240\n" \
"v_mfma_f32_16x16x32_bf16 v[128:131], a[224:227], a[140:143], v[128:131]\n" \
"ds_read_b64_tr_b16 a[212:213], v227 offset:12288\n" \
"v_mfma_f32_16x16x32_bf16 v[132:135], a[204:207], a[152:155], v[132:135]\n" \
"ds_read_b64_tr_b16 a[214:215], v227 offset:14336\n" \
"v_mfma_f32_16x16x32_bf16 v[132:135], a[224:227], a[156:159], v[132:135]\n" \
"buffer_load_dwordx4 v214, %[srdt], s49 offen offset:3072 lds\n" \
"v_mfma_f32_16x16x32_bf16 v[136:139], a[204:207], a[168:171], v[136:139]\n" \
"v_mfma_f32_16x16x32_bf16 v[136:139], a[224:227], a[172:175], v[136:139]\n" \
"buffer_load_dwordx4 a[108:111], v209, %[srdz], s50 offen offset:3072 sc1\n" \
"v_mfma_f32_16x16x32_bf16 v[140:143], a[204:207], a[184:187], v[140:143]\n" \
"ds_read_b64_tr_b16 a[228:229], v215 offset:512\n" \
"v_mfma_f32_16x16x32_bf16 v[140:143], a[224:227], a[188:191], v[140:143]\n" \
"ds_read_b64_tr_b16 a[230:231], v215 offset:2560\n" \
"v_mfma_f32_16x16x32_bf16 v[28:31], a[216:219], a[8:11], v[28:31]\n" \
"v_mfma_f32_16x16x32_bf16 v[28:31], a[220:223], a[12:15], v[28:31]\n" \
"v_mfma_f32_16x16x32_bf16 v[60:63], a[216:219], a[24:27], v[60:63]\n" \
"v_mfma_f32_16x16x32_bf16 v[60:63], a[220:223], a[28:31], v[60:63]\n" \
"v_mfma_f32_16x16x32_bf16 v[92:95], a[216:219], a[40:43], v[92:95]\n" \
"buffer_load_dwordx4 a[112:115], v210, %[srdz], s50 offen offset:0 sc1\n" \
"v_mfma_f32_16x16x32_bf16 v[92:95], a[220:223], a[44:47], v[92:95]\n" \
"v_cmp_u_f32_e64 s[44:45], v128, v132\n" \
"v_mfma_f32_16x16x32_bf16 v[124:127], a[216:219], a[56:59], v[124:127]\n" \
"v_mfma_f32_16x16x32_bf16 v[124:127], a[220:223], a[60:63], v[124:127]\n" \
"v_cmp_u_f32_e64 s[56:57], v136, v140\n" \
"s_or_b64 s[56:57], s[56:57], s[44:45]\n" \
"s_cbranch_scc1 Lsp3_%=\n" \
"Lbk3_%=:\n" \
"s_waitcnt vmcnt(63) lgkmcnt(2)\n" \
"s_nop 0\n" \
"v_mfma_f32_16x16x32_bf16 v[144:147], a[232:235], a[128:131], v[144:147]\n" \
"ds_read_b64_tr_b16 a[216:217], v215 offset:4608\n" \
"v_mfma_f32_16x16x32_bf16 v[144:147], a[236:239], a[132:135], v[144:147]\n" \
"ds_read_b64_tr_b16 a[218:219], v215 offset:6656\n" \
"v_mfma_f32_16x16x32_bf16 v[148:151], a[232:235], a[144:147], v[148:151]\n" \
"ds_read_b64_tr_b16 a[196:197], v215 offset:8704\n" \
"v_mfma_f32_16x16x32_bf16 v[148:151], a[236:239], a[148:151], v[148:151]\n" \
"ds_read_b64_tr_b16 a[198:199], v215 offset:10752\n" \
"v_mfma_f32_16x16x32_bf16 v[152:155], a[232:235], a[160:163], v[152:155]\n" \
"ds_read_b64_tr_b16 a[200:201], v215 offset:12800\n" \
"v_mfma_f32_16x16x32_bf16 v[152:155], a[236:239], a[164:167], v[152:155]\n" \
"ds_read_b64_tr_b16 a[202:203], v215 offset:14848\n" \
"v_mfma_f32_16x16x32_bf16 v[156:159], a[232:235], a[176:179], v[156:159]\n" \
"ds_read_b64_tr_b16 a[204:205], v227 offset:512\n" \
"buffer_load_dwordx4 a[116:119], v210, %[srdz], s50 offen offset:1024 sc1\n" \
"v_mfma_f32_16x16x32_bf16 v[156:159], a[236:239], a[180:183], v[156:159]\n" \
"ds_read_b64_tr_b16 a[206:207], v227 offset:2560\n" \
"v_mfma_f32_16x16x32_bf16 v[144:147], a[208:211], a[136:139], v[144:147]\n" \
"ds_read_b64_tr_b16 a[220:221], v227 offset:4608\n" \
"v_mfma_f32_16x16x32_bf16 v[144:147], a[212:215], a[140:143], v[144:147]\n" \
"ds_read_b64_tr_b16 a[222:223], v227 offset:6656\n" \
"v_mfma_f32_16x16x32_bf16 v[148:151], a[208:211], a[152:155], v[148:151]\n" \
"s_add_u32 m0, %[m0b], 98304\n" \
"s_cmp_gt_u32 s48, 4\n" \
"s_cselect_b32 s49, 4, 5\n" \
"s_lshl_b32 s49, s49, 18\n" \
"s_or_b32 s49, s49, s58\n" \
"buffer_load_dwordx4 %[wo0], %[srdt], s49 offen offset:0 lds\n" \
"v_mfma_f32_16x16x32_bf16 v[148:151], a[212:215], a[156:159], v[148:151]\n" \
"v_mfma_f32_16x16x32_bf16 v[152:155], a[208:211], a[168:171], v[152:155]\n" \
"buffer_load_dwordx4 a[120:123], v210, %[srdz], s50 offen offset:2048 sc1\n" \
"v_mfma_f32_16x16x32_bf16 v[152:155], a[212:215], a[172:175], v[152:155]\n" \
"v_mfma_f32_16x16x32_bf16 v[156:159], a[208:211], a[184:187], v[156:159]\n" \
"ds_read_b64_tr_b16 a[224:225], v227 offset:8704\n" \
"v_mfma_f32_16x16x32_bf16 v[156:159], a[212:215], a[188:191], v[156:159]\n" \
"s_waitcnt vmcnt(63) lgkmcnt(5)\n" \
"s_nop 0\n" \
"v_mfma_f32_16x16x32_bf16 v[160:163], a[228:231], a[128:131], v[160:163]\n" \
"ds_read_b64_tr_b16 a[226:227], v227 offset:10752\n" \
"v_mfma_f32_16x16x32_bf16 v[160:163], a[216:219], a[132:135], v[160:163]\n" \
"ds_read_b64_tr_b16 a[232:233], v227 offset:12800\n" \
"v_mfma_f32_16x16x32_bf16 v[164:167], a[228:231], a[144:147], v[164:167]\n" \
"ds_read_b64_tr_b16 a[234:235], v227 offset:14848\n" \
"v_mfma_f32_16x16x32_bf16 v[164:167], a[216:219], a[148:151], v[164:167]\n" \
"buffer_load_dwordx4 a[124:127], v210, %[srdz], s50 offen offset:3072 sc1\n" \
"v_mfma_f32_16x16x32_bf16 v[168:171], a[228:231], a[160:163], v[168:171]\n" \
"v_mfma_f32_16x16x32_bf16 v[168:171], a[216:219], a[164:167], v[168:171]\n" \
"v_mfma_f32_16x16x32_bf16 v[172:175], a[228:231], a[176:179], v[172:175]\n" \
"v_mfma_f32_16x16x32_bf16 v[172:175], a[216:219], a[180:183], v[172:175]\n" \
"v_mfma_f32_16x16x32_bf16 v[160:163], a[196:199], a[136:139], v[160:163]\n" \
"v_mfma_f32_16x16x32_bf16 v[160:163], a[200:203], a[140:143], v[160:163]\n" \
"buffer_load_dwordx4 v212, %[srdt], s49 offen offset:1024 lds\n" \
"v_mfma_f32_16x16x32_bf16 v[164:167], a[196:199], a[152:155], v[164:167]\n" \
"v_mfma_f32_16x16x32_bf16 v[164:167], a[200:203], a[156:159], v[164:167]\n" \
"v_mfma_f32_16x16x32_bf16 v[168:171], a[196:199], a[168:171], v[168:171]\n" \
"s_waitcnt vmcnt(21) lgkmcnt(0)\n" \
"s_barrier\n" \
"v_mfma_f32_16x16x32_bf16 v[168:171], a[200:203], a[172:175], v[168:171]\n" \
"ds_read_b64_tr_b16 a[236:237], v215 offset:16384\n" \
"v_mfma_f32_16x16x32_bf16 v[172:175], a[196:199], a[184:187], v[172:175]\n" \
"ds_read_b64_tr_b16 a[238:239], v215 offset:18432\n" \
"v_mfma_f32_16x16x32_bf16 v[172:175], a[200:203], a[188:191], v[172:175]\n" \
"s_waitcnt vmcnt(63) lgkmcnt(2)\n" \
"s_nop 0\n" \
"v_mfma_f32_16x16x32_bf16 v[176:179], a[204:207], a[128:131], v[176:179]\n" \
"ds_read_b64_tr_b16 a[208:209], v215 offset:20480\n" \
"s_cmp_gt_u32 s48, 4\n" \
"s_cselect_b32 s50, 4, 5\n" \
"s_mul_i32 s50, s50, 262144\n" \
"buffer_load_dwordx4 v[0:3], %[oq0], %[srdz], s50 offen offset:0 sc1\n" \
"v_mfma_f32_16x16x32_bf16 v[176:179], a[220:223], a[132:135], v[176:179]\n" \
"ds_read_b64_tr_b16 a[210:211], v215 offset:22528\n" \
"v_mfma_f32_16x16x32_bf16 v[180:183], a[204:207], a[144:147], v[180:183]\n" \
"ds_read_b64_tr_b16 a[212:213], v215 offset:24576\n" \
"v_mfma_f32_16x16x32_bf16 v[180:183], a[220:223], a[148:151], v[180:183]\n" \
"ds_read_b64_tr_b16 a[214:215], v215 offset:26624\n" \
"v_mfma_f32_16x16x32_bf16 v[184:187], a[204:207], a[160:163], v[184:187]\n" \
"ds_read_b64_tr_b16 a[228:229], v215 offset:28672\n" \
"buffer_load_dwordx4 v[8:11], %[oq0], %[srdz], s50 offen offset:1024 sc1\n" \
"v_mfma_f32_16x16x32_bf16 v[184:187], a[220:223], a[164:167], v[184:187]\n" \
"ds_read_b64_tr_b16 a[230:231], v215 offset:30720\n" \
"v_mfma_f32_16x16x32_bf16 v[188:191], a[204:207], a[176:179], v[188:191]\n" \
"ds_read_b64_tr_b16 a[216:217], v227 offset:16384\n" \
"v_mfma_f32_16x16x32_bf16 v[188:191], a[220:223], a[180:183], v[188:191]\n" \
"ds_read_b64_tr_b16 a[218:219], v227 offset:18432\n" \
"v_mfma_f32_16x16x32_bf16 v[176:179], a[224:227], a[136:139], v[176:179]\n" \
"ds_read_b64_tr_b16 a[196:197], v227 offset:20480\n" \
"buffer_load_dwordx4 v[16:19], %[oq0], %[srdz], s50 offen offset:2048 sc1\n" \
"v_mfma_f32_16x16x32_bf16 v[176:179], a[232:235], a[140:143], v[176:179]\n" \
"ds_read_b64_tr_b16 a[198:199], v227 offset:22528\n" \
"v_mfma_f32_16x16x32_bf16 v[180:183], a[224:227], a[152:155], v[180:183]\n" \
"buffer_load_dwordx4 v213, %[srdt], s49 offen offset:2048 lds\n" \
"v_mfma_f32_16x16x32_bf16 v[180:183], a[232:235], a[156:159], v[180:183]\n" \
"v_mfma_f32_16x16x32_bf16 v[184:187], a[224:227], a[168:171], v[184:187]\n" \
"buffer_load_dwordx4 v[24:27], %[oq0], %[srdz], s50 offen offset:3072 sc1\n" \
"v_mfma_f32_16x16x32_bf16 v[184:187], a[232:235], a[172:175], v[184:187]\n" \
"v_mfma_f32_16x16x32_bf16 v[188:191], a[224:227], a[184:187], v[188:191]\n" \
"ds_read_b64_tr_b16 a[200:201], v227 offset:24576\n" \
"v_mfma_f32_16x16x32_bf16 v[188:191], a[232:235], a[188:191], v[188:191]\n" \
"s_waitcnt vmcnt(63) lgkmcnt(5)\n" \
"s_nop 0\n" \
"v_mfma_f32_16x16x32_bf16 v[4:7], a[236:239], a[128:131], v[4:7]\n" \
"ds_read_b64_tr_b16 a[202:203], v227 offset:26624\n" \
"buffer_load_dwordx4 v[32:35], v208, %[srdz], s50 offen offset:0 sc1\n" \
"v_mfma_f32_16x16x32_bf16 v[4:7], a[208:211], a[132:135], v[4:7]\n" \
"ds_read_b64_tr_b16 a[204:205], v227 offset:28672\n" \
"v_mfma_f32_16x16x32_bf16 v[36:39], a[236:239], a[144:147], v[36:39]\n" \
"ds_read_b64_tr_b16 a[206:207], v227 offset:30720\n" \
"v_mfma_f32_16x16x32_bf16 v[36:39], a[208:211], a[148:151], v[36:39]\n" \
"buffer_load_dwordx4 v[40:43], v208, %[srdz], s50 offen offset:1024 sc1\n" \
"v_mfma_f32_16x16x32_bf16 v[68:71], a[236:239], a[160:163], v[68:71]\n" \
"v_mfma_f32_16x16x32_bf16 v[68:71], a[208:211], a[164:167], v[68:71]\n" \
"buffer_load_dwordx4 v[48:51], v208, %[srdz], s50 offen offset:2048 sc1\n" \
"v_mfma_f32_16x16x32_bf16 v[100:103], a[236:239], a[176:179], v[100:103]\n" \
"ds_read_b64_tr_b16 a[220:221], v215 offset:16896\n" \
"v_mfma_f32_16x16x32_bf16 v[100:103], a[208:211], a[180:183], v[100:103]\n" \
"ds_read_b64_tr_b16 a[222:223], v215 offset:18944\n" \
"v_mfma_f32_16x16x32_bf16 v[4:7], a[212:215], a[136:139], v[4:7]\n" \
"ds_read_b64_tr_b16 a[224:225], v215 offset:20992\n" \
"v_mfma_f32_16x16x32_bf16 v[4:7], a[228:231], a[140:143], v[4:7]\n" \
"ds_read_b64_tr_b16 a[226:227], v215 offset:23040\n" \
"v_mfma_f32_16x16x32_bf16 v[36:39], a[212:215], a[152:155], v[36:39]\n" \
"buffer_load_dwordx4 v214, %[srdt], s49 offen offset:3072 lds\n" \
"v_mfma_f32_16x16x32_bf16 v[36:39], a[228:231], a[156:159], v[36:39]\n" \
"v_mfma_f32_16x16x32_bf16 v[68:71], a[212:215], a[168:171], v[68:71]\n" \
"buffer_load_dwordx4 v[56:59], v208, %[srdz], s50 offen offset:3072 sc1\n" \
"v_mfma_f32_16x16x32_bf16 v[68:71], a[228:231], a[172:175], v[68:71]\n" \
"v_mfma_f32_16x16x32_bf16 v[100:103], a[212:215], a[184:187], v[100:103]\n" \
"ds_read_b64_tr_b16 a[232:233], v215 offset:25088\n" \
"v_mfma_f32_16x16x32_bf16 v[100:103], a[228:231], a[188:191], v[100:103]\n" \
"s_waitcnt vmcnt(63) lgkmcnt(5)\n" \
"s_nop 0\n" \
"v_mfma_f32_16x16x32_bf16 v[12:15], a[216:219], a[128:131], v[12:15]\n" \
"ds_read_b64_tr_b16 a[234:235], v215 offset:27136\n" \
"buffer_load_dwordx4 v[64:67], v209, %[srdz], s50 offen offset:0 sc1\n" \
"v_mfma_f32_16x16x32_bf16 v[12:15], a[196:199], a[132:135], v[12:15]\n" \
"ds_read_b64_tr_b16 a[236:237], v215 offset:29184\n" \
"v_mfma_f32_16x16x32_bf16 v[44:47], a[216:219], a[144:147], v[44:47]\n" \
"ds_read_b64_tr_b16 a[238:239], v215 offset:31232\n" \
"v_mfma_f32_16x16x32_bf16 v[44:47], a[196:199], a[148:151], v[44:47]\n" \
".if . - adaln_body != 49152\n" \
".error \"size model\"\n" \
".endif\n" \
"Lk11_%=:\n" \
"s_cbranch_execz adaln_kr\n" \
"s_nop 0\n" \
"buffer_load_dwordx4 v[72:75], v209, %[srdz], s50 offen offset:1024 sc1\n" \
"v_mfma_f32_16x16x32_bf16 v[76:79], a[216:219], a[160:163], v[76:79]\n" \
"v_mfma_f32_16x16x32_bf16 v[76:79], a[196:199], a[164:167], v[76:79]\n" \
"buffer_load_dwordx4 v[80:83], v209, %[srdz], s50 offen offset:2048 sc1\n" \
"v_mfma_f32_16x16x32_bf16 v[108:111], a[216:219], a[176:179], v[108:111]\n" \
"ds_read_b64_tr_b16 a[208:209], v227 offset:16896\n" \
"v_mfma_f32_16x16x32_bf16 v[108:111], a[196:199], a[180:183], v[108:111]\n" \
"ds_read_b64_tr_b16 a[210:211], v227 offset:18944\n" \
"v_mfma_f32_16x16x32_bf16 v[12:15], a[200:203], a[136:139], v[12:15]\n" \
"ds_read_b64_tr_b16 a[212:213], v227 offset:25088\n" \
"v_mfma_f32_16x16x32_bf16 v[12:15], a[204:207], a[140:143], v[12:15]\n" \
"ds_read_b64_tr_b16 a[214:215], v227 offset:27136\n" \
"v_mfma_f32_16x16x32_bf16 v[44:47], a[200:203], a[152:155], v[44:47]\n" \
"ds_read_b64_tr_b16 a[228:229], v227 offset:20992\n" \
"v_mfma_f32_16x16x32_bf16 v[44:47], a[204:207], a[156:159], v[44:47]\n" \
"ds_read_b64_tr_b16 a[230:231], v227 offset:23040\n" \
"buffer_load_dwordx4 v[88:91], v209, %[srdz], s50 offen offset:3072 sc1\n" \
"v_mfma_f32_16x16x32_bf16 v[76:79], a[200:203], a[168:171], v[76:79]\n" \
"ds_read_b64_tr_b16 a[216:217], v227 offset:29184\n" \
"v_mfma_f32_16x16x32_bf16 v[76:79], a[204:207], a[172:175], v[76:79]\n" \
"ds_read_b64_tr_b16 a[218:219], v227 offset:31232\n" \
"v_mfma_f32_16x16x32_bf16 v[108:111], a[200:203], a[184:187], v[108:111]\n" \
"buffer_load_dwordx4 v[96:99], v210, %[srdz], s50 offen offset:0 sc1\n" \
"v_mfma_f32_16x16x32_bf16 v[108:111], a[204:207], a[188:191], v[108:111]\n" \
"s_waitcnt vmcnt(63) lgkmcnt(12)\n" \
"s_nop 0\n" \
"v_mfma_f32_16x16x32_bf16 v[20:23], a[220:223], a[128:131], v[20:23]\n" \
"s_add_u32 m0, %[m0b], 114688\n" \
"s_cmp_gt_u32 s48, 5\n" \
"s_cselect_b32 s49, 5, 6\n" \
"s_lshl_b32 s49, s49, 18\n" \
"s_or_b32 s49, s49, s58\n" \
"buffer_load_dwordx4 %[wo0], %[srdt], s49 offen offset:0 lds\n" \
"v_mfma_f32_16x16x32_bf16 v[20:23], a[224:227], a[132:135], v[20:23]\n" \
"v_mfma_f32_16x16x32_bf16 v[52:55], a[220:223], a[144:147], v[52:55]\n" \
"buffer_load_dwordx4 v[104:107], v210, %[srdz], s50 offen offset:1024 sc1\n" \
"v_mfma_f32_16x16x32_bf16 v[52:55], a[224:227], a[148:151], v[52:55]\n" \
"v_mfma_f32_16x16x32_bf16 v[84:87], a[220:223], a[160:163], v[84:87]\n" \
"s_waitcnt vmcnt(24) lgkmcnt(0)\n" \
"s_barrier\n" \
"v_mfma_f32_16x16x32_bf16 v[84:87], a[224:227], a[164:167], v[84:87]\n" \
"ds_read_b64_tr_b16 a[196:197], v215 offset:32768\n" \
"v_mfma_f32_16x16x32_bf16 v[116:119], a[220:223], a[176:179], v[116:119]\n" \
"ds_read_b64_tr_b16 a[198:199], v215 offset:34816\n" \
"v_mfma_f32_16x16x32_bf16 v[116:119], a[224:227], a[180:183], v[116:119]\n" \
"ds_read_b64_tr_b16 a[200:201], v215 offset:36864\n" \
"buffer_load_dwordx4 v[112:115], v210, %[srdz], s50 offen offset:2048 sc1\n" \
"v_mfma_f32_16x16x32_bf16 v[20:23], a[232:235], a[136:139], v[20:23]\n" \
"ds_read_b64_tr_b16 a[202:203], v215 offset:38912\n" \
"v_mfma_f32_16x16x32_bf16 v[20:23], a[236:239], a[140:143], v[20:23]\n" \
"ds_read_b64_tr_b16 a[204:205], v215 offset:40960\n" \
"v_mfma_f32_16x16x32_bf16 v[52:55], a[232:235], a[152:155], v[52:55]\n" \
"ds_read_b64_tr_b16 a[206:207], v215 offset:43008\n" \
"v_mfma_f32_16x16x32_bf16 v[52:55], a[236:239], a[156:159], v[52:55]\n" \
"ds_read_b64_tr_b16 a[220:221], v215 offset:45056\n" \
"buffer_load_dwordx4 v[120:123], v210, %[srdz], s50 offen offset:3072 sc1\n" \
"v_mfma_f32_16x16x32_bf16 v[84:87], a[232:235], a[168:171], v[84:87]\n" \
"ds_read_b64_tr_b16 a[222:223], v215 offset:47104\n" \
"v_readlane_b32 s54, %[thi], 16\n" \
"v_mfma_f32_16x16x32_bf16 v[84:87], a[236:239], a[172:175], v[84:87]\n" \
"v_mfma_f32_16x16x32_bf16 v[116:119], a[232:235], a[184:187], v[116:119]\n" \
"v_mfma_f32_16x16x32_bf16 v[116:119], a[236:239], a[188:191], v[116:119]\n" \
"s_waitcnt vmcnt(23) lgkmcnt(4)\n" \
"s_nop 0\n" \
"v_mfma_f32_16x16x32_bf16 v[28:31], a[208:211], a[128:131], v[28:31]\n" \
"buffer_load_dwordx4 v212, %[srdt], s49 offen offset:1024 lds\n" \
"v_mfma_f32_16x16x32_bf16 v[28:31], a[228:231], a[132:135], v[28:31]\n" \
"v_mfma_f32_16x16x32_bf16 v[60:63], a[208:211], a[144:147], v[60:63]\n" \
"s_cmp_gt_u32 s48, 5\n" \
"s_cselect_b32 s50, 5, 6\n" \
"s_mul_i32 s50, s50, 262144\n" \
"buffer_load_dwordx4 a[0:3], %[oq0], %[srdz], s50 offen offset:0 sc1\n" \
"v_mfma_f32_16x16x32_bf16 v[60:63], a[228:231], a[148:151], v[60:63]\n" \
"v_mfma_f32_16x16x32_bf16 v[92:95], a[208:211], a[160:163], v[92:95]\n" \
"buffer_load_dword v232, v226, %[srdz], s54 offen sc1\n" \
"v_mfma_f32_16x16x32_bf16 v[92:95], a[228:231], a[164:167], v[92:95]\n" \
"v_mfma_f32_16x16x32_bf16 v[124:127], a[208:211], a[176:179], v[124:127]\n" \
"ds_read_b64_tr_b16 a[224:225], v227 offset:32768\n" \
"buffer_load_dwordx4 a[4:7], %[oq0], %[srdz], s50 offen offset:1024 sc1\n" \
"v_mfma_f32_16x16x32_bf16 v[124:127], a[228:231], a[180:183], v[124:127]\n" \
"ds_read_b64_tr_b16 a[226:227], v227 offset:34816\n" \
"v_mfma_f32_16x16x32_bf16 v[192:195], a[196:199], a[64:67], v[128:131]\n" \
"ds_read_b64_tr_b16 a[232:233], v227 offset:36864\n" \
"v_mfma_f32_16x16x32_bf16 v[192:195], a[200:203], a[68:71], v[192:195]\n" \
"ds_read_b64_tr_b16 a[234:235], v227 offset:38912\n" \
"v_mfma_f32_16x16x32_bf16 v[196:199], a[196:199], a[80:83], v[132:135]\n" \
"buffer_load_dwordx4 a[8:11], %[oq0], %[srdz], s50 offen offset:2048 sc1\n" \
"v_mfma_f32_16x16x32_bf16 v[196:199], a[200:203], a[84:87], v[196:199]\n" \
"v_mfma_f32_16x16x32_bf16 v[200:203], a[196:199], a[96:99], v[136:139]\n" \
"buffer_load_dwordx4 a[12:15], %[oq0], %[srdz], s50 offen offset:3072 sc1\n" \
"v_mfma_f32_16x16x32_bf16 v[200:203], a[200:203], a[100:103], v[200:203]\n" \
"v_mfma_f32_16x16x32_bf16 v[204:207], a[196:199], a[112:115], v[140:143]\n" \
"ds_read_b64_tr_b16 a[236:237], v227 offset:40960\n" \
"v_mfma_f32_16x16x32_bf16 v[204:207], a[200:203], a[116:119], v[204:207]\n" \
"s_waitcnt vmcnt(26) lgkmcnt(5)\n" \
"s_nop 0\n" \
"v_mfma_f32_16x16x32_bf16 v[192:195], a[204:207], a[72:75], v[192:195]\n" \
"ds_read_b64_tr_b16 a[238:239], v227 offset:43008\n" \
"v_mfma_f32_16x16x32_bf16 v[192:195], a[220:223], a[76:79], v[192:195]\n" \
"ds_read_b64_tr_b16 a[208:209], v227 offset:45056\n" \
"buffer_load_dwordx4 v213, %[srdt], s49 offen offset:2048 lds\n" \
"v_mfma_f32_16x16x32_bf16 v[196:199], a[204:207], a[88:91], v[196:199]\n" \
"ds_read_b64_tr_b16 a[210:211], v227 offset:47104\n" \
"v_mfma_f32_16x16x32_bf16 v[196:199], a[220:223], a[92:95], v[196:199]\n" \
"buffer_load_dwordx4 a[16:19], v208, %[srdz], s50 offen offset:0 sc1\n" \
"v_mfma_f32_16x16x32_bf16 v[200:203], a[204:207], a[104:107], v[200:203]\n" \
"v_mfma_f32_16x16x32_bf16 v[200:203], a[220:223], a[108:111], v[200:203]\n" \
"buffer_load_dwordx4 a[20:23], v208, %[srdz], s50 offen offset:1024 sc1\n" \
"v_mfma_f32_16x16x32_bf16 v[204:207], a[204:207], a[120:123], v[204:207]\n" \
"ds_read_b64_tr_b16 a[228:229], v215 offset:33280\n" \
"v_mfma_f32_16x16x32_bf16 v[204:207], a[220:223], a[124:127], v[204:207]\n" \
"ds_read_b64_tr_b16 a[230:231], v215 offset:35328\n" \
"v_mfma_f32_16x16x32_bf16 v[28:31], a[212:215], a[136:139], v[28:31]\n" \
"buffer_load_dwordx4 a[24:27], v208, %[srdz], s50 offen offset:2048 sc1\n" \
"v_mfma_f32_16x16x32_bf16 v[28:31], a[216:219], a[140:143], v[28:31]\n" \
"v_mfma_f32_16x16x32_bf16 v[60:63], a[212:215], a[152:155], v[60:63]\n" \
"buffer_load_dwordx4 a[28:31], v208, %[srdz], s50 offen offset:3072 sc1\n" \
"v_mfma_f32_16x16x32_bf16 v[60:63], a[216:219], a[156:159], v[60:63]\n" \
"v_mfma_f32_16x16x32_bf16 v[92:95], a[212:215], a[168:171], v[92:95]\n" \
"v_mfma_f32_16x16x32_bf16 v[92:95], a[216:219], a[172:175], v[92:95]\n" \
"v_cmp_u_f32_e64 s[44:45], v192, v196\n" \
"buffer_load_dwordx4 v214, %[srdt], s49 offen offset:3072 lds\n" \
"v_mfma_f32_16x16x32_bf16 v[124:127], a[212:215], a[184:187], v[124:127]\n" \
"v_mfma_f32_16x16x32_bf16 v[124:127], a[216:219], a[188:191], v[124:127]\n" \
"v_cmp_u_f32_e64 s[56:57], v200, v204\n" \
"s_or_b64 s[56:57], s[56:57], s[44:45]\n" \
"s_cbranch_scc1 Lsp4_%=\n" \
"Lbk4_%=:\n" \
"s_waitcnt vmcnt(63) lgkmcnt(2)\n" \
"s_nop 0\n" \
"v_mfma_f32_16x16x32_bf16 v[144:147], a[224:227], a[64:67], v[144:147]\n" \
"ds_read_b64_tr_b16 a[212:213], v215 offset:37376\n" \
"v_mfma_f32_16x16x32_bf16 v[144:147], a[232:235], a[68:71], v[144:147]\n" \
"ds_read_b64_tr_b16 a[214:215], v215 offset:39424\n" \
"buffer_load_dwordx4 a[32:35], v209, %[srdz], s50 offen offset:0 sc1\n" \
"v_mfma_f32_16x16x32_bf16 v[148:151], a[224:227], a[80:83], v[148:151]\n" \
"ds_read_b64_tr_b16 a[196:197], v215 offset:41472\n" \
"v_mfma_f32_16x16x32_bf16 v[148:151], a[232:235], a[84:87], v[148:151]\n" \
"ds_read_b64_tr_b16 a[198:199], v215 offset:43520\n" \
"v_mfma_f32_16x16x32_bf16 v[152:155], a[224:227], a[96:99], v[152:155]\n" \
"ds_read_b64_tr_b16 a[200:201], v215 offset:45568\n" \
"v_mfma_f32_16x16x32_bf16 v[152:155], a[232:235], a[100:103], v[152:155]\n" \
"ds_read_b64_tr_b16 a[202:203], v215 offset:47616\n" \
"buffer_load_dwordx4 a[36:39], v209, %[srdz], s50 offen offset:1024 sc1\n" \
"v_mfma_f32_16x16x32_bf16 v[156:159], a[224:227], a[112:115], v[156:159]\n" \
"ds_read_b64_tr_b16 a[204:205], v227 offset:33280\n" \
"v_mfma_f32_16x16x32_bf16 v[156:159], a[232:235], a[116:119], v[156:159]\n" \
"ds_read_b64_tr_b16 a[206:207], v227 offset:35328\n" \
"v_mfma_f32_16x16x32_bf16 v[144:147], a[236:239], a[72:75], v[144:147]\n" \
"ds_read_b64_tr_b16 a[216:217], v227 offset:41472\n" \
"v_mfma_f32_16x16x32_bf16 v[144:147], a[208:211], a[76:79], v[144:147]\n" \
"ds_read_b64_tr_b16 a[218:219], v227 offset:43520\n" \
"buffer_load_dwordx4 a[40:43], v209, %[srdz], s50 offen offset:2048 sc1\n" \
"v_mfma_f32_16x16x32_bf16 v[148:151], a[236:239], a[88:91], v[148:151]\n" \
"ds_read_b64_tr_b16 a[220:221], v227 offset:37376\n" \
"v_mfma_f32_16x16x32_bf16 v[148:151], a[208:211], a[92:95], v[148:151]\n" \
"ds_read_b64_tr_b16 a[222:223], v227 offset:39424\n" \
"v_mfma_f32_16x16x32_bf16 v[152:155], a[236:239], a[104:107], v[152:155]\n" \
"ds_read_b64_tr_b16 a[224:225], v227 offset:45568\n" \
"v_mfma_f32_16x16x32_bf16 v[152:155], a[208:211], a[108:111], v[152:155]\n" \
"ds_read_b64_tr_b16 a[226:227], v227 offset:47616\n" \
"buffer_load_dwordx4 a[44:47], v209, %[srdz], s50 offen offset:3072 sc1\n" \
"v_mfma_f32_16x16x32_bf16 v[156:159], a[236:239], a[120:123], v[156:159]\n" \
"v_mfma_f32_16x16x32_bf16 v[156:159], a[208:211], a[124:127], v[156:159]\n" \
"s_waitcnt vmcnt(63) lgkmcnt(12)\n" \
"s_nop 0\n" \
"v_mfma_f32_16x16x32_bf16 v[160:163], a[228:231], a[64:67], v[160:163]\n" \
"buffer_load_dwordx4 a[48:51], v210, %[srdz], s50 offen offset:0 sc1\n" \
"v_mfma_f32_16x16x32_bf16 v[160:163], a[212:215], a[68:71], v[160:163]\n" \
"v_mfma_f32_16x16x32_bf16 v[164:167], a[228:231], a[80:83], v[164:167]\n" \
"buffer_load_dwordx4 a[52:55], v210, %[srdz], s50 offen offset:1024 sc1\n" \
"v_mfma_f32_16x16x32_bf16 v[164:167], a[212:215], a[84:87], v[164:167]\n" \
"v_mfma_f32_16x16x32_bf16 v[168:171], a[228:231], a[96:99], v[168:171]\n" \
"s_waitcnt vmcnt(28) lgkmcnt(0)\n" \
"s_barrier\n" \
"v_mfma_f32_16x16x32_bf16 v[168:171], a[212:215], a[100:103], v[168:171]\n" \
"ds_read_b64_tr_b16 a[232:233], v220 offset:32768\n" \
"v_mfma_f32_16x16x32_bf16 v[172:175], a[228:231], a[112:115], v[172:175]\n" \
"ds_read_b64_tr_b16 a[234:235], v220 offset:34816\n" \
"v_mfma_f32_16x16x32_bf16 v[172:175], a[212:215], a[116:119], v[172:175]\n" \
"ds_read_b64_tr_b16 a[236:237], v220 offset:36864\n" \
"v_mfma_f32_16x16x32_bf16 v[160:163], a[196:199], a[72:75], v[160:163]\n" \
"ds_read_b64_tr_b16 a[238:239], v220 offset:38912\n" \
"v_mfma_f32_16x16x32_bf16 v[160:163], a[200:203], a[76:79], v[160:163]\n" \
"ds_read_b64_tr_b16 a[208:209], v220 offset:40960\n" \
"s_add_u32 m0, %[m0b], 49152\n" \
"s_cmp_gt_u32 s48, 6\n" \
"s_cselect_b32 s49, 6, 7\n" \
"s_lshl_b32 s49, s49, 18\n" \
"s_or_b32 s49, s49, s58\n" \
"buffer_load_dwordx4 %[wo0], %[srdt], s49 offen offset:0 lds\n" \
"v_mfma_f32_16x16x32_bf16 v[164:167], a[196:199], a[88:91], v[164:167]\n" \
"ds_read_b64_tr_b16 a[210:211], v220 offset:43008\n" \
"v_mfma_f32_16x16x32_bf16 v[164:167], a[200:203], a[92:95], v[164:167]\n" \
"ds_read_b64_tr_b16 a[228:229], v220 offset:45056\n" \
"buffer_load_dwordx4 a[56:59], v210, %[srdz], s50 offen offset:2048 sc1\n" \
"v_mfma_f32_16x16x32_bf16 v[168:171], a[196:199], a[104:107], v[168:171]\n" \
"ds_read_b64_tr_b16 a[230:231], v220 offset:47104\n" \
"v_mfma_f32_16x16x32_bf16 v[168:171], a[200:203], a[108:111], v[168:171]\n" \
"buffer_load_dwordx4 a[60:63], v210, %[srdz], s50 offen offset:3072 sc1\n" \
"v_mfma_f32_16x16x32_bf16 v[172:175], a[196:199], a[120:123], v[172:175]\n" \
"v_mfma_f32_16x16x32_bf16 v[172:175], a[200:203], a[124:127], v[172:175]\n" \
"s_waitcnt vmcnt(23) lgkmcnt(4)\n" \
"s_nop 0\n" \
"v_mfma_f32_16x16x32_bf16 v[176:179], a[204:207], a[64:67], v[176:179]\n" \
"v_mfma_f32_16x16x32_bf16 v[176:179], a[220:223], a[68:71], v[176:179]\n" \
"s_cmp_gt_u32 s48, 6\n" \
"s_cselect_b32 s50, 6, 7\n" \
"s_mul_i32 s50, s50, 262144\n" \
"buffer_load_dwordx4 a[128:131], %[oq0], %[srdz], s50 offen offset:0 sc1\n" \
"v_mfma_f32_16x16x32_bf16 v[180:183], a[204:207], a[80:83], v[180:183]\n" \
"v_mfma_f32_16x16x32_bf16 v[180:183], a[220:223], a[84:87], v[180:183]\n" \
"v_mfma_f32_16x16x32_bf16 v[184:187], a[204:207], a[96:99], v[184:187]\n" \
"v_mfma_f32_16x16x32_bf16 v[184:187], a[220:223], a[100:103], v[184:187]\n" \
"buffer_load_dwordx4 v212, %[srdt], s49 offen offset:1024 lds\n" \
"v_mfma_f32_16x16x32_bf16 v[188:191], a[204:207], a[112:115], v[188:191]\n" \
"ds_read_b64_tr_b16 a[212:213], v221 offset:32768\n" \
"v_mfma_f32_16x16x32_bf16 v[188:191], a[220:223], a[116:119], v[188:191]\n" \
"ds_read_b64_tr_b16 a[214:215], v221 offset:34816\n" \
"v_mfma_f32_16x16x32_bf16 v[128:131], a[232:235], v[0:3], v[192:195]\n" \
"ds_read_b64_tr_b16 a[196:197], v221 offset:36864\n" \
"buffer_load_dwordx4 a[132:135], %[oq0], %[srdz], s50 offen offset:1024 sc1\n" \
"v_mfma_f32_16x16x32_bf16 v[128:131], a[236:239], v[8:11], v[128:131]\n" \
"ds_read_b64_tr_b16 a[198:199], v221 offset:38912\n" \
"v_mfma_f32_16x16x32_bf16 v[132:135], a[232:235], v[32:35], v[196:199]\n" \
"buffer_load_dwordx4 a[136:139], %[oq0], %[srdz], s50 offen offset:2048 sc1\n" \
"v_mfma_f32_16x16x32_bf16 v[132:135], a[236:239], v[40:43], v[132:135]\n" \
"v_mfma_f32_16x16x32_bf16 v[136:139], a[232:235], v[64:67], v[200:203]\n" \
"v_mfma_f32_16x16x32_bf16 v[136:139], a[236:239], v[72:75], v[136:139]\n" \
"buffer_load_dwordx4 a[140:143], %[oq0], %[srdz], s50 offen offset:3072 sc1\n" \
"v_mfma_f32_16x16x32_bf16 v[140:143], a[232:235], v[96:99], v[204:207]\n" \
"ds_read_b64_tr_b16 a[200:201], v221 offset:40960\n" \
"v_mfma_f32_16x16x32_bf16 v[140:143], a[236:239], v[104:107], v[140:143]\n" \
"s_waitcnt vmcnt(26) lgkmcnt(5)\n" \
"s_nop 0\n" \
"v_mfma_f32_16x16x32_bf16 v[128:131], a[208:211], v[16:19], v[128:131]\n" \
"ds_read_b64_tr_b16 a[202:203], v221 offset:43008\n" \
"v_mfma_f32_16x16x32_bf16 v[128:131], a[228:231], v[24:27], v[128:131]\n" \
"ds_read_b64_tr_b16 a[204:205], v221 offset:45056\n" \
"v_mfma_f32_16x16x32_bf16 v[132:135], a[208:211], v[48:51], v[132:135]\n" \
"ds_read_b64_tr_b16 a[206:207], v221 offset:47104\n" \
"v_mfma_f32_16x16x32_bf16 v[132:135], a[228:231], v[56:59], v[132:135]\n" \
"buffer_load_dwordx4 a[144:147], v208, %[srdz], s50 offen offset:0 sc1\n" \
"v_mfma_f32_16x16x32_bf16 v[136:139], a[208:211], v[80:83], v[136:139]\n" \
"v_mfma_f32_16x16x32_bf16 v[136:139], a[228:231], v[88:91], v[136:139]\n" \
"buffer_load_dwordx4 v213, %[srdt], s49 offen offset:2048 lds\n" \
"v_mfma_f32_16x16x32_bf16 v[140:143], a[208:211], v[112:115], v[140:143]\n" \
"ds_read_b64_tr_b16 a[220:221], v220 offset:33280\n" \
"v_mfma_f32_16x16x32_bf16 v[140:143], a[228:231], v[120:123], v[140:143]\n" \
"ds_read_b64_tr_b16 a[222:223], v220 offset:35328\n" \
"v_mfma_f32_16x16x32_bf16 v[176:179], a[216:219], a[72:75], v[176:179]\n" \
"buffer_load_dwordx4 a[148:151], v208, %[srdz], s50 offen offset:1024 sc1\n" \
"v_mfma_f32_16x16x32_bf16 v[176:179], a[224:227], a[76:79], v[176:179]\n" \
"v_mfma_f32_16x16x32_bf16 v[180:183], a[216:219], a[88:91], v[180:183]\n" \
"buffer_load_dwordx4 a[152:155], v208, %[srdz], s50 offen offset:2048 sc1\n" \
"v_mfma_f32_16x16x32_bf16 v[180:183], a[224:227], a[92:95], v[180:183]\n" \
"v_mfma_f32_16x16x32_bf16 v[184:187], a[216:219], a[104:107], v[184:187]\n" \
"v_mfma_f32_16x16x32_bf16 v[184:187], a[224:227], a[108:111], v[184:187]\n" \
"v_cmp_u_f32_e64 s[44:45], v128, v132\n" \
"buffer_load_dwordx4 a[156:159], v208, %[srdz], s50 offen offset:3072 sc1\n" \
"v_mfma_f32_16x16x32_bf16 v[188:191], a[216:219], a[120:123], v[188:191]\n" \
"v_mfma_f32_16x16x32_bf16 v[188:191], a[224:227], a[124:127], v[188:191]\n" \
"v_cmp_u_f32_e64 s[56:57], v136, v140\n" \
"s_or_b64 s[56:57], s[56:57], s[44:45]\n" \
"s_cbranch_scc1 Lsp5_%=\n" \
"Lbk5_%=:\n" \
"s_waitcnt vmcnt(63) lgkmcnt(2)\n" \
"s_nop 0\n" \
"v_mfma_f32_16x16x32_bf16 v[144:147], a[212:215], v[0:3], v[144:147]\n" \
"ds_read_b64_tr_b16 a[216:217], v220 offset:37376\n" \
"v_mfma_f32_16x16x32_bf16 v[144:147], a[196:199], v[8:11], v[144:147]\n" \
"ds_read_b64_tr_b16 a[218:219], v220 offset:39424\n" \
"v_mfma_f32_16x16x32_bf16 v[148:151], a[212:215], v[32:35], v[148:151]\n" \
"ds_read_b64_tr_b16 a[208:209], v220 offset:41472\n" \
"v_mfma_f32_16x16x32_bf16 v[148:151], a[196:199], v[40:43], v[148:151]\n" \
"ds_read_b64_tr_b16 a[210:211], v220 offset:43520\n" \
"v_mfma_f32_16x16x32_bf16 v[152:155], a[212:215], v[64:67], v[152:155]\n" \
"ds_read_b64_tr_b16 a[224:225], v220 offset:45568\n" \
"buffer_load_dwordx4 a[160:163], v209, %[srdz], s50 offen offset:0 sc1\n" \
"v_mfma_f32_16x16x32_bf16 v[152:155], a[196:199], v[72:75], v[152:155]\n" \
"ds_read_b64_tr_b16 a[226:227], v220 offset:47616\n" \
"v_mfma_f32_16x16x32_bf16 v[156:159], a[212:215], v[96:99], v[156:159]\n" \
"ds_read_b64_tr_b16 a[228:229], v221 offset:33280\n" \
"buffer_load_dwordx4 v214, %[srdt], s49 offen offset:3072 lds\n" \
"v_mfma_f32_16x16x32_bf16 v[156:159], a[196:199], v[104:107], v[156:159]\n" \
"ds_read_b64_tr_b16 a[230:231], v221 offset:35328\n" \
"v_mfma_f32_16x16x32_bf16 v[144:147], a[200:203], v[16:19], v[144:147]\n" \
"ds_read_b64_tr_b16 a[232:233], v221 offset:41472\n" \
"buffer_load_dwordx4 a[164:167], v209, %[srdz], s50 offen offset:1024 sc1\n" \
"v_mfma_f32_16x16x32_bf16 v[144:147], a[204:207], v[24:27], v[144:147]\n" \
"ds_read_b64_tr_b16 a[234:235], v221 offset:43520\n" \
"v_mfma_f32_16x16x32_bf16 v[148:151], a[200:203], v[48:51], v[148:151]\n" \
"ds_read_b64_tr_b16 a[236:237], v221 offset:37376\n" \
"v_mfma_f32_16x16x32_bf16 v[148:151], a[204:207], v[56:59], v[148:151]\n" \
"ds_read_b64_tr_b16 a[238:239], v221 offset:39424\n" \
"v_mfma_f32_16x16x32_bf16 v[152:155], a[200:203], v[80:83], v[152:155]\n" \
"ds_read_b64_tr_b16 a[212:213], v221 offset:45568\n" \
"buffer_load_dwordx4 a[168:171], v209, %[srdz], s50 offen offset:2048 sc1\n" \
"v_mfma_f32_16x16x32_bf16 v[152:155], a[204:207], v[88:91], v[152:155]\n" \
"ds_read_b64_tr_b16 a[214:215], v221 offset:47616\n" \
"v_mfma_f32_16x16x32_bf16 v[156:159], a[200:203], v[112:115], v[156:159]\n" \
"buffer_load_dwordx4 a[172:175], v209, %[srdz], s50 offen offset:3072 sc1\n" \
"v_mfma_f32_16x16x32_bf16 v[156:159], a[204:207], v[120:123], v[156:159]\n" \
"s_waitcnt vmcnt(63) lgkmcnt(12)\n" \
"s_nop 0\n" \
"v_mfma_f32_16x16x32_bf16 v[160:163], a[220:223], v[0:3], v[160:163]\n" \
"v_mfma_f32_16x16x32_bf16 v[160:163], a[216:219], v[8:11], v[160:163]\n" \
"buffer_load_dwordx4 a[176:179], v210, %[srdz], s50 offen offset:0 sc1\n" \
"v_mfma_f32_16x16x32_bf16 v[164:167], a[220:223], v[32:35], v[164:167]\n" \
"v_mfma_f32_16x16x32_bf16 v[164:167], a[216:219], v[40:43], v[164:167]\n" \
"v_mfma_f32_16x16x32_bf16 v[168:171], a[220:223], v[64:67], v[168:171]\n" \
"s_waitcnt vmcnt(25) lgkmcnt(0)\n" \
"s_barrier\n" \
"v_mfma_f32_16x16x32_bf16 v[168:171], a[216:219], v[72:75], v[168:171]\n" \
"ds_read_b64_tr_b16 a[196:197], v220 offset:49152\n" \
"v_mfma_f32_16x16x32_bf16 v[172:175], a[220:223], v[96:99], v[172:175]\n" \
"ds_read_b64_tr_b16 a[198:199], v220 offset:51200\n" \
"v_mfma_f32_16x16x32_bf16 v[172:175], a[216:219], v[104:107], v[172:175]\n" \
"ds_read_b64_tr_b16 a[200:201], v220 offset:53248\n" \
"v_mfma_f32_16x16x32_bf16 v[160:163], a[208:211], v[16:19], v[160:163]\n" \
"ds_read_b64_tr_b16 a[202:203], v220 offset:55296\n" \
"buffer_load_dwordx4 a[180:183], v210, %[srdz], s50 offen offset:1024 sc1\n" \
"v_mfma_f32_16x16x32_bf16 v[160:163], a[224:227], v[24:27], v[160:163]\n" \
"ds_read_b64_tr_b16 a[204:205], v220 offset:57344\n" \
"v_mfma_f32_16x16x32_bf16 v[164:167], a[208:211], v[48:51], v[164:167]\n" \
"ds_read_b64_tr_b16 a[206:207], v220 offset:59392\n" \
"v_mfma_f32_16x16x32_bf16 v[164:167], a[224:227], v[56:59], v[164:167]\n" \
"ds_read_b64_tr_b16 a[220:221], v220 offset:61440\n" \
"v_mfma_f32_16x16x32_bf16 v[168:171], a[208:211], v[80:83], v[168:171]\n" \
"ds_read_b64_tr_b16 a[222:223], v220 offset:63488\n" \
"buffer_load_dwordx4 a[184:187], v210, %[srdz], s50 offen offset:2048 sc1\n" \
"v_mfma_f32_16x16x32_bf16 v[168:171], a[224:227], v[88:91], v[168:171]\n" \
"v_mfma_f32_16x16x32_bf16 v[172:175], a[208:211], v[112:115], v[172:175]\n" \
"buffer_load_dwordx4 a[188:191], v210, %[srdz], s50 offen offset:3072 sc1\n" \
"v_mfma_f32_16x16x32_bf16 v[172:175], a[224:227], v[120:123], v[172:175]\n" \
"s_waitcnt vmcnt(63) lgkmcnt(4)\n" \
"s_nop 0\n" \
"v_mfma_f32_16x16x32_bf16 v[176:179], a[228:231], v[0:3], v[176:179]\n" \
"v_mfma_f32_16x16x32_bf16 v[176:179], a[236:239], v[8:11], v[176:179]\n" \
"v_mfma_f32_16x16x32_bf16 v[180:183], a[228:231], v[32:35], v[180:183]\n" \
"v_mfma_f32_16x16x32_bf16 v[180:183], a[236:239], v[40:43], v[180:183]\n" \
"v_mfma_f32_16x16x32_bf16 v[184:187], a[228:231], v[64:67], v[184:187]\n" \
"v_mfma_f32_16x16x32_bf16 v[184:187], a[236:239], v[72:75], v[184:187]\n" \
"v_mfma_f32_16x16x32_bf16 v[188:191], a[228:231], v[96:99], v[188:191]\n" \
"ds_read_b64_tr_b16 a[216:217], v221 offset:49152\n" \
"s_waitcnt vmcnt(22)\n" \
"v_cmp_eq_u32_e32 vcc, 0, v232\n" \
"s_cbranch_vccnz Lspp_%=\n" \
"Lbkp_%=:\n" \
"s_nop 0\n" \
"v_mfma_f32_16x16x32_bf16 v[188:191], a[236:239], v[104:107], v[188:191]\n" \
"ds_read_b64_tr_b16 a[218:219], v221 offset:51200\n" \
"v_mfma_f32_16x16x32_bf16 v[192:195], a[196:199], a[0:3], v[128:131]\n" \
"ds_read_b64_tr_b16 a[208:209], v221 offset:53248\n" \
"v_mfma_f32_16x16x32_bf16 v[192:195], a[200:203], a[4:7], v[192:195]\n" \
"ds_read_b64_tr_b16 a[210:211], v221 offset:55296\n" \
"s_add_u32 m0, %[m0b], 0\n" \
"s_cmp_gt_u32 s48, 3\n" \
"s_cselect_b32 s49, 3, 4\n" \
"s_lshl_b32 s49, s49, 18\n" \
"s_or_b32 s49, s49, s59\n" \
"s_nop 0\n" \
"buffer_load_dwordx4 %[wo0], %[srdt], s49 offen offset:0 lds\n" \
"v_mfma_f32_16x16x32_bf16 v[196:199], a[196:199], a[16:19], v[132:135]\n" \
"v_mfma_f32_16x16x32_bf16 v[196:199], a[200:203], a[20:23], v[196:199]\n" \
"v_mfma_f32_16x16x32_bf16 v[200:203], a[196:199], a[32:35], v[136:139]\n" \
"v_mfma_f32_16x16x32_bf16 v[200:203], a[200:203], a[36:39], v[200:203]\n" \
"v_mfma_f32_16x16x32_bf16 v[204:207], a[196:199], a[48:51], v[140:143]\n" \
"ds_read_b64_tr_b16 a[224:225], v221 offset:57344\n" \
"v_mfma_f32_16x16x32_bf16 v[204:207], a[200:203], a[52:55], v[204:207]\n" \
"s_waitcnt vmcnt(20) lgkmcnt(5)\n" \
"s_nop 0\n" \
"v_mfma_f32_16x16x32_bf16 v[192:195], a[204:207], a[8:11], v[192:195]\n" \
"ds_read_b64_tr_b16 a[226:227], v221 offset:59392\n" \
"v_mfma_f32_16x16x32_bf16 v[192:195], a[220:223], a[12:15], v[192:195]\n" \
"ds_read_b64_tr_b16 a[228:229], v221 offset:61440\n" \
"v_mfma_f32_16x16x32_bf16 v[196:199], a[204:207], a[24:27], v[196:199]\n" \
"ds_read_b64_tr_b16 a[230:231], v221 offset:63488\n" \
"v_mfma_f32_16x16x32_bf16 v[196:199], a[220:223], a[28:31], v[196:199]\n" \
"buffer_load_dwordx4 v212, %[srdt], s49 offen offset:1024 lds\n" \
"v_mfma_f32_16x16x32_bf16 v[200:203], a[204:207], a[40:43], v[200:203]\n" \
"v_mfma_f32_16x16x32_bf16 v[200:203], a[220:223], a[44:47], v[200:203]\n" \
"v_mfma_f32_16x16x32_bf16 v[204:207], a[204:207], a[56:59], v[204:207]\n" \
"ds_read_b64_tr_b16 a[236:237], v220 offset:49664\n" \
"v_mfma_f32_16x16x32_bf16 v[204:207], a[220:223], a[60:63], v[204:207]\n" \
"ds_read_b64_tr_b16 a[238:239], v220 offset:51712\n" \
"v_mfma_f32_16x16x32_bf16 v[176:179], a[232:235], v[16:19], v[176:179]\n" \
"v_mfma_f32_16x16x32_bf16 v[176:179], a[212:215], v[24:27], v[176:179]\n" \
"v_mfma_f32_16x16x32_bf16 v[180:183], a[232:235], v[48:51], v[180:183]\n" \
"buffer_load_dwordx4 v213, %[srdt], s49 offen offset:2048 lds\n" \
"v_mfma_f32_16x16x32_bf16 v[180:183], a[212:215], v[56:59], v[180:183]\n" \
"v_mfma_f32_16x16x32_bf16 v[184:187], a[232:235], v[80:83], v[184:187]\n" \
"s_add_u32 m0, %[m0b], 98304\n" \
"s_mov_b32 s61, 0\n" \
"s_nop 0\n" \
"buffer_load_dwordx4 %[vdn], s[64:67], s61 offen offset:0 lds\n" \
"v_mfma_f32_16x16x32_bf16 v[184:187], a[212:215], v[88:91], v[184:187]\n" \
"v_cmp_u_f32_e64 s[44:45], v192, v196\n" \
"v_mfma_f32_16x16x32_bf16 v[188:191], a[232:235], v[112:115], v[188:191]\n" \
"v_mfma_f32_16x16x32_bf16 v[188:191], a[212:215], v[120:123], v[188:191]\n" \
"v_cmp_u_f32_e64 s[56:57], v200, v204\n" \
"s_or_b64 s[56:57], s[56:57], s[44:45]\n" \
"s_cbranch_scc1 Lsp6_%=\n" \
"Lbk6_%=:\n" \
"s_waitcnt vmcnt(63) lgkmcnt(2)\n" \
"s_nop 0\n" \
"v_mfma_f32_16x16x32_bf16 v[144:147], a[216:219], a[0:3], v[144:147]\n" \
"ds_read_b64_tr_b16 a[232:233], v220 offset:53760\n" \
"v_mfma_f32_16x16x32_bf16 v[144:147], a[208:211], a[4:7], v[144:147]\n" \
"ds_read_b64_tr_b16 a[234:235], v220 offset:55808\n" \
"v_mfma_f32_16x16x32_bf16 v[148:151], a[216:219], a[16:19], v[148:151]\n" \
"ds_read_b64_tr_b16 a[196:197], v220 offset:57856\n" \
"v_mfma_f32_16x16x32_bf16 v[148:151], a[208:211], a[20:23], v[148:151]\n" \
"ds_read_b64_tr_b16 a[198:199], v220 offset:59904\n" \
"v_mfma_f32_16x16x32_bf16 v[152:155], a[216:219], a[32:35], v[152:155]\n" \
"ds_read_b64_tr_b16 a[200:201], v220 offset:61952\n" \
"v_mfma_f32_16x16x32_bf16 v[152:155], a[208:211], a[36:39], v[152:155]\n" \
"ds_read_b64_tr_b16 a[202:203], v220 offset:64000\n" \
"v_mfma_f32_16x16x32_bf16 v[156:159], a[216:219], a[48:51], v[156:159]\n" \
"ds_read_b64_tr_b16 a[204:205], v221 offset:49664\n" \
"v_mfma_f32_16x16x32_bf16 v[156:159], a[208:211], a[52:55], v[156:159]\n" \
"ds_read_b64_tr_b16 a[206:207], v221 offset:51712\n" \
"s_add_u32 m0, %[m0b], 0\n" \
"s_nop 0\n" \
"buffer_load_dwordx4 v214, %[srdt], s49 offen offset:3072 lds\n" \
"v_mfma_f32_16x16x32_bf16 v[144:147], a[224:227], a[8:11], v[144:147]\n" \
"ds_read_b64_tr_b16 a[212:213], v221 offset:57856\n" \
"v_mfma_f32_16x16x32_bf16 v[144:147], a[228:231], a[12:15], v[144:147]\n" \
"ds_read_b64_tr_b16 a[214:215], v221 offset:59904\n" \
"v_mfma_f32_16x16x32_bf16 v[148:151], a[224:227], a[24:27], v[148:151]\n" \
"ds_read_b64_tr_b16 a[220:221], v221 offset:53760\n" \
"v_mfma_f32_16x16x32_bf16 v[148:151], a[228:231], a[28:31], v[148:151]\n" \
"ds_read_b64_tr_b16 a[222:223], v221 offset:55808\n" \
"v_mfma_f32_16x16x32_bf16 v[152:155], a[224:227], a[40:43], v[152:155]\n" \
"ds_read_b64_tr_b16 a[216:217], v221 offset:61952\n" \
"v_mfma_f32_16x16x32_bf16 v[152:155], a[228:231], a[44:47], v[152:155]\n" \
"ds_read_b64_tr_b16 a[218:219], v221 offset:64000\n" \
"v_mfma_f32_16x16x32_bf16 v[156:159], a[224:227], a[56:59], v[156:159]\n" \
"s_add_u32 m0, %[m0b], 99328\n" \
"s_mov_b32 s61, 4096\n" \
"buffer_load_dwordx4 %[vdn], s[64:67], s61 offen offset:0 lds\n" \
"v_mfma_f32_16x16x32_bf16 v[156:159], a[228:231], a[60:63], v[156:159]\n" \
"s_waitcnt vmcnt(63) lgkmcnt(12)\n" \
"s_nop 0\n" \
"v_mfma_f32_16x16x32_bf16 v[160:163], a[236:239], a[0:3], v[160:163]\n" \
"v_mfma_f32_16x16x32_bf16 v[160:163], a[232:235], a[4:7], v[160:163]\n" \
"v_mfma_f32_16x16x32_bf16 v[164:167], a[236:239], a[16:19], v[164:167]\n" \
"v_mfma_f32_16x16x32_bf16 v[164:167], a[232:235], a[20:23], v[164:167]\n" \
"s_add_u32 m0, %[m0b], 16384\n" \
"s_cmp_gt_u32 s48, 4\n" \
"s_cselect_b32 s49, 4, 5\n" \
"s_lshl_b32 s49, s49, 18\n" \
"s_or_b32 s49, s49, s59\n" \
"buffer_load_dwordx4 %[wo0], %[srdt], s49 offen offset:0 lds\n" \
"v_mfma_f32_16x16x32_bf16 v[168:171], a[236:239], a[32:35], v[168:171]\n" \
"s_waitcnt vmcnt(14) lgkmcnt(0)\n" \
"s_barrier\n" \
"v_mfma_f32_16x16x32_bf16 v[168:171], a[232:235], a[36:39], v[168:171]\n" \
"ds_read_b64_tr_b16 a[208:209], v215 offset:49152\n" \
"v_mfma_f32_16x16x32_bf16 v[172:175], a[236:239], a[48:51], v[172:175]\n" \
"ds_read_b64_tr_b16 a[210:211], v215 offset:51200\n" \
"v_mfma_f32_16x16x32_bf16 v[172:175], a[232:235], a[52:55], v[172:175]\n" \
"ds_read_b64_tr_b16 a[224:225], v215 offset:53248\n" \
"v_mfma_f32_16x16x32_bf16 v[160:163], a[196:199], a[8:11], v[160:163]\n" \
"ds_read_b64_tr_b16 a[226:227], v215 offset:55296\n" \
"v_mfma_f32_16x16x32_bf16 v[160:163], a[200:203], a[12:15], v[160:163]\n" \
"ds_read_b64_tr_b16 a[228:229], v215 offset:57344\n" \
"v_mfma_f32_16x16x32_bf16 v[164:167], a[196:199], a[24:27], v[164:167]\n" \
"ds_read_b64_tr_b16 a[230:231], v215 offset:59392\n" \
"v_mfma_f32_16x16x32_bf16 v[164:167], a[200:203], a[28:31], v[164:167]\n" \
"ds_read_b64_tr_b16 a[236:237], v215 offset:61440\n" \
"v_mfma_f32_16x16x32_bf16 v[168:171], a[196:199], a[40:43], v[168:171]\n" \
"ds_read_b64_tr_b16 a[238:239], v215 offset:63488\n" \
"v_mfma_f32_16x16x32_bf16 v[168:171], a[200:203], a[44:47], v[168:171]\n" \
"buffer_load_dwordx4 v212, %[srdt], s49 offen offset:1024 lds\n" \
"v_mfma_f32_16x16x32_bf16 v[172:175], a[196:199], a[56:59], v[172:175]\n" \
"v_mfma_f32_16x16x32_bf16 v[172:175], a[200:203], a[60:63], v[172:175]\n" \
"s_waitcnt vmcnt(10) lgkmcnt(4)\n" \
"s_nop 0\n" \
"v_mfma_f32_16x16x32_bf16 v[176:179], a[204:207], a[0:3], v[176:179]\n" \
"s_add_u32 m0, %[m0b], 100352\n" \
"s_mov_b32 s61, 8192\n" \
"buffer_load_dwordx4 %[vdn], s[64:67], s61 offen offset:0 lds\n" \
"v_mfma_f32_16x16x32_bf16 v[176:179], a[220:223], a[4:7], v[176:179]\n" \
"v_mfma_f32_16x16x32_bf16 v[180:183], a[204:207], a[16:19], v[180:183]\n" \
"v_mfma_f32_16x16x32_bf16 v[180:183], a[220:223], a[20:23], v[180:183]\n" \
"v_mfma_f32_16x16x32_bf16 v[184:187], a[204:207], a[32:35], v[184:187]\n" \
"v_mfma_f32_16x16x32_bf16 v[184:187], a[220:223], a[36:39], v[184:187]\n" \
"v_mfma_f32_16x16x32_bf16 v[188:191], a[204:207], a[48:51], v[188:191]\n" \
"ds_read_b64_tr_b16 a[232:233], v227 offset:49152\n" \
"v_mfma_f32_16x16x32_bf16 v[188:191], a[220:223], a[52:55], v[188:191]\n" \
"ds_read_b64_tr_b16 a[234:235], v227 offset:51200\n" \
"v_mfma_f32_16x16x32_bf16 v[128:131], a[208:211], a[128:131], v[192:195]\n" \
"ds_read_b64_tr_b16 a[196:197], v227 offset:53248\n" \
"v_mfma_f32_16x16x32_bf16 v[128:131], a[224:227], a[132:135], v[128:131]\n" \
"ds_read_b64_tr_b16 a[198:199], v227 offset:55296\n" \
"v_mfma_f32_16x16x32_bf16 v[132:135], a[208:211], a[144:147], v[196:199]\n" \
"s_add_u32 m0, %[m0b], 16384\n" \
"s_nop 0\n" \
"s_nop 0\n" \
"buffer_load_dwordx4 v213, %[srdt], s49 offen offset:2048 lds\n" \
"v_mfma_f32_16x16x32_bf16 v[132:135], a[224:227], a[148:151], v[132:135]\n" \
"v_mfma_f32_16x16x32_bf16 v[136:139], a[208:211], a[160:163], v[200:203]\n" \
"v_mfma_f32_16x16x32_bf16 v[136:139], a[224:227], a[164:167], v[136:139]\n" \
"v_mfma_f32_16x16x32_bf16 v[140:143], a[208:211], a[176:179], v[204:207]\n" \
"ds_read_b64_tr_b16 a[200:201], v227 offset:57344\n" \
"v_mfma_f32_16x16x32_bf16 v[140:143], a[224:227], a[180:183], v[140:143]\n" \
"s_waitcnt vmcnt(10) lgkmcnt(5)\n" \
"s_nop 0\n" \
"v_mfma_f32_16x16x32_bf16 v[128:131], a[228:231], a[136:139], v[128:131]\n" \
"ds_read_b64_tr_b16 a[202:203], v227 offset:59392\n" \
"v_mfma_f32_16x16x32_bf16 v[128:131], a[236:239], a[140:143], v[128:131]\n" \
"ds_read_b64_tr_b16 a[204:205], v227 offset:61440\n" \
"s_add_u32 m0, %[m0b], 101376\n" \
"s_mov_b32 s61, 12288\n" \
"buffer_load_dwordx4 %[vdn], s[64:67], s61 offen offset:0 lds\n" \
"v_mfma_f32_16x16x32_bf16 v[132:135], a[228:231], a[152:155], v[132:135]\n" \
"ds_read_b64_tr_b16 a[206:207], v227 offset:63488\n" \
"v_mfma_f32_16x16x32_bf16 v[132:135], a[236:239], a[156:159], v[132:135]\n" \
"s_add_u32 m0, %[m0b], 16384\n" \
"s_nop 0\n" \
"s_nop 0\n" \
"buffer_load_dwordx4 v214, %[srdt], s49 offen offset:3072 lds\n" \
"v_mfma_f32_16x16x32_bf16 v[136:139], a[228:231], a[168:171], v[136:139]\n" \
"v_mfma_f32_16x16x32_bf16 v[136:139], a[236:239], a[172:175], v[136:139]\n" \
"v_mfma_f32_16x16x32_bf16 v[140:143], a[228:231], a[184:187], v[140:143]\n" \
"ds_read_b64_tr_b16 a[220:221], v215 offset:49664\n" \
"v_mfma_f32_16x16x32_bf16 v[140:143], a[236:239], a[188:191], v[140:143]\n" \
"ds_read_b64_tr_b16 a[222:223], v215 offset:51712\n" \
"v_mfma_f32_16x16x32_bf16 v[176:179], a[212:215], a[8:11], v[176:179]\n" \
"v_mfma_f32_16x16x32_bf16 v[176:179], a[216:219], a[12:15], v[176:179]\n" \
"v_mfma_f32_16x16x32_bf16 v[180:183], a[212:215], a[24:27], v[180:183]\n" \
"v_mfma_f32_16x16x32_bf16 v[180:183], a[216:219], a[28:31], v[180:183]\n" \
"v_mfma_f32_16x16x32_bf16 v[184:187], a[212:215], a[40:43], v[184:187]\n" \
"v_mfma_f32_16x16x32_bf16 v[184:187], a[216:219], a[44:47], v[184:187]\n" \
"v_cmp_u_f32_e64 s[44:45], v128, v132\n" \
"v_mfma_f32_16x16x32_bf16 v[188:191], a[212:215], a[56:59], v[188:191]\n" \
"v_mfma_f32_16x16x32_bf16 v[188:191], a[216:219], a[60:63], v[188:191]\n" \
"v_cmp_u_f32_e64 s[56:57], v136, v140\n" \
"s_or_b64 s[56:57], s[56:57], s[44:45]\n" \
"s_cbranch_scc1 Lsp7_%=\n" \
"Lbk7_%=:\n" \
"s_waitcnt vmcnt(63) lgkmcnt(2)\n" \
"s_nop 0\n" \
"v_mfma_f32_16x16x32_bf16 v[144:147], a[232:235], a[128:131], v[144:147]\n" \
"ds_read_b64_tr_b16 a[212:213], v215 offset:53760\n" \
"v_mfma_f32_16x16x32_bf16 v[144:147], a[196:199], a[132:135], v[144:147]\n" \
"ds_read_b64_tr_b16 a[214:215], v215 offset:55808\n" \
"v_mfma_f32_16x16x32_bf16 v[148:151], a[232:235], a[144:147], v[148:151]\n" \
"ds_read_b64_tr_b16 a[208:209], v215 offset:57856\n" \
"v_mfma_f32_16x16x32_bf16 v[148:151], a[196:199], a[148:151], v[148:151]\n" \
"ds_read_b64_tr_b16 a[210:211], v215 offset:59904\n" \
"s_lshl_b32 s61, %[wid], 10\n" \
"v_lshrrev_b32_e32 v231, 3, v224\n" \
"v_add_u32_e64 v231, s61, v231\n" \
"s_add_u32 m0, %[m0b], 32768\n" \
"s_cmp_gt_u32 s48, 5\n" \
"s_cselect_b32 s49, 5, 6\n" \
"s_lshl_b32 s49, s49, 18\n" \
"s_or_b32 s49, s49, s59\n" \
".if . - adaln_body != 53248\n" \
".error \"size model\"\n" \
".endif\n" \
"Lk12_%=:\n" \
"s_cbranch_execz Lk13_%=\n" \
"s_nop 0\n" \
"buffer_load_dwordx4 %[wo0], %[srdt], s49 offen offset:0 lds\n" \
"v_mfma_f32_16x16x32_bf16 v[152:155], a[232:235], a[160:163], v[152:155]\n" \
"ds_read_b64_tr_b16 a[216:217], v215 offset:61952\n" \
"v_readlane_b32 s44, %[tlo], 20\n" \
"v_readlane_b32 s45, %[thi], 20\n" \
"v_lshrrev_b32_e64 v236, 1, v231\n" \
"v_mfma_f32_16x16x32_bf16 v[152:155], a[196:199], a[164:167], v[152:155]\n" \
"ds_read_b64_tr_b16 a[218:219], v215 offset:64000\n" \
"v_mfma_f32_16x16x32_bf16 v[156:159], a[232:235], a[176:179], v[156:159]\n" \
"ds_read_b64_tr_b16 a[224:225], v227 offset:49664\n" \
"v_mfma_f32_16x16x32_bf16 v[156:159], a[196:199], a[180:183], v[156:159]\n" \
"ds_read_b64_tr_b16 a[226:227], v227 offset:51712\n" \
"v_mfma_f32_16x16x32_bf16 v[144:147], a[200:203], a[136:139], v[144:147]\n" \
"ds_read_b64_tr_b16 a[228:229], v227 offset:53760\n" \
"v_mfma_f32_16x16x32_bf16 v[144:147], a[204:207], a[140:143], v[144:147]\n" \
"ds_read_b64_tr_b16 a[230:231], v227 offset:55808\n" \
"v_mfma_f32_16x16x32_bf16 v[148:151], a[200:203], a[152:155], v[148:151]\n" \
"s_mov_b32 s61, 262144\n" \
"buffer_load_dwordx4 v[192:195], v231, %[srdz], s61 offen offset:0 sc1\n" \
"v_mfma_f32_16x16x32_bf16 v[148:151], a[204:207], a[156:159], v[148:151]\n" \
"v_mfma_f32_16x16x32_bf16 v[152:155], a[200:203], a[168:171], v[152:155]\n" \
"buffer_load_dwordx4 v212, %[srdt], s49 offen offset:1024 lds\n" \
"v_mfma_f32_16x16x32_bf16 v[152:155], a[204:207], a[172:175], v[152:155]\n" \
"v_mfma_f32_16x16x32_bf16 v[156:159], a[200:203], a[184:187], v[156:159]\n" \
"ds_read_b64_tr_b16 a[236:237], v227 offset:57856\n" \
"v_mfma_f32_16x16x32_bf16 v[156:159], a[204:207], a[188:191], v[156:159]\n" \
"s_waitcnt vmcnt(63) lgkmcnt(5)\n" \
"s_nop 0\n" \
"v_mfma_f32_16x16x32_bf16 v[160:163], a[220:223], a[128:131], v[160:163]\n" \
"ds_read_b64_tr_b16 a[238:239], v227 offset:59904\n" \
"v_mfma_f32_16x16x32_bf16 v[160:163], a[212:215], a[132:135], v[160:163]\n" \
"ds_read_b64_tr_b16 a[232:233], v227 offset:61952\n" \
"v_mfma_f32_16x16x32_bf16 v[164:167], a[220:223], a[144:147], v[164:167]\n" \
"ds_read_b64_tr_b16 a[234:235], v227 offset:64000\n" \
"s_mov_b32 s61, 266240\n" \
"buffer_load_dwordx4 v[196:199], v231, %[srdz], s61 offen offset:0 sc1\n" \
"v_mfma_f32_16x16x32_bf16 v[164:167], a[212:215], a[148:151], v[164:167]\n" \
"v_mfma_f32_16x16x32_bf16 v[168:171], a[220:223], a[160:163], v[168:171]\n" \
"v_cvt_pk_bf16_f32 v128, v128, v129\n" \
"buffer_load_dwordx4 v213, %[srdt], s49 offen offset:2048 lds\n" \
"v_mfma_f32_16x16x32_bf16 v[168:171], a[212:215], a[164:167], v[168:171]\n" \
"v_cvt_pk_bf16_f32 v129, v130, v131\n" \
"v_mfma_f32_16x16x32_bf16 v[172:175], a[220:223], a[176:179], v[172:175]\n" \
"v_cvt_pk_bf16_f32 v130, v144, v145\n" \
"v_mfma_f32_16x16x32_bf16 v[172:175], a[212:215], a[180:183], v[172:175]\n" \
"v_cvt_pk_bf16_f32 v131, v146, v147\n" \
"v_mfma_f32_16x16x32_bf16 v[160:163], a[208:211], a[136:139], v[160:163]\n" \
"s_add_u32 s55, s46, 0\n" \
"s_nop 0\n" \
"buffer_store_dwordx4 v[128:131], %[vxs], %[srdz], s55 offen offset:0 sc1\n" \
"v_mfma_f32_16x16x32_bf16 v[160:163], a[216:219], a[140:143], v[160:163]\n" \
"v_mfma_f32_16x16x32_bf16 v[164:167], a[208:211], a[152:155], v[164:167]\n" \
"v_cvt_pk_bf16_f32 v132, v132, v133\n" \
"s_mov_b32 s61, 270336\n" \
"buffer_load_dwordx4 v[200:203], v231, %[srdz], s61 offen offset:0 sc1\n" \
"v_mfma_f32_16x16x32_bf16 v[164:167], a[216:219], a[156:159], v[164:167]\n" \
"v_cvt_pk_bf16_f32 v133, v134, v135\n" \
"v_mfma_f32_16x16x32_bf16 v[168:171], a[208:211], a[168:171], v[168:171]\n" \
"s_waitcnt vmcnt(14) lgkmcnt(0)\n" \
"s_barrier\n" \
"v_mfma_f32_16x16x32_bf16 v[168:171], a[216:219], a[172:175], v[168:171]\n" \
"ds_read_b64_tr_b16 a[196:197], v215 offset:0\n" \
"v_cvt_pk_bf16_f32 v134, v148, v149\n" \
"v_mfma_f32_16x16x32_bf16 v[172:175], a[208:211], a[184:187], v[172:175]\n" \
"ds_read_b64_tr_b16 a[198:199], v215 offset:2048\n" \
"v_cvt_pk_bf16_f32 v135, v150, v151\n" \
"v_mfma_f32_16x16x32_bf16 v[172:175], a[216:219], a[188:191], v[172:175]\n" \
"s_waitcnt vmcnt(63) lgkmcnt(2)\n" \
"s_nop 0\n" \
"v_mfma_f32_16x16x32_bf16 v[176:179], a[224:227], a[128:131], v[176:179]\n" \
"ds_read_b64_tr_b16 a[200:201], v215 offset:4096\n" \
"v_cvt_pk_bf16_f32 v136, v136, v137\n" \
"v_mfma_f32_16x16x32_bf16 v[176:179], a[228:231], a[132:135], v[176:179]\n" \
"ds_read_b64_tr_b16 a[202:203], v215 offset:6144\n" \
"v_cvt_pk_bf16_f32 v137, v138, v139\n" \
"v_mfma_f32_16x16x32_bf16 v[180:183], a[224:227], a[144:147], v[180:183]\n" \
"ds_read_b64_tr_b16 a[204:205], v215 offset:8192\n" \
"s_add_u32 s55, s46, 524288\n" \
"buffer_store_dwordx4 v[132:135], %[vxs], %[srdz], s55 offen offset:0 sc1\n" \
"v_mfma_f32_16x16x32_bf16 v[180:183], a[228:231], a[148:151], v[180:183]\n" \
"ds_read_b64_tr_b16 a[206:207], v215 offset:10240\n" \
"v_cvt_pk_bf16_f32 v138, v152, v153\n" \
"v_mfma_f32_16x16x32_bf16 v[184:187], a[224:227], a[160:163], v[184:187]\n" \
"ds_read_b64_tr_b16 a[220:221], v215 offset:12288\n" \
"v_cvt_pk_bf16_f32 v139, v154, v155\n" \
"s_mov_b32 s61, 274432\n" \
"buffer_load_dwordx4 v[204:207], v231, %[srdz], s61 offen offset:0 sc1\n" \
"v_mfma_f32_16x16x32_bf16 v[184:187], a[228:231], a[164:167], v[184:187]\n" \
"ds_read_b64_tr_b16 a[222:223], v215 offset:14336\n" \
"v_mfma_f32_16x16x32_bf16 v[188:191], a[224:227], a[176:179], v[188:191]\n" \
"ds_read_b64_tr_b16 a[212:213], v227 offset:0\n" \
"v_cvt_pk_bf16_f32 v140, v140, v141\n" \
"buffer_load_dwordx4 v214, %[srdt], s49 offen offset:3072 lds\n" \
"v_mfma_f32_16x16x32_bf16 v[188:191], a[228:231], a[180:183], v[188:191]\n" \
"ds_read_b64_tr_b16 a[214:215], v227 offset:2048\n" \
"v_cvt_pk_bf16_f32 v141, v142, v143\n" \
"v_mfma_f32_16x16x32_bf16 v[176:179], a[236:239], a[136:139], v[176:179]\n" \
"ds_read_b64_tr_b16 a[208:209], v227 offset:4096\n" \
"s_add_u32 s55, s46, 1048576\n" \
"buffer_store_dwordx4 v[136:139], %[vxs], %[srdz], s55 offen offset:0 sc1\n" \
"v_mfma_f32_16x16x32_bf16 v[176:179], a[232:235], a[140:143], v[176:179]\n" \
"ds_read_b64_tr_b16 a[210:211], v227 offset:6144\n" \
"v_cvt_pk_bf16_f32 v142, v156, v157\n" \
"v_mfma_f32_16x16x32_bf16 v[180:183], a[236:239], a[152:155], v[180:183]\n" \
"v_cvt_pk_bf16_f32 v143, v158, v159\n" \
"v_mfma_f32_16x16x32_bf16 v[180:183], a[232:235], a[156:159], v[180:183]\n" \
"s_add_u32 s55, s46, 1572864\n" \
"buffer_store_dwordx4 v[140:143], %[vxs], %[srdz], s55 offen offset:0 sc1\n" \
"v_mfma_f32_16x16x32_bf16 v[184:187], a[236:239], a[168:171], v[184:187]\n" \
"v_mfma_f32_16x16x32_bf16 v[184:187], a[232:235], a[172:175], v[184:187]\n" \
"global_load_dwordx2 v[232:233], v236, s[44:45]\n" \
"v_mfma_f32_16x16x32_bf16 v[188:191], a[236:239], a[184:187], v[188:191]\n" \
"ds_read_b64_tr_b16 a[216:217], v227 offset:8192\n" \
"v_mfma_f32_16x16x32_bf16 v[188:191], a[232:235], a[188:191], v[188:191]\n" \
"s_waitcnt vmcnt(63) lgkmcnt(5)\n" \
"s_nop 0\n" \
"v_mfma_f32_16x16x32_bf16 v[4:7], a[196:199], a[64:67], v[4:7]\n" \
"ds_read_b64_tr_b16 a[218:219], v227 offset:10240\n" \
"v_mfma_f32_16x16x32_bf16 v[4:7], a[200:203], a[68:71], v[4:7]\n" \
"ds_read_b64_tr_b16 a[224:225], v227 offset:12288\n" \
"v_mfma_f32_16x16x32_bf16 v[36:39], a[196:199], a[80:83], v[36:39]\n" \
"ds_read_b64_tr_b16 a[226:227], v227 offset:14336\n" \
"v_mfma_f32_16x16x32_bf16 v[36:39], a[200:203], a[84:87], v[36:39]\n" \
"v_mfma_f32_16x16x32_bf16 v[68:71], a[196:199], a[96:99], v[68:71]\n" \
"v_cvt_pk_bf16_f32 v160, v160, v161\n" \
"v_mfma_f32_16x16x32_bf16 v[68:71], a[200:203], a[100:103], v[68:71]\n" \
"v_cvt_pk_bf16_f32 v161, v162, v163\n" \
"v_mfma_f32_16x16x32_bf16 v[100:103], a[196:199], a[112:115], v[100:103]\n" \
"ds_read_b64_tr_b16 a[228:229], v215 offset:512\n" \
"v_cvt_pk_bf16_f32 v162, v176, v177\n" \
"v_mfma_f32_16x16x32_bf16 v[100:103], a[200:203], a[116:119], v[100:103]\n" \
"ds_read_b64_tr_b16 a[230:231], v215 offset:2560\n" \
"v_cvt_pk_bf16_f32 v163, v178, v179\n" \
"v_mfma_f32_16x16x32_bf16 v[4:7], a[204:207], a[72:75], v[4:7]\n" \
"ds_read_b64_tr_b16 a[236:237], v215 offset:4608\n" \
"v_mfma_f32_16x16x32_bf16 v[4:7], a[220:223], a[76:79], v[4:7]\n" \
"ds_read_b64_tr_b16 a[238:239], v215 offset:6656\n" \
"v_mfma_f32_16x16x32_bf16 v[36:39], a[204:207], a[88:91], v[36:39]\n" \
"s_add_u32 s55, s46, 4096\n" \
"buffer_store_dwordx4 v[160:163], %[vxs], %[srdz], s55 offen offset:0 sc1\n" \
"v_mfma_f32_16x16x32_bf16 v[36:39], a[220:223], a[92:95], v[36:39]\n" \
"v_cvt_pk_bf16_f32 v164, v164, v165\n" \
"v_cvt_pk_bf16_f32 v165, v166, v167\n" \
"v_mfma_f32_16x16x32_bf16 v[68:71], a[204:207], a[104:107], v[68:71]\n" \
"v_cvt_pk_bf16_f32 v166, v180, v181\n" \
"s_add_u32 m0, %[m0b], 49152\n" \
"s_cmp_gt_u32 s48, 6\n" \
"s_cselect_b32 s49, 6, 7\n" \
"s_lshl_b32 s49, s49, 18\n" \
"s_or_b32 s49, s49, s59\n" \
"buffer_load_dwordx4 %[wo0], %[srdt], s49 offen offset:0 lds\n" \
"v_mfma_f32_16x16x32_bf16 v[68:71], a[220:223], a[108:111], v[68:71]\n" \
"v_cvt_pk_bf16_f32 v167, v182, v183\n" \
"v_mfma_f32_16x16x32_bf16 v[100:103], a[204:207], a[120:123], v[100:103]\n" \
"ds_read_b64_tr_b16 a[232:233], v215 offset:8704\n" \
"v_mfma_f32_16x16x32_bf16 v[100:103], a[220:223], a[124:127], v[100:103]\n" \
"s_waitcnt vmcnt(63) lgkmcnt(5)\n" \
"s_nop 0\n" \
"v_mfma_f32_16x16x32_bf16 v[12:15], a[212:215], a[64:67], v[12:15]\n" \
"ds_read_b64_tr_b16 a[234:235], v215 offset:10752\n" \
"v_cvt_pk_bf16_f32 v168, v168, v169\n" \
"v_mfma_f32_16x16x32_bf16 v[12:15], a[208:211], a[68:71], v[12:15]\n" \
"ds_read_b64_tr_b16 a[196:197], v215 offset:12800\n" \
"s_add_u32 s55, s46, 528384\n" \
"buffer_store_dwordx4 v[164:167], %[vxs], %[srdz], s55 offen offset:0 sc1\n" \
"v_mfma_f32_16x16x32_bf16 v[44:47], a[212:215], a[80:83], v[44:47]\n" \
"ds_read_b64_tr_b16 a[198:199], v215 offset:14848\n" \
"v_cvt_pk_bf16_f32 v169, v170, v171\n" \
"v_mfma_f32_16x16x32_bf16 v[44:47], a[208:211], a[84:87], v[44:47]\n" \
"v_cvt_pk_bf16_f32 v170, v184, v185\n" \
"v_cvt_pk_bf16_f32 v171, v186, v187\n" \
"v_mfma_f32_16x16x32_bf16 v[76:79], a[212:215], a[96:99], v[76:79]\n" \
"s_add_u32 s55, s46, 1052672\n" \
"buffer_store_dwordx4 v[168:171], %[vxs], %[srdz], s55 offen offset:0 sc1\n" \
"v_mfma_f32_16x16x32_bf16 v[76:79], a[208:211], a[100:103], v[76:79]\n" \
"v_mfma_f32_16x16x32_bf16 v[108:111], a[212:215], a[112:115], v[108:111]\n" \
"ds_read_b64_tr_b16 a[200:201], v227 offset:512\n" \
"v_cvt_pk_bf16_f32 v172, v172, v173\n" \
"buffer_load_dwordx4 v212, %[srdt], s49 offen offset:1024 lds\n" \
"v_mfma_f32_16x16x32_bf16 v[108:111], a[208:211], a[116:119], v[108:111]\n" \
"ds_read_b64_tr_b16 a[202:203], v227 offset:2560\n" \
"v_cvt_pk_bf16_f32 v173, v174, v175\n" \
"v_mfma_f32_16x16x32_bf16 v[12:15], a[216:219], a[72:75], v[12:15]\n" \
"ds_read_b64_tr_b16 a[204:205], v227 offset:4608\n" \
"v_cvt_pk_bf16_f32 v174, v188, v189\n" \
"v_mfma_f32_16x16x32_bf16 v[12:15], a[224:227], a[76:79], v[12:15]\n" \
"ds_read_b64_tr_b16 a[206:207], v227 offset:6656\n" \
"v_cvt_pk_bf16_f32 v175, v190, v191\n" \
"v_mfma_f32_16x16x32_bf16 v[44:47], a[216:219], a[88:91], v[44:47]\n" \
"s_add_u32 s55, s46, 1576960\n" \
"buffer_store_dwordx4 v[172:175], %[vxs], %[srdz], s55 offen offset:0 sc1\n" \
"v_mfma_f32_16x16x32_bf16 v[44:47], a[224:227], a[92:95], v[44:47]\n" \
"v_mfma_f32_16x16x32_bf16 v[76:79], a[216:219], a[104:107], v[76:79]\n" \
"buffer_load_dwordx4 v213, %[srdt], s49 offen offset:2048 lds\n" \
"v_mfma_f32_16x16x32_bf16 v[76:79], a[224:227], a[108:111], v[76:79]\n" \
"v_mfma_f32_16x16x32_bf16 v[108:111], a[216:219], a[120:123], v[108:111]\n" \
"ds_read_b64_tr_b16 a[220:221], v227 offset:8704\n" \
"v_accvgpr_read_b32 v129, a240\n" \
"v_mfma_f32_16x16x32_bf16 v[108:111], a[224:227], a[124:127], v[108:111]\n" \
"s_waitcnt vmcnt(63) lgkmcnt(5)\n" \
"s_nop 0\n" \
"v_mfma_f32_16x16x32_bf16 v[20:23], a[228:231], a[64:67], v[20:23]\n" \
"ds_read_b64_tr_b16 a[222:223], v227 offset:10752\n" \
"v_lshlrev_b32_e64 v128, 16, v129\n" \
"v_mfma_f32_16x16x32_bf16 v[20:23], a[236:239], a[68:71], v[20:23]\n" \
"ds_read_b64_tr_b16 a[212:213], v227 offset:12800\n" \
"v_and_b32_e32 v129, 0xffff0000, v129\n" \
"v_mfma_f32_16x16x32_bf16 v[52:55], a[228:231], a[80:83], v[52:55]\n" \
"ds_read_b64_tr_b16 a[214:215], v227 offset:14848\n" \
"v_accvgpr_read_b32 v131, a241\n" \
"v_mfma_f32_16x16x32_bf16 v[52:55], a[236:239], a[84:87], v[52:55]\n" \
"v_lshlrev_b32_e64 v130, 16, v131\n" \
"v_and_b32_e32 v131, 0xffff0000, v131\n" \
"global_load_dword v237, %[vct], %[pct]\n" \
"v_mfma_f32_16x16x32_bf16 v[84:87], a[228:231], a[96:99], v[84:87]\n" \
"v_accvgpr_read_b32 v133, a242\n" \
"v_mfma_f32_16x16x32_bf16 v[84:87], a[236:239], a[100:103], v[84:87]\n" \
"v_lshlrev_b32_e64 v132, 16, v133\n" \
"s_add_u32 m0, %[m0b], 114688\n" \
"s_mov_b32 s61, 16384\n" \
"buffer_load_dwordx4 %[vdn], s[64:67], s61 offen offset:0 lds\n" \
"v_mfma_f32_16x16x32_bf16 v[116:119], a[228:231], a[112:115], v[116:119]\n" \
"v_and_b32_e32 v133, 0xffff0000, v133\n" \
"v_mfma_f32_16x16x32_bf16 v[116:119], a[236:239], a[116:119], v[116:119]\n" \
"v_accvgpr_read_b32 v135, a243\n" \
"s_add_u32 m0, %[m0b], 49152\n" \
"s_nop 0\n" \
"s_nop 0\n" \
"buffer_load_dwordx4 v214, %[srdt], s49 offen offset:3072 lds\n" \
"v_mfma_f32_16x16x32_bf16 v[20:23], a[232:235], a[72:75], v[20:23]\n" \
"v_lshlrev_b32_e64 v134, 16, v135\n" \
"v_mfma_f32_16x16x32_bf16 v[20:23], a[196:199], a[76:79], v[20:23]\n" \
"v_and_b32_e32 v135, 0xffff0000, v135\n" \
"v_mfma_f32_16x16x32_bf16 v[52:55], a[232:235], a[88:91], v[52:55]\n" \
"v_accvgpr_read_b32 v137, a244\n" \
"v_mfma_f32_16x16x32_bf16 v[52:55], a[196:199], a[92:95], v[52:55]\n" \
"v_lshlrev_b32_e64 v136, 16, v137\n" \
"v_mfma_f32_16x16x32_bf16 v[84:87], a[232:235], a[104:107], v[84:87]\n" \
"s_waitcnt vmcnt(23) lgkmcnt(0)\n" \
"s_barrier\n" \
"v_mfma_f32_16x16x32_bf16 v[84:87], a[196:199], a[108:111], v[84:87]\n" \
"ds_read_b64_tr_b16 a[208:209], v215 offset:16384\n" \
"v_and_b32_e32 v137, 0xffff0000, v137\n" \
"v_mfma_f32_16x16x32_bf16 v[116:119], a[232:235], a[120:123], v[116:119]\n" \
"ds_read_b64_tr_b16 a[210:211], v215 offset:18432\n" \
"v_accvgpr_read_b32 v139, a245\n" \
"v_mfma_f32_16x16x32_bf16 v[116:119], a[196:199], a[124:127], v[116:119]\n" \
"s_waitcnt vmcnt(63) lgkmcnt(2)\n" \
"s_nop 0\n" \
"v_mfma_f32_16x16x32_bf16 v[28:31], a[200:203], a[64:67], v[28:31]\n" \
"ds_read_b64_tr_b16 a[216:217], v215 offset:20480\n" \
"v_lshlrev_b32_e64 v138, 16, v139\n" \
"v_mfma_f32_16x16x32_bf16 v[28:31], a[204:207], a[68:71], v[28:31]\n" \
"ds_read_b64_tr_b16 a[218:219], v215 offset:22528\n" \
"v_and_b32_e32 v139, 0xffff0000, v139\n" \
"v_mfma_f32_16x16x32_bf16 v[60:63], a[200:203], a[80:83], v[60:63]\n" \
"ds_read_b64_tr_b16 a[224:225], v215 offset:24576\n" \
"v_accvgpr_read_b32 v141, a246\n" \
"v_mfma_f32_16x16x32_bf16 v[60:63], a[204:207], a[84:87], v[60:63]\n" \
"ds_read_b64_tr_b16 a[226:227], v215 offset:26624\n" \
"v_lshlrev_b32_e64 v140, 16, v141\n" \
"v_mfma_f32_16x16x32_bf16 v[92:95], a[200:203], a[96:99], v[92:95]\n" \
"ds_read_b64_tr_b16 a[228:229], v215 offset:28672\n" \
"v_and_b32_e32 v141, 0xffff0000, v141\n" \
"s_add_u32 m0, %[m0b], 115712\n" \
"s_mov_b32 s61, 20480\n" \
"buffer_load_dwordx4 %[vdn], s[64:67], s61 offen offset:0 lds\n" \
"v_mfma_f32_16x16x32_bf16 v[92:95], a[204:207], a[100:103], v[92:95]\n" \
"ds_read_b64_tr_b16 a[230:231], v215 offset:30720\n" \
"v_accvgpr_read_b32 v143, a247\n" \
"v_mfma_f32_16x16x32_bf16 v[124:127], a[200:203], a[112:115], v[124:127]\n" \
"ds_read_b64_tr_b16 a[236:237], v227 offset:16384\n" \
"v_lshlrev_b32_e64 v142, 16, v143\n" \
"v_mfma_f32_16x16x32_bf16 v[124:127], a[204:207], a[116:119], v[124:127]\n" \
"ds_read_b64_tr_b16 a[238:239], v227 offset:18432\n" \
"v_and_b32_e32 v143, 0xffff0000, v143\n" \
"v_mfma_f32_16x16x32_bf16 v[28:31], a[220:223], a[72:75], v[28:31]\n" \
"ds_read_b64_tr_b16 a[232:233], v227 offset:20480\n" \
"v_accvgpr_read_b32 v145, a248\n" \
"v_mfma_f32_16x16x32_bf16 v[28:31], a[212:215], a[76:79], v[28:31]\n" \
"ds_read_b64_tr_b16 a[234:235], v227 offset:22528\n" \
"v_lshlrev_b32_e64 v144, 16, v145\n" \
"v_mfma_f32_16x16x32_bf16 v[60:63], a[220:223], a[88:91], v[60:63]\n" \
"v_and_b32_e32 v145, 0xffff0000, v145\n" \
"v_accvgpr_read_b32 v147, a249\n" \
"v_mfma_f32_16x16x32_bf16 v[60:63], a[212:215], a[92:95], v[60:63]\n" \
"v_lshlrev_b32_e64 v146, 16, v147\n" \
"v_and_b32_e32 v147, 0xffff0000, v147\n" \
"v_mfma_f32_16x16x32_bf16 v[92:95], a[220:223], a[104:107], v[92:95]\n" \
"v_accvgpr_read_b32 v149, a250\n" \
"v_mfma_f32_16x16x32_bf16 v[92:95], a[212:215], a[108:111], v[92:95]\n" \
"v_lshlrev_b32_e64 v148, 16, v149\n" \
"v_mfma_f32_16x16x32_bf16 v[124:127], a[220:223], a[120:123], v[124:127]\n" \
"ds_read_b64_tr_b16 a[196:197], v227 offset:24576\n" \
"v_and_b32_e32 v149, 0xffff0000, v149\n" \
"v_mfma_f32_16x16x32_bf16 v[124:127], a[212:215], a[124:127], v[124:127]\n" \
"s_waitcnt vmcnt(63) lgkmcnt(5)\n" \
"s_nop 0\n" \
"v_mfma_f32_16x16x32_bf16 v[4:7], a[208:211], v[0:3], v[4:7]\n" \
"ds_read_b64_tr_b16 a[198:199], v227 offset:26624\n" \
"v_accvgpr_read_b32 v151, a251\n" \
"v_mfma_f32_16x16x32_bf16 v[4:7], a[216:219], v[8:11], v[4:7]\n" \
"ds_read_b64_tr_b16 a[200:201], v227 offset:28672\n" \
"v_lshlrev_b32_e64 v150, 16, v151\n" \
"v_mfma_f32_16x16x32_bf16 v[36:39], a[208:211], v[32:35], v[36:39]\n" \
"ds_read_b64_tr_b16 a[202:203], v227 offset:30720\n" \
"v_and_b32_e32 v151, 0xffff0000, v151\n" \
"v_mfma_f32_16x16x32_bf16 v[36:39], a[216:219], v[40:43], v[36:39]\n" \
"v_accvgpr_read_b32 v153, a252\n" \
"v_lshlrev_b32_e64 v152, 16, v153\n" \
"s_add_u32 m0, %[m0b], 116736\n" \
"s_mov_b32 s61, 24576\n" \
"buffer_load_dwordx4 %[vdn], s[64:67], s61 offen offset:0 lds\n" \
"v_mfma_f32_16x16x32_bf16 v[68:71], a[208:211], v[64:67], v[68:71]\n" \
"v_and_b32_e32 v153, 0xffff0000, v153\n" \
"v_mfma_f32_16x16x32_bf16 v[68:71], a[216:219], v[72:75], v[68:71]\n" \
"v_accvgpr_read_b32 v155, a253\n" \
"v_mfma_f32_16x16x32_bf16 v[100:103], a[208:211], v[96:99], v[100:103]\n" \
"v_lshlrev_b32_e64 v154, 16, v155\n" \
"v_mfma_f32_16x16x32_bf16 v[100:103], a[216:219], v[104:107], v[100:103]\n" \
"v_and_b32_e32 v155, 0xffff0000, v155\n" \
"v_mfma_f32_16x16x32_bf16 v[4:7], a[224:227], v[16:19], v[4:7]\n" \
"v_accvgpr_read_b32 v157, a254\n" \
"v_mfma_f32_16x16x32_bf16 v[4:7], a[228:231], v[24:27], v[4:7]\n" \
"v_lshlrev_b32_e64 v156, 16, v157\n" \
"v_mfma_f32_16x16x32_bf16 v[36:39], a[224:227], v[48:51], v[36:39]\n" \
"v_and_b32_e32 v157, 0xffff0000, v157\n" \
"v_mfma_f32_16x16x32_bf16 v[36:39], a[228:231], v[56:59], v[36:39]\n" \
"v_accvgpr_read_b32 v159, a255\n" \
"v_mfma_f32_16x16x32_bf16 v[68:71], a[224:227], v[80:83], v[68:71]\n" \
"s_waitcnt vmcnt(15) lgkmcnt(15)\n" \
"s_barrier\n" \
"v_mfma_f32_16x16x32_bf16 v[68:71], a[228:231], v[88:91], v[68:71]\n" \
"ds_read_b64_tr_b16 a[204:205], v215 offset:32768\n" \
"v_lshlrev_b32_e64 v158, 16, v159\n" \
"v_mfma_f32_16x16x32_bf16 v[100:103], a[224:227], v[112:115], v[100:103]\n" \
"ds_read_b64_tr_b16 a[206:207], v215 offset:34816\n" \
"v_and_b32_e32 v159, 0xffff0000, v159\n" \
"v_mfma_f32_16x16x32_bf16 v[100:103], a[228:231], v[120:123], v[100:103]\n" \
"s_waitcnt vmcnt(63) lgkmcnt(2)\n" \
"s_nop 0\n" \
"v_mfma_f32_16x16x32_bf16 v[12:15], a[236:239], v[0:3], v[12:15]\n" \
"ds_read_b64_tr_b16 a[220:221], v215 offset:36864\n" \
"v_and_b32_e32 v208, 0x7ff, %[vx0]\n" \
"v_mfma_f32_16x16x32_bf16 v[12:15], a[232:235], v[8:11], v[12:15]\n" \
"ds_read_b64_tr_b16 a[222:223], v215 offset:38912\n" \
"v_lshlrev_b32_e64 v208, 1, v208\n" \
"v_mfma_f32_16x16x32_bf16 v[44:47], a[236:239], v[32:35], v[44:47]\n" \
"ds_read_b64_tr_b16 a[212:213], v215 offset:40960\n" \
"v_xor_b32_e32 v209, 0x100, v208\n" \
"v_mfma_f32_16x16x32_bf16 v[44:47], a[232:235], v[40:43], v[44:47]\n" \
"ds_read_b64_tr_b16 a[214:215], v215 offset:43008\n" \
"v_lshrrev_b32_e64 v210, 3, v224\n" \
"v_mfma_f32_16x16x32_bf16 v[76:79], a[236:239], v[64:67], v[76:79]\n" \
"ds_read_b64_tr_b16 a[208:209], v215 offset:45056\n" \
"v_and_b32_e64 v210, 48, v210\n" \
"v_mfma_f32_16x16x32_bf16 v[76:79], a[232:235], v[72:75], v[76:79]\n" \
"ds_read_b64_tr_b16 a[210:211], v215 offset:47104\n" \
"s_lshl_b32 s61, %[wid], 12\n" \
"s_sub_u32 s61, %[m0b], s61\n" \
"v_add_u32_e32 v208, s61, v208\n" \
"v_add_u32_e32 v209, s61, v209\n" \
"v_add_u32_e64 v210, s61, v210\n" \
"v_mfma_f32_16x16x32_bf16 v[108:111], a[236:239], v[96:99], v[108:111]\n" \
"ds_read_b64_tr_b16 a[216:217], v227 offset:32768\n" \
"v_mfma_f32_16x16x32_bf16 v[108:111], a[232:235], v[104:107], v[108:111]\n" \
"ds_read_b64_tr_b16 a[218:219], v227 offset:34816\n" \
"s_waitcnt vmcnt(12)\n" \
"s_nop 0\n" \
"v_and_or_b32 v236, v192, v193, 0\n" \
"s_add_u32 m0, %[m0b], 117760\n" \
"s_mov_b32 s61, 28672\n" \
"buffer_load_dwordx4 %[vdn], s[64:67], s61 offen offset:0 lds\n" \
"v_mfma_f32_16x16x32_bf16 v[12:15], a[196:199], v[16:19], v[12:15]\n" \
"ds_read_b64_tr_b16 a[224:225], v227 offset:36864\n" \
"v_bitop3_b32 v236, v236, v194, v195 bitop3:0x80\n" \
"v_mfma_f32_16x16x32_bf16 v[12:15], a[200:203], v[24:27], v[12:15]\n" \
"ds_read_b64_tr_b16 a[226:227], v227 offset:38912\n" \
"v_bitop3_b32 v236, v236, v196, v197 bitop3:0x80\n" \
"v_mfma_f32_16x16x32_bf16 v[44:47], a[196:199], v[48:51], v[44:47]\n" \
"v_bitop3_b32 v236, v236, v198, v199 bitop3:0x80\n" \
"v_mfma_f32_16x16x32_bf16 v[44:47], a[200:203], v[56:59], v[44:47]\n" \
"v_bitop3_b32 v236, v236, v200, v201 bitop3:0x80\n" \
"v_mfma_f32_16x16x32_bf16 v[76:79], a[196:199], v[80:83], v[76:79]\n" \
"v_bitop3_b32 v236, v236, v202, v203 bitop3:0x80\n" \
"v_mfma_f32_16x16x32_bf16 v[76:79], a[200:203], v[88:91], v[76:79]\n" \
"v_bitop3_b32 v236, v236, v204, v205 bitop3:0x80\n" \
"v_mfma_f32_16x16x32_bf16 v[108:111], a[196:199], v[112:115], v[108:111]\n" \
"ds_read_b64_tr_b16 a[228:229], v227 offset:40960\n" \
"v_bitop3_b32 v236, v236, v206, v207 bitop3:0x80\n" \
"v_mfma_f32_16x16x32_bf16 v[108:111], a[200:203], v[120:123], v[108:111]\n" \
"s_waitcnt vmcnt(63) lgkmcnt(5)\n" \
"s_nop 0\n" \
"v_mfma_f32_16x16x32_bf16 v[4:7], a[204:207], a[0:3], v[4:7]\n" \
"ds_read_b64_tr_b16 a[230:231], v227 offset:43008\n" \
"s_mov_b32 s54, 0x07060302\n" \
"v_and_b32_e64 v236, 1, v236\n" \
"v_mfma_f32_16x16x32_bf16 v[4:7], a[220:223], a[4:7], v[4:7]\n" \
"ds_read_b64_tr_b16 a[236:237], v227 offset:45056\n" \
"v_lshrrev_b32_e64 v218, 1, v231\n" \
"v_mfma_f32_16x16x32_bf16 v[36:39], a[204:207], a[16:19], v[36:39]\n" \
"ds_read_b64_tr_b16 a[238:239], v227 offset:47104\n" \
"v_cmp_eq_u32_e64 s[56:57], 0, v236\n" \
"v_mfma_f32_16x16x32_bf16 v[36:39], a[220:223], a[20:23], v[36:39]\n" \
"s_waitcnt vmcnt(7)\n" \
"v_mov_b32_e32 v230, 1\n" \
"buffer_store_dword v230, v225, %[srdz], s62 offen offset:68 sc1\n" \
"v_mfma_f32_16x16x32_bf16 v[68:71], a[204:207], a[32:35], v[68:71]\n" \
"s_lshl_b32 s61, %[wid], 12\n" \
"s_sub_u32 s61, %[m0b], s61\n" \
"v_add_u32_e32 v231, s61, v231\n" \
"v_add_u32_e32 v218, s61, v218\n" \
"v_mov_b32_e64 v219, s61\n" \
"ds_write_b128 v231, v[192:195] offset:0\n" \
"v_mfma_f32_16x16x32_bf16 v[68:71], a[220:223], a[36:39], v[68:71]\n" \
"v_lshlrev_b32_e64 v234, 16, v233\n" \
"v_and_b32_e32 v235, 0xffff0000, v233\n" \
"v_mfma_f32_16x16x32_bf16 v[100:103], a[204:207], a[48:51], v[100:103]\n" \
"v_and_b32_e32 v233, 0xffff0000, v232\n" \
"v_mfma_f32_16x16x32_bf16 v[100:103], a[220:223], a[52:55], v[100:103]\n" \
"v_lshlrev_b32_e64 v232, 16, v232\n" \
"v_mfma_f32_16x16x32_bf16 v[4:7], a[212:215], a[8:11], v[4:7]\n" \
"v_mul_f32_e64 v234, v202, v234\n" \
"v_mfma_f32_16x16x32_bf16 v[4:7], a[208:211], a[12:15], v[4:7]\n" \
"v_mul_f32_e64 v235, v203, v235\n" \
"v_mfma_f32_16x16x32_bf16 v[36:39], a[212:215], a[24:27], v[36:39]\n" \
"v_mul_f32_e64 v233, v201, v233\n" \
"v_mfma_f32_16x16x32_bf16 v[36:39], a[208:211], a[28:31], v[36:39]\n" \
"v_mul_f32_e64 v232, v200, v232\n" \
"v_mfma_f32_16x16x32_bf16 v[68:71], a[212:215], a[40:43], v[68:71]\n" \
"s_waitcnt vmcnt(4) lgkmcnt(15)\n" \
"s_barrier\n" \
"v_mfma_f32_16x16x32_bf16 v[68:71], a[208:211], a[44:47], v[68:71]\n" \
"ds_read_b64_tr_b16 a[232:233], v215 offset:49152\n" \
"v_add_f32_e64 v223, v234, v235\n" \
"v_mfma_f32_16x16x32_bf16 v[100:103], a[212:215], a[56:59], v[100:103]\n" \
"ds_read_b64_tr_b16 a[234:235], v215 offset:51200\n" \
"v_add_f32_e64 v222, v232, v233\n" \
"v_mfma_f32_16x16x32_bf16 v[100:103], a[208:211], a[60:63], v[100:103]\n" \
"s_waitcnt vmcnt(63) lgkmcnt(3)\n" \
"s_nop 0\n" \
"v_mfma_f32_16x16x32_bf16 v[12:15], a[216:219], a[0:3], v[12:15]\n" \
"ds_read_b64_tr_b16 a[196:197], v215 offset:53248\n" \
"ds_write_b128 v231, v[232:235] offset:4096\n" \
"v_mfma_f32_16x16x32_bf16 v[12:15], a[224:227], a[4:7], v[12:15]\n" \
"ds_read_b64_tr_b16 a[198:199], v215 offset:55296\n" \
"v_add_f32_e64 v222, v222, v223\n" \
"v_mfma_f32_16x16x32_bf16 v[44:47], a[216:219], a[16:19], v[44:47]\n" \
"ds_read_b64_tr_b16 a[200:201], v215 offset:57344\n" \
"v_perm_b32 v196, v197, v196, s54\n" \
"v_mfma_f32_16x16x32_bf16 v[44:47], a[224:227], a[20:23], v[44:47]\n" \
"ds_read_b64_tr_b16 a[202:203], v215 offset:59392\n" \
"v_add_f32_dpp v222, v222, v222 quad_perm:[1,0,3,2] row_mask:0xf bank_mask:0xf\n" \
"v_mfma_f32_16x16x32_bf16 v[76:79], a[216:219], a[32:35], v[76:79]\n" \
"ds_read_b64_tr_b16 a[204:205], v215 offset:61440\n" \
"v_perm_b32 v197, v199, v198, s54\n" \
"v_mfma_f32_16x16x32_bf16 v[76:79], a[224:227], a[36:39], v[76:79]\n" \
"ds_read_b64_tr_b16 a[206:207], v215 offset:63488\n" \
"v_add_f32_dpp v222, v222, v222 quad_perm:[2,3,0,1] row_mask:0xf bank_mask:0xf\n" \
"v_mfma_f32_16x16x32_bf16 v[108:111], a[216:219], a[48:51], v[108:111]\n" \
"ds_read_b64_tr_b16 a[220:221], v227 offset:49152\n" \
"ds_write_b64 v218, v[196:197] offset:8192\n" \
"v_mfma_f32_16x16x32_bf16 v[108:111], a[224:227], a[52:55], v[108:111]\n" \
"ds_read_b64_tr_b16 a[222:223], v227 offset:51200\n" \
"v_add_f32_dpp v222, v222, v222 row_ror:4 row_mask:0xf bank_mask:0xf\n" \
"v_mfma_f32_16x16x32_bf16 v[12:15], a[228:231], a[8:11], v[12:15]\n" \
"ds_read_b64_tr_b16 a[212:213], v227 offset:53248\n" \
"v_perm_b32 v200, v201, v200, s54\n" \
"v_mfma_f32_16x16x32_bf16 v[12:15], a[236:239], a[12:15], v[12:15]\n" \
"ds_read_b64_tr_b16 a[214:215], v227 offset:55296\n" \
"v_add_f32_dpp v222, v222, v222 row_ror:8 row_mask:0xf bank_mask:0xf\n" \
"v_mfma_f32_16x16x32_bf16 v[44:47], a[228:231], a[24:27], v[44:47]\n" \
"v_perm_b32 v201, v203, v202, s54\n" \
"v_mov_b32_e64 v223, v222\n" \
"v_mfma_f32_16x16x32_bf16 v[44:47], a[236:239], a[28:31], v[44:47]\n" \
"ds_write_b64 v218, v[200:201] offset:10240\n" \
"s_nop 0\n" \
"v_permlane16_swap_b32_e32 v222, v223\n" \
"v_mfma_f32_16x16x32_bf16 v[76:79], a[228:231], a[40:43], v[76:79]\n" \
"v_perm_b32 v204, v205, v204, s54\n" \
"v_mfma_f32_16x16x32_bf16 v[76:79], a[236:239], a[44:47], v[76:79]\n" \
"v_add_f32_e64 v222, v222, v223\n" \
"v_mfma_f32_16x16x32_bf16 v[108:111], a[228:231], a[56:59], v[108:111]\n" \
"ds_read_b64_tr_b16 a[208:209], v227 offset:57344\n" \
"v_perm_b32 v205, v207, v206, s54\n" \
"v_mfma_f32_16x16x32_bf16 v[108:111], a[236:239], a[60:63], v[108:111]\n" \
"s_waitcnt vmcnt(63) lgkmcnt(7)\n" \
"s_nop 0\n" \
"v_mfma_f32_16x16x32_bf16 v[4:7], a[232:235], a[128:131], v[4:7]\n" \
"ds_read_b64_tr_b16 a[210:211], v227 offset:59392\n" \
"v_mov_b32_e64 v223, v222\n" \
"v_mfma_f32_16x16x32_bf16 v[4:7], a[196:199], a[132:135], v[4:7]\n" \
"ds_read_b64_tr_b16 a[216:217], v227 offset:61440\n" \
"ds_write_b64 v218, v[204:205] offset:14336\n" \
"v_mfma_f32_16x16x32_bf16 v[36:39], a[232:235], a[144:147], v[36:39]\n" \
"ds_read_b64_tr_b16 a[218:219], v227 offset:63488\n" \
"s_nop 0\n" \
"v_permlane32_swap_b32_e32 v222, v223\n" \
"v_mfma_f32_16x16x32_bf16 v[36:39], a[196:199], a[148:151], v[36:39]\n" \
"v_lshl_add_u32 v219, %[wid], 4, v219\n" \
"v_add_f32_e64 v222, v222, v223\n" \
"v_mfma_f32_16x16x32_bf16 v[68:71], a[232:235], a[160:163], v[68:71]\n" \
"s_cmp_lg_u64 s[56:57], 0\n" \
"s_cselect_b32 s61, 1, 0\n" \
"v_mov_b32_e64 v192, s61\n" \
"v_mfma_f32_16x16x32_bf16 v[68:71], a[196:199], a[164:167], v[68:71]\n" \
"v_mov_b32_e64 v193, v192\n" \
"v_mfma_f32_16x16x32_bf16 v[100:103], a[232:235], a[176:179], v[100:103]\n" \
"ds_read_b64_tr_b16 a[224:225], v215 offset:16896\n" \
"ds_write_b32 v219, v222 offset:12424\n" \
"v_mfma_f32_16x16x32_bf16 v[100:103], a[196:199], a[180:183], v[100:103]\n" \
"ds_read_b64_tr_b16 a[226:227], v215 offset:18944\n" \
"ds_write_b64 v219, v[192:193] offset:12416\n" \
"v_mfma_f32_16x16x32_bf16 v[4:7], a[200:203], a[136:139], v[4:7]\n" \
"ds_read_b64_tr_b16 a[228:229], v215 offset:20992\n" \
"v_mfma_f32_16x16x32_bf16 v[4:7], a[204:207], a[140:143], v[4:7]\n" \
"ds_read_b64_tr_b16 a[230:231], v215 offset:23040\n" \
"v_mfma_f32_16x16x32_bf16 v[36:39], a[200:203], a[152:155], v[36:39]\n" \
"v_mfma_f32_16x16x32_bf16 v[36:39], a[204:207], a[156:159], v[36:39]\n" \
"v_mfma_f32_16x16x32_bf16 v[68:71], a[200:203], a[168:171], v[68:71]\n" \
"v_mfma_f32_16x16x32_bf16 v[68:71], a[204:207], a[172:175], v[68:71]\n" \
"v_mfma_f32_16x16x32_bf16 v[100:103], a[200:203], a[184:187], v[100:103]\n" \
"ds_read_b64_tr_b16 a[236:237], v215 offset:25088\n" \
"v_mfma_f32_16x16x32_bf16 v[100:103], a[204:207], a[188:191], v[100:103]\n" \
"s_waitcnt vmcnt(63) lgkmcnt(3)\n" \
"s_barrier\n" \
"v_mfma_f32_16x16x32_bf16 v[12:15], a[220:223], a[128:131], v[12:15]\n" \
"ds_read_b64_tr_b16 a[238:239], v215 offset:27136\n" \
"ds_read_b128 v[204:207], v210 offset:12416\n" \
"v_mfma_f32_16x16x32_bf16 v[12:15], a[212:215], a[132:135], v[12:15]\n" \
"ds_read_b64_tr_b16 a[232:233], v215 offset:29184\n" \
"v_lshrrev_b32_e64 v200, 3, v224\n" \
"v_mfma_f32_16x16x32_bf16 v[44:47], a[220:223], a[144:147], v[44:47]\n" \
"ds_read_b64_tr_b16 a[234:235], v215 offset:31232\n" \
"ds_read_b128 v[160:163], v208 offset:0\n" \
"v_mfma_f32_16x16x32_bf16 v[44:47], a[212:215], a[148:151], v[44:47]\n" \
"v_bfe_u32 v201, v224, 8, 3\n" \
"v_and_b32_e64 v200, 16, v200\n" \
"v_mfma_f32_16x16x32_bf16 v[76:79], a[220:223], a[160:163], v[76:79]\n" \
"ds_read_b128 v[164:167], v208 offset:2048\n" \
".if . - adaln_body != 57344\n" \
".error \"size model\"\n" \
".endif\n" \
"Lk13_%=:\n" \
"s_cbranch_execz adaln_pg0\n" \
"s_nop 0\n" \
"v_lshlrev_b32_e64 v201, v201, 1\n" \
"v_mfma_f32_16x16x32_bf16 v[76:79], a[212:215], a[164:167], v[76:79]\n" \
"s_movk_i32 s61, 0x3f80\n" \
"s_nop 0\n" \
"v_lshlrev_b32_e64 v200, v200, s61\n" \
"v_readlane_b32 s68, %[thi], 18\n" \
"v_and_b32_e64 v216, 64, v226\n" \
"v_mfma_f32_16x16x32_bf16 v[108:111], a[220:223], a[176:179], v[108:111]\n" \
"ds_read_b64_tr_b16 a[196:197], v227 offset:16896\n" \
"ds_read_b128 v[168:171], v208 offset:128\n" \
"v_mfma_f32_16x16x32_bf16 v[108:111], a[212:215], a[180:183], v[108:111]\n" \
"ds_read_b64_tr_b16 a[198:199], v227 offset:18944\n" \
"v_bfe_i32 v192, v201, 0, 1\n" \
"v_add_u32_e64 v226, v216, v226\n" \
"v_mfma_f32_16x16x32_bf16 v[12:15], a[208:211], a[136:139], v[12:15]\n" \
"ds_read_b64_tr_b16 a[200:201], v227 offset:20992\n" \
"ds_read_b128 v[172:175], v208 offset:2176\n" \
"v_mfma_f32_16x16x32_bf16 v[12:15], a[216:219], a[140:143], v[12:15]\n" \
"ds_read_b64_tr_b16 a[202:203], v227 offset:23040\n" \
"v_and_b32_e32 v192, v200, v192\n" \
"v_lshlrev_b32_e32 v226, 7, v226\n" \
"v_mfma_f32_16x16x32_bf16 v[44:47], a[208:211], a[152:155], v[44:47]\n" \
"ds_read_b128 v[176:179], v209 offset:0\n" \
"v_bfe_i32 v193, v201, 1, 1\n" \
"v_mfma_f32_16x16x32_bf16 v[44:47], a[216:219], a[156:159], v[44:47]\n" \
"v_and_b32_e64 v193, v200, v193\n" \
"v_bfe_i32 v194, v201, 2, 1\n" \
"v_mfma_f32_16x16x32_bf16 v[76:79], a[208:211], a[168:171], v[76:79]\n" \
"ds_read_b128 v[180:183], v209 offset:2048\n" \
"v_and_b32_e64 v194, v200, v194\n" \
"v_mfma_f32_16x16x32_bf16 v[76:79], a[216:219], a[172:175], v[76:79]\n" \
"v_bfe_i32 v195, v201, 3, 1\n" \
"v_and_b32_e64 v195, v200, v195\n" \
"buffer_load_dword v216, v226, %[srdz], s68 offen offset:68 sc1\n" \
"v_mfma_f32_16x16x32_bf16 v[108:111], a[208:211], a[184:187], v[108:111]\n" \
"ds_read_b64_tr_b16 a[204:205], v227 offset:25088\n" \
"ds_read_b128 v[184:187], v209 offset:128\n" \
"v_lshrrev_b32_e64 v217, 3, v224\n" \
"s_add_u32 s51, %[sin], 4096\n" \
"v_mfma_f32_16x16x32_bf16 v[108:111], a[216:219], a[188:191], v[108:111]\n" \
"s_waitcnt vmcnt(63) lgkmcnt(12)\n" \
"s_nop 0\n" \
"v_mfma_f32_16x16x32_bf16 v[20:23], a[224:227], v[0:3], v[20:23]\n" \
"ds_read_b64_tr_b16 a[206:207], v227 offset:27136\n" \
"ds_read_b128 v[188:191], v209 offset:2176\n" \
"v_mfma_f32_16x16x32_bf16 v[20:23], a[228:231], v[8:11], v[20:23]\n" \
"ds_read_b64_tr_b16 a[220:221], v227 offset:29184\n" \
"v_bfe_i32 v196, v201, 4, 1\n" \
"v_mfma_f32_16x16x32_bf16 v[52:55], a[224:227], v[32:35], v[52:55]\n" \
"ds_read_b64_tr_b16 a[222:223], v227 offset:31232\n" \
"v_and_b32_e64 v196, v200, v196\n" \
"v_mfma_f32_16x16x32_bf16 v[52:55], a[228:231], v[40:43], v[52:55]\n" \
"v_bfe_i32 v197, v201, 5, 1\n" \
"v_and_b32_e64 v197, v200, v197\n" \
"v_mfma_f32_16x16x32_bf16 v[84:87], a[224:227], v[64:67], v[84:87]\n" \
"v_cvt_pk_bf16_f32 v4, v4, v5\n" \
"v_bfe_i32 v198, v201, 6, 1\n" \
"v_mfma_f32_16x16x32_bf16 v[84:87], a[228:231], v[72:75], v[84:87]\n" \
"v_cvt_pk_bf16_f32 v5, v6, v7\n" \
"v_and_b32_e64 v198, v200, v198\n" \
"v_mfma_f32_16x16x32_bf16 v[116:119], a[224:227], v[96:99], v[116:119]\n" \
"ds_read_b64_tr_b16 a[212:213], v215 offset:33280\n" \
"v_cvt_pk_bf16_f32 v6, v12, v13\n" \
"v_mfma_f32_16x16x32_bf16 v[116:119], a[228:231], v[104:107], v[116:119]\n" \
"ds_read_b64_tr_b16 a[214:215], v215 offset:35328\n" \
"v_cvt_pk_bf16_f32 v7, v14, v15\n" \
"v_mfma_f32_16x16x32_bf16 v[20:23], a[236:239], v[16:19], v[20:23]\n" \
"ds_read_b64_tr_b16 a[208:209], v215 offset:37376\n" \
"v_bfe_i32 v199, v201, 7, 1\n" \
"v_mfma_f32_16x16x32_bf16 v[20:23], a[232:235], v[24:27], v[20:23]\n" \
"ds_read_b64_tr_b16 a[210:211], v215 offset:39424\n" \
"v_and_b32_e64 v199, v200, v199\n" \
"v_mfma_f32_16x16x32_bf16 v[52:55], a[236:239], v[48:51], v[52:55]\n" \
"s_add_u32 s55, s46, 16384\n" \
"buffer_store_dwordx4 v[4:7], %[vxs], %[srdz], s55 offen offset:0 sc1\n" \
"v_mfma_f32_16x16x32_bf16 v[52:55], a[232:235], v[56:59], v[52:55]\n" \
"v_cvt_pk_bf16_f32 v36, v36, v37\n" \
"v_cvt_pk_bf16_f32 v37, v38, v39\n" \
"v_mfma_f32_16x16x32_bf16 v[84:87], a[236:239], v[80:83], v[84:87]\n" \
"v_cvt_pk_bf16_f32 v38, v44, v45\n" \
"s_waitcnt lgkmcnt(14)\n" \
"s_nop 0\n" \
"v_add_f32_dpp v206, v206, v206 quad_perm:[1,0,3,2] row_mask:0xf bank_mask:0xf\n" \
"v_mfma_f32_16x16x32_bf16 v[84:87], a[232:235], v[88:91], v[84:87]\n" \
"v_cvt_pk_bf16_f32 v39, v46, v47\n" \
"v_mfma_f32_16x16x32_bf16 v[116:119], a[236:239], v[112:115], v[116:119]\n" \
"ds_read_b64_tr_b16 a[216:217], v215 offset:41472\n" \
"v_add_f32_dpp v206, v206, v206 quad_perm:[2,3,0,1] row_mask:0xf bank_mask:0xf\n" \
"v_mfma_f32_16x16x32_bf16 v[116:119], a[232:235], v[120:123], v[116:119]\n" \
"s_waitcnt vmcnt(63) lgkmcnt(5)\n" \
"s_nop 0\n" \
"v_mfma_f32_16x16x32_bf16 v[28:31], a[196:199], v[0:3], v[28:31]\n" \
"ds_read_b64_tr_b16 a[218:219], v215 offset:43520\n" \
"v_cvt_pk_bf16_f32 v68, v68, v69\n" \
"v_mfma_f32_16x16x32_bf16 v[28:31], a[200:203], v[8:11], v[28:31]\n" \
"ds_read_b64_tr_b16 a[224:225], v215 offset:45568\n" \
"s_add_u32 s55, s46, 540672\n" \
"buffer_store_dwordx4 v[36:39], %[vxs], %[srdz], s55 offen offset:0 sc1\n" \
"v_mfma_f32_16x16x32_bf16 v[60:63], a[196:199], v[32:35], v[60:63]\n" \
"ds_read_b64_tr_b16 a[226:227], v215 offset:47616\n" \
"v_cvt_pk_bf16_f32 v69, v70, v71\n" \
"v_mfma_f32_16x16x32_bf16 v[60:63], a[200:203], v[40:43], v[60:63]\n" \
"v_cvt_pk_bf16_f32 v70, v76, v77\n" \
"v_cvt_pk_bf16_f32 v71, v78, v79\n" \
"v_mfma_f32_16x16x32_bf16 v[92:95], a[196:199], v[64:67], v[92:95]\n" \
"s_add_u32 s55, s46, 1064960\n" \
"buffer_store_dwordx4 v[68:71], %[vxs], %[srdz], s55 offen offset:0 sc1\n" \
"v_mfma_f32_16x16x32_bf16 v[92:95], a[200:203], v[72:75], v[92:95]\n" \
"v_mfma_f32_16x16x32_bf16 v[124:127], a[196:199], v[96:99], v[124:127]\n" \
"ds_read_b64_tr_b16 a[228:229], v227 offset:33280\n" \
"v_cvt_pk_bf16_f32 v100, v100, v101\n" \
"v_mfma_f32_16x16x32_bf16 v[124:127], a[200:203], v[104:107], v[124:127]\n" \
"ds_read_b64_tr_b16 a[230:231], v227 offset:35328\n" \
"v_cvt_pk_bf16_f32 v101, v102, v103\n" \
"v_mfma_f32_16x16x32_bf16 v[28:31], a[204:207], v[16:19], v[28:31]\n" \
"ds_read_b64_tr_b16 a[236:237], v227 offset:37376\n" \
"v_cvt_pk_bf16_f32 v102, v108, v109\n" \
"v_mfma_f32_16x16x32_bf16 v[28:31], a[220:223], v[24:27], v[28:31]\n" \
"ds_read_b64_tr_b16 a[238:239], v227 offset:39424\n" \
"v_cvt_pk_bf16_f32 v103, v110, v111\n" \
"v_mfma_f32_16x16x32_bf16 v[60:63], a[204:207], v[48:51], v[60:63]\n" \
"s_add_u32 s55, s46, 1589248\n" \
"buffer_store_dwordx4 v[100:103], %[vxs], %[srdz], s55 offen offset:0 sc1\n" \
"v_mfma_f32_16x16x32_bf16 v[60:63], a[220:223], v[56:59], v[60:63]\n" \
"v_mfma_f32_16x16x32_bf16 v[92:95], a[204:207], v[80:83], v[92:95]\n" \
"v_mov_b32_e64 v230, 1\n" \
"buffer_store_dword v230, v225, %[srdz], s62 offen offset:64 sc1\n" \
"v_mfma_f32_16x16x32_bf16 v[92:95], a[220:223], v[88:91], v[92:95]\n" \
"ds_read_b128 v[4:7], v208 offset:4096\n" \
"v_mfma_f32_16x16x32_bf16 v[124:127], a[204:207], v[112:115], v[124:127]\n" \
"ds_read_b64_tr_b16 a[232:233], v227 offset:41472\n" \
"v_mfma_f32_16x16x32_bf16 v[124:127], a[220:223], v[120:123], v[124:127]\n" \
"s_waitcnt vmcnt(63) lgkmcnt(6)\n" \
"s_nop 0\n" \
"v_mfma_f32_16x16x32_bf16 v[20:23], a[212:215], a[0:3], v[20:23]\n" \
"ds_read_b64_tr_b16 a[234:235], v227 offset:43520\n" \
"ds_read_b128 v[12:15], v208 offset:6144\n" \
"v_mfma_f32_16x16x32_bf16 v[20:23], a[208:211], a[4:7], v[20:23]\n" \
"ds_read_b64_tr_b16 a[196:197], v227 offset:45568\n" \
"v_mfma_f32_16x16x32_bf16 v[52:55], a[212:215], a[16:19], v[52:55]\n" \
"ds_read_b64_tr_b16 a[198:199], v227 offset:47616\n" \
"v_mfma_f32_16x16x32_bf16 v[52:55], a[208:211], a[20:23], v[52:55]\n" \
"ds_read_b128 v[36:39], v208 offset:4224\n" \
"v_mfma_f32_16x16x32_bf16 v[84:87], a[212:215], a[32:35], v[84:87]\n" \
"v_mfma_f32_16x16x32_bf16 v[84:87], a[208:211], a[36:39], v[84:87]\n" \
"v_mfma_f32_16x16x32_bf16 v[116:119], a[212:215], a[48:51], v[116:119]\n" \
"ds_read_b64_tr_b16 a[200:201], v215 offset:49664\n" \
"ds_read_b128 v[44:47], v208 offset:6272\n" \
"v_mfma_f32_16x16x32_bf16 v[116:119], a[208:211], a[52:55], v[116:119]\n" \
"ds_read_b64_tr_b16 a[202:203], v215 offset:51712\n" \
"v_mfma_f32_16x16x32_bf16 v[20:23], a[216:219], a[8:11], v[20:23]\n" \
"ds_read_b64_tr_b16 a[204:205], v215 offset:53760\n" \
"v_mfma_f32_16x16x32_bf16 v[20:23], a[224:227], a[12:15], v[20:23]\n" \
"ds_read_b64_tr_b16 a[206:207], v215 offset:55808\n" \
"ds_read_b128 v[68:71], v209 offset:4096\n" \
"v_mfma_f32_16x16x32_bf16 v[52:55], a[216:219], a[24:27], v[52:55]\n" \
"ds_read_b64_tr_b16 a[220:221], v215 offset:57856\n" \
"v_mfma_f32_16x16x32_bf16 v[52:55], a[224:227], a[28:31], v[52:55]\n" \
"ds_read_b64_tr_b16 a[222:223], v215 offset:59904\n" \
"v_mfma_f32_16x16x32_bf16 v[84:87], a[216:219], a[40:43], v[84:87]\n" \
"ds_read_b64_tr_b16 a[212:213], v215 offset:61952\n" \
"ds_read_b128 v[76:79], v209 offset:6144\n" \
"v_mfma_f32_16x16x32_bf16 v[84:87], a[224:227], a[44:47], v[84:87]\n" \
"ds_read_b64_tr_b16 a[214:215], v215 offset:64000\n" \
"v_mfma_f32_16x16x32_bf16 v[116:119], a[216:219], a[56:59], v[116:119]\n" \
"ds_read_b64_tr_b16 a[208:209], v227 offset:49664\n" \
"v_mfma_f32_16x16x32_bf16 v[116:119], a[224:227], a[60:63], v[116:119]\n" \
"s_waitcnt vmcnt(63) lgkmcnt(13)\n" \
"s_nop 0\n" \
"v_mfma_f32_16x16x32_bf16 v[28:31], a[228:231], a[0:3], v[28:31]\n" \
"ds_read_b64_tr_b16 a[210:211], v227 offset:51712\n" \
"ds_read_b128 v[100:103], v209 offset:4224\n" \
"v_mfma_f32_16x16x32_bf16 v[28:31], a[236:239], a[4:7], v[28:31]\n" \
"ds_read_b64_tr_b16 a[216:217], v227 offset:53760\n" \
"v_mfma_f32_16x16x32_bf16 v[60:63], a[228:231], a[16:19], v[60:63]\n" \
"ds_read_b64_tr_b16 a[218:219], v227 offset:55808\n" \
"ds_read_b128 v[108:111], v209 offset:6272\n" \
"v_mfma_f32_16x16x32_bf16 v[60:63], a[236:239], a[20:23], v[60:63]\n" \
"ds_read_b64_tr_b16 a[224:225], v227 offset:57856\n" \
"v_mfma_f32_16x16x32_bf16 v[92:95], a[228:231], a[32:35], v[92:95]\n" \
"ds_read_b64_tr_b16 a[226:227], v227 offset:59904\n" \
"v_mfma_f32_16x16x32_bf16 v[92:95], a[236:239], a[36:39], v[92:95]\n" \
"v_mfma_f32_16x16x32_bf16 v[124:127], a[228:231], a[48:51], v[124:127]\n" \
"v_mfma_f32_16x16x32_bf16 v[124:127], a[236:239], a[52:55], v[124:127]\n" \
"ds_read_b64_tr_b16 a[228:229], v227 offset:61952\n" \
"v_mfma_f32_16x16x32_bf16 v[28:31], a[232:235], a[8:11], v[28:31]\n" \
"ds_read_b64_tr_b16 a[230:231], v227 offset:64000\n" \
"s_waitcnt vmcnt(5)\n" \
"v_cmp_eq_u32_e32 vcc, 0, v216\n" \
"s_nop 0\n" \
"s_cmp_eq_u64 vcc, 0\n" \
"s_cselect_b32 %[got], 1, 0\n" \
"s_nop 0\n" \
"s_cselect_b32 s54, %[sin], 0x80000000\n" \
"s_cselect_b32 s51, s51, 0x80000000\n" \
"v_mfma_f32_16x16x32_bf16 v[28:31], a[196:199], a[12:15], v[28:31]\n" \
"v_mfma_f32_16x16x32_bf16 v[60:63], a[232:235], a[24:27], v[60:63]\n" \
"buffer_load_dwordx4 a[64:67], v217, %[srdz], s54 offen offset:0 sc1\n" \
"v_mfma_f32_16x16x32_bf16 v[60:63], a[196:199], a[28:31], v[60:63]\n" \
"v_mfma_f32_16x16x32_bf16 v[92:95], a[232:235], a[40:43], v[92:95]\n" \
"buffer_load_dwordx4 a[68:71], v217, %[srdz], s54 offen offset:1024 sc1\n" \
"v_mfma_f32_16x16x32_bf16 v[92:95], a[196:199], a[44:47], v[92:95]\n" \
"v_mfma_f32_16x16x32_bf16 v[124:127], a[232:235], a[56:59], v[124:127]\n" \
"buffer_load_dwordx4 a[72:75], v217, %[srdz], s54 offen offset:2048 sc1\n" \
"v_mfma_f32_16x16x32_bf16 v[124:127], a[196:199], a[60:63], v[124:127]\n" \
"s_waitcnt vmcnt(63) lgkmcnt(0)\n" \
"s_nop 0\n" \
"v_mfma_f32_16x16x32_bf16 v[20:23], a[200:203], a[128:131], v[20:23]\n" \
"buffer_load_dwordx4 a[76:79], v217, %[srdz], s54 offen offset:3072 sc1\n" \
"v_mfma_f32_16x16x32_bf16 v[20:23], a[204:207], a[132:135], v[20:23]\n" \
"v_mfma_f32_16x16x32_bf16 v[20:23], a[220:223], a[136:139], v[20:23]\n" \
"buffer_load_dwordx4 a[80:83], v217, %[srdz], s51 offen offset:0 sc1\n" \
"v_mfma_f32_16x16x32_bf16 v[20:23], a[212:215], a[140:143], v[20:23]\n" \
"v_mfma_f32_16x16x32_bf16 v[28:31], a[208:211], a[128:131], v[28:31]\n" \
"buffer_load_dwordx4 a[84:87], v217, %[srdz], s51 offen offset:1024 sc1\n" \
"v_mfma_f32_16x16x32_bf16 v[28:31], a[216:219], a[132:135], v[28:31]\n" \
"v_mfma_f32_16x16x32_bf16 v[28:31], a[224:227], a[136:139], v[28:31]\n" \
"buffer_load_dwordx4 a[88:91], v217, %[srdz], s51 offen offset:2048 sc1\n" \
"v_mfma_f32_16x16x32_bf16 v[28:31], a[228:231], a[140:143], v[28:31]\n" \
"v_mfma_f32_16x16x32_bf16 v[52:55], a[200:203], a[144:147], v[52:55]\n" \
"buffer_load_dwordx4 a[92:95], v217, %[srdz], s51 offen offset:3072 sc1\n" \
"v_mfma_f32_16x16x32_bf16 v[52:55], a[204:207], a[148:151], v[52:55]\n" \
"v_mfma_f32_16x16x32_bf16 v[52:55], a[220:223], a[152:155], v[52:55]\n" \
"v_mfma_f32_16x16x32_bf16 v[52:55], a[212:215], a[156:159], v[52:55]\n" \
"v_cvt_pk_bf16_f32 v20, v20, v21\n" \
"v_cvt_pk_bf16_f32 v21, v22, v23\n" \
"v_mfma_f32_16x16x32_bf16 v[60:63], a[208:211], a[144:147], v[60:63]\n" \
"buffer_load_dwordx2 v[228:229], v226, %[srdz], s68 offen offset:64 sc1\n" \
"v_mfma_f32_16x16x32_bf16 v[60:63], a[216:219], a[148:151], v[60:63]\n" \
"v_cvt_pk_bf16_f32 v22, v28, v29\n" \
"v_cvt_pk_bf16_f32 v23, v30, v31\n" \
"v_mfma_f32_16x16x32_bf16 v[60:63], a[224:227], a[152:155], v[60:63]\n" \
"s_add_u32 s55, s46, 20480\n" \
"buffer_store_dwordx4 v[20:23], %[vxs], %[srdz], s55 offen offset:0 sc1\n" \
"v_mfma_f32_16x16x32_bf16 v[60:63], a[228:231], a[156:159], v[60:63]\n" \
"v_mfma_f32_16x16x32_bf16 v[84:87], a[200:203], a[160:163], v[84:87]\n" \
"v_mfma_f32_16x16x32_bf16 v[84:87], a[204:207], a[164:167], v[84:87]\n" \
"v_mfma_f32_16x16x32_bf16 v[84:87], a[220:223], a[168:171], v[84:87]\n" \
"v_mfma_f32_16x16x32_bf16 v[84:87], a[212:215], a[172:175], v[84:87]\n" \
"v_cvt_pk_bf16_f32 v52, v52, v53\n" \
"v_cvt_pk_bf16_f32 v53, v54, v55\n" \
"v_mfma_f32_16x16x32_bf16 v[92:95], a[208:211], a[160:163], v[92:95]\n" \
"v_mfma_f32_16x16x32_bf16 v[92:95], a[216:219], a[164:167], v[92:95]\n" \
"v_cvt_pk_bf16_f32 v54, v60, v61\n" \
"v_cvt_pk_bf16_f32 v55, v62, v63\n" \
"v_mfma_f32_16x16x32_bf16 v[92:95], a[224:227], a[168:171], v[92:95]\n" \
"s_add_u32 s55, s46, 544768\n" \
"buffer_store_dwordx4 v[52:55], %[vxs], %[srdz], s55 offen offset:0 sc1\n" \
"v_mfma_f32_16x16x32_bf16 v[92:95], a[228:231], a[172:175], v[92:95]\n" \
"v_mfma_f32_16x16x32_bf16 v[116:119], a[200:203], a[176:179], v[116:119]\n" \
"v_mfma_f32_16x16x32_bf16 v[116:119], a[204:207], a[180:183], v[116:119]\n" \
"v_mfma_f32_16x16x32_bf16 v[116:119], a[220:223], a[184:187], v[116:119]\n" \
"v_mfma_f32_16x16x32_bf16 v[116:119], a[212:215], a[188:191], v[116:119]\n" \
"v_cvt_pk_bf16_f32 v84, v84, v85\n" \
"v_cvt_pk_bf16_f32 v85, v86, v87\n" \
"v_mfma_f32_16x16x32_bf16 v[124:127], a[208:211], a[176:179], v[124:127]\n" \
"v_mfma_f32_16x16x32_bf16 v[124:127], a[216:219], a[180:183], v[124:127]\n" \
"v_cvt_pk_bf16_f32 v86, v92, v93\n" \
"v_cvt_pk_bf16_f32 v87, v94, v95\n" \
"v_mfma_f32_16x16x32_bf16 v[124:127], a[224:227], a[184:187], v[124:127]\n" \
"s_add_u32 s55, s46, 1069056\n" \
"buffer_store_dwordx4 v[84:87], %[vxs], %[srdz], s55 offen offset:0 sc1\n" \
"v_mfma_f32_16x16x32_bf16 v[124:127], a[228:231], a[188:191], v[124:127]\n" \
"v_cvt_pk_bf16_f32 v116, v116, v117\n" \
"v_cvt_pk_bf16_f32 v117, v118, v119\n" \
"s_waitcnt lgkmcnt(0)\n" \
"s_nop 4\n" \
"v_cvt_pk_bf16_f32 v118, v124, v125\n" \
"v_cvt_pk_bf16_f32 v119, v126, v127\n" \
"s_add_u32 s55, s46, 1593344\n" \
"buffer_store_dwordx4 v[116:119], %[vxs], %[srdz], s55 offen offset:0 sc1\n" \
"s_branch Lend_%=\n" \
"s_nop 0\n" \
"Lsp1_%=:\n" \
"s_movk_i32 s51, 0x1000\n" \
"s_cmp_gt_u32 s48, 0\n" \
"s_cselect_b32 s61, 0, 1\n" \
"s_nop 0\n" \
"s_mul_i32 s61, s61, 262144\n" \
"Lsq1_%=:\n" \
"buffer_load_dwordx4 a[64:67], %[oq0], %[srdz], s61 offen offset:0 sc1\n" \
"buffer_load_dwordx4 a[68:71], %[oq0], %[srdz], s61 offen offset:1024 sc1\n" \
"buffer_load_dwordx4 a[72:75], %[oq0], %[srdz], s61 offen offset:2048 sc1\n" \
"buffer_load_dwordx4 a[76:79], %[oq0], %[srdz], s61 offen offset:3072 sc1\n" \
"buffer_load_dwordx4 a[80:83], v208, %[srdz], s61 offen offset:0 sc1\n" \
"buffer_load_dwordx4 a[84:87], v208, %[srdz], s61 offen offset:1024 sc1\n" \
"buffer_load_dwordx4 a[88:91], v208, %[srdz], s61 offen offset:2048 sc1\n" \
"buffer_load_dwordx4 a[92:95], v208, %[srdz], s61 offen offset:3072 sc1\n" \
"buffer_load_dwordx4 a[96:99], v209, %[srdz], s61 offen offset:0 sc1\n" \
"buffer_load_dwordx4 a[100:103], v209, %[srdz], s61 offen offset:1024 sc1\n" \
"buffer_load_dwordx4 a[104:107], v209, %[srdz], s61 offen offset:2048 sc1\n" \
"buffer_load_dwordx4 a[108:111], v209, %[srdz], s61 offen offset:3072 sc1\n" \
"buffer_load_dwordx4 a[112:115], v210, %[srdz], s61 offen offset:0 sc1\n" \
"buffer_load_dwordx4 a[116:119], v210, %[srdz], s61 offen offset:1024 sc1\n" \
"buffer_load_dwordx4 a[120:123], v210, %[srdz], s61 offen offset:2048 sc1\n" \
"buffer_load_dwordx4 a[124:127], v210, %[srdz], s61 offen offset:3072 sc1\n" \
"s_waitcnt vmcnt(0)\n" \
"s_nop 0\n" \
"v_mfma_f32_16x16x32_bf16 v[128:131], a[236:239], a[64:67], v[192:195]\n" \
"v_mfma_f32_16x16x32_bf16 v[128:131], a[196:199], a[68:71], v[128:131]\n" \
"v_mfma_f32_16x16x32_bf16 v[128:131], a[200:203], a[72:75], v[128:131]\n" \
"v_mfma_f32_16x16x32_bf16 v[128:131], a[204:207], a[76:79], v[128:131]\n" \
"v_mfma_f32_16x16x32_bf16 v[132:135], a[236:239], a[80:83], v[196:199]\n" \
"v_mfma_f32_16x16x32_bf16 v[132:135], a[196:199], a[84:87], v[132:135]\n" \
"v_mfma_f32_16x16x32_bf16 v[132:135], a[200:203], a[88:91], v[132:135]\n" \
"v_mfma_f32_16x16x32_bf16 v[132:135], a[204:207], a[92:95], v[132:135]\n" \
"v_mfma_f32_16x16x32_bf16 v[136:139], a[236:239], a[96:99], v[200:203]\n" \
"v_mfma_f32_16x16x32_bf16 v[136:139], a[196:199], a[100:103], v[136:139]\n" \
"v_mfma_f32_16x16x32_bf16 v[136:139], a[200:203], a[104:107], v[136:139]\n" \
"v_mfma_f32_16x16x32_bf16 v[136:139], a[204:207], a[108:111], v[136:139]\n" \
"v_mfma_f32_16x16x32_bf16 v[140:143], a[236:239], a[112:115], v[204:207]\n" \
"v_mfma_f32_16x16x32_bf16 v[140:143], a[196:199], a[116:119], v[140:143]\n" \
"v_mfma_f32_16x16x32_bf16 v[140:143], a[200:203], a[120:123], v[140:143]\n" \
"v_mfma_f32_16x16x32_bf16 v[140:143], a[204:207], a[124:127], v[140:143]\n" \
"s_nop 7\n" \
"s_nop 0\n" \
"v_cmp_u_f32_e64 s[44:45], v128, v132\n" \
"v_cmp_u_f32_e64 s[56:57], v136, v140\n" \
"s_or_b64 s[56:57], s[56:57], s[44:45]\n" \
"s_cbranch_scc0 Lbk1_%=\n" \
"s_sub_u32 s51, s51, 1\n" \
"s_cbranch_scc0 Lsq1_%=\n" \
"s_branch Lbk1_%=\n" \
"Lsp2_%=:\n" \
"s_movk_i32 s51, 0x1000\n" \
"s_cmp_gt_u32 s48, 1\n" \
"s_cselect_b32 s61, 1, 2\n" \
"s_mul_i32 s61, s61, 262144\n" \
"Lsq2_%=:\n" \
"buffer_load_dwordx4 a[0:3], %[oq0], %[srdz], s61 offen offset:0 sc1\n" \
"buffer_load_dwordx4 a[4:7], %[oq0], %[srdz], s61 offen offset:1024 sc1\n" \
"buffer_load_dwordx4 a[8:11], %[oq0], %[srdz], s61 offen offset:2048 sc1\n" \
"buffer_load_dwordx4 a[12:15], %[oq0], %[srdz], s61 offen offset:3072 sc1\n" \
"buffer_load_dwordx4 a[16:19], v208, %[srdz], s61 offen offset:0 sc1\n" \
"buffer_load_dwordx4 a[20:23], v208, %[srdz], s61 offen offset:1024 sc1\n" \
"buffer_load_dwordx4 a[24:27], v208, %[srdz], s61 offen offset:2048 sc1\n" \
"buffer_load_dwordx4 a[28:31], v208, %[srdz], s61 offen offset:3072 sc1\n" \
"buffer_load_dwordx4 a[32:35], v209, %[srdz], s61 offen offset:0 sc1\n" \
"buffer_load_dwordx4 a[36:39], v209, %[srdz], s61 offen offset:1024 sc1\n" \
"buffer_load_dwordx4 a[40:43], v209, %[srdz], s61 offen offset:2048 sc1\n" \
"buffer_load_dwordx4 a[44:47], v209, %[srdz], s61 offen offset:3072 sc1\n" \
"buffer_load_dwordx4 a[48:51], v210, %[srdz], s61 offen offset:0 sc1\n" \
"buffer_load_dwordx4 a[52:55], v210, %[srdz], s61 offen offset:1024 sc1\n" \
"buffer_load_dwordx4 a[56:59], v210, %[srdz], s61 offen offset:2048 sc1\n" \
"buffer_load_dwordx4 a[60:63], v210, %[srdz], s61 offen offset:3072 sc1\n" \
"s_waitcnt vmcnt(0)\n" \
"s_nop 0\n" \
"v_mfma_f32_16x16x32_bf16 v[192:195], a[196:199], a[0:3], v[128:131]\n" \
"v_mfma_f32_16x16x32_bf16 v[192:195], a[200:203], a[4:7], v[192:195]\n" \
"v_mfma_f32_16x16x32_bf16 v[192:195], a[204:207], a[8:11], v[192:195]\n" \
"v_mfma_f32_16x16x32_bf16 v[192:195], a[232:235], a[12:15], v[192:195]\n" \
"v_mfma_f32_16x16x32_bf16 v[196:199], a[196:199], a[16:19], v[132:135]\n" \
"v_mfma_f32_16x16x32_bf16 v[196:199], a[200:203], a[20:23], v[196:199]\n" \
"v_mfma_f32_16x16x32_bf16 v[196:199], a[204:207], a[24:27], v[196:199]\n" \
"v_mfma_f32_16x16x32_bf16 v[196:199], a[232:235], a[28:31], v[196:199]\n" \
"v_mfma_f32_16x16x32_bf16 v[200:203], a[196:199], a[32:35], v[136:139]\n" \
"v_mfma_f32_16x16x32_bf16 v[200:203], a[200:203], a[36:39], v[200:203]\n" \
"v_mfma_f32_16x16x32_bf16 v[200:203], a[204:207], a[40:43], v[200:203]\n" \
"v_mfma_f32_16x16x32_bf16 v[200:203], a[232:235], a[44:47], v[200:203]\n" \
"v_mfma_f32_16x16x32_bf16 v[204:207], a[196:199], a[48:51], v[140:143]\n" \
"v_mfma_f32_16x16x32_bf16 v[204:207], a[200:203], a[52:55], v[204:207]\n" \
"v_mfma_f32_16x16x32_bf16 v[204:207], a[204:207], a[56:59], v[204:207]\n" \
"v_mfma_f32_16x16x32_bf16 v[204:207], a[232:235], a[60:63], v[204:207]\n" \
"s_nop 7\n" \
"s_nop 0\n" \
"v_cmp_u_f32_e64 s[44:45], v192, v196\n" \
"v_cmp_u_f32_e64 s[56:57], v200, v204\n" \
"s_or_b64 s[56:57], s[56:57], s[44:45]\n" \
"s_cbranch_scc0 Lbk2_%=\n" \
"s_sub_u32 s51, s51, 1\n" \
"s_cbranch_scc0 Lsq2_%=\n" \
"s_branch Lbk2_%=\n" \
"Lsp3_%=:\n" \
"s_movk_i32 s51, 0x1000\n" \
"s_cmp_gt_u32 s48, 2\n" \
"s_cselect_b32 s61, 2, 3\n" \
"s_mul_i32 s61, s61, 262144\n" \
"Lsq3_%=:\n" \
"buffer_load_dwordx4 a[128:131], %[oq0], %[srdz], s61 offen offset:0 sc1\n" \
"buffer_load_dwordx4 a[132:135], %[oq0], %[srdz], s61 offen offset:1024 sc1\n" \
"buffer_load_dwordx4 a[136:139], %[oq0], %[srdz], s61 offen offset:2048 sc1\n" \
"buffer_load_dwordx4 a[140:143], %[oq0], %[srdz], s61 offen offset:3072 sc1\n" \
"buffer_load_dwordx4 a[144:147], v208, %[srdz], s61 offen offset:0 sc1\n" \
"buffer_load_dwordx4 a[148:151], v208, %[srdz], s61 offen offset:1024 sc1\n" \
"buffer_load_dwordx4 a[152:155], v208, %[srdz], s61 offen offset:2048 sc1\n" \
"buffer_load_dwordx4 a[156:159], v208, %[srdz], s61 offen offset:3072 sc1\n" \
"buffer_load_dwordx4 a[160:163], v209, %[srdz], s61 offen offset:0 sc1\n" \
"buffer_load_dwordx4 a[164:167], v209, %[srdz], s61 offen offset:1024 sc1\n" \
"buffer_load_dwordx4 a[168:171], v209, %[srdz], s61 offen offset:2048 sc1\n" \
"buffer_load_dwordx4 a[172:175], v209, %[srdz], s61 offen offset:3072 sc1\n" \
"buffer_load_dwordx4 a[176:179], v210, %[srdz], s61 offen offset:0 sc1\n" \
"buffer_load_dwordx4 a[180:183], v210, %[srdz], s61 offen offset:1024 sc1\n" \
"buffer_load_dwordx4 a[184:187], v210, %[srdz], s61 offen offset:2048 sc1\n" \
"buffer_load_dwordx4 a[188:191], v210, %[srdz], s61 offen offset:3072 sc1\n" \
"s_waitcnt vmcnt(0)\n" \
"s_nop 0\n" \
"v_mfma_f32_16x16x32_bf16 v[128:131], a[196:199], a[128:131], v[192:195]\n" \
"v_mfma_f32_16x16x32_bf16 v[128:131], a[200:203], a[132:135], v[128:131]\n" \
"v_mfma_f32_16x16x32_bf16 v[128:131], a[204:207], a[136:139], v[128:131]\n" \
"v_mfma_f32_16x16x32_bf16 v[128:131], a[224:227], a[140:143], v[128:131]\n" \
"v_mfma_f32_16x16x32_bf16 v[132:135], a[196:199], a[144:147], v[196:199]\n" \
"v_mfma_f32_16x16x32_bf16 v[132:135], a[200:203], a[148:151], v[132:135]\n" \
"v_mfma_f32_16x16x32_bf16 v[132:135], a[204:207], a[152:155], v[132:135]\n" \
"v_mfma_f32_16x16x32_bf16 v[132:135], a[224:227], a[156:159], v[132:135]\n" \
"v_mfma_f32_16x16x32_bf16 v[136:139], a[196:199], a[160:163], v[200:203]\n" \
"v_mfma_f32_16x16x32_bf16 v[136:139], a[200:203], a[164:167], v[136:139]\n" \
"v_mfma_f32_16x16x32_bf16 v[136:139], a[204:207], a[168:171], v[136:139]\n" \
"v_mfma_f32_16x16x32_bf16 v[136:139], a[224:227], a[172:175], v[136:139]\n" \
"v_mfma_f32_16x16x32_bf16 v[140:143], a[196:199], a[176:179], v[204:207]\n" \
"v_mfma_f32_16x16x32_bf16 v[140:143], a[200:203], a[180:183], v[140:143]\n" \
"v_mfma_f32_16x16x32_bf16 v[140:143], a[204:207], a[184:187], v[140:143]\n" \
"v_mfma_f32_16x16x32_bf16 v[140:143], a[224:227], a[188:191], v[140:143]\n" \
"s_nop 7\n" \
"s_nop 0\n" \
"v_cmp_u_f32_e64 s[44:45], v128, v132\n" \
"v_cmp_u_f32_e64 s[56:57], v136, v140\n" \
"s_or_b64 s[56:57], s[56:57], s[44:45]\n" \
"s_cbranch_scc0 Lbk3_%=\n" \
"s_sub_u32 s51, s51, 1\n" \
"s_cbranch_scc0 Lsq3_%=\n" \
"s_branch Lbk3_%=\n" \
"Lsp4_%=:\n" \
"s_movk_i32 s51, 0x1000\n" \
"s_cmp_gt_u32 s48, 3\n" \
"s_cselect_b32 s61, 3, 4\n" \
"s_mul_i32 s61, s61, 262144\n" \
"Lsq4_%=:\n" \
"buffer_load_dwordx4 a[64:67], %[oq0], %[srdz], s61 offen offset:0 sc1\n" \
"buffer_load_dwordx4 a[68:71], %[oq0], %[srdz], s61 offen offset:1024 sc1\n" \
"buffer_load_dwordx4 a[72:75], %[oq0], %[srdz], s61 offen offset:2048 sc1\n" \
"buffer_load_dwordx4 a[76:79], %[oq0], %[srdz], s61 offen offset:3072 sc1\n" \
"buffer_load_dwordx4 a[80:83], v208, %[srdz], s61 offen offset:0 sc1\n" \
"buffer_load_dwordx4 a[84:87], v208, %[srdz], s61 offen offset:1024 sc1\n" \
"buffer_load_dwordx4 a[88:91], v208, %[srdz], s61 offen offset:2048 sc1\n" \
"buffer_load_dwordx4 a[92:95], v208, %[srdz], s61 offen offset:3072 sc1\n" \
"buffer_load_dwordx4 a[96:99], v209, %[srdz], s61 offen offset:0 sc1\n" \
"buffer_load_dwordx4 a[100:103], v209, %[srdz], s61 offen offset:1024 sc1\n" \
"buffer_load_dwordx4 a[104:107], v209, %[srdz], s61 offen offset:2048 sc1\n" \
"buffer_load_dwordx4 a[108:111], v209, %[srdz], s61 offen offset:3072 sc1\n" \
"buffer_load_dwordx4 a[112:115], v210, %[srdz], s61 offen offset:0 sc1\n" \
"buffer_load_dwordx4 a[116:119], v210, %[srdz], s61 offen offset:1024 sc1\n" \
"buffer_load_dwordx4 a[120:123], v210, %[srdz], s61 offen offset:2048 sc1\n" \
"buffer_load_dwordx4 a[124:127], v210, %[srdz], s61 offen offset:3072 sc1\n" \
"s_waitcnt vmcnt(0)\n" \
"s_nop 0\n" \
"v_mfma_f32_16x16x32_bf16 v[192:195], a[196:199], a[64:67], v[128:131]\n" \
"v_mfma_f32_16x16x32_bf16 v[192:195], a[200:203], a[68:71], v[192:195]\n" \
"v_mfma_f32_16x16x32_bf16 v[192:195], a[204:207], a[72:75], v[192:195]\n" \
"v_mfma_f32_16x16x32_bf16 v[192:195], a[220:223], a[76:79], v[192:195]\n" \
"v_mfma_f32_16x16x32_bf16 v[196:199], a[196:199], a[80:83], v[132:135]\n" \
"v_mfma_f32_16x16x32_bf16 v[196:199], a[200:203], a[84:87], v[196:199]\n" \
"v_mfma_f32_16x16x32_bf16 v[196:199], a[204:207], a[88:91], v[196:199]\n" \
"v_mfma_f32_16x16x32_bf16 v[196:199], a[220:223], a[92:95], v[196:199]\n" \
"v_mfma_f32_16x16x32_bf16 v[200:203], a[196:199], a[96:99], v[136:139]\n" \
"v_mfma_f32_16x16x32_bf16 v[200:203], a[200:203], a[100:103], v[200:203]\n" \
"v_mfma_f32_16x16x32_bf16 v[200:203], a[204:207], a[104:107], v[200:203]\n" \
"v_mfma_f32_16x16x32_bf16 v[200:203], a[220:223], a[108:111], v[200:203]\n" \
"v_mfma_f32_16x16x32_bf16 v[204:207], a[196:199], a[112:115], v[140:143]\n" \
"v_mfma_f32_16x16x32_bf16 v[204:207], a[200:203], a[116:119], v[204:207]\n" \
"v_mfma_f32_16x16x32_bf16 v[204:207], a[204:207], a[120:123], v[204:207]\n" \
"v_mfma_f32_16x16x32_bf16 v[204:207], a[220:223], a[124:127], v[204:207]\n" \
"s_nop 7\n" \
"s_nop 0\n" \
"v_cmp_u_f32_e64 s[44:45], v192, v196\n" \
"v_cmp_u_f32_e64 s[56:57], v200, v204\n" \
"s_or_b64 s[56:57], s[56:57], s[44:45]\n" \
"s_cbranch_scc0 Lbk4_%=\n" \
"s_sub_u32 s51, s51, 1\n" \
"s_cbranch_scc0 Lsq4_%=\n" \
"s_branch Lbk4_%=\n" \
"Lsp5_%=:\n" \
"s_movk_i32 s51, 0x1000\n" \
"s_cmp_gt_u32 s48, 4\n" \
"s_cselect_b32 s61, 4, 5\n" \
"s_mul_i32 s61, s61, 262144\n" \
"Lsq5_%=:\n" \
"buffer_load_dwordx4 v[0:3], %[oq0], %[srdz], s61 offen offset:0 sc1\n" \
"buffer_load_dwordx4 v[8:11], %[oq0], %[srdz], s61 offen offset:1024 sc1\n" \
"buffer_load_dwordx4 v[16:19], %[oq0], %[srdz], s61 offen offset:2048 sc1\n" \
"buffer_load_dwordx4 v[24:27], %[oq0], %[srdz], s61 offen offset:3072 sc1\n" \
"buffer_load_dwordx4 v[32:35], v208, %[srdz], s61 offen offset:0 sc1\n" \
"buffer_load_dwordx4 v[40:43], v208, %[srdz], s61 offen offset:1024 sc1\n" \
"buffer_load_dwordx4 v[48:51], v208, %[srdz], s61 offen offset:2048 sc1\n" \
"buffer_load_dwordx4 v[56:59], v208, %[srdz], s61 offen offset:3072 sc1\n" \
"buffer_load_dwordx4 v[64:67], v209, %[srdz], s61 offen offset:0 sc1\n" \
"buffer_load_dwordx4 v[72:75], v209, %[srdz], s61 offen offset:1024 sc1\n" \
"buffer_load_dwordx4 v[80:83], v209, %[srdz], s61 offen offset:2048 sc1\n" \
"buffer_load_dwordx4 v[88:91], v209, %[srdz], s61 offen offset:3072 sc1\n" \
"buffer_load_dwordx4 v[96:99], v210, %[srdz], s61 offen offset:0 sc1\n" \
"buffer_load_dwordx4 v[104:107], v210, %[srdz], s61 offen offset:1024 sc1\n" \
"buffer_load_dwordx4 v[112:115], v210, %[srdz], s61 offen offset:2048 sc1\n" \
"buffer_load_dwordx4 v[120:123], v210, %[srdz], s61 offen offset:3072 sc1\n" \
"s_waitcnt vmcnt(0)\n" \
"s_nop 0\n" \
"v_mfma_f32_16x16x32_bf16 v[128:131], a[232:235], v[0:3], v[192:195]\n" \
"v_mfma_f32_16x16x32_bf16 v[128:131], a[236:239], v[8:11], v[128:131]\n" \
"v_mfma_f32_16x16x32_bf16 v[128:131], a[208:211], v[16:19], v[128:131]\n" \
"v_mfma_f32_16x16x32_bf16 v[128:131], a[228:231], v[24:27], v[128:131]\n" \
"v_mfma_f32_16x16x32_bf16 v[132:135], a[232:235], v[32:35], v[196:199]\n" \
"v_mfma_f32_16x16x32_bf16 v[132:135], a[236:239], v[40:43], v[132:135]\n" \
"v_mfma_f32_16x16x32_bf16 v[132:135], a[208:211], v[48:51], v[132:135]\n" \
"v_mfma_f32_16x16x32_bf16 v[132:135], a[228:231], v[56:59], v[132:135]\n" \
"v_mfma_f32_16x16x32_bf16 v[136:139], a[232:235], v[64:67], v[200:203]\n" \
"v_mfma_f32_16x16x32_bf16 v[136:139], a[236:239], v[72:75], v[136:139]\n" \
"v_mfma_f32_16x16x32_bf16 v[136:139], a[208:211], v[80:83], v[136:139]\n" \
"v_mfma_f32_16x16x32_bf16 v[136:139], a[228:231], v[88:91], v[136:139]\n" \
"v_mfma_f32_16x16x32_bf16 v[140:143], a[232:235], v[96:99], v[204:207]\n" \
"v_mfma_f32_16x16x32_bf16 v[140:143], a[236:239], v[104:107], v[140:143]\n" \
"v_mfma_f32_16x16x32_bf16 v[140:143], a[208:211], v[112:115], v[140:143]\n" \
"v_mfma_f32_16x16x32_bf16 v[140:143], a[228:231], v[120:123], v[140:143]\n" \
"s_nop 7\n" \
"s_nop 0\n" \
"v_cmp_u_f32_e64 s[44:45], v128, v132\n" \
"v_cmp_u_f32_e64 s[56:57], v136, v140\n" \
"s_or_b64 s[56:57], s[56:57], s[44:45]\n" \
"s_cbranch_scc0 Lbk5_%=\n" \
"s_sub_u32 s51, s51, 1\n" \
"s_cbranch_scc0 Lsq5_%=\n" \
"s_branch Lbk5_%=\n" \
"Lsp6_%=:\n" \
"s_movk_i32 s51, 0x1000\n" \
"s_cmp_gt_u32 s48, 5\n" \
"s_cselect_b32 s61, 5, 6\n" \
"s_mul_i32 s61, s61, 262144\n" \
"Lsq6_%=:\n" \
"buffer_load_dwordx4 a[0:3], %[oq0], %[srdz], s61 offen offset:0 sc1\n" \
"buffer_load_dwordx4 a[4:7], %[oq0], %[srdz], s61 offen offset:1024 sc1\n" \
"buffer_load_dwordx4 a[8:11], %[oq0], %[srdz], s61 offen offset:2048 sc1\n" \
"buffer_load_dwordx4 a[12:15], %[oq0], %[srdz], s61 offen offset:3072 sc1\n" \
"buffer_load_dwordx4 a[16:19], v208, %[srdz], s61 offen offset:0 sc1\n" \
"buffer_load_dwordx4 a[20:23], v208, %[srdz], s61 offen offset:1024 sc1\n" \
"buffer_load_dwordx4 a[24:27], v208, %[srdz], s61 offen offset:2048 sc1\n" \
"buffer_load_dwordx4 a[28:31], v208, %[srdz], s61 offen offset:3072 sc1\n" \
"buffer_load_dwordx4 a[32:35], v209, %[srdz], s61 offen offset:0 sc1\n" \
"buffer_load_dwordx4 a[36:39], v209, %[srdz], s61 offen offset:1024 sc1\n" \
"buffer_load_dwordx4 a[40:43], v209, %[srdz], s61 offen offset:2048 sc1\n" \
"buffer_load_dwordx4 a[44:47], v209, %[srdz], s61 offen offset:3072 sc1\n" \
"buffer_load_dwordx4 a[48:51], v210, %[srdz], s61 offen offset:0 sc1\n" \
"buffer_load_dwordx4 a[52:55], v210, %[srdz], s61 offen offset:1024 sc1\n" \
"buffer_load_dwordx4 a[56:59], v210, %[srdz], s61 offen offset:2048 sc1\n" \
"buffer_load_dwordx4 a[60:63], v210, %[srdz], s61 offen offset:3072 sc1\n" \
"s_waitcnt vmcnt(0)\n" \
"s_nop 0\n" \
"v_mfma_f32_16x16x32_bf16 v[192:195], a[196:199], a[0:3], v[128:131]\n" \
"v_mfma_f32_16x16x32_bf16 v[192:195], a[200:203], a[4:7], v[192:195]\n" \
"v_mfma_f32_16x16x32_bf16 v[192:195], a[204:207], a[8:11], v[192:195]\n" \
"v_mfma_f32_16x16x32_bf16 v[192:195], a[220:223], a[12:15], v[192:195]\n" \
"v_mfma_f32_16x16x32_bf16 v[196:199], a[196:199], a[16:19], v[132:135]\n" \
"v_mfma_f32_16x16x32_bf16 v[196:199], a[200:203], a[20:23], v[196:199]\n" \
"v_mfma_f32_16x16x32_bf16 v[196:199], a[204:207], a[24:27], v[196:199]\n" \
"v_mfma_f32_16x16x32_bf16 v[196:199], a[220:223], a[28:31], v[196:199]\n" \
"v_mfma_f32_16x16x32_bf16 v[200:203], a[196:199], a[32:35], v[136:139]\n" \
"v_mfma_f32_16x16x32_bf16 v[200:203], a[200:203], a[36:39], v[200:203]\n" \
"v_mfma_f32_16x16x32_bf16 v[200:203], a[204:207], a[40:43], v[200:203]\n" \
"v_mfma_f32_16x16x32_bf16 v[200:203], a[220:223], a[44:47], v[200:203]\n" \
"v_mfma_f32_16x16x32_bf16 v[204:207], a[196:199], a[48:51], v[140:143]\n" \
"v_mfma_f32_16x16x32_bf16 v[204:207], a[200:203], a[52:55], v[204:207]\n" \
"v_mfma_f32_16x16x32_bf16 v[204:207], a[204:207], a[56:59], v[204:207]\n" \
"v_mfma_f32_16x16x32_bf16 v[204:207], a[220:223], a[60:63], v[204:207]\n" \
"s_nop 7\n" \
"s_nop 0\n" \
"v_cmp_u_f32_e64 s[44:45], v192, v196\n" \
"v_cmp_u_f32_e64 s[56:57], v200, v204\n" \
"s_or_b64 s[56:57], s[56:57], s[44:45]\n" \
"s_cbranch_scc0 Lbk6_%=\n" \
"s_sub_u32 s51, s51, 1\n" \
"s_cbranch_scc0 Lsq6_%=\n" \
"s_branch Lbk6_%=\n" \
"Lsp7_%=:\n" \
"s_movk_i32 s51, 0x1000\n" \
"s_cmp_gt_u32 s48, 6\n" \
"s_cselect_b32 s61, 6, 7\n" \
"s_mul_i32 s61, s61, 262144\n" \
"Lsq7_%=:\n" \
"buffer_load_dwordx4 a[128:131], %[oq0], %[srdz], s61 offen offset:0 sc1\n" \
"buffer_load_dwordx4 a[132:135], %[oq0], %[srdz], s61 offen offset:1024 sc1\n" \
"buffer_load_dwordx4 a[136:139], %[oq0], %[srdz], s61 offen offset:2048 sc1\n" \
"buffer_load_dwordx4 a[140:143], %[oq0], %[srdz], s61 offen offset:3072 sc1\n" \
"buffer_load_dwordx4 a[144:147], v208, %[srdz], s61 offen offset:0 sc1\n" \
"buffer_load_dwordx4 a[148:151], v208, %[srdz], s61 offen offset:1024 sc1\n" \
"buffer_load_dwordx4 a[152:155], v208, %[srdz], s61 offen offset:2048 sc1\n" \
"buffer_load_dwordx4 a[156:159], v208, %[srdz], s61 offen offset:3072 sc1\n" \
"buffer_load_dwordx4 a[160:163], v209, %[srdz], s61 offen offset:0 sc1\n" \
"buffer_load_dwordx4 a[164:167], v209, %[srdz], s61 offen offset:1024 sc1\n" \
"buffer_load_dwordx4 a[168:171], v209, %[srdz], s61 offen offset:2048 sc1\n" \
"buffer_load_dwordx4 a[172:175], v209, %[srdz], s61 offen offset:3072 sc1\n" \
"buffer_load_dwordx4 a[176:179], v210, %[srdz], s61 offen offset:0 sc1\n" \
"buffer_load_dwordx4 a[180:183], v210, %[srdz], s61 offen offset:1024 sc1\n" \
"buffer_load_dwordx4 a[184:187], v210, %[srdz], s61 offen offset:2048 sc1\n" \
"buffer_load_dwordx4 a[188:191], v210, %[srdz], s61 offen offset:3072 sc1\n" \
"s_waitcnt vmcnt(0)\n" \
"s_nop 0\n" \
"v_mfma_f32_16x16x32_bf16 v[128:131], a[208:211], a[128:131], v[192:195]\n" \
"v_mfma_f32_16x16x32_bf16 v[128:131], a[224:227], a[132:135], v[128:131]\n" \
"v_mfma_f32_16x16x32_bf16 v[128:131], a[228:231], a[136:139], v[128:131]\n" \
"v_mfma_f32_16x16x32_bf16 v[128:131], a[236:239], a[140:143], v[128:131]\n" \
"v_mfma_f32_16x16x32_bf16 v[132:135], a[208:211], a[144:147], v[196:199]\n" \
"v_mfma_f32_16x16x32_bf16 v[132:135], a[224:227], a[148:151], v[132:135]\n" \
"v_mfma_f32_16x16x32_bf16 v[132:135], a[228:231], a[152:155], v[132:135]\n" \
"v_mfma_f32_16x16x32_bf16 v[132:135], a[236:239], a[156:159], v[132:135]\n" \
"v_mfma_f32_16x16x32_bf16 v[136:139], a[208:211], a[160:163], v[200:203]\n" \
"v_mfma_f32_16x16x32_bf16 v[136:139], a[224:227], a[164:167], v[136:139]\n" \
"v_mfma_f32_16x16x32_bf16 v[136:139], a[228:231], a[168:171], v[136:139]\n" \
"v_mfma_f32_16x16x32_bf16 v[136:139], a[236:239], a[172:175], v[136:139]\n" \
"v_mfma_f32_16x16x32_bf16 v[140:143], a[208:211], a[176:179], v[204:207]\n" \
"v_mfma_f32_16x16x32_bf16 v[140:143], a[224:227], a[180:183], v[140:143]\n" \
"v_mfma_f32_16x16x32_bf16 v[140:143], a[228:231], a[184:187], v[140:143]\n" \
"v_mfma_f32_16x16x32_bf16 v[140:143], a[236:239], a[188:191], v[140:143]\n" \
"s_nop 7\n" \
"s_nop 0\n" \
"v_cmp_u_f32_e64 s[44:45], v128, v132\n" \
"v_cmp_u_f32_e64 s[56:57], v136, v140\n" \
"s_or_b64 s[56:57], s[56:57], s[44:45]\n" \
"s_cbranch_scc0 Lbk7_%=\n" \
"s_sub_u32 s51, s51, 1\n" \
"s_cbranch_scc0 Lsq7_%=\n" \
"s_branch Lbk7_%=\n" \
"Lspk_%=:\n" \
"s_nop 0\n" \
"s_mov_b32 s51, 0x10000\n" \
"Lsqk_%=:\n" \
"buffer_load_dword v229, v226, %[srdz], s50 offen sc1\n" \
"s_waitcnt vmcnt(0)\n" \
"s_nop 0\n" \
"v_cmp_eq_u32_e64 s[56:57], 0, v229\n" \
"s_cmp_lg_u64 s[56:57], 0\n" \
"s_cbranch_scc0 Lbkk_%=\n" \
"s_sub_u32 s51, s51, 1\n" \
"s_cbranch_scc0 Lsqk_%=\n" \
"s_branch Lbkk_%=\n" \
"Lspp_%=:\n" \
"s_nop 0\n" \
"s_mov_b32 s51, 0x10000\n" \
"Lsqp_%=:\n" \
"buffer_load_dword v232, v226, %[srdz], s54 offen sc1\n" \
"s_waitcnt vmcnt(0)\n" \
"v_cmp_eq_u32_e32 vcc, 0, v232\n" \
"s_cbranch_vccz Lbkp_%=\n" \
"s_sub_u32 s51, s51, 1\n" \
"s_cbranch_scc0 Lsqp_%=\n" \
"s_branch Lbkp_%=\n" \
"Lprobe_%=:\n" \
"s_mov_b32 s62, -1\n" \
"s_mov_b32 s63, -1\n" \
"s_memtime s[56:57]\n" \
"s_waitcnt lgkmcnt(0)\n" \
"s_nop 0\n" \
"s_load_dword s61, %[pcnt], 0x29900\n" \
"s_waitcnt lgkmcnt(0)\n" \
"s_nop 0\n" \
"s_memtime s[58:59]\n" \
"s_waitcnt lgkmcnt(0)\n" \
"s_sub_u32 s58, s58, s56\n" \
"s_min_u32 s62, s62, s58\n" \
"s_nop 0\n" \
"s_memtime s[56:57]\n" \
"s_waitcnt lgkmcnt(0)\n" \
"s_nop 0\n" \
"s_load_dword s61, %[pcnt], 0x2b900\n" \
"s_waitcnt lgkmcnt(0)\n" \
"s_nop 0\n" \
"s_memtime s[58:59]\n" \
"s_waitcnt lgkmcnt(0)\n" \
"s_sub_u32 s58, s58, s56\n" \
"s_min_u32 s63, s63, s58\n" \
"s_nop 0\n" \
"s_memtime s[56:57]\n" \
"s_waitcnt lgkmcnt(0)\n" \
"s_nop 0\n" \
"s_load_dword s61, %[pcnt], 0x2dd80\n" \
"s_waitcnt lgkmcnt(0)\n" \
"s_nop 0\n" \
"s_memtime s[58:59]\n" \
"s_waitcnt lgkmcnt(0)\n" \
"s_sub_u32 s58, s58, s56\n" \
"s_min_u32 s62, s62, s58\n" \
"s_nop 0\n" \
"s_memtime s[56:57]\n" \
"s_waitcnt lgkmcnt(0)\n" \
"s_nop 0\n" \
"s_load_dword s61, %[pcnt], 0x2fd80\n" \
"s_waitcnt lgkmcnt(0)\n" \
"s_nop 0\n" \
"s_memtime s[58:59]\n" \
"s_waitcnt lgkmcnt(0)\n" \
"s_sub_u32 s58, s58, s56\n" \
"s_min_u32 s63, s63, s58\n" \
"s_nop 0\n" \
"s_memtime s[56:57]\n" \
"s_waitcnt lgkmcnt(0)\n" \
"s_nop 0\n" \
"s_load_dword s61, %[pcnt], 0x2a200\n" \
"s_waitcnt lgkmcnt(0)\n" \
"s_nop 0\n" \
"s_memtime s[58:59]\n" \
"s_waitcnt lgkmcnt(0)\n" \
"s_sub_u32 s58, s58, s56\n" \
"s_min_u32 s62, s62, s58\n" \
"s_nop 0\n" \
"s_memtime s[56:57]\n" \
"s_waitcnt lgkmcnt(0)\n" \
"s_nop 0\n" \
"s_load_dword s61, %[pcnt], 0x2c200\n" \
"s_waitcnt lgkmcnt(0)\n" \
"s_nop 0\n" \
"s_memtime s[58:59]\n" \
"s_waitcnt lgkmcnt(0)\n" \
"s_sub_u32 s58, s58, s56\n" \
"s_min_u32 s63, s63, s58\n" \
"s_cmp_lt_u32 s62, s63\n" \
"s_cselect_b32 s61, 8, 0\n" \
"s_mov_b64 exec, 1\n" \
"buffer_store_dwordx2 v[234:235], off, %[srdz], s61 offset:128 sc1\n" \
"s_mov_b64 exec, -1\n" \
"s_branch Lgo_%=\n" \
"Lnsp_%=:\n" \
"s_mov_b32 s61, 0x10000\n" \
"Lnsq_%=:\n" \
"buffer_load_dwordx4 v[230:233], off, %[srdz], 0 offset:128 sc1\n" \
"s_waitcnt vmcnt(0)\n" \
"v_cmp_eq_u64_e32 vcc, v[230:231], v[234:235]\n" \
"v_cmp_eq_u64_e64 s[56:57], v[232:233], v[234:235]\n" \
"s_nop 3\n" \
"s_or_b64 s[58:59], vcc, s[56:57]\n" \
"s_cbranch_scc1 Lnbk_%=\n" \
"s_sub_u32 s61, s61, 1\n" \
"s_cbranch_scc0 Lnsq_%=\n" \
"s_branch Lnbk_%=\n" \
"Lwalk_%=:\n" \
"s_mov_b64 exec, 0\n" \
"s_cmp_eq_u32 %[wid], 1\n" \
"s_cbranch_scc1 Lk0_%=\n" \
"s_cmp_eq_u32 %[wid], 2\n" \
"s_cbranch_scc1 Lk6_%=\n" \
"s_branch Lk12_%=\n" \
"adaln_kr:\n" \
"s_mov_b64 exec, -1\n" \
"s_branch Lgo_%=\n" \
"Lslow_%=:\n" \
"s_mov_b32 s52, 1\n" \
"s_mov_b32 s48, 0\n" \
"s_mov_b32 s51, 32\n" \
"s_nop 0\n" \
"s_mov_b32 s62, 0x80000000\n" \
"s_mov_b32 s63, 0x80000000\n" \
"s_waitcnt vmcnt(0)\n" \
"s_nop 0\n" \
"v_readlane_b32 s64, %[tlo], 9\n" \
"v_readlane_b32 s65, %[thi], 9\n" \
"s_mov_b32 s66, %[wbytes]\n" \
"v_mov_b32_e32 v229, %[vg0]\n" \
"s_mov_b32 s44, %[m0t]\n" \
"s_mov_b32 s45, 0\n" \
"v_xor_b32_e64 v208, 64, %[kb0]\n" \
"v_xor_b32_e32 v209, 0x80, %[kb0]\n" \
"v_xor_b32_e32 v210, 0xc0, %[kb0]\n" \
"v_xor_b32_e64 v212, 64, %[kd0]\n" \
"v_xor_b32_e32 v213, 0x80, %[kd0]\n" \
"v_xor_b32_e32 v214, 0xc0, %[kd0]\n" \
"v_xor_b32_e64 v211, 32, %[vvb]\n" \
"v_add_u32_e32 v215, 0xfffffc80, %[vd0]\n" \
"s_mov_b32 s66, 0\n" \
"s_add_u32 s40, %[m0b], 0\n" \
"s_add_u32 s41, %[m0b], 16384\n" \
"s_add_u32 s42, %[m0b], 32768\n" \
"s_add_u32 s43, %[m0b], 49152\n" \
"v_mov_b32_e32 v225, 0xff800000\n" \
"v_mov_b32_e32 v226, 0xff800000\n" \
"v_mov_b32_e32 v227, 0xff800000\n" \
"v_mov_b32_e32 v228, 0xff800000\n" \
"s_waitcnt vmcnt(0) lgkmcnt(0)\n" \
"s_barrier\n" \
"Lsl_%=:\n" \
"s_mov_b32 m0, s40\n" \
"s_nop 0\n" \
"buffer_load_dwordx4 %[kd0], %[srdk], s48 offen offset:0 lds\n" \
"buffer_load_dwordx4 v212, %[srdk], s48 offen offset:1024 lds\n" \
"buffer_load_dwordx4 v213, %[srdk], s48 offen offset:2048 lds\n" \
"buffer_load_dwordx4 v214, %[srdk], s48 offen offset:3072 lds\n" \
"s_waitcnt vmcnt(0)\n" \
"s_barrier\n" \
"ds_read_b128 a[64:67], %[kb0] offset:0\n" \
"ds_read_b128 a[68:71], v208 offset:0\n" \
"ds_read_b128 a[72:75], v209 offset:0\n" \
"ds_read_b128 a[76:79], v210 offset:0\n" \
"ds_read_b128 a[92:95], v210 offset:4096\n" \
"ds_read_b128 a[88:91], v209 offset:4096\n" \
"ds_read_b128 a[84:87], v208 offset:4096\n" \
"ds_read_b128 a[80:83], %[kb0] offset:4096\n" \
"ds_read_b128 a[96:99], %[kb0] offset:8192\n" \
"ds_read_b128 a[100:103], v208 offset:8192\n" \
"ds_read_b128 a[104:107], v209 offset:8192\n" \
"ds_read_b128 a[108:111], v210 offset:8192\n" \
"ds_read_b128 a[124:127], v210 offset:12288\n" \
"ds_read_b128 a[120:123], v209 offset:12288\n" \
"ds_read_b128 a[116:119], v208 offset:12288\n" \
"ds_read_b128 a[112:115], %[kb0] offset:12288\n" \
"s_waitcnt lgkmcnt(0)\n" \
"s_barrier\n" \
"v_mfma_f32_16x16x32_bf16 v[128:131], a[64:67], a[0:3], 0\n" \
"v_mfma_f32_16x16x32_bf16 v[128:131], a[68:71], a[4:7], v[128:131]\n" \
"v_mfma_f32_16x16x32_bf16 v[128:131], a[72:75], a[8:11], v[128:131]\n" \
"v_mfma_f32_16x16x32_bf16 v[128:131], a[76:79], a[12:15], v[128:131]\n" \
"v_mfma_f32_16x16x32_bf16 v[132:135], a[92:95], a[12:15], 0\n" \
"v_mfma_f32_16x16x32_bf16 v[132:135], a[88:91], a[8:11], v[132:135]\n" \
"v_mfma_f32_16x16x32_bf16 v[132:135], a[84:87], a[4:7], v[132:135]\n" \
"v_mfma_f32_16x16x32_bf16 v[132:135], a[80:83], a[0:3], v[132:135]\n" \
"v_mfma_f32_16x16x32_bf16 v[136:139], a[64:67], a[16:19], 0\n" \
"v_mfma_f32_16x16x32_bf16 v[136:139], a[68:71], a[20:23], v[136:139]\n" \
"v_mfma_f32_16x16x32_bf16 v[136:139], a[72:75], a[24:27], v[136:139]\n" \
"v_mfma_f32_16x16x32_bf16 v[136:139], a[76:79], a[28:31], v[136:139]\n" \
"v_mfma_f32_16x16x32_bf16 v[140:143], a[92:95], a[28:31], 0\n" \
"v_mfma_f32_16x16x32_bf16 v[140:143], a[88:91], a[24:27], v[140:143]\n" \
"v_mfma_f32_16x16x32_bf16 v[140:143], a[84:87], a[20:23], v[140:143]\n" \
"v_mfma_f32_16x16x32_bf16 v[140:143], a[80:83], a[16:19], v[140:143]\n" \
"v_mfma_f32_16x16x32_bf16 v[144:147], a[64:67], a[32:35], 0\n" \
"v_mfma_f32_16x16x32_bf16 v[144:147], a[68:71], a[36:39], v[144:147]\n" \
"v_mfma_f32_16x16x32_bf16 v[144:147], a[72:75], a[40:43], v[144:147]\n" \
"v_mfma_f32_16x16x32_bf16 v[144:147], a[76:79], a[44:47], v[144:147]\n" \
"v_mfma_f32_16x16x32_bf16 v[148:151], a[92:95], a[44:47], 0\n" \
"v_mfma_f32_16x16x32_bf16 v[148:151], a[88:91], a[40:43], v[148:151]\n" \
"v_mfma_f32_16x16x32_bf16 v[148:151], a[84:87], a[36:39], v[148:151]\n" \
"v_mfma_f32_16x16x32_bf16 v[148:151], a[80:83], a[32:35], v[148:151]\n" \
"v_mfma_f32_16x16x32_bf16 v[152:155], a[64:67], a[48:51], 0\n" \
"v_mfma_f32_16x16x32_bf16 v[152:155], a[68:71], a[52:55], v[152:155]\n" \
"v_mfma_f32_16x16x32_bf16 v[152:155], a[72:75], a[56:59], v[152:155]\n" \
"v_mfma_f32_16x16x32_bf16 v[152:155], a[76:79], a[60:63], v[152:155]\n" \
"v_mfma_f32_16x16x32_bf16 v[156:159], a[92:95], a[60:63], 0\n" \
"v_mfma_f32_16x16x32_bf16 v[156:159], a[88:91], a[56:59], v[156:159]\n" \
"v_mfma_f32_16x16x32_bf16 v[156:159], a[84:87], a[52:55], v[156:159]\n" \
"v_mfma_f32_16x16x32_bf16 v[156:159], a[80:83], a[48:51], v[156:159]\n" \
"s_nop 7\n" \
"s_nop 0\n" \
"v_max3_f32 v230, v128, v129, v130\n" \
"v_max3_f32 v231, v131, v132, v133\n" \
"v_max3_f32 v230, v230, v134, v135\n" \
"v_max3_f32 v225, v230, v231, v225\n" \
"v_max3_f32 v230, v136, v137, v138\n" \
"v_max3_f32 v231, v139, v140, v141\n" \
"v_max3_f32 v230, v230, v142, v143\n" \
"v_max3_f32 v226, v230, v231, v226\n" \
"v_max3_f32 v230, v144, v145, v146\n" \
"v_max3_f32 v231, v147, v148, v149\n" \
"v_max3_f32 v230, v230, v150, v151\n" \
"v_max3_f32 v227, v230, v231, v227\n" \
"v_max3_f32 v230, v152, v153, v154\n" \
"v_max3_f32 v231, v155, v156, v157\n" \
"v_max3_f32 v230, v230, v158, v159\n" \
"v_max3_f32 v228, v230, v231, v228\n" \
"s_nop 3\n" \
"s_nop 0\n" \
"v_mfma_f32_16x16x32_bf16 v[128:131], a[96:99], a[0:3], 0\n" \
"v_mfma_f32_16x16x32_bf16 v[128:131], a[100:103], a[4:7], v[128:131]\n" \
"v_mfma_f32_16x16x32_bf16 v[128:131], a[104:107], a[8:11], v[128:131]\n" \
"v_mfma_f32_16x16x32_bf16 v[128:131], a[108:111], a[12:15], v[128:131]\n" \
"v_mfma_f32_16x16x32_bf16 v[132:135], a[124:127], a[12:15], 0\n" \
"v_mfma_f32_16x16x32_bf16 v[132:135], a[120:123], a[8:11], v[132:135]\n" \
"v_mfma_f32_16x16x32_bf16 v[132:135], a[116:119], a[4:7], v[132:135]\n" \
"v_mfma_f32_16x16x32_bf16 v[132:135], a[112:115], a[0:3], v[132:135]\n" \
"v_mfma_f32_16x16x32_bf16 v[136:139], a[96:99], a[16:19], 0\n" \
"v_mfma_f32_16x16x32_bf16 v[136:139], a[100:103], a[20:23], v[136:139]\n" \
"v_mfma_f32_16x16x32_bf16 v[136:139], a[104:107], a[24:27], v[136:139]\n" \
"v_mfma_f32_16x16x32_bf16 v[136:139], a[108:111], a[28:31], v[136:139]\n" \
"v_mfma_f32_16x16x32_bf16 v[140:143], a[124:127], a[28:31], 0\n" \
"v_mfma_f32_16x16x32_bf16 v[140:143], a[120:123], a[24:27], v[140:143]\n" \
"v_mfma_f32_16x16x32_bf16 v[140:143], a[116:119], a[20:23], v[140:143]\n" \
"v_mfma_f32_16x16x32_bf16 v[140:143], a[112:115], a[16:19], v[140:143]\n" \
"v_mfma_f32_16x16x32_bf16 v[144:147], a[96:99], a[32:35], 0\n" \
"v_mfma_f32_16x16x32_bf16 v[144:147], a[100:103], a[36:39], v[144:147]\n" \
"v_mfma_f32_16x16x32_bf16 v[144:147], a[104:107], a[40:43], v[144:147]\n" \
"v_mfma_f32_16x16x32_bf16 v[144:147], a[108:111], a[44:47], v[144:147]\n" \
"v_mfma_f32_16x16x32_bf16 v[148:151], a[124:127], a[44:47], 0\n" \
"v_mfma_f32_16x16x32_bf16 v[148:151], a[120:123], a[40:43], v[148:151]\n" \
"v_mfma_f32_16x16x32_bf16 v[148:151], a[116:119], a[36:39], v[148:151]\n" \
"v_mfma_f32_16x16x32_bf16 v[148:151], a[112:115], a[32:35], v[148:151]\n" \
"v_mfma_f32_16x16x32_bf16 v[152:155], a[96:99], a[48:51], 0\n" \
"v_mfma_f32_16x16x32_bf16 v[152:155], a[100:103], a[52:55], v[152:155]\n" \
"v_mfma_f32_16x16x32_bf16 v[152:155], a[104:107], a[56:59], v[152:155]\n" \
"v_mfma_f32_16x16x32_bf16 v[152:155], a[108:111], a[60:63], v[152:155]\n" \
"v_mfma_f32_16x16x32_bf16 v[156:159], a[124:127], a[60:63], 0\n" \
"v_mfma_f32_16x16x32_bf16 v[156:159], a[120:123], a[56:59], v[156:159]\n" \
"v_mfma_f32_16x16x32_bf16 v[156:159], a[116:119], a[52:55], v[156:159]\n" \
"v_mfma_f32_16x16x32_bf16 v[156:159], a[112:115], a[48:51], v[156:159]\n" \
"s_nop 7\n" \
"s_nop 0\n" \
"v_max3_f32 v230, v128, v129, v130\n" \
"v_max3_f32 v231, v131, v132, v133\n" \
"v_max3_f32 v230, v230, v134, v135\n" \
"v_max3_f32 v225, v230, v231, v225\n" \
"v_max3_f32 v230, v136, v137, v138\n" \
"v_max3_f32 v231, v139, v140, v141\n" \
"v_max3_f32 v230, v230, v142, v143\n" \
"v_max3_f32 v226, v230, v231, v226\n" \
"v_max3_f32 v230, v144, v145, v146\n" \
"v_max3_f32 v231, v147, v148, v149\n" \
"v_max3_f32 v230, v230, v150, v151\n" \
"v_max3_f32 v227, v230, v231, v227\n" \
"v_max3_f32 v230, v152, v153, v154\n" \
"v_max3_f32 v231, v155, v156, v157\n" \
"v_max3_f32 v230, v230, v158, v159\n" \
"v_max3_f32 v228, v230, v231, v228\n" \
"s_nop 3\n" \
"s_add_u32 s48, s48, s54\n" \
"s_sub_u32 s51, s51, 1\n" \
"s_cmp_lg_u32 s51, 0\n" \
"s_cbranch_scc1 Lsl_%=\n" \
"s_nop 0\n" \
"v_mov_b32_e32 v232, v225\n" \
"v_mov_b32_e32 v233, v226\n" \
"v_mov_b32_e32 v234, v227\n" \
"v_mov_b32_e32 v235, v228\n" \
"s_nop 0\n" \
"v_permlane16_swap_b32_e32 v225, v232\n" \
"v_permlane16_swap_b32_e32 v226, v233\n" \
"v_permlane16_swap_b32_e32 v227, v234\n" \
"v_permlane16_swap_b32_e32 v228, v235\n" \
"v_max_f32_e32 v225, v225, v232\n" \
"v_max_f32_e32 v226, v226, v233\n" \
"v_max_f32_e32 v227, v227, v234\n" \
"v_max_f32_e32 v228, v228, v235\n" \
"v_mov_b32_e32 v232, v225\n" \
"v_mov_b32_e32 v233, v226\n" \
"v_mov_b32_e32 v234, v227\n" \
"v_mov_b32_e32 v235, v228\n" \
"s_nop 0\n" \
"v_permlane32_swap_b32_e32 v225, v232\n" \
"v_permlane32_swap_b32_e32 v226, v233\n" \
"v_permlane32_swap_b32_e32 v227, v234\n" \
"v_permlane32_swap_b32_e32 v228, v235\n" \
"v_max_f32_e32 v225, v225, v232\n" \
"v_max_f32_e32 v226, v226, v233\n" \
"v_max_f32_e32 v227, v227, v234\n" \
"v_max_f32_e32 v228, v228, v235\n" \
"s_cmp_lg_u32 s69, 0\n" \
"s_cbranch_scc1 Lnewm_%=\n" \
"v_sub_f32_e32 v225, 0, v160\n" \
"v_sub_f32_e32 v226, 0, v164\n" \
"v_sub_f32_e64 v227, 0, v168\n" \
"v_sub_f32_e32 v228, 0, v172\n" \
"Lnewm_%=:\n" \
"s_mov_b32 s48, 0\n" \
"s_mov_b32 m0, s40\n" \
"s_nop 0\n" \
"buffer_load_dwordx4 %[kd0], %[srdk], s48 offen offset:0 lds\n" \
"buffer_load_dwordx4 v212, %[srdk], s48 offen offset:1024 lds\n" \
"buffer_load_dwordx4 v213, %[srdk], s48 offen offset:2048 lds\n" \
"buffer_load_dwordx4 v214, %[srdk], s48 offen offset:3072 lds\n" \
"s_mov_b32 s48, 16384\n" \
"s_mov_b32 m0, s41\n" \
"s_nop 0\n" \
"buffer_load_dwordx4 %[kd0], %[srdk], s48 offen offset:0 lds\n" \
"buffer_load_dwordx4 v212, %[srdk], s48 offen offset:1024 lds\n" \
"buffer_load_dwordx4 v213, %[srdk], s48 offen offset:2048 lds\n" \
"buffer_load_dwordx4 v214, %[srdk], s48 offen offset:3072 lds\n" \
"s_add_u32 m0, %[m0b], 65536\n" \
"s_nop 0\n" \
"s_nop 0\n" \
"buffer_load_dwordx4 %[vd0], %[srdv], s48 offen offset:0 lds\n" \
"buffer_load_dwordx4 v215, %[srdv], s48 offen offset:1024 lds\n" \
"buffer_load_dwordx4 %[vd0], %[srdv], s48 offen offset:2048 lds\n" \
"buffer_load_dwordx4 v215, %[srdv], s48 offen offset:3072 lds\n" \
"s_mov_b32 s48, 32768\n" \
"s_mov_b32 m0, s42\n" \
"s_nop 0\n" \
"buffer_load_dwordx4 %[kd0], %[srdk], s48 offen offset:0 lds\n" \
"buffer_load_dwordx4 v212, %[srdk], s48 offen offset:1024 lds\n" \
"buffer_load_dwordx4 v213, %[srdk], s48 offen offset:2048 lds\n" \
"buffer_load_dwordx4 v214, %[srdk], s48 offen offset:3072 lds\n" \
"s_add_u32 m0, %[m0b], 81920\n" \
"s_nop 0\n" \
"s_nop 0\n" \
"buffer_load_dwordx4 %[vd0], %[srdv], s48 offen offset:0 lds\n" \
"buffer_load_dwordx4 v215, %[srdv], s48 offen offset:1024 lds\n" \
"buffer_load_dwordx4 %[vd0], %[srdv], s48 offen offset:2048 lds\n" \
"buffer_load_dwordx4 v215, %[srdv], s48 offen offset:3072 lds\n" \
"s_mov_b32 s48, 49152\n" \
"s_mov_b32 m0, s43\n" \
"s_nop 0\n" \
"buffer_load_dwordx4 %[kd0], %[srdk], s48 offen offset:0 lds\n" \
"buffer_load_dwordx4 v212, %[srdk], s48 offen offset:1024 lds\n" \
"buffer_load_dwordx4 v213, %[srdk], s48 offen offset:2048 lds\n" \
"buffer_load_dwordx4 v214, %[srdk], s48 offen offset:3072 lds\n" \
"s_add_u32 m0, %[m0b], 98304\n" \
"s_nop 0\n" \
"s_nop 0\n" \
"buffer_load_dwordx4 %[vd0], %[srdv], s48 offen offset:0 lds\n" \
"buffer_load_dwordx4 v215, %[srdv], s48 offen offset:1024 lds\n" \
"buffer_load_dwordx4 %[vd0], %[srdv], s48 offen offset:2048 lds\n" \
"buffer_load_dwordx4 v215, %[srdv], s48 offen offset:3072 lds\n" \
"s_waitcnt vmcnt(0)\n" \
"s_barrier\n" \
"ds_read_b128 a[64:67], %[kb0] offset:0\n" \
"ds_read_b128 a[68:71], v208 offset:0\n" \
"ds_read_b128 a[72:75], v209 offset:0\n" \
"ds_read_b128 a[76:79], v210 offset:0\n" \
"ds_read_b128 a[92:95], v210 offset:4096\n" \
"ds_read_b128 a[88:91], v209 offset:4096\n" \
"ds_read_b128 a[84:87], v208 offset:4096\n" \
"ds_read_b128 a[80:83], %[kb0] offset:4096\n" \
"s_mov_b32 s48, 65536\n" \
"s_mov_b32 s60, 0\n" \
"s_waitcnt lgkmcnt(0)\n" \
"v_mfma_f32_16x16x32_bf16 v[128:131], a[64:67], a[0:3], 0\n" \
"ds_read_b128 a[96:99], %[kb0] offset:8192\n" \
"v_mfma_f32_16x16x32_bf16 v[128:131], a[68:71], a[4:7], v[128:131]\n" \
"ds_read_b128 a[100:103], v208 offset:8192\n" \
"v_mfma_f32_16x16x32_bf16 v[128:131], a[72:75], a[8:11], v[128:131]\n" \
"ds_read_b128 a[104:107], v209 offset:8192\n" \
"v_mfma_f32_16x16x32_bf16 v[128:131], a[76:79], a[12:15], v[128:131]\n" \
"ds_read_b128 a[108:111], v210 offset:8192\n" \
"v_mfma_f32_16x16x32_bf16 v[132:135], a[92:95], a[12:15], 0\n" \
"ds_read_b128 a[124:127], v210 offset:12288\n" \
"v_mfma_f32_16x16x32_bf16 v[132:135], a[88:91], a[8:11], v[132:135]\n" \
"ds_read_b128 a[120:123], v209 offset:12288\n" \
"v_mfma_f32_16x16x32_bf16 v[132:135], a[84:87], a[4:7], v[132:135]\n" \
"ds_read_b128 a[116:119], v208 offset:12288\n" \
"v_mfma_f32_16x16x32_bf16 v[132:135], a[80:83], a[0:3], v[132:135]\n" \
"ds_read_b128 a[112:115], %[kb0] offset:12288\n" \
"v_mfma_f32_16x16x32_bf16 v[136:139], a[64:67], a[16:19], 0\n" \
"ds_read_b64_tr_b16 a[128:129], %[vvb] offset:0\n" \
"v_mfma_f32_16x16x32_bf16 v[136:139], a[68:71], a[20:23], v[136:139]\n" \
"ds_read_b64_tr_b16 a[130:131], %[vvb] offset:4096\n" \
"v_mfma_f32_16x16x32_bf16 v[136:139], a[72:75], a[24:27], v[136:139]\n" \
"ds_read_b64_tr_b16 a[132:133], v211 offset:0\n" \
"v_mfma_f32_16x16x32_bf16 v[136:139], a[76:79], a[28:31], v[136:139]\n" \
"ds_read_b64_tr_b16 a[134:135], v211 offset:4096\n" \
"v_mfma_f32_16x16x32_bf16 v[140:143], a[92:95], a[28:31], 0\n" \
"ds_read_b64_tr_b16 a[136:137], %[vvb] offset:512\n" \
"v_mfma_f32_16x16x32_bf16 v[140:143], a[88:91], a[24:27], v[140:143]\n" \
"ds_read_b64_tr_b16 a[138:139], %[vvb] offset:4608\n" \
"v_mfma_f32_16x16x32_bf16 v[140:143], a[84:87], a[20:23], v[140:143]\n" \
"ds_read_b64_tr_b16 a[140:141], v211 offset:512\n" \
"v_mfma_f32_16x16x32_bf16 v[140:143], a[80:83], a[16:19], v[140:143]\n" \
"ds_read_b64_tr_b16 a[142:143], v211 offset:4608\n" \
"v_mfma_f32_16x16x32_bf16 v[144:147], a[64:67], a[32:35], 0\n" \
"ds_read_b64_tr_b16 a[144:145], %[vvb] offset:1024\n" \
"v_mfma_f32_16x16x32_bf16 v[144:147], a[68:71], a[36:39], v[144:147]\n" \
"ds_read_b64_tr_b16 a[146:147], %[vvb] offset:5120\n" \
"v_mfma_f32_16x16x32_bf16 v[144:147], a[72:75], a[40:43], v[144:147]\n" \
"ds_read_b64_tr_b16 a[148:149], v211 offset:1024\n" \
"v_mfma_f32_16x16x32_bf16 v[144:147], a[76:79], a[44:47], v[144:147]\n" \
"ds_read_b64_tr_b16 a[150:151], v211 offset:5120\n" \
"v_mfma_f32_16x16x32_bf16 v[148:151], a[92:95], a[44:47], 0\n" \
"ds_read_b64_tr_b16 a[152:153], %[vvb] offset:1536\n" \
"v_mfma_f32_16x16x32_bf16 v[148:151], a[88:91], a[40:43], v[148:151]\n" \
"ds_read_b64_tr_b16 a[154:155], %[vvb] offset:5632\n" \
"v_mfma_f32_16x16x32_bf16 v[148:151], a[84:87], a[36:39], v[148:151]\n" \
"ds_read_b64_tr_b16 a[156:157], v211 offset:1536\n" \
"v_mfma_f32_16x16x32_bf16 v[148:151], a[80:83], a[32:35], v[148:151]\n" \
"ds_read_b64_tr_b16 a[158:159], v211 offset:5632\n" \
"v_mfma_f32_16x16x32_bf16 v[152:155], a[64:67], a[48:51], 0\n" \
"v_mfma_f32_16x16x32_bf16 v[152:155], a[68:71], a[52:55], v[152:155]\n" \
"v_mfma_f32_16x16x32_bf16 v[152:155], a[72:75], a[56:59], v[152:155]\n" \
"v_mfma_f32_16x16x32_bf16 v[152:155], a[76:79], a[60:63], v[152:155]\n" \
"v_mfma_f32_16x16x32_bf16 v[156:159], a[92:95], a[60:63], 0\n" \
"v_mfma_f32_16x16x32_bf16 v[156:159], a[88:91], a[56:59], v[156:159]\n" \
"v_mfma_f32_16x16x32_bf16 v[156:159], a[84:87], a[52:55], v[156:159]\n" \
"v_mfma_f32_16x16x32_bf16 v[156:159], a[80:83], a[48:51], v[156:159]\n" \
"s_branch Lhavem_%=\n" \
"s_nop 0\n" \
".p2align 12\n" \
"adaln_pg0:\n" \
"s_cbranch_execz adaln_pg1\n" \
"Lend_%=:\n" \
""

#define SRD_WORD3 0x00020000
DI u32x4 ld16_sc1(const void* p) { u32x4 v; asm volatile("global_load_dwordx4 %0, %1, off sc1" : "=v"(v) : "v"(p) : "memory"); return v; }
DI unsigned ord_key(float r, unsigned row) {
  const unsigned b = __float_as_uint(r), o = (b & 0x80000000u) ? ~b : (b | 0x80000000u);
  return max(o & 0xfffff800u, 0x800u) | (2047u - row);
}
DI void top2_merge(unsigned& a1, unsigned& a2, unsigned b1, unsigned b2) {
  const unsigned n1 = max(a1, b1), n2 = max(min(a1, b1), max(a2, b2)); a1 = n1; a2 = n2;
}
template <int CTRL> DI void top2_dpp(unsigned& k1, unsigned& k2) {
  const unsigned p1 = __builtin_amdgcn_update_dpp(0u, k1, CTRL, 0xf, 0xf, true), p2 = __builtin_amdgcn_update_dpp(0u, k2, CTRL, 0xf, 0xf, true); top2_merge(k1, k2, p1, p2);
}
DI float row_sum(float v) {
  v = dpp_add<DPP_XOR1>(v); v = dpp_add<DPP_XOR2>(v); v = dpp_add<DPP_ROR8>(v);
  v = swap16_add(v, v); return swap32_add(v, v);
}
DI unsigned h2_pair(float x0, float x1, float m, float rden, unsigned c1w, unsigned sbw) { return ln_pair(x0, x1, m, rden, lo16(c1w), hi16(c1w), lo16(sbw), hi16(sbw)); }
#define PAGE(n, nx) asm volatile("s_branch adaln_pe" #n "\n.p2align 12\nadaln_pg" #n ":\n\ts_cbranch_execz " nx "\nadaln_pe" #n ":")
#define STR_(x) #x
#define STR(x) STR_(x)
constexpr int MOE_WGS = DFF / 16;
constexpr unsigned RETRY_CAP = 4096;
#define ASLEEP 2
#define KLOOK 6
#define YSPLIT 8
#define FSLEEP 14
#define KSLEEP 3
#define YEARLY 8
#define FREST 0
#define KL0 1u
#define KLS 2u
#define KST 2u
#define KCHK 8
#define KPAUSE 2
#define KEY_SEL b

#define WBYTES (moe ? DM * DFF * 2 : 0)
namespace fa {
constexpr unsigned LDS_BYTES = 163840;
constexpr float QSCALE = 0.12751743082f;
struct Args { const u16 *x, *wmod, *wq, *kc, *vc, *wo, *wg, *wu, *wd, *c, *wr; char* ws; u16* out;
};
__global__ __launch_bounds__(256, 1) void fattn_k(const Args p) {
  extern __shared__ __attribute__((aligned(16))) char lds[];
  const unsigned wid = __builtin_amdgcn_readfirstlane(threadIdx.x >> 6);
  const unsigned b = blockIdx.x, idx = b >> 3, kg = b & 3u, m = idx & 7u, hd = 8u * kg + m, y = (b & 4u) + (idx >> 3);
  const unsigned ldsb = (unsigned)(size_t)lds;
  const unsigned m0b = ldsb + 4096u * wid, m0t = ldsb + 131072u + 8192u * wid;
  const __amdgpu_buffer_rsrc_t srdz = __builtin_amdgcn_make_buffer_rsrc((void*)p.ws, (short)0, (int)WS_END, SRD_WORD3);
  const bool moe = b < MOE_WGS;
  const unsigned sl = 4u * ((b & 7u) * 7u + (idx >> 2)) + (idx & 3u), yp2 = (y & 1u) * 2u;
  unsigned hx = (((y >> 2) ^ POL) & 1u) * 8192u;
  unsigned cx = 0, sin = (unsigned)WS_XIN + (((y & 3u) * 32u + hd) * 4u + wid) * BOX + (hx ^ ((~kg & 1u) * 8192u));
  unsigned sfl = (unsigned)WS_OFL + (y & 3u) * 32768u + hx + kg * 128u, spart = (unsigned)WS_PART + (b >> 1) * 16384u + ((((wid >> 1) ^ POL ^ 1u) & 1u) * 8192u) + (b & 1u) * 4096u + (wid & 1u) * 2048u;
  u32x2 xq[8]; unsigned fl, sd0, got, got2; unsigned long long tag; u16* pout;
  f32x2 xf[16]; f32x4 gaf[8], aw[8]; u32x4 alo, ahi; unsigned late; float arow;
#define FQ(i)
#define TQ(i)
  {
    const unsigned tid = threadIdx.x, lane = tid & 63u, l16 = lane & 15u, g = lane >> 4, hi = lane >> 5, r8 = (lane >> 2) & 7u;
    unsigned kb0 = ldsb + 256u * l16 + 16u * (g ^ l16);
    unsigned vvb = ldsb + 65536u + 2048u * hi + 288u * (g & 1u) + 64u * ((lane >> 2) & 3u) + 8u * (lane & 3u);
    unsigned vwb = ldsb + 65536u + 2048u * g + 64u * ((lane >> 2) & 3u) + 16u * (lane & 3u);
    unsigned kd0 = 4096u * wid + 256u * g + 16u * (l16 ^ g);
    unsigned vd0 = 4096u * wid + 256u * r8 + 64u * hi + 16u * ((lane & 3u) ^ (2u * (r8 >> 2))) + 1024u;
    unsigned wd0 = 8192u * (16u * wid + r8) + 64u * hi + 16u * (lane & 3u) + 4096u;
    unsigned hq0 = (unsigned)WS_H + (16u * y + 4u * wid) * 32768u + 16u * lane;
    unsigned oq0 = (unsigned)WS_XIN + (((y & 3u) * 32u + kg * 8u) * 4u + wid) * BOX + (hx ^ ((~kg & 1u) * 8192u)) + 32768u + 16u * lane;
    unsigned flag = ldsb + 49152u + 4u * wid, wbytes = moe ? DM * DFF * 2 : 0;
    const __amdgpu_buffer_rsrc_t srdw = __builtin_amdgcn_make_buffer_rsrc((void*)((const char*)p.wq + 256u * hd - 4096), (short)0, (int)(DM * DQ * 2 - 256u * hd + 4096u), SRD_WORD3);
    __amdgpu_buffer_rsrc_t srdk = __builtin_amdgcn_make_buffer_rsrc((void*)((const char*)p.kc + (size_t)hd * (SEQ * 256)), (short)0, SEQ * 256, SRD_WORD3);
    __amdgpu_buffer_rsrc_t srdv = __builtin_amdgcn_make_buffer_rsrc((void*)((const char*)p.vc + (size_t)hd * (SEQ * 256) - 17408), (short)0, SEQ * 256 + 17408, SRD_WORD3);
    unsigned long long pct; unsigned clen;
    asm volatile("s_getpc_b64 %0\n\ts_mov_b32 %1, adaln_text_end@rel32@lo+4" : "=s"(pct), "=s"(clen));
    const unsigned vct = min(((idx * 4u + wid) * 6u + min(lane, 5u)) * 128u, (clen - 4u) & ~3u);
      const unsigned gr = 128u * (b & 7u) + 4u * (tid >> 3), gc = 64u * (b >> 3) + 8u * (tid & 7u);
      u32x4 w[4], xs[4]; u32x2 ep, cv;
#define XV(i) ({ u32x4 v_; asm volatile("v_accvgpr_read_b32 %0, a[%4]\n\tv_accvgpr_read_b32 %1, a[%4 + 1]\n\tv_accvgpr_read_b32 %2, a[%4 + 2]\n\tv_accvgpr_read_b32 %3, a[%4 + 3]" : "=v"(v_[0]), "=v"(v_[1]), "=v"(v_[2]), "=v"(v_[3]) : "n"(200 + 4 * (i))); v_; })
      asm volatile("global_load_dwordx2 %0, %1, %2" : "=v"(cv) : "v"(gr * 2u), "s"(p.c) : "memory");
      #pragma unroll
      for (int j = 0; j < 4; ++j) asm volatile("global_load_dwordx4 %0, %1, %2 nt" : "=v"(w[j]) : "v"(((gr + j) * NMOD + gc) * 2u), "s"(p.wmod) : "memory");
      #pragma unroll
      for (int q = 0; q < 4; ++q) asm volatile("global_load_dwordx4 %0, %1, %2 offset:%3 nt" : "=v"(xs[q]) : "v"((8u * (y * 32u + hd) + 2u * wid) * 2048u + 16u * lane), "s"(p.x), "n"(1024 * q) : "memory");
      asm volatile("global_load_dwordx2 %0, %1, %2 sc1" : "=v"(ep) : "v"(0u), "s"(p.ws + WS_EP) : "memory");
      asm volatile("buffer_store_dwordx4 %0, %1, %2, 0 offen sc1\n\ts_nop 1" :: "v"(u32x4{0u, 0u, 0u, 0u}), "v"(tid < Z_SHARE / 16u ? (unsigned)WS_Z + b * Z_SHARE + tid * 16u : 0x80000000u), "s"(srdz) : "memory");
      FQ(6);
      asm volatile("global_load_dword a255, %0, %1" :: "v"(vct), "s"(pct) : "memory");
      asm volatile("s_mov_b64 exec, 0\n\ts_cmp_eq_u32 %0, 1\n\ts_cbranch_scc1 adaln_pg5\n\ts_cmp_eq_u32 %0, 2\n\ts_cbranch_scc1 adaln_pg6\n\ts_cmp_eq_u32 %0, 3\n\ts_cbranch_scc1 adaln_pg7\nadaln_fr:\n\ts_mov_b64 exec, -1" :: "s"(wid) : "scc");
      FQ(7);
      unsigned tq0 = (idx * 8u + wid * 2u + ((lane >> 3) & 1u)) * 8192u + (b & 4u) * 256u + (lane & 7u) * 128u + 4096u - 256u * m;
      const unsigned rb0 = 16u * y + 4u * (hd >> 3), ch = 16u * (hd & 7u) + (tid >> 4);
      unsigned vrw = ldsb + 135168u + wid * 256u + 32u * (tid & 7u) + 8u * g, vrr = ldsb + 135168u + 4u * lane;
      unsigned vmo = wid ? 0x80000000u : (unsigned)WS_MOD + b * 512u + lane * 8u, vst = (lane & 31u) ? 0x80000000u : (unsigned)WS_RST + (8u * (y * 32u + hd) + 2u * wid + hi) * 16u;
      unsigned vx2 = (rb0 * 16u + l16) * 2048u + ch * 16u;
      pout = p.out;
      asm volatile("s_waitcnt vmcnt(11)" : "+v"(cv), "+v"(tq0), "+v"(vrw), "+v"(vrr), "+v"(vmo), "+v"(vst), "+v"(vx2), "+s"(pout) :: "memory");
      auto silu2 = [](unsigned v) { const f32x2 f = unpk(v); return cvtpk(f[0] * __builtin_amdgcn_rcpf(1.f + __builtin_amdgcn_exp2f(f[0] * -1.44269504089f)), f[1] * __builtin_amdgcn_rcpf(1.f + __builtin_amdgcn_exp2f(f[1] * -1.44269504089f))); };
      auto dot2 = [](unsigned x, unsigned z, float c) { return __builtin_amdgcn_fdot2_f32_bf16(__builtin_bit_cast(bf16x2, x), __builtin_bit_cast(bf16x2, z), c, false); };
      unsigned s01 = silu2(cv[0]), s23 = silu2(cv[1]);
      asm volatile("s_waitcnt vmcnt(7)" : "+v"(s01), "+v"(s23), "+v"(w[0]), "+v"(w[1]), "+v"(w[2]), "+v"(w[3]) :: "memory");
      FQ(4);
      asm volatile("buffer_load_dword a255, %0, %1, 0 offen" :: "v"(tq0), "s"(srdw) : "memory");
      {
        float acc[8];
        #pragma unroll
        for (int e = 0; e < 4; ++e) {
          acc[2 * e]     = dot2(__builtin_amdgcn_perm(w[3][e], w[2][e], 0x05040100u), s23, dot2(__builtin_amdgcn_perm(w[1][e], w[0][e], 0x05040100u), s01, 0.f));
          acc[2 * e + 1] = dot2(__builtin_amdgcn_perm(w[3][e], w[2][e], 0x07060302u), s23, dot2(__builtin_amdgcn_perm(w[1][e], w[0][e], 0x07060302u), s01, 0.f));
        }
        float t[4], u[2];
        #pragma unroll
        for (int e = 0; e < 4; ++e) t[e] = swap32_add(acc[e], acc[e + 4]);
        #pragma unroll
        for (int e = 0; e < 2; ++e) u[e] = dpp_add<DPP_ROR8>(swap16_add(t[e], t[e + 2]));
        asm volatile("ds_write_b64 %5, %6\n\ts_waitcnt vmcnt(3)" : "+v"(xs[0]), "+v"(xs[1]), "+v"(xs[2]), "+v"(xs[3]), "+v"(ep) : "v"(vrw), "v"(f32x2{u[0], u[1]}) : "memory");
      }
      tag = (((unsigned long long)(unsigned)__builtin_amdgcn_readfirstlane(ep[1]) << 32) | (unsigned)__builtin_amdgcn_readfirstlane(ep[0])) + 1ull;
      const unsigned tl = (unsigned)tag & 0xffu, th = (unsigned)(tag >> 8);
      u32x4 sg;
      {
        float t4[4] = {0.f, 0.f, 0.f, 0.f};
        #pragma unroll
        for (int e = 0; e < 16; ++e) { const unsigned xw = xs[e >> 2][e & 3]; t4[2 * (e >> 3)] = dot2(xw, 0x3f803f80u, t4[2 * (e >> 3)]); t4[2 * (e >> 3) + 1] = dot2(xw, xw, t4[2 * (e >> 3) + 1]); }
        float u = swap16_add(swap32_add(t4[0], t4[2]), swap32_add(t4[1], t4[3]));
        u = dpp_add<DPP_XOR1>(u); u = dpp_add<DPP_XOR2>(u); u = dpp_add<DPP_ROR4>(u); u = dpp_add<DPP_ROR8>(u);
        const auto r16 = __builtin_amdgcn_permlane16_swap(__float_as_uint(u), __float_as_uint(u), false, false);
        const float mean = __uint_as_float(r16[0]) * (1.f / DM), m2 = mean * mean;
        float var = fmaf(__uint_as_float(r16[1]), 1.f / DM, -m2);
        if (__builtin_expect(__any(!(m2 <= var)), 0)) {
          const f32x2 mm = {__uint_as_float(__builtin_amdgcn_readlane(__float_as_uint(mean), 0)), __uint_as_float(__builtin_amdgcn_readlane(__float_as_uint(mean), 32))};
          f32x2 sq = {0.f, 0.f};
          #pragma unroll
          for (int e = 0; e < 8; ++e) {
            const f32x2 d0 = f32x2{lo16(xs[e >> 2][e & 3]), lo16(xs[2 + (e >> 2)][e & 3])} - mm, d1 = f32x2{hi16(xs[e >> 2][e & 3]), hi16(xs[2 + (e >> 2)][e & 3])} - mm;
            sq = __builtin_elementwise_fma(d1, d1, __builtin_elementwise_fma(d0, d0, sq));
          }
          const float v0 = wave_sum(sq[0]), v1 = wave_sum(sq[1]);
          var = (hi ? v1 : v0) * (1.f / DM);
        }
        sg = u32x4{cvtpk(0.f, mean) | tl, th, cvtpk(0.f, __builtin_amdgcn_sqrtf(rbh(rbh(var) + 1e-5f))) | tl, th};
      }
      {
        f32x2 ra, rc;
        asm volatile("s_waitcnt vmcnt(2) lgkmcnt(0)\n\ts_barrier\n\tds_read2st64_b32 %0, %2 offset1:1\n\tds_read2st64_b32 %1, %2 offset0:2 offset1:3\n\t"
                     "buffer_store_dwordx4 %3, %4, %5, 0 offen sc1\n\ts_waitcnt lgkmcnt(0)" : "=&v"(ra), "=&v"(rc) : "v"(vrr), "v"(sg), "v"(vst), "s"(srdz) : "memory");
        ra += rc;
        asm volatile("buffer_store_dwordx2 %0, %1, %2, 0 offen sc1" :: "v"(u32x2{((__float_as_uint(ra[0] + ra[1]) + 0x80u) & 0xffffff00u) | tl, th}), "v"(vmo), "s"(srdz) : "memory");
      }
      FQ(5);
      asm volatile("global_load_dwordx2 a[216:217], %0, %1" :: "v"(tid * 8u), "s"(p.c) : "memory");
      #pragma unroll
      for (int i = 0; i < 4; ++i) asm volatile("global_load_dwordx4 a[%2:%2 + 3], %0, %1" :: "v"(vx2 + i * 32768u), "s"(p.x), "n"(200 + 4 * i) : "memory");
      PAGE(5, "adaln_fr");
      unsigned lane2 = lane; asm volatile("" : "+v"(lane2));
#define lane lane2
    unsigned wo0 = (32u * wid + r8) * 2048u + m * 256u + 64u * hi + 16u * ((lane & 3u) ^ (2u * (r8 >> 2)));
    __amdgpu_buffer_rsrc_t srdt = __builtin_amdgcn_make_buffer_rsrc((void*)((const char*)p.wo + (size_t)kg * (DM * DM * 2)), (short)0, DM * DM * 2, SRD_WORD3);
    __amdgpu_buffer_rsrc_t srdp = __builtin_amdgcn_make_buffer_rsrc((void*)((const char*)(y < 4u ? p.kc : p.vc) + (size_t)hd * (SEQ * 256) + (y & 3u) * 4096u), (short)0, (int)(SEQ * 256 - (y & 3u) * 4096u), SRD_WORD3);
    unsigned vg0 = (256u * wid + (lane >> 1)) * (DFF * 2u) + sl * 32u + 16u * (lane & 1u);
    unsigned vdn = (16u * sl + hi) * 2048u + 512u * wid + 16u * (lane & 31u);
    const unsigned mc0 = 2u * DM + 64u * ((idx >> 2) * 8u + (b & 7u)) + 16u * (idx & 3u);
    const unsigned md0 = (256u * wid + (lane >> 1)) * (NMOD * 2u) + mc0 * 2u + 16u * (lane & 1u);
    sd0 = ldsb + 131072u + 8192u + 512u * wid + 16u * g;
    fl = md0;
    unsigned rd0 = ldsb + 131072u + 7168u + 8192u * ((lane >> 2) & 3u) + 256u * wid + 4u * (lane & 3u);
    unsigned ta0 = m0t + 256u * g + 32u * ((lane >> 2) & 3u) + 8u * (lane & 3u);
    const unsigned c8 = (b * 4u + wid) * 8192u, cd = moe ? sl * 32768u + wid * 8192u : 0u, cw = (idx * 4u + wid) * 16384u;
    const unsigned cm = moe ? ((sl & 3u) * 4u + wid) * 64u * (DFF * 2u) + (sl >> 2) * 128u : 0u;
    unsigned vxs = BOX * (l16 >> 1) + 64u * (l16 & 1u) + 16u * g;
    const unsigned sxs = (unsigned)WS_XIN + ((y & 3u) * 128u + wid * 32u) * BOX + (hx ^ ((~wid & 1u) * 8192u)) + (m >> 1) * 1024u + kg * 256u + (m & 1u) * 128u;
    unsigned vx0 = ((256u * y + 8u * hd + 2u * wid + ((lane >> 2) & 1u)) * DM + 256u * hi + 128u * (l16 >> 3) + 16u * (g & 1u) + 4u * (lane & 3u) + 32u * yp2) * 2u;
    unsigned tlo = 0, thi = 0;
#define tab(ln, lo, hi2) asm("v_writelane_b32 %0, %2, " #ln "\n\tv_writelane_b32 %1, %3, " #ln : "+v"(tlo), "+v"(thi) : "s"(lo), "s"(hi2))
#define tabp(ln, q, off) do { const unsigned long long a_ = (size_t)(q) + (off); tab(ln, (unsigned)a_, (unsigned)(a_ >> 32)); } while (0)
#define TORD 0
    tabp(1, p.x, ((y * 32u + hd) * 2u + (wid & 1u)) * 8192u); tabp(2, p.wu, moe ? c8 : 0u); tabp(3, p.wo, c8); tabp(4, p.wd, cd);
    tabp(5, p.wo, kg * 2097152u + cw); tabp(6, p.wo, kg * 2097152u + cw + 8192u); tabp(7, p.wg, cm);
    tabp(17, p.wmod, 0u); tab(18, sin + 32768u, sfl);
    tab(19, (unsigned)WS_OFL + (y & 3u) * 32768u + (hd >> 4) * 16384u + hx + (hd & 15u) * 512u + wid * 128u, 0u);
    tabp(8, p.wg, 0u); tabp(9, p.wu, 0u); tabp(10, p.wd, 0u);
    tab(11, m, m * 4u * BOX); tab(12, (y & 1u) * 128u, 128u - (y & 1u) * 128u);
    tab(13, b, (unsigned)WS_PFL + (y * 32u + hd) * 512u + kg * 128u); tab(14, sxs, 0u);
    tab(15, (unsigned)WS_MODT + (mc0 - 2u * DM + 4u * wid) * 4u, (mc0 >> 10) == 4u ? 0x3f800000u : 0u);
    tab(16, PPAR && (b & 1u) ? 0x80000000u : (unsigned)WS_PFL + y * 16384u + (hd * 4u + wid) * 4u, (unsigned)WS_PFL + (y * 32u + hd) * 512u + wid * 128u);
    const char* psig = p.ws + WS_CNT + y * 8192u + hd * 8u;
    const char* pcnt = p.ws + WS_CNT + y * 8192u + hd * 256u;
    tabp(20, p.wr, 0u); tab(21, spart, (unsigned)WS_ROWS + (y * 32u + hd) * 8192u + wid * 1024u);
      asm volatile("" : "+v"(tlo), "+v"(thi), "+v"(wo0), "+v"(vg0), "+v"(vdn), "+v"(ta0), "+v"(vxs), "+v"(vx0), "+v"(rd0), "+v"(fl), "+v"(sd0), "+v"(kb0), "+v"(vvb), "+v"(vwb), "+v"(kd0), "+v"(vd0), "+v"(wd0), "+v"(hq0), "+v"(oq0));
      asm volatile("" : "+s"(srdk), "+s"(srdv), "+s"(srdt), "+s"(srdp), "+s"(psig), "+s"(pcnt), "+s"(flag), "+s"(wbytes));
#undef lane
      u32x2 mp[8]; u32x4 rs;
      unsigned vlm = ((wid >> 1) * 16u + 2u * (hd & 7u) + (wid & 1u)) * 4096u + lane * 8u, vlr = (unsigned)WS_RST + ((rb0 + wid) * 16u + l16) * 16u;
#define LOOK9 "buffer_load_dwordx2 %0, %[vlm], %[srdz], %[som] offen sc1\n\tbuffer_load_dwordx2 %1, %[vlm], %[srdz], %[som] offen offset:512 sc1\n\tbuffer_load_dwordx2 %2, %[vlm], %[srdz], %[som] offen offset:1024 sc1\n\t" \
              "buffer_load_dwordx2 %3, %[vlm], %[srdz], %[som] offen offset:1536 sc1\n\tbuffer_load_dwordx2 %4, %[vlm], %[srdz], %[som] offen offset:2048 sc1\n\tbuffer_load_dwordx2 %5, %[vlm], %[srdz], %[som] offen offset:2560 sc1\n\t" \
              "buffer_load_dwordx2 %6, %[vlm], %[srdz], %[som] offen offset:3072 sc1\n\tbuffer_load_dwordx2 %7, %[vlm], %[srdz], %[som] offen offset:3584 sc1\n\tbuffer_load_dwordx4 %8, %[vlr], %[srdz], 0 offen sc1\n\t"
#define LREG(c) c "{v[0:1]}"(mp[0]), c "{v[2:3]}"(mp[1]), c "{v[4:5]}"(mp[2]), c "{v[6:7]}"(mp[3]), c "{v[8:9]}"(mp[4]), c "{v[10:11]}"(mp[5]), c "{v[12:13]}"(mp[6]), c "{v[14:15]}"(mp[7]), c "{v[16:19]}"(rs)
      asm volatile(LOOK9 : LREG("=&") : [vlm] "v"(vlm), [vlr] "v"(vlr), [srdz] "s"(srdz), [som] "s"((unsigned)WS_MOD) : "memory");
      #pragma unroll
      for (int q = 0; q < 14; ++q)
        asm volatile("s_mov_b32 m0, %0\n\ts_nop 0\n\tbuffer_load_dwordx4 %1, %2, %3 offen lds" :: "s"(m0b + 65536u + 16384u * (q >> 2) + 1024u * (q & 3)), "v"(wd0), "s"(srdw),
                     "s"(0x80000u * (q >> 2) + 65536u * ((q >> 1) & 1) + 128u * (q & 1)) : "m0", "memory");
      asm volatile("s_waitcnt vmcnt(23)" ::: "memory");
      { const u32x4 cs = XV(4); *(u32x2*)(lds + 131072 + 8192 + tid * 8u) = u32x2{silu2(cs[0]), silu2(cs[1])}; }
      f32x2 xu[16];
      #pragma unroll
      for (int i = 0; i < 4; ++i) { const u32x4 o = XV(i);
        #pragma unroll
        for (int e = 0; e < 4; ++e) { xu[4 * i + e] = unpk(o[e]); asm volatile("" : "+v"(xu[4 * i + e])); } }
      unsigned vpw = ldsb + 131072u + 4u * tid, vpr = ldsb + 131072u + 1024u + 32u * l16 + 8u * wid, vpc = ldsb + 131072u + 32u * (tid >> 4), vpq = ldsb + 131072u + 1024u + 32u * l16;
      unsigned vh = (unsigned)WS_H + rb0 * 32768u + ch * 256u + 16u * l16; float one = wid >> 1 ? 1.f : 0.f;
      {
        unsigned bl, bh, cnt = SPIN_CAP;
#define XO(d, r, t) "v_bitop3_b32 %[" #d "], v" #r ", %[" #t "], %[" #d "] bitop3:0xbe\n\t"
        asm volatile(
          "s_waitcnt vmcnt(14)\n\t"
          "s_branch Lm1_%=\n"
          "Lm0_%=:\n\t"
          "s_sleep " STR(ASLEEP) "\n\t"
          LOOK9
          "s_waitcnt vmcnt(0)\n"
          "Lm1_%=:\n\t"
          "v_bitop3_b32 %[bl], v0, v2, %[tl] bitop3:0x7e\n\t"
          "v_bitop3_b32 %[bh], v1, v3, %[th] bitop3:0x7e\n\t"
          XO(bl, 4, tl) XO(bh, 5, th) XO(bl, 6, tl) XO(bh, 7, th) XO(bl, 8, tl) XO(bh, 9, th) XO(bl, 10, tl) XO(bh, 11, th) XO(bl, 12, tl) XO(bh, 13, th) XO(bl, 14, tl) XO(bh, 15, th)
          XO(bl, 16, tl) XO(bh, 17, th) XO(bl, 18, tl) XO(bh, 19, th)
          "v_and_or_b32 %[bl], %[bl], %[ff], %[bh]\n\t"
          "v_cmp_ne_u32 vcc, 0, %[bl]\n\t"
          "s_sub_u32 %[cnt], %[cnt], 1\n\t"
          "s_cbranch_scc1 Lm2_%=\n\t"
          "s_cbranch_vccnz Lm0_%=\n"
          "Lm2_%=:"
          : LREG("+"), [bl] "=&v"(bl), [bh] "=&v"(bh), [cnt] "+s"(cnt), "+v"(vpw), "+v"(vpr), "+v"(vpc), "+v"(vpq), "+v"(vh), "+v"(one)
          : [vlm] "v"(vlm), [vlr] "v"(vlr), [srdz] "s"(srdz), [som] "s"((unsigned)WS_MOD), [tl] "s"(tl), [th] "s"(th), [ff] "v"(0xffu) : "vcc", "scc", "memory");
      }
      f32x4 a0, a1, c0, c1, t0, t1;
      {
        auto val = [&](int k) { return __uint_as_float(mp[k][0] & 0xffffff00u); };
        const float v = rbh(one + rbh(((val(0) + val(1)) + (val(2) + val(3))) + ((val(4) + val(5)) + (val(6) + val(7)))));
        const float rd = __builtin_amdgcn_rcpf(hi16(rs[2]));
        asm volatile("ds_write_b32 %0, %1\n\tds_write_b64 %2, %3" :: "v"(vpw), "v"(v * QSCALE), "v"(vpr), "v"(f32x2{-hi16(rs[0]) * rd, rd}) : "memory");
      }
      PAGE(6, "adaln_fr");
      asm volatile("s_waitcnt lgkmcnt(0)\n\ts_barrier\n\tds_read_b128 %4, %7\n\tds_read_b128 %0, %6\n\tds_read_b128 %2, %6 offset:512\n\tds_read_b128 %1, %6 offset:16\n\tds_read_b128 %3, %6 offset:528\n\tds_read_b128 %5, %7 offset:16\n\t"
                   "buffer_load_dword a255, %8, %9, %10 offen\n\ts_waitcnt lgkmcnt(0)"
                   : "=&v"(a0), "=&v"(a1), "=&v"(c0), "=&v"(c1), "=&v"(t0), "=&v"(t1) : "v"(vpc), "v"(vpq), "v"(tq0), "s"(srdw), "s"(0x200000u) : "memory");
      FQ(0);
      #pragma unroll
      for (int i = 0; i < 4; ++i) {
        const float mn = i < 2 ? t0[2 * i] : t1[2 * i - 4], rd = i < 2 ? t0[2 * i + 1] : t1[2 * i - 3];
        u32x4 o;
        #pragma unroll
        for (int e = 0; e < 4; ++e) {
          const f32x2 sc = e < 2 ? f32x2{c0[2 * e], c0[2 * e + 1]} : f32x2{c1[2 * e - 4], c1[2 * e - 3]}, sh = e < 2 ? f32x2{a0[2 * e], a0[2 * e + 1]} : f32x2{a1[2 * e - 4], a1[2 * e - 3]};
          o[e] = cvtpk2(__builtin_elementwise_fma(__builtin_elementwise_fma(xu[4 * i + e], f32x2{rd, rd}, f32x2{mn, mn}), sc, sh));
        }
        asm volatile("buffer_store_dwordx4 %0, %1, %2, %3 offen sc1\n\ts_nop 1" :: "v"(o), "v"(vh), "s"(srdz), "s"(32768u * i) : "memory");
      }
      FQ(1);
    asm volatile(FA_BODY
      : FA_OUTS, [sin] "+s"(sin), [oq0] "+v"(oq0), [tlo] "+v"(tlo), [thi] "+v"(thi), [cx] "+s"(cx)
      : [kb0] "v"(kb0), [vvb] "v"(vvb), [kd0] "v"(kd0), [vd0] "v"(vd0),
        [m0b] "s"(m0b), [flag] "s"(flag), [srdz] "s"(srdz), [srdw] "s"(srdw), [srdk] "s"(srdk), [srdv] "s"(srdv), [m0t] "s"(m0t), [srdt] "s"(srdt), [srdp] "s"(srdp),
        [tag] "s"(tag), [pcnt] "s"(pcnt), [psig] "s"(psig), [wid] "s"(wid), [rd0] "v"(rd0), [wo0] "v"(wo0), [vg0] "v"(vg0), [vx0] "v"(vx0), [px] "s"(p.x), [vct] "v"(vct), [pct] "s"(pct), [vdn] "v"(vdn), [ta0] "v"(ta0), [vxs] "v"(vxs),
        [wbytes] "s"(wbytes)
      : FA_CLOBBERS);
    cx = __builtin_amdgcn_readfirstlane(cx); hx ^= cx; sfl ^= cx; spart ^= cx;
  }
#define RDELAYN 16
#define RDELAY2 4
#define RDELAY3 8
#define RCHKN 5
#define EARLYOUT 1
  unsigned lane; asm volatile("v_mbcnt_lo_u32_b32 %0, -1, 0\n\tv_mbcnt_hi_u32_b32 %0, -1, %0" : "=v"(lane));
  const unsigned tid = wid * 64u + lane, n = lane & 15u, g = lane >> 4;
  const unsigned r0 = 256u * y + 8u * hd + 2u * wid, row = r0 + ((n >> 2) & 1u);
  const unsigned colb = 256u * (g >> 1) + 128u * (n >> 3) + 16u * (g & 1u) + 4u * (n & 3u);
  char* par = lds;
  const unsigned cq0 = colb + 32u * yp2, cq1 = colb + 32u * (2u ^ yp2);
  auto parq = [&](int o, int k) { return *(const u32x2*)(par + o + (k >> 2 ? cq1 : cq0) * 2u + 64 * ((k >> 1) & 1) + 1024 * (k & 1)); };
  auto parf = [&](int o, int k) { return *(const f32x4*)(par + o + (k >> 2 ? cq1 : cq0) * 4u + 128 * ((k >> 1) & 1) + 2048 * (k & 1)); };
  if (__builtin_expect(__any(late != 0u), 0)) {
    u32x4 mt[4]; u32x2 wr;
    asm volatile("s_barrier" ::: "memory");
    for (unsigned spins = 0; spins <= SPIN_CAP; ++spins) {
      asm volatile("s_nop 4\n\tglobal_load_dwordx2 %0, %1, %2" : "=v"(wr) : "v"(tid * 8u), "s"(p.wr) : "memory");
      #pragma unroll
      for (int k = 0; k < 4; ++k) asm volatile("buffer_load_dwordx4 %0, %1, %2, %3 offen sc1" : "=v"(mt[k]) : "v"(tid * 16u), "s"(srdz), "s"((unsigned)WS_MODT + 4096u * k) : "memory");
      asm volatile("s_waitcnt vmcnt(0)" : "+v"(mt[0]), "+v"(mt[1]), "+v"(mt[2]), "+v"(mt[3]), "+v"(wr) :: "memory");
      const u32x4 t = mt[0] & mt[1] & mt[2] & mt[3];
      if (__all((t[0] & t[1] & t[2] & t[3] & 1u) != 0u)) break;
    }
    const f32x4 a = __builtin_bit_cast(f32x4, mt[2]) * f32x4{lo16(wr[0]), hi16(wr[0]), lo16(wr[1]), hi16(wr[1])};
    *(u32x4*)(par + tid * 16u) = mt[0]; *(f32x4*)(par + 4096 + tid * 16u) = a;
    #pragma unroll
    for (int k = 1; k < 4; ++k) *(u32x2*)(par + (k == 3 ? 14336 : 6144 + 2048 * k) + tid * 8u) = u32x2{__builtin_amdgcn_perm(mt[k][1], mt[k][0], 0x07060302u), __builtin_amdgcn_perm(mt[k][3], mt[k][2], 0x07060302u)};
    *(float*)(par + 12424 + 16u * wid) = wave_sum((a[0] + a[1]) + (a[2] + a[3]));
    asm volatile("s_waitcnt lgkmcnt(0)\n\ts_barrier" ::: "memory");
    #pragma unroll
    for (int k = 0; k < 8; ++k) { gaf[k] = parf(0, k); aw[k] = parf(4096, k); }
    arow = dpp_add<DPP_XOR2>(dpp_add<DPP_XOR1>(*(const float*)(par + 12424 + 16u * (lane & 3u))));
  }
  unsigned vin = lane * 16u, vin3 = lane * 16u + 20480u, vin2 = lane * 16u + 16384u, kp = (unsigned)WS_KEYS + (b & 3u) * RSTR + hx + (lane >> 2) * KSTR + 32u * (lane & 3u);
  TQ(0);
  f32x2 x1[16], s2, d2, q2, sbf[16], gbf[16], c1f[16];
  float mrow, rdrow, drow;
  unsigned vkey, vkb, vkr, nrow, vpub, ta, tu, tb; char* pk;
  {
#define H2(v, h) f32x2{v[2 * h], v[2 * h + 1]}
#define PART(R, c) do { f32x4 t0, t1; \
      asm volatile("v_mfma_f32_16x16x32_bf16 %0, %2, a[" #R ":" #R "+3], 0\n\tv_mfma_f32_16x16x32_bf16 %1, %2, a[" #R "+8:" #R "+11], 0\n\t" \
                   "v_mfma_f32_16x16x32_bf16 %0, %3, a[" #R "+4:" #R "+7], %0\n\tv_mfma_f32_16x16x32_bf16 %1, %3, a[" #R "+12:" #R "+15], %1\n\ts_nop 6" : "=&v"(t0), "=&v"(t1) : "v"(alo), "v"(ahi)); \
      f32x2 u0 = H2(t0, 0), u1 = H2(t0, 1), u2 = H2(t1, 0), u3 = H2(t1, 1); \
      asm volatile("v_pk_fma_f32 %0, %0, %4, %8\n\tv_pk_fma_f32 %1, %1, %5, %9\n\tv_pk_fma_f32 %2, %2, %6, %10\n\tv_pk_fma_f32 %3, %3, %7, %11" : "+v"(u0), "+v"(u1), "+v"(u2), "+v"(u3) \
                   : "v"(H2(gaf[2 * c], 0)), "v"(H2(gaf[2 * c], 1)), "v"(H2(gaf[2 * c + 1], 0)), "v"(H2(gaf[2 * c + 1], 1)), "v"(xf[4 * c]), "v"(xf[4 * c + 1]), "v"(xf[4 * c + 2]), "v"(xf[4 * c + 3])); \
      x1[4 * c] = rb2(u0); x1[4 * c + 1] = rb2(u1); x1[4 * c + 2] = rb2(u2); x1[4 * c + 3] = rb2(u3); } while (0)
#define AC3(k) "v_pk_add_f32 %0, %0, %[y" #k "]\n\tv_pk_fma_f32 %1, %[y" #k "], %[w" #k "], %1\n\tv_pk_fma_f32 %2, %[y" #k "], %[y" #k "], %2\n\t"
    auto part_ = [&](int c2, f32x2& sa, f32x2& da, f32x2& qa) __attribute__((always_inline)) {
      if (c2 == 0) PART(64, 0); else if (c2 == 1) PART(80, 1); else if (c2 == 2) PART(96, 2); else PART(112, 3);
      asm volatile(AC3(0) AC3(1) AC3(2) AC3(3) : "+v"(sa), "+v"(da), "+v"(qa) : [y0] "v"(x1[4 * c2]), [y1] "v"(x1[4 * c2 + 1]), [y2] "v"(x1[4 * c2 + 2]), [y3] "v"(x1[4 * c2 + 3]),
                   [w0] "v"(H2(aw[2 * c2], 0)), [w1] "v"(H2(aw[2 * c2], 1)), [w2] "v"(H2(aw[2 * c2 + 1], 0)), [w3] "v"(H2(aw[2 * c2 + 1], 1)));
    };
#define RCV4(o, a, b, c, d) \
      "buffer_load_dwordx4 " #a ", %[vin" #o "], %[srdz], %[sin] offen sc1\n\t" \
      "buffer_load_dwordx4 " #b ", %[vin" #o "], %[srdz], %[sin] offen offset:1024 sc1\n\t" \
      "buffer_load_dwordx4 " #c ", %[vin" #o "], %[srdz], %[sin] offen offset:2048 sc1\n\t" \
      "buffer_load_dwordx4 " #d ", %[vin" #o "], %[srdz], %[sin] offen offset:3072 sc1\n\t"
#define RWAIT(lv, w) \
      "Lrq" #lv "_%=:\n\t" \
      "v_mov_b32 " w ", 0\n\t" \
      "s_mov_b32 %[sc], " STR(RCHKN) "\n\t" \
      LOOK2 \
      "Lrs" #lv "_%=:\n\t" \
      "ds_read_b32 %[tmp], %[vsh]\n\t" \
      "s_waitcnt lgkmcnt(0)\n\t" \
      "v_and_or_b32 %[tmp], %[tmp], " #lv ", " w "\n\t" \
      "v_cmp_eq_u32 vcc, 0, %[tmp]\n\t" \
      "s_cbranch_vccz Lrd" #lv "_%=\n\t" \
      "s_sub_u32 %[sc], %[sc], 1\n\t" \
      "s_cbranch_scc0 Lrs" #lv "_%=\n\t" \
      "s_sub_u32 %[cnt], %[cnt], 1\n\t" \
      "s_cselect_b32 %[cnt], 0, %[cnt]\n\t" \
      "s_waitcnt vmcnt(0)\n\t" \
      "v_cmp_eq_u32 vcc, 0, " w "\n\t" \
      "s_cbranch_scc1 Lrl" #lv "_%=\n\t" \
      "s_cbranch_vccnz Lrq" #lv "_%=\n" \
      "Lrd" #lv "_%=:\n\t" \
      "v_mov_b32 %[tmp], " #lv "\n\t" \
      "s_mov_b64 exec, 1\n\t" \
      "ds_or_b32 %[vsh], %[tmp]\n\t" \
      "s_mov_b64 exec, -1\n" \
      "Lrl" #lv "_%=:\n\t"
#define RCV8A RCV4(0, a[64:67], a[68:71], a[72:75], a[76:79]) RCV4(1, a[80:83], a[84:87], a[88:91], a[92:95])
#define RCV4C RCV4(2, a[96:99], a[100:103], a[104:107], a[108:111])
#define RCV4D RCV4(3, a[112:115], a[116:119], a[120:123], a[124:127])
#define RCV8B RCV4C RCV4D
#define LOOK2 "buffer_load_dwordx2 v[228:229], %[vfl], %[srdz], %[sfl] offen offset:64 sc1\n\t"
    unsigned cnt, tmp, sc;
    u32x2 sbq[8], gbq[8], c1q[8];
    #pragma unroll
    for (int k = 0; k < 8; ++k) { sbq[k] = parq(8192, k); c1q[k] = parq(10240, k); gbq[k] = parq(14336, k); }
    #pragma unroll
    for (int i = 0; i < 16; ++i) { sbf[i] = unpk(sbq[i >> 1][i & 1]); c1f[i] = unpk(c1q[i >> 1][i & 1]); asm volatile("" : "+v"(sbf[i]), "+v"(c1f[i])); }
    unsigned vfl = (lane & 15u) * 512u + (lane & 16u) * 1024u, vin1 = lane * 16u + 4096u;
    PAGE(1, "adaln_pg2");
    asm volatile(
      "s_mov_b32 %[go], 0\n\t"
      "s_cmp_lg_u32 %[got], 0\n\t"
      "s_waitcnt vmcnt(4)\n\t"
      "s_cbranch_scc1 Lrf_%=\n\t"
      "v_cmp_eq_u32 vcc, 0, v229\n\t"
      "s_cbranch_vccnz Lrk_%=\n\t"
      "s_mov_b32 %[got], 2\n\t"
      RCV8A
      "Lrf_%=:\n\t"
      "v_cmp_eq_u32 vcc, 0, v228\n\t"
      "s_cbranch_vccnz Lrk_%=\n\t"
      "s_mov_b32 %[go], 1\n\t"
      RCV4C
      "s_branch Lrn_%=\n"
      "Lrk_%=:\n\t"
      LOOK2
      "Lrn_%=:"
      : "+{v228}"(sd0), "+{v229}"(fl), [got] "+s"(got), [go] "=&s"(got2), [vfl] "+v"(vfl), [vin1] "+v"(vin1), "+v"(gbq[0]), "+v"(gbq[1]), "+v"(gbq[2]), "+v"(gbq[3]), "+v"(gbq[4]), "+v"(gbq[5]), "+v"(gbq[6]), "+v"(gbq[7])
      : [srdz] "s"(srdz), [sfl] "s"(sfl), [sin] "s"(sin), [vin0] "v"(vin), [vin2] "v"(vin2) : "vcc", "scc", "memory");
#define NRE
    #pragma unroll
    for (int i = 0; i < 16; ++i) { gbf[i] = unpk(gbq[i >> 1][i & 1]); asm volatile("" : "+v"(gbf[i])); }
    vkey = lane * RSTR + (b >> 4) * KSTR + (b & 15u) * 8u; vkb = ldsb + 12288u + 8u * wid + (lane & 4u); vkr = ldsb + 12288u + 4u * (lane & 7u); nrow = 2047u - row;
    vpub = (unsigned)WS_ROWS + (y * 32u + hd) * 8192u + (g >> 1) * 512u + ((n >> 3) * 8u + (g & 1u) * 4u + (n & 3u)) * 16u;
    ta = m0b + 65536u + 8u * lane; tu = m0t + 8u * lane; tb = m0b + 98304u + 16384u * (g >> 1) + 2048u * (g & 1u) + 512u * ((lane >> 2) & 3u) + 8u * (lane & 3u);
    pk = p.ws + WS_KEYS;
    s2 = f32x2{0.f, 0.f}; d2 = s2; q2 = s2;
    f32x2 s3, d3, q3;
    asm volatile(
      "s_mov_b32 %[cnt], 0x10000\n\t"
      "s_cmp_lg_u32 %[go], 0\n\t"
      "s_cbranch_scc1 Lro_%=\n\t"
      "s_cmp_lg_u32 %[got], 0\n\t"
      "s_waitcnt vmcnt(0)\n\t"
      "s_cbranch_scc1 Lrf_%=\n\t"
      "v_cmp_eq_u32 vcc, 0, v229\n\t"
      "s_cbranch_vccz Lrl2_%=\n\t"
      RWAIT(2, "v229")
      RCV8A
      "Lrf_%=:\n\t"
      "v_cmp_eq_u32 vcc, 0, v228\n\t"
      "s_cbranch_vccz Lrm_%=\n\t"
      RWAIT(4, "v228")
      RCV4C
      "s_sleep " STR(RDELAYN) "\n\t"
      "s_branch Lrp_%=\n"
      "Lrm_%=:\n\t"
      RCV4C
      "s_sleep " STR(RDELAY3) "\n\t"
      "s_branch Lrp_%=\n"
      "Lro_%=:\n\t"
      "s_sleep " STR(RDELAY2) "\n"
      "Lrp_%=:\n\t"
      RCV4D
      "s_cmp_eq_u32 %[got], 1\n\t"
      "s_cbranch_scc1 Lrz_%=\n\t"
      "s_waitcnt vmcnt(8)\n"
      "Lrz_%=:"
      : "+{v228}"(sd0), "+{v229}"(fl), [cnt] "=&s"(cnt), [tmp] "=&v"(tmp), [sc] "=&s"(sc), "+v"(s2),
        "+v"(vkey), "+v"(vkb), "+v"(vkr), "+v"(nrow), "+v"(vpub), "+v"(ta), "+v"(tu), "+v"(tb), "+s"(pk)
      : [srdz] "s"(srdz), [got] "s"(got), [go] "s"(got2), [vfl] "v"(vfl), [sfl] "s"(sfl), [sin] "s"(sin), [vin0] "v"(vin), [vin1] "v"(vin1), [vin2] "v"(vin2), [vin3] "v"(vin3), [vsh] "v"(ldsb + 12420u)
      : "vcc", "scc", "memory");
    part_(0, s2, d2, q2); part_(1, s2, d2, q2);
    TQ(2);
    s3 = s2; d3 = d2; q3 = q2;
    asm volatile("s_waitcnt vmcnt(4)" : "+{v228}"(sd0), "+{v229}"(fl), "+v"(s2), "+v"(d2), "+v"(q2), "+v"(s3), "+v"(d3), "+v"(q3) :: "memory");
    part_(2, s3, d3, q3);
    asm volatile("s_waitcnt vmcnt(0)" : "+v"(s3), "+v"(d3), "+v"(q3) :: "memory");
    TQ(3);
    part_(3, s3, d3, q3);
    if (__builtin_expect(__any(s3[0] + s3[1] != s3[0] + s3[1]), 0)) {
      unsigned spins = 0;
      do {
        asm volatile(RCV8B "s_waitcnt vmcnt(0)" :: [srdz] "s"(srdz), [sin] "s"(sin), [vin2] "v"(vin2), [vin3] "v"(vin3) : "memory"); NRE;
        s3 = s2; d3 = d2; q3 = q2;
        part_(2, s3, d3, q3); part_(3, s3, d3, q3);
      } while (__any(s3[0] + s3[1] != s3[0] + s3[1]) && ++spins < RETRY_CAP);
    }
    asm volatile("; all in");
    const float sv = s3[0] + s3[1], dv = d3[0] + d3[1], qv = q3[0] + q3[1];
    float u = swap16_add(swap32_add(sv, dv), swap32_add(qv, qv));
    u = dpp_add<DPP_XOR1>(u); u = dpp_add<DPP_XOR2>(u); u = dpp_add<DPP_ROR8>(u);
    const auto r16 = __builtin_amdgcn_permlane16_swap(__float_as_uint(u), __float_as_uint(u), false, false);
    const auto r32 = __builtin_amdgcn_permlane32_swap(r16[0], r16[0], false, false);
    const float mean = __uint_as_float(r32[0]) * (1.f / DM), m2v = mean * mean;
    drow = __uint_as_float(r32[1]);
    float var = fmaf(__uint_as_float(r16[1]), 1.f / DM, -m2v);
    if (__builtin_expect(__any(!(m2v <= var)), 0)) {
      f32x2 qq = {0.f, 0.f};
      #pragma unroll
      for (int e = 0; e < 16; ++e) { const f32x2 t = x1[e] - mean; qq = __builtin_elementwise_fma(t, t, qq); }
      var = row_sum(qq[0] + qq[1]) * (1.f / DM);
    }
    mrow = rbh(mean); rdrow = __builtin_amdgcn_rcpf(rbh(__builtin_amdgcn_sqrtf(rbh(rbh(var) + 1e-5f))));
  }
  unsigned* kb = (unsigned*)(lds + 12288);
  unsigned mykey, k1w;
  { const unsigned bb = __float_as_uint(fmaf(-mrow, arow, drow) * rdrow); mykey = max((bb ^ ((unsigned)((int)bb >> 31) | 0x80000000u)) & 0xfffff800u, 0x800u) | nrow; }
  asm volatile("ds_write_b32 %1, %2\n\ts_waitcnt lgkmcnt(0)\n\ts_barrier\n\tds_read_b32 %0, %3\n\ts_waitcnt lgkmcnt(0)" : "=&v"(k1w) : "v"(vkb), "v"(mykey), "v"(vkr) : "memory");
  bool cand;
  u32x4 ka, kc;
#define KOP "global_atomic_or_x2"
#define KSC " sc1"
  {
    const unsigned o1 = __builtin_amdgcn_update_dpp(0u, k1w, DPP_XOR1, 0xf, 0xf, true);
    unsigned k1 = max(k1w, o1), k2 = min(k1w, o1);
    top2_dpp<DPP_XOR2>(k1, k2); top2_dpp<0x141>(k1, k2);
    asm volatile("s_cmp_lg_u32 %[wid], 0\n\ts_cbranch_scc1 Lka_%=\n\ts_mov_b64 exec, %[mk]\n\t" KOP " %[va], %[kk], %[pk]" KSC "\n\ts_mov_b64 exec, -1\nLka_%=:"
                 :: [wid] "s"(wid), [mk] "n"((1u << KEY_REP) - 1u), [va] "v"(vkey), [kk] "v"(u32x2{k2, k1}), [pk] "s"(pk) : "scc", "memory");
    cand = mykey >= k2;
    vpub += mykey == k1 ? 0u : 2048u;
  }
  TQ(4);
  PAGE(2, "adaln_pg3");
#define TRA(r, a, o) asm volatile("ds_read_b64_tr_b16 a[%0:%0 + 1], %1 offset:%2" :: "n"(r), "v"(a), "n"(o) : "memory")
#define FRAGS(k) do { \
      TRA(128 + 8 * (k), ta, 2048 * ((k) >> 2) + 512 * ((k) & 1) + 16384 * (((k) >> 1) & 1)); TRA(130 + 8 * (k), ta, 2048 * ((k) >> 2) + 512 * ((k) & 1) + 16384 * (((k) >> 1) & 1) + 1024); \
      TRA(132 + 8 * (k), tu, 2048 * ((k) >> 2) + 512 * ((k) & 1) + 4096 * (((k) >> 1) & 1)); TRA(134 + 8 * (k), tu, 2048 * ((k) >> 2) + 512 * ((k) & 1) + 4096 * (((k) >> 1) & 1) + 1024); \
      TRA(192 + 4 * (k), tb, 64 * (k)); TRA(194 + 4 * (k), tb, 64 * (k) + 32); } while (0)
  f32x2 h2[16];
  {
    asm volatile("" : "+v"(mrow));
    const float nm = -mrow * rdrow;
    #pragma unroll
    for (int k = 0; k < 8; ++k) {
      #pragma unroll
      for (int e = 0; e < 2; ++e) { const int i = 2 * k + e; h2[i] = __builtin_elementwise_fma(__builtin_elementwise_fma(x1[i], f32x2{rdrow, rdrow}, f32x2{nm, nm}), c1f[i], sbf[i]); }
      if (k & 1) { asm volatile("" : "+v"(h2[2 * k]), "+v"(h2[2 * k + 1])); FRAGS(k >> 1); }
    }
  }
  const unsigned cw = __builtin_amdgcn_readfirstlane((unsigned)__ballot(cand) & 0x11u);
  auto publish = [&](const f32x2* v, unsigned off) __attribute__((always_inline)) {
    #pragma unroll
    for (int k = 0; k < 4; ++k) { const int q = 4 * (k >> 1) + (k & 1);
      asm volatile("buffer_store_dwordx4 %0, %1, %2, %3 offen sc1\n\ts_nop 1" :: "v"(u32x4{cvtpk2(v[2 * q]), cvtpk2(v[2 * q + 1]), cvtpk2(v[2 * q + 4]), cvtpk2(v[2 * q + 5])}), "v"(vpub), "s"(srdz), "s"(off + (k & 1) * 1024u + (((k >> 1) * 2u) ^ yp2) * 128u) : "memory"); }
  };
  if (cand) publish(h2, 0u);
  char* st = lds + 32768 + 4096u * wid;
  auto look1 = [&]() __attribute__((always_inline)) {
    asm volatile("s_cmp_lg_u32 %2, 0\n\ts_cbranch_scc1 Lkl_%=\n\tbuffer_load_dwordx4 %0, %3, %4, 0 offen sc1\n\tbuffer_load_dwordx4 %1, %3, %4, 0 offen offset:16 sc1\nLkl_%=:" : "=&{v[240:243]}"(ka), "=&{v[244:247]}"(kc) : "s"(wid), "v"(kp), "s"(srdz) : "scc", "memory");
  };
  if (KLOOK == 0) look1();
  #pragma unroll
  for (int k = 0; k < 8; ++k) {
    u32x2 yq;
    #pragma unroll
    for (int e = 0; e < 2; ++e) yq[e] = cvtpk2(__builtin_elementwise_fma(gbf[2 * k + e], h2[2 * k + e], x1[2 * k + e]));
    *(u32x2*)(st + (row & 1u) * 2048u + (colb + 32u * ((k >> 2) * 2u ^ yp2)) * 2u + 64 * ((k >> 1) & 1) + 1024 * (k & 1)) = yq;
    if (k & 1) FRAGS(4 + (k >> 1));
    if (k == KLOOK - 1) look1();
  }
  auto out_rows = [&](unsigned which) __attribute__((always_inline)) {
    #pragma unroll
    for (int r = 0; r < 2; ++r) {
      if ((which >> (4 * r)) & 1u) {
        const u32x4 a = *(const u32x4*)(st + 2048 * r + 16u * lane), c = *(const u32x4*)(st + 2048 * r + 1024 + 16u * lane);
        asm volatile("s_nop 4\n\tglobal_store_dwordx4 %0, %1, %3 offset:%4 sc1\n\tglobal_store_dwordx4 %0, %2, %3 offset:%5 sc1\n\ts_nop 1" :: "v"(r0 * 2048u + 16u * lane), "v"(a), "v"(c), "s"(pout), "n"(2048 * r), "n"(2048 * r + 1024) : "memory");
      }
    }
  };
  const unsigned un = tid & 127u, ca = 256u * (un >> 5) + 64u * ((un >> 4) & 1u) + 128u * ((un >> 3) & 1u) + 16u * ((un >> 2) & 1u) + 4u * (un & 3u);
  unsigned vrow = (unsigned)WS_ROWS + (moe ? wid * 512u + 16u * g : un * 16u), vsel = ldsb + 12288u + 40u + 4u * (moe ? lane & 1u : wid >> 1);
  unsigned mk[8];
  #pragma unroll
  for (int t = 0; t < 8; ++t) { mk[t] = (unsigned)__builtin_amdgcn_sbfe((int)(1u << (n >> 1)), t, 1); asm volatile("" : "+v"(mk[t])); }
  unsigned vred = ldsb + 24576u + wid * 256u + (lane & 1u) * 64u + 16u * g, vrf = ldsb + 24576u + ((lane & 1u) * 4u + g) * 16u + ((lane >> 1) & 3u) * 4u, vpt = spart + lane * 16u, vkk = ldsb + 12288u + 32u, vkw = vkk + (lane < 48u ? 64u : 0u);
  unsigned long long m2 = 0x0003000300030003ull;
  asm volatile("" : "+v"(vrow), "+v"(vsel), "+v"(vred), "+v"(vrf), "+v"(vpt), "+v"(vkk), "+v"(vkw), "+s"(m2));
  const unsigned later = __builtin_amdgcn_readfirstlane(wid && EARLYOUT ? cw : 0x11u);
  out_rows(~later);
  TQ(1);
  PAGE(3, "adaln_pg8");
  if (wid == 0) {
    unsigned cnt = SPIN_CAP, t;
    asm volatile(
      "s_waitcnt vmcnt(0)\n"
      "Lk0_%=:\n\t"
      "v_min3_u32 %[t], v240, v242, v244\n\t"
      "v_min_u32 %[t], %[t], v246\n\t"
      "v_cmp_eq_u32 vcc, 0, %[t]\n\t"
      "s_cbranch_vccz Lk1_%=\n\t"
      "s_sleep " STR(KSLEEP) "\n\t"
      "buffer_load_dwordx4 v[240:243], %[kp], %[srdz], 0 offen sc1\n\t"
      "buffer_load_dwordx4 v[244:247], %[kp], %[srdz], 0 offen offset:16 sc1\n\t"
      "s_sub_u32 %[cnt], %[cnt], 1\n\t"
      "s_waitcnt vmcnt(0)\n\t"
      "s_cbranch_scc0 Lk0_%=\n"
      "Lk1_%=:"
      : "+{v[240:243]}"(ka), "+{v[244:247]}"(kc), [t] "=&v"(t), [cnt] "+s"(cnt) : [kp] "v"(kp), [srdz] "s"(srdz) : "vcc", "scc", "memory");
    unsigned k1 = ka[1], k2 = ka[0];
    unsigned l1 = kc[1], l2 = kc[0];
    top2_merge(k1, k2, ka[3], ka[2]); top2_merge(l1, l2, kc[3], kc[2]); top2_merge(k1, k2, l1, l2);
    top2_dpp<DPP_XOR1>(k1, k2); top2_dpp<DPP_XOR2>(k1, k2); top2_dpp<0x141>(k1, k2); top2_dpp<0x140>(k1, k2);
    top2_merge(k1, k2, __builtin_amdgcn_update_dpp(0u, k1, 0x142, 0xa, 0xf, false), __builtin_amdgcn_update_dpp(0u, k2, 0x142, 0xa, 0xf, false));
    top2_merge(k1, k2, __builtin_amdgcn_update_dpp(0u, k1, 0x143, 0xc, 0xf, false), __builtin_amdgcn_update_dpp(0u, k2, 0x143, 0xc, 0xf, false));
    k1 = ~k1 & 2047u; k2 = ~k2 & 2047u;
    *(LAS u32x4*)vkw = u32x4{k1, k2, (k1 >> 3) * 8192u, (k2 >> 3) * 8192u + ((k1 ^ k2) < 8u ? 2048u : 0u)};
  }
  u32x2 kk; unsigned vo;
  asm volatile("s_waitcnt lgkmcnt(0)\n\ts_barrier\n\tds_read_b32 %0, %3\n\tds_read_b64 %1, %4\n\ts_waitcnt lgkmcnt(0)\n\tv_add_u32 %2, %2, %0" : "=&v"(vo), "=&v"(kk), "+v"(vrow) : "v"(vsel), "v"(vkk) : "memory");
  TQ(5);
  unsigned k1, k2, rest;
  auto routed = [&]() __attribute__((always_inline)) {
    asm volatile("" : "+v"(kk));
    k1 = __builtin_amdgcn_readfirstlane(kk[0]); k2 = __builtin_amdgcn_readfirstlane(kk[1]);
    rest = __builtin_amdgcn_readfirstlane(later & ((r0 != k1 && r0 != k2 ? 1u : 0u) | (r0 + 1u != k1 && r0 + 1u != k2 ? 16u : 0u)));
    if (__builtin_expect(rest != later, 0)) { if (row == k1 || row == k2) publish(x1, 4096u); }
  };
  if (__builtin_expect(!moe, 0)) {
    asm volatile("adaln_pg8:\n\ts_cbranch_execz adaln_pg4");
    PAGE(4, "adaln_kr");
    u32x4 ha; u32x2 w0, w1;
    asm volatile("s_nop 4\n\tglobal_load_dwordx2 %0, %2, %3\n\tglobal_load_dwordx2 %1, %2, %3 offset:64" : "=&v"(w0), "=&v"(w1) : "v"(ca * 2u), "s"(p.wr) : "memory");
    asm volatile("buffer_load_dwordx4 %0, %3, %4, 0 offen sc1\n\ts_waitcnt vmcnt(0)" : "=&v"(ha), "+v"(w0), "+v"(w1) : "v"(vrow), "s"(srdz) : "memory");
    for (unsigned spins = 0; __builtin_expect(__any(max(max(ha[0], ha[1]), max(ha[2], ha[3])) == 0xffffffffu), 0) && spins < RETRY_CAP; ++spins)
      asm volatile("buffer_load_dwordx4 %0, %1, %2, 0 offen sc1\n\ts_waitcnt vmcnt(0)" : "=&v"(ha) : "v"(vrow), "s"(srdz) : "memory");
    routed();
    if (b == 255u && tid == 0u) __hip_atomic_store((gu64*)(p.ws + WS_EP), tag, RLX_AGENT);
    char* rows = lds + 16384 + (tid >> 7) * 2048u + ca * 2u;
    *(u32x2*)rows = u32x2{ha[0], ha[1]}; *(u32x2*)(rows + 64) = u32x2{ha[2], ha[3]};
    {
      float dot = 0.f;
      #pragma unroll
      for (int e = 0; e < 4; ++e) { const unsigned w = e < 2 ? w0[e] : w1[e - 2]; dot = fmaf(hi16(ha[e]), hi16(w), fmaf(lo16(ha[e]), lo16(w), dot)); }
      dot = wave_sum(dot);
      if (lane == 0) kb[16u + wid] = __float_as_uint(dot);
    }
    asm volatile("s_waitcnt lgkmcnt(0)\n\ts_barrier" ::: "memory");
    TQ(6);
    const unsigned zj = (b & 4u) * 4u + (idx & 3u) * 4u + (b & 3u), pp = zj * 64u + lane, sq = pp & 1u, nn = 128u * (pp >> 8) + 16u * (2u * ((pp >> 6) & 3u) + ((pp >> 1) & 1u)) + ((pp >> 2) & 15u);
    float gbv = bf2f(*(const u16*)(par + 14336 + nn * 2u)), hv = bf2f(*(const u16*)(lds + 16384 + sq * 2048u + nn * 2u));
    float rsel = rbf(__builtin_amdgcn_rcpf(1.f + __builtin_amdgcn_exp2f(rbf(__uint_as_float(kb[16u + 2u * sq]) + __uint_as_float(kb[17u + 2u * sq])) * -1.44269504089f)));
    u16* vout = pout + (sq ? k2 : k1) * DM + nn;
    unsigned vp = (unsigned)WS_PART + (tid >> 5) * 16384u + (hx ^ 8192u) + ((tid >> 4) & 1u) * 4096u + (zj & 15u) * 256u + (tid & 15u) * 16u;
    unsigned vw = ldsb + 24576u + wid * 256u + (lane & 15u) * 16u + 4u * g, vr = ldsb + 24576u + 4u * lane;
    unsigned vx = (unsigned)WS_ROWS + 4096u + kb[10u + sq] + (nn >> 9) * 1024u + (32u * ((nn >> 8) & 1u) + 16u * ((nn >> 6) & 1u) + 8u * ((nn >> 7) & 1u) + 4u * ((nn >> 4) & 1u) + ((nn >> 2) & 3u)) * 16u + ((nn >> 5) & 1u) * 8u + (nn & 3u) * 2u;
    asm volatile("" : "+v"(gbv), "+v"(hv), "+v"(rsel), "+v"(vout), "+v"(vp), "+v"(vw), "+v"(vr), "+v"(vx));
    out_rows(rest);
    TQ(7);
    __builtin_amdgcn_s_sleep(FSLEEP);
    float u; unsigned xw;
    for (unsigned spins = 0;; ++spins) {
      f32x4 v[14];
      asm volatile("buffer_load_ushort %0, %1, %2, 0 offen sc1" : "=v"(xw) : "v"(vx), "s"(srdz) : "memory");
      #pragma unroll
      for (int k = 0; k < 14; ++k) asm volatile("buffer_load_dwordx4 %0, %1, %2, %3 offen sc1" : "=v"(v[k]) : "v"(vp), "s"(srdz), "s"(131072u * k) : "memory");
      asm volatile("s_waitcnt vmcnt(12)" : "+v"(v[0]), "+v"(v[1]), "+v"(xw) :: "memory");
      f32x4 sum = v[0] + v[1];
#define NEXT2(k) asm volatile("s_waitcnt vmcnt(%3)" : "+v"(v[k]), "+v"(v[k + 1]), "+v"(sum) : "n"(12 - k) : "memory"); sum += v[k]; sum += v[k + 1]
      NEXT2(2); NEXT2(4); NEXT2(6); NEXT2(8); NEXT2(10); NEXT2(12);
      u = swap16_add(swap32_add(sum[0], sum[2]), swap32_add(sum[1], sum[3]));
      if (!__any(u != u || xw == 0xffffu) || spins > RETRY_CAP) break;
    }
    TQ(1);
    f32x2 ra, rc;
    asm volatile("ds_write_b32 %0, %1\n\ts_waitcnt lgkmcnt(0)\n\ts_barrier" :: "v"(vw), "v"(u) : "memory");
    if (wid == 0) {
      asm volatile("ds_read2st64_b32 %0, %2 offset1:1\n\tds_read2st64_b32 %1, %2 offset0:2 offset1:3\n\ts_waitcnt lgkmcnt(0)" : "=&v"(ra), "=&v"(rc) : "v"(vr) : "memory");
      ra += rc;
      const unsigned ov = cvtpk(fmaf(gbv, fmaf(ra[0] + ra[1], rsel, hv), lo16(xw)), 0.f);
      asm volatile("global_store_short %0, %1, off sc1" :: "v"(vout), "v"(ov) : "memory");
    }
  } else {
    f32x4 dg, du;
    bf16x8 hb[8];
    auto fetch = [&]() __attribute__((always_inline)) {
      asm volatile("s_mov_b64 exec, %8\n\t"
                   "buffer_load_dwordx4 %0, %9, %10, 0 offen sc1\n\tbuffer_load_dwordx4 %1, %9, %10, 0 offen offset:64 sc1\n\tbuffer_load_dwordx4 %2, %9, %10, 0 offen offset:128 sc1\n\tbuffer_load_dwordx4 %3, %9, %10, 0 offen offset:192 sc1\n\t"
                   "buffer_load_dwordx4 %4, %9, %10, 0 offen offset:256 sc1\n\tbuffer_load_dwordx4 %5, %9, %10, 0 offen offset:320 sc1\n\tbuffer_load_dwordx4 %6, %9, %10, 0 offen offset:384 sc1\n\tbuffer_load_dwordx4 %7, %9, %10, 0 offen offset:448 sc1\n\t"
                   "s_mov_b64 exec, -1"
                   : "=&v"(hb[0]), "=&v"(hb[1]), "=&v"(hb[2]), "=&v"(hb[3]), "=&v"(hb[4]), "=&v"(hb[5]), "=&v"(hb[6]), "=&v"(hb[7]) : "s"(m2), "v"(vrow), "s"(srdz) : "memory");
    };
#define MM2(a, h, c0, c1) "v_mfma_f32_16x16x32_bf16 %0, a[" #a ":" #a "+3], %" #h ", " c0 "\n\tv_mfma_f32_16x16x32_bf16 %1, a[" #a "+4:" #a "+7], %" #h ", " c1 "\n\t"
#define MM16 MM2(128, 2, "0", "0") MM2(136, 3, "%0", "%1") MM2(144, 4, "%0", "%1") MM2(152, 5, "%0", "%1") MM2(160, 6, "%0", "%1") MM2(168, 7, "%0", "%1") MM2(176, 8, "%0", "%1") MM2(184, 9, "%0", "%1") "s_nop 7"
#define HB "+v"(hb[0]), "+v"(hb[1]), "+v"(hb[2]), "+v"(hb[3]), "+v"(hb[4]), "+v"(hb[5]), "+v"(hb[6]), "+v"(hb[7])
    fetch();
    routed();
    asm volatile("s_cmp_lg_u32 %10, %11\n\ts_waitcnt vmcnt(4)\n\ts_cbranch_scc1 Lh_%=\n\ts_waitcnt vmcnt(0)\nLh_%=:\n\t" MM16 : "=&v"(dg), "=&v"(du), HB : "s"(rest), "s"(later) : "scc", "memory");
    TQ(6);
    for (unsigned spins = 0; __builtin_expect((__ballot(dg[0] != dg[0]) & m2) != 0ull, 0) && spins < RETRY_CAP; ++spins) {
      fetch();
      asm volatile("s_waitcnt vmcnt(0)\n\t" MM16 : "=&v"(dg), "=&v"(du), HB :: "memory");
    }
    f32x2 ra, rc, rd, re;
    asm volatile("s_mov_b64 exec, %7\n\tds_write_b128 %4, %5\n\tds_write_b128 %4, %6 offset:128\n\ts_mov_b64 exec, -1\n\ts_waitcnt lgkmcnt(0)\n\ts_barrier\n\t"
                 "ds_read2_b32 %0, %8 offset1:32\n\tds_read2_b32 %1, %8 offset0:64 offset1:96\n\tds_read2_b32 %2, %8 offset0:128 offset1:160\n\tds_read2_b32 %3, %8 offset0:192 offset1:224\n\ts_waitcnt lgkmcnt(0)"
                 : "=&v"(ra), "=&v"(rc), "=&v"(rd), "=&v"(re) : "v"(vred), "v"(dg), "v"(du), "s"(m2), "v"(vrf) : "memory");
    u32x2 actp;
    {
      const f32x2 gu = rb2((ra + rc) + (rd + re));
      const float sg = __uint_as_float(cvtpk(0.f, gu[0] * __builtin_amdgcn_rcpf(1.f + __builtin_amdgcn_exp2f(gu[0] * -1.44269504089f))));
      const unsigned a = cvtpk(0.f, sg * gu[1]);
      const unsigned o2 = __builtin_amdgcn_update_dpp(0u, a, DPP_XOR2, 0xf, 0xf, true);
      const unsigned pr = (lane & 2u) ? ((o2 >> 16) | a) : ((a >> 16) | o2);
      const unsigned o4 = __builtin_amdgcn_update_dpp(0u, pr, DPP_ROR4, 0xf, 0xf, true);
      actp = (lane & 4u) ? u32x2{o4, pr} : u32x2{pr, o4};
    }
    f32x4 yv[2];
#define ABD(t) "v"(u32x4{actp[0] & mk[t], actp[1] & mk[t], actp[0] & mk[t + 1], actp[1] & mk[t + 1]})
#define MM3(a, w, c0, c1) "v_mfma_f32_16x16x32_bf16 %0, %" #a ", a[" #w ":" #w "+3], " c0 "\n\tv_mfma_f32_16x16x32_bf16 %1, %" #a ", a[" #w "+16:" #w "+19], " c1 "\n\t"
    asm volatile("s_nop 1\n\t" MM3(2, 192, "0", "0") MM3(3, 196, "%0", "%1") MM3(4, 200, "%0", "%1") MM3(5, 204, "%0", "%1") "s_nop 6\n\t"
                 "buffer_store_dwordx4 %0, %6, %7, 0 offen sc1\n\tbuffer_store_dwordx4 %1, %6, %7, 0 offen offset:1024 sc1\n\ts_nop 1"
                 : "=&v"(yv[0]), "=&v"(yv[1]) : ABD(0), ABD(2), ABD(4), ABD(6), "v"(vpt), "s"(srdz) : "memory");
    TQ(7);
    out_rows(rest);
  }
  asm volatile("adaln_text_end:");
}
}

extern "C" void kernel_launch(void* const* d_in, const int* in_sizes, int n_in, void* d_out, int out_size, void* d_ws, size_t ws_size, hipStream_t stream) {
  (void)in_sizes; (void)n_in; (void)out_size;
  if (ws_size < WS_END) return;
  const u16* x    = (const u16*)d_in[0];
  const u16* kc   = (const u16*)d_in[1];
  const u16* vc   = (const u16*)d_in[2];
  const u16* wq   = (const u16*)d_in[3];
  const u16* wo   = (const u16*)d_in[4];
  const u16* wr   = (const u16*)d_in[6];
  const u16* wg   = (const u16*)d_in[7];
  const u16* wu   = (const u16*)d_in[8];
  const u16* wd   = (const u16*)d_in[9];
  const u16* c    = (const u16*)d_in[10];
  const u16* wmod = (const u16*)d_in[11];
  u16* out = (u16*)d_out;
  char* ws = (char*)d_ws;
  fa::Args fr;
  fr.x = x; fr.wmod = wmod; fr.wq = wq; fr.kc = kc; fr.vc = vc; fr.wo = wo; fr.wg = wg; fr.wu = wu; fr.wd = wd; fr.ws = ws; fr.out = out; fr.c = c; fr.wr = wr;
  hipFuncSetAttribute((const void*)fa::fattn_k, hipFuncAttributeMaxDynamicSharedMemorySize, (int)fa::LDS_BYTES);
  hipLaunchKernelGGL(fa::fattn_k, dim3(256), dim3(256), fa::LDS_BYTES, stream, fr);
}
```

```cpp
#include <hip/hip_runtime.h>
#include <hip/hip_bf16.h>
#include <cstdint>

#pragma clang fp contract(off)
typedef float    f32x2  __attribute__((ext_vector_type(2)));
typedef float    f32x4  __attribute__((ext_vector_type(4)));
typedef __bf16   bf16x8 __attribute__((ext_vector_type(8)));
typedef __bf16   bf16x4 __attribute__((ext_vector_type(4)));
typedef __bf16   bf16x2 __attribute__((ext_vector_type(2)));
typedef unsigned u32x4  __attribute__((ext_vector_type(4)));
typedef unsigned u32x2  __attribute__((ext_vector_type(2)));
typedef unsigned short u16;
#define DI __device__ __forceinline__
constexpr int SEQ = 2048, DM = 1024, NHEAD = 32, DHEAD = 128, DQ = NHEAD * DHEAD, DFF = 3584, NMOD = 6 * DM;
constexpr size_t WS_EP    = 0;
constexpr size_t WS_RST   = 32768;
constexpr size_t WS_CNT   = 65536;
constexpr size_t WS_MOD   = 131072;
constexpr size_t WS_Z     = 262144;
constexpr size_t WS_MODT  = WS_Z;
constexpr unsigned KEY_REP = 8, Z_SHARE = 1280;
constexpr size_t WS_KEYS  = WS_Z + 16384;
static_assert(Z_SHARE * 256 == 327680, "zeroed area");
constexpr size_t WS_PFL   = WS_Z + 65536;
constexpr size_t WS_OFL   = WS_Z + 196608;
constexpr size_t WS_H     = 1048576;
constexpr size_t WS_PART  = 6u << 20;
constexpr size_t WS_ROWS  = 8u << 20;
constexpr size_t WS_XIN   = 10u << 20;
constexpr unsigned BOX    = 65536;
constexpr size_t WS_END   = WS_XIN + (size_t)BOX * (SEQ / 4);

DI float bf2f(u16 b) { return __uint_as_float((unsigned)b << 16); }
DI float rbf(float x) { return (float)(__bf16)x; }
DI float lo16(unsigned w) { return __uint_as_float(w << 16); }
DI float hi16(unsigned w) { return __uint_as_float(w & 0xffff0000u); }
DI unsigned cvtpk(float lo, float hi) {
  f32x2 v = {lo, hi}; bf16x2 b = __builtin_convertvector(v, bf16x2); return __builtin_bit_cast(unsigned, b);
}
template <int CTRL> DI float dpp_add(float v) { return v + __uint_as_float(__builtin_amdgcn_update_dpp(0u, __float_as_uint(v), CTRL, 0xf, 0xf, true)); }
DI float swap32_add(float a, float b) {
  auto r = __builtin_amdgcn_permlane32_swap(__float_as_uint(a), __float_as_uint(b), false, false); return __uint_as_float(r[0]) + __uint_as_float(r[1]);
}
DI float swap16_add(float a, float b) {
  auto r = __builtin_amdgcn_permlane16_swap(__float_as_uint(a), __float_as_uint(b), false, false); return __uint_as_float(r[0]) + __uint_as_float(r[1]);
}
constexpr int DPP_XOR1 = 0xB1, DPP_XOR2 = 0x4E, DPP_ROR4 = 0x124, DPP_ROR8 = 0x128;
DI float wave_sum(float v) {
  v = dpp_add<DPP_XOR1>(v); v = dpp_add<DPP_XOR2>(v); v = dpp_add<DPP_ROR4>(v); v = dpp_add<DPP_ROR8>(v);
  v = swap16_add(v, v); return swap32_add(v, v);
}

DI f32x2 unpk(unsigned w) { return f32x2{lo16(w), hi16(w)}; }
DI unsigned cvtpk2(f32x2 v) { return __builtin_bit_cast(unsigned, __builtin_convertvector(v, bf16x2)); }
DI float rbh(float v) { return __uint_as_float(cvtpk(0.f, v)); }
DI f32x2 rb2(f32x2 v) { return f32x2{__uint_as_float(cvtpk(0.f, v[0])), __uint_as_float(cvtpk(0.f, v[1]))}; }
#define GAS __attribute__((address_space(1)))
#define LAS __attribute__((address_space(3)))
typedef GAS unsigned long long gu64;
#define RLX_AGENT __ATOMIC_RELAXED, __HIP_MEMORY_SCOPE_AGENT
constexpr unsigned SPIN_CAP = 1u << 16;

static_assert(BOX == 65536, "box size");
#define NP_FILL 8
#define GOT_WAIT "8"
#define FA_CLOBBERS "v0", "v1", "v2", "v3", "v8", "v9", "v10", "v11", "v16", "v17", "v18", "v19", "v20", "v21", "v22", "v23", "v24", "v25", "v26", "v27", "v28", "v29", "v30", "v31", "v32", "v33", "v34", "v35", "v40", "v41", "v42", "v43", "v48", "v49", "v50", "v51", "v52", "v53", "v54", "v55", "v56", "v57", "v58", "v59", "v60", "v61", "v62", "v63", "v64", "v65", "v66", "v67", "v72", "v73", "v74", "v75", "v80", "v81", "v82", "v83", "v84", "v85", "v86", "v87", "v88", "v89", "v90", "v91", "v92", "v93", "v94", "v95", "v96", "v97", "v98", "v99", "v104", "v105", "v106", "v107", "v112", "v113", "v114", "v115", "v116", "v117", "v118", "v119", "v120", "v121", "v122", "v123", "v124", "v125", "v126", "v127", "v200", "v201", "v202", "v203", "v205", "v207", "v208", "v209", "v210", "v211", "v212", "v213", "v214", "v215", "v216", "v217", "v218", "v219", "v220", "v221", "v222", "v223", "v224", "v230", "v231", "v232", "v233", "v234", "v235", "v237", "a0", "a1", "a2", "a3", "a4", "a5", "a6", "a7", "a8", "a9", "a10", "a11", "a12", "a13", "a14", "a15", "a16", "a17", "a18", "a19", "a20", "a21", "a22", "a23", "a24", "a25", "a26", "a27", "a28", "a29", "a30", "a31", "a32", "a33", "a34", "a35", "a36", "a37", "a38", "a39", "a40", "a41", "a42", "a43", "a44", "a45", "a46", "a47", "a48", "a49", "a50", "a51", "a52", "a53", "a54", "a55", "a56", "a57", "a58", "a59", "a60", "a61", "a62", "a63", "a64", "a65", "a66", "a67", "a68", "a69", "a70", "a71", "a72", "a73", "a74", "a75", "a76", "a77", "a78", "a79", "a80", "a81", "a82", "a83", "a84", "a85", "a86", "a87", "a88", "a89", "a90", "a91", "a92", "a93", "a94", "a95", "a96", "a97", "a98", "a99", "a100", "a101", "a102", "a103", "a104", "a105", "a106", "a107", "a108", "a109", "a110", "a111", "a112", "a113", "a114", "a115", "a116", "a117", "a118", "a119", "a120", "a121", "a122", "a123", "a124", "a125", "a126", "a127", "a128", "a129", "a130", "a131", "a132", "a133", "a134", "a135", "a136", "a137", "a138", "a139", "a140", "a141", "a142", "a143", "a144", "a145", "a146", "a147", "a148", "a149", "a150", "a151", "a152", "a153", "a154", "a155", "a156", "a157", "a158", "a159", "a160", "a161", "a162", "a163", "a164", "a165", "a166", "a167", "a168", "a169", "a170", "a171", "a172", "a173", "a174", "a175", "a176", "a177", "a178", "a179", "a180", "a181", "a182", "a183", "a184", "a185", "a186", "a187", "a188", "a189", "a190", "a191", "a192", "a193", "a194", "a195", "a196", "a197", "a198", "a199", "a200", "a201", "a202", "a203", "a204", "a205", "a206", "a207", "a208", "a209", "a210", "a211", "a212", "a213", "a214", "a215", "a216", "a217", "a218", "a219", "a220", "a221", "a222", "a223", "a224", "a225", "a226", "a227", "a228", "a229", "a230", "a231", "a232", "a233", "a234", "a235", "a236", "a237", "a238", "a239", "a240", "a241", "a242", "a243", "a244", "a245", "a246", "a247", "a248", "a249", "a250", "a251", "a252", "a253", "a254", "a255", "s40", "s41", "s42", "s43", "s44", "s45", "s46", "s47", "s48", "s49", "s50", "s51", "s52", "s53", "s54", "s55", "s56", "s57", "s58", "s59", "s60", "s61", "s62", "s63", "s64", "s65", "s66", "s67", "s68", "s69", "vcc", "m0", "scc", "memory"
#define EARLY2 0
#define SHW 1
#define QROT 0
#define SKV 0
#define KVPAR 0
#define QT2 0
#define QSTAG 0
#define TLATE 0
#define NTV 5
#define BPERM 0
#define NEAR 3
#define PPAR 1
#define FA_OUTS [got] "=&s"(got), "=&{v[128:129]}"(xf[0]), "=&{v[130:131]}"(xf[1]), "=&{v[132:133]}"(xf[2]), "=&{v[134:135]}"(xf[3]), "=&{v[136:137]}"(xf[4]), "=&{v[138:139]}"(xf[5]), "=&{v[140:141]}"(xf[6]), "=&{v[142:143]}"(xf[7]), "=&{v[144:145]}"(xf[8]), "=&{v[146:147]}"(xf[9]), "=&{v[148:149]}"(xf[10]), "=&{v[150:151]}"(xf[11]), "=&{v[152:153]}"(xf[12]), "=&{v[154:155]}"(xf[13]), "=&{v[156:157]}"(xf[14]), "=&{v[158:159]}"(xf[15]), "=&{v[160:163]}"(gaf[0]), "=&{v[164:167]}"(gaf[1]), "=&{v[168:171]}"(gaf[2]), "=&{v[172:175]}"(gaf[3]), "=&{v[176:179]}"(gaf[4]), "=&{v[180:183]}"(gaf[5]), "=&{v[184:187]}"(gaf[6]), "=&{v[188:191]}"(gaf[7]), "=&{v[4:7]}"(aw[0]), "=&{v[12:15]}"(aw[1]), "=&{v[36:39]}"(aw[2]), "=&{v[44:47]}"(aw[3]), "=&{v[68:71]}"(aw[4]), "=&{v[76:79]}"(aw[5]), "=&{v[100:103]}"(aw[6]), "=&{v[108:111]}"(aw[7]), "=&{v[192:195]}"(alo), "=&{v[196:199]}"(ahi), "=&{v204}"(late), "=&{v206}"(arow), "+{v229}"(fl), "+{v225}"(vwb), "+{v226}"(wd0), "+{v227}"(hq0), "+{v228}"(sd0), "+{v236}"(tq0)
#define FA_BODY \
"s_branch adaln_body\n" \
".p2align 12\n" \
"adaln_pg7:\n" \
"adaln_body:\n" \
"s_cbranch_execz adaln_fr\n" \
"s_nop 0\n" \
"s_mov_b32 s55, 0x80000\n" \
"v_add_u32_e32 v219, 0x8000, v227\n" \
"v_add_u32_e32 v220, 0x10000, v227\n" \
"v_add_u32_e32 v221, 0x18000, v227\n" \
"s_mov_b32 s50, 0\n" \
"s_movk_i32 s53, 0x1000\n" \
"s_mov_b32 s52, 0\n" \
"s_nop 0\n" \
"s_mov_b32 s49, 1572864\n" \
"v_add_u32_e32 v216, 0xfffffc80, v226\n" \
"v_add_u32_e32 v217, 0xf800, v226\n" \
"v_add_u32_e32 v218, 0xf480, v226\n" \
"s_add_u32 s40, %[m0b], 0\n" \
"s_nop 0\n" \
"s_add_u32 s44, %[m0b], 65536\n" \
"s_add_u32 s41, %[m0b], 16384\n" \
"s_add_u32 s45, %[m0b], 81920\n" \
"s_add_u32 s42, %[m0b], 32768\n" \
"s_add_u32 s46, %[m0b], 98304\n" \
"s_add_u32 s43, %[m0b], 49152\n" \
"s_add_u32 s47, %[m0b], 114688\n" \
"v_xor_b32_e64 v208, 64, %[kb0]\n" \
"v_xor_b32_e32 v209, 0x80, %[kb0]\n" \
"v_xor_b32_e32 v210, 0xc0, %[kb0]\n" \
"v_xor_b32_e64 v212, 64, %[kd0]\n" \
"v_xor_b32_e32 v213, 0x80, %[kd0]\n" \
"v_xor_b32_e32 v214, 0xc0, %[kd0]\n" \
"v_xor_b32_e64 v211, 32, %[vvb]\n" \
"v_add_u32_e32 v215, 0xfffffc80, %[vd0]\n" \
"v_readlane_b32 s64, %[tlo], 17\n" \
"v_readlane_b32 s65, %[thi], 17\n" \
"s_mov_b32 s66, 12582912\n" \
"s_mov_b32 s67, 0x20000\n" \
"v_mov_b64_e32 v[0:1], 0\n" \
"v_mov_b64_e32 v[2:3], 0\n" \
"v_mov_b64_e32 v[4:5], 0\n" \
"v_mov_b64_e32 v[6:7], 0\n" \
"v_mov_b64_e32 v[8:9], 0\n" \
"v_mov_b64_e32 v[10:11], 0\n" \
"v_mov_b64_e32 v[12:13], 0\n" \
"v_mov_b64_e32 v[14:15], 0\n" \
"v_mov_b64_e32 v[16:17], 0\n" \
"v_mov_b64_e32 v[18:19], 0\n" \
"v_mov_b64_e32 v[20:21], 0\n" \
"v_mov_b64_e32 v[22:23], 0\n" \
"v_mov_b64_e32 v[24:25], 0\n" \
"v_mov_b64_e32 v[26:27], 0\n" \
"v_mov_b64_e32 v[28:29], 0\n" \
"v_mov_b64_e32 v[30:31], 0\n" \
"v_mov_b64_e32 v[32:33], 0\n" \
"v_mov_b64_e32 v[34:35], 0\n" \
"v_mov_b64_e32 v[36:37], 0\n" \
"v_mov_b64_e32 v[38:39], 0\n" \
"v_mov_b64_e32 v[40:41], 0\n" \
"v_mov_b64_e32 v[42:43], 0\n" \
"v_mov_b64_e32 v[44:45], 0\n" \
"v_mov_b64_e32 v[46:47], 0\n" \
"v_mov_b64_e32 v[48:49], 0\n" \
"v_mov_b64_e32 v[50:51], 0\n" \
"v_mov_b64_e32 v[52:53], 0\n" \
"v_mov_b64_e32 v[54:55], 0\n" \
"v_mov_b64_e32 v[56:57], 0\n" \
"v_mov_b64_e32 v[58:59], 0\n" \
"v_mov_b64_e32 v[60:61], 0\n" \
"v_mov_b64_e32 v[62:63], 0\n" \
"v_mov_b64_e32 v[64:65], 0\n" \
"v_mov_b64_e32 v[66:67], 0\n" \
"v_mov_b64_e32 v[68:69], 0\n" \
"v_mov_b64_e32 v[70:71], 0\n" \
"v_mov_b64_e32 v[72:73], 0\n" \
"v_mov_b64_e32 v[74:75], 0\n" \
"v_mov_b64_e32 v[76:77], 0\n" \
"v_mov_b64_e32 v[78:79], 0\n" \
"v_mov_b64_e32 v[80:81], 0\n" \
"v_mov_b64_e32 v[82:83], 0\n" \
"v_mov_b64_e32 v[84:85], 0\n" \
"v_mov_b64_e32 v[86:87], 0\n" \
"v_mov_b64_e32 v[88:89], 0\n" \
"v_mov_b64_e32 v[90:91], 0\n" \
"v_mov_b64_e32 v[92:93], 0\n" \
"v_mov_b64_e32 v[94:95], 0\n" \
"v_mov_b64_e32 v[96:97], 0\n" \
"v_mov_b64_e32 v[98:99], 0\n" \
"v_mov_b64_e32 v[100:101], 0\n" \
"v_mov_b64_e32 v[102:103], 0\n" \
"v_mov_b64_e32 v[104:105], 0\n" \
"v_mov_b64_e32 v[106:107], 0\n" \
"v_mov_b64_e32 v[108:109], 0\n" \
"v_mov_b64_e32 v[110:111], 0\n" \
"v_mov_b64_e32 v[112:113], 0\n" \
"v_mov_b64_e32 v[114:115], 0\n" \
"v_mov_b64_e32 v[116:117], 0\n" \
"v_mov_b64_e32 v[118:119], 0\n" \
"v_mov_b64_e32 v[120:121], 0\n" \
"v_mov_b64_e32 v[122:123], 0\n" \
"v_mov_b64_e32 v[124:125], 0\n" \
"v_mov_b64_e32 v[126:127], 0\n" \
"s_mov_b32 s51, 0x10000\n" \
"v_mbcnt_lo_u32_b32 v232, -1, 0\n" \
"v_mov_b64_e32 v[234:235], %[tag]\n" \
"v_lshlrev_b32_e32 v233, 3, v232\n" \
"v_lshlrev_b32_e32 v232, 8, v232\n" \
"s_waitcnt vmcnt(0)\n" \
"s_barrier\n" \
"s_cmp_lg_u32 %[wid], 0\n" \
"s_cbranch_scc1 Lwalk_%=\n" \
"s_mov_b32 exec_hi, 0\n" \
"global_store_dwordx2 v232, v[234:235], %[psig] sc1\n" \
"s_mov_b32 exec_hi, -1\n" \
"s_nop 0\n" \
"v_readlane_b32 s62, %[tlo], 13\n" \
"s_nop 3\n" \
"s_cmpk_eq_u32 s62, 252\n" \
"s_cbranch_scc1 Lprobe_%=\n" \
"Lgo_%=:\n" \
"s_nop 0\n" \
"ds_read_b64_tr_b16 v[128:129], v225 offset:0\n" \
"ds_read_b64_tr_b16 v[130:131], v225 offset:256\n" \
"ds_read_b64_tr_b16 v[132:133], v225 offset:8192\n" \
"ds_read_b64_tr_b16 v[134:135], v225 offset:8448\n" \
"ds_read_b64_tr_b16 v[136:137], v225 offset:8\n" \
"ds_read_b64_tr_b16 v[138:139], v225 offset:264\n" \
"ds_read_b64_tr_b16 v[140:141], v225 offset:8200\n" \
"ds_read_b64_tr_b16 v[142:143], v225 offset:8456\n" \
"ds_read_b64_tr_b16 v[144:145], v225 offset:512\n" \
"ds_read_b64_tr_b16 v[146:147], v225 offset:768\n" \
"ds_read_b64_tr_b16 v[148:149], v225 offset:8704\n" \
"ds_read_b64_tr_b16 v[150:151], v225 offset:8960\n" \
"ds_read_b64_tr_b16 v[152:153], v225 offset:520\n" \
"ds_read_b64_tr_b16 v[154:155], v225 offset:776\n" \
"ds_read_b64_tr_b16 v[156:157], v225 offset:8712\n" \
"ds_read_b64_tr_b16 v[158:159], v225 offset:8968\n" \
"s_mov_b32 s60, 4194304\n" \
"v_and_b32_e32 v233, 56, v233\n" \
"s_lshl_b32 s61, %[wid], 6\n" \
"v_add_u32_e64 v233, s61, v233\n" \
"Lpoll_%=:\n" \
"global_load_dwordx2 v[230:231], v233, %[pcnt] sc1\n" \
"s_waitcnt vmcnt(0)\n" \
"v_cmp_ne_u64_e32 vcc, v[230:231], v[234:235]\n" \
"s_sub_u32 s51, s51, 1\n" \
"s_cbranch_scc1 Lpd_%=\n" \
"s_nop 3\n" \
"s_cbranch_vccnz Lpoll_%=\n" \
"Lpd_%=:\n" \
"buffer_load_dwordx4 a[128:131], v227, %[srdz], s50 offen offset:0 sc1\n" \
"buffer_load_dwordx4 a[132:135], v219, %[srdz], s50 offen offset:0 sc1\n" \
"buffer_load_dwordx4 a[136:139], v220, %[srdz], s50 offen offset:0 sc1\n" \
"buffer_load_dwordx4 a[140:143], v221, %[srdz], s50 offen offset:0 sc1\n" \
"buffer_load_dwordx4 a[144:147], v227, %[srdz], s50 offen offset:1024 sc1\n" \
"buffer_load_dwordx4 a[148:151], v219, %[srdz], s50 offen offset:1024 sc1\n" \
"buffer_load_dwordx4 a[152:155], v220, %[srdz], s50 offen offset:1024 sc1\n" \
"buffer_load_dwordx4 a[156:159], v221, %[srdz], s50 offen offset:1024 sc1\n" \
"buffer_load_dwordx4 a[160:163], v227, %[srdz], s50 offen offset:2048 sc1\n" \
"buffer_load_dwordx4 a[164:167], v219, %[srdz], s50 offen offset:2048 sc1\n" \
"buffer_load_dwordx4 a[168:171], v220, %[srdz], s50 offen offset:2048 sc1\n" \
"buffer_load_dwordx4 a[172:175], v221, %[srdz], s50 offen offset:2048 sc1\n" \
"buffer_load_dwordx4 a[176:179], v227, %[srdz], s50 offen offset:3072 sc1\n" \
"buffer_load_dwordx4 a[180:183], v219, %[srdz], s50 offen offset:3072 sc1\n" \
"buffer_load_dwordx4 a[184:187], v220, %[srdz], s50 offen offset:3072 sc1\n" \
"buffer_load_dwordx4 a[188:191], v221, %[srdz], s50 offen offset:3072 sc1\n" \
"s_add_u32 s50, s50, s53\n" \
"s_nop 0\n" \
"buffer_load_dwordx4 a[192:195], v227, %[srdz], s50 offen offset:0 sc1\n" \
"buffer_load_dwordx4 a[196:199], v219, %[srdz], s50 offen offset:0 sc1\n" \
"buffer_load_dwordx4 a[200:203], v220, %[srdz], s50 offen offset:0 sc1\n" \
"buffer_load_dwordx4 a[204:207], v221, %[srdz], s50 offen offset:0 sc1\n" \
"buffer_load_dwordx4 a[208:211], v227, %[srdz], s50 offen offset:1024 sc1\n" \
"buffer_load_dwordx4 a[212:215], v219, %[srdz], s50 offen offset:1024 sc1\n" \
"buffer_load_dwordx4 a[216:219], v220, %[srdz], s50 offen offset:1024 sc1\n" \
"buffer_load_dwordx4 a[220:223], v221, %[srdz], s50 offen offset:1024 sc1\n" \
"s_mov_b32 s51, 3\n" \
"s_waitcnt vmcnt(16) lgkmcnt(0)\n" \
"Lq_%=:\n" \
"v_mfma_f32_16x16x32_bf16 v[0:3], v[128:131], a[128:131], v[0:3]\n" \
"ds_read_b64_tr_b16 v[160:161], v225 offset:1024\n" \
"v_mfma_f32_16x16x32_bf16 v[0:3], v[132:135], a[144:147], v[0:3]\n" \
"ds_read_b64_tr_b16 v[162:163], v225 offset:1280\n" \
"v_mfma_f32_16x16x32_bf16 v[32:35], v[128:131], a[132:135], v[32:35]\n" \
"s_mov_b32 m0, s47\n" \
"s_nop 0\n" \
"buffer_load_dwordx4 v217, %[srdw], s49 offen offset:2048 lds\n" \
"v_mfma_f32_16x16x32_bf16 v[32:35], v[132:135], a[148:151], v[32:35]\n" \
"ds_read_b64_tr_b16 v[164:165], v225 offset:9216\n" \
"v_mfma_f32_16x16x32_bf16 v[64:67], v[128:131], a[136:139], v[64:67]\n" \
"ds_read_b64_tr_b16 v[166:167], v225 offset:9472\n" \
"v_mfma_f32_16x16x32_bf16 v[64:67], v[132:135], a[152:155], v[64:67]\n" \
"v_mfma_f32_16x16x32_bf16 v[96:99], v[128:131], a[140:143], v[96:99]\n" \
"ds_read_b64_tr_b16 v[168:169], v225 offset:1032\n" \
"v_mfma_f32_16x16x32_bf16 v[96:99], v[132:135], a[156:159], v[96:99]\n" \
"ds_read_b64_tr_b16 v[170:171], v225 offset:1288\n" \
"v_mfma_f32_16x16x32_bf16 v[4:7], v[136:139], a[128:131], v[4:7]\n" \
"buffer_load_dwordx4 v218, %[srdw], s49 offen offset:3072 lds\n" \
"v_mfma_f32_16x16x32_bf16 v[4:7], v[140:143], a[144:147], v[4:7]\n" \
"ds_read_b64_tr_b16 v[172:173], v225 offset:9224\n" \
"v_mfma_f32_16x16x32_bf16 v[36:39], v[136:139], a[132:135], v[36:39]\n" \
"ds_read_b64_tr_b16 v[174:175], v225 offset:9480\n" \
"v_mfma_f32_16x16x32_bf16 v[36:39], v[140:143], a[148:151], v[36:39]\n" \
"v_mfma_f32_16x16x32_bf16 v[68:71], v[136:139], a[136:139], v[68:71]\n" \
"ds_read_b64_tr_b16 v[176:177], v225 offset:1536\n" \
"v_mfma_f32_16x16x32_bf16 v[68:71], v[140:143], a[152:155], v[68:71]\n" \
"ds_read_b64_tr_b16 v[178:179], v225 offset:1792\n" \
"v_mfma_f32_16x16x32_bf16 v[100:103], v[136:139], a[140:143], v[100:103]\n" \
"buffer_load_dwordx4 a[224:227], v227, %[srdz], s50 offen offset:2048 sc1\n" \
"v_mfma_f32_16x16x32_bf16 v[100:103], v[140:143], a[156:159], v[100:103]\n" \
"ds_read_b64_tr_b16 v[180:181], v225 offset:9728\n" \
"v_mfma_f32_16x16x32_bf16 v[8:11], v[144:147], a[128:131], v[8:11]\n" \
"ds_read_b64_tr_b16 v[182:183], v225 offset:9984\n" \
"v_mfma_f32_16x16x32_bf16 v[8:11], v[148:151], a[144:147], v[8:11]\n" \
"v_mfma_f32_16x16x32_bf16 v[40:43], v[144:147], a[132:135], v[40:43]\n" \
"ds_read_b64_tr_b16 v[184:185], v225 offset:1544\n" \
"v_mfma_f32_16x16x32_bf16 v[40:43], v[148:151], a[148:151], v[40:43]\n" \
"ds_read_b64_tr_b16 v[186:187], v225 offset:1800\n" \
"v_mfma_f32_16x16x32_bf16 v[72:75], v[144:147], a[136:139], v[72:75]\n" \
"buffer_load_dwordx4 a[228:231], v219, %[srdz], s50 offen offset:2048 sc1\n" \
"v_mfma_f32_16x16x32_bf16 v[72:75], v[148:151], a[152:155], v[72:75]\n" \
"ds_read_b64_tr_b16 v[188:189], v225 offset:9736\n" \
"v_mfma_f32_16x16x32_bf16 v[104:107], v[144:147], a[140:143], v[104:107]\n" \
"ds_read_b64_tr_b16 v[190:191], v225 offset:9992\n" \
"v_mfma_f32_16x16x32_bf16 v[104:107], v[148:151], a[156:159], v[104:107]\n" \
"v_mfma_f32_16x16x32_bf16 v[12:15], v[152:155], a[128:131], v[12:15]\n" \
"buffer_load_dwordx4 a[232:235], v220, %[srdz], s50 offen offset:2048 sc1\n" \
"v_mfma_f32_16x16x32_bf16 v[12:15], v[156:159], a[144:147], v[12:15]\n" \
"v_mfma_f32_16x16x32_bf16 v[44:47], v[152:155], a[132:135], v[44:47]\n" \
"v_mfma_f32_16x16x32_bf16 v[44:47], v[156:159], a[148:151], v[44:47]\n" \
"v_mfma_f32_16x16x32_bf16 v[76:79], v[152:155], a[136:139], v[76:79]\n" \
"buffer_load_dwordx4 a[236:239], v221, %[srdz], s50 offen offset:2048 sc1\n" \
"v_mfma_f32_16x16x32_bf16 v[76:79], v[156:159], a[152:155], v[76:79]\n" \
"v_mfma_f32_16x16x32_bf16 v[108:111], v[152:155], a[140:143], v[108:111]\n" \
"v_mfma_f32_16x16x32_bf16 v[108:111], v[156:159], a[156:159], v[108:111]\n" \
"s_waitcnt vmcnt(29) lgkmcnt(0)\n" \
"s_barrier\n" \
"v_mfma_f32_16x16x32_bf16 v[16:19], v[160:163], a[128:131], v[16:19]\n" \
"ds_read_b64_tr_b16 v[128:129], v225 offset:16384\n" \
"v_mfma_f32_16x16x32_bf16 v[16:19], v[164:167], a[144:147], v[16:19]\n" \
"ds_read_b64_tr_b16 v[130:131], v225 offset:16640\n" \
"v_mfma_f32_16x16x32_bf16 v[48:51], v[160:163], a[132:135], v[48:51]\n" \
"s_add_u32 s49, s49, s55\n" \
"s_mov_b32 m0, s44\n" \
"s_nop 0\n" \
"buffer_load_dwordx4 v226, %[srdw], s49 offen offset:0 lds\n" \
"v_mfma_f32_16x16x32_bf16 v[48:51], v[164:167], a[148:151], v[48:51]\n" \
"ds_read_b64_tr_b16 v[132:133], v225 offset:24576\n" \
"v_mfma_f32_16x16x32_bf16 v[80:83], v[160:163], a[136:139], v[80:83]\n" \
"ds_read_b64_tr_b16 v[134:135], v225 offset:24832\n" \
"v_mfma_f32_16x16x32_bf16 v[80:83], v[164:167], a[152:155], v[80:83]\n" \
"v_mfma_f32_16x16x32_bf16 v[112:115], v[160:163], a[140:143], v[112:115]\n" \
"ds_read_b64_tr_b16 v[136:137], v225 offset:16392\n" \
"v_mfma_f32_16x16x32_bf16 v[112:115], v[164:167], a[156:159], v[112:115]\n" \
"ds_read_b64_tr_b16 v[138:139], v225 offset:16648\n" \
"v_mfma_f32_16x16x32_bf16 v[20:23], v[168:171], a[128:131], v[20:23]\n" \
"buffer_load_dwordx4 v216, %[srdw], s49 offen offset:1024 lds\n" \
"v_mfma_f32_16x16x32_bf16 v[20:23], v[172:175], a[144:147], v[20:23]\n" \
"ds_read_b64_tr_b16 v[140:141], v225 offset:24584\n" \
"v_mfma_f32_16x16x32_bf16 v[52:55], v[168:171], a[132:135], v[52:55]\n" \
"ds_read_b64_tr_b16 v[142:143], v225 offset:24840\n" \
"v_mfma_f32_16x16x32_bf16 v[52:55], v[172:175], a[148:151], v[52:55]\n" \
"v_mfma_f32_16x16x32_bf16 v[84:87], v[168:171], a[136:139], v[84:87]\n" \
"ds_read_b64_tr_b16 v[144:145], v225 offset:16896\n" \
"v_mfma_f32_16x16x32_bf16 v[84:87], v[172:175], a[152:155], v[84:87]\n" \
"ds_read_b64_tr_b16 v[146:147], v225 offset:17152\n" \
"v_mfma_f32_16x16x32_bf16 v[116:119], v[168:171], a[140:143], v[116:119]\n" \
"buffer_load_dwordx4 a[240:243], v227, %[srdz], s50 offen offset:3072 sc1\n" \
"v_mfma_f32_16x16x32_bf16 v[116:119], v[172:175], a[156:159], v[116:119]\n" \
"ds_read_b64_tr_b16 v[148:149], v225 offset:25088\n" \
"v_mfma_f32_16x16x32_bf16 v[24:27], v[176:179], a[128:131], v[24:27]\n" \
"ds_read_b64_tr_b16 v[150:151], v225 offset:25344\n" \
"v_mfma_f32_16x16x32_bf16 v[24:27], v[180:183], a[144:147], v[24:27]\n" \
"v_mfma_f32_16x16x32_bf16 v[56:59], v[176:179], a[132:135], v[56:59]\n" \
"ds_read_b64_tr_b16 v[152:153], v225 offset:16904\n" \
"v_mfma_f32_16x16x32_bf16 v[56:59], v[180:183], a[148:151], v[56:59]\n" \
"ds_read_b64_tr_b16 v[154:155], v225 offset:17160\n" \
"v_mfma_f32_16x16x32_bf16 v[88:91], v[176:179], a[136:139], v[88:91]\n" \
"buffer_load_dwordx4 a[244:247], v219, %[srdz], s50 offen offset:3072 sc1\n" \
"v_mfma_f32_16x16x32_bf16 v[88:91], v[180:183], a[152:155], v[88:91]\n" \
"ds_read_b64_tr_b16 v[156:157], v225 offset:25096\n" \
"v_mfma_f32_16x16x32_bf16 v[120:123], v[176:179], a[140:143], v[120:123]\n" \
"ds_read_b64_tr_b16 v[158:159], v225 offset:25352\n" \
"v_mfma_f32_16x16x32_bf16 v[120:123], v[180:183], a[156:159], v[120:123]\n" \
"v_mfma_f32_16x16x32_bf16 v[28:31], v[184:187], a[128:131], v[28:31]\n" \
"buffer_load_dwordx4 a[248:251], v220, %[srdz], s50 offen offset:3072 sc1\n" \
"v_mfma_f32_16x16x32_bf16 v[28:31], v[188:191], a[144:147], v[28:31]\n" \
"v_mfma_f32_16x16x32_bf16 v[60:63], v[184:187], a[132:135], v[60:63]\n" \
"v_mfma_f32_16x16x32_bf16 v[60:63], v[188:191], a[148:151], v[60:63]\n" \
"v_mfma_f32_16x16x32_bf16 v[92:95], v[184:187], a[136:139], v[92:95]\n" \
"buffer_load_dwordx4 a[252:255], v221, %[srdz], s50 offen offset:3072 sc1\n" \
"v_mfma_f32_16x16x32_bf16 v[92:95], v[188:191], a[152:155], v[92:95]\n" \
"v_mfma_f32_16x16x32_bf16 v[124:127], v[184:187], a[140:143], v[124:127]\n" \
"v_mfma_f32_16x16x32_bf16 v[124:127], v[188:191], a[156:159], v[124:127]\n" \
"s_waitcnt vmcnt(20) lgkmcnt(0)\n" \
"v_mfma_f32_16x16x32_bf16 v[0:3], v[128:131], a[160:163], v[0:3]\n" \
"ds_read_b64_tr_b16 v[160:161], v225 offset:17408\n" \
"v_mfma_f32_16x16x32_bf16 v[0:3], v[132:135], a[176:179], v[0:3]\n" \
"ds_read_b64_tr_b16 v[162:163], v225 offset:17664\n" \
"v_mfma_f32_16x16x32_bf16 v[32:35], v[128:131], a[164:167], v[32:35]\n" \
"s_mov_b32 m0, s44\n" \
"s_nop 0\n" \
"buffer_load_dwordx4 v217, %[srdw], s49 offen offset:2048 lds\n" \
"v_mfma_f32_16x16x32_bf16 v[32:35], v[132:135], a[180:183], v[32:35]\n" \
"ds_read_b64_tr_b16 v[164:165], v225 offset:25600\n" \
"v_mfma_f32_16x16x32_bf16 v[64:67], v[128:131], a[168:171], v[64:67]\n" \
"ds_read_b64_tr_b16 v[166:167], v225 offset:25856\n" \
"v_mfma_f32_16x16x32_bf16 v[64:67], v[132:135], a[184:187], v[64:67]\n" \
"v_mfma_f32_16x16x32_bf16 v[96:99], v[128:131], a[172:175], v[96:99]\n" \
"ds_read_b64_tr_b16 v[168:169], v225 offset:17416\n" \
"v_mfma_f32_16x16x32_bf16 v[96:99], v[132:135], a[188:191], v[96:99]\n" \
"ds_read_b64_tr_b16 v[170:171], v225 offset:17672\n" \
"v_mfma_f32_16x16x32_bf16 v[4:7], v[136:139], a[160:163], v[4:7]\n" \
"buffer_load_dwordx4 v218, %[srdw], s49 offen offset:3072 lds\n" \
"v_mfma_f32_16x16x32_bf16 v[4:7], v[140:143], a[176:179], v[4:7]\n" \
"ds_read_b64_tr_b16 v[172:173], v225 offset:25608\n" \
"v_mfma_f32_16x16x32_bf16 v[36:39], v[136:139], a[164:167], v[36:39]\n" \
"ds_read_b64_tr_b16 v[174:175], v225 offset:25864\n" \
"v_mfma_f32_16x16x32_bf16 v[36:39], v[140:143], a[180:183], v[36:39]\n" \
"v_mfma_f32_16x16x32_bf16 v[68:71], v[136:139], a[168:171], v[68:71]\n" \
"ds_read_b64_tr_b16 v[176:177], v225 offset:17920\n" \
"v_mfma_f32_16x16x32_bf16 v[68:71], v[140:143], a[184:187], v[68:71]\n" \
"ds_read_b64_tr_b16 v[178:179], v225 offset:18176\n" \
"v_mfma_f32_16x16x32_bf16 v[100:103], v[136:139], a[172:175], v[100:103]\n" \
"s_add_u32 s50, s50, s53\n" \
"buffer_load_dwordx4 a[128:131], v227, %[srdz], s50 offen offset:0 sc1\n" \
"v_mfma_f32_16x16x32_bf16 v[100:103], v[140:143], a[188:191], v[100:103]\n" \
"ds_read_b64_tr_b16 v[180:181], v225 offset:26112\n" \
"v_mfma_f32_16x16x32_bf16 v[8:11], v[144:147], a[160:163], v[8:11]\n" \
"ds_read_b64_tr_b16 v[182:183], v225 offset:26368\n" \
"v_mfma_f32_16x16x32_bf16 v[8:11], v[148:151], a[176:179], v[8:11]\n" \
"v_mfma_f32_16x16x32_bf16 v[40:43], v[144:147], a[164:167], v[40:43]\n" \
"ds_read_b64_tr_b16 v[184:185], v225 offset:17928\n" \
"v_mfma_f32_16x16x32_bf16 v[40:43], v[148:151], a[180:183], v[40:43]\n" \
"ds_read_b64_tr_b16 v[186:187], v225 offset:18184\n" \
"v_mfma_f32_16x16x32_bf16 v[72:75], v[144:147], a[168:171], v[72:75]\n" \
"buffer_load_dwordx4 a[132:135], v219, %[srdz], s50 offen offset:0 sc1\n" \
"v_mfma_f32_16x16x32_bf16 v[72:75], v[148:151], a[184:187], v[72:75]\n" \
"ds_read_b64_tr_b16 v[188:189], v225 offset:26120\n" \
"v_mfma_f32_16x16x32_bf16 v[104:107], v[144:147], a[172:175], v[104:107]\n" \
"ds_read_b64_tr_b16 v[190:191], v225 offset:26376\n" \
"v_mfma_f32_16x16x32_bf16 v[104:107], v[148:151], a[188:191], v[104:107]\n" \
"v_mfma_f32_16x16x32_bf16 v[12:15], v[152:155], a[160:163], v[12:15]\n" \
"buffer_load_dwordx4 a[136:139], v220, %[srdz], s50 offen offset:0 sc1\n" \
"v_mfma_f32_16x16x32_bf16 v[12:15], v[156:159], a[176:179], v[12:15]\n" \
"v_mfma_f32_16x16x32_bf16 v[44:47], v[152:155], a[164:167], v[44:47]\n" \
"v_mfma_f32_16x16x32_bf16 v[44:47], v[156:159], a[180:183], v[44:47]\n" \
"v_mfma_f32_16x16x32_bf16 v[76:79], v[152:155], a[168:171], v[76:79]\n" \
"buffer_load_dwordx4 a[140:143], v221, %[srdz], s50 offen offset:0 sc1\n" \
"v_mfma_f32_16x16x32_bf16 v[76:79], v[156:159], a[184:187], v[76:79]\n" \
"v_mfma_f32_16x16x32_bf16 v[108:111], v[152:155], a[172:175], v[108:111]\n" \
"v_mfma_f32_16x16x32_bf16 v[108:111], v[156:159], a[188:191], v[108:111]\n" \
"s_waitcnt vmcnt(29) lgkmcnt(0)\n" \
"s_barrier\n" \
"v_mfma_f32_16x16x32_bf16 v[16:19], v[160:163], a[160:163], v[16:19]\n" \
"ds_read_b64_tr_b16 v[128:129], v225 offset:32768\n" \
"v_mfma_f32_16x16x32_bf16 v[16:19], v[164:167], a[176:179], v[16:19]\n" \
"ds_read_b64_tr_b16 v[130:131], v225 offset:33024\n" \
"v_mfma_f32_16x16x32_bf16 v[48:51], v[160:163], a[164:167], v[48:51]\n" \
"s_add_u32 s49, s49, s55\n" \
"s_mov_b32 m0, s45\n" \
"s_nop 0\n" \
"buffer_load_dwordx4 v226, %[srdw], s49 offen offset:0 lds\n" \
"v_mfma_f32_16x16x32_bf16 v[48:51], v[164:167], a[180:183], v[48:51]\n" \
"ds_read_b64_tr_b16 v[132:133], v225 offset:40960\n" \
"v_mfma_f32_16x16x32_bf16 v[80:83], v[160:163], a[168:171], v[80:83]\n" \
"ds_read_b64_tr_b16 v[134:135], v225 offset:41216\n" \
"v_mfma_f32_16x16x32_bf16 v[80:83], v[164:167], a[184:187], v[80:83]\n" \
"v_mfma_f32_16x16x32_bf16 v[112:115], v[160:163], a[172:175], v[112:115]\n" \
"ds_read_b64_tr_b16 v[136:137], v225 offset:32776\n" \
"v_mfma_f32_16x16x32_bf16 v[112:115], v[164:167], a[188:191], v[112:115]\n" \
"ds_read_b64_tr_b16 v[138:139], v225 offset:33032\n" \
"v_mfma_f32_16x16x32_bf16 v[20:23], v[168:171], a[160:163], v[20:23]\n" \
"buffer_load_dwordx4 v216, %[srdw], s49 offen offset:1024 lds\n" \
"v_mfma_f32_16x16x32_bf16 v[20:23], v[172:175], a[176:179], v[20:23]\n" \
"ds_read_b64_tr_b16 v[140:141], v225 offset:40968\n" \
"v_mfma_f32_16x16x32_bf16 v[52:55], v[168:171], a[164:167], v[52:55]\n" \
"ds_read_b64_tr_b16 v[142:143], v225 offset:41224\n" \
"v_mfma_f32_16x16x32_bf16 v[52:55], v[172:175], a[180:183], v[52:55]\n" \
"v_mfma_f32_16x16x32_bf16 v[84:87], v[168:171], a[168:171], v[84:87]\n" \
"ds_read_b64_tr_b16 v[144:145], v225 offset:33280\n" \
"v_mfma_f32_16x16x32_bf16 v[84:87], v[172:175], a[184:187], v[84:87]\n" \
"ds_read_b64_tr_b16 v[146:147], v225 offset:33536\n" \
"v_mfma_f32_16x16x32_bf16 v[116:119], v[168:171], a[172:175], v[116:119]\n" \
"buffer_load_dwordx4 a[144:147], v227, %[srdz], s50 offen offset:1024 sc1\n" \
"v_mfma_f32_16x16x32_bf16 v[116:119], v[172:175], a[188:191], v[116:119]\n" \
"ds_read_b64_tr_b16 v[148:149], v225 offset:41472\n" \
"v_mfma_f32_16x16x32_bf16 v[24:27], v[176:179], a[160:163], v[24:27]\n" \
"ds_read_b64_tr_b16 v[150:151], v225 offset:41728\n" \
"v_mfma_f32_16x16x32_bf16 v[24:27], v[180:183], a[176:179], v[24:27]\n" \
"v_mfma_f32_16x16x32_bf16 v[56:59], v[176:179], a[164:167], v[56:59]\n" \
"ds_read_b64_tr_b16 v[152:153], v225 offset:33288\n" \
"v_mfma_f32_16x16x32_bf16 v[56:59], v[180:183], a[180:183], v[56:59]\n" \
"ds_read_b64_tr_b16 v[154:155], v225 offset:33544\n" \
"v_mfma_f32_16x16x32_bf16 v[88:91], v[176:179], a[168:171], v[88:91]\n" \
"buffer_load_dwordx4 a[148:151], v219, %[srdz], s50 offen offset:1024 sc1\n" \
"v_mfma_f32_16x16x32_bf16 v[88:91], v[180:183], a[184:187], v[88:91]\n" \
"ds_read_b64_tr_b16 v[156:157], v225 offset:41480\n" \
"v_mfma_f32_16x16x32_bf16 v[120:123], v[176:179], a[172:175], v[120:123]\n" \
"ds_read_b64_tr_b16 v[158:159], v225 offset:41736\n" \
"v_mfma_f32_16x16x32_bf16 v[120:123], v[180:183], a[188:191], v[120:123]\n" \
"v_mfma_f32_16x16x32_bf16 v[28:31], v[184:187], a[160:163], v[28:31]\n" \
"buffer_load_dwordx4 a[152:155], v220, %[srdz], s50 offen offset:1024 sc1\n" \
"v_mfma_f32_16x16x32_bf16 v[28:31], v[188:191], a[176:179], v[28:31]\n" \
"v_mfma_f32_16x16x32_bf16 v[60:63], v[184:187], a[164:167], v[60:63]\n" \
"v_mfma_f32_16x16x32_bf16 v[60:63], v[188:191], a[180:183], v[60:63]\n" \
"v_mfma_f32_16x16x32_bf16 v[92:95], v[184:187], a[168:171], v[92:95]\n" \
"buffer_load_dwordx4 a[156:159], v221, %[srdz], s50 offen offset:1024 sc1\n" \
"v_mfma_f32_16x16x32_bf16 v[92:95], v[188:191], a[184:187], v[92:95]\n" \
"v_mfma_f32_16x16x32_bf16 v[124:127], v[184:187], a[172:175], v[124:127]\n" \
"v_mfma_f32_16x16x32_bf16 v[124:127], v[188:191], a[188:191], v[124:127]\n" \
"s_waitcnt vmcnt(24) lgkmcnt(0)\n" \
"v_mfma_f32_16x16x32_bf16 v[0:3], v[128:131], a[192:195], v[0:3]\n" \
"ds_read_b64_tr_b16 v[160:161], v225 offset:33792\n" \
"v_mfma_f32_16x16x32_bf16 v[0:3], v[132:135], a[208:211], v[0:3]\n" \
"ds_read_b64_tr_b16 v[162:163], v225 offset:34048\n" \
"v_mfma_f32_16x16x32_bf16 v[32:35], v[128:131], a[196:199], v[32:35]\n" \
"s_mov_b32 m0, s45\n" \
"s_nop 0\n" \
"buffer_load_dwordx4 v217, %[srdw], s49 offen offset:2048 lds\n" \
"v_mfma_f32_16x16x32_bf16 v[32:35], v[132:135], a[212:215], v[32:35]\n" \
"ds_read_b64_tr_b16 v[164:165], v225 offset:41984\n" \
"v_mfma_f32_16x16x32_bf16 v[64:67], v[128:131], a[200:203], v[64:67]\n" \
"ds_read_b64_tr_b16 v[166:167], v225 offset:42240\n" \
"v_mfma_f32_16x16x32_bf16 v[64:67], v[132:135], a[216:219], v[64:67]\n" \
"v_mfma_f32_16x16x32_bf16 v[96:99], v[128:131], a[204:207], v[96:99]\n" \
"ds_read_b64_tr_b16 v[168:169], v225 offset:33800\n" \
"v_mfma_f32_16x16x32_bf16 v[96:99], v[132:135], a[220:223], v[96:99]\n" \
"ds_read_b64_tr_b16 v[170:171], v225 offset:34056\n" \
"v_mfma_f32_16x16x32_bf16 v[4:7], v[136:139], a[192:195], v[4:7]\n" \
"buffer_load_dwordx4 v218, %[srdw], s49 offen offset:3072 lds\n" \
"v_mfma_f32_16x16x32_bf16 v[4:7], v[140:143], a[208:211], v[4:7]\n" \
"ds_read_b64_tr_b16 v[172:173], v225 offset:41992\n" \
"v_mfma_f32_16x16x32_bf16 v[36:39], v[136:139], a[196:199], v[36:39]\n" \
"ds_read_b64_tr_b16 v[174:175], v225 offset:42248\n" \
"v_mfma_f32_16x16x32_bf16 v[36:39], v[140:143], a[212:215], v[36:39]\n" \
"v_mfma_f32_16x16x32_bf16 v[68:71], v[136:139], a[200:203], v[68:71]\n" \
"ds_read_b64_tr_b16 v[176:177], v225 offset:34304\n" \
"v_mfma_f32_16x16x32_bf16 v[68:71], v[140:143], a[216:219], v[68:71]\n" \
"ds_read_b64_tr_b16 v[178:179], v225 offset:34560\n" \
"v_mfma_f32_16x16x32_bf16 v[100:103], v[136:139], a[204:207], v[100:103]\n" \
"buffer_load_dwordx4 a[160:163], v227, %[srdz], s50 offen offset:2048 sc1\n" \
"v_mfma_f32_16x16x32_bf16 v[100:103], v[140:143], a[220:223], v[100:103]\n" \
"ds_read_b64_tr_b16 v[180:181], v225 offset:42496\n" \
"v_mfma_f32_16x16x32_bf16 v[8:11], v[144:147], a[192:195], v[8:11]\n" \
"ds_read_b64_tr_b16 v[182:183], v225 offset:42752\n" \
"v_mfma_f32_16x16x32_bf16 v[8:11], v[148:151], a[208:211], v[8:11]\n" \
"v_mfma_f32_16x16x32_bf16 v[40:43], v[144:147], a[196:199], v[40:43]\n" \
"ds_read_b64_tr_b16 v[184:185], v225 offset:34312\n" \
"v_mfma_f32_16x16x32_bf16 v[40:43], v[148:151], a[212:215], v[40:43]\n" \
"ds_read_b64_tr_b16 v[186:187], v225 offset:34568\n" \
"v_mfma_f32_16x16x32_bf16 v[72:75], v[144:147], a[200:203], v[72:75]\n" \
"buffer_load_dwordx4 a[164:167], v219, %[srdz], s50 offen offset:2048 sc1\n" \
"v_mfma_f32_16x16x32_bf16 v[72:75], v[148:151], a[216:219], v[72:75]\n" \
"ds_read_b64_tr_b16 v[188:189], v225 offset:42504\n" \
"v_mfma_f32_16x16x32_bf16 v[104:107], v[144:147], a[204:207], v[104:107]\n" \
"ds_read_b64_tr_b16 v[190:191], v225 offset:42760\n" \
"v_mfma_f32_16x16x32_bf16 v[104:107], v[148:151], a[220:223], v[104:107]\n" \
"v_mfma_f32_16x16x32_bf16 v[12:15], v[152:155], a[192:195], v[12:15]\n" \
"buffer_load_dwordx4 a[168:171], v220, %[srdz], s50 offen offset:2048 sc1\n" \
"v_mfma_f32_16x16x32_bf16 v[12:15], v[156:159], a[208:211], v[12:15]\n" \
"v_mfma_f32_16x16x32_bf16 v[44:47], v[152:155], a[196:199], v[44:47]\n" \
"v_mfma_f32_16x16x32_bf16 v[44:47], v[156:159], a[212:215], v[44:47]\n" \
"v_mfma_f32_16x16x32_bf16 v[76:79], v[152:155], a[200:203], v[76:79]\n" \
"buffer_load_dwordx4 a[172:175], v221, %[srdz], s50 offen offset:2048 sc1\n" \
"v_mfma_f32_16x16x32_bf16 v[76:79], v[156:159], a[216:219], v[76:79]\n" \
"v_mfma_f32_16x16x32_bf16 v[108:111], v[152:155], a[204:207], v[108:111]\n" \
"v_mfma_f32_16x16x32_bf16 v[108:111], v[156:159], a[220:223], v[108:111]\n" \
"s_waitcnt vmcnt(28) lgkmcnt(0)\n" \
"s_barrier\n" \
"v_mfma_f32_16x16x32_bf16 v[16:19], v[160:163], a[192:195], v[16:19]\n" \
"ds_read_b64_tr_b16 v[128:129], v225 offset:49152\n" \
"v_mfma_f32_16x16x32_bf16 v[16:19], v[164:167], a[208:211], v[16:19]\n" \
"ds_read_b64_tr_b16 v[130:131], v225 offset:49408\n" \
"v_mfma_f32_16x16x32_bf16 v[48:51], v[160:163], a[196:199], v[48:51]\n" \
"s_add_u32 s49, s49, s55\n" \
"s_mov_b32 m0, s46\n" \
"s_nop 0\n" \
"buffer_load_dwordx4 v226, %[srdw], s49 offen offset:0 lds\n" \
"v_mfma_f32_16x16x32_bf16 v[48:51], v[164:167], a[212:215], v[48:51]\n" \
"ds_read_b64_tr_b16 v[132:133], v225 offset:57344\n" \
"v_mfma_f32_16x16x32_bf16 v[80:83], v[160:163], a[200:203], v[80:83]\n" \
"ds_read_b64_tr_b16 v[134:135], v225 offset:57600\n" \
"v_mfma_f32_16x16x32_bf16 v[80:83], v[164:167], a[216:219], v[80:83]\n" \
"v_mfma_f32_16x16x32_bf16 v[112:115], v[160:163], a[204:207], v[112:115]\n" \
"ds_read_b64_tr_b16 v[136:137], v225 offset:49160\n" \
"v_mfma_f32_16x16x32_bf16 v[112:115], v[164:167], a[220:223], v[112:115]\n" \
"ds_read_b64_tr_b16 v[138:139], v225 offset:49416\n" \
"v_mfma_f32_16x16x32_bf16 v[20:23], v[168:171], a[192:195], v[20:23]\n" \
"buffer_load_dwordx4 v216, %[srdw], s49 offen offset:1024 lds\n" \
"v_mfma_f32_16x16x32_bf16 v[20:23], v[172:175], a[208:211], v[20:23]\n" \
"ds_read_b64_tr_b16 v[140:141], v225 offset:57352\n" \
"v_mfma_f32_16x16x32_bf16 v[52:55], v[168:171], a[196:199], v[52:55]\n" \
"ds_read_b64_tr_b16 v[142:143], v225 offset:57608\n" \
"v_mfma_f32_16x16x32_bf16 v[52:55], v[172:175], a[212:215], v[52:55]\n" \
"v_mfma_f32_16x16x32_bf16 v[84:87], v[168:171], a[200:203], v[84:87]\n" \
"ds_read_b64_tr_b16 v[144:145], v225 offset:49664\n" \
"v_mfma_f32_16x16x32_bf16 v[84:87], v[172:175], a[216:219], v[84:87]\n" \
"ds_read_b64_tr_b16 v[146:147], v225 offset:49920\n" \
"v_mfma_f32_16x16x32_bf16 v[116:119], v[168:171], a[204:207], v[116:119]\n" \
"buffer_load_dwordx4 a[176:179], v227, %[srdz], s50 offen offset:3072 sc1\n" \
"v_mfma_f32_16x16x32_bf16 v[116:119], v[172:175], a[220:223], v[116:119]\n" \
"ds_read_b64_tr_b16 v[148:149], v225 offset:57856\n" \
"v_mfma_f32_16x16x32_bf16 v[24:27], v[176:179], a[192:195], v[24:27]\n" \
"ds_read_b64_tr_b16 v[150:151], v225 offset:58112\n" \
"v_mfma_f32_16x16x32_bf16 v[24:27], v[180:183], a[208:211], v[24:27]\n" \
"v_mfma_f32_16x16x32_bf16 v[56:59], v[176:179], a[196:199], v[56:59]\n" \
"ds_read_b64_tr_b16 v[152:153], v225 offset:49672\n" \
"v_mfma_f32_16x16x32_bf16 v[56:59], v[180:183], a[212:215], v[56:59]\n" \
"ds_read_b64_tr_b16 v[154:155], v225 offset:49928\n" \
"v_mfma_f32_16x16x32_bf16 v[88:91], v[176:179], a[200:203], v[88:91]\n" \
"buffer_load_dwordx4 a[180:183], v219, %[srdz], s50 offen offset:3072 sc1\n" \
"v_mfma_f32_16x16x32_bf16 v[88:91], v[180:183], a[216:219], v[88:91]\n" \
"ds_read_b64_tr_b16 v[156:157], v225 offset:57864\n" \
"v_mfma_f32_16x16x32_bf16 v[120:123], v[176:179], a[204:207], v[120:123]\n" \
"ds_read_b64_tr_b16 v[158:159], v225 offset:58120\n" \
"v_mfma_f32_16x16x32_bf16 v[120:123], v[180:183], a[220:223], v[120:123]\n" \
"v_mfma_f32_16x16x32_bf16 v[28:31], v[184:187], a[192:195], v[28:31]\n" \
"buffer_load_dwordx4 a[184:187], v220, %[srdz], s50 offen offset:3072 sc1\n" \
"v_mfma_f32_16x16x32_bf16 v[28:31], v[188:191], a[208:211], v[28:31]\n" \
"v_mfma_f32_16x16x32_bf16 v[60:63], v[184:187], a[196:199], v[60:63]\n" \
"v_mfma_f32_16x16x32_bf16 v[60:63], v[188:191], a[212:215], v[60:63]\n" \
"v_mfma_f32_16x16x32_bf16 v[92:95], v[184:187], a[200:203], v[92:95]\n" \
"buffer_load_dwordx4 a[188:191], v221, %[srdz], s50 offen offset:3072 sc1\n" \
"v_mfma_f32_16x16x32_bf16 v[92:95], v[188:191], a[216:219], v[92:95]\n" \
"v_mfma_f32_16x16x32_bf16 v[124:127], v[184:187], a[204:207], v[124:127]\n" \
"v_mfma_f32_16x16x32_bf16 v[124:127], v[188:191], a[220:223], v[124:127]\n" \
"s_waitcnt vmcnt(24) lgkmcnt(0)\n" \
"v_mfma_f32_16x16x32_bf16 v[0:3], v[128:131], a[224:227], v[0:3]\n" \
"ds_read_b64_tr_b16 v[160:161], v225 offset:50176\n" \
"v_mfma_f32_16x16x32_bf16 v[0:3], v[132:135], a[240:243], v[0:3]\n" \
"ds_read_b64_tr_b16 v[162:163], v225 offset:50432\n" \
"v_mfma_f32_16x16x32_bf16 v[32:35], v[128:131], a[228:231], v[32:35]\n" \
"s_mov_b32 m0, s46\n" \
"s_nop 0\n" \
"buffer_load_dwordx4 v217, %[srdw], s49 offen offset:2048 lds\n" \
"v_mfma_f32_16x16x32_bf16 v[32:35], v[132:135], a[244:247], v[32:35]\n" \
"ds_read_b64_tr_b16 v[164:165], v225 offset:58368\n" \
"v_mfma_f32_16x16x32_bf16 v[64:67], v[128:131], a[232:235], v[64:67]\n" \
"ds_read_b64_tr_b16 v[166:167], v225 offset:58624\n" \
"v_mfma_f32_16x16x32_bf16 v[64:67], v[132:135], a[248:251], v[64:67]\n" \
"v_mfma_f32_16x16x32_bf16 v[96:99], v[128:131], a[236:239], v[96:99]\n" \
"ds_read_b64_tr_b16 v[168:169], v225 offset:50184\n" \
"v_mfma_f32_16x16x32_bf16 v[96:99], v[132:135], a[252:255], v[96:99]\n" \
"ds_read_b64_tr_b16 v[170:171], v225 offset:50440\n" \
"v_mfma_f32_16x16x32_bf16 v[4:7], v[136:139], a[224:227], v[4:7]\n" \
"buffer_load_dwordx4 v218, %[srdw], s49 offen offset:3072 lds\n" \
"v_mfma_f32_16x16x32_bf16 v[4:7], v[140:143], a[240:243], v[4:7]\n" \
"ds_read_b64_tr_b16 v[172:173], v225 offset:58376\n" \
"v_mfma_f32_16x16x32_bf16 v[36:39], v[136:139], a[228:231], v[36:39]\n" \
"ds_read_b64_tr_b16 v[174:175], v225 offset:58632\n" \
"v_mfma_f32_16x16x32_bf16 v[36:39], v[140:143], a[244:247], v[36:39]\n" \
"v_mfma_f32_16x16x32_bf16 v[68:71], v[136:139], a[232:235], v[68:71]\n" \
"ds_read_b64_tr_b16 v[176:177], v225 offset:50688\n" \
"v_mfma_f32_16x16x32_bf16 v[68:71], v[140:143], a[248:251], v[68:71]\n" \
"ds_read_b64_tr_b16 v[178:179], v225 offset:50944\n" \
"v_mfma_f32_16x16x32_bf16 v[100:103], v[136:139], a[236:239], v[100:103]\n" \
"s_add_u32 s50, s50, s53\n" \
"buffer_load_dwordx4 a[192:195], v227, %[srdz], s50 offen offset:0 sc1\n" \
"v_mfma_f32_16x16x32_bf16 v[100:103], v[140:143], a[252:255], v[100:103]\n" \
"ds_read_b64_tr_b16 v[180:181], v225 offset:58880\n" \
"v_mfma_f32_16x16x32_bf16 v[8:11], v[144:147], a[224:227], v[8:11]\n" \
"ds_read_b64_tr_b16 v[182:183], v225 offset:59136\n" \
"v_mfma_f32_16x16x32_bf16 v[8:11], v[148:151], a[240:243], v[8:11]\n" \
"v_mfma_f32_16x16x32_bf16 v[40:43], v[144:147], a[228:231], v[40:43]\n" \
"ds_read_b64_tr_b16 v[184:185], v225 offset:50696\n" \
"v_mfma_f32_16x16x32_bf16 v[40:43], v[148:151], a[244:247], v[40:43]\n" \
"ds_read_b64_tr_b16 v[186:187], v225 offset:50952\n" \
"v_mfma_f32_16x16x32_bf16 v[72:75], v[144:147], a[232:235], v[72:75]\n" \
"buffer_load_dwordx4 a[196:199], v219, %[srdz], s50 offen offset:0 sc1\n" \
"v_mfma_f32_16x16x32_bf16 v[72:75], v[148:151], a[248:251], v[72:75]\n" \
"ds_read_b64_tr_b16 v[188:189], v225 offset:58888\n" \
"v_mfma_f32_16x16x32_bf16 v[104:107], v[144:147], a[236:239], v[104:107]\n" \
"ds_read_b64_tr_b16 v[190:191], v225 offset:59144\n" \
"v_mfma_f32_16x16x32_bf16 v[104:107], v[148:151], a[252:255], v[104:107]\n" \
"v_mfma_f32_16x16x32_bf16 v[12:15], v[152:155], a[224:227], v[12:15]\n" \
"buffer_load_dwordx4 a[200:203], v220, %[srdz], s50 offen offset:0 sc1\n" \
"v_mfma_f32_16x16x32_bf16 v[12:15], v[156:159], a[240:243], v[12:15]\n" \
"v_mfma_f32_16x16x32_bf16 v[44:47], v[152:155], a[228:231], v[44:47]\n" \
"buffer_load_dword v237, v236, %[srdw], s60 offen\n" \
"s_add_u32 s60, s60, 0x200000\n" \
"v_mfma_f32_16x16x32_bf16 v[44:47], v[156:159], a[244:247], v[44:47]\n" \
".if . - adaln_body != 4096\n" \
".error \"size model\"\n" \
".endif\n" \
"Lk0_%=:\n" \
"s_cbranch_execz Lk1_%=\n" \
"s_nop 0\n" \
"v_mfma_f32_16x16x32_bf16 v[76:79], v[152:155], a[232:235], v[76:79]\n" \
"buffer_load_dwordx4 a[204:207], v221, %[srdz], s50 offen offset:0 sc1\n" \
"v_mfma_f32_16x16x32_bf16 v[76:79], v[156:159], a[248:251], v[76:79]\n" \
"v_mfma_f32_16x16x32_bf16 v[108:111], v[152:155], a[236:239], v[108:111]\n" \
"v_mfma_f32_16x16x32_bf16 v[108:111], v[156:159], a[252:255], v[108:111]\n" \
"s_waitcnt vmcnt(29) lgkmcnt(0)\n" \
"s_barrier\n" \
"v_mfma_f32_16x16x32_bf16 v[16:19], v[160:163], a[224:227], v[16:19]\n" \
"ds_read_b64_tr_b16 v[128:129], v225 offset:0\n" \
"v_mfma_f32_16x16x32_bf16 v[16:19], v[164:167], a[240:243], v[16:19]\n" \
"ds_read_b64_tr_b16 v[130:131], v225 offset:256\n" \
"v_mfma_f32_16x16x32_bf16 v[48:51], v[160:163], a[228:231], v[48:51]\n" \
"s_add_u32 s49, s49, s55\n" \
"s_mov_b32 m0, s47\n" \
"s_nop 0\n" \
"buffer_load_dwordx4 v226, %[srdw], s49 offen offset:0 lds\n" \
"v_mfma_f32_16x16x32_bf16 v[48:51], v[164:167], a[244:247], v[48:51]\n" \
"ds_read_b64_tr_b16 v[132:133], v225 offset:8192\n" \
"v_mfma_f32_16x16x32_bf16 v[80:83], v[160:163], a[232:235], v[80:83]\n" \
"ds_read_b64_tr_b16 v[134:135], v225 offset:8448\n" \
"v_mfma_f32_16x16x32_bf16 v[80:83], v[164:167], a[248:251], v[80:83]\n" \
"v_mfma_f32_16x16x32_bf16 v[112:115], v[160:163], a[236:239], v[112:115]\n" \
"ds_read_b64_tr_b16 v[136:137], v225 offset:8\n" \
"v_mfma_f32_16x16x32_bf16 v[112:115], v[164:167], a[252:255], v[112:115]\n" \
"ds_read_b64_tr_b16 v[138:139], v225 offset:264\n" \
"v_mfma_f32_16x16x32_bf16 v[20:23], v[168:171], a[224:227], v[20:23]\n" \
"buffer_load_dwordx4 v216, %[srdw], s49 offen offset:1024 lds\n" \
"v_mfma_f32_16x16x32_bf16 v[20:23], v[172:175], a[240:243], v[20:23]\n" \
"ds_read_b64_tr_b16 v[140:141], v225 offset:8200\n" \
"v_mfma_f32_16x16x32_bf16 v[52:55], v[168:171], a[228:231], v[52:55]\n" \
"ds_read_b64_tr_b16 v[142:143], v225 offset:8456\n" \
"v_mfma_f32_16x16x32_bf16 v[52:55], v[172:175], a[244:247], v[52:55]\n" \
"v_mfma_f32_16x16x32_bf16 v[84:87], v[168:171], a[232:235], v[84:87]\n" \
"ds_read_b64_tr_b16 v[144:145], v225 offset:512\n" \
"v_mfma_f32_16x16x32_bf16 v[84:87], v[172:175], a[248:251], v[84:87]\n" \
"ds_read_b64_tr_b16 v[146:147], v225 offset:768\n" \
"v_mfma_f32_16x16x32_bf16 v[116:119], v[168:171], a[236:239], v[116:119]\n" \
"buffer_load_dwordx4 a[208:211], v227, %[srdz], s50 offen offset:1024 sc1\n" \
"v_mfma_f32_16x16x32_bf16 v[116:119], v[172:175], a[252:255], v[116:119]\n" \
"ds_read_b64_tr_b16 v[148:149], v225 offset:8704\n" \
"v_mfma_f32_16x16x32_bf16 v[24:27], v[176:179], a[224:227], v[24:27]\n" \
"ds_read_b64_tr_b16 v[150:151], v225 offset:8960\n" \
"v_mfma_f32_16x16x32_bf16 v[24:27], v[180:183], a[240:243], v[24:27]\n" \
"v_mfma_f32_16x16x32_bf16 v[56:59], v[176:179], a[228:231], v[56:59]\n" \
"ds_read_b64_tr_b16 v[152:153], v225 offset:520\n" \
"v_mfma_f32_16x16x32_bf16 v[56:59], v[180:183], a[244:247], v[56:59]\n" \
"ds_read_b64_tr_b16 v[154:155], v225 offset:776\n" \
"v_mfma_f32_16x16x32_bf16 v[88:91], v[176:179], a[232:235], v[88:91]\n" \
"buffer_load_dwordx4 a[212:215], v219, %[srdz], s50 offen offset:1024 sc1\n" \
"v_mfma_f32_16x16x32_bf16 v[88:91], v[180:183], a[248:251], v[88:91]\n" \
"ds_read_b64_tr_b16 v[156:157], v225 offset:8712\n" \
"v_mfma_f32_16x16x32_bf16 v[120:123], v[176:179], a[236:239], v[120:123]\n" \
"ds_read_b64_tr_b16 v[158:159], v225 offset:8968\n" \
"v_mfma_f32_16x16x32_bf16 v[120:123], v[180:183], a[252:255], v[120:123]\n" \
"v_mfma_f32_16x16x32_bf16 v[28:31], v[184:187], a[224:227], v[28:31]\n" \
"buffer_load_dwordx4 a[216:219], v220, %[srdz], s50 offen offset:1024 sc1\n" \
"v_mfma_f32_16x16x32_bf16 v[28:31], v[188:191], a[240:243], v[28:31]\n" \
"v_mfma_f32_16x16x32_bf16 v[60:63], v[184:187], a[228:231], v[60:63]\n" \
"v_mfma_f32_16x16x32_bf16 v[60:63], v[188:191], a[244:247], v[60:63]\n" \
"v_mfma_f32_16x16x32_bf16 v[92:95], v[184:187], a[232:235], v[92:95]\n" \
"buffer_load_dwordx4 a[220:223], v221, %[srdz], s50 offen offset:1024 sc1\n" \
"v_mfma_f32_16x16x32_bf16 v[92:95], v[188:191], a[248:251], v[92:95]\n" \
"v_mfma_f32_16x16x32_bf16 v[124:127], v[184:187], a[236:239], v[124:127]\n" \
"v_mfma_f32_16x16x32_bf16 v[124:127], v[188:191], a[252:255], v[124:127]\n" \
"s_waitcnt vmcnt(25) lgkmcnt(0)\n" \
"s_sub_u32 s51, s51, 1\n" \
"s_cmp_lg_u32 s51, 0\n" \
"s_cbranch_scc1 Lq_%=\n" \
"s_nop 0\n" \
"v_mfma_f32_16x16x32_bf16 v[0:3], v[128:131], a[128:131], v[0:3]\n" \
"ds_read_b64_tr_b16 v[160:161], v225 offset:1024\n" \
"v_mfma_f32_16x16x32_bf16 v[0:3], v[132:135], a[144:147], v[0:3]\n" \
"ds_read_b64_tr_b16 v[162:163], v225 offset:1280\n" \
"v_mfma_f32_16x16x32_bf16 v[32:35], v[128:131], a[132:135], v[32:35]\n" \
"s_mov_b32 m0, s47\n" \
"s_nop 0\n" \
"buffer_load_dwordx4 v217, %[srdw], s49 offen offset:2048 lds\n" \
"v_mfma_f32_16x16x32_bf16 v[32:35], v[132:135], a[148:151], v[32:35]\n" \
"ds_read_b64_tr_b16 v[164:165], v225 offset:9216\n" \
"v_mfma_f32_16x16x32_bf16 v[64:67], v[128:131], a[136:139], v[64:67]\n" \
"ds_read_b64_tr_b16 v[166:167], v225 offset:9472\n" \
"v_mfma_f32_16x16x32_bf16 v[64:67], v[132:135], a[152:155], v[64:67]\n" \
"v_mfma_f32_16x16x32_bf16 v[96:99], v[128:131], a[140:143], v[96:99]\n" \
"ds_read_b64_tr_b16 v[168:169], v225 offset:1032\n" \
"v_mfma_f32_16x16x32_bf16 v[96:99], v[132:135], a[156:159], v[96:99]\n" \
"ds_read_b64_tr_b16 v[170:171], v225 offset:1288\n" \
"v_mfma_f32_16x16x32_bf16 v[4:7], v[136:139], a[128:131], v[4:7]\n" \
"buffer_load_dwordx4 v218, %[srdw], s49 offen offset:3072 lds\n" \
"v_mfma_f32_16x16x32_bf16 v[4:7], v[140:143], a[144:147], v[4:7]\n" \
"ds_read_b64_tr_b16 v[172:173], v225 offset:9224\n" \
"v_mfma_f32_16x16x32_bf16 v[36:39], v[136:139], a[132:135], v[36:39]\n" \
"ds_read_b64_tr_b16 v[174:175], v225 offset:9480\n" \
"v_mfma_f32_16x16x32_bf16 v[36:39], v[140:143], a[148:151], v[36:39]\n" \
"v_mfma_f32_16x16x32_bf16 v[68:71], v[136:139], a[136:139], v[68:71]\n" \
"ds_read_b64_tr_b16 v[176:177], v225 offset:1536\n" \
"v_mfma_f32_16x16x32_bf16 v[68:71], v[140:143], a[152:155], v[68:71]\n" \
"ds_read_b64_tr_b16 v[178:179], v225 offset:1792\n" \
"v_mfma_f32_16x16x32_bf16 v[100:103], v[136:139], a[140:143], v[100:103]\n" \
"buffer_load_dwordx4 a[224:227], v227, %[srdz], s50 offen offset:2048 sc1\n" \
"v_mfma_f32_16x16x32_bf16 v[100:103], v[140:143], a[156:159], v[100:103]\n" \
"ds_read_b64_tr_b16 v[180:181], v225 offset:9728\n" \
"v_mfma_f32_16x16x32_bf16 v[8:11], v[144:147], a[128:131], v[8:11]\n" \
"ds_read_b64_tr_b16 v[182:183], v225 offset:9984\n" \
"v_mfma_f32_16x16x32_bf16 v[8:11], v[148:151], a[144:147], v[8:11]\n" \
"buffer_load_dwordx4 v[230:233], off, %[srdz], 0 offset:128 sc1\n" \
"v_mfma_f32_16x16x32_bf16 v[40:43], v[144:147], a[132:135], v[40:43]\n" \
"ds_read_b64_tr_b16 v[184:185], v225 offset:1544\n" \
"v_mfma_f32_16x16x32_bf16 v[40:43], v[148:151], a[148:151], v[40:43]\n" \
"ds_read_b64_tr_b16 v[186:187], v225 offset:1800\n" \
"v_mfma_f32_16x16x32_bf16 v[72:75], v[144:147], a[136:139], v[72:75]\n" \
"buffer_load_dwordx4 a[228:231], v219, %[srdz], s50 offen offset:2048 sc1\n" \
"v_mfma_f32_16x16x32_bf16 v[72:75], v[148:151], a[152:155], v[72:75]\n" \
"ds_read_b64_tr_b16 v[188:189], v225 offset:9736\n" \
"v_mfma_f32_16x16x32_bf16 v[104:107], v[144:147], a[140:143], v[104:107]\n" \
"ds_read_b64_tr_b16 v[190:191], v225 offset:9992\n" \
"v_mfma_f32_16x16x32_bf16 v[104:107], v[148:151], a[156:159], v[104:107]\n" \
"v_mfma_f32_16x16x32_bf16 v[12:15], v[152:155], a[128:131], v[12:15]\n" \
"buffer_load_dwordx4 a[232:235], v220, %[srdz], s50 offen offset:2048 sc1\n" \
"v_mfma_f32_16x16x32_bf16 v[12:15], v[156:159], a[144:147], v[12:15]\n" \
"v_mfma_f32_16x16x32_bf16 v[44:47], v[152:155], a[132:135], v[44:47]\n" \
"v_mfma_f32_16x16x32_bf16 v[44:47], v[156:159], a[148:151], v[44:47]\n" \
"v_mfma_f32_16x16x32_bf16 v[76:79], v[152:155], a[136:139], v[76:79]\n" \
"buffer_load_dwordx4 a[236:239], v221, %[srdz], s50 offen offset:2048 sc1\n" \
"v_mfma_f32_16x16x32_bf16 v[76:79], v[156:159], a[152:155], v[76:79]\n" \
"v_mfma_f32_16x16x32_bf16 v[108:111], v[152:155], a[140:143], v[108:111]\n" \
"v_mfma_f32_16x16x32_bf16 v[108:111], v[156:159], a[156:159], v[108:111]\n" \
"s_waitcnt vmcnt(30) lgkmcnt(0)\n" \
"s_barrier\n" \
"v_mfma_f32_16x16x32_bf16 v[16:19], v[160:163], a[128:131], v[16:19]\n" \
"ds_read_b64_tr_b16 v[128:129], v225 offset:16384\n" \
"v_mfma_f32_16x16x32_bf16 v[16:19], v[164:167], a[144:147], v[16:19]\n" \
"ds_read_b64_tr_b16 v[130:131], v225 offset:16640\n" \
"v_mfma_f32_16x16x32_bf16 v[48:51], v[160:163], a[132:135], v[48:51]\n" \
"s_mov_b32 s48, 16384\n" \
"s_mov_b32 m0, s44\n" \
"s_nop 0\n" \
"buffer_load_dwordx4 %[vd0], %[srdv], s48 offen offset:0 lds\n" \
"v_mfma_f32_16x16x32_bf16 v[48:51], v[164:167], a[148:151], v[48:51]\n" \
"ds_read_b64_tr_b16 v[132:133], v225 offset:24576\n" \
"v_mfma_f32_16x16x32_bf16 v[80:83], v[160:163], a[136:139], v[80:83]\n" \
"ds_read_b64_tr_b16 v[134:135], v225 offset:24832\n" \
"v_mfma_f32_16x16x32_bf16 v[80:83], v[164:167], a[152:155], v[80:83]\n" \
"v_mfma_f32_16x16x32_bf16 v[112:115], v[160:163], a[140:143], v[112:115]\n" \
"ds_read_b64_tr_b16 v[136:137], v225 offset:16392\n" \
"v_mfma_f32_16x16x32_bf16 v[112:115], v[164:167], a[156:159], v[112:115]\n" \
"ds_read_b64_tr_b16 v[138:139], v225 offset:16648\n" \
"v_mfma_f32_16x16x32_bf16 v[20:23], v[168:171], a[128:131], v[20:23]\n" \
"buffer_load_dwordx4 v215, %[srdv], s48 offen offset:1024 lds\n" \
"v_mfma_f32_16x16x32_bf16 v[20:23], v[172:175], a[144:147], v[20:23]\n" \
"ds_read_b64_tr_b16 v[140:141], v225 offset:24584\n" \
"v_mfma_f32_16x16x32_bf16 v[52:55], v[168:171], a[132:135], v[52:55]\n" \
"ds_read_b64_tr_b16 v[142:143], v225 offset:24840\n" \
"v_mfma_f32_16x16x32_bf16 v[52:55], v[172:175], a[148:151], v[52:55]\n" \
"v_mfma_f32_16x16x32_bf16 v[84:87], v[168:171], a[136:139], v[84:87]\n" \
"ds_read_b64_tr_b16 v[144:145], v225 offset:16896\n" \
"v_mfma_f32_16x16x32_bf16 v[84:87], v[172:175], a[152:155], v[84:87]\n" \
"ds_read_b64_tr_b16 v[146:147], v225 offset:17152\n" \
"v_mfma_f32_16x16x32_bf16 v[116:119], v[168:171], a[140:143], v[116:119]\n" \
"buffer_load_dwordx4 a[240:243], v227, %[srdz], s50 offen offset:3072 sc1\n" \
"v_mfma_f32_16x16x32_bf16 v[116:119], v[172:175], a[156:159], v[116:119]\n" \
"ds_read_b64_tr_b16 v[148:149], v225 offset:25088\n" \
"v_mfma_f32_16x16x32_bf16 v[24:27], v[176:179], a[128:131], v[24:27]\n" \
"ds_read_b64_tr_b16 v[150:151], v225 offset:25344\n" \
"v_mfma_f32_16x16x32_bf16 v[24:27], v[180:183], a[144:147], v[24:27]\n" \
"v_mfma_f32_16x16x32_bf16 v[56:59], v[176:179], a[132:135], v[56:59]\n" \
"ds_read_b64_tr_b16 v[152:153], v225 offset:16904\n" \
"v_mfma_f32_16x16x32_bf16 v[56:59], v[180:183], a[148:151], v[56:59]\n" \
"ds_read_b64_tr_b16 v[154:155], v225 offset:17160\n" \
"v_mfma_f32_16x16x32_bf16 v[88:91], v[176:179], a[136:139], v[88:91]\n" \
"buffer_load_dwordx4 a[244:247], v219, %[srdz], s50 offen offset:3072 sc1\n" \
"v_mfma_f32_16x16x32_bf16 v[88:91], v[180:183], a[152:155], v[88:91]\n" \
"ds_read_b64_tr_b16 v[156:157], v225 offset:25096\n" \
"v_mfma_f32_16x16x32_bf16 v[120:123], v[176:179], a[140:143], v[120:123]\n" \
"ds_read_b64_tr_b16 v[158:159], v225 offset:25352\n" \
"v_mfma_f32_16x16x32_bf16 v[120:123], v[180:183], a[156:159], v[120:123]\n" \
"v_mfma_f32_16x16x32_bf16 v[28:31], v[184:187], a[128:131], v[28:31]\n" \
"buffer_load_dwordx4 a[248:251], v220, %[srdz], s50 offen offset:3072 sc1\n" \
"v_mfma_f32_16x16x32_bf16 v[28:31], v[188:191], a[144:147], v[28:31]\n" \
"v_mfma_f32_16x16x32_bf16 v[60:63], v[184:187], a[132:135], v[60:63]\n" \
"v_mfma_f32_16x16x32_bf16 v[60:63], v[188:191], a[148:151], v[60:63]\n" \
"v_mfma_f32_16x16x32_bf16 v[92:95], v[184:187], a[136:139], v[92:95]\n" \
"buffer_load_dwordx4 a[252:255], v221, %[srdz], s50 offen offset:3072 sc1\n" \
"v_mfma_f32_16x16x32_bf16 v[92:95], v[188:191], a[152:155], v[92:95]\n" \
"v_mfma_f32_16x16x32_bf16 v[124:127], v[184:187], a[140:143], v[124:127]\n" \
"v_mfma_f32_16x16x32_bf16 v[124:127], v[188:191], a[156:159], v[124:127]\n" \
"s_waitcnt vmcnt(26) lgkmcnt(0)\n" \
"v_mfma_f32_16x16x32_bf16 v[0:3], v[128:131], a[160:163], v[0:3]\n" \
"ds_read_b64_tr_b16 v[160:161], v225 offset:17408\n" \
"v_mfma_f32_16x16x32_bf16 v[0:3], v[132:135], a[176:179], v[0:3]\n" \
"ds_read_b64_tr_b16 v[162:163], v225 offset:17664\n" \
"v_mfma_f32_16x16x32_bf16 v[32:35], v[128:131], a[164:167], v[32:35]\n" \
"s_mov_b32 s48, 16384\n" \
"s_mov_b32 m0, s44\n" \
"s_nop 0\n" \
"buffer_load_dwordx4 %[vd0], %[srdv], s48 offen offset:2048 lds\n" \
"v_mfma_f32_16x16x32_bf16 v[32:35], v[132:135], a[180:183], v[32:35]\n" \
"ds_read_b64_tr_b16 v[164:165], v225 offset:25600\n" \
"v_mfma_f32_16x16x32_bf16 v[64:67], v[128:131], a[168:171], v[64:67]\n" \
"ds_read_b64_tr_b16 v[166:167], v225 offset:25856\n" \
"v_mfma_f32_16x16x32_bf16 v[64:67], v[132:135], a[184:187], v[64:67]\n" \
"v_mfma_f32_16x16x32_bf16 v[96:99], v[128:131], a[172:175], v[96:99]\n" \
"ds_read_b64_tr_b16 v[168:169], v225 offset:17416\n" \
"v_mfma_f32_16x16x32_bf16 v[96:99], v[132:135], a[188:191], v[96:99]\n" \
"ds_read_b64_tr_b16 v[170:171], v225 offset:17672\n" \
"v_mfma_f32_16x16x32_bf16 v[4:7], v[136:139], a[160:163], v[4:7]\n" \
"buffer_load_dwordx4 v215, %[srdv], s48 offen offset:3072 lds\n" \
"v_mfma_f32_16x16x32_bf16 v[4:7], v[140:143], a[176:179], v[4:7]\n" \
"ds_read_b64_tr_b16 v[172:173], v225 offset:25608\n" \
"v_mfma_f32_16x16x32_bf16 v[36:39], v[136:139], a[164:167], v[36:39]\n" \
"ds_read_b64_tr_b16 v[174:175], v225 offset:25864\n" \
"v_mfma_f32_16x16x32_bf16 v[36:39], v[140:143], a[180:183], v[36:39]\n" \
"v_mfma_f32_16x16x32_bf16 v[68:71], v[136:139], a[168:171], v[68:71]\n" \
"ds_read_b64_tr_b16 v[176:177], v225 offset:17920\n" \
"v_mfma_f32_16x16x32_bf16 v[68:71], v[140:143], a[184:187], v[68:71]\n" \
"ds_read_b64_tr_b16 v[178:179], v225 offset:18176\n" \
"v_mfma_f32_16x16x32_bf16 v[100:103], v[136:139], a[172:175], v[100:103]\n" \
"s_mov_b32 s48, 0\n" \
"s_mov_b32 m0, s40\n" \
"s_nop 0\n" \
"buffer_load_dwordx4 %[kd0], %[srdk], s48 offen offset:0 lds\n" \
"v_mfma_f32_16x16x32_bf16 v[100:103], v[140:143], a[188:191], v[100:103]\n" \
"ds_read_b64_tr_b16 v[180:181], v225 offset:26112\n" \
"v_mfma_f32_16x16x32_bf16 v[8:11], v[144:147], a[160:163], v[8:11]\n" \
"ds_read_b64_tr_b16 v[182:183], v225 offset:26368\n" \
"v_mfma_f32_16x16x32_bf16 v[8:11], v[148:151], a[176:179], v[8:11]\n" \
"v_mfma_f32_16x16x32_bf16 v[40:43], v[144:147], a[164:167], v[40:43]\n" \
"ds_read_b64_tr_b16 v[184:185], v225 offset:17928\n" \
"v_mfma_f32_16x16x32_bf16 v[40:43], v[148:151], a[180:183], v[40:43]\n" \
"ds_read_b64_tr_b16 v[186:187], v225 offset:18184\n" \
"v_mfma_f32_16x16x32_bf16 v[72:75], v[144:147], a[168:171], v[72:75]\n" \
"buffer_load_dwordx4 v212, %[srdk], s48 offen offset:1024 lds\n" \
"v_mfma_f32_16x16x32_bf16 v[72:75], v[148:151], a[184:187], v[72:75]\n" \
"ds_read_b64_tr_b16 v[188:189], v225 offset:26120\n" \
"v_mfma_f32_16x16x32_bf16 v[104:107], v[144:147], a[172:175], v[104:107]\n" \
"ds_read_b64_tr_b16 v[190:191], v225 offset:26376\n" \
"v_mfma_f32_16x16x32_bf16 v[104:107], v[148:151], a[188:191], v[104:107]\n" \
"v_mfma_f32_16x16x32_bf16 v[12:15], v[152:155], a[160:163], v[12:15]\n" \
"buffer_load_dwordx4 v213, %[srdk], s48 offen offset:2048 lds\n" \
"v_mfma_f32_16x16x32_bf16 v[12:15], v[156:159], a[176:179], v[12:15]\n" \
"v_mfma_f32_16x16x32_bf16 v[44:47], v[152:155], a[164:167], v[44:47]\n" \
"v_mfma_f32_16x16x32_bf16 v[44:47], v[156:159], a[180:183], v[44:47]\n" \
"v_mfma_f32_16x16x32_bf16 v[76:79], v[152:155], a[168:171], v[76:79]\n" \
"buffer_load_dwordx4 v214, %[srdk], s48 offen offset:3072 lds\n" \
"v_mfma_f32_16x16x32_bf16 v[76:79], v[156:159], a[184:187], v[76:79]\n" \
"v_mfma_f32_16x16x32_bf16 v[108:111], v[152:155], a[172:175], v[108:111]\n" \
"v_mfma_f32_16x16x32_bf16 v[108:111], v[156:159], a[188:191], v[108:111]\n" \
"s_waitcnt vmcnt(30) lgkmcnt(0)\n" \
"s_barrier\n" \
"v_mfma_f32_16x16x32_bf16 v[16:19], v[160:163], a[160:163], v[16:19]\n" \
"ds_read_b64_tr_b16 v[128:129], v225 offset:32768\n" \
"v_mfma_f32_16x16x32_bf16 v[16:19], v[164:167], a[176:179], v[16:19]\n" \
"ds_read_b64_tr_b16 v[130:131], v225 offset:33024\n" \
"v_mfma_f32_16x16x32_bf16 v[48:51], v[160:163], a[164:167], v[48:51]\n" \
"s_mov_b32 s48, 32768\n" \
"s_mov_b32 m0, s45\n" \
"s_nop 0\n" \
"buffer_load_dwordx4 %[vd0], %[srdv], s48 offen offset:0 lds\n" \
"v_mfma_f32_16x16x32_bf16 v[48:51], v[164:167], a[180:183], v[48:51]\n" \
"ds_read_b64_tr_b16 v[132:133], v225 offset:40960\n" \
"v_mfma_f32_16x16x32_bf16 v[80:83], v[160:163], a[168:171], v[80:83]\n" \
"ds_read_b64_tr_b16 v[134:135], v225 offset:41216\n" \
"v_mfma_f32_16x16x32_bf16 v[80:83], v[164:167], a[184:187], v[80:83]\n" \
"v_mfma_f32_16x16x32_bf16 v[112:115], v[160:163], a[172:175], v[112:115]\n" \
"ds_read_b64_tr_b16 v[136:137], v225 offset:32776\n" \
"v_mfma_f32_16x16x32_bf16 v[112:115], v[164:167], a[188:191], v[112:115]\n" \
"ds_read_b64_tr_b16 v[138:139], v225 offset:33032\n" \
"v_mfma_f32_16x16x32_bf16 v[20:23], v[168:171], a[160:163], v[20:23]\n" \
"buffer_load_dwordx4 v215, %[srdv], s48 offen offset:1024 lds\n" \
"v_mfma_f32_16x16x32_bf16 v[20:23], v[172:175], a[176:179], v[20:23]\n" \
"ds_read_b64_tr_b16 v[140:141], v225 offset:40968\n" \
"v_mfma_f32_16x16x32_bf16 v[52:55], v[168:171], a[164:167], v[52:55]\n" \
"ds_read_b64_tr_b16 v[142:143], v225 offset:41224\n" \
"v_mfma_f32_16x16x32_bf16 v[52:55], v[172:175], a[180:183], v[52:55]\n" \
"v_mfma_f32_16x16x32_bf16 v[84:87], v[168:171], a[168:171], v[84:87]\n" \
"ds_read_b64_tr_b16 v[144:145], v225 offset:33280\n" \
"v_mfma_f32_16x16x32_bf16 v[84:87], v[172:175], a[184:187], v[84:87]\n" \
"ds_read_b64_tr_b16 v[146:147], v225 offset:33536\n" \
"v_mfma_f32_16x16x32_bf16 v[116:119], v[168:171], a[172:175], v[116:119]\n" \
"s_mov_b32 s48, 16384\n" \
"s_mov_b32 m0, s41\n" \
"s_nop 0\n" \
"buffer_load_dwordx4 %[kd0], %[srdk], s48 offen offset:0 lds\n" \
"v_mfma_f32_16x16x32_bf16 v[116:119], v[172:175], a[188:191], v[116:119]\n" \
"ds_read_b64_tr_b16 v[148:149], v225 offset:41472\n" \
"v_mfma_f32_16x16x32_bf16 v[24:27], v[176:179], a[160:163], v[24:27]\n" \
"ds_read_b64_tr_b16 v[150:151], v225 offset:41728\n" \
"v_mfma_f32_16x16x32_bf16 v[24:27], v[180:183], a[176:179], v[24:27]\n" \
"s_waitcnt vmcnt(18)\n" \
"v_cmp_eq_u64_e32 vcc, v[230:231], v[234:235]\n" \
"v_cmp_eq_u64_e64 s[56:57], v[232:233], v[234:235]\n" \
"s_nop 3\n" \
"s_or_b64 s[58:59], vcc, s[56:57]\n" \
"s_cbranch_scc0 Lnsp_%=\n" \
"Lnbk_%=:\n" \
"v_mfma_f32_16x16x32_bf16 v[56:59], v[176:179], a[164:167], v[56:59]\n" \
"ds_read_b64_tr_b16 v[152:153], v225 offset:33288\n" \
"v_mfma_f32_16x16x32_bf16 v[56:59], v[180:183], a[180:183], v[56:59]\n" \
"ds_read_b64_tr_b16 v[154:155], v225 offset:33544\n" \
"s_cmp_lg_u64 s[56:57], 0\n" \
"s_cselect_b32 %[cx], 0x2000, 0\n" \
"s_xor_b32 %[sin], %[sin], %[cx]\n" \
"v_mfma_f32_16x16x32_bf16 v[88:91], v[176:179], a[168:171], v[88:91]\n" \
"buffer_load_dwordx4 v212, %[srdk], s48 offen offset:1024 lds\n" \
"v_mfma_f32_16x16x32_bf16 v[88:91], v[180:183], a[184:187], v[88:91]\n" \
"ds_read_b64_tr_b16 v[156:157], v225 offset:41480\n" \
"v_mfma_f32_16x16x32_bf16 v[120:123], v[176:179], a[172:175], v[120:123]\n" \
"ds_read_b64_tr_b16 v[158:159], v225 offset:41736\n" \
"v_mfma_f32_16x16x32_bf16 v[120:123], v[180:183], a[188:191], v[120:123]\n" \
"s_mov_b64 exec, 0x2c4000\n" \
"v_xor_b32_e32 %[tlo], %[cx], %[tlo]\n" \
"s_mov_b64 exec, 0x40000\n" \
"v_xor_b32_e32 %[thi], %[cx], %[thi]\n" \
"s_mov_b64 exec, -1\n" \
"v_mfma_f32_16x16x32_bf16 v[28:31], v[184:187], a[160:163], v[28:31]\n" \
"buffer_load_dwordx4 v213, %[srdk], s48 offen offset:2048 lds\n" \
"v_mfma_f32_16x16x32_bf16 v[28:31], v[188:191], a[176:179], v[28:31]\n" \
"v_xor_b32_e32 %[oq0], %[cx], %[oq0]\n" \
"v_mfma_f32_16x16x32_bf16 v[60:63], v[184:187], a[164:167], v[60:63]\n" \
"v_mfma_f32_16x16x32_bf16 v[60:63], v[188:191], a[180:183], v[60:63]\n" \
"v_mfma_f32_16x16x32_bf16 v[92:95], v[184:187], a[168:171], v[92:95]\n" \
"buffer_load_dwordx4 v214, %[srdk], s48 offen offset:3072 lds\n" \
"v_mfma_f32_16x16x32_bf16 v[92:95], v[188:191], a[184:187], v[92:95]\n" \
"v_mfma_f32_16x16x32_bf16 v[124:127], v[184:187], a[172:175], v[124:127]\n" \
"v_mfma_f32_16x16x32_bf16 v[124:127], v[188:191], a[188:191], v[124:127]\n" \
"s_waitcnt vmcnt(25) lgkmcnt(0)\n" \
"v_mfma_f32_16x16x32_bf16 v[0:3], v[128:131], a[192:195], v[0:3]\n" \
"ds_read_b64_tr_b16 v[160:161], v225 offset:33792\n" \
"v_mfma_f32_16x16x32_bf16 v[0:3], v[132:135], a[208:211], v[0:3]\n" \
"ds_read_b64_tr_b16 v[162:163], v225 offset:34048\n" \
"v_mfma_f32_16x16x32_bf16 v[32:35], v[128:131], a[196:199], v[32:35]\n" \
"s_mov_b32 s48, 32768\n" \
"s_mov_b32 m0, s45\n" \
"s_nop 0\n" \
"buffer_load_dwordx4 %[vd0], %[srdv], s48 offen offset:2048 lds\n" \
"v_mfma_f32_16x16x32_bf16 v[32:35], v[132:135], a[212:215], v[32:35]\n" \
"ds_read_b64_tr_b16 v[164:165], v225 offset:41984\n" \
"v_mfma_f32_16x16x32_bf16 v[64:67], v[128:131], a[200:203], v[64:67]\n" \
"ds_read_b64_tr_b16 v[166:167], v225 offset:42240\n" \
"v_mfma_f32_16x16x32_bf16 v[64:67], v[132:135], a[216:219], v[64:67]\n" \
"v_mfma_f32_16x16x32_bf16 v[96:99], v[128:131], a[204:207], v[96:99]\n" \
"ds_read_b64_tr_b16 v[168:169], v225 offset:33800\n" \
"v_mfma_f32_16x16x32_bf16 v[96:99], v[132:135], a[220:223], v[96:99]\n" \
"ds_read_b64_tr_b16 v[170:171], v225 offset:34056\n" \
"v_mfma_f32_16x16x32_bf16 v[4:7], v[136:139], a[192:195], v[4:7]\n" \
"buffer_load_dwordx4 v215, %[srdv], s48 offen offset:3072 lds\n" \
"v_mfma_f32_16x16x32_bf16 v[4:7], v[140:143], a[208:211], v[4:7]\n" \
"ds_read_b64_tr_b16 v[172:173], v225 offset:41992\n" \
"v_mfma_f32_16x16x32_bf16 v[36:39], v[136:139], a[196:199], v[36:39]\n" \
"ds_read_b64_tr_b16 v[174:175], v225 offset:42248\n" \
"v_mfma_f32_16x16x32_bf16 v[36:39], v[140:143], a[212:215], v[36:39]\n" \
"v_mfma_f32_16x16x32_bf16 v[68:71], v[136:139], a[200:203], v[68:71]\n" \
"ds_read_b64_tr_b16 v[176:177], v225 offset:34304\n" \
"v_mfma_f32_16x16x32_bf16 v[68:71], v[140:143], a[216:219], v[68:71]\n" \
"ds_read_b64_tr_b16 v[178:179], v225 offset:34560\n" \
"v_mfma_f32_16x16x32_bf16 v[100:103], v[136:139], a[204:207], v[100:103]\n" \
"s_mov_b32 s48, 32768\n" \
"s_mov_b32 m0, s42\n" \
"s_nop 0\n" \
"buffer_load_dwordx4 %[kd0], %[srdk], s48 offen offset:0 lds\n" \
"v_mfma_f32_16x16x32_bf16 v[100:103], v[140:143], a[220:223], v[100:103]\n" \
"ds_read_b64_tr_b16 v[180:181], v225 offset:42496\n" \
"v_mfma_f32_16x16x32_bf16 v[8:11], v[144:147], a[192:195], v[8:11]\n" \
"ds_read_b64_tr_b16 v[182:183], v225 offset:42752\n" \
"v_mfma_f32_16x16x32_bf16 v[8:11], v[148:151], a[208:211], v[8:11]\n" \
"v_mfma_f32_16x16x32_bf16 v[40:43], v[144:147], a[196:199], v[40:43]\n" \
"ds_read_b64_tr_b16 v[184:185], v225 offset:34312\n" \
"v_mfma_f32_16x16x32_bf16 v[40:43], v[148:151], a[212:215], v[40:43]\n" \
"ds_read_b64_tr_b16 v[186:187], v225 offset:34568\n" \
"v_mfma_f32_16x16x32_bf16 v[72:75], v[144:147], a[200:203], v[72:75]\n" \
"buffer_load_dwordx4 v212, %[srdk], s48 offen offset:1024 lds\n" \
"v_mfma_f32_16x16x32_bf16 v[72:75], v[148:151], a[216:219], v[72:75]\n" \
"ds_read_b64_tr_b16 v[188:189], v225 offset:42504\n" \
"v_mfma_f32_16x16x32_bf16 v[104:107], v[144:147], a[204:207], v[104:107]\n" \
"ds_read_b64_tr_b16 v[190:191], v225 offset:42760\n" \
"v_mfma_f32_16x16x32_bf16 v[104:107], v[148:151], a[220:223], v[104:107]\n" \
"v_mfma_f32_16x16x32_bf16 v[12:15], v[152:155], a[192:195], v[12:15]\n" \
"buffer_load_dwordx4 v213, %[srdk], s48 offen offset:2048 lds\n" \
"v_mfma_f32_16x16x32_bf16 v[12:15], v[156:159], a[208:211], v[12:15]\n" \
"v_mfma_f32_16x16x32_bf16 v[44:47], v[152:155], a[196:199], v[44:47]\n" \
"v_mfma_f32_16x16x32_bf16 v[44:47], v[156:159], a[212:215], v[44:47]\n" \
"v_mfma_f32_16x16x32_bf16 v[76:79], v[152:155], a[200:203], v[76:79]\n" \
"buffer_load_dwordx4 v214, %[srdk], s48 offen offset:3072 lds\n" \
"v_mfma_f32_16x16x32_bf16 v[76:79], v[156:159], a[216:219], v[76:79]\n" \
"v_mfma_f32_16x16x32_bf16 v[108:111], v[152:155], a[204:207], v[108:111]\n" \
"v_mfma_f32_16x16x32_bf16 v[108:111], v[156:159], a[220:223], v[108:111]\n" \
"s_waitcnt vmcnt(29) lgkmcnt(0)\n" \
"s_barrier\n" \
"v_mfma_f32_16x16x32_bf16 v[16:19], v[160:163], a[192:195], v[16:19]\n" \
"ds_read_b64_tr_b16 v[128:129], v225 offset:49152\n" \
"s_mov_b32 s54, 0x4000\n" \
"v_mfma_f32_16x16x32_bf16 v[16:19], v[164:167], a[208:211], v[16:19]\n" \
"ds_read_b64_tr_b16 v[130:131], v225 offset:49408\n" \
"v_mfma_f32_16x16x32_bf16 v[48:51], v[160:163], a[196:199], v[48:51]\n" \
"s_mov_b32 s48, 49152\n" \
"s_mov_b32 m0, s46\n" \
"s_nop 0\n" \
"buffer_load_dwordx4 %[vd0], %[srdv], s48 offen offset:0 lds\n" \
"v_mbcnt_lo_u32_b32 v231, -1, 0\n" \
"v_mfma_f32_16x16x32_bf16 v[48:51], v[164:167], a[212:215], v[48:51]\n" \
"ds_read_b64_tr_b16 v[132:133], v225 offset:57344\n" \
"v_mfma_f32_16x16x32_bf16 v[80:83], v[160:163], a[200:203], v[80:83]\n" \
"ds_read_b64_tr_b16 v[134:135], v225 offset:57600\n" \
"v_mfma_f32_16x16x32_bf16 v[80:83], v[164:167], a[216:219], v[80:83]\n" \
"v_mbcnt_hi_u32_b32 v231, -1, v231\n" \
"v_mfma_f32_16x16x32_bf16 v[112:115], v[160:163], a[204:207], v[112:115]\n" \
"ds_read_b64_tr_b16 v[136:137], v225 offset:49160\n" \
"v_mfma_f32_16x16x32_bf16 v[112:115], v[164:167], a[220:223], v[112:115]\n" \
"ds_read_b64_tr_b16 v[138:139], v225 offset:49416\n" \
"v_mfma_f32_16x16x32_bf16 v[20:23], v[168:171], a[192:195], v[20:23]\n" \
"buffer_load_dwordx4 v215, %[srdv], s48 offen offset:1024 lds\n" \
"v_lshrrev_b32_e64 v232, 4, v231\n" \
"v_mfma_f32_16x16x32_bf16 v[20:23], v[172:175], a[208:211], v[20:23]\n" \
"ds_read_b64_tr_b16 v[140:141], v225 offset:57352\n" \
"v_mfma_f32_16x16x32_bf16 v[52:55], v[168:171], a[196:199], v[52:55]\n" \
"ds_read_b64_tr_b16 v[142:143], v225 offset:57608\n" \
"v_xor_b32_e64 v232, v232, v231\n" \
"v_mfma_f32_16x16x32_bf16 v[52:55], v[172:175], a[212:215], v[52:55]\n" \
"v_mfma_f32_16x16x32_bf16 v[84:87], v[168:171], a[200:203], v[84:87]\n" \
"ds_read_b64_tr_b16 v[144:145], v225 offset:49664\n" \
"v_mfma_f32_16x16x32_bf16 v[84:87], v[172:175], a[216:219], v[84:87]\n" \
"ds_read_b64_tr_b16 v[146:147], v225 offset:49920\n" \
"v_bfe_i32 v232, v232, 1, 1\n" \
"v_mfma_f32_16x16x32_bf16 v[116:119], v[168:171], a[204:207], v[116:119]\n" \
"s_mov_b32 s48, 49152\n" \
"s_mov_b32 m0, s43\n" \
"s_nop 0\n" \
"buffer_load_dwordx4 %[kd0], %[srdk], s48 offen offset:0 lds\n" \
"v_mfma_f32_16x16x32_bf16 v[116:119], v[172:175], a[220:223], v[116:119]\n" \
"ds_read_b64_tr_b16 v[148:149], v225 offset:57856\n" \
"v_mfma_f32_16x16x32_bf16 v[24:27], v[176:179], a[192:195], v[24:27]\n" \
"ds_read_b64_tr_b16 v[150:151], v225 offset:58112\n" \
"v_not_b32_e64 v232, v232\n" \
"v_mfma_f32_16x16x32_bf16 v[24:27], v[180:183], a[208:211], v[24:27]\n" \
"v_mfma_f32_16x16x32_bf16 v[56:59], v[176:179], a[196:199], v[56:59]\n" \
"ds_read_b64_tr_b16 v[152:153], v225 offset:49672\n" \
"v_mfma_f32_16x16x32_bf16 v[56:59], v[180:183], a[212:215], v[56:59]\n" \
"ds_read_b64_tr_b16 v[154:155], v225 offset:49928\n" \
"v_and_b32_e32 v230, 0x3f803f80, v232\n" \
"v_mfma_f32_16x16x32_bf16 v[88:91], v[176:179], a[200:203], v[88:91]\n" \
"buffer_load_dwordx4 v212, %[srdk], s48 offen offset:1024 lds\n" \
"v_mfma_f32_16x16x32_bf16 v[88:91], v[180:183], a[216:219], v[88:91]\n" \
"ds_read_b64_tr_b16 v[156:157], v225 offset:57864\n" \
"v_readlane_b32 s62, %[tlo], 18\n" \
"v_mfma_f32_16x16x32_bf16 v[120:123], v[176:179], a[204:207], v[120:123]\n" \
"ds_read_b64_tr_b16 v[158:159], v225 offset:58120\n" \
"v_mfma_f32_16x16x32_bf16 v[120:123], v[180:183], a[220:223], v[120:123]\n" \
"v_mfma_f32_16x16x32_bf16 v[28:31], v[184:187], a[192:195], v[28:31]\n" \
"buffer_load_dwordx4 v213, %[srdk], s48 offen offset:2048 lds\n" \
"v_readlane_b32 s61, %[tlo], 21\n" \
"v_mfma_f32_16x16x32_bf16 v[28:31], v[188:191], a[208:211], v[28:31]\n" \
"v_mfma_f32_16x16x32_bf16 v[60:63], v[184:187], a[196:199], v[60:63]\n" \
"v_mfma_f32_16x16x32_bf16 v[60:63], v[188:191], a[212:215], v[60:63]\n" \
"v_lshlrev_b32_e64 v224, 7, v231\n" \
"v_mfma_f32_16x16x32_bf16 v[92:95], v[184:187], a[200:203], v[92:95]\n" \
"buffer_load_dwordx4 v214, %[srdk], s48 offen offset:3072 lds\n" \
"v_mfma_f32_16x16x32_bf16 v[92:95], v[188:191], a[216:219], v[92:95]\n" \
"v_mfma_f32_16x16x32_bf16 v[124:127], v[184:187], a[204:207], v[124:127]\n" \
"v_mfma_f32_16x16x32_bf16 v[124:127], v[188:191], a[220:223], v[124:127]\n" \
"s_waitcnt vmcnt(24) lgkmcnt(0)\n" \
"v_mfma_f32_16x16x32_bf16 v[0:3], v[128:131], a[224:227], v[0:3]\n" \
"ds_read_b64_tr_b16 v[160:161], v225 offset:50176\n" \
"v_accvgpr_write_b32 a192, v230\n" \
"v_mfma_f32_16x16x32_bf16 v[0:3], v[132:135], a[240:243], v[0:3]\n" \
"ds_read_b64_tr_b16 v[162:163], v225 offset:50432\n" \
"v_mfma_f32_16x16x32_bf16 v[32:35], v[128:131], a[228:231], v[32:35]\n" \
"s_mov_b32 s48, 49152\n" \
"s_mov_b32 m0, s46\n" \
"s_nop 0\n" \
"buffer_load_dwordx4 %[vd0], %[srdv], s48 offen offset:2048 lds\n" \
"v_accvgpr_write_b32 a193, v230\n" \
"v_mfma_f32_16x16x32_bf16 v[32:35], v[132:135], a[244:247], v[32:35]\n" \
"ds_read_b64_tr_b16 v[164:165], v225 offset:58368\n" \
"v_mfma_f32_16x16x32_bf16 v[64:67], v[128:131], a[232:235], v[64:67]\n" \
"ds_read_b64_tr_b16 v[166:167], v225 offset:58624\n" \
"v_accvgpr_write_b32 a194, v230\n" \
"v_mfma_f32_16x16x32_bf16 v[64:67], v[132:135], a[248:251], v[64:67]\n" \
"v_mfma_f32_16x16x32_bf16 v[96:99], v[128:131], a[236:239], v[96:99]\n" \
"ds_read_b64_tr_b16 v[168:169], v225 offset:50184\n" \
"v_accvgpr_write_b32 a195, v230\n" \
"v_mfma_f32_16x16x32_bf16 v[96:99], v[132:135], a[252:255], v[96:99]\n" \
"ds_read_b64_tr_b16 v[170:171], v225 offset:50440\n" \
"v_mfma_f32_16x16x32_bf16 v[4:7], v[136:139], a[224:227], v[4:7]\n" \
"buffer_load_dwordx4 v215, %[srdv], s48 offen offset:3072 lds\n" \
"v_accvgpr_write_b32 a196, -1\n" \
"v_mfma_f32_16x16x32_bf16 v[4:7], v[140:143], a[240:243], v[4:7]\n" \
"ds_read_b64_tr_b16 v[172:173], v225 offset:58376\n" \
"v_mfma_f32_16x16x32_bf16 v[36:39], v[136:139], a[228:231], v[36:39]\n" \
"ds_read_b64_tr_b16 v[174:175], v225 offset:58632\n" \
"v_accvgpr_write_b32 a197, -1\n" \
"v_mfma_f32_16x16x32_bf16 v[36:39], v[140:143], a[244:247], v[36:39]\n" \
"v_mfma_f32_16x16x32_bf16 v[68:71], v[136:139], a[232:235], v[68:71]\n" \
"ds_read_b64_tr_b16 v[176:177], v225 offset:50688\n" \
"v_accvgpr_write_b32 a198, -1\n" \
"v_mfma_f32_16x16x32_bf16 v[68:71], v[140:143], a[248:251], v[68:71]\n" \
"ds_read_b64_tr_b16 v[178:179], v225 offset:50944\n" \
"v_mfma_f32_16x16x32_bf16 v[100:103], v[136:139], a[236:239], v[100:103]\n" \
"v_accvgpr_write_b32 a199, -1\n" \
"v_mfma_f32_16x16x32_bf16 v[100:103], v[140:143], a[252:255], v[100:103]\n" \
"ds_read_b64_tr_b16 v[180:181], v225 offset:58880\n" \
"v_mfma_f32_16x16x32_bf16 v[8:11], v[144:147], a[224:227], v[8:11]\n" \
"ds_read_b64_tr_b16 v[182:183], v225 offset:59136\n" \
"v_lshrrev_b32_e32 v231, 3, v224\n" \
"v_mfma_f32_16x16x32_bf16 v[8:11], v[148:151], a[240:243], v[8:11]\n" \
"v_mfma_f32_16x16x32_bf16 v[40:43], v[144:147], a[228:231], v[40:43]\n" \
"ds_read_b64_tr_b16 v[184:185], v225 offset:50696\n" \
"v_mov_b32_e64 v232, -1\n" \
"v_mfma_f32_16x16x32_bf16 v[40:43], v[148:151], a[244:247], v[40:43]\n" \
"ds_read_b64_tr_b16 v[186:187], v225 offset:50952\n" \
"v_mfma_f32_16x16x32_bf16 v[72:75], v[144:147], a[232:235], v[72:75]\n" \
"v_mov_b32_e64 v233, -1\n" \
"v_mfma_f32_16x16x32_bf16 v[72:75], v[148:151], a[248:251], v[72:75]\n" \
"ds_read_b64_tr_b16 v[188:189], v225 offset:58888\n" \
"v_mfma_f32_16x16x32_bf16 v[104:107], v[144:147], a[236:239], v[104:107]\n" \
"ds_read_b64_tr_b16 v[190:191], v225 offset:59144\n" \
"v_mov_b32_e64 v234, -1\n" \
"v_mfma_f32_16x16x32_bf16 v[104:107], v[148:151], a[252:255], v[104:107]\n" \
"v_mfma_f32_16x16x32_bf16 v[12:15], v[152:155], a[224:227], v[12:15]\n" \
"v_mov_b32_e64 v235, -1\n" \
"v_mfma_f32_16x16x32_bf16 v[12:15], v[156:159], a[240:243], v[12:15]\n" \
"v_mfma_f32_16x16x32_bf16 v[44:47], v[152:155], a[228:231], v[44:47]\n" \
"buffer_store_dwordx4 v[232:235], v231, %[srdz], s61 offen offset:0 sc1\n" \
"v_mfma_f32_16x16x32_bf16 v[44:47], v[156:159], a[244:247], v[44:47]\n" \
"v_mfma_f32_16x16x32_bf16 v[76:79], v[152:155], a[232:235], v[76:79]\n" \
"buffer_store_dwordx4 v[232:235], v231, %[srdz], s61 offen offset:1024 sc1\n" \
"v_mfma_f32_16x16x32_bf16 v[76:79], v[156:159], a[248:251], v[76:79]\n" \
"v_mfma_f32_16x16x32_bf16 v[108:111], v[152:155], a[236:239], v[108:111]\n" \
"v_mfma_f32_16x16x32_bf16 v[108:111], v[156:159], a[252:255], v[108:111]\n" \
"s_waitcnt vmcnt(22) lgkmcnt(0)\n" \
"s_barrier\n" \
"v_mfma_f32_16x16x32_bf16 v[16:19], v[160:163], a[224:227], v[16:19]\n" \
"ds_read_b128 a[64:67], %[kb0] offset:0\n" \
"v_mfma_f32_16x16x32_bf16 v[16:19], v[164:167], a[240:243], v[16:19]\n" \
"v_mfma_f32_16x16x32_bf16 v[20:23], v[168:171], a[224:227], v[20:23]\n" \
"v_cvt_pk_bf16_f32 v230, v0, v1\n" \
"v_cvt_pk_bf16_f32 v231, v2, v3\n" \
"v_cvt_pk_bf16_f32 v232, v4, v5\n" \
"ds_read_b128 a[68:71], v208 offset:0\n" \
"v_mfma_f32_16x16x32_bf16 v[20:23], v[172:175], a[240:243], v[20:23]\n" \
"v_cvt_pk_bf16_f32 v233, v6, v7\n" \
"v_accvgpr_write_b32 a0, v230\n" \
".if . - adaln_body != 8192\n" \
".error \"size model\"\n" \
".endif\n" \
"Lk1_%=:\n" \
"s_cbranch_execz Lk2_%=\n" \
"s_nop 0\n" \
"v_accvgpr_write_b32 a1, v231\n" \
"v_mfma_f32_16x16x32_bf16 v[24:27], v[176:179], a[224:227], v[24:27]\n" \
"v_accvgpr_write_b32 a2, v232\n" \
"v_accvgpr_write_b32 a3, v233\n" \
"v_cvt_pk_bf16_f32 v234, v8, v9\n" \
"ds_read_b128 a[72:75], v209 offset:0\n" \
"v_mfma_f32_16x16x32_bf16 v[24:27], v[180:183], a[240:243], v[24:27]\n" \
"v_cvt_pk_bf16_f32 v235, v10, v11\n" \
"v_cvt_pk_bf16_f32 v236, v12, v13\n" \
"v_cvt_pk_bf16_f32 v237, v14, v15\n" \
"v_mfma_f32_16x16x32_bf16 v[28:31], v[184:187], a[224:227], v[28:31]\n" \
"v_accvgpr_write_b32 a4, v234\n" \
"v_accvgpr_write_b32 a5, v235\n" \
"v_accvgpr_write_b32 a6, v236\n" \
"ds_read_b128 a[76:79], v210 offset:0\n" \
"v_mfma_f32_16x16x32_bf16 v[28:31], v[188:191], a[240:243], v[28:31]\n" \
"v_accvgpr_write_b32 a7, v237\n" \
"v_cvt_pk_bf16_f32 v230, v32, v33\n" \
"v_cvt_pk_bf16_f32 v231, v34, v35\n" \
"v_mfma_f32_16x16x32_bf16 v[48:51], v[160:163], a[228:231], v[48:51]\n" \
"v_cvt_pk_bf16_f32 v232, v36, v37\n" \
"v_cvt_pk_bf16_f32 v233, v38, v39\n" \
"v_accvgpr_write_b32 a16, v230\n" \
"ds_read_b128 a[92:95], v210 offset:4096\n" \
"v_mfma_f32_16x16x32_bf16 v[48:51], v[164:167], a[244:247], v[48:51]\n" \
"v_accvgpr_write_b32 a17, v231\n" \
"v_accvgpr_write_b32 a18, v232\n" \
"v_accvgpr_write_b32 a19, v233\n" \
"v_mfma_f32_16x16x32_bf16 v[52:55], v[168:171], a[228:231], v[52:55]\n" \
"v_cvt_pk_bf16_f32 v234, v40, v41\n" \
"v_cvt_pk_bf16_f32 v235, v42, v43\n" \
"v_cvt_pk_bf16_f32 v236, v44, v45\n" \
"ds_read_b128 a[88:91], v209 offset:4096\n" \
"v_mfma_f32_16x16x32_bf16 v[52:55], v[172:175], a[244:247], v[52:55]\n" \
"v_cvt_pk_bf16_f32 v237, v46, v47\n" \
"v_accvgpr_write_b32 a20, v234\n" \
"v_accvgpr_write_b32 a21, v235\n" \
"v_mfma_f32_16x16x32_bf16 v[56:59], v[176:179], a[228:231], v[56:59]\n" \
"v_accvgpr_write_b32 a22, v236\n" \
"v_accvgpr_write_b32 a23, v237\n" \
"v_cvt_pk_bf16_f32 v230, v16, v17\n" \
"ds_read_b128 a[84:87], v208 offset:4096\n" \
"v_mfma_f32_16x16x32_bf16 v[56:59], v[180:183], a[244:247], v[56:59]\n" \
"v_cvt_pk_bf16_f32 v231, v18, v19\n" \
"v_cvt_pk_bf16_f32 v232, v20, v21\n" \
"v_cvt_pk_bf16_f32 v233, v22, v23\n" \
"v_mfma_f32_16x16x32_bf16 v[60:63], v[184:187], a[228:231], v[60:63]\n" \
"v_accvgpr_write_b32 a8, v230\n" \
"v_accvgpr_write_b32 a9, v231\n" \
"v_accvgpr_write_b32 a10, v232\n" \
"ds_read_b128 a[80:83], %[kb0] offset:4096\n" \
"v_mfma_f32_16x16x32_bf16 v[60:63], v[188:191], a[244:247], v[60:63]\n" \
"v_accvgpr_write_b32 a11, v233\n" \
"v_cvt_pk_bf16_f32 v234, v24, v25\n" \
"v_cvt_pk_bf16_f32 v235, v26, v27\n" \
"v_mfma_f32_16x16x32_bf16 v[80:83], v[160:163], a[232:235], v[80:83]\n" \
"v_cvt_pk_bf16_f32 v236, v28, v29\n" \
"v_cvt_pk_bf16_f32 v237, v30, v31\n" \
"v_accvgpr_write_b32 a12, v234\n" \
"v_mfma_f32_16x16x32_bf16 v[80:83], v[164:167], a[248:251], v[80:83]\n" \
"v_accvgpr_write_b32 a13, v235\n" \
"v_accvgpr_write_b32 a14, v236\n" \
"v_accvgpr_write_b32 a15, v237\n" \
"v_mfma_f32_16x16x32_bf16 v[84:87], v[168:171], a[232:235], v[84:87]\n" \
"v_cvt_pk_bf16_f32 v230, v64, v65\n" \
"v_cvt_pk_bf16_f32 v231, v66, v67\n" \
"v_cvt_pk_bf16_f32 v232, v68, v69\n" \
"v_mfma_f32_16x16x32_bf16 v[84:87], v[172:175], a[248:251], v[84:87]\n" \
"v_cvt_pk_bf16_f32 v233, v70, v71\n" \
"v_accvgpr_write_b32 a32, v230\n" \
"v_accvgpr_write_b32 a33, v231\n" \
"v_mfma_f32_16x16x32_bf16 v[88:91], v[176:179], a[232:235], v[88:91]\n" \
"v_accvgpr_write_b32 a34, v232\n" \
"v_accvgpr_write_b32 a35, v233\n" \
"v_cvt_pk_bf16_f32 v234, v72, v73\n" \
"v_mfma_f32_16x16x32_bf16 v[88:91], v[180:183], a[248:251], v[88:91]\n" \
"v_cvt_pk_bf16_f32 v235, v74, v75\n" \
"v_cvt_pk_bf16_f32 v236, v76, v77\n" \
"v_cvt_pk_bf16_f32 v237, v78, v79\n" \
"v_mfma_f32_16x16x32_bf16 v[92:95], v[184:187], a[232:235], v[92:95]\n" \
"v_accvgpr_write_b32 a36, v234\n" \
"v_accvgpr_write_b32 a37, v235\n" \
"v_accvgpr_write_b32 a38, v236\n" \
"v_mfma_f32_16x16x32_bf16 v[92:95], v[188:191], a[248:251], v[92:95]\n" \
"v_accvgpr_write_b32 a39, v237\n" \
"v_cvt_pk_bf16_f32 v230, v48, v49\n" \
"v_cvt_pk_bf16_f32 v231, v50, v51\n" \
"v_mfma_f32_16x16x32_bf16 v[112:115], v[160:163], a[236:239], v[112:115]\n" \
"v_cvt_pk_bf16_f32 v232, v52, v53\n" \
"v_cvt_pk_bf16_f32 v233, v54, v55\n" \
"v_accvgpr_write_b32 a24, v230\n" \
"v_mfma_f32_16x16x32_bf16 v[112:115], v[164:167], a[252:255], v[112:115]\n" \
"v_accvgpr_write_b32 a25, v231\n" \
"v_accvgpr_write_b32 a26, v232\n" \
"v_accvgpr_write_b32 a27, v233\n" \
"v_mfma_f32_16x16x32_bf16 v[116:119], v[168:171], a[236:239], v[116:119]\n" \
"v_cvt_pk_bf16_f32 v234, v56, v57\n" \
"v_cvt_pk_bf16_f32 v235, v58, v59\n" \
"v_cvt_pk_bf16_f32 v236, v60, v61\n" \
"v_mfma_f32_16x16x32_bf16 v[116:119], v[172:175], a[252:255], v[116:119]\n" \
"v_cvt_pk_bf16_f32 v237, v62, v63\n" \
"v_accvgpr_write_b32 a28, v234\n" \
"v_accvgpr_write_b32 a29, v235\n" \
"v_mfma_f32_16x16x32_bf16 v[120:123], v[176:179], a[236:239], v[120:123]\n" \
"v_accvgpr_write_b32 a30, v236\n" \
"v_accvgpr_write_b32 a31, v237\n" \
"v_cvt_pk_bf16_f32 v230, v96, v97\n" \
"v_mfma_f32_16x16x32_bf16 v[120:123], v[180:183], a[252:255], v[120:123]\n" \
"v_cvt_pk_bf16_f32 v231, v98, v99\n" \
"v_cvt_pk_bf16_f32 v232, v100, v101\n" \
"v_cvt_pk_bf16_f32 v233, v102, v103\n" \
"v_mfma_f32_16x16x32_bf16 v[124:127], v[184:187], a[236:239], v[124:127]\n" \
"v_accvgpr_write_b32 a48, v230\n" \
"v_accvgpr_write_b32 a49, v231\n" \
"v_accvgpr_write_b32 a50, v232\n" \
"v_mfma_f32_16x16x32_bf16 v[124:127], v[188:191], a[252:255], v[124:127]\n" \
"v_accvgpr_write_b32 a51, v233\n" \
"s_waitcnt vmcnt(63) lgkmcnt(0)\n" \
"s_nop 0\n" \
"s_mov_b32 s48, 65536\n" \
"s_mov_b32 s60, 0\n" \
"s_waitcnt lgkmcnt(0)\n" \
"v_mfma_f32_16x16x32_bf16 v[128:131], a[64:67], a[0:3], 0\n" \
"ds_read_b128 a[96:99], %[kb0] offset:8192\n" \
"v_cvt_pk_bf16_f32 v234, v80, v81\n" \
"v_cvt_pk_bf16_f32 v235, v82, v83\n" \
"v_mfma_f32_16x16x32_bf16 v[128:131], a[68:71], a[4:7], v[128:131]\n" \
"ds_read_b128 a[100:103], v208 offset:8192\n" \
"v_cvt_pk_bf16_f32 v236, v84, v85\n" \
"v_cvt_pk_bf16_f32 v237, v86, v87\n" \
"v_mfma_f32_16x16x32_bf16 v[128:131], a[72:75], a[8:11], v[128:131]\n" \
"ds_read_b128 a[104:107], v209 offset:8192\n" \
"v_accvgpr_write_b32 a40, v234\n" \
"v_accvgpr_write_b32 a41, v235\n" \
"v_mfma_f32_16x16x32_bf16 v[128:131], a[76:79], a[12:15], v[128:131]\n" \
"ds_read_b128 a[108:111], v210 offset:8192\n" \
"v_accvgpr_write_b32 a42, v236\n" \
"v_accvgpr_write_b32 a43, v237\n" \
"v_mfma_f32_16x16x32_bf16 v[132:135], a[92:95], a[12:15], 0\n" \
"ds_read_b128 a[124:127], v210 offset:12288\n" \
"v_cvt_pk_bf16_f32 v230, v88, v89\n" \
"v_cvt_pk_bf16_f32 v231, v90, v91\n" \
"v_mfma_f32_16x16x32_bf16 v[132:135], a[88:91], a[8:11], v[132:135]\n" \
"ds_read_b128 a[120:123], v209 offset:12288\n" \
"v_cvt_pk_bf16_f32 v232, v92, v93\n" \
"v_cvt_pk_bf16_f32 v233, v94, v95\n" \
"v_mfma_f32_16x16x32_bf16 v[132:135], a[84:87], a[4:7], v[132:135]\n" \
"ds_read_b128 a[116:119], v208 offset:12288\n" \
"v_accvgpr_write_b32 a44, v230\n" \
"v_accvgpr_write_b32 a45, v231\n" \
"v_mfma_f32_16x16x32_bf16 v[132:135], a[80:83], a[0:3], v[132:135]\n" \
"ds_read_b128 a[112:115], %[kb0] offset:12288\n" \
"v_accvgpr_write_b32 a46, v232\n" \
"v_accvgpr_write_b32 a47, v233\n" \
"v_mfma_f32_16x16x32_bf16 v[136:139], a[64:67], a[16:19], 0\n" \
"ds_read_b64_tr_b16 a[128:129], %[vvb] offset:0\n" \
"v_cvt_pk_bf16_f32 v234, v104, v105\n" \
"v_cvt_pk_bf16_f32 v235, v106, v107\n" \
"v_mfma_f32_16x16x32_bf16 v[136:139], a[68:71], a[20:23], v[136:139]\n" \
"ds_read_b64_tr_b16 a[130:131], %[vvb] offset:4096\n" \
"v_cvt_pk_bf16_f32 v236, v108, v109\n" \
"v_cvt_pk_bf16_f32 v237, v110, v111\n" \
"v_mfma_f32_16x16x32_bf16 v[136:139], a[72:75], a[24:27], v[136:139]\n" \
"ds_read_b64_tr_b16 a[132:133], v211 offset:0\n" \
"v_accvgpr_write_b32 a52, v234\n" \
"v_accvgpr_write_b32 a53, v235\n" \
"v_mfma_f32_16x16x32_bf16 v[136:139], a[76:79], a[28:31], v[136:139]\n" \
"ds_read_b64_tr_b16 a[134:135], v211 offset:4096\n" \
"v_accvgpr_write_b32 a54, v236\n" \
"v_accvgpr_write_b32 a55, v237\n" \
"v_mfma_f32_16x16x32_bf16 v[140:143], a[92:95], a[28:31], 0\n" \
"ds_read_b64_tr_b16 a[136:137], %[vvb] offset:512\n" \
"v_cvt_pk_bf16_f32 v230, v112, v113\n" \
"v_cvt_pk_bf16_f32 v231, v114, v115\n" \
"v_mfma_f32_16x16x32_bf16 v[140:143], a[88:91], a[24:27], v[140:143]\n" \
"ds_read_b64_tr_b16 a[138:139], %[vvb] offset:4608\n" \
"v_cvt_pk_bf16_f32 v232, v116, v117\n" \
"v_cvt_pk_bf16_f32 v233, v118, v119\n" \
"v_mfma_f32_16x16x32_bf16 v[140:143], a[84:87], a[20:23], v[140:143]\n" \
"ds_read_b64_tr_b16 a[140:141], v211 offset:512\n" \
"v_accvgpr_write_b32 a56, v230\n" \
"v_accvgpr_write_b32 a57, v231\n" \
"v_mfma_f32_16x16x32_bf16 v[140:143], a[80:83], a[16:19], v[140:143]\n" \
"ds_read_b64_tr_b16 a[142:143], v211 offset:4608\n" \
"v_accvgpr_write_b32 a58, v232\n" \
"v_accvgpr_write_b32 a59, v233\n" \
"v_mfma_f32_16x16x32_bf16 v[144:147], a[64:67], a[32:35], 0\n" \
"ds_read_b64_tr_b16 a[144:145], %[vvb] offset:1024\n" \
"v_cvt_pk_bf16_f32 v234, v120, v121\n" \
"v_cvt_pk_bf16_f32 v235, v122, v123\n" \
"v_mfma_f32_16x16x32_bf16 v[144:147], a[68:71], a[36:39], v[144:147]\n" \
"ds_read_b64_tr_b16 a[146:147], %[vvb] offset:5120\n" \
"v_cvt_pk_bf16_f32 v236, v124, v125\n" \
"v_cvt_pk_bf16_f32 v237, v126, v127\n" \
"v_mfma_f32_16x16x32_bf16 v[144:147], a[72:75], a[40:43], v[144:147]\n" \
"ds_read_b64_tr_b16 a[148:149], v211 offset:1024\n" \
"v_accvgpr_write_b32 a60, v234\n" \
"v_accvgpr_write_b32 a61, v235\n" \
"v_mfma_f32_16x16x32_bf16 v[144:147], a[76:79], a[44:47], v[144:147]\n" \
"ds_read_b64_tr_b16 a[150:151], v211 offset:5120\n" \
"v_accvgpr_write_b32 a62, v236\n" \
"v_accvgpr_write_b32 a63, v237\n" \
"v_mfma_f32_16x16x32_bf16 v[148:151], a[92:95], a[44:47], 0\n" \
"ds_read_b64_tr_b16 a[152:153], %[vvb] offset:1536\n" \
"v_max3_f32 v192, v128, v129, v130\n" \
"v_max3_f32 v193, v131, v132, v133\n" \
"v_mfma_f32_16x16x32_bf16 v[148:151], a[88:91], a[40:43], v[148:151]\n" \
"ds_read_b64_tr_b16 a[154:155], %[vvb] offset:5632\n" \
"v_max3_f32 v192, v192, v134, v135\n" \
"v_max_f32_e64 v225, v192, v193\n" \
"v_mfma_f32_16x16x32_bf16 v[148:151], a[84:87], a[36:39], v[148:151]\n" \
"ds_read_b64_tr_b16 a[156:157], v211 offset:1536\n" \
"v_max3_f32 v192, v136, v137, v138\n" \
"v_max3_f32 v193, v139, v140, v141\n" \
"v_mfma_f32_16x16x32_bf16 v[148:151], a[80:83], a[32:35], v[148:151]\n" \
"ds_read_b64_tr_b16 a[158:159], v211 offset:5632\n" \
"v_max3_f32 v192, v192, v142, v143\n" \
"v_max_f32_e64 v226, v192, v193\n" \
"v_mfma_f32_16x16x32_bf16 v[152:155], a[64:67], a[48:51], 0\n" \
"ds_read_b128 a[200:203], v228 offset:0\n" \
"s_mov_b32 s44, %[m0t]\n" \
"s_mov_b32 s45, 0\n" \
"v_mfma_f32_16x16x32_bf16 v[152:155], a[68:71], a[52:55], v[152:155]\n" \
"ds_read_b128 a[204:207], v228 offset:64\n" \
"v_mfma_f32_16x16x32_bf16 v[152:155], a[72:75], a[56:59], v[152:155]\n" \
"ds_read_b128 a[208:211], v228 offset:128\n" \
"v_mfma_f32_16x16x32_bf16 v[152:155], a[76:79], a[60:63], v[152:155]\n" \
"ds_read_b128 a[212:215], v228 offset:192\n" \
"v_mfma_f32_16x16x32_bf16 v[156:159], a[92:95], a[60:63], 0\n" \
"ds_read_b128 a[216:219], v228 offset:256\n" \
"v_mfma_f32_16x16x32_bf16 v[156:159], a[88:91], a[56:59], v[156:159]\n" \
"ds_read_b128 a[220:223], v228 offset:320\n" \
"v_mfma_f32_16x16x32_bf16 v[156:159], a[84:87], a[52:55], v[156:159]\n" \
"ds_read_b128 a[224:227], v228 offset:384\n" \
"v_max3_f32 v192, v144, v145, v146\n" \
"v_max3_f32 v193, v147, v148, v149\n" \
"v_mfma_f32_16x16x32_bf16 v[156:159], a[80:83], a[48:51], v[156:159]\n" \
"ds_read_b128 a[228:231], v228 offset:448\n" \
"v_max3_f32 v192, v192, v150, v151\n" \
"v_max_f32_e64 v227, v192, v193\n" \
"s_nop 3\n" \
"s_nop 0\n" \
"v_max3_f32 v230, v152, v153, v154\n" \
"v_max3_f32 v231, v155, v156, v157\n" \
"v_max3_f32 v230, v230, v158, v159\n" \
"v_max_f32_e32 v228, v230, v231\n" \
"v_mov_b32_e32 v232, v225\n" \
"v_mov_b32_e32 v233, v226\n" \
"v_mov_b32_e32 v234, v227\n" \
"v_mov_b32_e32 v235, v228\n" \
"s_nop 0\n" \
"v_permlane16_swap_b32_e32 v225, v232\n" \
"v_permlane16_swap_b32_e32 v226, v233\n" \
"v_permlane16_swap_b32_e32 v227, v234\n" \
"v_permlane16_swap_b32_e32 v228, v235\n" \
"v_max_f32_e32 v225, v225, v232\n" \
"v_max_f32_e32 v226, v226, v233\n" \
"v_max_f32_e32 v227, v227, v234\n" \
"v_max_f32_e32 v228, v228, v235\n" \
"v_mov_b32_e32 v232, v225\n" \
"v_mov_b32_e32 v233, v226\n" \
"v_mov_b32_e32 v234, v227\n" \
"v_mov_b32_e32 v235, v228\n" \
"s_nop 0\n" \
"v_permlane32_swap_b32_e32 v225, v232\n" \
"v_permlane32_swap_b32_e32 v226, v233\n" \
"v_permlane32_swap_b32_e32 v227, v234\n" \
"v_permlane32_swap_b32_e32 v228, v235\n" \
"v_max_f32_e32 v225, v225, v232\n" \
"v_max_f32_e32 v226, v226, v233\n" \
"v_max_f32_e32 v227, v227, v234\n" \
"v_max_f32_e64 v228, v228, v235\n" \
"v_add_f32_e32 v225, 0x41800000, v225\n" \
"v_add_f32_e32 v226, 0x41800000, v226\n" \
"v_add_f32_e32 v227, 0x41800000, v227\n" \
"v_add_f32_e32 v228, 0x41800000, v228\n" \
"Lhavem_%=:\n" \
"s_nop 3\n" \
"v_mov_b64_e32 v[176:177], 0\n" \
"v_mov_b64_e32 v[178:179], 0\n" \
"v_mov_b64_e32 v[180:181], 0\n" \
"v_mov_b64_e32 v[182:183], 0\n" \
"s_and_b32 s68, s52, s69\n" \
"s_cselect_b32 s68, 0, 0x39400000\n" \
"s_waitcnt lgkmcnt(14)\n" \
"v_mfma_f32_16x16x32_bf16 v[230:233], a[128:131], a[128:131], 0\n" \
"v_sub_f32_e32 v160, 0, v225\n" \
"v_sub_f32_e32 v161, 0, v225\n" \
"v_sub_f32_e32 v162, 0, v225\n" \
"v_sub_f32_e32 v163, 0, v225\n" \
"v_sub_f32_e32 v164, 0, v226\n" \
"v_sub_f32_e32 v165, 0, v226\n" \
"v_sub_f32_e32 v166, 0, v226\n" \
"v_sub_f32_e32 v167, 0, v226\n" \
"v_sub_f32_e32 v168, 0, v227\n" \
"v_sub_f32_e32 v169, 0, v227\n" \
"v_sub_f32_e32 v170, 0, v227\n" \
"v_sub_f32_e32 v171, 0, v227\n" \
"v_sub_f32_e32 v172, 0, v228\n" \
"v_sub_f32_e32 v173, 0, v228\n" \
"v_sub_f32_e32 v174, 0, v228\n" \
"v_sub_f32_e32 v175, 0, v228\n" \
"v_sub_f32_e32 v128, v128, v225\n" \
"v_sub_f32_e32 v129, v129, v225\n" \
"v_sub_f32_e32 v130, v130, v225\n" \
"v_sub_f32_e32 v131, v131, v225\n" \
"v_sub_f32_e32 v132, v132, v225\n" \
"v_sub_f32_e32 v133, v133, v225\n" \
"v_sub_f32_e32 v134, v134, v225\n" \
"v_sub_f32_e32 v135, v135, v225\n" \
"v_sub_f32_e32 v136, v136, v226\n" \
"v_sub_f32_e32 v137, v137, v226\n" \
"v_sub_f32_e32 v138, v138, v226\n" \
"v_sub_f32_e32 v139, v139, v226\n" \
"v_sub_f32_e32 v140, v140, v226\n" \
"v_sub_f32_e32 v141, v141, v226\n" \
"v_sub_f32_e32 v142, v142, v226\n" \
"v_sub_f32_e32 v143, v143, v226\n" \
"v_sub_f32_e32 v144, v144, v227\n" \
"v_sub_f32_e32 v145, v145, v227\n" \
"v_sub_f32_e32 v146, v146, v227\n" \
"v_sub_f32_e32 v147, v147, v227\n" \
"v_sub_f32_e32 v148, v148, v227\n" \
"v_sub_f32_e32 v149, v149, v227\n" \
"v_sub_f32_e32 v150, v150, v227\n" \
"v_sub_f32_e32 v151, v151, v227\n" \
"v_sub_f32_e32 v152, v152, v228\n" \
"v_sub_f32_e32 v153, v153, v228\n" \
"v_sub_f32_e32 v154, v154, v228\n" \
"v_sub_f32_e32 v155, v155, v228\n" \
"v_sub_f32_e32 v156, v156, v228\n" \
"v_sub_f32_e32 v157, v157, v228\n" \
"v_sub_f32_e32 v158, v158, v228\n" \
"v_sub_f32_e32 v159, v159, v228\n" \
"v_exp_f32_e32 v128, v128\n" \
"v_exp_f32_e32 v129, v129\n" \
"v_exp_f32_e32 v130, v130\n" \
"v_exp_f32_e32 v131, v131\n" \
"v_exp_f32_e32 v132, v132\n" \
"v_exp_f32_e32 v133, v133\n" \
"v_exp_f32_e32 v134, v134\n" \
"v_exp_f32_e32 v135, v135\n" \
"v_exp_f32_e32 v136, v136\n" \
"v_exp_f32_e32 v137, v137\n" \
"v_exp_f32_e32 v138, v138\n" \
"v_exp_f32_e32 v139, v139\n" \
"v_exp_f32_e32 v140, v140\n" \
"v_exp_f32_e32 v141, v141\n" \
"v_exp_f32_e32 v142, v142\n" \
"v_exp_f32_e32 v143, v143\n" \
"v_exp_f32_e32 v144, v144\n" \
"v_exp_f32_e32 v145, v145\n" \
"v_exp_f32_e32 v146, v146\n" \
"v_exp_f32_e32 v147, v147\n" \
"v_exp_f32_e32 v148, v148\n" \
"v_exp_f32_e32 v149, v149\n" \
"v_exp_f32_e32 v150, v150\n" \
"v_exp_f32_e32 v151, v151\n" \
"v_exp_f32_e32 v152, v152\n" \
"v_exp_f32_e32 v153, v153\n" \
"v_exp_f32_e32 v154, v154\n" \
"v_exp_f32_e64 v155, v155\n" \
"v_bfe_i32 v228, v224, 7, 1\n" \
"v_and_b32_e32 v228, 0xaaaa, v228\n" \
"v_or_b32_e32 v228, 0x4444, v228\n" \
"v_cvt_pk_bf16_f32 v184, v128, v129\n" \
"v_cvt_pk_bf16_f32 v185, v130, v131\n" \
"v_cvt_pk_bf16_f32 v186, v132, v133\n" \
"v_cvt_pk_bf16_f32 v187, v134, v135\n" \
"v_cvt_pk_bf16_f32 v188, v136, v137\n" \
"v_cvt_pk_bf16_f32 v189, v138, v139\n" \
"v_cvt_pk_bf16_f32 v190, v140, v141\n" \
"v_cvt_pk_bf16_f32 v191, v142, v143\n" \
"v_cvt_pk_bf16_f32 v216, v144, v145\n" \
"v_cvt_pk_bf16_f32 v217, v146, v147\n" \
"v_cvt_pk_bf16_f32 v218, v148, v149\n" \
"v_cvt_pk_bf16_f32 v219, v150, v151\n" \
"v_max3_f32 v230, |v230|, |v231|, |v232|\n" \
"v_max_f32_e64 v230, v230, |v233|\n" \
"v_sqrt_f32_e32 v230, v230\n" \
"s_nop 0\n" \
"v_mul_legacy_f32 v225, s68, v230\n" \
"v_mov_b32_e32 v230, v225\n" \
"v_mov_b32_e32 v231, v225\n" \
"v_mov_b32_e32 v232, v225\n" \
"v_mov_b32_e32 v233, v225\n" \
"v_lshrrev_b32_e64 v235, 3, v224\n" \
"v_readlane_b32 s68, %[tlo], 13\n" \
"s_xor_b32 s61, %[sin], 0x2000\n" \
"s_sub_u32 s61, s61, %[sin]\n" \
"s_add_u32 s61, s61, 0x200000\n" \
"v_add_u32_e32 v226, s61, v235\n" \
"s_bitcmp0_b32 s68, 0\n" \
"s_cselect_b64 vcc, -1, 0\n" \
"s_waitcnt vmcnt(2) lgkmcnt(0)\n" \
"s_barrier\n" \
"v_mfma_f32_16x16x32_bf16 v[128:131], a[96:99], a[0:3], v[160:163]\n" \
"v_exp_f32_e64 v156, v156\n" \
"v_mfma_f32_16x16x32_bf16 v[128:131], a[100:103], a[4:7], v[128:131]\n" \
"s_add_u32 m0, %[m0b], 114688\n" \
"v_mfma_f32_16x16x32_bf16 v[128:131], a[104:107], a[8:11], v[128:131]\n" \
"v_exp_f32_e64 v157, v157\n" \
"v_mfma_f32_16x16x32_bf16 v[128:131], a[108:111], a[12:15], v[128:131]\n" \
"v_cvt_pk_bf16_f32 v220, v152, v153\n" \
"v_cvt_pk_bf16_f32 v221, v154, v155\n" \
"v_mfma_f32_16x16x32_bf16 v[132:135], a[124:127], a[12:15], v[160:163]\n" \
"v_exp_f32_e64 v158, v158\n" \
"v_mfma_f32_16x16x32_bf16 v[132:135], a[120:123], a[8:11], v[132:135]\n" \
"v_mfma_f32_16x16x32_bf16 v[132:135], a[116:119], a[4:7], v[132:135]\n" \
"buffer_load_dwordx4 %[vd0], %[srdv], s48 offen offset:0 lds\n" \
"v_mfma_f32_16x16x32_bf16 v[132:135], a[112:115], a[0:3], v[132:135]\n" \
"v_exp_f32_e64 v159, v159\n" \
"v_mfma_f32_16x16x32_bf16 v[0:3], a[128:131], v[184:187], v[230:233]\n" \
"ds_read_b128 a[64:67], %[kb0] offset:16384\n" \
"v_mfma_f32_16x16x32_bf16 v[32:35], a[128:131], v[188:191], v[230:233]\n" \
"v_exp_f32_e64 v128, v128\n" \
"v_mfma_f32_16x16x32_bf16 v[4:7], a[132:135], v[184:187], v[230:233]\n" \
"v_cvt_pk_bf16_f32 v222, v156, v157\n" \
"v_cvt_pk_bf16_f32 v223, v158, v159\n" \
"v_mfma_f32_16x16x32_bf16 v[36:39], a[132:135], v[188:191], v[230:233]\n" \
"v_exp_f32_e64 v129, v129\n" \
"v_mfma_f32_16x16x32_bf16 v[8:11], a[136:139], v[184:187], v[230:233]\n" \
"ds_read_b128 a[68:71], v208 offset:16384\n" \
"v_mfma_f32_16x16x32_bf16 v[40:43], a[136:139], v[188:191], v[230:233]\n" \
"v_exp_f32_e64 v130, v130\n" \
"v_mfma_f32_16x16x32_bf16 v[12:15], a[140:143], v[184:187], v[230:233]\n" \
"ds_read_b128 a[72:75], v209 offset:16384\n" \
"v_mfma_f32_16x16x32_bf16 v[44:47], a[140:143], v[188:191], v[230:233]\n" \
"v_exp_f32_e64 v131, v131\n" \
"v_smfmac_f32_16x16x64_bf16 v[176:179], a[192:195], v[184:191], v228\n" \
"v_mfma_f32_16x16x32_bf16 v[136:139], a[96:99], a[16:19], v[164:167]\n" \
"v_exp_f32_e64 v132, v132\n" \
"v_mfma_f32_16x16x32_bf16 v[136:139], a[100:103], a[20:23], v[136:139]\n" \
"ds_read_b128 a[76:79], v210 offset:16384\n" \
"v_mfma_f32_16x16x32_bf16 v[136:139], a[104:107], a[24:27], v[136:139]\n" \
"v_exp_f32_e64 v133, v133\n" \
"v_mfma_f32_16x16x32_bf16 v[136:139], a[108:111], a[28:31], v[136:139]\n" \
"v_cvt_pk_bf16_f32 v192, v128, v129\n" \
"v_cvt_pk_bf16_f32 v193, v130, v131\n" \
"v_mfma_f32_16x16x32_bf16 v[140:143], a[124:127], a[28:31], v[164:167]\n" \
"v_exp_f32_e64 v134, v134\n" \
"v_mfma_f32_16x16x32_bf16 v[140:143], a[120:123], a[24:27], v[140:143]\n" \
"ds_read_b128 a[92:95], v210 offset:20480\n" \
"v_mfma_f32_16x16x32_bf16 v[140:143], a[116:119], a[20:23], v[140:143]\n" \
"buffer_load_dwordx4 v215, %[srdv], s48 offen offset:1024 lds\n" \
"v_mfma_f32_16x16x32_bf16 v[140:143], a[112:115], a[16:19], v[140:143]\n" \
"v_exp_f32_e64 v135, v135\n" \
"v_mfma_f32_16x16x32_bf16 v[16:19], a[144:147], v[184:187], v[230:233]\n" \
"ds_read_b128 a[88:91], v209 offset:20480\n" \
"v_mfma_f32_16x16x32_bf16 v[48:51], a[144:147], v[188:191], v[230:233]\n" \
"v_exp_f32_e64 v136, v136\n" \
"v_mfma_f32_16x16x32_bf16 v[20:23], a[148:151], v[184:187], v[230:233]\n" \
"v_cvt_pk_bf16_f32 v194, v132, v133\n" \
"v_cvt_pk_bf16_f32 v195, v134, v135\n" \
"v_mfma_f32_16x16x32_bf16 v[52:55], a[148:151], v[188:191], v[230:233]\n" \
"v_exp_f32_e64 v137, v137\n" \
"v_mfma_f32_16x16x32_bf16 v[24:27], a[152:155], v[184:187], v[230:233]\n" \
"v_exp_f32_e64 v138, v138\n" \
"v_mfma_f32_16x16x32_bf16 v[56:59], a[152:155], v[188:191], v[230:233]\n" \
"ds_read_b128 a[84:87], v208 offset:20480\n" \
"v_mfma_f32_16x16x32_bf16 v[28:31], a[156:159], v[184:187], v[230:233]\n" \
"v_exp_f32_e64 v139, v139\n" \
"v_mfma_f32_16x16x32_bf16 v[60:63], a[156:159], v[188:191], v[230:233]\n" \
"v_mfma_f32_16x16x32_bf16 v[144:147], a[96:99], a[32:35], v[168:171]\n" \
"v_exp_f32_e64 v140, v140\n" \
"v_mfma_f32_16x16x32_bf16 v[144:147], a[100:103], a[36:39], v[144:147]\n" \
"ds_read_b128 a[80:83], %[kb0] offset:20480\n" \
"v_mfma_f32_16x16x32_bf16 v[144:147], a[104:107], a[40:43], v[144:147]\n" \
"v_exp_f32_e64 v141, v141\n" \
"v_mfma_f32_16x16x32_bf16 v[144:147], a[108:111], a[44:47], v[144:147]\n" \
"v_cvt_pk_bf16_f32 v196, v136, v137\n" \
"v_cvt_pk_bf16_f32 v197, v138, v139\n" \
"v_mfma_f32_16x16x32_bf16 v[148:151], a[124:127], a[44:47], v[168:171]\n" \
"v_exp_f32_e64 v142, v142\n" \
"v_mfma_f32_16x16x32_bf16 v[148:151], a[120:123], a[40:43], v[148:151]\n" \
"v_mfma_f32_16x16x32_bf16 v[148:151], a[116:119], a[36:39], v[148:151]\n" \
"buffer_load_dwordx4 %[vd0], %[srdv], s48 offen offset:2048 lds\n" \
"v_mfma_f32_16x16x32_bf16 v[148:151], a[112:115], a[32:35], v[148:151]\n" \
"v_exp_f32_e64 v143, v143\n" \
"v_mfma_f32_16x16x32_bf16 v[64:67], a[128:131], v[216:219], v[230:233]\n" \
"ds_read_b64_tr_b16 a[160:161], %[vvb] offset:8192\n" \
"v_mfma_f32_16x16x32_bf16 v[96:99], a[128:131], v[220:223], v[230:233]\n" \
"v_exp_f32_e64 v144, v144\n" \
"v_mfma_f32_16x16x32_bf16 v[68:71], a[132:135], v[216:219], v[230:233]\n" \
"v_cvt_pk_bf16_f32 v198, v140, v141\n" \
"v_cvt_pk_bf16_f32 v199, v142, v143\n" \
"v_mfma_f32_16x16x32_bf16 v[100:103], a[132:135], v[220:223], v[230:233]\n" \
"v_exp_f32_e64 v145, v145\n" \
"v_mfma_f32_16x16x32_bf16 v[72:75], a[136:139], v[216:219], v[230:233]\n" \
"ds_read_b64_tr_b16 a[162:163], %[vvb] offset:12288\n" \
"ds_read_b64_tr_b16 a[164:165], v211 offset:8192\n" \
"v_mfma_f32_16x16x32_bf16 v[104:107], a[136:139], v[220:223], v[230:233]\n" \
"v_exp_f32_e64 v146, v146\n" \
"v_mfma_f32_16x16x32_bf16 v[76:79], a[140:143], v[216:219], v[230:233]\n" \
"ds_read_b64_tr_b16 a[166:167], v211 offset:12288\n" \
"v_mfma_f32_16x16x32_bf16 v[108:111], a[140:143], v[220:223], v[230:233]\n" \
"v_exp_f32_e64 v147, v147\n" \
"v_smfmac_f32_16x16x64_bf16 v[180:183], a[192:195], v[216:223], v228\n" \
"ds_read_b64_tr_b16 a[168:169], %[vvb] offset:8704\n" \
"v_mfma_f32_16x16x32_bf16 v[152:155], a[96:99], a[48:51], v[172:175]\n" \
"v_exp_f32_e64 v148, v148\n" \
"v_mfma_f32_16x16x32_bf16 v[152:155], a[100:103], a[52:55], v[152:155]\n" \
"ds_read_b64_tr_b16 a[170:171], %[vvb] offset:12800\n" \
"v_mfma_f32_16x16x32_bf16 v[152:155], a[104:107], a[56:59], v[152:155]\n" \
"v_exp_f32_e64 v149, v149\n" \
"v_mfma_f32_16x16x32_bf16 v[152:155], a[108:111], a[60:63], v[152:155]\n" \
"v_cvt_pk_bf16_f32 v200, v144, v145\n" \
"v_cvt_pk_bf16_f32 v201, v146, v147\n" \
"v_mfma_f32_16x16x32_bf16 v[156:159], a[124:127], a[60:63], v[172:175]\n" \
"v_exp_f32_e64 v150, v150\n" \
"v_mfma_f32_16x16x32_bf16 v[156:159], a[120:123], a[56:59], v[156:159]\n" \
"ds_read_b64_tr_b16 a[172:173], v211 offset:8704\n" \
"v_mfma_f32_16x16x32_bf16 v[156:159], a[116:119], a[52:55], v[156:159]\n" \
"buffer_load_dwordx4 v215, %[srdv], s48 offen offset:3072 lds\n" \
"v_mfma_f32_16x16x32_bf16 v[156:159], a[112:115], a[48:51], v[156:159]\n" \
"v_exp_f32_e64 v151, v151\n" \
"v_mfma_f32_16x16x32_bf16 v[80:83], a[144:147], v[216:219], v[230:233]\n" \
"ds_read_b64_tr_b16 a[174:175], v211 offset:12800\n" \
"ds_read_b64_tr_b16 a[176:177], %[vvb] offset:9216\n" \
"v_mfma_f32_16x16x32_bf16 v[112:115], a[144:147], v[220:223], v[230:233]\n" \
"v_exp_f32_e64 v152, v152\n" \
"v_mfma_f32_16x16x32_bf16 v[84:87], a[148:151], v[216:219], v[230:233]\n" \
"v_cvt_pk_bf16_f32 v202, v148, v149\n" \
"v_cvt_pk_bf16_f32 v203, v150, v151\n" \
"v_mfma_f32_16x16x32_bf16 v[116:119], a[148:151], v[220:223], v[230:233]\n" \
"v_exp_f32_e64 v153, v153\n" \
"v_mfma_f32_16x16x32_bf16 v[88:91], a[152:155], v[216:219], v[230:233]\n" \
"v_exp_f32_e64 v154, v154\n" \
"v_mfma_f32_16x16x32_bf16 v[120:123], a[152:155], v[220:223], v[230:233]\n" \
"ds_read_b64_tr_b16 a[178:179], %[vvb] offset:13312\n" \
"v_mfma_f32_16x16x32_bf16 v[92:95], a[156:159], v[216:219], v[230:233]\n" \
"v_exp_f32_e64 v155, v155\n" \
"v_mov_b64_e32 v[216:217], 0\n" \
"v_mov_b64_e32 v[218:219], 0\n" \
"v_mfma_f32_16x16x32_bf16 v[124:127], a[156:159], v[220:223], v[230:233]\n" \
"ds_read_b64_tr_b16 a[180:181], v211 offset:9216\n" \
"s_waitcnt lgkmcnt(13)\n" \
"s_nop 0\n" \
"LmA_%=:\n" \
"v_mfma_f32_16x16x32_bf16 v[128:131], a[64:67], a[0:3], v[160:163]\n" \
"v_exp_f32_e64 v156, v156\n" \
"v_mfma_f32_16x16x32_bf16 v[128:131], a[68:71], a[4:7], v[128:131]\n" \
"ds_read_b64_tr_b16 a[182:183], v211 offset:13312\n" \
"s_add_u32 m0, %[m0b], 0\n" \
"v_mfma_f32_16x16x32_bf16 v[128:131], a[72:75], a[8:11], v[128:131]\n" \
"v_exp_f32_e32 v157, v157\n" \
"v_mfma_f32_16x16x32_bf16 v[128:131], a[76:79], a[12:15], v[128:131]\n" \
"v_cvt_pk_bf16_f32 v204, v152, v153\n" \
"v_cvt_pk_bf16_f32 v205, v154, v155\n" \
"v_mfma_f32_16x16x32_bf16 v[132:135], a[92:95], a[12:15], v[160:163]\n" \
"v_exp_f32_e64 v158, v158\n" \
"v_mfma_f32_16x16x32_bf16 v[132:135], a[88:91], a[8:11], v[132:135]\n" \
"ds_read_b64_tr_b16 a[184:185], %[vvb] offset:9728\n" \
"s_waitcnt lgkmcnt(5)\n" \
"v_mfma_f32_16x16x32_bf16 v[132:135], a[84:87], a[4:7], v[132:135]\n" \
"buffer_load_dwordx4 %[kd0], %[srdk], s48 offen offset:0 lds\n" \
"v_mfma_f32_16x16x32_bf16 v[132:135], a[80:83], a[0:3], v[132:135]\n" \
"v_exp_f32_e32 v159, v159\n" \
"v_mfma_f32_16x16x32_bf16 v[64:67], a[128:131], v[216:219], v[64:67]\n" \
"ds_read_b64_tr_b16 a[186:187], %[vvb] offset:13824\n" \
"ds_read_b64_tr_b16 a[188:189], v211 offset:9728\n" \
"v_mfma_f32_16x16x32_bf16 v[64:67], a[160:163], v[200:203], v[64:67]\n" \
"v_exp_f32_e64 v128, v128\n" \
"v_mfma_f32_16x16x32_bf16 v[68:71], a[132:135], v[216:219], v[68:71]\n" \
"v_cvt_pk_bf16_f32 v206, v156, v157\n" \
"v_cvt_pk_bf16_f32 v207, v158, v159\n" \
"v_mfma_f32_16x16x32_bf16 v[68:71], a[164:167], v[200:203], v[68:71]\n" \
"v_exp_f32_e64 v129, v129\n" \
"v_mfma_f32_16x16x32_bf16 v[72:75], a[136:139], v[216:219], v[72:75]\n" \
"ds_read_b64_tr_b16 a[190:191], v211 offset:13824\n" \
"ds_read_b64_tr_b16 a[128:129], %[vvb] offset:16384\n" \
"s_add_u32 s60, s60, 1\n" \
"s_nop 0\n" \
"v_readlane_b32 s58, %[tlo], s60\n" \
"v_mfma_f32_16x16x32_bf16 v[72:75], a[168:171], v[200:203], v[72:75]\n" \
"v_exp_f32_e64 v130, v130\n" \
"v_mfma_f32_16x16x32_bf16 v[76:79], a[140:143], v[216:219], v[76:79]\n" \
"ds_read_b64_tr_b16 a[130:131], %[vvb] offset:20480\n" \
"ds_read_b64_tr_b16 a[132:133], v211 offset:16384\n" \
"v_readlane_b32 s59, %[thi], s60\n" \
"v_mfma_f32_16x16x32_bf16 v[76:79], a[172:175], v[200:203], v[76:79]\n" \
"v_exp_f32_e64 v131, v131\n" \
".if . - adaln_body != 12288\n" \
".error \"size model\"\n" \
".endif\n" \
"Lk2_%=:\n" \
"s_cbranch_execz Lk3_%=\n" \
"s_nop 0\n" \
"v_smfmac_f32_16x16x64_bf16 v[176:179], a[192:195], v[192:199], v228\n" \
"ds_read_b64_tr_b16 a[134:135], v211 offset:20480\n" \
"ds_read_b64_tr_b16 a[136:137], %[vvb] offset:16896\n" \
"v_mfma_f32_16x16x32_bf16 v[136:139], a[64:67], a[16:19], v[164:167]\n" \
"v_exp_f32_e64 v132, v132\n" \
"v_mfma_f32_16x16x32_bf16 v[136:139], a[68:71], a[20:23], v[136:139]\n" \
"ds_read_b64_tr_b16 a[138:139], %[vvb] offset:20992\n" \
"ds_read_b64_tr_b16 a[140:141], v211 offset:16896\n" \
"v_mfma_f32_16x16x32_bf16 v[136:139], a[72:75], a[24:27], v[136:139]\n" \
"v_exp_f32_e64 v133, v133\n" \
"v_mfma_f32_16x16x32_bf16 v[136:139], a[76:79], a[28:31], v[136:139]\n" \
"v_cvt_pk_bf16_f32 v184, v128, v129\n" \
"v_cvt_pk_bf16_f32 v185, v130, v131\n" \
"v_mfma_f32_16x16x32_bf16 v[140:143], a[92:95], a[28:31], v[164:167]\n" \
"v_exp_f32_e64 v134, v134\n" \
"v_mfma_f32_16x16x32_bf16 v[140:143], a[88:91], a[24:27], v[140:143]\n" \
"ds_read_b64_tr_b16 a[142:143], v211 offset:20992\n" \
"s_waitcnt lgkmcnt(8)\n" \
"v_mfma_f32_16x16x32_bf16 v[140:143], a[84:87], a[20:23], v[140:143]\n" \
"buffer_load_dwordx4 v212, %[srdk], s48 offen offset:1024 lds\n" \
"v_mfma_f32_16x16x32_bf16 v[140:143], a[80:83], a[16:19], v[140:143]\n" \
"v_exp_f32_e32 v135, v135\n" \
"v_mfma_f32_16x16x32_bf16 v[80:83], a[144:147], v[216:219], v[80:83]\n" \
"ds_read_b64_tr_b16 a[144:145], %[vvb] offset:17408\n" \
"ds_read_b64_tr_b16 a[146:147], %[vvb] offset:21504\n" \
"v_mfma_f32_16x16x32_bf16 v[80:83], a[176:179], v[200:203], v[80:83]\n" \
"v_exp_f32_e64 v136, v136\n" \
"v_mfma_f32_16x16x32_bf16 v[84:87], a[148:151], v[216:219], v[84:87]\n" \
"v_cvt_pk_bf16_f32 v186, v132, v133\n" \
"v_cvt_pk_bf16_f32 v187, v134, v135\n" \
"v_mfma_f32_16x16x32_bf16 v[84:87], a[180:183], v[200:203], v[84:87]\n" \
"v_exp_f32_e64 v137, v137\n" \
"v_mfma_f32_16x16x32_bf16 v[88:91], a[152:155], v[216:219], v[88:91]\n" \
"v_exp_f32_e64 v138, v138\n" \
"v_mfma_f32_16x16x32_bf16 v[88:91], a[184:187], v[200:203], v[88:91]\n" \
"ds_read_b64_tr_b16 a[148:149], v211 offset:17408\n" \
"ds_read_b64_tr_b16 a[150:151], v211 offset:21504\n" \
"v_mfma_f32_16x16x32_bf16 v[92:95], a[156:159], v[216:219], v[92:95]\n" \
"v_exp_f32_e64 v139, v139\n" \
"v_mfma_f32_16x16x32_bf16 v[92:95], a[188:191], v[200:203], v[92:95]\n" \
"ds_read_b64_tr_b16 a[152:153], %[vvb] offset:17920\n" \
"v_mfma_f32_16x16x32_bf16 v[144:147], a[64:67], a[32:35], v[168:171]\n" \
"v_exp_f32_e64 v140, v140\n" \
"v_mfma_f32_16x16x32_bf16 v[144:147], a[68:71], a[36:39], v[144:147]\n" \
"ds_read_b64_tr_b16 a[154:155], %[vvb] offset:22016\n" \
"ds_read_b64_tr_b16 a[156:157], v211 offset:17920\n" \
"v_mfma_f32_16x16x32_bf16 v[144:147], a[72:75], a[40:43], v[144:147]\n" \
"v_exp_f32_e64 v141, v141\n" \
"v_mfma_f32_16x16x32_bf16 v[144:147], a[76:79], a[44:47], v[144:147]\n" \
"v_cvt_pk_bf16_f32 v188, v136, v137\n" \
"v_cvt_pk_bf16_f32 v189, v138, v139\n" \
"v_mfma_f32_16x16x32_bf16 v[148:151], a[92:95], a[44:47], v[168:171]\n" \
"v_exp_f32_e64 v142, v142\n" \
"v_mfma_f32_16x16x32_bf16 v[148:151], a[88:91], a[40:43], v[148:151]\n" \
"ds_read_b64_tr_b16 a[158:159], v211 offset:22016\n" \
"s_waitcnt lgkmcnt(8)\n" \
"v_mfma_f32_16x16x32_bf16 v[148:151], a[84:87], a[36:39], v[148:151]\n" \
"buffer_load_dwordx4 v213, %[srdk], s48 offen offset:2048 lds\n" \
"v_mfma_f32_16x16x32_bf16 v[148:151], a[80:83], a[32:35], v[148:151]\n" \
"v_exp_f32_e32 v143, v143\n" \
"v_mfma_f32_16x16x32_bf16 v[0:3], a[160:163], v[192:195], v[0:3]\n" \
"ds_read_b128 a[96:99], %[kb0] offset:24576\n" \
"v_mfma_f32_16x16x32_bf16 v[0:3], a[128:131], v[184:187], v[0:3]\n" \
"v_exp_f32_e64 v144, v144\n" \
"v_mfma_f32_16x16x32_bf16 v[4:7], a[164:167], v[192:195], v[4:7]\n" \
"v_cvt_pk_bf16_f32 v190, v140, v141\n" \
"v_cvt_pk_bf16_f32 v191, v142, v143\n" \
"v_mfma_f32_16x16x32_bf16 v[4:7], a[132:135], v[184:187], v[4:7]\n" \
"v_exp_f32_e64 v145, v145\n" \
"v_mfma_f32_16x16x32_bf16 v[8:11], a[168:171], v[192:195], v[8:11]\n" \
"ds_read_b128 a[100:103], v208 offset:24576\n" \
"v_mfma_f32_16x16x32_bf16 v[8:11], a[136:139], v[184:187], v[8:11]\n" \
"v_exp_f32_e64 v146, v146\n" \
"v_mfma_f32_16x16x32_bf16 v[12:15], a[172:175], v[192:195], v[12:15]\n" \
"ds_read_b128 a[104:107], v209 offset:24576\n" \
"v_mfma_f32_16x16x32_bf16 v[12:15], a[140:143], v[184:187], v[12:15]\n" \
"v_exp_f32_e64 v147, v147\n" \
"v_smfmac_f32_16x16x64_bf16 v[180:183], a[192:195], v[200:207], v228\n" \
"ds_read_b128 a[108:111], v210 offset:24576\n" \
"v_mfma_f32_16x16x32_bf16 v[152:155], a[64:67], a[48:51], v[172:175]\n" \
"v_exp_f32_e64 v148, v148\n" \
"v_mfma_f32_16x16x32_bf16 v[152:155], a[68:71], a[52:55], v[152:155]\n" \
"ds_read_b128 a[124:127], v210 offset:28672\n" \
"v_mfma_f32_16x16x32_bf16 v[152:155], a[72:75], a[56:59], v[152:155]\n" \
"v_exp_f32_e64 v149, v149\n" \
"v_mfma_f32_16x16x32_bf16 v[152:155], a[76:79], a[60:63], v[152:155]\n" \
"v_cvt_pk_bf16_f32 v216, v144, v145\n" \
"v_cvt_pk_bf16_f32 v217, v146, v147\n" \
"v_mfma_f32_16x16x32_bf16 v[156:159], a[92:95], a[60:63], v[172:175]\n" \
"v_exp_f32_e64 v150, v150\n" \
"v_mfma_f32_16x16x32_bf16 v[156:159], a[88:91], a[56:59], v[156:159]\n" \
"ds_read_b128 a[120:123], v209 offset:28672\n" \
"s_waitcnt lgkmcnt(6)\n" \
"v_mfma_f32_16x16x32_bf16 v[156:159], a[84:87], a[52:55], v[156:159]\n" \
"buffer_load_dwordx4 v214, %[srdk], s48 offen offset:3072 lds\n" \
"v_mfma_f32_16x16x32_bf16 v[156:159], a[80:83], a[48:51], v[156:159]\n" \
"v_exp_f32_e32 v151, v151\n" \
"v_mfma_f32_16x16x32_bf16 v[16:19], a[176:179], v[192:195], v[16:19]\n" \
"ds_read_b128 a[116:119], v208 offset:28672\n" \
"v_mfma_f32_16x16x32_bf16 v[16:19], a[144:147], v[184:187], v[16:19]\n" \
"v_exp_f32_e64 v152, v152\n" \
"v_mfma_f32_16x16x32_bf16 v[20:23], a[180:183], v[192:195], v[20:23]\n" \
"v_cvt_pk_bf16_f32 v218, v148, v149\n" \
"v_cvt_pk_bf16_f32 v219, v150, v151\n" \
"v_mfma_f32_16x16x32_bf16 v[20:23], a[148:151], v[184:187], v[20:23]\n" \
"v_exp_f32_e64 v153, v153\n" \
"v_mfma_f32_16x16x32_bf16 v[24:27], a[184:187], v[192:195], v[24:27]\n" \
"v_exp_f32_e64 v154, v154\n" \
"v_mfma_f32_16x16x32_bf16 v[24:27], a[152:155], v[184:187], v[24:27]\n" \
"ds_read_b128 a[112:115], %[kb0] offset:28672\n" \
"s_add_u32 s48, s48, 0x4000\n" \
"s_mov_b32 m0, s44\n" \
"s_addk_i32 s44, 0x400\n" \
"buffer_load_dwordx4 v229, s[64:67], s45 offen offset:0 nt lds\n" \
"s_add_u32 s45, s45, 393216\n" \
"v_mfma_f32_16x16x32_bf16 v[28:31], a[188:191], v[192:195], v[28:31]\n" \
"v_exp_f32_e64 v155, v155\n" \
"v_mfma_f32_16x16x32_bf16 v[28:31], a[156:159], v[184:187], v[28:31]\n" \
"s_waitcnt vmcnt(13) lgkmcnt(2)\n" \
"s_barrier\n" \
"v_mfma_f32_16x16x32_bf16 v[128:131], a[96:99], a[0:3], v[160:163]\n" \
"v_exp_f32_e64 v156, v156\n" \
"v_mfma_f32_16x16x32_bf16 v[128:131], a[100:103], a[4:7], v[128:131]\n" \
"s_add_u32 m0, %[m0b], 65536\n" \
"v_mfma_f32_16x16x32_bf16 v[128:131], a[104:107], a[8:11], v[128:131]\n" \
"v_exp_f32_e64 v157, v157\n" \
"v_mfma_f32_16x16x32_bf16 v[128:131], a[108:111], a[12:15], v[128:131]\n" \
"v_cvt_pk_bf16_f32 v220, v152, v153\n" \
"v_cvt_pk_bf16_f32 v221, v154, v155\n" \
"v_mfma_f32_16x16x32_bf16 v[132:135], a[124:127], a[12:15], v[160:163]\n" \
"v_exp_f32_e64 v158, v158\n" \
"v_mfma_f32_16x16x32_bf16 v[132:135], a[120:123], a[8:11], v[132:135]\n" \
"s_waitcnt lgkmcnt(0)\n" \
"v_mfma_f32_16x16x32_bf16 v[132:135], a[116:119], a[4:7], v[132:135]\n" \
"buffer_load_dwordx4 %[vd0], %[srdv], s48 offen offset:0 lds\n" \
"v_mfma_f32_16x16x32_bf16 v[132:135], a[112:115], a[0:3], v[132:135]\n" \
"v_exp_f32_e32 v159, v159\n" \
"v_mfma_f32_16x16x32_bf16 v[32:35], a[160:163], v[196:199], v[32:35]\n" \
"ds_read_b128 a[64:67], %[kb0] offset:32768\n" \
"v_mfma_f32_16x16x32_bf16 v[32:35], a[128:131], v[188:191], v[32:35]\n" \
"v_exp_f32_e64 v128, v128\n" \
"v_mfma_f32_16x16x32_bf16 v[36:39], a[164:167], v[196:199], v[36:39]\n" \
"v_cvt_pk_bf16_f32 v222, v156, v157\n" \
"v_cvt_pk_bf16_f32 v223, v158, v159\n" \
"v_mfma_f32_16x16x32_bf16 v[36:39], a[132:135], v[188:191], v[36:39]\n" \
"v_exp_f32_e64 v129, v129\n" \
"v_mfma_f32_16x16x32_bf16 v[40:43], a[168:171], v[196:199], v[40:43]\n" \
"ds_read_b128 a[68:71], v208 offset:32768\n" \
"v_mfma_f32_16x16x32_bf16 v[40:43], a[136:139], v[188:191], v[40:43]\n" \
"v_exp_f32_e64 v130, v130\n" \
"v_mfma_f32_16x16x32_bf16 v[44:47], a[172:175], v[196:199], v[44:47]\n" \
"ds_read_b128 a[72:75], v209 offset:32768\n" \
"v_mfma_f32_16x16x32_bf16 v[44:47], a[140:143], v[188:191], v[44:47]\n" \
"v_exp_f32_e64 v131, v131\n" \
"v_smfmac_f32_16x16x64_bf16 v[176:179], a[192:195], v[184:191], v228\n" \
"v_mfma_f32_16x16x32_bf16 v[136:139], a[96:99], a[16:19], v[164:167]\n" \
"v_exp_f32_e64 v132, v132\n" \
"v_mfma_f32_16x16x32_bf16 v[136:139], a[100:103], a[20:23], v[136:139]\n" \
"ds_read_b128 a[76:79], v210 offset:32768\n" \
"v_mfma_f32_16x16x32_bf16 v[136:139], a[104:107], a[24:27], v[136:139]\n" \
"v_exp_f32_e64 v133, v133\n" \
"v_mfma_f32_16x16x32_bf16 v[136:139], a[108:111], a[28:31], v[136:139]\n" \
"v_cvt_pk_bf16_f32 v192, v128, v129\n" \
"v_cvt_pk_bf16_f32 v193, v130, v131\n" \
"v_mfma_f32_16x16x32_bf16 v[140:143], a[124:127], a[28:31], v[164:167]\n" \
"v_exp_f32_e64 v134, v134\n" \
"v_mfma_f32_16x16x32_bf16 v[140:143], a[120:123], a[24:27], v[140:143]\n" \
"ds_read_b128 a[92:95], v210 offset:36864\n" \
"v_mfma_f32_16x16x32_bf16 v[140:143], a[116:119], a[20:23], v[140:143]\n" \
"buffer_load_dwordx4 v215, %[srdv], s48 offen offset:1024 lds\n" \
"v_mfma_f32_16x16x32_bf16 v[140:143], a[112:115], a[16:19], v[140:143]\n" \
"v_exp_f32_e64 v135, v135\n" \
"v_mfma_f32_16x16x32_bf16 v[48:51], a[176:179], v[196:199], v[48:51]\n" \
"ds_read_b128 a[88:91], v209 offset:36864\n" \
"v_mfma_f32_16x16x32_bf16 v[48:51], a[144:147], v[188:191], v[48:51]\n" \
"v_exp_f32_e64 v136, v136\n" \
"v_mfma_f32_16x16x32_bf16 v[52:55], a[180:183], v[196:199], v[52:55]\n" \
"v_cvt_pk_bf16_f32 v194, v132, v133\n" \
"v_cvt_pk_bf16_f32 v195, v134, v135\n" \
"v_mfma_f32_16x16x32_bf16 v[52:55], a[148:151], v[188:191], v[52:55]\n" \
"v_exp_f32_e64 v137, v137\n" \
"v_mfma_f32_16x16x32_bf16 v[56:59], a[184:187], v[196:199], v[56:59]\n" \
"v_exp_f32_e64 v138, v138\n" \
"v_mfma_f32_16x16x32_bf16 v[56:59], a[152:155], v[188:191], v[56:59]\n" \
"ds_read_b128 a[84:87], v208 offset:36864\n" \
"v_mfma_f32_16x16x32_bf16 v[60:63], a[188:191], v[196:199], v[60:63]\n" \
"v_exp_f32_e64 v139, v139\n" \
"v_mfma_f32_16x16x32_bf16 v[60:63], a[156:159], v[188:191], v[60:63]\n" \
"s_cmp_lg_u32 %[wid], 0\n" \
"s_cbranch_scc1 LtA3_%=\n" \
"global_load_dword v237, v224, s[58:59]\n" \
"s_add_u32 s61, s48, 49152\n" \
"s_mov_b32 exec_hi, 0\n" \
"s_nop 0\n" \
"buffer_load_dword v237, v224, %[srdp], s61 offen\n" \
"s_mov_b32 exec_hi, -1\n" \
"s_nop 0\n" \
"LtA3_%=:\n" \
"v_mfma_f32_16x16x32_bf16 v[144:147], a[96:99], a[32:35], v[168:171]\n" \
"v_exp_f32_e64 v140, v140\n" \
"v_mfma_f32_16x16x32_bf16 v[144:147], a[100:103], a[36:39], v[144:147]\n" \
"ds_read_b128 a[80:83], %[kb0] offset:36864\n" \
"v_mfma_f32_16x16x32_bf16 v[144:147], a[104:107], a[40:43], v[144:147]\n" \
"v_exp_f32_e64 v141, v141\n" \
"v_mfma_f32_16x16x32_bf16 v[144:147], a[108:111], a[44:47], v[144:147]\n" \
"v_cvt_pk_bf16_f32 v196, v136, v137\n" \
"v_cvt_pk_bf16_f32 v197, v138, v139\n" \
"v_mfma_f32_16x16x32_bf16 v[148:151], a[124:127], a[44:47], v[168:171]\n" \
"v_exp_f32_e64 v142, v142\n" \
"v_mfma_f32_16x16x32_bf16 v[148:151], a[120:123], a[40:43], v[148:151]\n" \
"v_mfma_f32_16x16x32_bf16 v[148:151], a[116:119], a[36:39], v[148:151]\n" \
"buffer_load_dwordx4 %[vd0], %[srdv], s48 offen offset:2048 lds\n" \
"v_mfma_f32_16x16x32_bf16 v[148:151], a[112:115], a[32:35], v[148:151]\n" \
"v_exp_f32_e64 v143, v143\n" \
"v_mfma_f32_16x16x32_bf16 v[96:99], a[160:163], v[204:207], v[96:99]\n" \
"ds_read_b64_tr_b16 a[160:161], %[vvb] offset:24576\n" \
"v_mfma_f32_16x16x32_bf16 v[96:99], a[128:131], v[220:223], v[96:99]\n" \
"v_exp_f32_e64 v144, v144\n" \
"v_mfma_f32_16x16x32_bf16 v[100:103], a[164:167], v[204:207], v[100:103]\n" \
"v_cvt_pk_bf16_f32 v198, v140, v141\n" \
"v_cvt_pk_bf16_f32 v199, v142, v143\n" \
"v_mfma_f32_16x16x32_bf16 v[100:103], a[132:135], v[220:223], v[100:103]\n" \
"v_exp_f32_e64 v145, v145\n" \
"v_mfma_f32_16x16x32_bf16 v[104:107], a[168:171], v[204:207], v[104:107]\n" \
"ds_read_b64_tr_b16 a[162:163], %[vvb] offset:28672\n" \
"ds_read_b64_tr_b16 a[164:165], v211 offset:24576\n" \
"v_mfma_f32_16x16x32_bf16 v[104:107], a[136:139], v[220:223], v[104:107]\n" \
"v_exp_f32_e64 v146, v146\n" \
"v_mfma_f32_16x16x32_bf16 v[108:111], a[172:175], v[204:207], v[108:111]\n" \
"ds_read_b64_tr_b16 a[166:167], v211 offset:28672\n" \
"v_mfma_f32_16x16x32_bf16 v[108:111], a[140:143], v[220:223], v[108:111]\n" \
"v_exp_f32_e64 v147, v147\n" \
"v_smfmac_f32_16x16x64_bf16 v[180:183], a[192:195], v[216:223], v228\n" \
"ds_read_b64_tr_b16 a[168:169], %[vvb] offset:25088\n" \
"v_mfma_f32_16x16x32_bf16 v[152:155], a[96:99], a[48:51], v[172:175]\n" \
"v_exp_f32_e64 v148, v148\n" \
"v_mfma_f32_16x16x32_bf16 v[152:155], a[100:103], a[52:55], v[152:155]\n" \
"ds_read_b64_tr_b16 a[170:171], %[vvb] offset:29184\n" \
"v_mfma_f32_16x16x32_bf16 v[152:155], a[104:107], a[56:59], v[152:155]\n" \
"v_exp_f32_e64 v149, v149\n" \
"v_mfma_f32_16x16x32_bf16 v[152:155], a[108:111], a[60:63], v[152:155]\n" \
"v_cvt_pk_bf16_f32 v200, v144, v145\n" \
"v_cvt_pk_bf16_f32 v201, v146, v147\n" \
"v_mfma_f32_16x16x32_bf16 v[156:159], a[124:127], a[60:63], v[172:175]\n" \
"v_exp_f32_e64 v150, v150\n" \
"v_mfma_f32_16x16x32_bf16 v[156:159], a[120:123], a[56:59], v[156:159]\n" \
"ds_read_b64_tr_b16 a[172:173], v211 offset:25088\n" \
"v_mfma_f32_16x16x32_bf16 v[156:159], a[116:119], a[52:55], v[156:159]\n" \
"buffer_load_dwordx4 v215, %[srdv], s48 offen offset:3072 lds\n" \
"v_mfma_f32_16x16x32_bf16 v[156:159], a[112:115], a[48:51], v[156:159]\n" \
"v_exp_f32_e64 v151, v151\n" \
"v_mfma_f32_16x16x32_bf16 v[112:115], a[176:179], v[204:207], v[112:115]\n" \
"ds_read_b64_tr_b16 a[174:175], v211 offset:29184\n" \
"ds_read_b64_tr_b16 a[176:177], %[vvb] offset:25600\n" \
"v_mfma_f32_16x16x32_bf16 v[112:115], a[144:147], v[220:223], v[112:115]\n" \
"v_exp_f32_e64 v152, v152\n" \
"v_mfma_f32_16x16x32_bf16 v[116:119], a[180:183], v[204:207], v[116:119]\n" \
"v_cvt_pk_bf16_f32 v202, v148, v149\n" \
"v_cvt_pk_bf16_f32 v203, v150, v151\n" \
"v_mfma_f32_16x16x32_bf16 v[116:119], a[148:151], v[220:223], v[116:119]\n" \
"v_exp_f32_e64 v153, v153\n" \
"v_mfma_f32_16x16x32_bf16 v[120:123], a[184:187], v[204:207], v[120:123]\n" \
"v_exp_f32_e64 v154, v154\n" \
"v_mfma_f32_16x16x32_bf16 v[120:123], a[152:155], v[220:223], v[120:123]\n" \
"ds_read_b64_tr_b16 a[178:179], %[vvb] offset:29696\n" \
"v_mfma_f32_16x16x32_bf16 v[124:127], a[188:191], v[204:207], v[124:127]\n" \
"v_exp_f32_e64 v155, v155\n" \
"v_mfma_f32_16x16x32_bf16 v[124:127], a[156:159], v[220:223], v[124:127]\n" \
"ds_read_b64_tr_b16 a[180:181], v211 offset:25600\n" \
"s_waitcnt lgkmcnt(13)\n" \
"v_mfma_f32_16x16x32_bf16 v[128:131], a[64:67], a[0:3], v[160:163]\n" \
"v_exp_f32_e32 v156, v156\n" \
"v_mfma_f32_16x16x32_bf16 v[128:131], a[68:71], a[4:7], v[128:131]\n" \
"ds_read_b64_tr_b16 a[182:183], v211 offset:29696\n" \
"s_add_u32 m0, %[m0b], 16384\n" \
"v_mfma_f32_16x16x32_bf16 v[128:131], a[72:75], a[8:11], v[128:131]\n" \
"v_exp_f32_e64 v157, v157\n" \
"v_mfma_f32_16x16x32_bf16 v[128:131], a[76:79], a[12:15], v[128:131]\n" \
"v_cvt_pk_bf16_f32 v204, v152, v153\n" \
"v_cvt_pk_bf16_f32 v205, v154, v155\n" \
"v_mfma_f32_16x16x32_bf16 v[132:135], a[92:95], a[12:15], v[160:163]\n" \
"v_exp_f32_e64 v158, v158\n" \
"v_mfma_f32_16x16x32_bf16 v[132:135], a[88:91], a[8:11], v[132:135]\n" \
"ds_read_b64_tr_b16 a[184:185], %[vvb] offset:26112\n" \
"s_waitcnt lgkmcnt(5)\n" \
"v_mfma_f32_16x16x32_bf16 v[132:135], a[84:87], a[4:7], v[132:135]\n" \
"buffer_load_dwordx4 %[kd0], %[srdk], s48 offen offset:0 lds\n" \
"v_mfma_f32_16x16x32_bf16 v[132:135], a[80:83], a[0:3], v[132:135]\n" \
"v_exp_f32_e32 v159, v159\n" \
"v_mfma_f32_16x16x32_bf16 v[64:67], a[128:131], v[216:219], v[64:67]\n" \
"ds_read_b64_tr_b16 a[186:187], %[vvb] offset:30208\n" \
"ds_read_b64_tr_b16 a[188:189], v211 offset:26112\n" \
"v_mfma_f32_16x16x32_bf16 v[64:67], a[160:163], v[200:203], v[64:67]\n" \
"v_exp_f32_e64 v128, v128\n" \
"v_mfma_f32_16x16x32_bf16 v[68:71], a[132:135], v[216:219], v[68:71]\n" \
"v_cvt_pk_bf16_f32 v206, v156, v157\n" \
"v_cvt_pk_bf16_f32 v207, v158, v159\n" \
"v_mfma_f32_16x16x32_bf16 v[68:71], a[164:167], v[200:203], v[68:71]\n" \
"v_exp_f32_e64 v129, v129\n" \
"v_mfma_f32_16x16x32_bf16 v[72:75], a[136:139], v[216:219], v[72:75]\n" \
"ds_read_b64_tr_b16 a[190:191], v211 offset:30208\n" \
"ds_read_b64_tr_b16 a[128:129], %[vvb] offset:32768\n" \
"v_mfma_f32_16x16x32_bf16 v[72:75], a[168:171], v[200:203], v[72:75]\n" \
"v_exp_f32_e64 v130, v130\n" \
"v_mfma_f32_16x16x32_bf16 v[76:79], a[140:143], v[216:219], v[76:79]\n" \
"ds_read_b64_tr_b16 a[130:131], %[vvb] offset:36864\n" \
"ds_read_b64_tr_b16 a[132:133], v211 offset:32768\n" \
"v_mfma_f32_16x16x32_bf16 v[76:79], a[172:175], v[200:203], v[76:79]\n" \
"v_exp_f32_e64 v131, v131\n" \
"v_smfmac_f32_16x16x64_bf16 v[176:179], a[192:195], v[192:199], v228\n" \
"ds_read_b64_tr_b16 a[134:135], v211 offset:36864\n" \
"ds_read_b64_tr_b16 a[136:137], %[vvb] offset:33280\n" \
"v_mfma_f32_16x16x32_bf16 v[136:139], a[64:67], a[16:19], v[164:167]\n" \
"v_exp_f32_e64 v132, v132\n" \
"v_mfma_f32_16x16x32_bf16 v[136:139], a[68:71], a[20:23], v[136:139]\n" \
"ds_read_b64_tr_b16 a[138:139], %[vvb] offset:37376\n" \
"ds_read_b64_tr_b16 a[140:141], v211 offset:33280\n" \
"v_mfma_f32_16x16x32_bf16 v[136:139], a[72:75], a[24:27], v[136:139]\n" \
"v_exp_f32_e64 v133, v133\n" \
"v_mfma_f32_16x16x32_bf16 v[136:139], a[76:79], a[28:31], v[136:139]\n" \
"v_cvt_pk_bf16_f32 v184, v128, v129\n" \
"v_cvt_pk_bf16_f32 v185, v130, v131\n" \
"v_mfma_f32_16x16x32_bf16 v[140:143], a[92:95], a[28:31], v[164:167]\n" \
"v_exp_f32_e64 v134, v134\n" \
"v_mfma_f32_16x16x32_bf16 v[140:143], a[88:91], a[24:27], v[140:143]\n" \
"ds_read_b64_tr_b16 a[142:143], v211 offset:37376\n" \
"s_waitcnt lgkmcnt(8)\n" \
"v_mfma_f32_16x16x32_bf16 v[140:143], a[84:87], a[20:23], v[140:143]\n" \
"buffer_load_dwordx4 v212, %[srdk], s48 offen offset:1024 lds\n" \
"v_mfma_f32_16x16x32_bf16 v[140:143], a[80:83], a[16:19], v[140:143]\n" \
"v_exp_f32_e32 v135, v135\n" \
"v_mfma_f32_16x16x32_bf16 v[80:83], a[144:147], v[216:219], v[80:83]\n" \
"ds_read_b64_tr_b16 a[144:145], %[vvb] offset:33792\n" \
"ds_read_b64_tr_b16 a[146:147], %[vvb] offset:37888\n" \
"v_mfma_f32_16x16x32_bf16 v[80:83], a[176:179], v[200:203], v[80:83]\n" \
"v_exp_f32_e64 v136, v136\n" \
"v_mfma_f32_16x16x32_bf16 v[84:87], a[148:151], v[216:219], v[84:87]\n" \
"v_cvt_pk_bf16_f32 v186, v132, v133\n" \
"v_cvt_pk_bf16_f32 v187, v134, v135\n" \
"v_mfma_f32_16x16x32_bf16 v[84:87], a[180:183], v[200:203], v[84:87]\n" \
"v_exp_f32_e64 v137, v137\n" \
"v_mfma_f32_16x16x32_bf16 v[88:91], a[152:155], v[216:219], v[88:91]\n" \
"v_exp_f32_e64 v138, v138\n" \
"v_mfma_f32_16x16x32_bf16 v[88:91], a[184:187], v[200:203], v[88:91]\n" \
"ds_read_b64_tr_b16 a[148:149], v211 offset:33792\n" \
"ds_read_b64_tr_b16 a[150:151], v211 offset:37888\n" \
"v_mfma_f32_16x16x32_bf16 v[92:95], a[156:159], v[216:219], v[92:95]\n" \
"v_exp_f32_e64 v139, v139\n" \
"v_mfma_f32_16x16x32_bf16 v[92:95], a[188:191], v[200:203], v[92:95]\n" \
"ds_read_b64_tr_b16 a[152:153], %[vvb] offset:34304\n" \
"s_cbranch_vccz Lpp1_%=\n" \
"buffer_store_dwordx4 a[196:199], v235, %[srdz], s62 offen offset:0 sc1\n" \
"buffer_store_dwordx4 a[196:199], v226, %[srdz], s62 offen offset:0 sc1\n" \
"Lpp1_%=:\n" \
"v_mfma_f32_16x16x32_bf16 v[144:147], a[64:67], a[32:35], v[168:171]\n" \
"v_exp_f32_e32 v140, v140\n" \
"v_mfma_f32_16x16x32_bf16 v[144:147], a[68:71], a[36:39], v[144:147]\n" \
"ds_read_b64_tr_b16 a[154:155], %[vvb] offset:38400\n" \
"ds_read_b64_tr_b16 a[156:157], v211 offset:34304\n" \
"v_mfma_f32_16x16x32_bf16 v[144:147], a[72:75], a[40:43], v[144:147]\n" \
"v_exp_f32_e64 v141, v141\n" \
"v_mfma_f32_16x16x32_bf16 v[144:147], a[76:79], a[44:47], v[144:147]\n" \
"v_cvt_pk_bf16_f32 v188, v136, v137\n" \
"v_cvt_pk_bf16_f32 v189, v138, v139\n" \
"v_mfma_f32_16x16x32_bf16 v[148:151], a[92:95], a[44:47], v[168:171]\n" \
"v_exp_f32_e64 v142, v142\n" \
"v_mfma_f32_16x16x32_bf16 v[148:151], a[88:91], a[40:43], v[148:151]\n" \
"ds_read_b64_tr_b16 a[158:159], v211 offset:38400\n" \
"s_waitcnt lgkmcnt(8)\n" \
"v_mfma_f32_16x16x32_bf16 v[148:151], a[84:87], a[36:39], v[148:151]\n" \
"buffer_load_dwordx4 v213, %[srdk], s48 offen offset:2048 lds\n" \
"v_mfma_f32_16x16x32_bf16 v[148:151], a[80:83], a[32:35], v[148:151]\n" \
"v_exp_f32_e32 v143, v143\n" \
"v_mfma_f32_16x16x32_bf16 v[0:3], a[160:163], v[192:195], v[0:3]\n" \
"ds_read_b128 a[96:99], %[kb0] offset:40960\n" \
"v_mfma_f32_16x16x32_bf16 v[0:3], a[128:131], v[184:187], v[0:3]\n" \
"v_exp_f32_e64 v144, v144\n" \
"v_mfma_f32_16x16x32_bf16 v[4:7], a[164:167], v[192:195], v[4:7]\n" \
"v_cvt_pk_bf16_f32 v190, v140, v141\n" \
"v_cvt_pk_bf16_f32 v191, v142, v143\n" \
"v_mfma_f32_16x16x32_bf16 v[4:7], a[132:135], v[184:187], v[4:7]\n" \
"v_exp_f32_e64 v145, v145\n" \
"v_mfma_f32_16x16x32_bf16 v[8:11], a[168:171], v[192:195], v[8:11]\n" \
"ds_read_b128 a[100:103], v208 offset:40960\n" \
"v_mfma_f32_16x16x32_bf16 v[8:11], a[136:139], v[184:187], v[8:11]\n" \
"v_exp_f32_e64 v146, v146\n" \
"v_mfma_f32_16x16x32_bf16 v[12:15], a[172:175], v[192:195], v[12:15]\n" \
"ds_read_b128 a[104:107], v209 offset:40960\n" \
"v_mfma_f32_16x16x32_bf16 v[12:15], a[140:143], v[184:187], v[12:15]\n" \
"v_exp_f32_e64 v147, v147\n" \
"v_smfmac_f32_16x16x64_bf16 v[180:183], a[192:195], v[200:207], v228\n" \
"ds_read_b128 a[108:111], v210 offset:40960\n" \
"v_mfma_f32_16x16x32_bf16 v[152:155], a[64:67], a[48:51], v[172:175]\n" \
"v_exp_f32_e64 v148, v148\n" \
"v_mfma_f32_16x16x32_bf16 v[152:155], a[68:71], a[52:55], v[152:155]\n" \
"ds_read_b128 a[124:127], v210 offset:45056\n" \
"v_mfma_f32_16x16x32_bf16 v[152:155], a[72:75], a[56:59], v[152:155]\n" \
"v_exp_f32_e64 v149, v149\n" \
"v_mfma_f32_16x16x32_bf16 v[152:155], a[76:79], a[60:63], v[152:155]\n" \
"v_cvt_pk_bf16_f32 v216, v144, v145\n" \
"v_cvt_pk_bf16_f32 v217, v146, v147\n" \
"v_mfma_f32_16x16x32_bf16 v[156:159], a[92:95], a[60:63], v[172:175]\n" \
"v_exp_f32_e64 v150, v150\n" \
"v_mfma_f32_16x16x32_bf16 v[156:159], a[88:91], a[56:59], v[156:159]\n" \
"ds_read_b128 a[120:123], v209 offset:45056\n" \
"s_waitcnt lgkmcnt(6)\n" \
"v_mfma_f32_16x16x32_bf16 v[156:159], a[84:87], a[52:55], v[156:159]\n" \
"buffer_load_dwordx4 v214, %[srdk], s48 offen offset:3072 lds\n" \
"v_mfma_f32_16x16x32_bf16 v[156:159], a[80:83], a[48:51], v[156:159]\n" \
"v_exp_f32_e32 v151, v151\n" \
"v_mfma_f32_16x16x32_bf16 v[16:19], a[176:179], v[192:195], v[16:19]\n" \
"ds_read_b128 a[116:119], v208 offset:45056\n" \
"v_mfma_f32_16x16x32_bf16 v[16:19], a[144:147], v[184:187], v[16:19]\n" \
"v_exp_f32_e64 v152, v152\n" \
"v_mfma_f32_16x16x32_bf16 v[20:23], a[180:183], v[192:195], v[20:23]\n" \
"v_cvt_pk_bf16_f32 v218, v148, v149\n" \
"v_cvt_pk_bf16_f32 v219, v150, v151\n" \
"v_mfma_f32_16x16x32_bf16 v[20:23], a[148:151], v[184:187], v[20:23]\n" \
"v_exp_f32_e64 v153, v153\n" \
"v_mfma_f32_16x16x32_bf16 v[24:27], a[184:187], v[192:195], v[24:27]\n" \
"v_exp_f32_e64 v154, v154\n" \
"v_mfma_f32_16x16x32_bf16 v[24:27], a[152:155], v[184:187], v[24:27]\n" \
"ds_read_b128 a[112:115], %[kb0] offset:45056\n" \
"s_add_u32 s48, s48, 0x4000\n" \
"v_mfma_f32_16x16x32_bf16 v[28:31], a[188:191], v[192:195], v[28:31]\n" \
"v_exp_f32_e64 v155, v155\n" \
"v_mfma_f32_16x16x32_bf16 v[28:31], a[156:159], v[184:187], v[28:31]\n" \
"s_waitcnt vmcnt(13) lgkmcnt(2)\n" \
"s_barrier\n" \
"v_mfma_f32_16x16x32_bf16 v[128:131], a[96:99], a[0:3], v[160:163]\n" \
"v_exp_f32_e64 v156, v156\n" \
"v_mfma_f32_16x16x32_bf16 v[128:131], a[100:103], a[4:7], v[128:131]\n" \
"s_add_u32 m0, %[m0b], 81920\n" \
"v_mfma_f32_16x16x32_bf16 v[128:131], a[104:107], a[8:11], v[128:131]\n" \
"v_exp_f32_e64 v157, v157\n" \
"v_mfma_f32_16x16x32_bf16 v[128:131], a[108:111], a[12:15], v[128:131]\n" \
"v_cvt_pk_bf16_f32 v220, v152, v153\n" \
"v_cvt_pk_bf16_f32 v221, v154, v155\n" \
"v_mfma_f32_16x16x32_bf16 v[132:135], a[124:127], a[12:15], v[160:163]\n" \
"v_exp_f32_e64 v158, v158\n" \
"v_mfma_f32_16x16x32_bf16 v[132:135], a[120:123], a[8:11], v[132:135]\n" \
"s_waitcnt lgkmcnt(0)\n" \
"v_mfma_f32_16x16x32_bf16 v[132:135], a[116:119], a[4:7], v[132:135]\n" \
"buffer_load_dwordx4 %[vd0], %[srdv], s48 offen offset:0 lds\n" \
"v_mfma_f32_16x16x32_bf16 v[132:135], a[112:115], a[0:3], v[132:135]\n" \
"v_exp_f32_e32 v159, v159\n" \
"v_mfma_f32_16x16x32_bf16 v[32:35], a[160:163], v[196:199], v[32:35]\n" \
"ds_read_b128 a[64:67], %[kb0] offset:49152\n" \
"v_mfma_f32_16x16x32_bf16 v[32:35], a[128:131], v[188:191], v[32:35]\n" \
"v_exp_f32_e64 v128, v128\n" \
"v_mfma_f32_16x16x32_bf16 v[36:39], a[164:167], v[196:199], v[36:39]\n" \
"v_cvt_pk_bf16_f32 v222, v156, v157\n" \
"v_cvt_pk_bf16_f32 v223, v158, v159\n" \
"v_mfma_f32_16x16x32_bf16 v[36:39], a[132:135], v[188:191], v[36:39]\n" \
"v_exp_f32_e64 v129, v129\n" \
"v_mfma_f32_16x16x32_bf16 v[40:43], a[168:171], v[196:199], v[40:43]\n" \
"ds_read_b128 a[68:71], v208 offset:49152\n" \
"v_mfma_f32_16x16x32_bf16 v[40:43], a[136:139], v[188:191], v[40:43]\n" \
"v_exp_f32_e64 v130, v130\n" \
"v_mfma_f32_16x16x32_bf16 v[44:47], a[172:175], v[196:199], v[44:47]\n" \
"ds_read_b128 a[72:75], v209 offset:49152\n" \
"v_mfma_f32_16x16x32_bf16 v[44:47], a[140:143], v[188:191], v[44:47]\n" \
"v_exp_f32_e64 v131, v131\n" \
"v_smfmac_f32_16x16x64_bf16 v[176:179], a[192:195], v[184:191], v228\n" \
"v_mfma_f32_16x16x32_bf16 v[136:139], a[96:99], a[16:19], v[164:167]\n" \
"v_exp_f32_e64 v132, v132\n" \
"v_mfma_f32_16x16x32_bf16 v[136:139], a[100:103], a[20:23], v[136:139]\n" \
"ds_read_b128 a[76:79], v210 offset:49152\n" \
"v_mfma_f32_16x16x32_bf16 v[136:139], a[104:107], a[24:27], v[136:139]\n" \
"v_exp_f32_e64 v133, v133\n" \
"v_mfma_f32_16x16x32_bf16 v[136:139], a[108:111], a[28:31], v[136:139]\n" \
"v_cvt_pk_bf16_f32 v192, v128, v129\n" \
"v_cvt_pk_bf16_f32 v193, v130, v131\n" \
"v_mfma_f32_16x16x32_bf16 v[140:143], a[124:127], a[28:31], v[164:167]\n" \
"v_exp_f32_e64 v134, v134\n" \
"v_mfma_f32_16x16x32_bf16 v[140:143], a[120:123], a[24:27], v[140:143]\n" \
"ds_read_b128 a[92:95], v210 offset:53248\n" \
"v_mfma_f32_16x16x32_bf16 v[140:143], a[116:119], a[20:23], v[140:143]\n" \
"buffer_load_dwordx4 v215, %[srdv], s48 offen offset:1024 lds\n" \
"v_mfma_f32_16x16x32_bf16 v[140:143], a[112:115], a[16:19], v[140:143]\n" \
"v_exp_f32_e64 v135, v135\n" \
"v_mfma_f32_16x16x32_bf16 v[48:51], a[176:179], v[196:199], v[48:51]\n" \
"ds_read_b128 a[88:91], v209 offset:53248\n" \
"v_mfma_f32_16x16x32_bf16 v[48:51], a[144:147], v[188:191], v[48:51]\n" \
"v_exp_f32_e64 v136, v136\n" \
"v_mfma_f32_16x16x32_bf16 v[52:55], a[180:183], v[196:199], v[52:55]\n" \
"v_cvt_pk_bf16_f32 v194, v132, v133\n" \
"v_cvt_pk_bf16_f32 v195, v134, v135\n" \
"v_mfma_f32_16x16x32_bf16 v[52:55], a[148:151], v[188:191], v[52:55]\n" \
"v_exp_f32_e64 v137, v137\n" \
"v_mfma_f32_16x16x32_bf16 v[56:59], a[184:187], v[196:199], v[56:59]\n" \
"v_exp_f32_e64 v138, v138\n" \
"v_mfma_f32_16x16x32_bf16 v[56:59], a[152:155], v[188:191], v[56:59]\n" \
"ds_read_b128 a[84:87], v208 offset:53248\n" \
"v_mfma_f32_16x16x32_bf16 v[60:63], a[188:191], v[196:199], v[60:63]\n" \
"v_exp_f32_e64 v139, v139\n" \
"v_mfma_f32_16x16x32_bf16 v[60:63], a[156:159], v[188:191], v[60:63]\n" \
"s_cmp_lg_u32 %[wid], 1\n" \
"s_cbranch_scc1 LtA5_%=\n" \
"global_load_dword v237, v224, s[58:59]\n" \
"s_add_u32 s61, s48, 49152\n" \
"s_mov_b32 exec_hi, 0\n" \
"s_nop 0\n" \
"buffer_load_dword v237, v224, %[srdp], s61 offen\n" \
"s_mov_b32 exec_hi, -1\n" \
"s_nop 0\n" \
"LtA5_%=:\n" \
"v_mfma_f32_16x16x32_bf16 v[144:147], a[96:99], a[32:35], v[168:171]\n" \
"v_exp_f32_e64 v140, v140\n" \
"v_mfma_f32_16x16x32_bf16 v[144:147], a[100:103], a[36:39], v[144:147]\n" \
"ds_read_b128 a[80:83], %[kb0] offset:53248\n" \
"v_mfma_f32_16x16x32_bf16 v[144:147], a[104:107], a[40:43], v[144:147]\n" \
"v_exp_f32_e64 v141, v141\n" \
"v_mfma_f32_16x16x32_bf16 v[144:147], a[108:111], a[44:47], v[144:147]\n" \
"v_cvt_pk_bf16_f32 v196, v136, v137\n" \
"v_cvt_pk_bf16_f32 v197, v138, v139\n" \
"v_mfma_f32_16x16x32_bf16 v[148:151], a[124:127], a[44:47], v[168:171]\n" \
"v_exp_f32_e64 v142, v142\n" \
"v_mfma_f32_16x16x32_bf16 v[148:151], a[120:123], a[40:43], v[148:151]\n" \
"v_mfma_f32_16x16x32_bf16 v[148:151], a[116:119], a[36:39], v[148:151]\n" \
"buffer_load_dwordx4 %[vd0], %[srdv], s48 offen offset:2048 lds\n" \
"v_mfma_f32_16x16x32_bf16 v[148:151], a[112:115], a[32:35], v[148:151]\n" \
"v_exp_f32_e64 v143, v143\n" \
"v_mfma_f32_16x16x32_bf16 v[96:99], a[160:163], v[204:207], v[96:99]\n" \
"ds_read_b64_tr_b16 a[160:161], %[vvb] offset:40960\n" \
"v_mfma_f32_16x16x32_bf16 v[96:99], a[128:131], v[220:223], v[96:99]\n" \
"v_exp_f32_e64 v144, v144\n" \
"v_mfma_f32_16x16x32_bf16 v[100:103], a[164:167], v[204:207], v[100:103]\n" \
"v_cvt_pk_bf16_f32 v198, v140, v141\n" \
"v_cvt_pk_bf16_f32 v199, v142, v143\n" \
"v_mfma_f32_16x16x32_bf16 v[100:103], a[132:135], v[220:223], v[100:103]\n" \
"v_exp_f32_e64 v145, v145\n" \
"v_mfma_f32_16x16x32_bf16 v[104:107], a[168:171], v[204:207], v[104:107]\n" \
"ds_read_b64_tr_b16 a[162:163], %[vvb] offset:45056\n" \
".if . - adaln_body != 16384\n" \
".error \"size model\"\n" \
".endif\n" \
"Lk3_%=:\n" \
"s_cbranch_execz Lk4_%=\n" \
"s_nop 0\n" \
"ds_read_b64_tr_b16 a[164:165], v211 offset:40960\n" \
"v_mfma_f32_16x16x32_bf16 v[104:107], a[136:139], v[220:223], v[104:107]\n" \
"v_exp_f32_e64 v146, v146\n" \
"v_mfma_f32_16x16x32_bf16 v[108:111], a[172:175], v[204:207], v[108:111]\n" \
"ds_read_b64_tr_b16 a[166:167], v211 offset:45056\n" \
"v_mfma_f32_16x16x32_bf16 v[108:111], a[140:143], v[220:223], v[108:111]\n" \
"v_exp_f32_e64 v147, v147\n" \
"v_smfmac_f32_16x16x64_bf16 v[180:183], a[192:195], v[216:223], v228\n" \
"ds_read_b64_tr_b16 a[168:169], %[vvb] offset:41472\n" \
"v_mfma_f32_16x16x32_bf16 v[152:155], a[96:99], a[48:51], v[172:175]\n" \
"v_exp_f32_e64 v148, v148\n" \
"v_mfma_f32_16x16x32_bf16 v[152:155], a[100:103], a[52:55], v[152:155]\n" \
"ds_read_b64_tr_b16 a[170:171], %[vvb] offset:45568\n" \
"v_mfma_f32_16x16x32_bf16 v[152:155], a[104:107], a[56:59], v[152:155]\n" \
"v_exp_f32_e64 v149, v149\n" \
"v_mfma_f32_16x16x32_bf16 v[152:155], a[108:111], a[60:63], v[152:155]\n" \
"v_cvt_pk_bf16_f32 v200, v144, v145\n" \
"v_cvt_pk_bf16_f32 v201, v146, v147\n" \
"v_mfma_f32_16x16x32_bf16 v[156:159], a[124:127], a[60:63], v[172:175]\n" \
"v_exp_f32_e64 v150, v150\n" \
"v_mfma_f32_16x16x32_bf16 v[156:159], a[120:123], a[56:59], v[156:159]\n" \
"ds_read_b64_tr_b16 a[172:173], v211 offset:41472\n" \
"v_mfma_f32_16x16x32_bf16 v[156:159], a[116:119], a[52:55], v[156:159]\n" \
"buffer_load_dwordx4 v215, %[srdv], s48 offen offset:3072 lds\n" \
"v_mfma_f32_16x16x32_bf16 v[156:159], a[112:115], a[48:51], v[156:159]\n" \
"v_exp_f32_e64 v151, v151\n" \
"v_mfma_f32_16x16x32_bf16 v[112:115], a[176:179], v[204:207], v[112:115]\n" \
"ds_read_b64_tr_b16 a[174:175], v211 offset:45568\n" \
"ds_read_b64_tr_b16 a[176:177], %[vvb] offset:41984\n" \
"v_mfma_f32_16x16x32_bf16 v[112:115], a[144:147], v[220:223], v[112:115]\n" \
"v_exp_f32_e64 v152, v152\n" \
"v_mfma_f32_16x16x32_bf16 v[116:119], a[180:183], v[204:207], v[116:119]\n" \
"v_cvt_pk_bf16_f32 v202, v148, v149\n" \
"v_cvt_pk_bf16_f32 v203, v150, v151\n" \
"v_mfma_f32_16x16x32_bf16 v[116:119], a[148:151], v[220:223], v[116:119]\n" \
"v_exp_f32_e64 v153, v153\n" \
"v_mfma_f32_16x16x32_bf16 v[120:123], a[184:187], v[204:207], v[120:123]\n" \
"v_exp_f32_e64 v154, v154\n" \
"v_mfma_f32_16x16x32_bf16 v[120:123], a[152:155], v[220:223], v[120:123]\n" \
"ds_read_b64_tr_b16 a[178:179], %[vvb] offset:46080\n" \
"v_mfma_f32_16x16x32_bf16 v[124:127], a[188:191], v[204:207], v[124:127]\n" \
"v_exp_f32_e64 v155, v155\n" \
"v_mfma_f32_16x16x32_bf16 v[124:127], a[156:159], v[220:223], v[124:127]\n" \
"ds_read_b64_tr_b16 a[180:181], v211 offset:41984\n" \
"s_waitcnt lgkmcnt(13)\n" \
"v_mfma_f32_16x16x32_bf16 v[128:131], a[64:67], a[0:3], v[160:163]\n" \
"v_exp_f32_e32 v156, v156\n" \
"v_mfma_f32_16x16x32_bf16 v[128:131], a[68:71], a[4:7], v[128:131]\n" \
"ds_read_b64_tr_b16 a[182:183], v211 offset:46080\n" \
"s_add_u32 m0, %[m0b], 32768\n" \
"v_mfma_f32_16x16x32_bf16 v[128:131], a[72:75], a[8:11], v[128:131]\n" \
"v_exp_f32_e64 v157, v157\n" \
"v_mfma_f32_16x16x32_bf16 v[128:131], a[76:79], a[12:15], v[128:131]\n" \
"v_cvt_pk_bf16_f32 v204, v152, v153\n" \
"v_cvt_pk_bf16_f32 v205, v154, v155\n" \
"v_mfma_f32_16x16x32_bf16 v[132:135], a[92:95], a[12:15], v[160:163]\n" \
"v_exp_f32_e64 v158, v158\n" \
"v_mfma_f32_16x16x32_bf16 v[132:135], a[88:91], a[8:11], v[132:135]\n" \
"ds_read_b64_tr_b16 a[184:185], %[vvb] offset:42496\n" \
"s_waitcnt lgkmcnt(5)\n" \
"v_mfma_f32_16x16x32_bf16 v[132:135], a[84:87], a[4:7], v[132:135]\n" \
"buffer_load_dwordx4 %[kd0], %[srdk], s48 offen offset:0 lds\n" \
"v_mfma_f32_16x16x32_bf16 v[132:135], a[80:83], a[0:3], v[132:135]\n" \
"v_exp_f32_e32 v159, v159\n" \
"v_mfma_f32_16x16x32_bf16 v[64:67], a[128:131], v[216:219], v[64:67]\n" \
"ds_read_b64_tr_b16 a[186:187], %[vvb] offset:46592\n" \
"ds_read_b64_tr_b16 a[188:189], v211 offset:42496\n" \
"v_mfma_f32_16x16x32_bf16 v[64:67], a[160:163], v[200:203], v[64:67]\n" \
"v_exp_f32_e64 v128, v128\n" \
"v_mfma_f32_16x16x32_bf16 v[68:71], a[132:135], v[216:219], v[68:71]\n" \
"v_cvt_pk_bf16_f32 v206, v156, v157\n" \
"v_cvt_pk_bf16_f32 v207, v158, v159\n" \
"v_mfma_f32_16x16x32_bf16 v[68:71], a[164:167], v[200:203], v[68:71]\n" \
"v_exp_f32_e64 v129, v129\n" \
"v_mfma_f32_16x16x32_bf16 v[72:75], a[136:139], v[216:219], v[72:75]\n" \
"ds_read_b64_tr_b16 a[190:191], v211 offset:46592\n" \
"ds_read_b64_tr_b16 a[128:129], %[vvb] offset:49152\n" \
"v_mfma_f32_16x16x32_bf16 v[72:75], a[168:171], v[200:203], v[72:75]\n" \
"v_exp_f32_e64 v130, v130\n" \
"v_mfma_f32_16x16x32_bf16 v[76:79], a[140:143], v[216:219], v[76:79]\n" \
"ds_read_b64_tr_b16 a[130:131], %[vvb] offset:53248\n" \
"ds_read_b64_tr_b16 a[132:133], v211 offset:49152\n" \
"v_mfma_f32_16x16x32_bf16 v[76:79], a[172:175], v[200:203], v[76:79]\n" \
"v_exp_f32_e64 v131, v131\n" \
"v_smfmac_f32_16x16x64_bf16 v[176:179], a[192:195], v[192:199], v228\n" \
"ds_read_b64_tr_b16 a[134:135], v211 offset:53248\n" \
"ds_read_b64_tr_b16 a[136:137], %[vvb] offset:49664\n" \
"v_mfma_f32_16x16x32_bf16 v[136:139], a[64:67], a[16:19], v[164:167]\n" \
"v_exp_f32_e64 v132, v132\n" \
"v_mfma_f32_16x16x32_bf16 v[136:139], a[68:71], a[20:23], v[136:139]\n" \
"ds_read_b64_tr_b16 a[138:139], %[vvb] offset:53760\n" \
"ds_read_b64_tr_b16 a[140:141], v211 offset:49664\n" \
"v_mfma_f32_16x16x32_bf16 v[136:139], a[72:75], a[24:27], v[136:139]\n" \
"v_exp_f32_e64 v133, v133\n" \
"v_mfma_f32_16x16x32_bf16 v[136:139], a[76:79], a[28:31], v[136:139]\n" \
"v_cvt_pk_bf16_f32 v184, v128, v129\n" \
"v_cvt_pk_bf16_f32 v185, v130, v131\n" \
"v_mfma_f32_16x16x32_bf16 v[140:143], a[92:95], a[28:31], v[164:167]\n" \
"v_exp_f32_e64 v134, v134\n" \
"v_mfma_f32_16x16x32_bf16 v[140:143], a[88:91], a[24:27], v[140:143]\n" \
"ds_read_b64_tr_b16 a[142:143], v211 offset:53760\n" \
"s_waitcnt lgkmcnt(8)\n" \
"v_mfma_f32_16x16x32_bf16 v[140:143], a[84:87], a[20:23], v[140:143]\n" \
"buffer_load_dwordx4 v212, %[srdk], s48 offen offset:1024 lds\n" \
"v_mfma_f32_16x16x32_bf16 v[140:143], a[80:83], a[16:19], v[140:143]\n" \
"v_exp_f32_e32 v135, v135\n" \
"v_mfma_f32_16x16x32_bf16 v[80:83], a[144:147], v[216:219], v[80:83]\n" \
"ds_read_b64_tr_b16 a[144:145], %[vvb] offset:50176\n" \
"ds_read_b64_tr_b16 a[146:147], %[vvb] offset:54272\n" \
"v_mfma_f32_16x16x32_bf16 v[80:83], a[176:179], v[200:203], v[80:83]\n" \
"v_exp_f32_e64 v136, v136\n" \
"v_mfma_f32_16x16x32_bf16 v[84:87], a[148:151], v[216:219], v[84:87]\n" \
"v_cvt_pk_bf16_f32 v186, v132, v133\n" \
"v_cvt_pk_bf16_f32 v187, v134, v135\n" \
"v_mfma_f32_16x16x32_bf16 v[84:87], a[180:183], v[200:203], v[84:87]\n" \
"v_exp_f32_e64 v137, v137\n" \
"v_mfma_f32_16x16x32_bf16 v[88:91], a[152:155], v[216:219], v[88:91]\n" \
"v_exp_f32_e64 v138, v138\n" \
"v_mfma_f32_16x16x32_bf16 v[88:91], a[184:187], v[200:203], v[88:91]\n" \
"ds_read_b64_tr_b16 a[148:149], v211 offset:50176\n" \
"ds_read_b64_tr_b16 a[150:151], v211 offset:54272\n" \
"v_mfma_f32_16x16x32_bf16 v[92:95], a[156:159], v[216:219], v[92:95]\n" \
"v_exp_f32_e64 v139, v139\n" \
"v_mfma_f32_16x16x32_bf16 v[92:95], a[188:191], v[200:203], v[92:95]\n" \
"ds_read_b64_tr_b16 a[152:153], %[vvb] offset:50688\n" \
"global_load_dword v237, %[vct], %[pct]\n" \
"v_mfma_f32_16x16x32_bf16 v[144:147], a[64:67], a[32:35], v[168:171]\n" \
"v_exp_f32_e64 v140, v140\n" \
"v_mfma_f32_16x16x32_bf16 v[144:147], a[68:71], a[36:39], v[144:147]\n" \
"ds_read_b64_tr_b16 a[154:155], %[vvb] offset:54784\n" \
"ds_read_b64_tr_b16 a[156:157], v211 offset:50688\n" \
"v_mfma_f32_16x16x32_bf16 v[144:147], a[72:75], a[40:43], v[144:147]\n" \
"v_exp_f32_e64 v141, v141\n" \
"v_mfma_f32_16x16x32_bf16 v[144:147], a[76:79], a[44:47], v[144:147]\n" \
"v_cvt_pk_bf16_f32 v188, v136, v137\n" \
"v_cvt_pk_bf16_f32 v189, v138, v139\n" \
"v_mfma_f32_16x16x32_bf16 v[148:151], a[92:95], a[44:47], v[168:171]\n" \
"v_exp_f32_e64 v142, v142\n" \
"v_mfma_f32_16x16x32_bf16 v[148:151], a[88:91], a[40:43], v[148:151]\n" \
"ds_read_b64_tr_b16 a[158:159], v211 offset:54784\n" \
"s_waitcnt lgkmcnt(8)\n" \
"v_mfma_f32_16x16x32_bf16 v[148:151], a[84:87], a[36:39], v[148:151]\n" \
"buffer_load_dwordx4 v213, %[srdk], s48 offen offset:2048 lds\n" \
"v_mfma_f32_16x16x32_bf16 v[148:151], a[80:83], a[32:35], v[148:151]\n" \
"v_exp_f32_e32 v143, v143\n" \
"v_mfma_f32_16x16x32_bf16 v[0:3], a[160:163], v[192:195], v[0:3]\n" \
"ds_read_b128 a[96:99], %[kb0] offset:57344\n" \
"v_mfma_f32_16x16x32_bf16 v[0:3], a[128:131], v[184:187], v[0:3]\n" \
"v_exp_f32_e64 v144, v144\n" \
"v_mfma_f32_16x16x32_bf16 v[4:7], a[164:167], v[192:195], v[4:7]\n" \
"v_cvt_pk_bf16_f32 v190, v140, v141\n" \
"v_cvt_pk_bf16_f32 v191, v142, v143\n" \
"v_mfma_f32_16x16x32_bf16 v[4:7], a[132:135], v[184:187], v[4:7]\n" \
"v_exp_f32_e64 v145, v145\n" \
"v_mfma_f32_16x16x32_bf16 v[8:11], a[168:171], v[192:195], v[8:11]\n" \
"ds_read_b128 a[100:103], v208 offset:57344\n" \
"v_mfma_f32_16x16x32_bf16 v[8:11], a[136:139], v[184:187], v[8:11]\n" \
"v_exp_f32_e64 v146, v146\n" \
"v_mfma_f32_16x16x32_bf16 v[12:15], a[172:175], v[192:195], v[12:15]\n" \
"ds_read_b128 a[104:107], v209 offset:57344\n" \
"v_mfma_f32_16x16x32_bf16 v[12:15], a[140:143], v[184:187], v[12:15]\n" \
"v_exp_f32_e64 v147, v147\n" \
"v_smfmac_f32_16x16x64_bf16 v[180:183], a[192:195], v[200:207], v228\n" \
"ds_read_b128 a[108:111], v210 offset:57344\n" \
"v_mfma_f32_16x16x32_bf16 v[152:155], a[64:67], a[48:51], v[172:175]\n" \
"v_exp_f32_e64 v148, v148\n" \
"v_mfma_f32_16x16x32_bf16 v[152:155], a[68:71], a[52:55], v[152:155]\n" \
"ds_read_b128 a[124:127], v210 offset:61440\n" \
"v_mfma_f32_16x16x32_bf16 v[152:155], a[72:75], a[56:59], v[152:155]\n" \
"v_exp_f32_e64 v149, v149\n" \
"v_mfma_f32_16x16x32_bf16 v[152:155], a[76:79], a[60:63], v[152:155]\n" \
"v_cvt_pk_bf16_f32 v216, v144, v145\n" \
"v_cvt_pk_bf16_f32 v217, v146, v147\n" \
"v_mfma_f32_16x16x32_bf16 v[156:159], a[92:95], a[60:63], v[172:175]\n" \
"v_exp_f32_e64 v150, v150\n" \
"v_mfma_f32_16x16x32_bf16 v[156:159], a[88:91], a[56:59], v[156:159]\n" \
"ds_read_b128 a[120:123], v209 offset:61440\n" \
"s_waitcnt lgkmcnt(6)\n" \
"v_mfma_f32_16x16x32_bf16 v[156:159], a[84:87], a[52:55], v[156:159]\n" \
"buffer_load_dwordx4 v214, %[srdk], s48 offen offset:3072 lds\n" \
"v_mfma_f32_16x16x32_bf16 v[156:159], a[80:83], a[48:51], v[156:159]\n" \
"v_exp_f32_e32 v151, v151\n" \
"v_mfma_f32_16x16x32_bf16 v[16:19], a[176:179], v[192:195], v[16:19]\n" \
"ds_read_b128 a[116:119], v208 offset:61440\n" \
"v_mfma_f32_16x16x32_bf16 v[16:19], a[144:147], v[184:187], v[16:19]\n" \
"v_exp_f32_e64 v152, v152\n" \
"v_mfma_f32_16x16x32_bf16 v[20:23], a[180:183], v[192:195], v[20:23]\n" \
"v_cvt_pk_bf16_f32 v218, v148, v149\n" \
"v_cvt_pk_bf16_f32 v219, v150, v151\n" \
"v_mfma_f32_16x16x32_bf16 v[20:23], a[148:151], v[184:187], v[20:23]\n" \
"v_exp_f32_e64 v153, v153\n" \
"v_mfma_f32_16x16x32_bf16 v[24:27], a[184:187], v[192:195], v[24:27]\n" \
"v_exp_f32_e64 v154, v154\n" \
"v_mfma_f32_16x16x32_bf16 v[24:27], a[152:155], v[184:187], v[24:27]\n" \
"ds_read_b128 a[112:115], %[kb0] offset:61440\n" \
"s_add_u32 s48, s48, 0x4000\n" \
"s_mov_b32 m0, s44\n" \
"s_addk_i32 s44, 0x400\n" \
"buffer_load_dwordx4 v229, s[64:67], s45 offen offset:0 nt lds\n" \
"s_add_u32 s45, s45, 393216\n" \
"v_mfma_f32_16x16x32_bf16 v[28:31], a[188:191], v[192:195], v[28:31]\n" \
"v_exp_f32_e64 v155, v155\n" \
"v_mfma_f32_16x16x32_bf16 v[28:31], a[156:159], v[184:187], v[28:31]\n" \
"s_waitcnt vmcnt(14) lgkmcnt(2)\n" \
"s_barrier\n" \
"v_mfma_f32_16x16x32_bf16 v[128:131], a[96:99], a[0:3], v[160:163]\n" \
"v_exp_f32_e64 v156, v156\n" \
"v_mfma_f32_16x16x32_bf16 v[128:131], a[100:103], a[4:7], v[128:131]\n" \
"s_add_u32 m0, %[m0b], 98304\n" \
"v_mfma_f32_16x16x32_bf16 v[128:131], a[104:107], a[8:11], v[128:131]\n" \
"v_exp_f32_e64 v157, v157\n" \
"v_mfma_f32_16x16x32_bf16 v[128:131], a[108:111], a[12:15], v[128:131]\n" \
"v_cvt_pk_bf16_f32 v220, v152, v153\n" \
"v_cvt_pk_bf16_f32 v221, v154, v155\n" \
"v_mfma_f32_16x16x32_bf16 v[132:135], a[124:127], a[12:15], v[160:163]\n" \
"v_exp_f32_e64 v158, v158\n" \
"v_mfma_f32_16x16x32_bf16 v[132:135], a[120:123], a[8:11], v[132:135]\n" \
"s_waitcnt lgkmcnt(0)\n" \
"v_mfma_f32_16x16x32_bf16 v[132:135], a[116:119], a[4:7], v[132:135]\n" \
"buffer_load_dwordx4 %[vd0], %[srdv], s48 offen offset:0 lds\n" \
"v_mfma_f32_16x16x32_bf16 v[132:135], a[112:115], a[0:3], v[132:135]\n" \
"v_exp_f32_e32 v159, v159\n" \
"v_mfma_f32_16x16x32_bf16 v[32:35], a[160:163], v[196:199], v[32:35]\n" \
"ds_read_b128 a[64:67], %[kb0] offset:0\n" \
"v_mfma_f32_16x16x32_bf16 v[32:35], a[128:131], v[188:191], v[32:35]\n" \
"v_exp_f32_e64 v128, v128\n" \
"v_mfma_f32_16x16x32_bf16 v[36:39], a[164:167], v[196:199], v[36:39]\n" \
"v_cvt_pk_bf16_f32 v222, v156, v157\n" \
"v_cvt_pk_bf16_f32 v223, v158, v159\n" \
"v_mfma_f32_16x16x32_bf16 v[36:39], a[132:135], v[188:191], v[36:39]\n" \
"v_exp_f32_e64 v129, v129\n" \
"v_mfma_f32_16x16x32_bf16 v[40:43], a[168:171], v[196:199], v[40:43]\n" \
"ds_read_b128 a[68:71], v208 offset:0\n" \
"v_mfma_f32_16x16x32_bf16 v[40:43], a[136:139], v[188:191], v[40:43]\n" \
"v_exp_f32_e64 v130, v130\n" \
"v_mfma_f32_16x16x32_bf16 v[44:47], a[172:175], v[196:199], v[44:47]\n" \
"ds_read_b128 a[72:75], v209 offset:0\n" \
"v_mfma_f32_16x16x32_bf16 v[44:47], a[140:143], v[188:191], v[44:47]\n" \
"v_exp_f32_e64 v131, v131\n" \
"v_smfmac_f32_16x16x64_bf16 v[176:179], a[192:195], v[184:191], v228\n" \
"v_mfma_f32_16x16x32_bf16 v[136:139], a[96:99], a[16:19], v[164:167]\n" \
"v_exp_f32_e64 v132, v132\n" \
"v_mfma_f32_16x16x32_bf16 v[136:139], a[100:103], a[20:23], v[136:139]\n" \
"ds_read_b128 a[76:79], v210 offset:0\n" \
"v_mfma_f32_16x16x32_bf16 v[136:139], a[104:107], a[24:27], v[136:139]\n" \
"v_exp_f32_e64 v133, v133\n" \
"v_mfma_f32_16x16x32_bf16 v[136:139], a[108:111], a[28:31], v[136:139]\n" \
"v_cvt_pk_bf16_f32 v192, v128, v129\n" \
"v_cvt_pk_bf16_f32 v193, v130, v131\n" \
"v_mfma_f32_16x16x32_bf16 v[140:143], a[124:127], a[28:31], v[164:167]\n" \
"v_exp_f32_e64 v134, v134\n" \
"v_mfma_f32_16x16x32_bf16 v[140:143], a[120:123], a[24:27], v[140:143]\n" \
"ds_read_b128 a[92:95], v210 offset:4096\n" \
"v_mfma_f32_16x16x32_bf16 v[140:143], a[116:119], a[20:23], v[140:143]\n" \
"buffer_load_dwordx4 v215, %[srdv], s48 offen offset:1024 lds\n" \
"v_mfma_f32_16x16x32_bf16 v[140:143], a[112:115], a[16:19], v[140:143]\n" \
"v_exp_f32_e64 v135, v135\n" \
"v_mfma_f32_16x16x32_bf16 v[48:51], a[176:179], v[196:199], v[48:51]\n" \
"ds_read_b128 a[88:91], v209 offset:4096\n" \
"v_mfma_f32_16x16x32_bf16 v[48:51], a[144:147], v[188:191], v[48:51]\n" \
"v_exp_f32_e64 v136, v136\n" \
"v_mfma_f32_16x16x32_bf16 v[52:55], a[180:183], v[196:199], v[52:55]\n" \
"v_cvt_pk_bf16_f32 v194, v132, v133\n" \
"v_cvt_pk_bf16_f32 v195, v134, v135\n" \
"v_mfma_f32_16x16x32_bf16 v[52:55], a[148:151], v[188:191], v[52:55]\n" \
"v_exp_f32_e64 v137, v137\n" \
"v_mfma_f32_16x16x32_bf16 v[56:59], a[184:187], v[196:199], v[56:59]\n" \
"v_exp_f32_e64 v138, v138\n" \
"v_mfma_f32_16x16x32_bf16 v[56:59], a[152:155], v[188:191], v[56:59]\n" \
"ds_read_b128 a[84:87], v208 offset:4096\n" \
"v_mfma_f32_16x16x32_bf16 v[60:63], a[188:191], v[196:199], v[60:63]\n" \
"v_exp_f32_e64 v139, v139\n" \
"v_mfma_f32_16x16x32_bf16 v[60:63], a[156:159], v[188:191], v[60:63]\n" \
"s_cmp_lg_u32 %[wid], 2\n" \
"s_cbranch_scc1 LtA7_%=\n" \
"global_load_dword v237, v224, s[58:59]\n" \
"s_add_u32 s61, s48, 49152\n" \
"s_mov_b32 exec_hi, 0\n" \
"s_nop 0\n" \
"buffer_load_dword v237, v224, %[srdp], s61 offen\n" \
"s_mov_b32 exec_hi, -1\n" \
"s_nop 0\n" \
"LtA7_%=:\n" \
"v_mfma_f32_16x16x32_bf16 v[144:147], a[96:99], a[32:35], v[168:171]\n" \
"v_exp_f32_e64 v140, v140\n" \
"v_mfma_f32_16x16x32_bf16 v[144:147], a[100:103], a[36:39], v[144:147]\n" \
"ds_read_b128 a[80:83], %[kb0] offset:4096\n" \
"v_mfma_f32_16x16x32_bf16 v[144:147], a[104:107], a[40:43], v[144:147]\n" \
"v_exp_f32_e64 v141, v141\n" \
"v_mfma_f32_16x16x32_bf16 v[144:147], a[108:111], a[44:47], v[144:147]\n" \
"v_cvt_pk_bf16_f32 v196, v136, v137\n" \
"v_cvt_pk_bf16_f32 v197, v138, v139\n" \
"v_mfma_f32_16x16x32_bf16 v[148:151], a[124:127], a[44:47], v[168:171]\n" \
"v_exp_f32_e64 v142, v142\n" \
"v_mfma_f32_16x16x32_bf16 v[148:151], a[120:123], a[40:43], v[148:151]\n" \
"v_mfma_f32_16x16x32_bf16 v[148:151], a[116:119], a[36:39], v[148:151]\n" \
"buffer_load_dwordx4 %[vd0], %[srdv], s48 offen offset:2048 lds\n" \
"v_mfma_f32_16x16x32_bf16 v[148:151], a[112:115], a[32:35], v[148:151]\n" \
"v_exp_f32_e64 v143, v143\n" \
"v_mfma_f32_16x16x32_bf16 v[96:99], a[160:163], v[204:207], v[96:99]\n" \
"ds_read_b64_tr_b16 a[160:161], %[vvb] offset:57344\n" \
"v_mfma_f32_16x16x32_bf16 v[96:99], a[128:131], v[220:223], v[96:99]\n" \
"v_exp_f32_e64 v144, v144\n" \
"v_mfma_f32_16x16x32_bf16 v[100:103], a[164:167], v[204:207], v[100:103]\n" \
"v_cvt_pk_bf16_f32 v198, v140, v141\n" \
"v_cvt_pk_bf16_f32 v199, v142, v143\n" \
"v_mfma_f32_16x16x32_bf16 v[100:103], a[132:135], v[220:223], v[100:103]\n" \
"v_exp_f32_e64 v145, v145\n" \
"v_mfma_f32_16x16x32_bf16 v[104:107], a[168:171], v[204:207], v[104:107]\n" \
"ds_read_b64_tr_b16 a[162:163], %[vvb] offset:61440\n" \
"ds_read_b64_tr_b16 a[164:165], v211 offset:57344\n" \
"v_mfma_f32_16x16x32_bf16 v[104:107], a[136:139], v[220:223], v[104:107]\n" \
"v_exp_f32_e64 v146, v146\n" \
"v_mfma_f32_16x16x32_bf16 v[108:111], a[172:175], v[204:207], v[108:111]\n" \
"ds_read_b64_tr_b16 a[166:167], v211 offset:61440\n" \
"v_mfma_f32_16x16x32_bf16 v[108:111], a[140:143], v[220:223], v[108:111]\n" \
"v_exp_f32_e64 v147, v147\n" \
"v_smfmac_f32_16x16x64_bf16 v[180:183], a[192:195], v[216:223], v228\n" \
"ds_read_b64_tr_b16 a[168:169], %[vvb] offset:57856\n" \
"v_mfma_f32_16x16x32_bf16 v[152:155], a[96:99], a[48:51], v[172:175]\n" \
"v_exp_f32_e64 v148, v148\n" \
"v_mfma_f32_16x16x32_bf16 v[152:155], a[100:103], a[52:55], v[152:155]\n" \
"ds_read_b64_tr_b16 a[170:171], %[vvb] offset:61952\n" \
"v_mfma_f32_16x16x32_bf16 v[152:155], a[104:107], a[56:59], v[152:155]\n" \
"v_exp_f32_e64 v149, v149\n" \
"v_mfma_f32_16x16x32_bf16 v[152:155], a[108:111], a[60:63], v[152:155]\n" \
"v_cvt_pk_bf16_f32 v200, v144, v145\n" \
"v_cvt_pk_bf16_f32 v201, v146, v147\n" \
"v_mfma_f32_16x16x32_bf16 v[156:159], a[124:127], a[60:63], v[172:175]\n" \
"v_exp_f32_e64 v150, v150\n" \
"v_mfma_f32_16x16x32_bf16 v[156:159], a[120:123], a[56:59], v[156:159]\n" \
"ds_read_b64_tr_b16 a[172:173], v211 offset:57856\n" \
"v_mfma_f32_16x16x32_bf16 v[156:159], a[116:119], a[52:55], v[156:159]\n" \
"buffer_load_dwordx4 v215, %[srdv], s48 offen offset:3072 lds\n" \
"v_mfma_f32_16x16x32_bf16 v[156:159], a[112:115], a[48:51], v[156:159]\n" \
"v_exp_f32_e64 v151, v151\n" \
"v_mfma_f32_16x16x32_bf16 v[112:115], a[176:179], v[204:207], v[112:115]\n" \
"ds_read_b64_tr_b16 a[174:175], v211 offset:61952\n" \
"ds_read_b64_tr_b16 a[176:177], %[vvb] offset:58368\n" \
"v_mfma_f32_16x16x32_bf16 v[112:115], a[144:147], v[220:223], v[112:115]\n" \
"v_exp_f32_e64 v152, v152\n" \
"v_mfma_f32_16x16x32_bf16 v[116:119], a[180:183], v[204:207], v[116:119]\n" \
"v_cvt_pk_bf16_f32 v202, v148, v149\n" \
"v_cvt_pk_bf16_f32 v203, v150, v151\n" \
"v_mfma_f32_16x16x32_bf16 v[116:119], a[148:151], v[220:223], v[116:119]\n" \
"v_exp_f32_e64 v153, v153\n" \
"v_mfma_f32_16x16x32_bf16 v[120:123], a[184:187], v[204:207], v[120:123]\n" \
"v_exp_f32_e64 v154, v154\n" \
"v_mfma_f32_16x16x32_bf16 v[120:123], a[152:155], v[220:223], v[120:123]\n" \
"ds_read_b64_tr_b16 a[178:179], %[vvb] offset:62464\n" \
"v_mfma_f32_16x16x32_bf16 v[124:127], a[188:191], v[204:207], v[124:127]\n" \
"v_exp_f32_e64 v155, v155\n" \
"v_mfma_f32_16x16x32_bf16 v[124:127], a[156:159], v[220:223], v[124:127]\n" \
"ds_read_b64_tr_b16 a[180:181], v211 offset:58368\n" \
"s_waitcnt lgkmcnt(13)\n" \
"v_mfma_f32_16x16x32_bf16 v[128:131], a[64:67], a[0:3], v[160:163]\n" \
"v_exp_f32_e32 v156, v156\n" \
"v_mfma_f32_16x16x32_bf16 v[128:131], a[68:71], a[4:7], v[128:131]\n" \
"ds_read_b64_tr_b16 a[182:183], v211 offset:62464\n" \
"s_add_u32 m0, %[m0b], 49152\n" \
"v_mfma_f32_16x16x32_bf16 v[128:131], a[72:75], a[8:11], v[128:131]\n" \
"v_exp_f32_e64 v157, v157\n" \
"v_mfma_f32_16x16x32_bf16 v[128:131], a[76:79], a[12:15], v[128:131]\n" \
"v_cvt_pk_bf16_f32 v204, v152, v153\n" \
"v_cvt_pk_bf16_f32 v205, v154, v155\n" \
"v_mfma_f32_16x16x32_bf16 v[132:135], a[92:95], a[12:15], v[160:163]\n" \
"v_exp_f32_e64 v158, v158\n" \
"v_mfma_f32_16x16x32_bf16 v[132:135], a[88:91], a[8:11], v[132:135]\n" \
"ds_read_b64_tr_b16 a[184:185], %[vvb] offset:58880\n" \
"s_waitcnt lgkmcnt(5)\n" \
"v_mfma_f32_16x16x32_bf16 v[132:135], a[84:87], a[4:7], v[132:135]\n" \
"buffer_load_dwordx4 %[kd0], %[srdk], s48 offen offset:0 lds\n" \
"v_mfma_f32_16x16x32_bf16 v[132:135], a[80:83], a[0:3], v[132:135]\n" \
"v_exp_f32_e32 v159, v159\n" \
"v_mfma_f32_16x16x32_bf16 v[64:67], a[128:131], v[216:219], v[64:67]\n" \
"ds_read_b64_tr_b16 a[186:187], %[vvb] offset:62976\n" \
"ds_read_b64_tr_b16 a[188:189], v211 offset:58880\n" \
"v_mfma_f32_16x16x32_bf16 v[64:67], a[160:163], v[200:203], v[64:67]\n" \
"v_exp_f32_e64 v128, v128\n" \
"v_mfma_f32_16x16x32_bf16 v[68:71], a[132:135], v[216:219], v[68:71]\n" \
"v_cvt_pk_bf16_f32 v206, v156, v157\n" \
"v_cvt_pk_bf16_f32 v207, v158, v159\n" \
"v_mfma_f32_16x16x32_bf16 v[68:71], a[164:167], v[200:203], v[68:71]\n" \
"v_exp_f32_e64 v129, v129\n" \
"v_mfma_f32_16x16x32_bf16 v[72:75], a[136:139], v[216:219], v[72:75]\n" \
"ds_read_b64_tr_b16 a[190:191], v211 offset:62976\n" \
"ds_read_b64_tr_b16 a[128:129], %[vvb] offset:0\n" \
"v_mfma_f32_16x16x32_bf16 v[72:75], a[168:171], v[200:203], v[72:75]\n" \
"v_exp_f32_e64 v130, v130\n" \
"v_mfma_f32_16x16x32_bf16 v[76:79], a[140:143], v[216:219], v[76:79]\n" \
"ds_read_b64_tr_b16 a[130:131], %[vvb] offset:4096\n" \
"ds_read_b64_tr_b16 a[132:133], v211 offset:0\n" \
"v_mfma_f32_16x16x32_bf16 v[76:79], a[172:175], v[200:203], v[76:79]\n" \
"v_exp_f32_e64 v131, v131\n" \
"v_smfmac_f32_16x16x64_bf16 v[176:179], a[192:195], v[192:199], v228\n" \
"ds_read_b64_tr_b16 a[134:135], v211 offset:4096\n" \
"ds_read_b64_tr_b16 a[136:137], %[vvb] offset:512\n" \
"v_mfma_f32_16x16x32_bf16 v[136:139], a[64:67], a[16:19], v[164:167]\n" \
"v_exp_f32_e64 v132, v132\n" \
"v_mfma_f32_16x16x32_bf16 v[136:139], a[68:71], a[20:23], v[136:139]\n" \
"ds_read_b64_tr_b16 a[138:139], %[vvb] offset:4608\n" \
"ds_read_b64_tr_b16 a[140:141], v211 offset:512\n" \
"v_mfma_f32_16x16x32_bf16 v[136:139], a[72:75], a[24:27], v[136:139]\n" \
"v_exp_f32_e64 v133, v133\n" \
"v_mfma_f32_16x16x32_bf16 v[136:139], a[76:79], a[28:31], v[136:139]\n" \
"v_cvt_pk_bf16_f32 v184, v128, v129\n" \
"v_cvt_pk_bf16_f32 v185, v130, v131\n" \
"v_mfma_f32_16x16x32_bf16 v[140:143], a[92:95], a[28:31], v[164:167]\n" \
"v_exp_f32_e64 v134, v134\n" \
"v_mfma_f32_16x16x32_bf16 v[140:143], a[88:91], a[24:27], v[140:143]\n" \
"ds_read_b64_tr_b16 a[142:143], v211 offset:4608\n" \
"s_waitcnt lgkmcnt(8)\n" \
"v_mfma_f32_16x16x32_bf16 v[140:143], a[84:87], a[20:23], v[140:143]\n" \
"buffer_load_dwordx4 v212, %[srdk], s48 offen offset:1024 lds\n" \
"v_mfma_f32_16x16x32_bf16 v[140:143], a[80:83], a[16:19], v[140:143]\n" \
"v_exp_f32_e32 v135, v135\n" \
"v_mfma_f32_16x16x32_bf16 v[80:83], a[144:147], v[216:219], v[80:83]\n" \
"ds_read_b64_tr_b16 a[144:145], %[vvb] offset:1024\n" \
"ds_read_b64_tr_b16 a[146:147], %[vvb] offset:5120\n" \
"v_mfma_f32_16x16x32_bf16 v[80:83], a[176:179], v[200:203], v[80:83]\n" \
"v_exp_f32_e64 v136, v136\n" \
"v_mfma_f32_16x16x32_bf16 v[84:87], a[148:151], v[216:219], v[84:87]\n" \
"v_cvt_pk_bf16_f32 v186, v132, v133\n" \
"v_cvt_pk_bf16_f32 v187, v134, v135\n" \
"v_mfma_f32_16x16x32_bf16 v[84:87], a[180:183], v[200:203], v[84:87]\n" \
"v_exp_f32_e64 v137, v137\n" \
"v_mfma_f32_16x16x32_bf16 v[88:91], a[152:155], v[216:219], v[88:91]\n" \
"v_exp_f32_e64 v138, v138\n" \
"v_mfma_f32_16x16x32_bf16 v[88:91], a[184:187], v[200:203], v[88:91]\n" \
"ds_read_b64_tr_b16 a[148:149], v211 offset:1024\n" \
"ds_read_b64_tr_b16 a[150:151], v211 offset:5120\n" \
"v_mfma_f32_16x16x32_bf16 v[92:95], a[156:159], v[216:219], v[92:95]\n" \
"v_exp_f32_e64 v139, v139\n" \
"v_mfma_f32_16x16x32_bf16 v[92:95], a[188:191], v[200:203], v[92:95]\n" \
"ds_read_b64_tr_b16 a[152:153], %[vvb] offset:1536\n" \
"s_cbranch_vccz Lpp2_%=\n" \
"buffer_store_dwordx4 a[196:199], v235, %[srdz], s62 offen offset:1024 sc1\n" \
"buffer_store_dwordx4 a[196:199], v226, %[srdz], s62 offen offset:1024 sc1\n" \
"Lpp2_%=:\n" \
"s_add_u32 s62, s62, 2048\n" \
"v_mfma_f32_16x16x32_bf16 v[144:147], a[64:67], a[32:35], v[168:171]\n" \
"v_exp_f32_e32 v140, v140\n" \
"v_mfma_f32_16x16x32_bf16 v[144:147], a[68:71], a[36:39], v[144:147]\n" \
"ds_read_b64_tr_b16 a[154:155], %[vvb] offset:5632\n" \
"ds_read_b64_tr_b16 a[156:157], v211 offset:1536\n" \
"v_mfma_f32_16x16x32_bf16 v[144:147], a[72:75], a[40:43], v[144:147]\n" \
"v_exp_f32_e64 v141, v141\n" \
"v_mfma_f32_16x16x32_bf16 v[144:147], a[76:79], a[44:47], v[144:147]\n" \
"v_cvt_pk_bf16_f32 v188, v136, v137\n" \
"v_cvt_pk_bf16_f32 v189, v138, v139\n" \
"v_mfma_f32_16x16x32_bf16 v[148:151], a[92:95], a[44:47], v[168:171]\n" \
"v_exp_f32_e64 v142, v142\n" \
"v_mfma_f32_16x16x32_bf16 v[148:151], a[88:91], a[40:43], v[148:151]\n" \
"ds_read_b64_tr_b16 a[158:159], v211 offset:5632\n" \
"s_waitcnt lgkmcnt(8)\n" \
"v_mfma_f32_16x16x32_bf16 v[148:151], a[84:87], a[36:39], v[148:151]\n" \
"buffer_load_dwordx4 v213, %[srdk], s48 offen offset:2048 lds\n" \
"v_mfma_f32_16x16x32_bf16 v[148:151], a[80:83], a[32:35], v[148:151]\n" \
"v_exp_f32_e32 v143, v143\n" \
"v_mfma_f32_16x16x32_bf16 v[0:3], a[160:163], v[192:195], v[0:3]\n" \
"ds_read_b128 a[96:99], %[kb0] offset:8192\n" \
"v_mfma_f32_16x16x32_bf16 v[0:3], a[128:131], v[184:187], v[0:3]\n" \
"v_exp_f32_e64 v144, v144\n" \
"v_mfma_f32_16x16x32_bf16 v[4:7], a[164:167], v[192:195], v[4:7]\n" \
"v_cvt_pk_bf16_f32 v190, v140, v141\n" \
"v_cvt_pk_bf16_f32 v191, v142, v143\n" \
"v_mfma_f32_16x16x32_bf16 v[4:7], a[132:135], v[184:187], v[4:7]\n" \
"v_exp_f32_e64 v145, v145\n" \
"v_mfma_f32_16x16x32_bf16 v[8:11], a[168:171], v[192:195], v[8:11]\n" \
"ds_read_b128 a[100:103], v208 offset:8192\n" \
"v_mfma_f32_16x16x32_bf16 v[8:11], a[136:139], v[184:187], v[8:11]\n" \
"v_exp_f32_e64 v146, v146\n" \
"v_mfma_f32_16x16x32_bf16 v[12:15], a[172:175], v[192:195], v[12:15]\n" \
"ds_read_b128 a[104:107], v209 offset:8192\n" \
"v_mfma_f32_16x16x32_bf16 v[12:15], a[140:143], v[184:187], v[12:15]\n" \
"v_exp_f32_e64 v147, v147\n" \
"v_smfmac_f32_16x16x64_bf16 v[180:183], a[192:195], v[200:207], v228\n" \
"ds_read_b128 a[108:111], v210 offset:8192\n" \
"v_mfma_f32_16x16x32_bf16 v[152:155], a[64:67], a[48:51], v[172:175]\n" \
"v_exp_f32_e64 v148, v148\n" \
"v_mfma_f32_16x16x32_bf16 v[152:155], a[68:71], a[52:55], v[152:155]\n" \
"ds_read_b128 a[124:127], v210 offset:12288\n" \
"v_mfma_f32_16x16x32_bf16 v[152:155], a[72:75], a[56:59], v[152:155]\n" \
"v_exp_f32_e64 v149, v149\n" \
"v_mfma_f32_16x16x32_bf16 v[152:155], a[76:79], a[60:63], v[152:155]\n" \
"v_cvt_pk_bf16_f32 v216, v144, v145\n" \
"v_cvt_pk_bf16_f32 v217, v146, v147\n" \
"v_mfma_f32_16x16x32_bf16 v[156:159], a[92:95], a[60:63], v[172:175]\n" \
"v_exp_f32_e64 v150, v150\n" \
"v_mfma_f32_16x16x32_bf16 v[156:159], a[88:91], a[56:59], v[156:159]\n" \
"ds_read_b128 a[120:123], v209 offset:12288\n" \
"s_waitcnt lgkmcnt(6)\n" \
"v_mfma_f32_16x16x32_bf16 v[156:159], a[84:87], a[52:55], v[156:159]\n" \
"buffer_load_dwordx4 v214, %[srdk], s48 offen offset:3072 lds\n" \
"v_mfma_f32_16x16x32_bf16 v[156:159], a[80:83], a[48:51], v[156:159]\n" \
"v_exp_f32_e32 v151, v151\n" \
"v_mfma_f32_16x16x32_bf16 v[16:19], a[176:179], v[192:195], v[16:19]\n" \
"ds_read_b128 a[116:119], v208 offset:12288\n" \
"v_mfma_f32_16x16x32_bf16 v[16:19], a[144:147], v[184:187], v[16:19]\n" \
"v_exp_f32_e64 v152, v152\n" \
"v_mfma_f32_16x16x32_bf16 v[20:23], a[180:183], v[192:195], v[20:23]\n" \
"v_cvt_pk_bf16_f32 v218, v148, v149\n" \
"v_cvt_pk_bf16_f32 v219, v150, v151\n" \
"v_mfma_f32_16x16x32_bf16 v[20:23], a[148:151], v[184:187], v[20:23]\n" \
"v_exp_f32_e64 v153, v153\n" \
"v_mfma_f32_16x16x32_bf16 v[24:27], a[184:187], v[192:195], v[24:27]\n" \
"v_exp_f32_e64 v154, v154\n" \
"v_mfma_f32_16x16x32_bf16 v[24:27], a[152:155], v[184:187], v[24:27]\n" \
"ds_read_b128 a[112:115], %[kb0] offset:12288\n" \
"s_add_u32 s48, s48, 0x4000\n" \
"v_mfma_f32_16x16x32_bf16 v[28:31], a[188:191], v[192:195], v[28:31]\n" \
"v_exp_f32_e64 v155, v155\n" \
"v_mfma_f32_16x16x32_bf16 v[28:31], a[156:159], v[184:187], v[28:31]\n" \
"s_waitcnt vmcnt(14) lgkmcnt(2)\n" \
"s_barrier\n" \
"v_mfma_f32_16x16x32_bf16 v[128:131], a[96:99], a[0:3], v[160:163]\n" \
"v_exp_f32_e64 v156, v156\n" \
"v_mfma_f32_16x16x32_bf16 v[128:131], a[100:103], a[4:7], v[128:131]\n" \
"s_add_u32 m0, %[m0b], 114688\n" \
"v_mfma_f32_16x16x32_bf16 v[128:131], a[104:107], a[8:11], v[128:131]\n" \
"v_exp_f32_e64 v157, v157\n" \
"v_mfma_f32_16x16x32_bf16 v[128:131], a[108:111], a[12:15], v[128:131]\n" \
"v_cvt_pk_bf16_f32 v220, v152, v153\n" \
"v_cvt_pk_bf16_f32 v221, v154, v155\n" \
"v_mfma_f32_16x16x32_bf16 v[132:135], a[124:127], a[12:15], v[160:163]\n" \
"v_exp_f32_e64 v158, v158\n" \
"v_mfma_f32_16x16x32_bf16 v[132:135], a[120:123], a[8:11], v[132:135]\n" \
"s_waitcnt lgkmcnt(0)\n" \
"v_mfma_f32_16x16x32_bf16 v[132:135], a[116:119], a[4:7], v[132:135]\n" \
"buffer_load_dwordx4 %[vd0], %[srdv], s48 offen offset:0 lds\n" \
"v_mfma_f32_16x16x32_bf16 v[132:135], a[112:115], a[0:3], v[132:135]\n" \
"v_exp_f32_e32 v159, v159\n" \
".if . - adaln_body != 20480\n" \
".error \"size model\"\n" \
".endif\n" \
"Lk4_%=:\n" \
"s_cbranch_execz Lk5_%=\n" \
"s_nop 0\n" \
"v_mfma_f32_16x16x32_bf16 v[32:35], a[160:163], v[196:199], v[32:35]\n" \
"ds_read_b128 a[64:67], %[kb0] offset:16384\n" \
"v_mfma_f32_16x16x32_bf16 v[32:35], a[128:131], v[188:191], v[32:35]\n" \
"v_exp_f32_e64 v128, v128\n" \
"v_mfma_f32_16x16x32_bf16 v[36:39], a[164:167], v[196:199], v[36:39]\n" \
"v_cvt_pk_bf16_f32 v222, v156, v157\n" \
"v_cvt_pk_bf16_f32 v223, v158, v159\n" \
"v_mfma_f32_16x16x32_bf16 v[36:39], a[132:135], v[188:191], v[36:39]\n" \
"v_exp_f32_e64 v129, v129\n" \
"v_mfma_f32_16x16x32_bf16 v[40:43], a[168:171], v[196:199], v[40:43]\n" \
"ds_read_b128 a[68:71], v208 offset:16384\n" \
"v_mfma_f32_16x16x32_bf16 v[40:43], a[136:139], v[188:191], v[40:43]\n" \
"v_exp_f32_e64 v130, v130\n" \
"v_mfma_f32_16x16x32_bf16 v[44:47], a[172:175], v[196:199], v[44:47]\n" \
"ds_read_b128 a[72:75], v209 offset:16384\n" \
"v_mfma_f32_16x16x32_bf16 v[44:47], a[140:143], v[188:191], v[44:47]\n" \
"v_exp_f32_e64 v131, v131\n" \
"v_smfmac_f32_16x16x64_bf16 v[176:179], a[192:195], v[184:191], v228\n" \
"v_mfma_f32_16x16x32_bf16 v[136:139], a[96:99], a[16:19], v[164:167]\n" \
"v_exp_f32_e64 v132, v132\n" \
"v_mfma_f32_16x16x32_bf16 v[136:139], a[100:103], a[20:23], v[136:139]\n" \
"ds_read_b128 a[76:79], v210 offset:16384\n" \
"v_mfma_f32_16x16x32_bf16 v[136:139], a[104:107], a[24:27], v[136:139]\n" \
"v_exp_f32_e64 v133, v133\n" \
"v_mfma_f32_16x16x32_bf16 v[136:139], a[108:111], a[28:31], v[136:139]\n" \
"v_cvt_pk_bf16_f32 v192, v128, v129\n" \
"v_cvt_pk_bf16_f32 v193, v130, v131\n" \
"v_mfma_f32_16x16x32_bf16 v[140:143], a[124:127], a[28:31], v[164:167]\n" \
"v_exp_f32_e64 v134, v134\n" \
"v_mfma_f32_16x16x32_bf16 v[140:143], a[120:123], a[24:27], v[140:143]\n" \
"ds_read_b128 a[92:95], v210 offset:20480\n" \
"v_mfma_f32_16x16x32_bf16 v[140:143], a[116:119], a[20:23], v[140:143]\n" \
"buffer_load_dwordx4 v215, %[srdv], s48 offen offset:1024 lds\n" \
"v_mfma_f32_16x16x32_bf16 v[140:143], a[112:115], a[16:19], v[140:143]\n" \
"v_exp_f32_e64 v135, v135\n" \
"v_mfma_f32_16x16x32_bf16 v[48:51], a[176:179], v[196:199], v[48:51]\n" \
"ds_read_b128 a[88:91], v209 offset:20480\n" \
"v_mfma_f32_16x16x32_bf16 v[48:51], a[144:147], v[188:191], v[48:51]\n" \
"v_exp_f32_e64 v136, v136\n" \
"v_mfma_f32_16x16x32_bf16 v[52:55], a[180:183], v[196:199], v[52:55]\n" \
"v_cvt_pk_bf16_f32 v194, v132, v133\n" \
"v_cvt_pk_bf16_f32 v195, v134, v135\n" \
"v_mfma_f32_16x16x32_bf16 v[52:55], a[148:151], v[188:191], v[52:55]\n" \
"v_exp_f32_e64 v137, v137\n" \
"v_mfma_f32_16x16x32_bf16 v[56:59], a[184:187], v[196:199], v[56:59]\n" \
"v_exp_f32_e64 v138, v138\n" \
"v_mfma_f32_16x16x32_bf16 v[56:59], a[152:155], v[188:191], v[56:59]\n" \
"ds_read_b128 a[84:87], v208 offset:20480\n" \
"v_mfma_f32_16x16x32_bf16 v[60:63], a[188:191], v[196:199], v[60:63]\n" \
"v_exp_f32_e64 v139, v139\n" \
"v_mfma_f32_16x16x32_bf16 v[60:63], a[156:159], v[188:191], v[60:63]\n" \
"s_cmp_lg_u32 %[wid], 3\n" \
"s_cbranch_scc1 LtA1_%=\n" \
"global_load_dword v237, v224, s[58:59]\n" \
"s_add_u32 s61, s48, 49152\n" \
"s_mov_b32 exec_hi, 0\n" \
"s_nop 0\n" \
"buffer_load_dword v237, v224, %[srdp], s61 offen\n" \
"s_mov_b32 exec_hi, -1\n" \
"s_nop 0\n" \
"LtA1_%=:\n" \
"v_mfma_f32_16x16x32_bf16 v[144:147], a[96:99], a[32:35], v[168:171]\n" \
"v_exp_f32_e64 v140, v140\n" \
"v_mfma_f32_16x16x32_bf16 v[144:147], a[100:103], a[36:39], v[144:147]\n" \
"ds_read_b128 a[80:83], %[kb0] offset:20480\n" \
"v_mfma_f32_16x16x32_bf16 v[144:147], a[104:107], a[40:43], v[144:147]\n" \
"v_exp_f32_e64 v141, v141\n" \
"v_mfma_f32_16x16x32_bf16 v[144:147], a[108:111], a[44:47], v[144:147]\n" \
"v_cvt_pk_bf16_f32 v196, v136, v137\n" \
"v_cvt_pk_bf16_f32 v197, v138, v139\n" \
"v_mfma_f32_16x16x32_bf16 v[148:151], a[124:127], a[44:47], v[168:171]\n" \
"v_exp_f32_e64 v142, v142\n" \
"v_mfma_f32_16x16x32_bf16 v[148:151], a[120:123], a[40:43], v[148:151]\n" \
"v_mfma_f32_16x16x32_bf16 v[148:151], a[116:119], a[36:39], v[148:151]\n" \
"buffer_load_dwordx4 %[vd0], %[srdv], s48 offen offset:2048 lds\n" \
"v_mfma_f32_16x16x32_bf16 v[148:151], a[112:115], a[32:35], v[148:151]\n" \
"v_exp_f32_e64 v143, v143\n" \
"v_mfma_f32_16x16x32_bf16 v[96:99], a[160:163], v[204:207], v[96:99]\n" \
"ds_read_b64_tr_b16 a[160:161], %[vvb] offset:8192\n" \
"v_mfma_f32_16x16x32_bf16 v[96:99], a[128:131], v[220:223], v[96:99]\n" \
"v_exp_f32_e64 v144, v144\n" \
"v_mfma_f32_16x16x32_bf16 v[100:103], a[164:167], v[204:207], v[100:103]\n" \
"v_cvt_pk_bf16_f32 v198, v140, v141\n" \
"v_cvt_pk_bf16_f32 v199, v142, v143\n" \
"v_mfma_f32_16x16x32_bf16 v[100:103], a[132:135], v[220:223], v[100:103]\n" \
"v_exp_f32_e64 v145, v145\n" \
"v_mfma_f32_16x16x32_bf16 v[104:107], a[168:171], v[204:207], v[104:107]\n" \
"ds_read_b64_tr_b16 a[162:163], %[vvb] offset:12288\n" \
"ds_read_b64_tr_b16 a[164:165], v211 offset:8192\n" \
"v_mfma_f32_16x16x32_bf16 v[104:107], a[136:139], v[220:223], v[104:107]\n" \
"v_exp_f32_e64 v146, v146\n" \
"v_mfma_f32_16x16x32_bf16 v[108:111], a[172:175], v[204:207], v[108:111]\n" \
"ds_read_b64_tr_b16 a[166:167], v211 offset:12288\n" \
"v_mfma_f32_16x16x32_bf16 v[108:111], a[140:143], v[220:223], v[108:111]\n" \
"v_exp_f32_e64 v147, v147\n" \
"v_smfmac_f32_16x16x64_bf16 v[180:183], a[192:195], v[216:223], v228\n" \
"ds_read_b64_tr_b16 a[168:169], %[vvb] offset:8704\n" \
"v_mfma_f32_16x16x32_bf16 v[152:155], a[96:99], a[48:51], v[172:175]\n" \
"v_exp_f32_e64 v148, v148\n" \
"v_mfma_f32_16x16x32_bf16 v[152:155], a[100:103], a[52:55], v[152:155]\n" \
"ds_read_b64_tr_b16 a[170:171], %[vvb] offset:12800\n" \
"v_mfma_f32_16x16x32_bf16 v[152:155], a[104:107], a[56:59], v[152:155]\n" \
"v_exp_f32_e64 v149, v149\n" \
"v_mfma_f32_16x16x32_bf16 v[152:155], a[108:111], a[60:63], v[152:155]\n" \
"v_cvt_pk_bf16_f32 v200, v144, v145\n" \
"v_cvt_pk_bf16_f32 v201, v146, v147\n" \
"v_mfma_f32_16x16x32_bf16 v[156:159], a[124:127], a[60:63], v[172:175]\n" \
"v_exp_f32_e64 v150, v150\n" \
"v_mfma_f32_16x16x32_bf16 v[156:159], a[120:123], a[56:59], v[156:159]\n" \
"ds_read_b64_tr_b16 a[172:173], v211 offset:8704\n" \
"v_mfma_f32_16x16x32_bf16 v[156:159], a[116:119], a[52:55], v[156:159]\n" \
"buffer_load_dwordx4 v215, %[srdv], s48 offen offset:3072 lds\n" \
"v_mfma_f32_16x16x32_bf16 v[156:159], a[112:115], a[48:51], v[156:159]\n" \
"v_exp_f32_e64 v151, v151\n" \
"v_mfma_f32_16x16x32_bf16 v[112:115], a[176:179], v[204:207], v[112:115]\n" \
"ds_read_b64_tr_b16 a[174:175], v211 offset:12800\n" \
"ds_read_b64_tr_b16 a[176:177], %[vvb] offset:9216\n" \
"v_mfma_f32_16x16x32_bf16 v[112:115], a[144:147], v[220:223], v[112:115]\n" \
"v_exp_f32_e64 v152, v152\n" \
"v_mfma_f32_16x16x32_bf16 v[116:119], a[180:183], v[204:207], v[116:119]\n" \
"v_cvt_pk_bf16_f32 v202, v148, v149\n" \
"v_cvt_pk_bf16_f32 v203, v150, v151\n" \
"v_mfma_f32_16x16x32_bf16 v[116:119], a[148:151], v[220:223], v[116:119]\n" \
"v_exp_f32_e64 v153, v153\n" \
"v_mfma_f32_16x16x32_bf16 v[120:123], a[184:187], v[204:207], v[120:123]\n" \
"v_exp_f32_e64 v154, v154\n" \
"v_mfma_f32_16x16x32_bf16 v[120:123], a[152:155], v[220:223], v[120:123]\n" \
"ds_read_b64_tr_b16 a[178:179], %[vvb] offset:13312\n" \
"v_mfma_f32_16x16x32_bf16 v[124:127], a[188:191], v[204:207], v[124:127]\n" \
"v_exp_f32_e64 v155, v155\n" \
"v_mfma_f32_16x16x32_bf16 v[124:127], a[156:159], v[220:223], v[124:127]\n" \
"ds_read_b64_tr_b16 a[180:181], v211 offset:9216\n" \
"s_waitcnt lgkmcnt(13)\n" \
"s_cmp_lg_u32 s60, 4\n" \
"s_cbranch_scc1 LmA_%=\n" \
"s_nop 0\n" \
"v_readlane_b32 s64, %[tlo], 9\n" \
"v_readlane_b32 s65, %[thi], 9\n" \
"s_mov_b32 s66, %[wbytes]\n" \
"v_mov_b32_e32 v229, %[vg0]\n" \
"s_mov_b32 s44, %[m0t]\n" \
"s_mov_b32 s45, 0\n" \
"s_cmp_lg_u32 s52, 0\n" \
"s_cbranch_scc1 LmB_%=\n" \
"v_readlane_b32 s61, %[thi], 21\n" \
"s_add_u32 s62, %[sin], 16384\n" \
"s_add_u32 s63, %[sin], 49152\n" \
"s_add_u32 s55, s61, 4096\n" \
"s_nop 1\n" \
"s_nop 0\n" \
"buffer_store_dwordx4 a[196:199], v235, %[srdz], s61 offen offset:0 sc1\n" \
"buffer_store_dwordx4 a[196:199], v235, %[srdz], s55 offen offset:0 sc1\n" \
"s_waitcnt vmcnt(14)\n" \
"s_nop 0\n" \
"ds_read_b64_tr_b16 a[232:233], %[ta0] offset:0\n" \
"ds_read_b64_tr_b16 a[234:235], %[ta0] offset:128\n" \
"ds_read_b64_tr_b16 a[236:237], %[ta0] offset:1024\n" \
"ds_read_b64_tr_b16 a[238:239], %[ta0] offset:1152\n" \
"ds_read_b64_tr_b16 a[240:241], %[ta0] offset:2048\n" \
"ds_read_b64_tr_b16 a[242:243], %[ta0] offset:2176\n" \
"ds_read_b64_tr_b16 a[244:245], %[ta0] offset:3072\n" \
"ds_read_b64_tr_b16 a[246:247], %[ta0] offset:3200\n" \
"ds_read_b64_tr_b16 a[248:249], %[ta0] offset:4096\n" \
"ds_read_b64_tr_b16 a[250:251], %[ta0] offset:4224\n" \
"ds_read_b64_tr_b16 a[252:253], %[ta0] offset:5120\n" \
"ds_read_b64_tr_b16 a[254:255], %[ta0] offset:5248\n" \
"v_add_u32_e32 v234, %[m0t], v235\n" \
"s_waitcnt lgkmcnt(8)\n" \
"v_mfma_f32_16x16x32_bf16 v[230:233], a[232:235], a[200:203], 0\n" \
"v_mfma_f32_16x16x32_bf16 v[230:233], a[236:239], a[204:207], v[230:233]\n" \
"ds_read_b64_tr_b16 a[232:233], %[ta0] offset:6144\n" \
"ds_read_b64_tr_b16 a[234:235], %[ta0] offset:6272\n" \
"ds_read_b64_tr_b16 a[236:237], %[ta0] offset:7168\n" \
"ds_read_b64_tr_b16 a[238:239], %[ta0] offset:7296\n" \
"s_waitcnt lgkmcnt(4)\n" \
"s_nop 0\n" \
"v_mfma_f32_16x16x32_bf16 v[230:233], a[240:243], a[208:211], v[230:233]\n" \
"v_mfma_f32_16x16x32_bf16 v[230:233], a[244:247], a[212:215], v[230:233]\n" \
"v_mfma_f32_16x16x32_bf16 v[230:233], a[248:251], a[216:219], v[230:233]\n" \
"v_mfma_f32_16x16x32_bf16 v[230:233], a[252:255], a[220:223], v[230:233]\n" \
"s_waitcnt lgkmcnt(0)\n" \
"s_nop 0\n" \
"v_mfma_f32_16x16x32_bf16 v[230:233], a[232:235], a[224:227], v[230:233]\n" \
"v_mfma_f32_16x16x32_bf16 v[230:233], a[236:239], a[228:231], v[230:233]\n" \
"s_nop 7\n" \
"s_nop 0\n" \
"ds_write_b128 v234, v[230:233] offset:7168\n" \
"LmB_%=:\n" \
"v_mfma_f32_16x16x32_bf16 v[128:131], a[64:67], a[0:3], v[160:163]\n" \
"v_exp_f32_e64 v156, v156\n" \
"v_mfma_f32_16x16x32_bf16 v[128:131], a[68:71], a[4:7], v[128:131]\n" \
"ds_read_b64_tr_b16 a[182:183], v211 offset:13312\n" \
"s_add_u32 m0, %[m0b], 0\n" \
"v_mfma_f32_16x16x32_bf16 v[128:131], a[72:75], a[8:11], v[128:131]\n" \
"v_exp_f32_e32 v157, v157\n" \
"v_mfma_f32_16x16x32_bf16 v[128:131], a[76:79], a[12:15], v[128:131]\n" \
"v_cvt_pk_bf16_f32 v204, v152, v153\n" \
"v_cvt_pk_bf16_f32 v205, v154, v155\n" \
"v_mfma_f32_16x16x32_bf16 v[132:135], a[92:95], a[12:15], v[160:163]\n" \
"v_exp_f32_e64 v158, v158\n" \
"v_mfma_f32_16x16x32_bf16 v[132:135], a[88:91], a[8:11], v[132:135]\n" \
"ds_read_b64_tr_b16 a[184:185], %[vvb] offset:9728\n" \
"s_waitcnt lgkmcnt(5)\n" \
"v_mfma_f32_16x16x32_bf16 v[132:135], a[84:87], a[4:7], v[132:135]\n" \
"buffer_load_dwordx4 %[kd0], %[srdk], s48 offen offset:0 lds\n" \
"v_mfma_f32_16x16x32_bf16 v[132:135], a[80:83], a[0:3], v[132:135]\n" \
"v_exp_f32_e32 v159, v159\n" \
"v_mfma_f32_16x16x32_bf16 v[64:67], a[128:131], v[216:219], v[64:67]\n" \
"ds_read_b64_tr_b16 a[186:187], %[vvb] offset:13824\n" \
"ds_read_b64_tr_b16 a[188:189], v211 offset:9728\n" \
"v_mfma_f32_16x16x32_bf16 v[64:67], a[160:163], v[200:203], v[64:67]\n" \
"v_exp_f32_e64 v128, v128\n" \
"v_mfma_f32_16x16x32_bf16 v[68:71], a[132:135], v[216:219], v[68:71]\n" \
"v_cvt_pk_bf16_f32 v206, v156, v157\n" \
"v_cvt_pk_bf16_f32 v207, v158, v159\n" \
"v_mfma_f32_16x16x32_bf16 v[68:71], a[164:167], v[200:203], v[68:71]\n" \
"v_exp_f32_e64 v129, v129\n" \
"v_mfma_f32_16x16x32_bf16 v[72:75], a[136:139], v[216:219], v[72:75]\n" \
"ds_read_b64_tr_b16 a[190:191], v211 offset:13824\n" \
"ds_read_b64_tr_b16 a[128:129], %[vvb] offset:16384\n" \
"s_add_u32 s60, s60, 1\n" \
"s_nop 0\n" \
"v_readlane_b32 s58, %[tlo], s60\n" \
"v_mfma_f32_16x16x32_bf16 v[72:75], a[168:171], v[200:203], v[72:75]\n" \
"v_exp_f32_e64 v130, v130\n" \
"v_mfma_f32_16x16x32_bf16 v[76:79], a[140:143], v[216:219], v[76:79]\n" \
"ds_read_b64_tr_b16 a[130:131], %[vvb] offset:20480\n" \
"ds_read_b64_tr_b16 a[132:133], v211 offset:16384\n" \
"v_readlane_b32 s59, %[thi], s60\n" \
"v_mfma_f32_16x16x32_bf16 v[76:79], a[172:175], v[200:203], v[76:79]\n" \
"v_exp_f32_e64 v131, v131\n" \
"v_smfmac_f32_16x16x64_bf16 v[176:179], a[192:195], v[192:199], v228\n" \
"ds_read_b64_tr_b16 a[134:135], v211 offset:20480\n" \
"ds_read_b64_tr_b16 a[136:137], %[vvb] offset:16896\n" \
"s_cmp_eq_u32 s60, 7\n" \
"s_cselect_b32 s61, 56, 1\n" \
"v_mul_u32_u24_e64 v236, s61, v224\n" \
"v_mfma_f32_16x16x32_bf16 v[136:139], a[64:67], a[16:19], v[164:167]\n" \
"v_exp_f32_e64 v132, v132\n" \
"v_mfma_f32_16x16x32_bf16 v[136:139], a[68:71], a[20:23], v[136:139]\n" \
"ds_read_b64_tr_b16 a[138:139], %[vvb] offset:20992\n" \
"ds_read_b64_tr_b16 a[140:141], v211 offset:16896\n" \
"v_mfma_f32_16x16x32_bf16 v[136:139], a[72:75], a[24:27], v[136:139]\n" \
"v_exp_f32_e64 v133, v133\n" \
"v_mfma_f32_16x16x32_bf16 v[136:139], a[76:79], a[28:31], v[136:139]\n" \
"v_cvt_pk_bf16_f32 v184, v128, v129\n" \
"v_cvt_pk_bf16_f32 v185, v130, v131\n" \
"v_mfma_f32_16x16x32_bf16 v[140:143], a[92:95], a[28:31], v[164:167]\n" \
"v_exp_f32_e64 v134, v134\n" \
"v_mfma_f32_16x16x32_bf16 v[140:143], a[88:91], a[24:27], v[140:143]\n" \
"ds_read_b64_tr_b16 a[142:143], v211 offset:20992\n" \
"s_waitcnt lgkmcnt(8)\n" \
"v_mfma_f32_16x16x32_bf16 v[140:143], a[84:87], a[20:23], v[140:143]\n" \
"buffer_load_dwordx4 v212, %[srdk], s48 offen offset:1024 lds\n" \
"v_mfma_f32_16x16x32_bf16 v[140:143], a[80:83], a[16:19], v[140:143]\n" \
"v_exp_f32_e32 v135, v135\n" \
"v_mfma_f32_16x16x32_bf16 v[80:83], a[144:147], v[216:219], v[80:83]\n" \
"ds_read_b64_tr_b16 a[144:145], %[vvb] offset:17408\n" \
"ds_read_b64_tr_b16 a[146:147], %[vvb] offset:21504\n" \
"v_mfma_f32_16x16x32_bf16 v[80:83], a[176:179], v[200:203], v[80:83]\n" \
"v_exp_f32_e64 v136, v136\n" \
"v_mfma_f32_16x16x32_bf16 v[84:87], a[148:151], v[216:219], v[84:87]\n" \
"v_cvt_pk_bf16_f32 v186, v132, v133\n" \
"v_cvt_pk_bf16_f32 v187, v134, v135\n" \
"v_mfma_f32_16x16x32_bf16 v[84:87], a[180:183], v[200:203], v[84:87]\n" \
"v_exp_f32_e64 v137, v137\n" \
"v_mfma_f32_16x16x32_bf16 v[88:91], a[152:155], v[216:219], v[88:91]\n" \
"v_exp_f32_e64 v138, v138\n" \
"v_mfma_f32_16x16x32_bf16 v[88:91], a[184:187], v[200:203], v[88:91]\n" \
"ds_read_b64_tr_b16 a[148:149], v211 offset:17408\n" \
"ds_read_b64_tr_b16 a[150:151], v211 offset:21504\n" \
"v_mfma_f32_16x16x32_bf16 v[92:95], a[156:159], v[216:219], v[92:95]\n" \
"v_exp_f32_e64 v139, v139\n" \
"v_mfma_f32_16x16x32_bf16 v[92:95], a[188:191], v[200:203], v[92:95]\n" \
"ds_read_b64_tr_b16 a[152:153], %[vvb] offset:17920\n" \
"s_cbranch_vccz Lpp3_%=\n" \
"buffer_store_dwordx4 a[196:199], v235, %[srdz], s63 offen offset:0 sc1\n" \
"buffer_store_dwordx4 a[196:199], v226, %[srdz], s63 offen offset:0 sc1\n" \
"Lpp3_%=:\n" \
"v_mfma_f32_16x16x32_bf16 v[144:147], a[64:67], a[32:35], v[168:171]\n" \
"v_exp_f32_e32 v140, v140\n" \
"v_mfma_f32_16x16x32_bf16 v[144:147], a[68:71], a[36:39], v[144:147]\n" \
"ds_read_b64_tr_b16 a[154:155], %[vvb] offset:22016\n" \
"ds_read_b64_tr_b16 a[156:157], v211 offset:17920\n" \
"v_mfma_f32_16x16x32_bf16 v[144:147], a[72:75], a[40:43], v[144:147]\n" \
"v_exp_f32_e64 v141, v141\n" \
"v_mfma_f32_16x16x32_bf16 v[144:147], a[76:79], a[44:47], v[144:147]\n" \
"v_cvt_pk_bf16_f32 v188, v136, v137\n" \
"v_cvt_pk_bf16_f32 v189, v138, v139\n" \
"v_mfma_f32_16x16x32_bf16 v[148:151], a[92:95], a[44:47], v[168:171]\n" \
"v_exp_f32_e64 v142, v142\n" \
"v_mfma_f32_16x16x32_bf16 v[148:151], a[88:91], a[40:43], v[148:151]\n" \
"ds_read_b64_tr_b16 a[158:159], v211 offset:22016\n" \
"s_waitcnt lgkmcnt(8)\n" \
"v_mfma_f32_16x16x32_bf16 v[148:151], a[84:87], a[36:39], v[148:151]\n" \
"buffer_load_dwordx4 v213, %[srdk], s48 offen offset:2048 lds\n" \
"v_mfma_f32_16x16x32_bf16 v[148:151], a[80:83], a[32:35], v[148:151]\n" \
"v_exp_f32_e32 v143, v143\n" \
"v_mfma_f32_16x16x32_bf16 v[0:3], a[160:163], v[192:195], v[0:3]\n" \
"ds_read_b128 a[96:99], %[kb0] offset:24576\n" \
"v_mfma_f32_16x16x32_bf16 v[0:3], a[128:131], v[184:187], v[0:3]\n" \
"v_exp_f32_e64 v144, v144\n" \
"v_mfma_f32_16x16x32_bf16 v[4:7], a[164:167], v[192:195], v[4:7]\n" \
"v_cvt_pk_bf16_f32 v190, v140, v141\n" \
"v_cvt_pk_bf16_f32 v191, v142, v143\n" \
"v_mfma_f32_16x16x32_bf16 v[4:7], a[132:135], v[184:187], v[4:7]\n" \
"v_exp_f32_e64 v145, v145\n" \
"v_mfma_f32_16x16x32_bf16 v[8:11], a[168:171], v[192:195], v[8:11]\n" \
"ds_read_b128 a[100:103], v208 offset:24576\n" \
"v_mfma_f32_16x16x32_bf16 v[8:11], a[136:139], v[184:187], v[8:11]\n" \
"v_exp_f32_e64 v146, v146\n" \
"v_mfma_f32_16x16x32_bf16 v[12:15], a[172:175], v[192:195], v[12:15]\n" \
"ds_read_b128 a[104:107], v209 offset:24576\n" \
"v_mfma_f32_16x16x32_bf16 v[12:15], a[140:143], v[184:187], v[12:15]\n" \
"v_exp_f32_e64 v147, v147\n" \
"v_smfmac_f32_16x16x64_bf16 v[180:183], a[192:195], v[200:207], v228\n" \
"ds_read_b128 a[108:111], v210 offset:24576\n" \
"v_mfma_f32_16x16x32_bf16 v[152:155], a[64:67], a[48:51], v[172:175]\n" \
"v_exp_f32_e64 v148, v148\n" \
"v_mfma_f32_16x16x32_bf16 v[152:155], a[68:71], a[52:55], v[152:155]\n" \
"ds_read_b128 a[124:127], v210 offset:28672\n" \
"v_mfma_f32_16x16x32_bf16 v[152:155], a[72:75], a[56:59], v[152:155]\n" \
"v_exp_f32_e64 v149, v149\n" \
"v_mfma_f32_16x16x32_bf16 v[152:155], a[76:79], a[60:63], v[152:155]\n" \
"v_cvt_pk_bf16_f32 v216, v144, v145\n" \
"v_cvt_pk_bf16_f32 v217, v146, v147\n" \
"v_mfma_f32_16x16x32_bf16 v[156:159], a[92:95], a[60:63], v[172:175]\n" \
"v_exp_f32_e64 v150, v150\n" \
"v_mfma_f32_16x16x32_bf16 v[156:159], a[88:91], a[56:59], v[156:159]\n" \
"ds_read_b128 a[120:123], v209 offset:28672\n" \
"s_waitcnt lgkmcnt(6)\n" \
"v_mfma_f32_16x16x32_bf16 v[156:159], a[84:87], a[52:55], v[156:159]\n" \
"buffer_load_dwordx4 v214, %[srdk], s48 offen offset:3072 lds\n" \
"v_mfma_f32_16x16x32_bf16 v[156:159], a[80:83], a[48:51], v[156:159]\n" \
"v_exp_f32_e32 v151, v151\n" \
"v_mfma_f32_16x16x32_bf16 v[16:19], a[176:179], v[192:195], v[16:19]\n" \
"ds_read_b128 a[116:119], v208 offset:28672\n" \
"v_mfma_f32_16x16x32_bf16 v[16:19], a[144:147], v[184:187], v[16:19]\n" \
"v_exp_f32_e64 v152, v152\n" \
"v_mfma_f32_16x16x32_bf16 v[20:23], a[180:183], v[192:195], v[20:23]\n" \
"v_cvt_pk_bf16_f32 v218, v148, v149\n" \
"v_cvt_pk_bf16_f32 v219, v150, v151\n" \
"v_mfma_f32_16x16x32_bf16 v[20:23], a[148:151], v[184:187], v[20:23]\n" \
"v_exp_f32_e64 v153, v153\n" \
"v_mfma_f32_16x16x32_bf16 v[24:27], a[184:187], v[192:195], v[24:27]\n" \
"v_exp_f32_e64 v154, v154\n" \
"v_mfma_f32_16x16x32_bf16 v[24:27], a[152:155], v[184:187], v[24:27]\n" \
"ds_read_b128 a[112:115], %[kb0] offset:28672\n" \
"s_add_u32 s48, s48, 0x4000\n" \
"s_mov_b32 m0, s44\n" \
"s_addk_i32 s44, 0x400\n" \
"buffer_load_dwordx4 v229, s[64:67], s45 offen offset:0 lds\n" \
"s_add_u32 s45, s45, 229376\n" \
"v_mfma_f32_16x16x32_bf16 v[28:31], a[188:191], v[192:195], v[28:31]\n" \
"v_exp_f32_e64 v155, v155\n" \
"v_mfma_f32_16x16x32_bf16 v[28:31], a[156:159], v[184:187], v[28:31]\n" \
"s_waitcnt vmcnt(13) lgkmcnt(2)\n" \
"s_barrier\n" \
"v_mfma_f32_16x16x32_bf16 v[128:131], a[96:99], a[0:3], v[160:163]\n" \
"v_exp_f32_e64 v156, v156\n" \
"v_mfma_f32_16x16x32_bf16 v[128:131], a[100:103], a[4:7], v[128:131]\n" \
"s_add_u32 m0, %[m0b], 65536\n" \
"v_mfma_f32_16x16x32_bf16 v[128:131], a[104:107], a[8:11], v[128:131]\n" \
"v_exp_f32_e64 v157, v157\n" \
"v_mfma_f32_16x16x32_bf16 v[128:131], a[108:111], a[12:15], v[128:131]\n" \
"v_cvt_pk_bf16_f32 v220, v152, v153\n" \
"v_cvt_pk_bf16_f32 v221, v154, v155\n" \
"v_mfma_f32_16x16x32_bf16 v[132:135], a[124:127], a[12:15], v[160:163]\n" \
"v_exp_f32_e64 v158, v158\n" \
"v_mfma_f32_16x16x32_bf16 v[132:135], a[120:123], a[8:11], v[132:135]\n" \
"s_waitcnt lgkmcnt(0)\n" \
"v_mfma_f32_16x16x32_bf16 v[132:135], a[116:119], a[4:7], v[132:135]\n" \
"buffer_load_dwordx4 %[vd0], %[srdv], s48 offen offset:0 lds\n" \
"v_mfma_f32_16x16x32_bf16 v[132:135], a[112:115], a[0:3], v[132:135]\n" \
"v_exp_f32_e32 v159, v159\n" \
"v_mfma_f32_16x16x32_bf16 v[32:35], a[160:163], v[196:199], v[32:35]\n" \
"ds_read_b128 a[64:67], %[kb0] offset:32768\n" \
"v_mfma_f32_16x16x32_bf16 v[32:35], a[128:131], v[188:191], v[32:35]\n" \
"v_exp_f32_e64 v128, v128\n" \
"v_mfma_f32_16x16x32_bf16 v[36:39], a[164:167], v[196:199], v[36:39]\n" \
"v_cvt_pk_bf16_f32 v222, v156, v157\n" \
"v_cvt_pk_bf16_f32 v223, v158, v159\n" \
"v_mfma_f32_16x16x32_bf16 v[36:39], a[132:135], v[188:191], v[36:39]\n" \
"v_exp_f32_e64 v129, v129\n" \
"v_mfma_f32_16x16x32_bf16 v[40:43], a[168:171], v[196:199], v[40:43]\n" \
"ds_read_b128 a[68:71], v208 offset:32768\n" \
"v_mfma_f32_16x16x32_bf16 v[40:43], a[136:139], v[188:191], v[40:43]\n" \
"v_exp_f32_e64 v130, v130\n" \
"v_mfma_f32_16x16x32_bf16 v[44:47], a[172:175], v[196:199], v[44:47]\n" \
"ds_read_b128 a[72:75], v209 offset:32768\n" \
"v_mfma_f32_16x16x32_bf16 v[44:47], a[140:143], v[188:191], v[44:47]\n" \
"v_exp_f32_e64 v131, v131\n" \
"v_smfmac_f32_16x16x64_bf16 v[176:179], a[192:195], v[184:191], v228\n" \
"v_mfma_f32_16x16x32_bf16 v[136:139], a[96:99], a[16:19], v[164:167]\n" \
"v_exp_f32_e64 v132, v132\n" \
"v_mfma_f32_16x16x32_bf16 v[136:139], a[100:103], a[20:23], v[136:139]\n" \
"ds_read_b128 a[76:79], v210 offset:32768\n" \
"v_mfma_f32_16x16x32_bf16 v[136:139], a[104:107], a[24:27], v[136:139]\n" \
"v_exp_f32_e64 v133, v133\n" \
"v_mfma_f32_16x16x32_bf16 v[136:139], a[108:111], a[28:31], v[136:139]\n" \
"v_cvt_pk_bf16_f32 v192, v128, v129\n" \
"v_cvt_pk_bf16_f32 v193, v130, v131\n" \
"v_mfma_f32_16x16x32_bf16 v[140:143], a[124:127], a[28:31], v[164:167]\n" \
"v_exp_f32_e64 v134, v134\n" \
"v_mfma_f32_16x16x32_bf16 v[140:143], a[120:123], a[24:27], v[140:143]\n" \
"ds_read_b128 a[92:95], v210 offset:36864\n" \
"v_mfma_f32_16x16x32_bf16 v[140:143], a[116:119], a[20:23], v[140:143]\n" \
"buffer_load_dwordx4 v215, %[srdv], s48 offen offset:1024 lds\n" \
"v_mfma_f32_16x16x32_bf16 v[140:143], a[112:115], a[16:19], v[140:143]\n" \
"v_exp_f32_e64 v135, v135\n" \
"v_mfma_f32_16x16x32_bf16 v[48:51], a[176:179], v[196:199], v[48:51]\n" \
"ds_read_b128 a[88:91], v209 offset:36864\n" \
"v_mfma_f32_16x16x32_bf16 v[48:51], a[144:147], v[188:191], v[48:51]\n" \
"v_exp_f32_e64 v136, v136\n" \
"v_mfma_f32_16x16x32_bf16 v[52:55], a[180:183], v[196:199], v[52:55]\n" \
"v_cvt_pk_bf16_f32 v194, v132, v133\n" \
"v_cvt_pk_bf16_f32 v195, v134, v135\n" \
"v_mfma_f32_16x16x32_bf16 v[52:55], a[148:151], v[188:191], v[52:55]\n" \
"v_exp_f32_e64 v137, v137\n" \
"v_mfma_f32_16x16x32_bf16 v[56:59], a[184:187], v[196:199], v[56:59]\n" \
"v_exp_f32_e64 v138, v138\n" \
"v_mfma_f32_16x16x32_bf16 v[56:59], a[152:155], v[188:191], v[56:59]\n" \
"ds_read_b128 a[84:87], v208 offset:36864\n" \
"v_mfma_f32_16x16x32_bf16 v[60:63], a[188:191], v[196:199], v[60:63]\n" \
"v_exp_f32_e64 v139, v139\n" \
"v_mfma_f32_16x16x32_bf16 v[60:63], a[156:159], v[188:191], v[60:63]\n" \
"s_cmp_lg_u32 %[wid], 0\n" \
"s_cbranch_scc1 LtB3_%=\n" \
"global_load_dword v237, v236, s[58:59]\n" \
"s_add_u32 s61, s48, 49152\n" \
"s_mov_b32 exec_hi, 0\n" \
"s_nop 0\n" \
"buffer_load_dword v237, v224, %[srdp], s61 offen\n" \
"s_mov_b32 exec_hi, -1\n" \
"s_nop 0\n" \
"LtB3_%=:\n" \
"v_mfma_f32_16x16x32_bf16 v[144:147], a[96:99], a[32:35], v[168:171]\n" \
"v_exp_f32_e64 v140, v140\n" \
"v_mfma_f32_16x16x32_bf16 v[144:147], a[100:103], a[36:39], v[144:147]\n" \
"ds_read_b128 a[80:83], %[kb0] offset:36864\n" \
"v_mfma_f32_16x16x32_bf16 v[144:147], a[104:107], a[40:43], v[144:147]\n" \
"v_exp_f32_e64 v141, v141\n" \
"v_mfma_f32_16x16x32_bf16 v[144:147], a[108:111], a[44:47], v[144:147]\n" \
"v_cvt_pk_bf16_f32 v196, v136, v137\n" \
"v_cvt_pk_bf16_f32 v197, v138, v139\n" \
"v_mfma_f32_16x16x32_bf16 v[148:151], a[124:127], a[44:47], v[168:171]\n" \
"v_exp_f32_e64 v142, v142\n" \
"v_mfma_f32_16x16x32_bf16 v[148:151], a[120:123], a[40:43], v[148:151]\n" \
"v_mfma_f32_16x16x32_bf16 v[148:151], a[116:119], a[36:39], v[148:151]\n" \
"buffer_load_dwordx4 %[vd0], %[srdv], s48 offen offset:2048 lds\n" \
"v_mfma_f32_16x16x32_bf16 v[148:151], a[112:115], a[32:35], v[148:151]\n" \
"v_exp_f32_e64 v143, v143\n" \
"v_mfma_f32_16x16x32_bf16 v[96:99], a[160:163], v[204:207], v[96:99]\n" \
"ds_read_b64_tr_b16 a[160:161], %[vvb] offset:24576\n" \
"v_mfma_f32_16x16x32_bf16 v[96:99], a[128:131], v[220:223], v[96:99]\n" \
"v_exp_f32_e64 v144, v144\n" \
"v_mfma_f32_16x16x32_bf16 v[100:103], a[164:167], v[204:207], v[100:103]\n" \
"v_cvt_pk_bf16_f32 v198, v140, v141\n" \
"v_cvt_pk_bf16_f32 v199, v142, v143\n" \
"v_mfma_f32_16x16x32_bf16 v[100:103], a[132:135], v[220:223], v[100:103]\n" \
"v_exp_f32_e64 v145, v145\n" \
"v_mfma_f32_16x16x32_bf16 v[104:107], a[168:171], v[204:207], v[104:107]\n" \
"ds_read_b64_tr_b16 a[162:163], %[vvb] offset:28672\n" \
"ds_read_b64_tr_b16 a[164:165], v211 offset:24576\n" \
"v_mfma_f32_16x16x32_bf16 v[104:107], a[136:139], v[220:223], v[104:107]\n" \
"v_exp_f32_e64 v146, v146\n" \
"v_mfma_f32_16x16x32_bf16 v[108:111], a[172:175], v[204:207], v[108:111]\n" \
"ds_read_b64_tr_b16 a[166:167], v211 offset:28672\n" \
"v_mfma_f32_16x16x32_bf16 v[108:111], a[140:143], v[220:223], v[108:111]\n" \
"v_exp_f32_e64 v147, v147\n" \
"v_smfmac_f32_16x16x64_bf16 v[180:183], a[192:195], v[216:223], v228\n" \
"ds_read_b64_tr_b16 a[168:169], %[vvb] offset:25088\n" \
"s_cbranch_vccz Lpp4_%=\n" \
"buffer_store_dwordx4 a[196:199], v235, %[srdz], s62 offen offset:0 sc1\n" \
"buffer_store_dwordx4 a[196:199], v226, %[srdz], s62 offen offset:0 sc1\n" \
"Lpp4_%=:\n" \
"v_mfma_f32_16x16x32_bf16 v[152:155], a[96:99], a[48:51], v[172:175]\n" \
"v_exp_f32_e32 v148, v148\n" \
"v_mfma_f32_16x16x32_bf16 v[152:155], a[100:103], a[52:55], v[152:155]\n" \
"ds_read_b64_tr_b16 a[170:171], %[vvb] offset:29184\n" \
"v_mfma_f32_16x16x32_bf16 v[152:155], a[104:107], a[56:59], v[152:155]\n" \
"v_exp_f32_e64 v149, v149\n" \
"v_mfma_f32_16x16x32_bf16 v[152:155], a[108:111], a[60:63], v[152:155]\n" \
"v_cvt_pk_bf16_f32 v200, v144, v145\n" \
"v_cvt_pk_bf16_f32 v201, v146, v147\n" \
"v_mfma_f32_16x16x32_bf16 v[156:159], a[124:127], a[60:63], v[172:175]\n" \
"v_exp_f32_e64 v150, v150\n" \
"v_mfma_f32_16x16x32_bf16 v[156:159], a[120:123], a[56:59], v[156:159]\n" \
"ds_read_b64_tr_b16 a[172:173], v211 offset:25088\n" \
"v_mfma_f32_16x16x32_bf16 v[156:159], a[116:119], a[52:55], v[156:159]\n" \
"buffer_load_dwordx4 v215, %[srdv], s48 offen offset:3072 lds\n" \
"v_mfma_f32_16x16x32_bf16 v[156:159], a[112:115], a[48:51], v[156:159]\n" \
"v_exp_f32_e64 v151, v151\n" \
"v_mfma_f32_16x16x32_bf16 v[112:115], a[176:179], v[204:207], v[112:115]\n" \
"ds_read_b64_tr_b16 a[174:175], v211 offset:29184\n" \
"ds_read_b64_tr_b16 a[176:177], %[vvb] offset:25600\n" \
"v_mfma_f32_16x16x32_bf16 v[112:115], a[144:147], v[220:223], v[112:115]\n" \
"v_exp_f32_e64 v152, v152\n" \
"v_mfma_f32_16x16x32_bf16 v[116:119], a[180:183], v[204:207], v[116:119]\n" \
"v_cvt_pk_bf16_f32 v202, v148, v149\n" \
"v_cvt_pk_bf16_f32 v203, v150, v151\n" \
"v_mfma_f32_16x16x32_bf16 v[116:119], a[148:151], v[220:223], v[116:119]\n" \
"v_exp_f32_e64 v153, v153\n" \
"v_mfma_f32_16x16x32_bf16 v[120:123], a[184:187], v[204:207], v[120:123]\n" \
"v_exp_f32_e64 v154, v154\n" \
"v_mfma_f32_16x16x32_bf16 v[120:123], a[152:155], v[220:223], v[120:123]\n" \
"ds_read_b64_tr_b16 a[178:179], %[vvb] offset:29696\n" \
"v_mfma_f32_16x16x32_bf16 v[124:127], a[188:191], v[204:207], v[124:127]\n" \
"v_exp_f32_e64 v155, v155\n" \
"v_mfma_f32_16x16x32_bf16 v[124:127], a[156:159], v[220:223], v[124:127]\n" \
"ds_read_b64_tr_b16 a[180:181], v211 offset:25600\n" \
"s_waitcnt lgkmcnt(13)\n" \
"v_mfma_f32_16x16x32_bf16 v[128:131], a[64:67], a[0:3], v[160:163]\n" \
"v_exp_f32_e32 v156, v156\n" \
"v_mfma_f32_16x16x32_bf16 v[128:131], a[68:71], a[4:7], v[128:131]\n" \
"ds_read_b64_tr_b16 a[182:183], v211 offset:29696\n" \
"s_add_u32 m0, %[m0b], 16384\n" \
"v_mfma_f32_16x16x32_bf16 v[128:131], a[72:75], a[8:11], v[128:131]\n" \
"v_exp_f32_e64 v157, v157\n" \
"v_mfma_f32_16x16x32_bf16 v[128:131], a[76:79], a[12:15], v[128:131]\n" \
"v_cvt_pk_bf16_f32 v204, v152, v153\n" \
"v_cvt_pk_bf16_f32 v205, v154, v155\n" \
"v_mfma_f32_16x16x32_bf16 v[132:135], a[92:95], a[12:15], v[160:163]\n" \
"v_exp_f32_e64 v158, v158\n" \
"v_mfma_f32_16x16x32_bf16 v[132:135], a[88:91], a[8:11], v[132:135]\n" \
"ds_read_b64_tr_b16 a[184:185], %[vvb] offset:26112\n" \
"s_waitcnt lgkmcnt(5)\n" \
"v_mfma_f32_16x16x32_bf16 v[132:135], a[84:87], a[4:7], v[132:135]\n" \
"buffer_load_dwordx4 %[kd0], %[srdk], s48 offen offset:0 lds\n" \
"v_mfma_f32_16x16x32_bf16 v[132:135], a[80:83], a[0:3], v[132:135]\n" \
"v_exp_f32_e32 v159, v159\n" \
"v_mfma_f32_16x16x32_bf16 v[64:67], a[128:131], v[216:219], v[64:67]\n" \
"ds_read_b64_tr_b16 a[186:187], %[vvb] offset:30208\n" \
"ds_read_b64_tr_b16 a[188:189], v211 offset:26112\n" \
"v_mfma_f32_16x16x32_bf16 v[64:67], a[160:163], v[200:203], v[64:67]\n" \
"v_exp_f32_e64 v128, v128\n" \
"v_mfma_f32_16x16x32_bf16 v[68:71], a[132:135], v[216:219], v[68:71]\n" \
"v_cvt_pk_bf16_f32 v206, v156, v157\n" \
"v_cvt_pk_bf16_f32 v207, v158, v159\n" \
"v_mfma_f32_16x16x32_bf16 v[68:71], a[164:167], v[200:203], v[68:71]\n" \
"v_exp_f32_e64 v129, v129\n" \
"v_mfma_f32_16x16x32_bf16 v[72:75], a[136:139], v[216:219], v[72:75]\n" \
"ds_read_b64_tr_b16 a[190:191], v211 offset:30208\n" \
"ds_read_b64_tr_b16 a[128:129], %[vvb] offset:32768\n" \
"v_mfma_f32_16x16x32_bf16 v[72:75], a[168:171], v[200:203], v[72:75]\n" \
"v_exp_f32_e64 v130, v130\n" \
"v_mfma_f32_16x16x32_bf16 v[76:79], a[140:143], v[216:219], v[76:79]\n" \
".if . - adaln_body != 24576\n" \
".error \"size model\"\n" \
".endif\n" \
"Lk5_%=:\n" \
"s_cbranch_execz adaln_kr\n" \
"s_nop 0\n" \
"ds_read_b64_tr_b16 a[130:131], %[vvb] offset:36864\n" \
"ds_read_b64_tr_b16 a[132:133], v211 offset:32768\n" \
"v_mfma_f32_16x16x32_bf16 v[76:79], a[172:175], v[200:203], v[76:79]\n" \
"v_exp_f32_e64 v131, v131\n" \
"v_smfmac_f32_16x16x64_bf16 v[176:179], a[192:195], v[192:199], v228\n" \
"ds_read_b64_tr_b16 a[134:135], v211 offset:36864\n" \
"ds_read_b64_tr_b16 a[136:137], %[vvb] offset:33280\n" \
"v_mfma_f32_16x16x32_bf16 v[136:139], a[64:67], a[16:19], v[164:167]\n" \
"v_exp_f32_e64 v132, v132\n" \
"v_mfma_f32_16x16x32_bf16 v[136:139], a[68:71], a[20:23], v[136:139]\n" \
"ds_read_b64_tr_b16 a[138:139], %[vvb] offset:37376\n" \
"ds_read_b64_tr_b16 a[140:141], v211 offset:33280\n" \
"v_mfma_f32_16x16x32_bf16 v[136:139], a[72:75], a[24:27], v[136:139]\n" \
"v_exp_f32_e64 v133, v133\n" \
"v_mfma_f32_16x16x32_bf16 v[136:139], a[76:79], a[28:31], v[136:139]\n" \
"v_cvt_pk_bf16_f32 v184, v128, v129\n" \
"v_cvt_pk_bf16_f32 v185, v130, v131\n" \
"v_mfma_f32_16x16x32_bf16 v[140:143], a[92:95], a[28:31], v[164:167]\n" \
"v_exp_f32_e64 v134, v134\n" \
"v_mfma_f32_16x16x32_bf16 v[140:143], a[88:91], a[24:27], v[140:143]\n" \
"ds_read_b64_tr_b16 a[142:143], v211 offset:37376\n" \
"s_waitcnt lgkmcnt(8)\n" \
"v_mfma_f32_16x16x32_bf16 v[140:143], a[84:87], a[20:23], v[140:143]\n" \
"buffer_load_dwordx4 v212, %[srdk], s48 offen offset:1024 lds\n" \
"v_mfma_f32_16x16x32_bf16 v[140:143], a[80:83], a[16:19], v[140:143]\n" \
"v_exp_f32_e32 v135, v135\n" \
"v_mfma_f32_16x16x32_bf16 v[80:83], a[144:147], v[216:219], v[80:83]\n" \
"ds_read_b64_tr_b16 a[144:145], %[vvb] offset:33792\n" \
"ds_read_b64_tr_b16 a[146:147], %[vvb] offset:37888\n" \
"v_mfma_f32_16x16x32_bf16 v[80:83], a[176:179], v[200:203], v[80:83]\n" \
"v_exp_f32_e64 v136, v136\n" \
"v_mfma_f32_16x16x32_bf16 v[84:87], a[148:151], v[216:219], v[84:87]\n" \
"v_cvt_pk_bf16_f32 v186, v132, v133\n" \
"v_cvt_pk_bf16_f32 v187, v134, v135\n" \
"v_mfma_f32_16x16x32_bf16 v[84:87], a[180:183], v[200:203], v[84:87]\n" \
"v_exp_f32_e64 v137, v137\n" \
"v_mfma_f32_16x16x32_bf16 v[88:91], a[152:155], v[216:219], v[88:91]\n" \
"v_exp_f32_e64 v138, v138\n" \
"v_mfma_f32_16x16x32_bf16 v[88:91], a[184:187], v[200:203], v[88:91]\n" \
"ds_read_b64_tr_b16 a[148:149], v211 offset:33792\n" \
"ds_read_b64_tr_b16 a[150:151], v211 offset:37888\n" \
"v_mfma_f32_16x16x32_bf16 v[92:95], a[156:159], v[216:219], v[92:95]\n" \
"v_exp_f32_e64 v139, v139\n" \
"v_mfma_f32_16x16x32_bf16 v[92:95], a[188:191], v[200:203], v[92:95]\n" \
"ds_read_b64_tr_b16 a[152:153], %[vvb] offset:34304\n" \
"s_cbranch_vccz Lpp5_%=\n" \
"buffer_store_dwordx4 a[196:199], v235, %[srdz], s63 offen offset:1024 sc1\n" \
"buffer_store_dwordx4 a[196:199], v226, %[srdz], s63 offen offset:1024 sc1\n" \
"Lpp5_%=:\n" \
"v_mfma_f32_16x16x32_bf16 v[144:147], a[64:67], a[32:35], v[168:171]\n" \
"v_exp_f32_e32 v140, v140\n" \
"v_mfma_f32_16x16x32_bf16 v[144:147], a[68:71], a[36:39], v[144:147]\n" \
"ds_read_b64_tr_b16 a[154:155], %[vvb] offset:38400\n" \
"ds_read_b64_tr_b16 a[156:157], v211 offset:34304\n" \
"v_mfma_f32_16x16x32_bf16 v[144:147], a[72:75], a[40:43], v[144:147]\n" \
"v_exp_f32_e64 v141, v141\n" \
"v_mfma_f32_16x16x32_bf16 v[144:147], a[76:79], a[44:47], v[144:147]\n" \
"v_cvt_pk_bf16_f32 v188, v136, v137\n" \
"v_cvt_pk_bf16_f32 v189, v138, v139\n" \
"v_mfma_f32_16x16x32_bf16 v[148:151], a[92:95], a[44:47], v[168:171]\n" \
"v_exp_f32_e64 v142, v142\n" \
"v_mfma_f32_16x16x32_bf16 v[148:151], a[88:91], a[40:43], v[148:151]\n" \
"ds_read_b64_tr_b16 a[158:159], v211 offset:38400\n" \
"s_waitcnt lgkmcnt(8)\n" \
"v_mfma_f32_16x16x32_bf16 v[148:151], a[84:87], a[36:39], v[148:151]\n" \
"buffer_load_dwordx4 v213, %[srdk], s48 offen offset:2048 lds\n" \
"v_mfma_f32_16x16x32_bf16 v[148:151], a[80:83], a[32:35], v[148:151]\n" \
"v_exp_f32_e32 v143, v143\n" \
"v_mfma_f32_16x16x32_bf16 v[0:3], a[160:163], v[192:195], v[0:3]\n" \
"ds_read_b128 a[96:99], %[kb0] offset:40960\n" \
"v_mfma_f32_16x16x32_bf16 v[0:3], a[128:131], v[184:187], v[0:3]\n" \
"v_exp_f32_e64 v144, v144\n" \
"v_mfma_f32_16x16x32_bf16 v[4:7], a[164:167], v[192:195], v[4:7]\n" \
"v_cvt_pk_bf16_f32 v190, v140, v141\n" \
"v_cvt_pk_bf16_f32 v191, v142, v143\n" \
"v_mfma_f32_16x16x32_bf16 v[4:7], a[132:135], v[184:187], v[4:7]\n" \
"v_exp_f32_e64 v145, v145\n" \
"v_mfma_f32_16x16x32_bf16 v[8:11], a[168:171], v[192:195], v[8:11]\n" \
"ds_read_b128 a[100:103], v208 offset:40960\n" \
"v_mfma_f32_16x16x32_bf16 v[8:11], a[136:139], v[184:187], v[8:11]\n" \
"v_exp_f32_e64 v146, v146\n" \
"v_mfma_f32_16x16x32_bf16 v[12:15], a[172:175], v[192:195], v[12:15]\n" \
"ds_read_b128 a[104:107], v209 offset:40960\n" \
"v_mfma_f32_16x16x32_bf16 v[12:15], a[140:143], v[184:187], v[12:15]\n" \
"v_exp_f32_e64 v147, v147\n" \
"v_smfmac_f32_16x16x64_bf16 v[180:183], a[192:195], v[200:207], v228\n" \
"ds_read_b128 a[108:111], v210 offset:40960\n" \
"v_mfma_f32_16x16x32_bf16 v[152:155], a[64:67], a[48:51], v[172:175]\n" \
"v_exp_f32_e64 v148, v148\n" \
"v_mfma_f32_16x16x32_bf16 v[152:155], a[68:71], a[52:55], v[152:155]\n" \
"ds_read_b128 a[124:127], v210 offset:45056\n" \
"v_mfma_f32_16x16x32_bf16 v[152:155], a[72:75], a[56:59], v[152:155]\n" \
"v_exp_f32_e64 v149, v149\n" \
"v_mfma_f32_16x16x32_bf16 v[152:155], a[76:79], a[60:63], v[152:155]\n" \
"v_cvt_pk_bf16_f32 v216, v144, v145\n" \
"v_cvt_pk_bf16_f32 v217, v146, v147\n" \
"v_mfma_f32_16x16x32_bf16 v[156:159], a[92:95], a[60:63], v[172:175]\n" \
"v_exp_f32_e64 v150, v150\n" \
"v_mfma_f32_16x16x32_bf16 v[156:159], a[88:91], a[56:59], v[156:159]\n" \
"ds_read_b128 a[120:123], v209 offset:45056\n" \
"s_waitcnt lgkmcnt(6)\n" \
"v_mfma_f32_16x16x32_bf16 v[156:159], a[84:87], a[52:55], v[156:159]\n" \
"buffer_load_dwordx4 v214, %[srdk], s48 offen offset:3072 lds\n" \
"v_mfma_f32_16x16x32_bf16 v[156:159], a[80:83], a[48:51], v[156:159]\n" \
"v_exp_f32_e32 v151, v151\n" \
"v_mfma_f32_16x16x32_bf16 v[16:19], a[176:179], v[192:195], v[16:19]\n" \
"ds_read_b128 a[116:119], v208 offset:45056\n" \
"v_mfma_f32_16x16x32_bf16 v[16:19], a[144:147], v[184:187], v[16:19]\n" \
"v_exp_f32_e64 v152, v152\n" \
"v_mfma_f32_16x16x32_bf16 v[20:23], a[180:183], v[192:195], v[20:23]\n" \
"v_cvt_pk_bf16_f32 v218, v148, v149\n" \
"v_cvt_pk_bf16_f32 v219, v150, v151\n" \
"v_mfma_f32_16x16x32_bf16 v[20:23], a[148:151], v[184:187], v[20:23]\n" \
"v_exp_f32_e64 v153, v153\n" \
"v_mfma_f32_16x16x32_bf16 v[24:27], a[184:187], v[192:195], v[24:27]\n" \
"v_exp_f32_e64 v154, v154\n" \
"v_mfma_f32_16x16x32_bf16 v[24:27], a[152:155], v[184:187], v[24:27]\n" \
"ds_read_b128 a[112:115], %[kb0] offset:45056\n" \
"s_add_u32 s48, s48, 0x4000\n" \
"v_mfma_f32_16x16x32_bf16 v[28:31], a[188:191], v[192:195], v[28:31]\n" \
"v_exp_f32_e64 v155, v155\n" \
"v_mfma_f32_16x16x32_bf16 v[28:31], a[156:159], v[184:187], v[28:31]\n" \
"s_waitcnt vmcnt(13) lgkmcnt(2)\n" \
"s_barrier\n" \
"v_mfma_f32_16x16x32_bf16 v[128:131], a[96:99], a[0:3], v[160:163]\n" \
"v_exp_f32_e64 v156, v156\n" \
"v_mfma_f32_16x16x32_bf16 v[128:131], a[100:103], a[4:7], v[128:131]\n" \
"s_add_u32 m0, %[m0b], 81920\n" \
"v_mfma_f32_16x16x32_bf16 v[128:131], a[104:107], a[8:11], v[128:131]\n" \
"v_exp_f32_e64 v157, v157\n" \
"v_mfma_f32_16x16x32_bf16 v[128:131], a[108:111], a[12:15], v[128:131]\n" \
"v_cvt_pk_bf16_f32 v220, v152, v153\n" \
"v_cvt_pk_bf16_f32 v221, v154, v155\n" \
"v_mfma_f32_16x16x32_bf16 v[132:135], a[124:127], a[12:15], v[160:163]\n" \
"v_exp_f32_e64 v158, v158\n" \
"v_mfma_f32_16x16x32_bf16 v[132:135], a[120:123], a[8:11], v[132:135]\n" \
"s_waitcnt lgkmcnt(0)\n" \
"v_mfma_f32_16x16x32_bf16 v[132:135], a[116:119], a[4:7], v[132:135]\n" \
"buffer_load_dwordx4 %[vd0], %[srdv], s48 offen offset:0 lds\n" \
"v_mfma_f32_16x16x32_bf16 v[132:135], a[112:115], a[0:3], v[132:135]\n" \
"v_exp_f32_e32 v159, v159\n" \
"v_mfma_f32_16x16x32_bf16 v[32:35], a[160:163], v[196:199], v[32:35]\n" \
"ds_read_b128 a[64:67], %[kb0] offset:49152\n" \
"v_mfma_f32_16x16x32_bf16 v[32:35], a[128:131], v[188:191], v[32:35]\n" \
"v_exp_f32_e64 v128, v128\n" \
"v_mfma_f32_16x16x32_bf16 v[36:39], a[164:167], v[196:199], v[36:39]\n" \
"v_cvt_pk_bf16_f32 v222, v156, v157\n" \
"v_cvt_pk_bf16_f32 v223, v158, v159\n" \
"v_mfma_f32_16x16x32_bf16 v[36:39], a[132:135], v[188:191], v[36:39]\n" \
"v_exp_f32_e64 v129, v129\n" \
"v_mfma_f32_16x16x32_bf16 v[40:43], a[168:171], v[196:199], v[40:43]\n" \
"ds_read_b128 a[68:71], v208 offset:49152\n" \
"v_mfma_f32_16x16x32_bf16 v[40:43], a[136:139], v[188:191], v[40:43]\n" \
"v_exp_f32_e64 v130, v130\n" \
"v_mfma_f32_16x16x32_bf16 v[44:47], a[172:175], v[196:199], v[44:47]\n" \
"ds_read_b128 a[72:75], v209 offset:49152\n" \
"v_mfma_f32_16x16x32_bf16 v[44:47], a[140:143], v[188:191], v[44:47]\n" \
"v_exp_f32_e64 v131, v131\n" \
"v_smfmac_f32_16x16x64_bf16 v[176:179], a[192:195], v[184:191], v228\n" \
"v_mfma_f32_16x16x32_bf16 v[136:139], a[96:99], a[16:19], v[164:167]\n" \
"v_exp_f32_e64 v132, v132\n" \
"v_mfma_f32_16x16x32_bf16 v[136:139], a[100:103], a[20:23], v[136:139]\n" \
"ds_read_b128 a[76:79], v210 offset:49152\n" \
"v_mfma_f32_16x16x32_bf16 v[136:139], a[104:107], a[24:27], v[136:139]\n" \
"v_exp_f32_e64 v133, v133\n" \
"v_mfma_f32_16x16x32_bf16 v[136:139], a[108:111], a[28:31], v[136:139]\n" \
"v_cvt_pk_bf16_f32 v192, v128, v129\n" \
"v_cvt_pk_bf16_f32 v193, v130, v131\n" \
"v_mfma_f32_16x16x32_bf16 v[140:143], a[124:127], a[28:31], v[164:167]\n" \
"v_exp_f32_e64 v134, v134\n" \
"v_mfma_f32_16x16x32_bf16 v[140:143], a[120:123], a[24:27], v[140:143]\n" \
"ds_read_b128 a[92:95], v210 offset:53248\n" \
"v_mfma_f32_16x16x32_bf16 v[140:143], a[116:119], a[20:23], v[140:143]\n" \
"buffer_load_dwordx4 v215, %[srdv], s48 offen offset:1024 lds\n" \
"v_mfma_f32_16x16x32_bf16 v[140:143], a[112:115], a[16:19], v[140:143]\n" \
"v_exp_f32_e64 v135, v135\n" \
"v_mfma_f32_16x16x32_bf16 v[48:51], a[176:179], v[196:199], v[48:51]\n" \
"ds_read_b128 a[88:91], v209 offset:53248\n" \
"v_mfma_f32_16x16x32_bf16 v[48:51], a[144:147], v[188:191], v[48:51]\n" \
"v_exp_f32_e64 v136, v136\n" \
"v_mfma_f32_16x16x32_bf16 v[52:55], a[180:183], v[196:199], v[52:55]\n" \
"v_cvt_pk_bf16_f32 v194, v132, v133\n" \
"v_cvt_pk_bf16_f32 v195, v134, v135\n" \
"v_mfma_f32_16x16x32_bf16 v[52:55], a[148:151], v[188:191], v[52:55]\n" \
"v_exp_f32_e64 v137, v137\n" \
"v_mfma_f32_16x16x32_bf16 v[56:59], a[184:187], v[196:199], v[56:59]\n" \
"v_exp_f32_e64 v138, v138\n" \
"v_mfma_f32_16x16x32_bf16 v[56:59], a[152:155], v[188:191], v[56:59]\n" \
"ds_read_b128 a[84:87], v208 offset:53248\n" \
"v_mfma_f32_16x16x32_bf16 v[60:63], a[188:191], v[196:199], v[60:63]\n" \
"v_exp_f32_e64 v139, v139\n" \
"v_mfma_f32_16x16x32_bf16 v[60:63], a[156:159], v[188:191], v[60:63]\n" \
"s_cmp_lg_u32 %[wid], 1\n" \
"s_cbranch_scc1 LtB5_%=\n" \
"global_load_dword v237, v236, s[58:59]\n" \
"s_add_u32 s61, s48, 49152\n" \
"s_mov_b32 exec_hi, 0\n" \
"s_nop 0\n" \
"buffer_load_dword v237, v224, %[srdp], s61 offen\n" \
"s_mov_b32 exec_hi, -1\n" \
"s_nop 0\n" \
"LtB5_%=:\n" \
"v_mfma_f32_16x16x32_bf16 v[144:147], a[96:99], a[32:35], v[168:171]\n" \
"v_exp_f32_e64 v140, v140\n" \
"v_mfma_f32_16x16x32_bf16 v[144:147], a[100:103], a[36:39], v[144:147]\n" \
"ds_read_b128 a[80:83], %[kb0] offset:53248\n" \
"v_mfma_f32_16x16x32_bf16 v[144:147], a[104:107], a[40:43], v[144:147]\n" \
"v_exp_f32_e64 v141, v141\n" \
"v_mfma_f32_16x16x32_bf16 v[144:147], a[108:111], a[44:47], v[144:147]\n" \
"v_cvt_pk_bf16_f32 v196, v136, v137\n" \
"v_cvt_pk_bf16_f32 v197, v138, v139\n" \
"v_mfma_f32_16x16x32_bf16 v[148:151], a[124:127], a[44:47], v[168:171]\n" \
"v_exp_f32_e64 v142, v142\n" \
"v_mfma_f32_16x16x32_bf16 v[148:151], a[120:123], a[40:43], v[148:151]\n" \
"v_mfma_f32_16x16x32_bf16 v[148:151], a[116:119], a[36:39], v[148:151]\n" \
"buffer_load_dwordx4 %[vd0], %[srdv], s48 offen offset:2048 lds\n" \
"v_mfma_f32_16x16x32_bf16 v[148:151], a[112:115], a[32:35], v[148:151]\n" \
"v_exp_f32_e64 v143, v143\n" \
"v_mfma_f32_16x16x32_bf16 v[96:99], a[160:163], v[204:207], v[96:99]\n" \
"ds_read_b64_tr_b16 a[160:161], %[vvb] offset:40960\n" \
"v_mfma_f32_16x16x32_bf16 v[96:99], a[128:131], v[220:223], v[96:99]\n" \
"v_exp_f32_e64 v144, v144\n" \
"v_mfma_f32_16x16x32_bf16 v[100:103], a[164:167], v[204:207], v[100:103]\n" \
"v_cvt_pk_bf16_f32 v198, v140, v141\n" \
"v_cvt_pk_bf16_f32 v199, v142, v143\n" \
"v_mfma_f32_16x16x32_bf16 v[100:103], a[132:135], v[220:223], v[100:103]\n" \
"v_exp_f32_e64 v145, v145\n" \
"v_mfma_f32_16x16x32_bf16 v[104:107], a[168:171], v[204:207], v[104:107]\n" \
"ds_read_b64_tr_b16 a[162:163], %[vvb] offset:45056\n" \
"ds_read_b64_tr_b16 a[164:165], v211 offset:40960\n" \
"v_mfma_f32_16x16x32_bf16 v[104:107], a[136:139], v[220:223], v[104:107]\n" \
"v_exp_f32_e64 v146, v146\n" \
"v_mfma_f32_16x16x32_bf16 v[108:111], a[172:175], v[204:207], v[108:111]\n" \
"ds_read_b64_tr_b16 a[166:167], v211 offset:45056\n" \
"v_mfma_f32_16x16x32_bf16 v[108:111], a[140:143], v[220:223], v[108:111]\n" \
"v_exp_f32_e64 v147, v147\n" \
"v_smfmac_f32_16x16x64_bf16 v[180:183], a[192:195], v[216:223], v228\n" \
"ds_read_b64_tr_b16 a[168:169], %[vvb] offset:41472\n" \
"v_mfma_f32_16x16x32_bf16 v[152:155], a[96:99], a[48:51], v[172:175]\n" \
"v_exp_f32_e64 v148, v148\n" \
"v_mfma_f32_16x16x32_bf16 v[152:155], a[100:103], a[52:55], v[152:155]\n" \
"ds_read_b64_tr_b16 a[170:171], %[vvb] offset:45568\n" \
"v_mfma_f32_16x16x32_bf16 v[152:155], a[104:107], a[56:59], v[152:155]\n" \
"v_exp_f32_e64 v149, v149\n" \
"v_mfma_f32_16x16x32_bf16 v[152:155], a[108:111], a[60:63], v[152:155]\n" \
"v_cvt_pk_bf16_f32 v200, v144, v145\n" \
"v_cvt_pk_bf16_f32 v201, v146, v147\n" \
"v_mfma_f32_16x16x32_bf16 v[156:159], a[124:127], a[60:63], v[172:175]\n" \
"v_exp_f32_e64 v150, v150\n" \
"v_mfma_f32_16x16x32_bf16 v[156:159], a[120:123], a[56:59], v[156:159]\n" \
"ds_read_b64_tr_b16 a[172:173], v211 offset:41472\n" \
"v_mfma_f32_16x16x32_bf16 v[156:159], a[116:119], a[52:55], v[156:159]\n" \
"buffer_load_dwordx4 v215, %[srdv], s48 offen offset:3072 lds\n" \
"v_mfma_f32_16x16x32_bf16 v[156:159], a[112:115], a[48:51], v[156:159]\n" \
"v_exp_f32_e64 v151, v151\n" \
"v_mfma_f32_16x16x32_bf16 v[112:115], a[176:179], v[204:207], v[112:115]\n" \
"ds_read_b64_tr_b16 a[174:175], v211 offset:45568\n" \
"ds_read_b64_tr_b16 a[176:177], %[vvb] offset:41984\n" \
"v_mfma_f32_16x16x32_bf16 v[112:115], a[144:147], v[220:223], v[112:115]\n" \
"v_exp_f32_e64 v152, v152\n" \
"v_mfma_f32_16x16x32_bf16 v[116:119], a[180:183], v[204:207], v[116:119]\n" \
"v_cvt_pk_bf16_f32 v202, v148, v149\n" \
"v_cvt_pk_bf16_f32 v203, v150, v151\n" \
"v_mfma_f32_16x16x32_bf16 v[116:119], a[148:151], v[220:223], v[116:119]\n" \
"v_exp_f32_e64 v153, v153\n" \
"v_mfma_f32_16x16x32_bf16 v[120:123], a[184:187], v[204:207], v[120:123]\n" \
"v_exp_f32_e64 v154, v154\n" \
"v_mfma_f32_16x16x32_bf16 v[120:123], a[152:155], v[220:223], v[120:123]\n" \
"ds_read_b64_tr_b16 a[178:179], %[vvb] offset:46080\n" \
"v_mfma_f32_16x16x32_bf16 v[124:127], a[188:191], v[204:207], v[124:127]\n" \
"v_exp_f32_e64 v155, v155\n" \
"v_mfma_f32_16x16x32_bf16 v[124:127], a[156:159], v[220:223], v[124:127]\n" \
"ds_read_b64_tr_b16 a[180:181], v211 offset:41984\n" \
"s_waitcnt lgkmcnt(13)\n" \
"v_mfma_f32_16x16x32_bf16 v[128:131], a[64:67], a[0:3], v[160:163]\n" \
"v_exp_f32_e32 v156, v156\n" \
"v_mfma_f32_16x16x32_bf16 v[128:131], a[68:71], a[4:7], v[128:131]\n" \
"ds_read_b64_tr_b16 a[182:183], v211 offset:46080\n" \
"s_add_u32 m0, %[m0b], 32768\n" \
"v_mfma_f32_16x16x32_bf16 v[128:131], a[72:75], a[8:11], v[128:131]\n" \
"v_exp_f32_e64 v157, v157\n" \
"v_mfma_f32_16x16x32_bf16 v[128:131], a[76:79], a[12:15], v[128:131]\n" \
"v_cvt_pk_bf16_f32 v204, v152, v153\n" \
"v_cvt_pk_bf16_f32 v205, v154, v155\n" \
"v_mfma_f32_16x16x32_bf16 v[132:135], a[92:95], a[12:15], v[160:163]\n" \
"v_exp_f32_e64 v158, v158\n" \
"v_mfma_f32_16x16x32_bf16 v[132:135], a[88:91], a[8:11], v[132:135]\n" \
"ds_read_b64_tr_b16 a[184:185], %[vvb] offset:42496\n" \
"s_waitcnt lgkmcnt(5)\n" \
"v_mfma_f32_16x16x32_bf16 v[132:135], a[84:87], a[4:7], v[132:135]\n" \
"buffer_load_dwordx4 %[kd0], %[srdk], s48 offen offset:0 lds\n" \
"v_mfma_f32_16x16x32_bf16 v[132:135], a[80:83], a[0:3], v[132:135]\n" \
"v_exp_f32_e32 v159, v159\n" \
"v_mfma_f32_16x16x32_bf16 v[64:67], a[128:131], v[216:219], v[64:67]\n" \
"ds_read_b64_tr_b16 a[186:187], %[vvb] offset:46592\n" \
"ds_read_b64_tr_b16 a[188:189], v211 offset:42496\n" \
"v_mfma_f32_16x16x32_bf16 v[64:67], a[160:163], v[200:203], v[64:67]\n" \
"v_exp_f32_e64 v128, v128\n" \
"v_mfma_f32_16x16x32_bf16 v[68:71], a[132:135], v[216:219], v[68:71]\n" \
"v_cvt_pk_bf16_f32 v206, v156, v157\n" \
"v_cvt_pk_bf16_f32 v207, v158, v159\n" \
"v_mfma_f32_16x16x32_bf16 v[68:71], a[164:167], v[200:203], v[68:71]\n" \
"v_exp_f32_e64 v129, v129\n" \
"v_mfma_f32_16x16x32_bf16 v[72:75], a[136:139], v[216:219], v[72:75]\n" \
"ds_read_b64_tr_b16 a[190:191], v211 offset:46592\n" \
"ds_read_b64_tr_b16 a[128:129], %[vvb] offset:49152\n" \
"v_mfma_f32_16x16x32_bf16 v[72:75], a[168:171], v[200:203], v[72:75]\n" \
"v_exp_f32_e64 v130, v130\n" \
"v_mfma_f32_16x16x32_bf16 v[76:79], a[140:143], v[216:219], v[76:79]\n" \
"ds_read_b64_tr_b16 a[130:131], %[vvb] offset:53248\n" \
"ds_read_b64_tr_b16 a[132:133], v211 offset:49152\n" \
"v_mfma_f32_16x16x32_bf16 v[76:79], a[172:175], v[200:203], v[76:79]\n" \
"v_exp_f32_e64 v131, v131\n" \
"v_smfmac_f32_16x16x64_bf16 v[176:179], a[192:195], v[192:199], v228\n" \
"ds_read_b64_tr_b16 a[134:135], v211 offset:53248\n" \
"ds_read_b64_tr_b16 a[136:137], %[vvb] offset:49664\n" \
"s_cbranch_vccz Lpp6_%=\n" \
"buffer_store_dwordx4 a[196:199], v235, %[srdz], s62 offen offset:1024 sc1\n" \
"buffer_store_dwordx4 a[196:199], v226, %[srdz], s62 offen offset:1024 sc1\n" \
"Lpp6_%=:\n" \
"v_mfma_f32_16x16x32_bf16 v[136:139], a[64:67], a[16:19], v[164:167]\n" \
"v_exp_f32_e32 v132, v132\n" \
"v_mfma_f32_16x16x32_bf16 v[136:139], a[68:71], a[20:23], v[136:139]\n" \
"ds_read_b64_tr_b16 a[138:139], %[vvb] offset:53760\n" \
"ds_read_b64_tr_b16 a[140:141], v211 offset:49664\n" \
"v_mfma_f32_16x16x32_bf16 v[136:139], a[72:75], a[24:27], v[136:139]\n" \
"v_exp_f32_e64 v133, v133\n" \
"v_mfma_f32_16x16x32_bf16 v[136:139], a[76:79], a[28:31], v[136:139]\n" \
"v_cvt_pk_bf16_f32 v184, v128, v129\n" \
"v_cvt_pk_bf16_f32 v185, v130, v131\n" \
"v_mfma_f32_16x16x32_bf16 v[140:143], a[92:95], a[28:31], v[164:167]\n" \
"v_exp_f32_e64 v134, v134\n" \
"v_mfma_f32_16x16x32_bf16 v[140:143], a[88:91], a[24:27], v[140:143]\n" \
"ds_read_b64_tr_b16 a[142:143], v211 offset:53760\n" \
"s_waitcnt lgkmcnt(8)\n" \
"v_mfma_f32_16x16x32_bf16 v[140:143], a[84:87], a[20:23], v[140:143]\n" \
"buffer_load_dwordx4 v212, %[srdk], s48 offen offset:1024 lds\n" \
"v_mfma_f32_16x16x32_bf16 v[140:143], a[80:83], a[16:19], v[140:143]\n" \
"v_exp_f32_e32 v135, v135\n" \
"v_mfma_f32_16x16x32_bf16 v[80:83], a[144:147], v[216:219], v[80:83]\n" \
"ds_read_b64_tr_b16 a[144:145], %[vvb] offset:50176\n" \
"ds_read_b64_tr_b16 a[146:147], %[vvb] offset:54272\n" \
"v_mfma_f32_16x16x32_bf16 v[80:83], a[176:179], v[200:203], v[80:83]\n" \
"v_exp_f32_e64 v136, v136\n" \
"v_mfma_f32_16x16x32_bf16 v[84:87], a[148:151], v[216:219], v[84:87]\n" \
"v_cvt_pk_bf16_f32 v186, v132, v133\n" \
"v_cvt_pk_bf16_f32 v187, v134, v135\n" \
"v_mfma_f32_16x16x32_bf16 v[84:87], a[180:183], v[200:203], v[84:87]\n" \
"v_exp_f32_e64 v137, v137\n" \
"v_mfma_f32_16x16x32_bf16 v[88:91], a[152:155], v[216:219], v[88:91]\n" \
"v_exp_f32_e64 v138, v138\n" \
"v_mfma_f32_16x16x32_bf16 v[88:91], a[184:187], v[200:203], v[88:91]\n" \
"ds_read_b64_tr_b16 a[148:149], v211 offset:50176\n" \
"ds_read_b64_tr_b16 a[150:151], v211 offset:54272\n" \
"v_mfma_f32_16x16x32_bf16 v[92:95], a[156:159], v[216:219], v[92:95]\n" \
"v_exp_f32_e64 v139, v139\n" \
"v_mfma_f32_16x16x32_bf16 v[92:95], a[188:191], v[200:203], v[92:95]\n" \
"ds_read_b64_tr_b16 a[152:153], %[vvb] offset:50688\n" \
"global_load_dword v237, %[vct], %[pct]\n" \
"v_mfma_f32_16x16x32_bf16 v[144:147], a[64:67], a[32:35], v[168:171]\n" \
"v_exp_f32_e64 v140, v140\n" \
"v_mfma_f32_16x16x32_bf16 v[144:147], a[68:71], a[36:39], v[144:147]\n" \
"ds_read_b64_tr_b16 a[154:155], %[vvb] offset:54784\n" \
"ds_read_b64_tr_b16 a[156:157], v211 offset:50688\n" \
"v_mfma_f32_16x16x32_bf16 v[144:147], a[72:75], a[40:43], v[144:147]\n" \
"v_exp_f32_e64 v141, v141\n" \
"v_mfma_f32_16x16x32_bf16 v[144:147], a[76:79], a[44:47], v[144:147]\n" \
"v_cvt_pk_bf16_f32 v188, v136, v137\n" \
"v_cvt_pk_bf16_f32 v189, v138, v139\n" \
"v_mfma_f32_16x16x32_bf16 v[148:151], a[92:95], a[44:47], v[168:171]\n" \
"v_exp_f32_e64 v142, v142\n" \
"v_mfma_f32_16x16x32_bf16 v[148:151], a[88:91], a[40:43], v[148:151]\n" \
"ds_read_b64_tr_b16 a[158:159], v211 offset:54784\n" \
"s_waitcnt lgkmcnt(8)\n" \
"v_mfma_f32_16x16x32_bf16 v[148:151], a[84:87], a[36:39], v[148:151]\n" \
"buffer_load_dwordx4 v213, %[srdk], s48 offen offset:2048 lds\n" \
"v_mfma_f32_16x16x32_bf16 v[148:151], a[80:83], a[32:35], v[148:151]\n" \
"v_exp_f32_e32 v143, v143\n" \
"v_mfma_f32_16x16x32_bf16 v[0:3], a[160:163], v[192:195], v[0:3]\n" \
"ds_read_b128 a[96:99], %[kb0] offset:57344\n" \
"v_mfma_f32_16x16x32_bf16 v[0:3], a[128:131], v[184:187], v[0:3]\n" \
"v_exp_f32_e64 v144, v144\n" \
"v_mfma_f32_16x16x32_bf16 v[4:7], a[164:167], v[192:195], v[4:7]\n" \
"v_cvt_pk_bf16_f32 v190, v140, v141\n" \
"v_cvt_pk_bf16_f32 v191, v142, v143\n" \
"v_mfma_f32_16x16x32_bf16 v[4:7], a[132:135], v[184:187], v[4:7]\n" \
"v_exp_f32_e64 v145, v145\n" \
"v_mfma_f32_16x16x32_bf16 v[8:11], a[168:171], v[192:195], v[8:11]\n" \
"ds_read_b128 a[100:103], v208 offset:57344\n" \
"v_mfma_f32_16x16x32_bf16 v[8:11], a[136:139], v[184:187], v[8:11]\n" \
"v_exp_f32_e64 v146, v146\n" \
"v_mfma_f32_16x16x32_bf16 v[12:15], a[172:175], v[192:195], v[12:15]\n" \
"ds_read_b128 a[104:107], v209 offset:57344\n" \
"v_mfma_f32_16x16x32_bf16 v[12:15], a[140:143], v[184:187], v[12:15]\n" \
"v_exp_f32_e64 v147, v147\n" \
"v_smfmac_f32_16x16x64_bf16 v[180:183], a[192:195], v[200:207], v228\n" \
"ds_read_b128 a[108:111], v210 offset:57344\n" \
"v_mfma_f32_16x16x32_bf16 v[152:155], a[64:67], a[48:51], v[172:175]\n" \
"v_exp_f32_e64 v148, v148\n" \
"v_mfma_f32_16x16x32_bf16 v[152:155], a[68:71], a[52:55], v[152:155]\n" \
"ds_read_b128 a[124:127], v210 offset:61440\n" \
"v_mfma_f32_16x16x32_bf16 v[152:155], a[72:75], a[56:59], v[152:155]\n" \
"v_exp_f32_e64 v149, v149\n" \
"v_mfma_f32_16x16x32_bf16 v[152:155], a[76:79], a[60:63], v[152:155]\n" \
"v_cvt_pk_bf16_f32 v216, v144, v145\n" \
"v_cvt_pk_bf16_f32 v217, v146, v147\n" \
"v_mfma_f32_16x16x32_bf16 v[156:159], a[92:95], a[60:63], v[172:175]\n" \
"v_exp_f32_e64 v150, v150\n" \
"v_mfma_f32_16x16x32_bf16 v[156:159], a[88:91], a[56:59], v[156:159]\n" \
"ds_read_b128 a[120:123], v209 offset:61440\n" \
"s_waitcnt lgkmcnt(6)\n" \
"v_mfma_f32_16x16x32_bf16 v[156:159], a[84:87], a[52:55], v[156:159]\n" \
"buffer_load_dwordx4 v214, %[srdk], s48 offen offset:3072 lds\n" \
"v_mfma_f32_16x16x32_bf16 v[156:159], a[80:83], a[48:51], v[156:159]\n" \
"v_exp_f32_e32 v151, v151\n" \
"v_mfma_f32_16x16x32_bf16 v[16:19], a[176:179], v[192:195], v[16:19]\n" \
"ds_read_b128 a[116:119], v208 offset:61440\n" \
"v_mfma_f32_16x16x32_bf16 v[16:19], a[144:147], v[184:187], v[16:19]\n" \
"v_exp_f32_e64 v152, v152\n" \
"v_mfma_f32_16x16x32_bf16 v[20:23], a[180:183], v[192:195], v[20:23]\n" \
"v_cvt_pk_bf16_f32 v218, v148, v149\n" \
"v_cvt_pk_bf16_f32 v219, v150, v151\n" \
"v_mfma_f32_16x16x32_bf16 v[20:23], a[148:151], v[184:187], v[20:23]\n" \
"v_exp_f32_e64 v153, v153\n" \
"v_mfma_f32_16x16x32_bf16 v[24:27], a[184:187], v[192:195], v[24:27]\n" \
"v_exp_f32_e64 v154, v154\n" \
"v_mfma_f32_16x16x32_bf16 v[24:27], a[152:155], v[184:187], v[24:27]\n" \
"ds_read_b128 a[112:115], %[kb0] offset:61440\n" \
"s_add_u32 s48, s48, 0x4000\n" \
"s_mov_b32 m0, s44\n" \
"s_addk_i32 s44, 0x400\n" \
"buffer_load_dwordx4 v229, s[64:67], s45 offen offset:0 lds\n" \
"s_add_u32 s45, s45, 229376\n" \
"v_mfma_f32_16x16x32_bf16 v[28:31], a[188:191], v[192:195], v[28:31]\n" \
"v_exp_f32_e64 v155, v155\n" \
"v_mfma_f32_16x16x32_bf16 v[28:31], a[156:159], v[184:187], v[28:31]\n" \
"s_waitcnt vmcnt(14) lgkmcnt(2)\n" \
"s_barrier\n" \
"v_mfma_f32_16x16x32_bf16 v[128:131], a[96:99], a[0:3], v[160:163]\n" \
"v_exp_f32_e64 v156, v156\n" \
"v_mfma_f32_16x16x32_bf16 v[128:131], a[100:103], a[4:7], v[128:131]\n" \
"s_add_u32 m0, %[m0b], 98304\n" \
"v_mfma_f32_16x16x32_bf16 v[128:131], a[104:107], a[8:11], v[128:131]\n" \
"v_exp_f32_e64 v157, v157\n" \
"v_mfma_f32_16x16x32_bf16 v[128:131], a[108:111], a[12:15], v[128:131]\n" \
"v_cvt_pk_bf16_f32 v220, v152, v153\n" \
"v_cvt_pk_bf16_f32 v221, v154, v155\n" \
"v_mfma_f32_16x16x32_bf16 v[132:135], a[124:127], a[12:15], v[160:163]\n" \
"v_exp_f32_e64 v158, v158\n" \
"v_mfma_f32_16x16x32_bf16 v[132:135], a[120:123], a[8:11], v[132:135]\n" \
"s_waitcnt lgkmcnt(0)\n" \
"v_mfma_f32_16x16x32_bf16 v[132:135], a[116:119], a[4:7], v[132:135]\n" \
"buffer_load_dwordx4 %[vd0], %[srdv], s48 offen offset:0 lds\n" \
"v_mfma_f32_16x16x32_bf16 v[132:135], a[112:115], a[0:3], v[132:135]\n" \
"v_exp_f32_e32 v159, v159\n" \
"v_mfma_f32_16x16x32_bf16 v[32:35], a[160:163], v[196:199], v[32:35]\n" \
"ds_read_b128 a[64:67], %[kb0] offset:0\n" \
"v_mfma_f32_16x16x32_bf16 v[32:35], a[128:131], v[188:191], v[32:35]\n" \
"v_exp_f32_e64 v128, v128\n" \
"v_mfma_f32_16x16x32_bf16 v[36:39], a[164:167], v[196:199], v[36:39]\n" \
"v_cvt_pk_bf16_f32 v222, v156, v157\n" \
"v_cvt_pk_bf16_f32 v223, v158, v159\n" \
"v_mfma_f32_16x16x32_bf16 v[36:39], a[132:135], v[188:191], v[36:39]\n" \
"v_exp_f32_e64 v129, v129\n" \
"v_mfma_f32_16x16x32_bf16 v[40:43], a[168:171], v[196:199], v[40:43]\n" \
"ds_read_b128 a[68:71], v208 offset:0\n" \
"v_mfma_f32_16x16x32_bf16 v[40:43], a[136:139], v[188:191], v[40:43]\n" \
"v_exp_f32_e64 v130, v130\n" \
"v_mfma_f32_16x16x32_bf16 v[44:47], a[172:175], v[196:199], v[44:47]\n" \
"ds_read_b128 a[72:75], v209 offset:0\n" \
"v_mfma_f32_16x16x32_bf16 v[44:47], a[140:143], v[188:191], v[44:47]\n" \
"v_exp_f32_e64 v131, v131\n" \
"v_smfmac_f32_16x16x64_bf16 v[176:179], a[192:195], v[184:191], v228\n" \
"s_cbranch_vccz Lpp7_%=\n" \
"buffer_store_dwordx4 a[196:199], v235, %[srdz], s63 offen offset:2048 sc1\n" \
"buffer_store_dwordx4 a[196:199], v226, %[srdz], s63 offen offset:2048 sc1\n" \
"Lpp7_%=:\n" \
"s_add_u32 s63, s63, 2560\n" \
"v_mfma_f32_16x16x32_bf16 v[136:139], a[96:99], a[16:19], v[164:167]\n" \
"v_exp_f32_e32 v132, v132\n" \
"v_mfma_f32_16x16x32_bf16 v[136:139], a[100:103], a[20:23], v[136:139]\n" \
"ds_read_b128 a[76:79], v210 offset:0\n" \
"v_mfma_f32_16x16x32_bf16 v[136:139], a[104:107], a[24:27], v[136:139]\n" \
"v_exp_f32_e64 v133, v133\n" \
"v_mfma_f32_16x16x32_bf16 v[136:139], a[108:111], a[28:31], v[136:139]\n" \
"v_cvt_pk_bf16_f32 v192, v128, v129\n" \
"v_cvt_pk_bf16_f32 v193, v130, v131\n" \
"v_mfma_f32_16x16x32_bf16 v[140:143], a[124:127], a[28:31], v[164:167]\n" \
"v_exp_f32_e64 v134, v134\n" \
"v_mfma_f32_16x16x32_bf16 v[140:143], a[120:123], a[24:27], v[140:143]\n" \
"ds_read_b128 a[92:95], v210 offset:4096\n" \
"v_mfma_f32_16x16x32_bf16 v[140:143], a[116:119], a[20:23], v[140:143]\n" \
"buffer_load_dwordx4 v215, %[srdv], s48 offen offset:1024 lds\n" \
"v_mfma_f32_16x16x32_bf16 v[140:143], a[112:115], a[16:19], v[140:143]\n" \
"v_exp_f32_e64 v135, v135\n" \
"v_mfma_f32_16x16x32_bf16 v[48:51], a[176:179], v[196:199], v[48:51]\n" \
"ds_read_b128 a[88:91], v209 offset:4096\n" \
"v_mfma_f32_16x16x32_bf16 v[48:51], a[144:147], v[188:191], v[48:51]\n" \
"v_exp_f32_e64 v136, v136\n" \
"v_mfma_f32_16x16x32_bf16 v[52:55], a[180:183], v[196:199], v[52:55]\n" \
"v_cvt_pk_bf16_f32 v194, v132, v133\n" \
"v_cvt_pk_bf16_f32 v195, v134, v135\n" \
"v_mfma_f32_16x16x32_bf16 v[52:55], a[148:151], v[188:191], v[52:55]\n" \
"v_exp_f32_e64 v137, v137\n" \
"v_mfma_f32_16x16x32_bf16 v[56:59], a[184:187], v[196:199], v[56:59]\n" \
"v_exp_f32_e64 v138, v138\n" \
"v_mfma_f32_16x16x32_bf16 v[56:59], a[152:155], v[188:191], v[56:59]\n" \
"ds_read_b128 a[84:87], v208 offset:4096\n" \
"v_mfma_f32_16x16x32_bf16 v[60:63], a[188:191], v[196:199], v[60:63]\n" \
"v_exp_f32_e64 v139, v139\n" \
"v_mfma_f32_16x16x32_bf16 v[60:63], a[156:159], v[188:191], v[60:63]\n" \
"s_cmp_lg_u32 %[wid], 2\n" \
"s_cbranch_scc1 LtB7_%=\n" \
"global_load_dword v237, v236, s[58:59]\n" \
"s_add_u32 s61, s48, 49152\n" \
"s_mov_b32 exec_hi, 0\n" \
"s_nop 0\n" \
"buffer_load_dword v237, v224, %[srdp], s61 offen\n" \
"s_mov_b32 exec_hi, -1\n" \
"s_nop 0\n" \
"LtB7_%=:\n" \
"v_mfma_f32_16x16x32_bf16 v[144:147], a[96:99], a[32:35], v[168:171]\n" \
"v_exp_f32_e64 v140, v140\n" \
"v_mfma_f32_16x16x32_bf16 v[144:147], a[100:103], a[36:39], v[144:147]\n" \
"ds_read_b128 a[80:83], %[kb0] offset:4096\n" \
"v_mfma_f32_16x16x32_bf16 v[144:147], a[104:107], a[40:43], v[144:147]\n" \
"v_exp_f32_e64 v141, v141\n" \
"v_mfma_f32_16x16x32_bf16 v[144:147], a[108:111], a[44:47], v[144:147]\n" \
"v_cvt_pk_bf16_f32 v196, v136, v137\n" \
"v_cvt_pk_bf16_f32 v197, v138, v139\n" \
"v_mfma_f32_16x16x32_bf16 v[148:151], a[124:127], a[44:47], v[168:171]\n" \
"v_exp_f32_e64 v142, v142\n" \
"v_mfma_f32_16x16x32_bf16 v[148:151], a[120:123], a[40:43], v[148:151]\n" \
"v_mfma_f32_16x16x32_bf16 v[148:151], a[116:119], a[36:39], v[148:151]\n" \
"buffer_load_dwordx4 %[vd0], %[srdv], s48 offen offset:2048 lds\n" \
"v_mfma_f32_16x16x32_bf16 v[148:151], a[112:115], a[32:35], v[148:151]\n" \
"v_exp_f32_e64 v143, v143\n" \
"v_mfma_f32_16x16x32_bf16 v[96:99], a[160:163], v[204:207], v[96:99]\n" \
".if . - adaln_body != 28672\n" \
".error \"size model\"\n" \
".endif\n" \
"Lk6_%=:\n" \
"s_cbranch_execz Lk7_%=\n" \
"s_nop 0\n" \
"ds_read_b64_tr_b16 a[160:161], %[vvb] offset:57344\n" \
"v_mfma_f32_16x16x32_bf16 v[96:99], a[128:131], v[220:223], v[96:99]\n" \
"v_exp_f32_e64 v144, v144\n" \
"v_mfma_f32_16x16x32_bf16 v[100:103], a[164:167], v[204:207], v[100:103]\n" \
"v_cvt_pk_bf16_f32 v198, v140, v141\n" \
"v_cvt_pk_bf16_f32 v199, v142, v143\n" \
"v_mfma_f32_16x16x32_bf16 v[100:103], a[132:135], v[220:223], v[100:103]\n" \
"v_exp_f32_e64 v145, v145\n" \
"v_mfma_f32_16x16x32_bf16 v[104:107], a[168:171], v[204:207], v[104:107]\n" \
"ds_read_b64_tr_b16 a[162:163], %[vvb] offset:61440\n" \
"ds_read_b64_tr_b16 a[164:165], v211 offset:57344\n" \
"v_mfma_f32_16x16x32_bf16 v[104:107], a[136:139], v[220:223], v[104:107]\n" \
"v_exp_f32_e64 v146, v146\n" \
"v_mfma_f32_16x16x32_bf16 v[108:111], a[172:175], v[204:207], v[108:111]\n" \
"ds_read_b64_tr_b16 a[166:167], v211 offset:61440\n" \
"v_mfma_f32_16x16x32_bf16 v[108:111], a[140:143], v[220:223], v[108:111]\n" \
"v_exp_f32_e64 v147, v147\n" \
"v_smfmac_f32_16x16x64_bf16 v[180:183], a[192:195], v[216:223], v228\n" \
"ds_read_b64_tr_b16 a[168:169], %[vvb] offset:57856\n" \
"v_mfma_f32_16x16x32_bf16 v[152:155], a[96:99], a[48:51], v[172:175]\n" \
"v_exp_f32_e64 v148, v148\n" \
"v_mfma_f32_16x16x32_bf16 v[152:155], a[100:103], a[52:55], v[152:155]\n" \
"ds_read_b64_tr_b16 a[170:171], %[vvb] offset:61952\n" \
"v_mfma_f32_16x16x32_bf16 v[152:155], a[104:107], a[56:59], v[152:155]\n" \
"v_exp_f32_e64 v149, v149\n" \
"v_mfma_f32_16x16x32_bf16 v[152:155], a[108:111], a[60:63], v[152:155]\n" \
"v_cvt_pk_bf16_f32 v200, v144, v145\n" \
"v_cvt_pk_bf16_f32 v201, v146, v147\n" \
"v_mfma_f32_16x16x32_bf16 v[156:159], a[124:127], a[60:63], v[172:175]\n" \
"v_exp_f32_e64 v150, v150\n" \
"v_mfma_f32_16x16x32_bf16 v[156:159], a[120:123], a[56:59], v[156:159]\n" \
"ds_read_b64_tr_b16 a[172:173], v211 offset:57856\n" \
"v_mfma_f32_16x16x32_bf16 v[156:159], a[116:119], a[52:55], v[156:159]\n" \
"buffer_load_dwordx4 v215, %[srdv], s48 offen offset:3072 lds\n" \
"v_mfma_f32_16x16x32_bf16 v[156:159], a[112:115], a[48:51], v[156:159]\n" \
"v_exp_f32_e64 v151, v151\n" \
"v_mfma_f32_16x16x32_bf16 v[112:115], a[176:179], v[204:207], v[112:115]\n" \
"ds_read_b64_tr_b16 a[174:175], v211 offset:61952\n" \
"ds_read_b64_tr_b16 a[176:177], %[vvb] offset:58368\n" \
"v_mfma_f32_16x16x32_bf16 v[112:115], a[144:147], v[220:223], v[112:115]\n" \
"v_exp_f32_e64 v152, v152\n" \
"v_mfma_f32_16x16x32_bf16 v[116:119], a[180:183], v[204:207], v[116:119]\n" \
"v_cvt_pk_bf16_f32 v202, v148, v149\n" \
"v_cvt_pk_bf16_f32 v203, v150, v151\n" \
"v_mfma_f32_16x16x32_bf16 v[116:119], a[148:151], v[220:223], v[116:119]\n" \
"v_exp_f32_e64 v153, v153\n" \
"v_mfma_f32_16x16x32_bf16 v[120:123], a[184:187], v[204:207], v[120:123]\n" \
"v_exp_f32_e64 v154, v154\n" \
"v_mfma_f32_16x16x32_bf16 v[120:123], a[152:155], v[220:223], v[120:123]\n" \
"ds_read_b64_tr_b16 a[178:179], %[vvb] offset:62464\n" \
"v_mfma_f32_16x16x32_bf16 v[124:127], a[188:191], v[204:207], v[124:127]\n" \
"v_exp_f32_e64 v155, v155\n" \
"v_mfma_f32_16x16x32_bf16 v[124:127], a[156:159], v[220:223], v[124:127]\n" \
"ds_read_b64_tr_b16 a[180:181], v211 offset:58368\n" \
"s_waitcnt lgkmcnt(13)\n" \
"v_mfma_f32_16x16x32_bf16 v[128:131], a[64:67], a[0:3], v[160:163]\n" \
"v_exp_f32_e32 v156, v156\n" \
"v_mfma_f32_16x16x32_bf16 v[128:131], a[68:71], a[4:7], v[128:131]\n" \
"ds_read_b64_tr_b16 a[182:183], v211 offset:62464\n" \
"s_add_u32 m0, %[m0b], 49152\n" \
"v_mfma_f32_16x16x32_bf16 v[128:131], a[72:75], a[8:11], v[128:131]\n" \
"v_exp_f32_e64 v157, v157\n" \
"v_mfma_f32_16x16x32_bf16 v[128:131], a[76:79], a[12:15], v[128:131]\n" \
"v_cvt_pk_bf16_f32 v204, v152, v153\n" \
"v_cvt_pk_bf16_f32 v205, v154, v155\n" \
"v_mfma_f32_16x16x32_bf16 v[132:135], a[92:95], a[12:15], v[160:163]\n" \
"v_exp_f32_e64 v158, v158\n" \
"v_mfma_f32_16x16x32_bf16 v[132:135], a[88:91], a[8:11], v[132:135]\n" \
"ds_read_b64_tr_b16 a[184:185], %[vvb] offset:58880\n" \
"s_waitcnt lgkmcnt(5)\n" \
"v_mfma_f32_16x16x32_bf16 v[132:135], a[84:87], a[4:7], v[132:135]\n" \
"buffer_load_dwordx4 %[kd0], %[srdk], s48 offen offset:0 lds\n" \
"v_mfma_f32_16x16x32_bf16 v[132:135], a[80:83], a[0:3], v[132:135]\n" \
"v_exp_f32_e32 v159, v159\n" \
"v_mfma_f32_16x16x32_bf16 v[64:67], a[128:131], v[216:219], v[64:67]\n" \
"ds_read_b64_tr_b16 a[186:187], %[vvb] offset:62976\n" \
"ds_read_b64_tr_b16 a[188:189], v211 offset:58880\n" \
"v_mfma_f32_16x16x32_bf16 v[64:67], a[160:163], v[200:203], v[64:67]\n" \
"v_exp_f32_e64 v128, v128\n" \
"v_mfma_f32_16x16x32_bf16 v[68:71], a[132:135], v[216:219], v[68:71]\n" \
"v_cvt_pk_bf16_f32 v206, v156, v157\n" \
"v_cvt_pk_bf16_f32 v207, v158, v159\n" \
"v_mfma_f32_16x16x32_bf16 v[68:71], a[164:167], v[200:203], v[68:71]\n" \
"v_exp_f32_e64 v129, v129\n" \
"v_mfma_f32_16x16x32_bf16 v[72:75], a[136:139], v[216:219], v[72:75]\n" \
"ds_read_b64_tr_b16 a[190:191], v211 offset:62976\n" \
"ds_read_b64_tr_b16 a[128:129], %[vvb] offset:0\n" \
"v_mfma_f32_16x16x32_bf16 v[72:75], a[168:171], v[200:203], v[72:75]\n" \
"v_exp_f32_e64 v130, v130\n" \
"v_mfma_f32_16x16x32_bf16 v[76:79], a[140:143], v[216:219], v[76:79]\n" \
"ds_read_b64_tr_b16 a[130:131], %[vvb] offset:4096\n" \
"ds_read_b64_tr_b16 a[132:133], v211 offset:0\n" \
"v_mfma_f32_16x16x32_bf16 v[76:79], a[172:175], v[200:203], v[76:79]\n" \
"v_exp_f32_e64 v131, v131\n" \
"v_smfmac_f32_16x16x64_bf16 v[176:179], a[192:195], v[192:199], v228\n" \
"ds_read_b64_tr_b16 a[134:135], v211 offset:4096\n" \
"ds_read_b64_tr_b16 a[136:137], %[vvb] offset:512\n" \
"v_mfma_f32_16x16x32_bf16 v[136:139], a[64:67], a[16:19], v[164:167]\n" \
"v_exp_f32_e64 v132, v132\n" \
"v_mfma_f32_16x16x32_bf16 v[136:139], a[68:71], a[20:23], v[136:139]\n" \
"ds_read_b64_tr_b16 a[138:139], %[vvb] offset:4608\n" \
"ds_read_b64_tr_b16 a[140:141], v211 offset:512\n" \
"v_mfma_f32_16x16x32_bf16 v[136:139], a[72:75], a[24:27], v[136:139]\n" \
"v_exp_f32_e64 v133, v133\n" \
"v_mfma_f32_16x16x32_bf16 v[136:139], a[76:79], a[28:31], v[136:139]\n" \
"v_cvt_pk_bf16_f32 v184, v128, v129\n" \
"v_cvt_pk_bf16_f32 v185, v130, v131\n" \
"v_mfma_f32_16x16x32_bf16 v[140:143], a[92:95], a[28:31], v[164:167]\n" \
"v_exp_f32_e64 v134, v134\n" \
"v_mfma_f32_16x16x32_bf16 v[140:143], a[88:91], a[24:27], v[140:143]\n" \
"ds_read_b64_tr_b16 a[142:143], v211 offset:4608\n" \
"s_waitcnt lgkmcnt(8)\n" \
"v_mfma_f32_16x16x32_bf16 v[140:143], a[84:87], a[20:23], v[140:143]\n" \
"buffer_load_dwordx4 v212, %[srdk], s48 offen offset:1024 lds\n" \
"v_mfma_f32_16x16x32_bf16 v[140:143], a[80:83], a[16:19], v[140:143]\n" \
"v_exp_f32_e32 v135, v135\n" \
"v_mfma_f32_16x16x32_bf16 v[80:83], a[144:147], v[216:219], v[80:83]\n" \
"ds_read_b64_tr_b16 a[144:145], %[vvb] offset:1024\n" \
"ds_read_b64_tr_b16 a[146:147], %[vvb] offset:5120\n" \
"v_mfma_f32_16x16x32_bf16 v[80:83], a[176:179], v[200:203], v[80:83]\n" \
"v_exp_f32_e64 v136, v136\n" \
"v_mfma_f32_16x16x32_bf16 v[84:87], a[148:151], v[216:219], v[84:87]\n" \
"v_cvt_pk_bf16_f32 v186, v132, v133\n" \
"v_cvt_pk_bf16_f32 v187, v134, v135\n" \
"v_mfma_f32_16x16x32_bf16 v[84:87], a[180:183], v[200:203], v[84:87]\n" \
"v_exp_f32_e64 v137, v137\n" \
"v_mfma_f32_16x16x32_bf16 v[88:91], a[152:155], v[216:219], v[88:91]\n" \
"v_exp_f32_e64 v138, v138\n" \
"v_mfma_f32_16x16x32_bf16 v[88:91], a[184:187], v[200:203], v[88:91]\n" \
"ds_read_b64_tr_b16 a[148:149], v211 offset:1024\n" \
"ds_read_b64_tr_b16 a[150:151], v211 offset:5120\n" \
"v_mfma_f32_16x16x32_bf16 v[92:95], a[156:159], v[216:219], v[92:95]\n" \
"v_exp_f32_e64 v139, v139\n" \
"v_mfma_f32_16x16x32_bf16 v[92:95], a[188:191], v[200:203], v[92:95]\n" \
"ds_read_b64_tr_b16 a[152:153], %[vvb] offset:1536\n" \
"s_cbranch_vccz Lpp8_%=\n" \
"buffer_store_dwordx4 a[196:199], v235, %[srdz], s62 offen offset:2048 sc1\n" \
"buffer_store_dwordx4 a[196:199], v226, %[srdz], s62 offen offset:2048 sc1\n" \
"Lpp8_%=:\n" \
"s_add_u32 s62, s62, 2560\n" \
"v_mfma_f32_16x16x32_bf16 v[144:147], a[64:67], a[32:35], v[168:171]\n" \
"v_exp_f32_e32 v140, v140\n" \
"v_mfma_f32_16x16x32_bf16 v[144:147], a[68:71], a[36:39], v[144:147]\n" \
"ds_read_b64_tr_b16 a[154:155], %[vvb] offset:5632\n" \
"ds_read_b64_tr_b16 a[156:157], v211 offset:1536\n" \
"v_mfma_f32_16x16x32_bf16 v[144:147], a[72:75], a[40:43], v[144:147]\n" \
"v_exp_f32_e64 v141, v141\n" \
"v_mfma_f32_16x16x32_bf16 v[144:147], a[76:79], a[44:47], v[144:147]\n" \
"v_cvt_pk_bf16_f32 v188, v136, v137\n" \
"v_cvt_pk_bf16_f32 v189, v138, v139\n" \
"v_mfma_f32_16x16x32_bf16 v[148:151], a[92:95], a[44:47], v[168:171]\n" \
"v_exp_f32_e64 v142, v142\n" \
"v_mfma_f32_16x16x32_bf16 v[148:151], a[88:91], a[40:43], v[148:151]\n" \
"ds_read_b64_tr_b16 a[158:159], v211 offset:5632\n" \
"s_waitcnt lgkmcnt(8)\n" \
"v_mfma_f32_16x16x32_bf16 v[148:151], a[84:87], a[36:39], v[148:151]\n" \
"buffer_load_dwordx4 v213, %[srdk], s48 offen offset:2048 lds\n" \
"v_mfma_f32_16x16x32_bf16 v[148:151], a[80:83], a[32:35], v[148:151]\n" \
"v_exp_f32_e32 v143, v143\n" \
"v_mfma_f32_16x16x32_bf16 v[0:3], a[160:163], v[192:195], v[0:3]\n" \
"ds_read_b128 a[96:99], %[kb0] offset:8192\n" \
"v_mfma_f32_16x16x32_bf16 v[0:3], a[128:131], v[184:187], v[0:3]\n" \
"v_exp_f32_e64 v144, v144\n" \
"v_mfma_f32_16x16x32_bf16 v[4:7], a[164:167], v[192:195], v[4:7]\n" \
"v_cvt_pk_bf16_f32 v190, v140, v141\n" \
"v_cvt_pk_bf16_f32 v191, v142, v143\n" \
"v_mfma_f32_16x16x32_bf16 v[4:7], a[132:135], v[184:187], v[4:7]\n" \
"v_exp_f32_e64 v145, v145\n" \
"v_mfma_f32_16x16x32_bf16 v[8:11], a[168:171], v[192:195], v[8:11]\n" \
"ds_read_b128 a[100:103], v208 offset:8192\n" \
"v_mfma_f32_16x16x32_bf16 v[8:11], a[136:139], v[184:187], v[8:11]\n" \
"v_exp_f32_e64 v146, v146\n" \
"v_mfma_f32_16x16x32_bf16 v[12:15], a[172:175], v[192:195], v[12:15]\n" \
"ds_read_b128 a[104:107], v209 offset:8192\n" \
"v_mfma_f32_16x16x32_bf16 v[12:15], a[140:143], v[184:187], v[12:15]\n" \
"v_exp_f32_e64 v147, v147\n" \
"v_smfmac_f32_16x16x64_bf16 v[180:183], a[192:195], v[200:207], v228\n" \
"ds_read_b128 a[108:111], v210 offset:8192\n" \
"v_mfma_f32_16x16x32_bf16 v[152:155], a[64:67], a[48:51], v[172:175]\n" \
"v_exp_f32_e64 v148, v148\n" \
"v_mfma_f32_16x16x32_bf16 v[152:155], a[68:71], a[52:55], v[152:155]\n" \
"ds_read_b128 a[124:127], v210 offset:12288\n" \
"v_mfma_f32_16x16x32_bf16 v[152:155], a[72:75], a[56:59], v[152:155]\n" \
"v_exp_f32_e64 v149, v149\n" \
"v_mfma_f32_16x16x32_bf16 v[152:155], a[76:79], a[60:63], v[152:155]\n" \
"v_cvt_pk_bf16_f32 v216, v144, v145\n" \
"v_cvt_pk_bf16_f32 v217, v146, v147\n" \
"v_mfma_f32_16x16x32_bf16 v[156:159], a[92:95], a[60:63], v[172:175]\n" \
"v_exp_f32_e64 v150, v150\n" \
"v_mfma_f32_16x16x32_bf16 v[156:159], a[88:91], a[56:59], v[156:159]\n" \
"ds_read_b128 a[120:123], v209 offset:12288\n" \
"s_waitcnt lgkmcnt(6)\n" \
"v_mfma_f32_16x16x32_bf16 v[156:159], a[84:87], a[52:55], v[156:159]\n" \
"buffer_load_dwordx4 v214, %[srdk], s48 offen offset:3072 lds\n" \
"v_mfma_f32_16x16x32_bf16 v[156:159], a[80:83], a[48:51], v[156:159]\n" \
"v_exp_f32_e32 v151, v151\n" \
"v_mfma_f32_16x16x32_bf16 v[16:19], a[176:179], v[192:195], v[16:19]\n" \
"ds_read_b128 a[116:119], v208 offset:12288\n" \
"v_mfma_f32_16x16x32_bf16 v[16:19], a[144:147], v[184:187], v[16:19]\n" \
"v_exp_f32_e64 v152, v152\n" \
"v_mfma_f32_16x16x32_bf16 v[20:23], a[180:183], v[192:195], v[20:23]\n" \
"v_cvt_pk_bf16_f32 v218, v148, v149\n" \
"v_cvt_pk_bf16_f32 v219, v150, v151\n" \
"v_mfma_f32_16x16x32_bf16 v[20:23], a[148:151], v[184:187], v[20:23]\n" \
"v_exp_f32_e64 v153, v153\n" \
"v_mfma_f32_16x16x32_bf16 v[24:27], a[184:187], v[192:195], v[24:27]\n" \
"v_exp_f32_e64 v154, v154\n" \
"v_mfma_f32_16x16x32_bf16 v[24:27], a[152:155], v[184:187], v[24:27]\n" \
"ds_read_b128 a[112:115], %[kb0] offset:12288\n" \
"s_add_u32 s48, s48, 0x4000\n" \
"v_mfma_f32_16x16x32_bf16 v[28:31], a[188:191], v[192:195], v[28:31]\n" \
"v_exp_f32_e64 v155, v155\n" \
"v_mfma_f32_16x16x32_bf16 v[28:31], a[156:159], v[184:187], v[28:31]\n" \
"s_waitcnt vmcnt(14) lgkmcnt(2)\n" \
"s_barrier\n" \
"v_mfma_f32_16x16x32_bf16 v[128:131], a[96:99], a[0:3], v[160:163]\n" \
"v_exp_f32_e64 v156, v156\n" \
"v_mfma_f32_16x16x32_bf16 v[128:131], a[100:103], a[4:7], v[128:131]\n" \
"s_add_u32 m0, %[m0b], 114688\n" \
"v_mfma_f32_16x16x32_bf16 v[128:131], a[104:107], a[8:11], v[128:131]\n" \
"v_exp_f32_e64 v157, v157\n" \
"v_mfma_f32_16x16x32_bf16 v[128:131], a[108:111], a[12:15], v[128:131]\n" \
"v_cvt_pk_bf16_f32 v220, v152, v153\n" \
"v_cvt_pk_bf16_f32 v221, v154, v155\n" \
"v_mfma_f32_16x16x32_bf16 v[132:135], a[124:127], a[12:15], v[160:163]\n" \
"v_exp_f32_e64 v158, v158\n" \
"v_mfma_f32_16x16x32_bf16 v[132:135], a[120:123], a[8:11], v[132:135]\n" \
"s_waitcnt lgkmcnt(0)\n" \
"v_mfma_f32_16x16x32_bf16 v[132:135], a[116:119], a[4:7], v[132:135]\n" \
"buffer_load_dwordx4 %[vd0], %[srdv], s48 offen offset:0 lds\n" \
"v_mfma_f32_16x16x32_bf16 v[132:135], a[112:115], a[0:3], v[132:135]\n" \
"v_exp_f32_e32 v159, v159\n" \
"v_mfma_f32_16x16x32_bf16 v[32:35], a[160:163], v[196:199], v[32:35]\n" \
"ds_read_b128 a[64:67], %[kb0] offset:16384\n" \
"v_mfma_f32_16x16x32_bf16 v[32:35], a[128:131], v[188:191], v[32:35]\n" \
"v_exp_f32_e64 v128, v128\n" \
"v_mfma_f32_16x16x32_bf16 v[36:39], a[164:167], v[196:199], v[36:39]\n" \
"v_cvt_pk_bf16_f32 v222, v156, v157\n" \
"v_cvt_pk_bf16_f32 v223, v158, v159\n" \
"v_mfma_f32_16x16x32_bf16 v[36:39], a[132:135], v[188:191], v[36:39]\n" \
"v_exp_f32_e64 v129, v129\n" \
"v_mfma_f32_16x16x32_bf16 v[40:43], a[168:171], v[196:199], v[40:43]\n" \
"ds_read_b128 a[68:71], v208 offset:16384\n" \
"v_mfma_f32_16x16x32_bf16 v[40:43], a[136:139], v[188:191], v[40:43]\n" \
"v_exp_f32_e64 v130, v130\n" \
"v_mfma_f32_16x16x32_bf16 v[44:47], a[172:175], v[196:199], v[44:47]\n" \
"ds_read_b128 a[72:75], v209 offset:16384\n" \
"v_mfma_f32_16x16x32_bf16 v[44:47], a[140:143], v[188:191], v[44:47]\n" \
"v_exp_f32_e64 v131, v131\n" \
"v_smfmac_f32_16x16x64_bf16 v[176:179], a[192:195], v[184:191], v228\n" \
"v_mfma_f32_16x16x32_bf16 v[136:139], a[96:99], a[16:19], v[164:167]\n" \
"v_exp_f32_e64 v132, v132\n" \
"v_mfma_f32_16x16x32_bf16 v[136:139], a[100:103], a[20:23], v[136:139]\n" \
"ds_read_b128 a[76:79], v210 offset:16384\n" \
"v_mfma_f32_16x16x32_bf16 v[136:139], a[104:107], a[24:27], v[136:139]\n" \
"v_exp_f32_e64 v133, v133\n" \
"v_mfma_f32_16x16x32_bf16 v[136:139], a[108:111], a[28:31], v[136:139]\n" \
"v_cvt_pk_bf16_f32 v192, v128, v129\n" \
"v_cvt_pk_bf16_f32 v193, v130, v131\n" \
"v_mfma_f32_16x16x32_bf16 v[140:143], a[124:127], a[28:31], v[164:167]\n" \
"v_exp_f32_e64 v134, v134\n" \
"v_mfma_f32_16x16x32_bf16 v[140:143], a[120:123], a[24:27], v[140:143]\n" \
"ds_read_b128 a[92:95], v210 offset:20480\n" \
"v_mfma_f32_16x16x32_bf16 v[140:143], a[116:119], a[20:23], v[140:143]\n" \
"buffer_load_dwordx4 v215, %[srdv], s48 offen offset:1024 lds\n" \
"v_mfma_f32_16x16x32_bf16 v[140:143], a[112:115], a[16:19], v[140:143]\n" \
"v_exp_f32_e64 v135, v135\n" \
"v_mfma_f32_16x16x32_bf16 v[48:51], a[176:179], v[196:199], v[48:51]\n" \
"ds_read_b128 a[88:91], v209 offset:20480\n" \
"v_mfma_f32_16x16x32_bf16 v[48:51], a[144:147], v[188:191], v[48:51]\n" \
"v_exp_f32_e64 v136, v136\n" \
"v_mfma_f32_16x16x32_bf16 v[52:55], a[180:183], v[196:199], v[52:55]\n" \
"v_cvt_pk_bf16_f32 v194, v132, v133\n" \
"v_cvt_pk_bf16_f32 v195, v134, v135\n" \
"v_mfma_f32_16x16x32_bf16 v[52:55], a[148:151], v[188:191], v[52:55]\n" \
"v_exp_f32_e64 v137, v137\n" \
"v_mfma_f32_16x16x32_bf16 v[56:59], a[184:187], v[196:199], v[56:59]\n" \
"v_exp_f32_e64 v138, v138\n" \
"v_mfma_f32_16x16x32_bf16 v[56:59], a[152:155], v[188:191], v[56:59]\n" \
"ds_read_b128 a[84:87], v208 offset:20480\n" \
"v_mfma_f32_16x16x32_bf16 v[60:63], a[188:191], v[196:199], v[60:63]\n" \
"v_exp_f32_e64 v139, v139\n" \
"v_mfma_f32_16x16x32_bf16 v[60:63], a[156:159], v[188:191], v[60:63]\n" \
"s_cmp_lg_u32 %[wid], 3\n" \
"s_cbranch_scc1 LtB1_%=\n" \
"global_load_dword v237, v236, s[58:59]\n" \
"s_add_u32 s61, s48, 49152\n" \
"s_mov_b32 exec_hi, 0\n" \
"s_nop 0\n" \
"buffer_load_dword v237, v224, %[srdp], s61 offen\n" \
"s_mov_b32 exec_hi, -1\n" \
"s_nop 0\n" \
"LtB1_%=:\n" \
"v_mfma_f32_16x16x32_bf16 v[144:147], a[96:99], a[32:35], v[168:171]\n" \
"v_exp_f32_e64 v140, v140\n" \
"v_mfma_f32_16x16x32_bf16 v[144:147], a[100:103], a[36:39], v[144:147]\n" \
"ds_read_b128 a[80:83], %[kb0] offset:20480\n" \
"v_mfma_f32_16x16x32_bf16 v[144:147], a[104:107], a[40:43], v[144:147]\n" \
"v_exp_f32_e64 v141, v141\n" \
"v_mfma_f32_16x16x32_bf16 v[144:147], a[108:111], a[44:47], v[144:147]\n" \
"v_cvt_pk_bf16_f32 v196, v136, v137\n" \
"v_cvt_pk_bf16_f32 v197, v138, v139\n" \
"v_mfma_f32_16x16x32_bf16 v[148:151], a[124:127], a[44:47], v[168:171]\n" \
"v_exp_f32_e64 v142, v142\n" \
"v_mfma_f32_16x16x32_bf16 v[148:151], a[120:123], a[40:43], v[148:151]\n" \
"v_mfma_f32_16x16x32_bf16 v[148:151], a[116:119], a[36:39], v[148:151]\n" \
"buffer_load_dwordx4 %[vd0], %[srdv], s48 offen offset:2048 lds\n" \
"v_mfma_f32_16x16x32_bf16 v[148:151], a[112:115], a[32:35], v[148:151]\n" \
"v_exp_f32_e64 v143, v143\n" \
"v_mfma_f32_16x16x32_bf16 v[96:99], a[160:163], v[204:207], v[96:99]\n" \
"ds_read_b64_tr_b16 a[160:161], %[vvb] offset:8192\n" \
"v_mfma_f32_16x16x32_bf16 v[96:99], a[128:131], v[220:223], v[96:99]\n" \
"v_exp_f32_e64 v144, v144\n" \
"v_mfma_f32_16x16x32_bf16 v[100:103], a[164:167], v[204:207], v[100:103]\n" \
"v_cvt_pk_bf16_f32 v198, v140, v141\n" \
"v_cvt_pk_bf16_f32 v199, v142, v143\n" \
"v_mfma_f32_16x16x32_bf16 v[100:103], a[132:135], v[220:223], v[100:103]\n" \
"v_exp_f32_e64 v145, v145\n" \
"v_mfma_f32_16x16x32_bf16 v[104:107], a[168:171], v[204:207], v[104:107]\n" \
"ds_read_b64_tr_b16 a[162:163], %[vvb] offset:12288\n" \
"ds_read_b64_tr_b16 a[164:165], v211 offset:8192\n" \
"v_mfma_f32_16x16x32_bf16 v[104:107], a[136:139], v[220:223], v[104:107]\n" \
"v_exp_f32_e64 v146, v146\n" \
"v_mfma_f32_16x16x32_bf16 v[108:111], a[172:175], v[204:207], v[108:111]\n" \
"ds_read_b64_tr_b16 a[166:167], v211 offset:12288\n" \
"v_mfma_f32_16x16x32_bf16 v[108:111], a[140:143], v[220:223], v[108:111]\n" \
"v_exp_f32_e64 v147, v147\n" \
"v_smfmac_f32_16x16x64_bf16 v[180:183], a[192:195], v[216:223], v228\n" \
"ds_read_b64_tr_b16 a[168:169], %[vvb] offset:8704\n" \
"v_mfma_f32_16x16x32_bf16 v[152:155], a[96:99], a[48:51], v[172:175]\n" \
"v_exp_f32_e64 v148, v148\n" \
"v_mfma_f32_16x16x32_bf16 v[152:155], a[100:103], a[52:55], v[152:155]\n" \
"ds_read_b64_tr_b16 a[170:171], %[vvb] offset:12800\n" \
"v_mfma_f32_16x16x32_bf16 v[152:155], a[104:107], a[56:59], v[152:155]\n" \
"v_exp_f32_e64 v149, v149\n" \
"v_mfma_f32_16x16x32_bf16 v[152:155], a[108:111], a[60:63], v[152:155]\n" \
"v_cvt_pk_bf16_f32 v200, v144, v145\n" \
"v_cvt_pk_bf16_f32 v201, v146, v147\n" \
"v_mfma_f32_16x16x32_bf16 v[156:159], a[124:127], a[60:63], v[172:175]\n" \
"v_exp_f32_e64 v150, v150\n" \
"v_mfma_f32_16x16x32_bf16 v[156:159], a[120:123], a[56:59], v[156:159]\n" \
"ds_read_b64_tr_b16 a[172:173], v211 offset:8704\n" \
"v_mfma_f32_16x16x32_bf16 v[156:159], a[116:119], a[52:55], v[156:159]\n" \
"buffer_load_dwordx4 v215, %[srdv], s48 offen offset:3072 lds\n" \
"v_mfma_f32_16x16x32_bf16 v[156:159], a[112:115], a[48:51], v[156:159]\n" \
"v_exp_f32_e64 v151, v151\n" \
"v_mfma_f32_16x16x32_bf16 v[112:115], a[176:179], v[204:207], v[112:115]\n" \
"ds_read_b64_tr_b16 a[174:175], v211 offset:12800\n" \
"ds_read_b64_tr_b16 a[176:177], %[vvb] offset:9216\n" \
"v_mfma_f32_16x16x32_bf16 v[112:115], a[144:147], v[220:223], v[112:115]\n" \
"v_exp_f32_e64 v152, v152\n" \
"v_mfma_f32_16x16x32_bf16 v[116:119], a[180:183], v[204:207], v[116:119]\n" \
"v_cvt_pk_bf16_f32 v202, v148, v149\n" \
"v_cvt_pk_bf16_f32 v203, v150, v151\n" \
"v_mfma_f32_16x16x32_bf16 v[116:119], a[148:151], v[220:223], v[116:119]\n" \
"v_exp_f32_e64 v153, v153\n" \
"v_mfma_f32_16x16x32_bf16 v[120:123], a[184:187], v[204:207], v[120:123]\n" \
"v_exp_f32_e64 v154, v154\n" \
"v_mfma_f32_16x16x32_bf16 v[120:123], a[152:155], v[220:223], v[120:123]\n" \
"ds_read_b64_tr_b16 a[178:179], %[vvb] offset:13312\n" \
"v_mfma_f32_16x16x32_bf16 v[124:127], a[188:191], v[204:207], v[124:127]\n" \
"v_exp_f32_e64 v155, v155\n" \
"v_mfma_f32_16x16x32_bf16 v[124:127], a[156:159], v[220:223], v[124:127]\n" \
"ds_read_b64_tr_b16 a[180:181], v211 offset:9216\n" \
"s_waitcnt lgkmcnt(13)\n" \
"s_cmp_lg_u32 s60, 7\n" \
"s_cbranch_scc1 LmB_%=\n" \
"s_nop 0\n" \
"v_readlane_b32 s48, %[tlo], 11\n" \
"v_readlane_b32 s58, %[tlo], 12\n" \
"v_mfma_f32_16x16x32_bf16 v[128:131], a[64:67], a[0:3], v[160:163]\n" \
"v_exp_f32_e64 v156, v156\n" \
"v_mfma_f32_16x16x32_bf16 v[128:131], a[68:71], a[4:7], v[128:131]\n" \
"ds_read_b64_tr_b16 a[182:183], v211 offset:13312\n" \
"s_add_u32 m0, %[m0b], 0\n" \
"s_mov_b32 s49, s48\n" \
"v_mfma_f32_16x16x32_bf16 v[128:131], a[72:75], a[8:11], v[128:131]\n" \
"v_exp_f32_e64 v157, v157\n" \
"v_mfma_f32_16x16x32_bf16 v[128:131], a[76:79], a[12:15], v[128:131]\n" \
"v_cvt_pk_bf16_f32 v204, v152, v153\n" \
"v_cvt_pk_bf16_f32 v205, v154, v155\n" \
"v_mfma_f32_16x16x32_bf16 v[132:135], a[92:95], a[12:15], v[160:163]\n" \
"v_exp_f32_e64 v158, v158\n" \
"v_mfma_f32_16x16x32_bf16 v[132:135], a[88:91], a[8:11], v[132:135]\n" \
"ds_read_b64_tr_b16 a[184:185], %[vvb] offset:9728\n" \
"s_lshl_b32 s49, s49, 18\n" \
"s_or_b32 s49, s49, s58\n" \
"v_accvgpr_write_b32 a228, v225\n" \
"s_waitcnt lgkmcnt(5)\n" \
"v_mfma_f32_16x16x32_bf16 v[132:135], a[84:87], a[4:7], v[132:135]\n" \
"buffer_load_dwordx4 %[wo0], %[srdt], s49 offen offset:0 lds\n" \
"v_mfma_f32_16x16x32_bf16 v[132:135], a[80:83], a[0:3], v[132:135]\n" \
"v_exp_f32_e32 v159, v159\n" \
"v_mfma_f32_16x16x32_bf16 v[64:67], a[128:131], v[216:219], v[64:67]\n" \
"ds_read_b64_tr_b16 a[186:187], %[vvb] offset:13824\n" \
"ds_read_b64_tr_b16 a[188:189], v211 offset:9728\n" \
"v_readlane_b32 s60, %[thi], 11\n" \
"v_mfma_f32_16x16x32_bf16 v[64:67], a[160:163], v[200:203], v[64:67]\n" \
"v_exp_f32_e64 v128, v128\n" \
"v_mfma_f32_16x16x32_bf16 v[68:71], a[132:135], v[216:219], v[68:71]\n" \
"v_cvt_pk_bf16_f32 v206, v156, v157\n" \
"v_cvt_pk_bf16_f32 v207, v158, v159\n" \
"v_mfma_f32_16x16x32_bf16 v[68:71], a[164:167], v[200:203], v[68:71]\n" \
"v_exp_f32_e64 v129, v129\n" \
"v_mfma_f32_16x16x32_bf16 v[72:75], a[136:139], v[216:219], v[72:75]\n" \
"ds_read_b64_tr_b16 a[190:191], v211 offset:13824\n" \
"ds_read_b64_tr_b16 a[128:129], %[vvb] offset:16384\n" \
"v_readlane_b32 s59, %[thi], 12\n" \
"v_mfma_f32_16x16x32_bf16 v[72:75], a[168:171], v[200:203], v[72:75]\n" \
"v_exp_f32_e64 v130, v130\n" \
"v_mfma_f32_16x16x32_bf16 v[76:79], a[140:143], v[216:219], v[76:79]\n" \
"ds_read_b64_tr_b16 a[130:131], %[vvb] offset:20480\n" \
"ds_read_b64_tr_b16 a[132:133], v211 offset:16384\n" \
"v_readlane_b32 s53, %[tlo], 13\n" \
"v_mfma_f32_16x16x32_bf16 v[76:79], a[172:175], v[200:203], v[76:79]\n" \
"v_exp_f32_e64 v131, v131\n" \
"v_smfmac_f32_16x16x64_bf16 v[176:179], a[192:195], v[192:199], v228\n" \
"ds_read_b64_tr_b16 a[134:135], v211 offset:20480\n" \
"ds_read_b64_tr_b16 a[136:137], %[vvb] offset:16896\n" \
"v_readlane_b32 s50, %[thi], 13\n" \
"v_mfma_f32_16x16x32_bf16 v[136:139], a[64:67], a[16:19], v[164:167]\n" \
"v_exp_f32_e64 v132, v132\n" \
"v_mfma_f32_16x16x32_bf16 v[136:139], a[68:71], a[20:23], v[136:139]\n" \
"ds_read_b64_tr_b16 a[138:139], %[vvb] offset:20992\n" \
"ds_read_b64_tr_b16 a[140:141], v211 offset:16896\n" \
"v_readlane_b32 s46, %[tlo], 14\n" \
"v_add_u32_e32 v212, 0x3c00, %[wo0]\n" \
"v_mfma_f32_16x16x32_bf16 v[136:139], a[72:75], a[24:27], v[136:139]\n" \
"v_exp_f32_e64 v133, v133\n" \
"v_mfma_f32_16x16x32_bf16 v[136:139], a[76:79], a[28:31], v[136:139]\n" \
"v_cvt_pk_bf16_f32 v184, v128, v129\n" \
"v_cvt_pk_bf16_f32 v185, v130, v131\n" \
"v_mfma_f32_16x16x32_bf16 v[140:143], a[92:95], a[28:31], v[164:167]\n" \
"v_exp_f32_e64 v134, v134\n" \
"v_mfma_f32_16x16x32_bf16 v[140:143], a[88:91], a[24:27], v[140:143]\n" \
"ds_read_b64_tr_b16 a[142:143], v211 offset:20992\n" \
"v_readlane_b32 s47, %[thi], 14\n" \
"v_add_u32_e32 v213, 0x7800, %[wo0]\n" \
"s_waitcnt lgkmcnt(8)\n" \
"v_mfma_f32_16x16x32_bf16 v[140:143], a[84:87], a[20:23], v[140:143]\n" \
"buffer_load_dwordx4 v212, %[srdt], s49 offen offset:1024 lds\n" \
"v_mfma_f32_16x16x32_bf16 v[140:143], a[80:83], a[16:19], v[140:143]\n" \
"v_exp_f32_e32 v135, v135\n" \
"v_mfma_f32_16x16x32_bf16 v[80:83], a[144:147], v[216:219], v[80:83]\n" \
"ds_read_b64_tr_b16 a[144:145], %[vvb] offset:17408\n" \
"ds_read_b64_tr_b16 a[146:147], %[vvb] offset:21504\n" \
"ds_read_b32 v230, %[rd0]\n" \
"v_mfma_f32_16x16x32_bf16 v[80:83], a[176:179], v[200:203], v[80:83]\n" \
"v_exp_f32_e64 v136, v136\n" \
"v_mfma_f32_16x16x32_bf16 v[84:87], a[148:151], v[216:219], v[84:87]\n" \
"v_cvt_pk_bf16_f32 v186, v132, v133\n" \
"v_cvt_pk_bf16_f32 v187, v134, v135\n" \
"v_mfma_f32_16x16x32_bf16 v[84:87], a[180:183], v[200:203], v[84:87]\n" \
"v_exp_f32_e64 v137, v137\n" \
"v_mfma_f32_16x16x32_bf16 v[88:91], a[152:155], v[216:219], v[88:91]\n" \
"v_exp_f32_e64 v138, v138\n" \
"v_mfma_f32_16x16x32_bf16 v[88:91], a[184:187], v[200:203], v[88:91]\n" \
"ds_read_b64_tr_b16 a[148:149], v211 offset:17408\n" \
"v_readlane_b32 s55, %[thi], 15\n" \
"ds_read_b64_tr_b16 a[150:151], v211 offset:21504\n" \
"v_lshrrev_b32_e64 v234, 5, v224\n" \
"v_mfma_f32_16x16x32_bf16 v[92:95], a[156:159], v[216:219], v[92:95]\n" \
"v_exp_f32_e64 v139, v139\n" \
"v_mfma_f32_16x16x32_bf16 v[92:95], a[188:191], v[200:203], v[92:95]\n" \
"ds_read_b64_tr_b16 a[152:153], %[vvb] offset:17920\n" \
"v_mfma_f32_16x16x32_bf16 v[144:147], a[64:67], a[32:35], v[168:171]\n" \
"v_exp_f32_e64 v140, v140\n" \
"v_mfma_f32_16x16x32_bf16 v[144:147], a[68:71], a[36:39], v[144:147]\n" \
"ds_read_b64_tr_b16 a[154:155], %[vvb] offset:22016\n" \
"ds_read_b64_tr_b16 a[156:157], v211 offset:17920\n" \
"s_waitcnt lgkmcnt(5)\n" \
"v_add_f32_dpp v230, v230, v230 row_ror:4 row_mask:0xf bank_mask:0xf\n" \
"v_mfma_f32_16x16x32_bf16 v[144:147], a[72:75], a[40:43], v[144:147]\n" \
"v_exp_f32_e32 v141, v141\n" \
"v_mfma_f32_16x16x32_bf16 v[144:147], a[76:79], a[44:47], v[144:147]\n" \
"v_cvt_pk_bf16_f32 v188, v136, v137\n" \
"v_cvt_pk_bf16_f32 v189, v138, v139\n" \
"v_mfma_f32_16x16x32_bf16 v[148:151], a[92:95], a[44:47], v[168:171]\n" \
"v_exp_f32_e64 v142, v142\n" \
"v_mfma_f32_16x16x32_bf16 v[148:151], a[88:91], a[40:43], v[148:151]\n" \
"ds_read_b64_tr_b16 a[158:159], v211 offset:22016\n" \
"v_add_f32_dpp v230, v230, v230 row_ror:8 row_mask:0xf bank_mask:0xf\n" \
"s_waitcnt lgkmcnt(9)\n" \
"v_mfma_f32_16x16x32_bf16 v[148:151], a[84:87], a[36:39], v[148:151]\n" \
"buffer_load_dwordx4 v213, %[srdt], s49 offen offset:2048 lds\n" \
"v_mfma_f32_16x16x32_bf16 v[148:151], a[80:83], a[32:35], v[148:151]\n" \
"v_exp_f32_e32 v143, v143\n" \
"v_mfma_f32_16x16x32_bf16 v[0:3], a[160:163], v[192:195], v[0:3]\n" \
"ds_read_b128 a[96:99], %[kb0] offset:24576\n" \
"v_cvt_pk_bf16_f32 v230, v230, v230\n" \
"v_mfma_f32_16x16x32_bf16 v[0:3], a[128:131], v[184:187], v[0:3]\n" \
"v_exp_f32_e64 v144, v144\n" \
"v_mfma_f32_16x16x32_bf16 v[4:7], a[164:167], v[192:195], v[4:7]\n" \
"v_cvt_pk_bf16_f32 v190, v140, v141\n" \
"v_cvt_pk_bf16_f32 v191, v142, v143\n" \
"v_mfma_f32_16x16x32_bf16 v[4:7], a[132:135], v[184:187], v[4:7]\n" \
"v_exp_f32_e64 v145, v145\n" \
"v_mfma_f32_16x16x32_bf16 v[8:11], a[168:171], v[192:195], v[8:11]\n" \
"ds_read_b128 a[100:103], v208 offset:24576\n" \
"v_lshlrev_b32_e64 v230, 16, v230\n" \
"v_mfma_f32_16x16x32_bf16 v[8:11], a[136:139], v[184:187], v[8:11]\n" \
"v_exp_f32_e64 v146, v146\n" \
"v_mfma_f32_16x16x32_bf16 v[12:15], a[172:175], v[192:195], v[12:15]\n" \
"ds_read_b128 a[104:107], v209 offset:24576\n" \
"v_add_f32_e64 v230, s55, v230\n" \
"v_add_u32_e32 v214, 0xb400, %[wo0]\n" \
"v_mfma_f32_16x16x32_bf16 v[12:15], a[140:143], v[184:187], v[12:15]\n" \
"v_exp_f32_e64 v147, v147\n" \
"v_smfmac_f32_16x16x64_bf16 v[180:183], a[192:195], v[200:207], v228\n" \
"ds_read_b128 a[108:111], v210 offset:24576\n" \
"v_cvt_pk_bf16_f32 v230, v230, v230\n" \
"v_readlane_b32 s55, %[tlo], 15\n" \
"v_mfma_f32_16x16x32_bf16 v[152:155], a[64:67], a[48:51], v[172:175]\n" \
"v_exp_f32_e64 v148, v148\n" \
"v_mfma_f32_16x16x32_bf16 v[152:155], a[68:71], a[52:55], v[152:155]\n" \
"ds_read_b128 a[124:127], v210 offset:28672\n" \
"v_lshl_or_b32 v230, v230, 16, 1\n" \
"v_mfma_f32_16x16x32_bf16 v[152:155], a[72:75], a[56:59], v[152:155]\n" \
"v_exp_f32_e64 v149, v149\n" \
"v_mfma_f32_16x16x32_bf16 v[152:155], a[76:79], a[60:63], v[152:155]\n" \
"v_cvt_pk_bf16_f32 v216, v144, v145\n" \
"v_cvt_pk_bf16_f32 v217, v146, v147\n" \
"v_mfma_f32_16x16x32_bf16 v[156:159], a[92:95], a[60:63], v[172:175]\n" \
"v_exp_f32_e64 v150, v150\n" \
"v_mfma_f32_16x16x32_bf16 v[156:159], a[88:91], a[56:59], v[156:159]\n" \
"ds_read_b128 a[120:123], v209 offset:28672\n" \
"s_cmp_lg_u32 s52, 0\n" \
"s_cselect_b32 s55, 0x80000000, s55\n" \
"s_mov_b64 exec, 15\n" \
"buffer_store_dword v230, v234, %[srdz], s55 offen sc1\n" \
"s_mov_b64 exec, -1\n" \
"s_waitcnt lgkmcnt(6)\n" \
"v_mfma_f32_16x16x32_bf16 v[156:159], a[84:87], a[52:55], v[156:159]\n" \
"buffer_load_dwordx4 v214, %[srdt], s49 offen offset:3072 lds\n" \
".if . - adaln_body != 32768\n" \
".error \"size model\"\n" \
".endif\n" \
"Lk7_%=:\n" \
"s_cbranch_execz Lk8_%=\n" \
"s_nop 0\n" \
"v_mfma_f32_16x16x32_bf16 v[156:159], a[80:83], a[48:51], v[156:159]\n" \
"v_exp_f32_e64 v151, v151\n" \
"v_mfma_f32_16x16x32_bf16 v[16:19], a[176:179], v[192:195], v[16:19]\n" \
"ds_read_b128 a[116:119], v208 offset:28672\n" \
"v_mfma_f32_16x16x32_bf16 v[16:19], a[144:147], v[184:187], v[16:19]\n" \
"v_exp_f32_e64 v152, v152\n" \
"v_mfma_f32_16x16x32_bf16 v[20:23], a[180:183], v[192:195], v[20:23]\n" \
"v_cvt_pk_bf16_f32 v218, v148, v149\n" \
"v_cvt_pk_bf16_f32 v219, v150, v151\n" \
"v_mfma_f32_16x16x32_bf16 v[20:23], a[148:151], v[184:187], v[20:23]\n" \
"v_exp_f32_e64 v153, v153\n" \
"v_mfma_f32_16x16x32_bf16 v[24:27], a[184:187], v[192:195], v[24:27]\n" \
"v_exp_f32_e64 v154, v154\n" \
"v_mfma_f32_16x16x32_bf16 v[24:27], a[152:155], v[184:187], v[24:27]\n" \
"ds_read_b128 a[112:115], %[kb0] offset:28672\n" \
"s_mov_b32 m0, s44\n" \
"s_addk_i32 s44, 0x400\n" \
"buffer_load_dwordx4 v229, s[64:67], s45 offen offset:0 lds\n" \
"s_add_u32 s45, s45, 229376\n" \
"v_mfma_f32_16x16x32_bf16 v[28:31], a[188:191], v[192:195], v[28:31]\n" \
"v_exp_f32_e64 v155, v155\n" \
"v_mfma_f32_16x16x32_bf16 v[28:31], a[156:159], v[184:187], v[28:31]\n" \
"s_waitcnt vmcnt(6) lgkmcnt(2)\n" \
"s_barrier\n" \
"v_mfma_f32_16x16x32_bf16 v[128:131], a[96:99], a[0:3], v[160:163]\n" \
"v_exp_f32_e64 v156, v156\n" \
"s_mov_b32 m0, s44\n" \
"s_addk_i32 s44, 0x400\n" \
"buffer_load_dwordx4 v229, s[64:67], s45 offen offset:0 lds\n" \
"s_add_u32 s45, s45, 229376\n" \
"v_readlane_b32 s64, %[tlo], 8\n" \
"v_readlane_b32 s65, %[thi], 8\n" \
"v_mfma_f32_16x16x32_bf16 v[128:131], a[100:103], a[4:7], v[128:131]\n" \
"v_mbcnt_lo_u32_b32 v231, -1, 0\n" \
"v_mfma_f32_16x16x32_bf16 v[128:131], a[104:107], a[8:11], v[128:131]\n" \
"v_exp_f32_e64 v157, v157\n" \
"v_mfma_f32_16x16x32_bf16 v[128:131], a[108:111], a[12:15], v[128:131]\n" \
"v_cvt_pk_bf16_f32 v220, v152, v153\n" \
"v_cvt_pk_bf16_f32 v221, v154, v155\n" \
"v_mfma_f32_16x16x32_bf16 v[132:135], a[124:127], a[12:15], v[160:163]\n" \
"v_exp_f32_e64 v158, v158\n" \
"v_mfma_f32_16x16x32_bf16 v[132:135], a[120:123], a[8:11], v[132:135]\n" \
"v_mbcnt_hi_u32_b32 v231, -1, v231\n" \
"s_waitcnt lgkmcnt(0)\n" \
"v_mfma_f32_16x16x32_bf16 v[132:135], a[116:119], a[4:7], v[132:135]\n" \
"s_add_u32 m0, %[m0b], 65536\n" \
"s_mov_b32 s61, 0\n" \
"buffer_load_dwordx4 %[vg0], s[64:67], s61 offen offset:0 lds\n" \
"v_mfma_f32_16x16x32_bf16 v[132:135], a[112:115], a[0:3], v[132:135]\n" \
"v_exp_f32_e64 v159, v159\n" \
"v_mfma_f32_16x16x32_bf16 v[32:35], a[160:163], v[196:199], v[32:35]\n" \
"ds_read_b128 a[64:67], %[kb0] offset:32768\n" \
"v_and_b32_e64 v226, 31, v231\n" \
"v_mfma_f32_16x16x32_bf16 v[32:35], a[128:131], v[188:191], v[32:35]\n" \
"v_exp_f32_e64 v128, v128\n" \
"v_mfma_f32_16x16x32_bf16 v[36:39], a[164:167], v[196:199], v[36:39]\n" \
"v_cvt_pk_bf16_f32 v222, v156, v157\n" \
"v_cvt_pk_bf16_f32 v223, v158, v159\n" \
"v_mfma_f32_16x16x32_bf16 v[36:39], a[132:135], v[188:191], v[36:39]\n" \
"v_exp_f32_e64 v129, v129\n" \
"v_mfma_f32_16x16x32_bf16 v[40:43], a[168:171], v[196:199], v[40:43]\n" \
"ds_read_b128 a[68:71], v208 offset:32768\n" \
"v_lshlrev_b32_e64 v226, 2, v226\n" \
"v_mfma_f32_16x16x32_bf16 v[40:43], a[136:139], v[188:191], v[40:43]\n" \
"v_exp_f32_e64 v130, v130\n" \
"v_mfma_f32_16x16x32_bf16 v[44:47], a[172:175], v[196:199], v[44:47]\n" \
"ds_read_b128 a[72:75], v209 offset:32768\n" \
"v_cmp_gt_u32_e32 vcc, 1, v231\n" \
"v_mfma_f32_16x16x32_bf16 v[44:47], a[140:143], v[188:191], v[44:47]\n" \
"v_exp_f32_e32 v131, v131\n" \
"v_smfmac_f32_16x16x64_bf16 v[176:179], a[192:195], v[184:191], v228\n" \
"v_mov_b32_e32 v225, 0x80000000\n" \
"v_mfma_f32_16x16x32_bf16 v[136:139], a[96:99], a[16:19], v[164:167]\n" \
"v_exp_f32_e64 v132, v132\n" \
"v_mfma_f32_16x16x32_bf16 v[136:139], a[100:103], a[20:23], v[136:139]\n" \
"ds_read_b128 a[76:79], v210 offset:32768\n" \
"v_mfma_f32_16x16x32_bf16 v[136:139], a[104:107], a[24:27], v[136:139]\n" \
"v_exp_f32_e64 v133, v133\n" \
"v_mfma_f32_16x16x32_bf16 v[136:139], a[108:111], a[28:31], v[136:139]\n" \
"v_cvt_pk_bf16_f32 v192, v128, v129\n" \
"v_cvt_pk_bf16_f32 v193, v130, v131\n" \
"v_mfma_f32_16x16x32_bf16 v[140:143], a[124:127], a[28:31], v[164:167]\n" \
"v_exp_f32_e64 v134, v134\n" \
"v_mfma_f32_16x16x32_bf16 v[140:143], a[120:123], a[24:27], v[140:143]\n" \
"ds_read_b128 a[92:95], v210 offset:36864\n" \
"v_cndmask_b32_e32 v225, v225, v224, vcc\n" \
"v_mfma_f32_16x16x32_bf16 v[140:143], a[116:119], a[20:23], v[140:143]\n" \
"s_add_u32 m0, %[m0b], 66560\n" \
"s_mov_b32 s61, 229376\n" \
"buffer_load_dwordx4 %[vg0], s[64:67], s61 offen offset:0 lds\n" \
"v_mfma_f32_16x16x32_bf16 v[140:143], a[112:115], a[16:19], v[140:143]\n" \
"v_exp_f32_e32 v135, v135\n" \
"v_mfma_f32_16x16x32_bf16 v[48:51], a[176:179], v[196:199], v[48:51]\n" \
"ds_read_b128 a[88:91], v209 offset:36864\n" \
"v_mfma_f32_16x16x32_bf16 v[48:51], a[144:147], v[188:191], v[48:51]\n" \
"v_exp_f32_e64 v136, v136\n" \
"v_mfma_f32_16x16x32_bf16 v[52:55], a[180:183], v[196:199], v[52:55]\n" \
"v_cvt_pk_bf16_f32 v194, v132, v133\n" \
"v_cvt_pk_bf16_f32 v195, v134, v135\n" \
"v_mfma_f32_16x16x32_bf16 v[52:55], a[148:151], v[188:191], v[52:55]\n" \
"v_exp_f32_e64 v137, v137\n" \
"v_mfma_f32_16x16x32_bf16 v[56:59], a[184:187], v[196:199], v[56:59]\n" \
"v_exp_f32_e64 v138, v138\n" \
"v_mfma_f32_16x16x32_bf16 v[56:59], a[152:155], v[188:191], v[56:59]\n" \
"ds_read_b128 a[84:87], v208 offset:36864\n" \
"v_mfma_f32_16x16x32_bf16 v[60:63], a[188:191], v[196:199], v[60:63]\n" \
"v_exp_f32_e64 v139, v139\n" \
"v_mfma_f32_16x16x32_bf16 v[60:63], a[156:159], v[188:191], v[60:63]\n" \
"v_mfma_f32_16x16x32_bf16 v[144:147], a[96:99], a[32:35], v[168:171]\n" \
"v_exp_f32_e64 v140, v140\n" \
"v_mfma_f32_16x16x32_bf16 v[144:147], a[100:103], a[36:39], v[144:147]\n" \
"ds_read_b128 a[80:83], %[kb0] offset:36864\n" \
"s_sub_u32 s61, s48, 0\n" \
"s_cmp_lt_u32 s61, 5\n" \
"s_cselect_b64 vcc, -1, 0\n" \
"v_mfma_f32_16x16x32_bf16 v[144:147], a[104:107], a[40:43], v[144:147]\n" \
"v_exp_f32_e32 v141, v141\n" \
"v_mfma_f32_16x16x32_bf16 v[144:147], a[108:111], a[44:47], v[144:147]\n" \
"v_cvt_pk_bf16_f32 v196, v136, v137\n" \
"v_cvt_pk_bf16_f32 v197, v138, v139\n" \
"v_mfma_f32_16x16x32_bf16 v[148:151], a[124:127], a[44:47], v[168:171]\n" \
"v_exp_f32_e64 v142, v142\n" \
"v_mfma_f32_16x16x32_bf16 v[148:151], a[120:123], a[40:43], v[148:151]\n" \
"v_readlane_b32 s55, %[tlo], 16\n" \
"v_mfma_f32_16x16x32_bf16 v[148:151], a[116:119], a[36:39], v[148:151]\n" \
"s_add_u32 m0, %[m0b], 67584\n" \
"s_mov_b32 s61, 458752\n" \
"buffer_load_dwordx4 %[vg0], s[64:67], s61 offen offset:0 lds\n" \
"v_mfma_f32_16x16x32_bf16 v[148:151], a[112:115], a[32:35], v[148:151]\n" \
"v_exp_f32_e64 v143, v143\n" \
"v_mfma_f32_16x16x32_bf16 v[96:99], a[160:163], v[204:207], v[96:99]\n" \
"ds_read_b64_tr_b16 a[160:161], %[vvb] offset:24576\n" \
"v_and_b32_e64 v232, 31, v231\n" \
"v_mfma_f32_16x16x32_bf16 v[96:99], a[128:131], v[220:223], v[96:99]\n" \
"v_exp_f32_e64 v144, v144\n" \
"v_mfma_f32_16x16x32_bf16 v[100:103], a[164:167], v[204:207], v[100:103]\n" \
"v_cvt_pk_bf16_f32 v198, v140, v141\n" \
"v_cvt_pk_bf16_f32 v199, v142, v143\n" \
"v_mfma_f32_16x16x32_bf16 v[100:103], a[132:135], v[220:223], v[100:103]\n" \
"v_exp_f32_e64 v145, v145\n" \
"v_mfma_f32_16x16x32_bf16 v[104:107], a[168:171], v[204:207], v[104:107]\n" \
"ds_read_b64_tr_b16 a[162:163], %[vvb] offset:28672\n" \
"ds_read_b64_tr_b16 a[164:165], v211 offset:24576\n" \
"v_lshlrev_b32_e64 v232, 9, v232\n" \
"v_mfma_f32_16x16x32_bf16 v[104:107], a[136:139], v[220:223], v[104:107]\n" \
"v_exp_f32_e64 v146, v146\n" \
"v_mfma_f32_16x16x32_bf16 v[108:111], a[172:175], v[204:207], v[108:111]\n" \
"ds_read_b64_tr_b16 a[166:167], v211 offset:28672\n" \
"v_and_b32_e64 v233, 32, v231\n" \
"v_mfma_f32_16x16x32_bf16 v[108:111], a[140:143], v[220:223], v[108:111]\n" \
"v_exp_f32_e64 v147, v147\n" \
"v_smfmac_f32_16x16x64_bf16 v[180:183], a[192:195], v[216:223], v228\n" \
"ds_read_b64_tr_b16 a[168:169], %[vvb] offset:25088\n" \
"v_mfma_f32_16x16x32_bf16 v[152:155], a[96:99], a[48:51], v[172:175]\n" \
"v_exp_f32_e64 v148, v148\n" \
"v_mfma_f32_16x16x32_bf16 v[152:155], a[100:103], a[52:55], v[152:155]\n" \
"ds_read_b64_tr_b16 a[170:171], %[vvb] offset:29184\n" \
"v_lshl_add_u32 v232, v233, 2, v232\n" \
"v_mfma_f32_16x16x32_bf16 v[152:155], a[104:107], a[56:59], v[152:155]\n" \
"v_exp_f32_e64 v149, v149\n" \
"v_mfma_f32_16x16x32_bf16 v[152:155], a[108:111], a[60:63], v[152:155]\n" \
"v_cvt_pk_bf16_f32 v200, v144, v145\n" \
"v_cvt_pk_bf16_f32 v201, v146, v147\n" \
"v_mfma_f32_16x16x32_bf16 v[156:159], a[124:127], a[60:63], v[172:175]\n" \
"v_exp_f32_e64 v150, v150\n" \
"v_mfma_f32_16x16x32_bf16 v[156:159], a[120:123], a[56:59], v[156:159]\n" \
"ds_read_b64_tr_b16 a[172:173], v211 offset:25088\n" \
"v_mov_b32_e64 v230, 1\n" \
"v_mfma_f32_16x16x32_bf16 v[156:159], a[116:119], a[52:55], v[156:159]\n" \
"s_add_u32 m0, %[m0b], 68608\n" \
"s_mov_b32 s61, 688128\n" \
"buffer_load_dwordx4 %[vg0], s[64:67], s61 offen offset:0 lds\n" \
"v_mfma_f32_16x16x32_bf16 v[156:159], a[112:115], a[48:51], v[156:159]\n" \
"v_exp_f32_e64 v151, v151\n" \
"v_mfma_f32_16x16x32_bf16 v[112:115], a[176:179], v[204:207], v[112:115]\n" \
"ds_read_b64_tr_b16 a[174:175], v211 offset:29184\n" \
"ds_read_b64_tr_b16 a[176:177], %[vvb] offset:25600\n" \
"v_mfma_f32_16x16x32_bf16 v[112:115], a[144:147], v[220:223], v[112:115]\n" \
"v_exp_f32_e64 v152, v152\n" \
"v_mfma_f32_16x16x32_bf16 v[116:119], a[180:183], v[204:207], v[116:119]\n" \
"v_cvt_pk_bf16_f32 v202, v148, v149\n" \
"v_cvt_pk_bf16_f32 v203, v150, v151\n" \
"v_mfma_f32_16x16x32_bf16 v[116:119], a[148:151], v[220:223], v[116:119]\n" \
"v_exp_f32_e64 v153, v153\n" \
"v_mfma_f32_16x16x32_bf16 v[120:123], a[184:187], v[204:207], v[120:123]\n" \
"v_exp_f32_e64 v154, v154\n" \
"v_mfma_f32_16x16x32_bf16 v[120:123], a[152:155], v[220:223], v[120:123]\n" \
"ds_read_b64_tr_b16 a[178:179], %[vvb] offset:29696\n" \
"buffer_store_dword v230, v232, %[srdz], s55 offen sc1\n" \
"v_mfma_f32_16x16x32_bf16 v[124:127], a[188:191], v[204:207], v[124:127]\n" \
"v_exp_f32_e64 v155, v155\n" \
"v_mfma_f32_16x16x32_bf16 v[124:127], a[156:159], v[220:223], v[124:127]\n" \
"ds_read_b64_tr_b16 a[180:181], v211 offset:25600\n" \
"s_waitcnt lgkmcnt(13)\n" \
"v_mfma_f32_16x16x32_bf16 v[128:131], a[64:67], a[0:3], v[160:163]\n" \
"v_exp_f32_e32 v156, v156\n" \
"v_mfma_f32_16x16x32_bf16 v[128:131], a[68:71], a[4:7], v[128:131]\n" \
"ds_read_b64_tr_b16 a[182:183], v211 offset:29696\n" \
"s_add_u32 m0, %[m0b], 16384\n" \
"s_mov_b32 s49, s48\n" \
"v_mfma_f32_16x16x32_bf16 v[128:131], a[72:75], a[8:11], v[128:131]\n" \
"v_exp_f32_e32 v157, v157\n" \
"v_mfma_f32_16x16x32_bf16 v[128:131], a[76:79], a[12:15], v[128:131]\n" \
"v_cvt_pk_bf16_f32 v204, v152, v153\n" \
"v_cvt_pk_bf16_f32 v205, v154, v155\n" \
"v_mfma_f32_16x16x32_bf16 v[132:135], a[92:95], a[12:15], v[160:163]\n" \
"v_exp_f32_e64 v158, v158\n" \
"v_mfma_f32_16x16x32_bf16 v[132:135], a[88:91], a[8:11], v[132:135]\n" \
"ds_read_b64_tr_b16 a[184:185], %[vvb] offset:26112\n" \
"s_lshl_b32 s49, s49, 18\n" \
"s_or_b32 s49, s49, s59\n" \
"s_waitcnt lgkmcnt(5)\n" \
"v_mfma_f32_16x16x32_bf16 v[132:135], a[84:87], a[4:7], v[132:135]\n" \
"buffer_load_dwordx4 %[wo0], %[srdt], s49 offen offset:0 lds\n" \
"v_mfma_f32_16x16x32_bf16 v[132:135], a[80:83], a[0:3], v[132:135]\n" \
"v_exp_f32_e32 v159, v159\n" \
"v_mfma_f32_16x16x32_bf16 v[64:67], a[128:131], v[216:219], v[64:67]\n" \
"ds_read_b64_tr_b16 a[186:187], %[vvb] offset:30208\n" \
"ds_read_b64_tr_b16 a[188:189], v211 offset:26112\n" \
"v_xor_b32_e32 v232, 0x80, %[vx0]\n" \
"v_mfma_f32_16x16x32_bf16 v[64:67], a[160:163], v[200:203], v[64:67]\n" \
"v_exp_f32_e64 v128, v128\n" \
"v_mfma_f32_16x16x32_bf16 v[68:71], a[132:135], v[216:219], v[68:71]\n" \
"v_cvt_pk_bf16_f32 v206, v156, v157\n" \
"v_cvt_pk_bf16_f32 v207, v158, v159\n" \
"v_mfma_f32_16x16x32_bf16 v[68:71], a[164:167], v[200:203], v[68:71]\n" \
"v_exp_f32_e64 v129, v129\n" \
"v_mfma_f32_16x16x32_bf16 v[72:75], a[136:139], v[216:219], v[72:75]\n" \
"ds_read_b64_tr_b16 a[190:191], v211 offset:30208\n" \
"ds_read_b64_tr_b16 a[128:129], %[vvb] offset:32768\n" \
"v_mfma_f32_16x16x32_bf16 v[72:75], a[168:171], v[200:203], v[72:75]\n" \
"v_exp_f32_e64 v130, v130\n" \
"v_mfma_f32_16x16x32_bf16 v[76:79], a[140:143], v[216:219], v[76:79]\n" \
"ds_read_b64_tr_b16 a[130:131], %[vvb] offset:36864\n" \
"ds_read_b64_tr_b16 a[132:133], v211 offset:32768\n" \
"v_mfma_f32_16x16x32_bf16 v[76:79], a[172:175], v[200:203], v[76:79]\n" \
"v_exp_f32_e64 v131, v131\n" \
"v_smfmac_f32_16x16x64_bf16 v[176:179], a[192:195], v[192:199], v228\n" \
"ds_read_b64_tr_b16 a[134:135], v211 offset:36864\n" \
"ds_read_b64_tr_b16 a[136:137], %[vvb] offset:33280\n" \
"global_load_dwordx2 a[240:241], %[vx0], %[px] offset:0\n" \
"v_mfma_f32_16x16x32_bf16 v[136:139], a[64:67], a[16:19], v[164:167]\n" \
"v_exp_f32_e64 v132, v132\n" \
"v_mfma_f32_16x16x32_bf16 v[136:139], a[68:71], a[20:23], v[136:139]\n" \
"ds_read_b64_tr_b16 a[138:139], %[vvb] offset:37376\n" \
"ds_read_b64_tr_b16 a[140:141], v211 offset:33280\n" \
"v_mfma_f32_16x16x32_bf16 v[136:139], a[72:75], a[24:27], v[136:139]\n" \
"v_exp_f32_e64 v133, v133\n" \
"v_mfma_f32_16x16x32_bf16 v[136:139], a[76:79], a[28:31], v[136:139]\n" \
"v_cvt_pk_bf16_f32 v184, v128, v129\n" \
"v_cvt_pk_bf16_f32 v185, v130, v131\n" \
"v_mfma_f32_16x16x32_bf16 v[140:143], a[92:95], a[28:31], v[164:167]\n" \
"v_exp_f32_e64 v134, v134\n" \
"v_mfma_f32_16x16x32_bf16 v[140:143], a[88:91], a[24:27], v[140:143]\n" \
"ds_read_b64_tr_b16 a[142:143], v211 offset:37376\n" \
"s_waitcnt lgkmcnt(8)\n" \
"v_mfma_f32_16x16x32_bf16 v[140:143], a[84:87], a[20:23], v[140:143]\n" \
"buffer_load_dwordx4 v212, %[srdt], s49 offen offset:1024 lds\n" \
"v_mfma_f32_16x16x32_bf16 v[140:143], a[80:83], a[16:19], v[140:143]\n" \
"v_exp_f32_e32 v135, v135\n" \
"v_mfma_f32_16x16x32_bf16 v[80:83], a[144:147], v[216:219], v[80:83]\n" \
"ds_read_b64_tr_b16 a[144:145], %[vvb] offset:33792\n" \
"ds_read_b64_tr_b16 a[146:147], %[vvb] offset:37888\n" \
"v_mfma_f32_16x16x32_bf16 v[80:83], a[176:179], v[200:203], v[80:83]\n" \
"v_exp_f32_e64 v136, v136\n" \
"v_mfma_f32_16x16x32_bf16 v[84:87], a[148:151], v[216:219], v[84:87]\n" \
"v_cvt_pk_bf16_f32 v186, v132, v133\n" \
"v_cvt_pk_bf16_f32 v187, v134, v135\n" \
"v_mfma_f32_16x16x32_bf16 v[84:87], a[180:183], v[200:203], v[84:87]\n" \
"v_exp_f32_e64 v137, v137\n" \
"v_mfma_f32_16x16x32_bf16 v[88:91], a[152:155], v[216:219], v[88:91]\n" \
"v_exp_f32_e64 v138, v138\n" \
"v_mfma_f32_16x16x32_bf16 v[88:91], a[184:187], v[200:203], v[88:91]\n" \
"ds_read_b64_tr_b16 a[148:149], v211 offset:33792\n" \
"ds_read_b64_tr_b16 a[150:151], v211 offset:37888\n" \
"v_mfma_f32_16x16x32_bf16 v[92:95], a[156:159], v[216:219], v[92:95]\n" \
"v_exp_f32_e64 v139, v139\n" \
"v_mfma_f32_16x16x32_bf16 v[92:95], a[188:191], v[200:203], v[92:95]\n" \
"ds_read_b64_tr_b16 a[152:153], %[vvb] offset:34304\n" \
"global_load_dwordx2 a[242:243], %[vx0], %[px] offset:1024\n" \
"v_mfma_f32_16x16x32_bf16 v[144:147], a[64:67], a[32:35], v[168:171]\n" \
"v_exp_f32_e64 v140, v140\n" \
"v_mfma_f32_16x16x32_bf16 v[144:147], a[68:71], a[36:39], v[144:147]\n" \
"ds_read_b64_tr_b16 a[154:155], %[vvb] offset:38400\n" \
"ds_read_b64_tr_b16 a[156:157], v211 offset:34304\n" \
"v_mfma_f32_16x16x32_bf16 v[144:147], a[72:75], a[40:43], v[144:147]\n" \
"v_exp_f32_e64 v141, v141\n" \
"v_mfma_f32_16x16x32_bf16 v[144:147], a[76:79], a[44:47], v[144:147]\n" \
"v_cvt_pk_bf16_f32 v188, v136, v137\n" \
"v_cvt_pk_bf16_f32 v189, v138, v139\n" \
"v_mfma_f32_16x16x32_bf16 v[148:151], a[92:95], a[44:47], v[168:171]\n" \
"v_exp_f32_e64 v142, v142\n" \
"v_mfma_f32_16x16x32_bf16 v[148:151], a[88:91], a[40:43], v[148:151]\n" \
"ds_read_b64_tr_b16 a[158:159], v211 offset:38400\n" \
"s_waitcnt lgkmcnt(8)\n" \
"v_mfma_f32_16x16x32_bf16 v[148:151], a[84:87], a[36:39], v[148:151]\n" \
"buffer_load_dwordx4 v213, %[srdt], s49 offen offset:2048 lds\n" \
"v_mfma_f32_16x16x32_bf16 v[148:151], a[80:83], a[32:35], v[148:151]\n" \
"v_exp_f32_e32 v143, v143\n" \
"v_mfma_f32_16x16x32_bf16 v[0:3], a[160:163], v[192:195], v[0:3]\n" \
"ds_read_b128 a[96:99], %[kb0] offset:40960\n" \
"v_mfma_f32_16x16x32_bf16 v[0:3], a[128:131], v[184:187], v[0:3]\n" \
"v_exp_f32_e64 v144, v144\n" \
"v_mfma_f32_16x16x32_bf16 v[4:7], a[164:167], v[192:195], v[4:7]\n" \
"v_cvt_pk_bf16_f32 v190, v140, v141\n" \
"v_cvt_pk_bf16_f32 v191, v142, v143\n" \
"v_mfma_f32_16x16x32_bf16 v[4:7], a[132:135], v[184:187], v[4:7]\n" \
"v_exp_f32_e64 v145, v145\n" \
"v_mfma_f32_16x16x32_bf16 v[8:11], a[168:171], v[192:195], v[8:11]\n" \
"ds_read_b128 a[100:103], v208 offset:40960\n" \
"v_mfma_f32_16x16x32_bf16 v[8:11], a[136:139], v[184:187], v[8:11]\n" \
"v_exp_f32_e64 v146, v146\n" \
"v_mfma_f32_16x16x32_bf16 v[12:15], a[172:175], v[192:195], v[12:15]\n" \
"ds_read_b128 a[104:107], v209 offset:40960\n" \
"v_mfma_f32_16x16x32_bf16 v[12:15], a[140:143], v[184:187], v[12:15]\n" \
"v_exp_f32_e64 v147, v147\n" \
"v_smfmac_f32_16x16x64_bf16 v[180:183], a[192:195], v[200:207], v228\n" \
"ds_read_b128 a[108:111], v210 offset:40960\n" \
"global_load_dwordx2 a[248:249], v232, %[px] offset:0\n" \
"v_mfma_f32_16x16x32_bf16 v[152:155], a[64:67], a[48:51], v[172:175]\n" \
"v_exp_f32_e64 v148, v148\n" \
"v_mfma_f32_16x16x32_bf16 v[152:155], a[68:71], a[52:55], v[152:155]\n" \
"ds_read_b128 a[124:127], v210 offset:45056\n" \
"v_mfma_f32_16x16x32_bf16 v[152:155], a[72:75], a[56:59], v[152:155]\n" \
"v_exp_f32_e64 v149, v149\n" \
"v_mfma_f32_16x16x32_bf16 v[152:155], a[76:79], a[60:63], v[152:155]\n" \
"v_cvt_pk_bf16_f32 v216, v144, v145\n" \
"v_cvt_pk_bf16_f32 v217, v146, v147\n" \
"v_mfma_f32_16x16x32_bf16 v[156:159], a[92:95], a[60:63], v[172:175]\n" \
"v_exp_f32_e64 v150, v150\n" \
"v_mfma_f32_16x16x32_bf16 v[156:159], a[88:91], a[56:59], v[156:159]\n" \
"ds_read_b128 a[120:123], v209 offset:45056\n" \
"s_waitcnt lgkmcnt(6)\n" \
"v_mfma_f32_16x16x32_bf16 v[156:159], a[84:87], a[52:55], v[156:159]\n" \
"buffer_load_dwordx4 v214, %[srdt], s49 offen offset:3072 lds\n" \
"v_mfma_f32_16x16x32_bf16 v[156:159], a[80:83], a[48:51], v[156:159]\n" \
"v_exp_f32_e32 v151, v151\n" \
"v_mfma_f32_16x16x32_bf16 v[16:19], a[176:179], v[192:195], v[16:19]\n" \
"ds_read_b128 a[116:119], v208 offset:45056\n" \
"v_mfma_f32_16x16x32_bf16 v[16:19], a[144:147], v[184:187], v[16:19]\n" \
"v_exp_f32_e64 v152, v152\n" \
"v_mfma_f32_16x16x32_bf16 v[20:23], a[180:183], v[192:195], v[20:23]\n" \
"v_cvt_pk_bf16_f32 v218, v148, v149\n" \
"v_cvt_pk_bf16_f32 v219, v150, v151\n" \
"v_mfma_f32_16x16x32_bf16 v[20:23], a[148:151], v[184:187], v[20:23]\n" \
"v_exp_f32_e64 v153, v153\n" \
"v_mfma_f32_16x16x32_bf16 v[24:27], a[184:187], v[192:195], v[24:27]\n" \
"v_exp_f32_e64 v154, v154\n" \
"v_mfma_f32_16x16x32_bf16 v[24:27], a[152:155], v[184:187], v[24:27]\n" \
"ds_read_b128 a[112:115], %[kb0] offset:45056\n" \
"global_load_dwordx2 a[250:251], v232, %[px] offset:1024\n" \
"v_mfma_f32_16x16x32_bf16 v[28:31], a[188:191], v[192:195], v[28:31]\n" \
"v_exp_f32_e64 v155, v155\n" \
"v_mfma_f32_16x16x32_bf16 v[28:31], a[156:159], v[184:187], v[28:31]\n" \
"s_waitcnt vmcnt(63) lgkmcnt(2)\n" \
"s_barrier\n" \
"v_mfma_f32_16x16x32_bf16 v[128:131], a[96:99], a[0:3], v[160:163]\n" \
"v_exp_f32_e64 v156, v156\n" \
"v_mfma_f32_16x16x32_bf16 v[128:131], a[100:103], a[4:7], v[128:131]\n" \
"v_lshrrev_b32_e64 v227, 5, v231\n" \
"v_mfma_f32_16x16x32_bf16 v[128:131], a[104:107], a[8:11], v[128:131]\n" \
"v_exp_f32_e64 v157, v157\n" \
"v_mfma_f32_16x16x32_bf16 v[128:131], a[108:111], a[12:15], v[128:131]\n" \
"v_cvt_pk_bf16_f32 v220, v152, v153\n" \
"v_cvt_pk_bf16_f32 v221, v154, v155\n" \
"v_mfma_f32_16x16x32_bf16 v[132:135], a[124:127], a[12:15], v[160:163]\n" \
"v_exp_f32_e64 v158, v158\n" \
"v_mfma_f32_16x16x32_bf16 v[132:135], a[120:123], a[8:11], v[132:135]\n" \
"v_lshlrev_b32_e32 v227, 10, v227\n" \
"s_waitcnt lgkmcnt(0)\n" \
"v_mfma_f32_16x16x32_bf16 v[132:135], a[116:119], a[4:7], v[132:135]\n" \
"s_add_u32 m0, %[m0b], 81920\n" \
"s_mov_b32 s61, 917504\n" \
"buffer_load_dwordx4 %[vg0], s[64:67], s61 offen offset:0 lds\n" \
"v_mfma_f32_16x16x32_bf16 v[132:135], a[112:115], a[0:3], v[132:135]\n" \
"v_exp_f32_e64 v159, v159\n" \
"v_mfma_f32_16x16x32_bf16 v[32:35], a[160:163], v[196:199], v[32:35]\n" \
"ds_read_b128 a[64:67], %[kb0] offset:49152\n" \
"v_add_u32_e32 v227, 0x10000, v227\n" \
"v_mfma_f32_16x16x32_bf16 v[32:35], a[128:131], v[188:191], v[32:35]\n" \
"v_exp_f32_e64 v128, v128\n" \
"v_mfma_f32_16x16x32_bf16 v[36:39], a[164:167], v[196:199], v[36:39]\n" \
"v_cvt_pk_bf16_f32 v222, v156, v157\n" \
"v_cvt_pk_bf16_f32 v223, v158, v159\n" \
"v_mfma_f32_16x16x32_bf16 v[36:39], a[132:135], v[188:191], v[36:39]\n" \
"v_exp_f32_e64 v129, v129\n" \
"v_mfma_f32_16x16x32_bf16 v[40:43], a[168:171], v[196:199], v[40:43]\n" \
"ds_read_b128 a[68:71], v208 offset:49152\n" \
"v_sub_u32_e64 v215, %[vvb], v227\n" \
"v_mfma_f32_16x16x32_bf16 v[40:43], a[136:139], v[188:191], v[40:43]\n" \
"v_exp_f32_e64 v130, v130\n" \
"v_mfma_f32_16x16x32_bf16 v[44:47], a[172:175], v[196:199], v[44:47]\n" \
"ds_read_b128 a[72:75], v209 offset:49152\n" \
"v_xor_b32_e64 v227, 32, v215\n" \
"v_mfma_f32_16x16x32_bf16 v[44:47], a[140:143], v[188:191], v[44:47]\n" \
"v_exp_f32_e64 v131, v131\n" \
"v_smfmac_f32_16x16x64_bf16 v[176:179], a[192:195], v[184:191], v228\n" \
"global_load_dwordx2 a[244:245], %[vx0], %[px] offset:64\n" \
"v_mfma_f32_16x16x32_bf16 v[136:139], a[96:99], a[16:19], v[164:167]\n" \
"v_exp_f32_e64 v132, v132\n" \
"v_mfma_f32_16x16x32_bf16 v[136:139], a[100:103], a[20:23], v[136:139]\n" \
"ds_read_b128 a[76:79], v210 offset:49152\n" \
"v_mfma_f32_16x16x32_bf16 v[136:139], a[104:107], a[24:27], v[136:139]\n" \
"v_exp_f32_e64 v133, v133\n" \
"v_mfma_f32_16x16x32_bf16 v[136:139], a[108:111], a[28:31], v[136:139]\n" \
"v_cvt_pk_bf16_f32 v192, v128, v129\n" \
"v_cvt_pk_bf16_f32 v193, v130, v131\n" \
"v_mfma_f32_16x16x32_bf16 v[140:143], a[124:127], a[28:31], v[164:167]\n" \
"v_exp_f32_e64 v134, v134\n" \
"v_mfma_f32_16x16x32_bf16 v[140:143], a[120:123], a[24:27], v[140:143]\n" \
"ds_read_b128 a[92:95], v210 offset:53248\n" \
"v_mfma_f32_16x16x32_bf16 v[140:143], a[116:119], a[20:23], v[140:143]\n" \
"s_add_u32 m0, %[m0b], 82944\n" \
"s_mov_b32 s61, 1146880\n" \
"buffer_load_dwordx4 %[vg0], s[64:67], s61 offen offset:0 lds\n" \
"v_mfma_f32_16x16x32_bf16 v[140:143], a[112:115], a[16:19], v[140:143]\n" \
"v_exp_f32_e64 v135, v135\n" \
"v_mfma_f32_16x16x32_bf16 v[48:51], a[176:179], v[196:199], v[48:51]\n" \
"ds_read_b128 a[88:91], v209 offset:53248\n" \
"v_mfma_f32_16x16x32_bf16 v[48:51], a[144:147], v[188:191], v[48:51]\n" \
"v_exp_f32_e64 v136, v136\n" \
"v_mfma_f32_16x16x32_bf16 v[52:55], a[180:183], v[196:199], v[52:55]\n" \
"v_cvt_pk_bf16_f32 v194, v132, v133\n" \
"v_cvt_pk_bf16_f32 v195, v134, v135\n" \
"v_mfma_f32_16x16x32_bf16 v[52:55], a[148:151], v[188:191], v[52:55]\n" \
"v_exp_f32_e64 v137, v137\n" \
"v_mfma_f32_16x16x32_bf16 v[56:59], a[184:187], v[196:199], v[56:59]\n" \
"v_exp_f32_e64 v138, v138\n" \
"v_mfma_f32_16x16x32_bf16 v[56:59], a[152:155], v[188:191], v[56:59]\n" \
"ds_read_b128 a[84:87], v208 offset:53248\n" \
"v_mfma_f32_16x16x32_bf16 v[60:63], a[188:191], v[196:199], v[60:63]\n" \
"v_exp_f32_e64 v139, v139\n" \
"v_mfma_f32_16x16x32_bf16 v[60:63], a[156:159], v[188:191], v[60:63]\n" \
"global_load_dwordx2 a[246:247], %[vx0], %[px] offset:1088\n" \
"v_mfma_f32_16x16x32_bf16 v[144:147], a[96:99], a[32:35], v[168:171]\n" \
"v_exp_f32_e64 v140, v140\n" \
"v_mfma_f32_16x16x32_bf16 v[144:147], a[100:103], a[36:39], v[144:147]\n" \
"ds_read_b128 a[80:83], %[kb0] offset:53248\n" \
"v_mfma_f32_16x16x32_bf16 v[144:147], a[104:107], a[40:43], v[144:147]\n" \
"v_exp_f32_e64 v141, v141\n" \
"v_mfma_f32_16x16x32_bf16 v[144:147], a[108:111], a[44:47], v[144:147]\n" \
"v_cvt_pk_bf16_f32 v196, v136, v137\n" \
"v_cvt_pk_bf16_f32 v197, v138, v139\n" \
"v_mfma_f32_16x16x32_bf16 v[148:151], a[124:127], a[44:47], v[168:171]\n" \
"v_exp_f32_e64 v142, v142\n" \
"v_mfma_f32_16x16x32_bf16 v[148:151], a[120:123], a[40:43], v[148:151]\n" \
"v_mfma_f32_16x16x32_bf16 v[148:151], a[116:119], a[36:39], v[148:151]\n" \
"s_add_u32 m0, %[m0b], 83968\n" \
"s_mov_b32 s61, 1376256\n" \
"buffer_load_dwordx4 %[vg0], s[64:67], s61 offen offset:0 lds\n" \
"v_mfma_f32_16x16x32_bf16 v[148:151], a[112:115], a[32:35], v[148:151]\n" \
"v_exp_f32_e64 v143, v143\n" \
"v_mfma_f32_16x16x32_bf16 v[96:99], a[160:163], v[204:207], v[96:99]\n" \
"ds_read_b64_tr_b16 a[160:161], %[vvb] offset:40960\n" \
"v_mfma_f32_16x16x32_bf16 v[96:99], a[128:131], v[220:223], v[96:99]\n" \
"v_exp_f32_e64 v144, v144\n" \
"v_mfma_f32_16x16x32_bf16 v[100:103], a[164:167], v[204:207], v[100:103]\n" \
"v_cvt_pk_bf16_f32 v198, v140, v141\n" \
"v_cvt_pk_bf16_f32 v199, v142, v143\n" \
"v_mfma_f32_16x16x32_bf16 v[100:103], a[132:135], v[220:223], v[100:103]\n" \
"v_exp_f32_e64 v145, v145\n" \
"v_mfma_f32_16x16x32_bf16 v[104:107], a[168:171], v[204:207], v[104:107]\n" \
"ds_read_b64_tr_b16 a[162:163], %[vvb] offset:45056\n" \
"ds_read_b64_tr_b16 a[164:165], v211 offset:40960\n" \
"v_mfma_f32_16x16x32_bf16 v[104:107], a[136:139], v[220:223], v[104:107]\n" \
"v_exp_f32_e64 v146, v146\n" \
"v_mfma_f32_16x16x32_bf16 v[108:111], a[172:175], v[204:207], v[108:111]\n" \
"ds_read_b64_tr_b16 a[166:167], v211 offset:45056\n" \
"v_mfma_f32_16x16x32_bf16 v[108:111], a[140:143], v[220:223], v[108:111]\n" \
"v_exp_f32_e64 v147, v147\n" \
"v_smfmac_f32_16x16x64_bf16 v[180:183], a[192:195], v[216:223], v228\n" \
"ds_read_b64_tr_b16 a[168:169], %[vvb] offset:41472\n" \
"global_load_dwordx2 a[252:253], v232, %[px] offset:64\n" \
"v_mfma_f32_16x16x32_bf16 v[152:155], a[96:99], a[48:51], v[172:175]\n" \
"v_exp_f32_e64 v148, v148\n" \
"v_mfma_f32_16x16x32_bf16 v[152:155], a[100:103], a[52:55], v[152:155]\n" \
"ds_read_b64_tr_b16 a[170:171], %[vvb] offset:45568\n" \
"v_mfma_f32_16x16x32_bf16 v[152:155], a[104:107], a[56:59], v[152:155]\n" \
"v_exp_f32_e64 v149, v149\n" \
"v_mfma_f32_16x16x32_bf16 v[152:155], a[108:111], a[60:63], v[152:155]\n" \
"v_cvt_pk_bf16_f32 v200, v144, v145\n" \
"v_cvt_pk_bf16_f32 v201, v146, v147\n" \
"v_mfma_f32_16x16x32_bf16 v[156:159], a[124:127], a[60:63], v[172:175]\n" \
"v_exp_f32_e64 v150, v150\n" \
"v_mfma_f32_16x16x32_bf16 v[156:159], a[120:123], a[56:59], v[156:159]\n" \
"ds_read_b64_tr_b16 a[172:173], v211 offset:41472\n" \
"v_mfma_f32_16x16x32_bf16 v[156:159], a[116:119], a[52:55], v[156:159]\n" \
"s_add_u32 m0, %[m0b], 84992\n" \
"s_mov_b32 s61, 1605632\n" \
"buffer_load_dwordx4 %[vg0], s[64:67], s61 offen offset:0 lds\n" \
"v_mfma_f32_16x16x32_bf16 v[156:159], a[112:115], a[48:51], v[156:159]\n" \
"v_exp_f32_e64 v151, v151\n" \
"v_mfma_f32_16x16x32_bf16 v[112:115], a[176:179], v[204:207], v[112:115]\n" \
"ds_read_b64_tr_b16 a[174:175], v211 offset:45568\n" \
"ds_read_b64_tr_b16 a[176:177], %[vvb] offset:41984\n" \
"v_mfma_f32_16x16x32_bf16 v[112:115], a[144:147], v[220:223], v[112:115]\n" \
"v_exp_f32_e64 v152, v152\n" \
"v_mfma_f32_16x16x32_bf16 v[116:119], a[180:183], v[204:207], v[116:119]\n" \
"v_cvt_pk_bf16_f32 v202, v148, v149\n" \
"v_cvt_pk_bf16_f32 v203, v150, v151\n" \
"v_mfma_f32_16x16x32_bf16 v[116:119], a[148:151], v[220:223], v[116:119]\n" \
"v_exp_f32_e64 v153, v153\n" \
"v_mfma_f32_16x16x32_bf16 v[120:123], a[184:187], v[204:207], v[120:123]\n" \
"v_exp_f32_e64 v154, v154\n" \
"v_mfma_f32_16x16x32_bf16 v[120:123], a[152:155], v[220:223], v[120:123]\n" \
"ds_read_b64_tr_b16 a[178:179], %[vvb] offset:46080\n" \
"global_load_dwordx2 a[254:255], v232, %[px] offset:1088\n" \
"v_mfma_f32_16x16x32_bf16 v[124:127], a[188:191], v[204:207], v[124:127]\n" \
"v_exp_f32_e64 v155, v155\n" \
"v_mfma_f32_16x16x32_bf16 v[124:127], a[156:159], v[220:223], v[124:127]\n" \
"ds_read_b64_tr_b16 a[180:181], v211 offset:41984\n" \
"s_waitcnt lgkmcnt(13)\n" \
"v_mfma_f32_16x16x32_bf16 v[128:131], a[64:67], a[0:3], v[160:163]\n" \
"v_exp_f32_e32 v156, v156\n" \
"v_mfma_f32_16x16x32_bf16 v[128:131], a[68:71], a[4:7], v[128:131]\n" \
"ds_read_b64_tr_b16 a[182:183], v211 offset:46080\n" \
"s_add_u32 m0, %[m0b], 32768\n" \
"s_cmp_gt_u32 s48, 0\n" \
"v_mfma_f32_16x16x32_bf16 v[128:131], a[72:75], a[8:11], v[128:131]\n" \
"v_exp_f32_e32 v157, v157\n" \
"v_mfma_f32_16x16x32_bf16 v[128:131], a[76:79], a[12:15], v[128:131]\n" \
"v_cvt_pk_bf16_f32 v204, v152, v153\n" \
"v_cvt_pk_bf16_f32 v205, v154, v155\n" \
"v_mfma_f32_16x16x32_bf16 v[132:135], a[92:95], a[12:15], v[160:163]\n" \
"v_exp_f32_e64 v158, v158\n" \
"v_mfma_f32_16x16x32_bf16 v[132:135], a[88:91], a[8:11], v[132:135]\n" \
"ds_read_b64_tr_b16 a[184:185], %[vvb] offset:42496\n" \
"s_cselect_b32 s49, 0, 1\n" \
"s_lshl_b32 s49, s49, 18\n" \
"s_or_b32 s49, s49, s58\n" \
"s_waitcnt lgkmcnt(5)\n" \
"v_mfma_f32_16x16x32_bf16 v[132:135], a[84:87], a[4:7], v[132:135]\n" \
".if . - adaln_body != 36864\n" \
".error \"size model\"\n" \
".endif\n" \
"Lk8_%=:\n" \
"s_cbranch_execz Lk9_%=\n" \
"s_nop 0\n" \
"buffer_load_dwordx4 %[wo0], %[srdt], s49 offen offset:0 lds\n" \
"v_mfma_f32_16x16x32_bf16 v[132:135], a[80:83], a[0:3], v[132:135]\n" \
"v_exp_f32_e64 v159, v159\n" \
"v_mfma_f32_16x16x32_bf16 v[64:67], a[128:131], v[216:219], v[64:67]\n" \
"ds_read_b64_tr_b16 a[186:187], %[vvb] offset:46592\n" \
"ds_read_b64_tr_b16 a[188:189], v211 offset:42496\n" \
"v_mfma_f32_16x16x32_bf16 v[64:67], a[160:163], v[200:203], v[64:67]\n" \
"v_exp_f32_e64 v128, v128\n" \
"v_mfma_f32_16x16x32_bf16 v[68:71], a[132:135], v[216:219], v[68:71]\n" \
"v_cvt_pk_bf16_f32 v206, v156, v157\n" \
"v_cvt_pk_bf16_f32 v207, v158, v159\n" \
"v_mfma_f32_16x16x32_bf16 v[68:71], a[164:167], v[200:203], v[68:71]\n" \
"v_exp_f32_e64 v129, v129\n" \
"v_mfma_f32_16x16x32_bf16 v[72:75], a[136:139], v[216:219], v[72:75]\n" \
"ds_read_b64_tr_b16 a[190:191], v211 offset:46592\n" \
"ds_read_b64_tr_b16 a[128:129], %[vvb] offset:49152\n" \
"v_mfma_f32_16x16x32_bf16 v[72:75], a[168:171], v[200:203], v[72:75]\n" \
"v_exp_f32_e64 v130, v130\n" \
"v_mfma_f32_16x16x32_bf16 v[76:79], a[140:143], v[216:219], v[76:79]\n" \
"ds_read_b64_tr_b16 a[130:131], %[vvb] offset:53248\n" \
"ds_read_b64_tr_b16 a[132:133], v211 offset:49152\n" \
"v_mfma_f32_16x16x32_bf16 v[76:79], a[172:175], v[200:203], v[76:79]\n" \
"v_exp_f32_e64 v131, v131\n" \
"v_smfmac_f32_16x16x64_bf16 v[176:179], a[192:195], v[192:199], v228\n" \
"ds_read_b64_tr_b16 a[134:135], v211 offset:53248\n" \
"ds_read_b64_tr_b16 a[136:137], %[vvb] offset:49664\n" \
"v_mfma_f32_16x16x32_bf16 v[136:139], a[64:67], a[16:19], v[164:167]\n" \
"v_exp_f32_e64 v132, v132\n" \
"v_mfma_f32_16x16x32_bf16 v[136:139], a[68:71], a[20:23], v[136:139]\n" \
"ds_read_b64_tr_b16 a[138:139], %[vvb] offset:53760\n" \
"ds_read_b64_tr_b16 a[140:141], v211 offset:49664\n" \
"v_mfma_f32_16x16x32_bf16 v[136:139], a[72:75], a[24:27], v[136:139]\n" \
"v_exp_f32_e64 v133, v133\n" \
"v_mfma_f32_16x16x32_bf16 v[136:139], a[76:79], a[28:31], v[136:139]\n" \
"v_cvt_pk_bf16_f32 v184, v128, v129\n" \
"v_cvt_pk_bf16_f32 v185, v130, v131\n" \
"v_mfma_f32_16x16x32_bf16 v[140:143], a[92:95], a[28:31], v[164:167]\n" \
"v_exp_f32_e64 v134, v134\n" \
"v_mfma_f32_16x16x32_bf16 v[140:143], a[88:91], a[24:27], v[140:143]\n" \
"ds_read_b64_tr_b16 a[142:143], v211 offset:53760\n" \
"s_waitcnt lgkmcnt(8)\n" \
"v_mfma_f32_16x16x32_bf16 v[140:143], a[84:87], a[20:23], v[140:143]\n" \
"buffer_load_dwordx4 v212, %[srdt], s49 offen offset:1024 lds\n" \
"v_mfma_f32_16x16x32_bf16 v[140:143], a[80:83], a[16:19], v[140:143]\n" \
"v_exp_f32_e32 v135, v135\n" \
"v_mfma_f32_16x16x32_bf16 v[80:83], a[144:147], v[216:219], v[80:83]\n" \
"ds_read_b64_tr_b16 a[144:145], %[vvb] offset:50176\n" \
"ds_read_b64_tr_b16 a[146:147], %[vvb] offset:54272\n" \
"v_mfma_f32_16x16x32_bf16 v[80:83], a[176:179], v[200:203], v[80:83]\n" \
"v_exp_f32_e64 v136, v136\n" \
"v_mfma_f32_16x16x32_bf16 v[84:87], a[148:151], v[216:219], v[84:87]\n" \
"v_cvt_pk_bf16_f32 v186, v132, v133\n" \
"v_cvt_pk_bf16_f32 v187, v134, v135\n" \
"v_mfma_f32_16x16x32_bf16 v[84:87], a[180:183], v[200:203], v[84:87]\n" \
"v_exp_f32_e64 v137, v137\n" \
"v_mfma_f32_16x16x32_bf16 v[88:91], a[152:155], v[216:219], v[88:91]\n" \
"v_exp_f32_e64 v138, v138\n" \
"v_mfma_f32_16x16x32_bf16 v[88:91], a[184:187], v[200:203], v[88:91]\n" \
"ds_read_b64_tr_b16 a[148:149], v211 offset:50176\n" \
"ds_read_b64_tr_b16 a[150:151], v211 offset:54272\n" \
"v_readlane_b32 s64, %[tlo], 10\n" \
"v_mfma_f32_16x16x32_bf16 v[92:95], a[156:159], v[216:219], v[92:95]\n" \
"v_exp_f32_e64 v139, v139\n" \
"v_mfma_f32_16x16x32_bf16 v[92:95], a[188:191], v[200:203], v[92:95]\n" \
"ds_read_b64_tr_b16 a[152:153], %[vvb] offset:50688\n" \
"v_readlane_b32 s65, %[thi], 10\n" \
"v_mfma_f32_16x16x32_bf16 v[144:147], a[64:67], a[32:35], v[168:171]\n" \
"v_exp_f32_e64 v140, v140\n" \
"v_mfma_f32_16x16x32_bf16 v[144:147], a[68:71], a[36:39], v[144:147]\n" \
"ds_read_b64_tr_b16 a[154:155], %[vvb] offset:54784\n" \
"ds_read_b64_tr_b16 a[156:157], v211 offset:50688\n" \
"v_mfma_f32_16x16x32_bf16 v[144:147], a[72:75], a[40:43], v[144:147]\n" \
"v_exp_f32_e64 v141, v141\n" \
"v_mfma_f32_16x16x32_bf16 v[144:147], a[76:79], a[44:47], v[144:147]\n" \
"v_cvt_pk_bf16_f32 v188, v136, v137\n" \
"v_cvt_pk_bf16_f32 v189, v138, v139\n" \
"v_mfma_f32_16x16x32_bf16 v[148:151], a[92:95], a[44:47], v[168:171]\n" \
"v_exp_f32_e64 v142, v142\n" \
"v_mfma_f32_16x16x32_bf16 v[148:151], a[88:91], a[40:43], v[148:151]\n" \
"ds_read_b64_tr_b16 a[158:159], v211 offset:54784\n" \
"s_waitcnt lgkmcnt(8)\n" \
"v_mfma_f32_16x16x32_bf16 v[148:151], a[84:87], a[36:39], v[148:151]\n" \
"buffer_load_dwordx4 v213, %[srdt], s49 offen offset:2048 lds\n" \
"v_max3_f32 v232, v33, v38, v43\n" \
"v_mfma_f32_16x16x32_bf16 v[148:151], a[80:83], a[32:35], v[148:151]\n" \
"v_exp_f32_e32 v143, v143\n" \
"v_mfma_f32_16x16x32_bf16 v[0:3], a[160:163], v[192:195], v[0:3]\n" \
"ds_read_b128 a[96:99], %[kb0] offset:57344\n" \
"v_min3_f32 v233, v33, v38, v43\n" \
"v_mfma_f32_16x16x32_bf16 v[0:3], a[128:131], v[184:187], v[0:3]\n" \
"v_exp_f32_e64 v144, v144\n" \
"v_mfma_f32_16x16x32_bf16 v[4:7], a[164:167], v[192:195], v[4:7]\n" \
"v_cvt_pk_bf16_f32 v190, v140, v141\n" \
"v_cvt_pk_bf16_f32 v191, v142, v143\n" \
"v_mfma_f32_16x16x32_bf16 v[4:7], a[132:135], v[184:187], v[4:7]\n" \
"v_exp_f32_e64 v145, v145\n" \
"v_mfma_f32_16x16x32_bf16 v[8:11], a[168:171], v[192:195], v[8:11]\n" \
"ds_read_b128 a[100:103], v208 offset:57344\n" \
"v_max3_f32 v232, v232, v48, v53\n" \
"v_mfma_f32_16x16x32_bf16 v[8:11], a[136:139], v[184:187], v[8:11]\n" \
"v_exp_f32_e64 v146, v146\n" \
"v_mfma_f32_16x16x32_bf16 v[12:15], a[172:175], v[192:195], v[12:15]\n" \
"ds_read_b128 a[104:107], v209 offset:57344\n" \
"v_min3_f32 v233, v233, v48, v53\n" \
"v_mfma_f32_16x16x32_bf16 v[12:15], a[140:143], v[184:187], v[12:15]\n" \
"v_exp_f32_e64 v147, v147\n" \
"v_smfmac_f32_16x16x64_bf16 v[180:183], a[192:195], v[200:207], v228\n" \
"ds_read_b128 a[108:111], v210 offset:57344\n" \
"buffer_load_dword v229, v226, %[srdz], s50 offen sc1\n" \
"v_mfma_f32_16x16x32_bf16 v[152:155], a[64:67], a[48:51], v[172:175]\n" \
"v_exp_f32_e64 v148, v148\n" \
"v_mfma_f32_16x16x32_bf16 v[152:155], a[68:71], a[52:55], v[152:155]\n" \
"ds_read_b128 a[124:127], v210 offset:61440\n" \
"v_sub_f32_e64 v234, v232, v233\n" \
"v_mfma_f32_16x16x32_bf16 v[152:155], a[72:75], a[56:59], v[152:155]\n" \
"v_exp_f32_e64 v149, v149\n" \
"v_mfma_f32_16x16x32_bf16 v[152:155], a[76:79], a[60:63], v[152:155]\n" \
"v_cvt_pk_bf16_f32 v216, v144, v145\n" \
"v_cvt_pk_bf16_f32 v217, v146, v147\n" \
"v_mfma_f32_16x16x32_bf16 v[156:159], a[92:95], a[60:63], v[172:175]\n" \
"v_exp_f32_e64 v150, v150\n" \
"v_mfma_f32_16x16x32_bf16 v[156:159], a[88:91], a[56:59], v[156:159]\n" \
"ds_read_b128 a[120:123], v209 offset:61440\n" \
"s_waitcnt lgkmcnt(6)\n" \
"v_mfma_f32_16x16x32_bf16 v[156:159], a[84:87], a[52:55], v[156:159]\n" \
"buffer_load_dwordx4 v214, %[srdt], s49 offen offset:3072 lds\n" \
"v_mfma_f32_16x16x32_bf16 v[156:159], a[80:83], a[48:51], v[156:159]\n" \
"v_exp_f32_e32 v151, v151\n" \
"v_mfma_f32_16x16x32_bf16 v[16:19], a[176:179], v[192:195], v[16:19]\n" \
"ds_read_b128 a[116:119], v208 offset:61440\n" \
"v_mfma_f32_16x16x32_bf16 v[16:19], a[144:147], v[184:187], v[16:19]\n" \
"v_exp_f32_e64 v152, v152\n" \
"v_mfma_f32_16x16x32_bf16 v[20:23], a[180:183], v[192:195], v[20:23]\n" \
"v_cvt_pk_bf16_f32 v218, v148, v149\n" \
"v_cvt_pk_bf16_f32 v219, v150, v151\n" \
"v_mfma_f32_16x16x32_bf16 v[20:23], a[148:151], v[184:187], v[20:23]\n" \
"v_exp_f32_e64 v153, v153\n" \
"v_mfma_f32_16x16x32_bf16 v[24:27], a[184:187], v[192:195], v[24:27]\n" \
"v_exp_f32_e64 v154, v154\n" \
"v_mfma_f32_16x16x32_bf16 v[24:27], a[152:155], v[184:187], v[24:27]\n" \
"ds_read_b128 a[112:115], %[kb0] offset:61440\n" \
"v_mfma_f32_16x16x32_bf16 v[28:31], a[188:191], v[192:195], v[28:31]\n" \
"v_exp_f32_e64 v155, v155\n" \
"v_mfma_f32_16x16x32_bf16 v[28:31], a[156:159], v[184:187], v[28:31]\n" \
"s_waitcnt vmcnt(28) lgkmcnt(2)\n" \
"s_barrier\n" \
"v_mfma_f32_16x16x32_bf16 v[128:131], a[96:99], a[0:3], v[160:163]\n" \
"v_exp_f32_e64 v156, v156\n" \
"v_mfma_f32_16x16x32_bf16 v[128:131], a[100:103], a[4:7], v[128:131]\n" \
"s_add_u32 m0, %[m0b], 98304\n" \
"s_cmp_gt_u32 s48, 0\n" \
"v_mfma_f32_16x16x32_bf16 v[128:131], a[104:107], a[8:11], v[128:131]\n" \
"v_exp_f32_e32 v157, v157\n" \
"v_mfma_f32_16x16x32_bf16 v[128:131], a[108:111], a[12:15], v[128:131]\n" \
"v_cvt_pk_bf16_f32 v220, v152, v153\n" \
"v_cvt_pk_bf16_f32 v221, v154, v155\n" \
"v_mfma_f32_16x16x32_bf16 v[132:135], a[124:127], a[12:15], v[160:163]\n" \
"v_exp_f32_e64 v158, v158\n" \
"v_mfma_f32_16x16x32_bf16 v[132:135], a[120:123], a[8:11], v[132:135]\n" \
"s_cselect_b32 s49, 0, 1\n" \
"s_lshl_b32 s49, s49, 18\n" \
"s_or_b32 s49, s49, s59\n" \
"v_max3_f32 v232, v97, v102, v107\n" \
"s_waitcnt lgkmcnt(0)\n" \
"v_mfma_f32_16x16x32_bf16 v[132:135], a[116:119], a[4:7], v[132:135]\n" \
"buffer_load_dwordx4 %[wo0], %[srdt], s49 offen offset:0 lds\n" \
"v_mfma_f32_16x16x32_bf16 v[132:135], a[112:115], a[0:3], v[132:135]\n" \
"v_exp_f32_e64 v159, v159\n" \
"v_mfma_f32_16x16x32_bf16 v[32:35], a[160:163], v[196:199], v[32:35]\n" \
"ds_read_b64_tr_b16 a[196:197], v215 offset:0\n" \
"v_min3_f32 v233, v97, v102, v107\n" \
"v_mfma_f32_16x16x32_bf16 v[32:35], a[128:131], v[188:191], v[32:35]\n" \
"v_exp_f32_e64 v128, v128\n" \
"v_mfma_f32_16x16x32_bf16 v[36:39], a[164:167], v[196:199], v[36:39]\n" \
"v_cvt_pk_bf16_f32 v222, v156, v157\n" \
"v_cvt_pk_bf16_f32 v223, v158, v159\n" \
"v_mfma_f32_16x16x32_bf16 v[36:39], a[132:135], v[188:191], v[36:39]\n" \
"v_exp_f32_e64 v129, v129\n" \
"v_mfma_f32_16x16x32_bf16 v[40:43], a[168:171], v[196:199], v[40:43]\n" \
"ds_read_b64_tr_b16 a[198:199], v215 offset:2048\n" \
"v_max3_f32 v232, v232, v112, v117\n" \
"v_mfma_f32_16x16x32_bf16 v[40:43], a[136:139], v[188:191], v[40:43]\n" \
"v_exp_f32_e64 v130, v130\n" \
"v_mfma_f32_16x16x32_bf16 v[44:47], a[172:175], v[196:199], v[44:47]\n" \
"ds_read_b64_tr_b16 a[200:201], v215 offset:4096\n" \
"v_min3_f32 v233, v233, v112, v117\n" \
"v_mfma_f32_16x16x32_bf16 v[44:47], a[140:143], v[188:191], v[44:47]\n" \
"v_exp_f32_e64 v131, v131\n" \
"v_smfmac_f32_16x16x64_bf16 v[176:179], a[192:195], v[184:191], v228\n" \
"v_sub_f32_e32 v232, v232, v233\n" \
"v_min_f32_e32 v234, v234, v232\n" \
"v_mfma_f32_16x16x32_bf16 v[136:139], a[96:99], a[16:19], v[164:167]\n" \
"v_exp_f32_e64 v132, v132\n" \
"v_mfma_f32_16x16x32_bf16 v[136:139], a[100:103], a[20:23], v[136:139]\n" \
"ds_read_b64_tr_b16 a[202:203], v215 offset:6144\n" \
"v_max3_f32 v232, v1, v6, v11\n" \
"v_mfma_f32_16x16x32_bf16 v[136:139], a[104:107], a[24:27], v[136:139]\n" \
"v_exp_f32_e64 v133, v133\n" \
"v_mfma_f32_16x16x32_bf16 v[136:139], a[108:111], a[28:31], v[136:139]\n" \
"v_cvt_pk_bf16_f32 v192, v128, v129\n" \
"v_cvt_pk_bf16_f32 v193, v130, v131\n" \
"v_mfma_f32_16x16x32_bf16 v[140:143], a[124:127], a[28:31], v[164:167]\n" \
"v_exp_f32_e64 v134, v134\n" \
"v_mfma_f32_16x16x32_bf16 v[140:143], a[120:123], a[24:27], v[140:143]\n" \
"ds_read_b64_tr_b16 a[204:205], v215 offset:8192\n" \
"v_min3_f32 v233, v1, v6, v11\n" \
"v_mfma_f32_16x16x32_bf16 v[140:143], a[116:119], a[20:23], v[140:143]\n" \
"buffer_load_dwordx4 v212, %[srdt], s49 offen offset:1024 lds\n" \
"v_max3_f32 v232, v232, v16, v21\n" \
"v_mfma_f32_16x16x32_bf16 v[140:143], a[112:115], a[16:19], v[140:143]\n" \
"v_exp_f32_e64 v135, v135\n" \
"v_mfma_f32_16x16x32_bf16 v[48:51], a[176:179], v[196:199], v[48:51]\n" \
"ds_read_b64_tr_b16 a[206:207], v215 offset:10240\n" \
"v_add_u32_e32 v208, 0x1000, %[oq0]\n" \
"v_mfma_f32_16x16x32_bf16 v[48:51], a[144:147], v[188:191], v[48:51]\n" \
"v_exp_f32_e64 v136, v136\n" \
"v_mfma_f32_16x16x32_bf16 v[52:55], a[180:183], v[196:199], v[52:55]\n" \
"v_cvt_pk_bf16_f32 v194, v132, v133\n" \
"v_cvt_pk_bf16_f32 v195, v134, v135\n" \
"v_mfma_f32_16x16x32_bf16 v[52:55], a[148:151], v[188:191], v[52:55]\n" \
"v_exp_f32_e64 v137, v137\n" \
"v_mfma_f32_16x16x32_bf16 v[56:59], a[184:187], v[196:199], v[56:59]\n" \
"v_exp_f32_e64 v138, v138\n" \
"v_mfma_f32_16x16x32_bf16 v[56:59], a[152:155], v[188:191], v[56:59]\n" \
"ds_read_b64_tr_b16 a[208:209], v215 offset:12288\n" \
"v_add_u32_e32 v209, 0x4000, %[oq0]\n" \
"v_mfma_f32_16x16x32_bf16 v[60:63], a[188:191], v[196:199], v[60:63]\n" \
"v_exp_f32_e64 v139, v139\n" \
"v_mfma_f32_16x16x32_bf16 v[60:63], a[156:159], v[188:191], v[60:63]\n" \
"v_add_u32_e32 v210, 0x5000, %[oq0]\n" \
"v_mfma_f32_16x16x32_bf16 v[144:147], a[96:99], a[32:35], v[168:171]\n" \
"v_exp_f32_e64 v140, v140\n" \
"v_mfma_f32_16x16x32_bf16 v[144:147], a[100:103], a[36:39], v[144:147]\n" \
"ds_read_b64_tr_b16 a[210:211], v215 offset:14336\n" \
"v_min3_f32 v233, v233, v16, v21\n" \
"v_mfma_f32_16x16x32_bf16 v[144:147], a[104:107], a[40:43], v[144:147]\n" \
"v_exp_f32_e64 v141, v141\n" \
"v_mfma_f32_16x16x32_bf16 v[144:147], a[108:111], a[44:47], v[144:147]\n" \
"v_cvt_pk_bf16_f32 v196, v136, v137\n" \
"v_cvt_pk_bf16_f32 v197, v138, v139\n" \
"v_mfma_f32_16x16x32_bf16 v[148:151], a[124:127], a[44:47], v[168:171]\n" \
"v_exp_f32_e64 v142, v142\n" \
"v_mfma_f32_16x16x32_bf16 v[148:151], a[120:123], a[40:43], v[148:151]\n" \
"v_sub_f32_e32 v232, v232, v233\n" \
"v_min_f32_e32 v234, v234, v232\n" \
"v_mfma_f32_16x16x32_bf16 v[148:151], a[116:119], a[36:39], v[148:151]\n" \
"buffer_load_dwordx4 v213, %[srdt], s49 offen offset:2048 lds\n" \
"v_max3_f32 v232, v65, v70, v75\n" \
"v_mfma_f32_16x16x32_bf16 v[148:151], a[112:115], a[32:35], v[148:151]\n" \
"v_exp_f32_e64 v143, v143\n" \
"v_mfma_f32_16x16x32_bf16 v[96:99], a[160:163], v[204:207], v[96:99]\n" \
"ds_read_b64_tr_b16 a[160:161], %[vvb] offset:57344\n" \
"v_min3_f32 v233, v65, v70, v75\n" \
"v_mfma_f32_16x16x32_bf16 v[96:99], a[128:131], v[220:223], v[96:99]\n" \
"v_exp_f32_e64 v144, v144\n" \
"v_mfma_f32_16x16x32_bf16 v[100:103], a[164:167], v[204:207], v[100:103]\n" \
"v_cvt_pk_bf16_f32 v198, v140, v141\n" \
"v_cvt_pk_bf16_f32 v199, v142, v143\n" \
"v_mfma_f32_16x16x32_bf16 v[100:103], a[132:135], v[220:223], v[100:103]\n" \
"v_exp_f32_e64 v145, v145\n" \
"v_mfma_f32_16x16x32_bf16 v[104:107], a[168:171], v[204:207], v[104:107]\n" \
"ds_read_b64_tr_b16 a[162:163], %[vvb] offset:61440\n" \
"ds_read_b64_tr_b16 a[164:165], v211 offset:57344\n" \
"v_mfma_f32_16x16x32_bf16 v[104:107], a[136:139], v[220:223], v[104:107]\n" \
"v_exp_f32_e64 v146, v146\n" \
"v_mfma_f32_16x16x32_bf16 v[108:111], a[172:175], v[204:207], v[108:111]\n" \
"ds_read_b64_tr_b16 a[166:167], v211 offset:61440\n" \
"v_max3_f32 v232, v232, v80, v85\n" \
"v_mfma_f32_16x16x32_bf16 v[108:111], a[140:143], v[220:223], v[108:111]\n" \
"v_exp_f32_e64 v147, v147\n" \
"v_smfmac_f32_16x16x64_bf16 v[180:183], a[192:195], v[216:223], v228\n" \
"ds_read_b64_tr_b16 a[168:169], %[vvb] offset:57856\n" \
"v_min3_f32 v233, v233, v80, v85\n" \
"v_mfma_f32_16x16x32_bf16 v[152:155], a[96:99], a[48:51], v[172:175]\n" \
"v_exp_f32_e64 v148, v148\n" \
"v_mfma_f32_16x16x32_bf16 v[152:155], a[100:103], a[52:55], v[152:155]\n" \
"ds_read_b64_tr_b16 a[170:171], %[vvb] offset:61952\n" \
"v_sub_f32_e64 v232, v232, v233\n" \
"v_mfma_f32_16x16x32_bf16 v[152:155], a[104:107], a[56:59], v[152:155]\n" \
"v_exp_f32_e64 v149, v149\n" \
"v_mfma_f32_16x16x32_bf16 v[152:155], a[108:111], a[60:63], v[152:155]\n" \
"v_cvt_pk_bf16_f32 v200, v144, v145\n" \
"v_cvt_pk_bf16_f32 v201, v146, v147\n" \
"v_mfma_f32_16x16x32_bf16 v[156:159], a[124:127], a[60:63], v[172:175]\n" \
"v_exp_f32_e64 v150, v150\n" \
"v_mfma_f32_16x16x32_bf16 v[156:159], a[120:123], a[56:59], v[156:159]\n" \
"ds_read_b64_tr_b16 a[172:173], v211 offset:57856\n" \
"v_min_f32_e64 v234, v234, v232\n" \
"v_mfma_f32_16x16x32_bf16 v[156:159], a[116:119], a[52:55], v[156:159]\n" \
"buffer_load_dwordx4 v214, %[srdt], s49 offen offset:3072 lds\n" \
"v_accvgpr_read_b32 v233, a228\n" \
"v_mul_f32_e32 v233, 0x3a000000, v233\n" \
"v_mfma_f32_16x16x32_bf16 v[156:159], a[112:115], a[48:51], v[156:159]\n" \
"v_exp_f32_e64 v151, v151\n" \
"v_mfma_f32_16x16x32_bf16 v[112:115], a[176:179], v[204:207], v[112:115]\n" \
"ds_read_b64_tr_b16 a[174:175], v211 offset:61952\n" \
"ds_read_b64_tr_b16 a[176:177], %[vvb] offset:58368\n" \
"v_mfma_f32_16x16x32_bf16 v[112:115], a[144:147], v[220:223], v[112:115]\n" \
"v_exp_f32_e64 v152, v152\n" \
"v_mfma_f32_16x16x32_bf16 v[116:119], a[180:183], v[204:207], v[116:119]\n" \
"v_cvt_pk_bf16_f32 v202, v148, v149\n" \
"v_cvt_pk_bf16_f32 v203, v150, v151\n" \
"v_mfma_f32_16x16x32_bf16 v[116:119], a[148:151], v[220:223], v[116:119]\n" \
"v_exp_f32_e64 v153, v153\n" \
"v_mfma_f32_16x16x32_bf16 v[120:123], a[184:187], v[204:207], v[120:123]\n" \
"v_exp_f32_e64 v154, v154\n" \
"v_mfma_f32_16x16x32_bf16 v[120:123], a[152:155], v[220:223], v[120:123]\n" \
"ds_read_b64_tr_b16 a[178:179], %[vvb] offset:62464\n" \
"s_waitcnt vmcnt(5)\n" \
"v_cmp_eq_u32_e64 s[56:57], 0, v229\n" \
"s_cmp_lg_u64 s[56:57], 0\n" \
"s_cbranch_scc1 Lspk_%=\n" \
"Lbkk_%=:\n" \
"v_mfma_f32_16x16x32_bf16 v[124:127], a[188:191], v[204:207], v[124:127]\n" \
"v_exp_f32_e32 v155, v155\n" \
"v_mfma_f32_16x16x32_bf16 v[124:127], a[156:159], v[220:223], v[124:127]\n" \
"ds_read_b64_tr_b16 a[180:181], v211 offset:58368\n" \
"ds_read_b64_tr_b16 a[182:183], v211 offset:62464\n" \
"ds_read_b64_tr_b16 a[184:185], %[vvb] offset:58880\n" \
"ds_read_b64_tr_b16 a[186:187], %[vvb] offset:62976\n" \
"ds_read_b64_tr_b16 a[188:189], v211 offset:58880\n" \
"ds_read_b64_tr_b16 a[190:191], v211 offset:62976\n" \
"s_waitcnt lgkmcnt(8)\n" \
"s_nop 0\n" \
"v_smfmac_f32_16x16x64_bf16 v[176:179], a[192:195], v[192:199], v228\n" \
"v_exp_f32_e64 v156, v156\n" \
"ds_read_b64_tr_b16 a[212:213], v227 offset:0\n" \
"v_mfma_f32_16x16x32_bf16 v[0:3], a[160:163], v[192:195], v[0:3]\n" \
"v_exp_f32_e64 v157, v157\n" \
"ds_read_b64_tr_b16 a[214:215], v227 offset:2048\n" \
"v_mfma_f32_16x16x32_bf16 v[4:7], a[164:167], v[192:195], v[4:7]\n" \
"v_cvt_pk_bf16_f32 v204, v152, v153\n" \
"ds_read_b64_tr_b16 a[216:217], v227 offset:4096\n" \
"v_mfma_f32_16x16x32_bf16 v[8:11], a[168:171], v[192:195], v[8:11]\n" \
"v_cvt_pk_bf16_f32 v205, v154, v155\n" \
"ds_read_b64_tr_b16 a[218:219], v227 offset:6144\n" \
"v_mfma_f32_16x16x32_bf16 v[12:15], a[172:175], v[192:195], v[12:15]\n" \
"v_exp_f32_e64 v158, v158\n" \
"ds_read_b64_tr_b16 a[220:221], v227 offset:8192\n" \
"s_waitcnt lgkmcnt(5)\n" \
"s_nop 0\n" \
"v_mfma_f32_16x16x32_bf16 v[16:19], a[176:179], v[192:195], v[16:19]\n" \
"v_exp_f32_e64 v159, v159\n" \
"ds_read_b64_tr_b16 a[222:223], v227 offset:10240\n" \
"v_mfma_f32_16x16x32_bf16 v[20:23], a[180:183], v[192:195], v[20:23]\n" \
"v_cvt_pk_bf16_f32 v206, v156, v157\n" \
"ds_read_b64_tr_b16 a[224:225], v227 offset:12288\n" \
"v_accvgpr_read_b32 v229, a228\n" \
"v_mfma_f32_16x16x32_bf16 v[24:27], a[184:187], v[192:195], v[24:27]\n" \
"v_cvt_pk_bf16_f32 v207, v158, v159\n" \
"ds_read_b64_tr_b16 a[226:227], v227 offset:14336\n" \
"v_mfma_f32_16x16x32_bf16 v[28:31], a[188:191], v[192:195], v[28:31]\n" \
"v_add_f32_e64 v176, v176, v177\n" \
"v_smfmac_f32_16x16x64_bf16 v[180:183], a[192:195], v[200:207], v228\n" \
"v_add_f32_e32 v178, v178, v179\n" \
"v_add_f32_e32 v231, v176, v178\n" \
"v_rcp_f32_e32 v176, v176\n" \
"v_rcp_f32_e32 v178, v178\n" \
"v_mul_f32_e64 v177, -v229, v176\n" \
"v_mul_f32_e64 v179, -v229, v178\n" \
"v_mfma_f32_16x16x32_bf16 v[32:35], a[160:163], v[196:199], v[32:35]\n" \
"v_pk_fma_f32 v[0:1], v[0:1], v[176:177], v[176:177] op_sel:[0,0,1] op_sel_hi:[1,0,1]\n" \
"v_pk_fma_f32 v[2:3], v[2:3], v[176:177], v[176:177] op_sel:[0,0,1] op_sel_hi:[1,0,1]\n" \
"v_pk_fma_f32 v[4:5], v[4:5], v[176:177], v[176:177] op_sel:[0,0,1] op_sel_hi:[1,0,1]\n" \
"v_pk_fma_f32 v[6:7], v[6:7], v[176:177], v[176:177] op_sel:[0,0,1] op_sel_hi:[1,0,1]\n" \
"v_cvt_pk_bf16_f32 v0, v0, v1\n" \
"v_cvt_pk_bf16_f32 v1, v2, v3\n" \
"v_cvt_pk_bf16_f32 v2, v4, v5\n" \
"v_cvt_pk_bf16_f32 v3, v6, v7\n" \
"v_mfma_f32_16x16x32_bf16 v[36:39], a[164:167], v[196:199], v[36:39]\n" \
"s_mov_b32 s55, 0x4e800000\n" \
"v_mov_b32_e32 v236, %[flag]\n" \
"v_add_f32_e32 v180, v180, v181\n" \
"v_add_f32_e32 v182, v182, v183\n" \
"v_add_f32_e32 v231, v231, v180\n" \
"v_add_f32_e32 v231, v231, v182\n" \
"v_rcp_f32_e32 v180, v180\n" \
"v_cmp_nle_f32_e64 s[56:57], v231, s55\n" \
"v_mfma_f32_16x16x32_bf16 v[40:43], a[168:171], v[196:199], v[40:43]\n" \
"v_rcp_f32_e64 v182, v182\n" \
"v_cmp_nle_f32_e64 s[44:45], v233, v234\n" \
"v_mul_f32_e64 v181, -v229, v180\n" \
"v_mul_f32_e64 v183, -v229, v182\n" \
"s_nop 1\n" \
"s_or_b64 s[56:57], s[56:57], s[44:45]\n" \
"s_cselect_b32 s55, 1, 0\n" \
"v_mov_b32_e32 v230, s55\n" \
"v_mfma_f32_16x16x32_bf16 v[44:47], a[172:175], v[196:199], v[44:47]\n" \
"s_mov_b32 s69, s55\n" \
"s_andn2_b32 s55, s55, s52\n" \
"s_cselect_b64 vcc, 0, vcc\n" \
"s_nop 0\n" \
"ds_write_b32 v236, v230\n" \
"v_and_b32_e32 v236, -16, v236\n" \
"s_cbranch_vccz Lg21_%=\n" \
"buffer_store_dwordx4 v[0:3], %[oq0], %[srdz], s60 offen offset:0 sc1\n" \
"Lg21_%=:\n" \
"v_pk_fma_f32 v[8:9], v[8:9], v[176:177], v[176:177] op_sel:[0,0,1] op_sel_hi:[1,0,1]\n" \
"v_pk_fma_f32 v[10:11], v[10:11], v[176:177], v[176:177] op_sel:[0,0,1] op_sel_hi:[1,0,1]\n" \
"v_mfma_f32_16x16x32_bf16 v[48:51], a[176:179], v[196:199], v[48:51]\n" \
"v_pk_fma_f32 v[12:13], v[12:13], v[176:177], v[176:177] op_sel:[0,0,1] op_sel_hi:[1,0,1]\n" \
"v_pk_fma_f32 v[14:15], v[14:15], v[176:177], v[176:177] op_sel:[0,0,1] op_sel_hi:[1,0,1]\n" \
"v_cvt_pk_bf16_f32 v8, v8, v9\n" \
"v_cvt_pk_bf16_f32 v9, v10, v11\n" \
"v_cvt_pk_bf16_f32 v10, v12, v13\n" \
"v_cvt_pk_bf16_f32 v11, v14, v15\n" \
"s_cbranch_vccz Lg22_%=\n" \
"s_nop 0\n" \
"buffer_store_dwordx4 v[8:11], %[oq0], %[srdz], s60 offen offset:1024 sc1\n" \
"Lg22_%=:\n" \
"v_pk_fma_f32 v[16:17], v[16:17], v[176:177], v[176:177] op_sel:[0,0,1] op_sel_hi:[1,0,1]\n" \
"v_mfma_f32_16x16x32_bf16 v[52:55], a[180:183], v[196:199], v[52:55]\n" \
"v_pk_fma_f32 v[18:19], v[18:19], v[176:177], v[176:177] op_sel:[0,0,1] op_sel_hi:[1,0,1]\n" \
"v_pk_fma_f32 v[20:21], v[20:21], v[176:177], v[176:177] op_sel:[0,0,1] op_sel_hi:[1,0,1]\n" \
"v_pk_fma_f32 v[22:23], v[22:23], v[176:177], v[176:177] op_sel:[0,0,1] op_sel_hi:[1,0,1]\n" \
"v_cvt_pk_bf16_f32 v16, v16, v17\n" \
"v_cvt_pk_bf16_f32 v17, v18, v19\n" \
"v_cvt_pk_bf16_f32 v18, v20, v21\n" \
"v_cvt_pk_bf16_f32 v19, v22, v23\n" \
"s_cbranch_vccz Lg23_%=\n" \
"s_nop 0\n" \
"buffer_store_dwordx4 v[16:19], %[oq0], %[srdz], s60 offen offset:2048 sc1\n" \
"Lg23_%=:\n" \
"v_mfma_f32_16x16x32_bf16 v[56:59], a[184:187], v[196:199], v[56:59]\n" \
"v_pk_fma_f32 v[24:25], v[24:25], v[176:177], v[176:177] op_sel:[0,0,1] op_sel_hi:[1,0,1]\n" \
"v_pk_fma_f32 v[26:27], v[26:27], v[176:177], v[176:177] op_sel:[0,0,1] op_sel_hi:[1,0,1]\n" \
"v_pk_fma_f32 v[28:29], v[28:29], v[176:177], v[176:177] op_sel:[0,0,1] op_sel_hi:[1,0,1]\n" \
"v_pk_fma_f32 v[30:31], v[30:31], v[176:177], v[176:177] op_sel:[0,0,1] op_sel_hi:[1,0,1]\n" \
"v_cvt_pk_bf16_f32 v24, v24, v25\n" \
"v_cvt_pk_bf16_f32 v25, v26, v27\n" \
"v_cvt_pk_bf16_f32 v26, v28, v29\n" \
"v_cvt_pk_bf16_f32 v27, v30, v31\n" \
"v_mfma_f32_16x16x32_bf16 v[60:63], a[188:191], v[196:199], v[60:63]\n" \
"s_cbranch_vccz Lg24_%=\n" \
"s_nop 0\n" \
"buffer_store_dwordx4 v[24:27], %[oq0], %[srdz], s60 offen offset:3072 sc1\n" \
"Lg24_%=:\n" \
"v_mfma_f32_16x16x32_bf16 v[64:67], a[128:131], v[216:219], v[64:67]\n" \
"v_fma_f32 v32, v32, v178, v179\n" \
"v_fma_f32 v33, v33, v178, v179\n" \
"v_fma_f32 v34, v34, v178, v179\n" \
"v_fma_f32 v35, v35, v178, v179\n" \
"v_fma_f32 v36, v36, v178, v179\n" \
"v_fma_f32 v37, v37, v178, v179\n" \
"v_mfma_f32_16x16x32_bf16 v[64:67], a[160:163], v[200:203], v[64:67]\n" \
"v_fma_f32 v38, v38, v178, v179\n" \
"v_fma_f32 v39, v39, v178, v179\n" \
"v_cvt_pk_bf16_f32 v32, v32, v33\n" \
"v_cvt_pk_bf16_f32 v33, v34, v35\n" \
"v_mfma_f32_16x16x32_bf16 v[68:71], a[132:135], v[216:219], v[68:71]\n" \
"v_cvt_pk_bf16_f32 v34, v36, v37\n" \
"v_cvt_pk_bf16_f32 v35, v38, v39\n" \
"s_cbranch_vccz Lg25_%=\n" \
"s_nop 0\n" \
"buffer_store_dwordx4 v[32:35], v208, %[srdz], s60 offen offset:0 sc1\n" \
"Lg25_%=:\n" \
"v_mfma_f32_16x16x32_bf16 v[68:71], a[164:167], v[200:203], v[68:71]\n" \
"v_fma_f32 v40, v40, v178, v179\n" \
"v_fma_f32 v41, v41, v178, v179\n" \
"v_fma_f32 v42, v42, v178, v179\n" \
"v_fma_f32 v43, v43, v178, v179\n" \
"v_fma_f32 v44, v44, v178, v179\n" \
"v_fma_f32 v45, v45, v178, v179\n" \
"v_mfma_f32_16x16x32_bf16 v[72:75], a[136:139], v[216:219], v[72:75]\n" \
"v_fma_f32 v46, v46, v178, v179\n" \
"v_fma_f32 v47, v47, v178, v179\n" \
"v_cvt_pk_bf16_f32 v40, v40, v41\n" \
"v_cvt_pk_bf16_f32 v41, v42, v43\n" \
"v_mfma_f32_16x16x32_bf16 v[72:75], a[168:171], v[200:203], v[72:75]\n" \
"v_cvt_pk_bf16_f32 v42, v44, v45\n" \
"v_cvt_pk_bf16_f32 v43, v46, v47\n" \
"s_cbranch_vccz Lg26_%=\n" \
"s_nop 0\n" \
"buffer_store_dwordx4 v[40:43], v208, %[srdz], s60 offen offset:1024 sc1\n" \
"Lg26_%=:\n" \
"v_mfma_f32_16x16x32_bf16 v[76:79], a[140:143], v[216:219], v[76:79]\n" \
"v_fma_f32 v48, v48, v178, v179\n" \
"v_fma_f32 v49, v49, v178, v179\n" \
"v_fma_f32 v50, v50, v178, v179\n" \
"v_fma_f32 v51, v51, v178, v179\n" \
"v_fma_f32 v52, v52, v178, v179\n" \
"v_fma_f32 v53, v53, v178, v179\n" \
"v_mfma_f32_16x16x32_bf16 v[76:79], a[172:175], v[200:203], v[76:79]\n" \
"v_fma_f32 v54, v54, v178, v179\n" \
"v_fma_f32 v55, v55, v178, v179\n" \
"v_cvt_pk_bf16_f32 v48, v48, v49\n" \
"v_cvt_pk_bf16_f32 v49, v50, v51\n" \
"v_mfma_f32_16x16x32_bf16 v[80:83], a[144:147], v[216:219], v[80:83]\n" \
"v_cvt_pk_bf16_f32 v50, v52, v53\n" \
"v_cvt_pk_bf16_f32 v51, v54, v55\n" \
"s_cbranch_vccz Lg27_%=\n" \
"s_nop 0\n" \
"buffer_store_dwordx4 v[48:51], v208, %[srdz], s60 offen offset:2048 sc1\n" \
"Lg27_%=:\n" \
"v_mfma_f32_16x16x32_bf16 v[80:83], a[176:179], v[200:203], v[80:83]\n" \
"v_fma_f32 v56, v56, v178, v179\n" \
"v_fma_f32 v57, v57, v178, v179\n" \
"v_fma_f32 v58, v58, v178, v179\n" \
"v_fma_f32 v59, v59, v178, v179\n" \
"v_fma_f32 v60, v60, v178, v179\n" \
"v_fma_f32 v61, v61, v178, v179\n" \
"v_mfma_f32_16x16x32_bf16 v[84:87], a[148:151], v[216:219], v[84:87]\n" \
"v_fma_f32 v62, v62, v178, v179\n" \
"v_fma_f32 v63, v63, v178, v179\n" \
"v_cvt_pk_bf16_f32 v56, v56, v57\n" \
"v_cvt_pk_bf16_f32 v57, v58, v59\n" \
"v_mfma_f32_16x16x32_bf16 v[84:87], a[180:183], v[200:203], v[84:87]\n" \
"v_cvt_pk_bf16_f32 v58, v60, v61\n" \
"v_cvt_pk_bf16_f32 v59, v62, v63\n" \
"s_cbranch_vccz Lg28_%=\n" \
"s_nop 0\n" \
"buffer_store_dwordx4 v[56:59], v208, %[srdz], s60 offen offset:3072 sc1\n" \
"Lg28_%=:\n" \
"v_mfma_f32_16x16x32_bf16 v[88:91], a[152:155], v[216:219], v[88:91]\n" \
"v_mfma_f32_16x16x32_bf16 v[88:91], a[184:187], v[200:203], v[88:91]\n" \
"v_mfma_f32_16x16x32_bf16 v[92:95], a[156:159], v[216:219], v[92:95]\n" \
"v_mfma_f32_16x16x32_bf16 v[92:95], a[188:191], v[200:203], v[92:95]\n" \
"s_waitcnt vmcnt(12) lgkmcnt(0)\n" \
"s_bitcmp1_b32 0x1f, s48\n" \
"s_cbranch_scc1 Lcw1_%=\n" \
"s_waitcnt vmcnt(4)\n" \
"Lcw1_%=:\n" \
"s_barrier\n" \
"s_nop 0\n" \
"ds_read_b128 v[232:235], v236\n" \
"v_mfma_f32_16x16x32_bf16 v[96:99], a[160:163], v[204:207], v[96:99]\n" \
"v_pk_fma_f32 v[64:65], v[64:65], v[180:181], v[180:181] op_sel:[0,0,1] op_sel_hi:[1,0,1]\n" \
"v_pk_fma_f32 v[66:67], v[66:67], v[180:181], v[180:181] op_sel:[0,0,1] op_sel_hi:[1,0,1]\n" \
"v_pk_fma_f32 v[68:69], v[68:69], v[180:181], v[180:181] op_sel:[0,0,1] op_sel_hi:[1,0,1]\n" \
"v_pk_fma_f32 v[70:71], v[70:71], v[180:181], v[180:181] op_sel:[0,0,1] op_sel_hi:[1,0,1]\n" \
"v_cvt_pk_bf16_f32 v64, v64, v65\n" \
"v_mfma_f32_16x16x32_bf16 v[100:103], a[164:167], v[204:207], v[100:103]\n" \
"v_cvt_pk_bf16_f32 v65, v66, v67\n" \
"v_cvt_pk_bf16_f32 v66, v68, v69\n" \
"v_cvt_pk_bf16_f32 v67, v70, v71\n" \
"s_cbranch_vccz Lg29_%=\n" \
"s_nop 0\n" \
"buffer_store_dwordx4 v[64:67], v209, %[srdz], s60 offen offset:0 sc1\n" \
"Lg29_%=:\n" \
"v_pk_fma_f32 v[72:73], v[72:73], v[180:181], v[180:181] op_sel:[0,0,1] op_sel_hi:[1,0,1]\n" \
"v_mfma_f32_16x16x32_bf16 v[104:107], a[168:171], v[204:207], v[104:107]\n" \
"v_pk_fma_f32 v[74:75], v[74:75], v[180:181], v[180:181] op_sel:[0,0,1] op_sel_hi:[1,0,1]\n" \
"v_pk_fma_f32 v[76:77], v[76:77], v[180:181], v[180:181] op_sel:[0,0,1] op_sel_hi:[1,0,1]\n" \
"v_pk_fma_f32 v[78:79], v[78:79], v[180:181], v[180:181] op_sel:[0,0,1] op_sel_hi:[1,0,1]\n" \
"v_cvt_pk_bf16_f32 v72, v72, v73\n" \
"v_cvt_pk_bf16_f32 v73, v74, v75\n" \
"v_mfma_f32_16x16x32_bf16 v[108:111], a[172:175], v[204:207], v[108:111]\n" \
"v_cvt_pk_bf16_f32 v74, v76, v77\n" \
"v_cvt_pk_bf16_f32 v75, v78, v79\n" \
"s_cbranch_vccz Lg30_%=\n" \
"s_nop 0\n" \
"buffer_store_dwordx4 v[72:75], v209, %[srdz], s60 offen offset:1024 sc1\n" \
"Lg30_%=:\n" \
"v_pk_fma_f32 v[80:81], v[80:81], v[180:181], v[180:181] op_sel:[0,0,1] op_sel_hi:[1,0,1]\n" \
"v_pk_fma_f32 v[82:83], v[82:83], v[180:181], v[180:181] op_sel:[0,0,1] op_sel_hi:[1,0,1]\n" \
"v_mfma_f32_16x16x32_bf16 v[112:115], a[176:179], v[204:207], v[112:115]\n" \
"v_pk_fma_f32 v[84:85], v[84:85], v[180:181], v[180:181] op_sel:[0,0,1] op_sel_hi:[1,0,1]\n" \
"v_pk_fma_f32 v[86:87], v[86:87], v[180:181], v[180:181] op_sel:[0,0,1] op_sel_hi:[1,0,1]\n" \
".if . - adaln_body != 40960\n" \
".error \"size model\"\n" \
".endif\n" \
"Lk9_%=:\n" \
"s_cbranch_execz Lk10_%=\n" \
"s_nop 0\n" \
"v_cvt_pk_bf16_f32 v80, v80, v81\n" \
"v_cvt_pk_bf16_f32 v81, v82, v83\n" \
"v_cvt_pk_bf16_f32 v82, v84, v85\n" \
"v_mfma_f32_16x16x32_bf16 v[116:119], a[180:183], v[204:207], v[116:119]\n" \
"v_cvt_pk_bf16_f32 v83, v86, v87\n" \
"s_cbranch_vccz Lg31_%=\n" \
"s_nop 0\n" \
"buffer_store_dwordx4 v[80:83], v209, %[srdz], s60 offen offset:2048 sc1\n" \
"Lg31_%=:\n" \
"v_pk_fma_f32 v[88:89], v[88:89], v[180:181], v[180:181] op_sel:[0,0,1] op_sel_hi:[1,0,1]\n" \
"v_pk_fma_f32 v[90:91], v[90:91], v[180:181], v[180:181] op_sel:[0,0,1] op_sel_hi:[1,0,1]\n" \
"v_pk_fma_f32 v[92:93], v[92:93], v[180:181], v[180:181] op_sel:[0,0,1] op_sel_hi:[1,0,1]\n" \
"v_mfma_f32_16x16x32_bf16 v[120:123], a[184:187], v[204:207], v[120:123]\n" \
"v_pk_fma_f32 v[94:95], v[94:95], v[180:181], v[180:181] op_sel:[0,0,1] op_sel_hi:[1,0,1]\n" \
"v_cvt_pk_bf16_f32 v88, v88, v89\n" \
"v_cvt_pk_bf16_f32 v89, v90, v91\n" \
"v_cvt_pk_bf16_f32 v90, v92, v93\n" \
"v_cvt_pk_bf16_f32 v91, v94, v95\n" \
"v_mfma_f32_16x16x32_bf16 v[124:127], a[188:191], v[204:207], v[124:127]\n" \
"s_cbranch_vccz Lg32_%=\n" \
"s_nop 0\n" \
"buffer_store_dwordx4 v[88:91], v209, %[srdz], s60 offen offset:3072 sc1\n" \
"Lg32_%=:\n" \
"s_waitcnt lgkmcnt(0)\n" \
"s_nop 0\n" \
"v_or3_b32 v232, v232, v233, v234\n" \
"v_or_b32_e32 v232, v232, v235\n" \
"s_nop 0\n" \
"v_readfirstlane_b32 s55, v232\n" \
"s_nop 3\n" \
"s_andn2_b32 s55, s55, s52\n" \
"s_cbranch_scc1 Lslow_%=\n" \
"v_mfma_f32_16x16x32_bf16 v[192:195], a[196:199], v[0:3], 0\n" \
"v_mfma_f32_16x16x32_bf16 v[192:195], a[200:203], v[8:11], v[192:195]\n" \
"v_fma_f32 v96, v96, v182, v183\n" \
"v_fma_f32 v97, v97, v182, v183\n" \
"v_fma_f32 v98, v98, v182, v183\n" \
"v_fma_f32 v99, v99, v182, v183\n" \
"v_mfma_f32_16x16x32_bf16 v[192:195], a[204:207], v[16:19], v[192:195]\n" \
"v_fma_f32 v100, v100, v182, v183\n" \
"v_fma_f32 v101, v101, v182, v183\n" \
"v_fma_f32 v102, v102, v182, v183\n" \
"v_fma_f32 v103, v103, v182, v183\n" \
"s_add_u32 m0, %[m0b], 114688\n" \
"s_cmp_gt_u32 s48, 1\n" \
"s_cselect_b32 s49, 1, 2\n" \
"s_lshl_b32 s49, s49, 18\n" \
"s_or_b32 s49, s49, s58\n" \
"buffer_load_dwordx4 %[wo0], %[srdt], s49 offen offset:0 lds\n" \
"v_mfma_f32_16x16x32_bf16 v[192:195], a[208:211], v[24:27], v[192:195]\n" \
"v_cvt_pk_bf16_f32 v96, v96, v97\n" \
"v_cvt_pk_bf16_f32 v97, v98, v99\n" \
"v_mfma_f32_16x16x32_bf16 v[144:147], a[212:215], v[0:3], 0\n" \
"v_cvt_pk_bf16_f32 v98, v100, v101\n" \
"v_mfma_f32_16x16x32_bf16 v[144:147], a[216:219], v[8:11], v[144:147]\n" \
"v_cvt_pk_bf16_f32 v99, v102, v103\n" \
"s_cbranch_vccz Lg1_%=\n" \
"s_nop 0\n" \
"buffer_store_dwordx4 v[96:99], v210, %[srdz], s60 offen offset:0 sc1\n" \
"Lg1_%=:\n" \
"v_mfma_f32_16x16x32_bf16 v[144:147], a[220:223], v[16:19], v[144:147]\n" \
"ds_read_b64_tr_b16 a[228:229], v215 offset:512\n" \
"v_fma_f32 v104, v104, v182, v183\n" \
"v_fma_f32 v105, v105, v182, v183\n" \
"v_fma_f32 v106, v106, v182, v183\n" \
"v_fma_f32 v107, v107, v182, v183\n" \
"v_mfma_f32_16x16x32_bf16 v[144:147], a[224:227], v[24:27], v[144:147]\n" \
"ds_read_b64_tr_b16 a[230:231], v215 offset:2560\n" \
"v_fma_f32 v108, v108, v182, v183\n" \
"v_fma_f32 v109, v109, v182, v183\n" \
"v_mfma_f32_16x16x32_bf16 v[196:199], a[196:199], v[32:35], 0\n" \
"ds_read_b64_tr_b16 a[232:233], v215 offset:4608\n" \
"v_fma_f32 v110, v110, v182, v183\n" \
"v_fma_f32 v111, v111, v182, v183\n" \
"v_cvt_pk_bf16_f32 v104, v104, v105\n" \
"v_mfma_f32_16x16x32_bf16 v[196:199], a[200:203], v[40:43], v[196:199]\n" \
"ds_read_b64_tr_b16 a[234:235], v215 offset:6656\n" \
"v_cvt_pk_bf16_f32 v105, v106, v107\n" \
"v_cvt_pk_bf16_f32 v106, v108, v109\n" \
"v_mfma_f32_16x16x32_bf16 v[196:199], a[204:207], v[48:51], v[196:199]\n" \
"v_cvt_pk_bf16_f32 v107, v110, v111\n" \
"s_cbranch_vccz Lg2_%=\n" \
"s_nop 0\n" \
"buffer_store_dwordx4 v[104:107], v210, %[srdz], s60 offen offset:1024 sc1\n" \
"Lg2_%=:\n" \
"v_mfma_f32_16x16x32_bf16 v[196:199], a[208:211], v[56:59], v[196:199]\n" \
"v_fma_f32 v112, v112, v182, v183\n" \
"v_fma_f32 v113, v113, v182, v183\n" \
"v_mfma_f32_16x16x32_bf16 v[148:151], a[212:215], v[32:35], 0\n" \
"v_fma_f32 v114, v114, v182, v183\n" \
"v_fma_f32 v115, v115, v182, v183\n" \
"v_fma_f32 v116, v116, v182, v183\n" \
"v_fma_f32 v117, v117, v182, v183\n" \
"buffer_load_dwordx4 v212, %[srdt], s49 offen offset:1024 lds\n" \
"v_mfma_f32_16x16x32_bf16 v[148:151], a[216:219], v[40:43], v[148:151]\n" \
"v_fma_f32 v118, v118, v182, v183\n" \
"v_fma_f32 v119, v119, v182, v183\n" \
"v_cvt_pk_bf16_f32 v112, v112, v113\n" \
"v_mfma_f32_16x16x32_bf16 v[148:151], a[220:223], v[48:51], v[148:151]\n" \
"ds_read_b64_tr_b16 a[236:237], v215 offset:8704\n" \
"v_cvt_pk_bf16_f32 v113, v114, v115\n" \
"v_mfma_f32_16x16x32_bf16 v[148:151], a[224:227], v[56:59], v[148:151]\n" \
"v_cvt_pk_bf16_f32 v114, v116, v117\n" \
"v_cvt_pk_bf16_f32 v115, v118, v119\n" \
"v_mfma_f32_16x16x32_bf16 v[200:203], a[196:199], v[64:67], 0\n" \
"ds_read_b64_tr_b16 a[238:239], v215 offset:10752\n" \
"s_cbranch_vccz Lg3_%=\n" \
"s_nop 0\n" \
"buffer_store_dwordx4 v[112:115], v210, %[srdz], s60 offen offset:2048 sc1\n" \
"Lg3_%=:\n" \
"v_fma_f32 v120, v120, v182, v183\n" \
"v_fma_f32 v121, v121, v182, v183\n" \
"v_mfma_f32_16x16x32_bf16 v[200:203], a[200:203], v[72:75], v[200:203]\n" \
"v_fma_f32 v122, v122, v182, v183\n" \
"v_fma_f32 v123, v123, v182, v183\n" \
"v_fma_f32 v124, v124, v182, v183\n" \
"v_fma_f32 v125, v125, v182, v183\n" \
"v_mfma_f32_16x16x32_bf16 v[200:203], a[204:207], v[80:83], v[200:203]\n" \
"v_fma_f32 v126, v126, v182, v183\n" \
"v_fma_f32 v127, v127, v182, v183\n" \
"v_mfma_f32_16x16x32_bf16 v[200:203], a[208:211], v[88:91], v[200:203]\n" \
"v_cvt_pk_bf16_f32 v120, v120, v121\n" \
"v_cvt_pk_bf16_f32 v121, v122, v123\n" \
"v_mfma_f32_16x16x32_bf16 v[152:155], a[212:215], v[64:67], 0\n" \
"v_cvt_pk_bf16_f32 v122, v124, v125\n" \
"v_cvt_pk_bf16_f32 v123, v126, v127\n" \
"v_mfma_f32_16x16x32_bf16 v[152:155], a[216:219], v[72:75], v[152:155]\n" \
"s_cbranch_vccz Lg4_%=\n" \
"s_nop 0\n" \
"buffer_store_dwordx4 v[120:123], v210, %[srdz], s60 offen offset:3072 sc1\n" \
"Lg4_%=:\n" \
"v_mfma_f32_16x16x32_bf16 v[152:155], a[220:223], v[80:83], v[152:155]\n" \
"v_add_u32_e32 v220, 0x10000, v215\n" \
"v_mfma_f32_16x16x32_bf16 v[152:155], a[224:227], v[88:91], v[152:155]\n" \
"v_mov_b32_e64 v230, 1\n" \
"v_readlane_b32 s62, %[tlo], 19\n" \
"v_readlane_b32 s63, %[thi], 19\n" \
"v_and_b32_e32 v233, 0x380, v224\n" \
"v_lshlrev_b32_e64 v233, 2, v233\n" \
"buffer_load_dwordx4 v213, %[srdt], s49 offen offset:2048 lds\n" \
"v_mfma_f32_16x16x32_bf16 v[204:207], a[196:199], v[96:99], 0\n" \
"v_add_u32_e32 v221, 0x10000, v227\n" \
"v_mfma_f32_16x16x32_bf16 v[204:207], a[200:203], v[104:107], v[204:207]\n" \
"ds_read_b64_tr_b16 a[196:197], v215 offset:12800\n" \
"v_mfma_f32_16x16x32_bf16 v[204:207], a[204:207], v[112:115], v[204:207]\n" \
"ds_read_b64_tr_b16 a[198:199], v215 offset:14848\n" \
"v_mfma_f32_16x16x32_bf16 v[204:207], a[208:211], v[120:123], v[204:207]\n" \
"ds_read_b64_tr_b16 a[200:201], v227 offset:512\n" \
"v_mfma_f32_16x16x32_bf16 v[156:159], a[212:215], v[96:99], 0\n" \
"ds_read_b64_tr_b16 a[202:203], v227 offset:2560\n" \
"v_mfma_f32_16x16x32_bf16 v[156:159], a[216:219], v[104:107], v[156:159]\n" \
"ds_read_b64_tr_b16 a[204:205], v227 offset:4608\n" \
"v_mfma_f32_16x16x32_bf16 v[156:159], a[220:223], v[112:115], v[156:159]\n" \
"ds_read_b64_tr_b16 a[206:207], v227 offset:6656\n" \
"v_mfma_f32_16x16x32_bf16 v[156:159], a[224:227], v[120:123], v[156:159]\n" \
"s_waitcnt vmcnt(63) lgkmcnt(4)\n" \
"s_nop 0\n" \
"v_mfma_f32_16x16x32_bf16 v[160:163], a[228:231], v[0:3], 0\n" \
"ds_read_b64_tr_b16 a[208:209], v227 offset:8704\n" \
"v_mfma_f32_16x16x32_bf16 v[160:163], a[232:235], v[8:11], v[160:163]\n" \
"ds_read_b64_tr_b16 a[210:211], v227 offset:10752\n" \
"buffer_load_dwordx4 v214, %[srdt], s49 offen offset:3072 lds\n" \
"v_mfma_f32_16x16x32_bf16 v[164:167], a[228:231], v[32:35], 0\n" \
"ds_read_b64_tr_b16 a[212:213], v227 offset:12800\n" \
"v_mfma_f32_16x16x32_bf16 v[164:167], a[232:235], v[40:43], v[164:167]\n" \
"ds_read_b64_tr_b16 a[214:215], v227 offset:14848\n" \
"v_mfma_f32_16x16x32_bf16 v[168:171], a[228:231], v[64:67], 0\n" \
"v_mfma_f32_16x16x32_bf16 v[168:171], a[232:235], v[72:75], v[168:171]\n" \
"v_mfma_f32_16x16x32_bf16 v[172:175], a[228:231], v[96:99], 0\n" \
"ds_read_b64_tr_b16 a[216:217], v215 offset:16384\n" \
"v_mfma_f32_16x16x32_bf16 v[172:175], a[232:235], v[104:107], v[172:175]\n" \
"ds_read_b64_tr_b16 a[218:219], v215 offset:18432\n" \
"v_mfma_f32_16x16x32_bf16 v[160:163], a[236:239], v[16:19], v[160:163]\n" \
"ds_read_b64_tr_b16 a[220:221], v215 offset:20480\n" \
"v_mfma_f32_16x16x32_bf16 v[160:163], a[196:199], v[24:27], v[160:163]\n" \
"ds_read_b64_tr_b16 a[222:223], v215 offset:22528\n" \
"v_mfma_f32_16x16x32_bf16 v[164:167], a[236:239], v[48:51], v[164:167]\n" \
"v_mfma_f32_16x16x32_bf16 v[164:167], a[196:199], v[56:59], v[164:167]\n" \
"v_mfma_f32_16x16x32_bf16 v[168:171], a[236:239], v[80:83], v[168:171]\n" \
"s_waitcnt vmcnt(63) lgkmcnt(0)\n" \
"s_barrier\n" \
"v_mfma_f32_16x16x32_bf16 v[168:171], a[196:199], v[88:91], v[168:171]\n" \
"v_mfma_f32_16x16x32_bf16 v[172:175], a[236:239], v[112:115], v[172:175]\n" \
"ds_read_b64_tr_b16 a[224:225], v215 offset:24576\n" \
"v_mfma_f32_16x16x32_bf16 v[172:175], a[196:199], v[120:123], v[172:175]\n" \
"s_waitcnt vmcnt(63) lgkmcnt(5)\n" \
"s_nop 0\n" \
"v_mfma_f32_16x16x32_bf16 v[176:179], a[200:203], v[0:3], 0\n" \
"ds_read_b64_tr_b16 a[226:227], v215 offset:26624\n" \
"v_mfma_f32_16x16x32_bf16 v[176:179], a[204:207], v[8:11], v[176:179]\n" \
"ds_read_b64_tr_b16 a[228:229], v215 offset:28672\n" \
"s_add_u32 m0, %[m0b], 49152\n" \
"s_cmp_gt_u32 s48, 1\n" \
"s_cselect_b32 s49, 1, 2\n" \
"s_lshl_b32 s49, s49, 18\n" \
"s_or_b32 s49, s49, s59\n" \
"buffer_load_dwordx4 %[wo0], %[srdt], s49 offen offset:0 lds\n" \
"v_mfma_f32_16x16x32_bf16 v[180:183], a[200:203], v[32:35], 0\n" \
"ds_read_b64_tr_b16 a[230:231], v215 offset:30720\n" \
"v_mfma_f32_16x16x32_bf16 v[180:183], a[204:207], v[40:43], v[180:183]\n" \
"v_mfma_f32_16x16x32_bf16 v[184:187], a[200:203], v[64:67], 0\n" \
"v_mfma_f32_16x16x32_bf16 v[184:187], a[204:207], v[72:75], v[184:187]\n" \
"buffer_load_dwordx4 v212, %[srdt], s49 offen offset:1024 lds\n" \
"v_mfma_f32_16x16x32_bf16 v[188:191], a[200:203], v[96:99], 0\n" \
"ds_read_b64_tr_b16 a[232:233], v227 offset:16384\n" \
"v_mfma_f32_16x16x32_bf16 v[188:191], a[204:207], v[104:107], v[188:191]\n" \
"ds_read_b64_tr_b16 a[234:235], v227 offset:18432\n" \
"v_mfma_f32_16x16x32_bf16 v[176:179], a[208:211], v[16:19], v[176:179]\n" \
"ds_read_b64_tr_b16 a[236:237], v227 offset:20480\n" \
"v_mfma_f32_16x16x32_bf16 v[176:179], a[212:215], v[24:27], v[176:179]\n" \
"ds_read_b64_tr_b16 a[238:239], v227 offset:22528\n" \
"v_mfma_f32_16x16x32_bf16 v[180:183], a[208:211], v[48:51], v[180:183]\n" \
"v_mfma_f32_16x16x32_bf16 v[180:183], a[212:215], v[56:59], v[180:183]\n" \
"v_mfma_f32_16x16x32_bf16 v[184:187], a[208:211], v[80:83], v[184:187]\n" \
"v_mfma_f32_16x16x32_bf16 v[184:187], a[212:215], v[88:91], v[184:187]\n" \
"buffer_load_dwordx4 v213, %[srdt], s49 offen offset:2048 lds\n" \
"v_mfma_f32_16x16x32_bf16 v[188:191], a[208:211], v[112:115], v[188:191]\n" \
"ds_read_b64_tr_b16 a[196:197], v227 offset:24576\n" \
"v_mfma_f32_16x16x32_bf16 v[188:191], a[212:215], v[120:123], v[188:191]\n" \
"s_waitcnt vmcnt(63) lgkmcnt(5)\n" \
"s_nop 0\n" \
"v_mfma_f32_16x16x32_bf16 v[4:7], a[216:219], v[0:3], 0\n" \
"ds_read_b64_tr_b16 a[198:199], v227 offset:26624\n" \
"v_mfma_f32_16x16x32_bf16 v[4:7], a[220:223], v[8:11], v[4:7]\n" \
"ds_read_b64_tr_b16 a[200:201], v227 offset:28672\n" \
"v_mfma_f32_16x16x32_bf16 v[36:39], a[216:219], v[32:35], 0\n" \
"ds_read_b64_tr_b16 a[202:203], v227 offset:30720\n" \
"v_mfma_f32_16x16x32_bf16 v[36:39], a[220:223], v[40:43], v[36:39]\n" \
"v_mfma_f32_16x16x32_bf16 v[68:71], a[216:219], v[64:67], 0\n" \
"buffer_load_dwordx4 v214, %[srdt], s49 offen offset:3072 lds\n" \
"v_mfma_f32_16x16x32_bf16 v[68:71], a[220:223], v[72:75], v[68:71]\n" \
"v_mfma_f32_16x16x32_bf16 v[100:103], a[216:219], v[96:99], 0\n" \
"ds_read_b64_tr_b16 a[204:205], v215 offset:16896\n" \
"v_mfma_f32_16x16x32_bf16 v[100:103], a[220:223], v[104:107], v[100:103]\n" \
"ds_read_b64_tr_b16 a[206:207], v215 offset:18944\n" \
"v_mfma_f32_16x16x32_bf16 v[4:7], a[224:227], v[16:19], v[4:7]\n" \
"ds_read_b64_tr_b16 a[208:209], v215 offset:20992\n" \
"v_mfma_f32_16x16x32_bf16 v[4:7], a[228:231], v[24:27], v[4:7]\n" \
"ds_read_b64_tr_b16 a[210:211], v215 offset:23040\n" \
"v_mfma_f32_16x16x32_bf16 v[36:39], a[224:227], v[48:51], v[36:39]\n" \
"v_mfma_f32_16x16x32_bf16 v[36:39], a[228:231], v[56:59], v[36:39]\n" \
"v_mfma_f32_16x16x32_bf16 v[68:71], a[224:227], v[80:83], v[68:71]\n" \
"v_mfma_f32_16x16x32_bf16 v[68:71], a[228:231], v[88:91], v[68:71]\n" \
"v_mfma_f32_16x16x32_bf16 v[100:103], a[224:227], v[112:115], v[100:103]\n" \
"ds_read_b64_tr_b16 a[212:213], v215 offset:25088\n" \
"v_mfma_f32_16x16x32_bf16 v[100:103], a[228:231], v[120:123], v[100:103]\n" \
"s_waitcnt vmcnt(63) lgkmcnt(5)\n" \
"s_nop 0\n" \
"v_mfma_f32_16x16x32_bf16 v[12:15], a[232:235], v[0:3], 0\n" \
"ds_read_b64_tr_b16 a[214:215], v215 offset:27136\n" \
"s_cmp_gt_u32 s48, 0\n" \
"s_cselect_b32 s50, 0, 1\n" \
"s_mul_i32 s50, s50, 262144\n" \
"buffer_load_dwordx4 a[64:67], %[oq0], %[srdz], s50 offen offset:0 sc1\n" \
"v_mfma_f32_16x16x32_bf16 v[12:15], a[236:239], v[8:11], v[12:15]\n" \
"ds_read_b64_tr_b16 a[216:217], v215 offset:29184\n" \
"v_mfma_f32_16x16x32_bf16 v[44:47], a[232:235], v[32:35], 0\n" \
"ds_read_b64_tr_b16 a[218:219], v215 offset:31232\n" \
"buffer_load_dwordx4 a[68:71], %[oq0], %[srdz], s50 offen offset:1024 sc1\n" \
"v_mfma_f32_16x16x32_bf16 v[44:47], a[236:239], v[40:43], v[44:47]\n" \
"v_mfma_f32_16x16x32_bf16 v[76:79], a[232:235], v[64:67], 0\n" \
"buffer_load_dwordx4 a[80:83], v208, %[srdz], s50 offen offset:0 sc1\n" \
"v_mfma_f32_16x16x32_bf16 v[76:79], a[236:239], v[72:75], v[76:79]\n" \
"v_mfma_f32_16x16x32_bf16 v[108:111], a[232:235], v[96:99], 0\n" \
"ds_read_b64_tr_b16 a[220:221], v227 offset:16896\n" \
"buffer_load_dwordx4 a[84:87], v208, %[srdz], s50 offen offset:1024 sc1\n" \
"v_mfma_f32_16x16x32_bf16 v[108:111], a[236:239], v[104:107], v[108:111]\n" \
"ds_read_b64_tr_b16 a[222:223], v227 offset:18944\n" \
"v_mfma_f32_16x16x32_bf16 v[12:15], a[196:199], v[16:19], v[12:15]\n" \
"ds_read_b64_tr_b16 a[224:225], v227 offset:25088\n" \
"buffer_load_dwordx4 a[96:99], v209, %[srdz], s50 offen offset:0 sc1\n" \
"v_mfma_f32_16x16x32_bf16 v[12:15], a[200:203], v[24:27], v[12:15]\n" \
"ds_read_b64_tr_b16 a[226:227], v227 offset:27136\n" \
"v_mfma_f32_16x16x32_bf16 v[44:47], a[196:199], v[48:51], v[44:47]\n" \
"ds_read_b64_tr_b16 a[228:229], v227 offset:20992\n" \
"buffer_load_dwordx4 a[100:103], v209, %[srdz], s50 offen offset:1024 sc1\n" \
"v_mfma_f32_16x16x32_bf16 v[44:47], a[200:203], v[56:59], v[44:47]\n" \
"ds_read_b64_tr_b16 a[230:231], v227 offset:23040\n" \
"v_mfma_f32_16x16x32_bf16 v[76:79], a[196:199], v[80:83], v[76:79]\n" \
"ds_read_b64_tr_b16 a[232:233], v227 offset:29184\n" \
"buffer_load_dwordx4 a[112:115], v210, %[srdz], s50 offen offset:0 sc1\n" \
"v_mfma_f32_16x16x32_bf16 v[76:79], a[200:203], v[88:91], v[76:79]\n" \
"ds_read_b64_tr_b16 a[234:235], v227 offset:31232\n" \
"v_mfma_f32_16x16x32_bf16 v[108:111], a[196:199], v[112:115], v[108:111]\n" \
"ds_read_b64_tr_b16 a[236:237], v215 offset:32768\n" \
"buffer_load_dwordx4 a[116:119], v210, %[srdz], s50 offen offset:1024 sc1\n" \
"v_mfma_f32_16x16x32_bf16 v[108:111], a[200:203], v[120:123], v[108:111]\n" \
"s_waitcnt vmcnt(63) lgkmcnt(13)\n" \
"s_nop 0\n" \
"v_mfma_f32_16x16x32_bf16 v[20:23], a[204:207], v[0:3], 0\n" \
"ds_read_b64_tr_b16 a[238:239], v215 offset:34816\n" \
"buffer_load_dwordx4 a[72:75], %[oq0], %[srdz], s50 offen offset:2048 sc1\n" \
"v_mfma_f32_16x16x32_bf16 v[20:23], a[208:211], v[8:11], v[20:23]\n" \
"ds_read_b64_tr_b16 a[196:197], v215 offset:36864\n" \
"v_mfma_f32_16x16x32_bf16 v[52:55], a[204:207], v[32:35], 0\n" \
"ds_read_b64_tr_b16 a[198:199], v215 offset:38912\n" \
"buffer_load_dwordx4 a[76:79], %[oq0], %[srdz], s50 offen offset:3072 sc1\n" \
"v_mfma_f32_16x16x32_bf16 v[52:55], a[208:211], v[40:43], v[52:55]\n" \
"v_mfma_f32_16x16x32_bf16 v[84:87], a[204:207], v[64:67], 0\n" \
"s_waitcnt vmcnt(63) lgkmcnt(4)\n" \
"s_barrier\n" \
"v_mfma_f32_16x16x32_bf16 v[84:87], a[208:211], v[72:75], v[84:87]\n" \
"buffer_load_dwordx4 a[88:91], v208, %[srdz], s50 offen offset:2048 sc1\n" \
"v_mfma_f32_16x16x32_bf16 v[116:119], a[204:207], v[96:99], 0\n" \
"ds_read_b64_tr_b16 a[200:201], v215 offset:40960\n" \
"v_mfma_f32_16x16x32_bf16 v[116:119], a[208:211], v[104:107], v[116:119]\n" \
"ds_read_b64_tr_b16 a[202:203], v215 offset:43008\n" \
"buffer_load_dwordx4 a[92:95], v208, %[srdz], s50 offen offset:3072 sc1\n" \
"v_mfma_f32_16x16x32_bf16 v[20:23], a[212:215], v[16:19], v[20:23]\n" \
"ds_read_b64_tr_b16 a[204:205], v215 offset:45056\n" \
"v_mfma_f32_16x16x32_bf16 v[20:23], a[216:219], v[24:27], v[20:23]\n" \
"ds_read_b64_tr_b16 a[206:207], v215 offset:47104\n" \
"buffer_load_dwordx4 a[104:107], v209, %[srdz], s50 offen offset:2048 sc1\n" \
"v_mfma_f32_16x16x32_bf16 v[52:55], a[212:215], v[48:51], v[52:55]\n" \
"v_mfma_f32_16x16x32_bf16 v[52:55], a[216:219], v[56:59], v[52:55]\n" \
"buffer_load_dwordx4 a[108:111], v209, %[srdz], s50 offen offset:3072 sc1\n" \
"v_mfma_f32_16x16x32_bf16 v[84:87], a[212:215], v[80:83], v[84:87]\n" \
"v_mfma_f32_16x16x32_bf16 v[84:87], a[216:219], v[88:91], v[84:87]\n" \
"buffer_load_dwordx4 a[120:123], v210, %[srdz], s50 offen offset:2048 sc1\n" \
"v_mfma_f32_16x16x32_bf16 v[116:119], a[212:215], v[112:115], v[116:119]\n" \
"v_mfma_f32_16x16x32_bf16 v[116:119], a[216:219], v[120:123], v[116:119]\n" \
"s_waitcnt vmcnt(63) lgkmcnt(10)\n" \
"s_nop 0\n" \
"v_mfma_f32_16x16x32_bf16 v[28:31], a[220:223], v[0:3], 0\n" \
"buffer_load_dwordx4 a[124:127], v210, %[srdz], s50 offen offset:3072 sc1\n" \
"v_mfma_f32_16x16x32_bf16 v[28:31], a[228:231], v[8:11], v[28:31]\n" \
"s_sub_u32 s61, s48, 5\n" \
"s_cmp_lt_u32 s61, 3\n" \
"s_cselect_b64 vcc, -1, 0\n" \
"s_nop 0\n" \
"v_mfma_f32_16x16x32_bf16 v[60:63], a[220:223], v[32:35], 0\n" \
"s_cbranch_vccz Lg5_%=\n" \
"s_nop 0\n" \
"buffer_store_dwordx4 v[0:3], %[oq0], %[srdz], s60 offen offset:0 sc1\n" \
"Lg5_%=:\n" \
"v_mfma_f32_16x16x32_bf16 v[60:63], a[228:231], v[40:43], v[60:63]\n" \
"v_mfma_f32_16x16x32_bf16 v[92:95], a[220:223], v[64:67], 0\n" \
"s_cmp_gt_u32 s48, 1\n" \
"s_cselect_b32 s50, 1, 2\n" \
"s_mul_i32 s50, s50, 262144\n" \
"buffer_load_dwordx4 a[0:3], %[oq0], %[srdz], s50 offen offset:0 sc1\n" \
"v_mfma_f32_16x16x32_bf16 v[92:95], a[228:231], v[72:75], v[92:95]\n" \
"v_mfma_f32_16x16x32_bf16 v[124:127], a[220:223], v[96:99], 0\n" \
"ds_read_b64_tr_b16 a[208:209], v227 offset:32768\n" \
"v_mfma_f32_16x16x32_bf16 v[124:127], a[228:231], v[104:107], v[124:127]\n" \
"ds_read_b64_tr_b16 a[210:211], v227 offset:34816\n" \
"s_waitcnt vmcnt(15) lgkmcnt(6)\n" \
"s_nop 0\n" \
"v_mfma_f32_16x16x32_bf16 v[128:131], a[236:239], a[64:67], v[192:195]\n" \
"ds_read_b64_tr_b16 a[212:213], v227 offset:36864\n" \
"v_mfma_f32_16x16x32_bf16 v[128:131], a[196:199], a[68:71], v[128:131]\n" \
"ds_read_b64_tr_b16 a[214:215], v227 offset:38912\n" \
"buffer_load_dwordx4 a[4:7], %[oq0], %[srdz], s50 offen offset:1024 sc1\n" \
"s_waitcnt vmcnt(14) lgkmcnt(15)\n" \
"s_nop 0\n" \
"v_mfma_f32_16x16x32_bf16 v[132:135], a[236:239], a[80:83], v[196:199]\n" \
"v_mfma_f32_16x16x32_bf16 v[132:135], a[196:199], a[84:87], v[132:135]\n" \
"buffer_load_dwordx4 a[8:11], %[oq0], %[srdz], s50 offen offset:2048 sc1\n" \
"s_waitcnt vmcnt(13) lgkmcnt(15)\n" \
"s_nop 0\n" \
"v_mfma_f32_16x16x32_bf16 v[136:139], a[236:239], a[96:99], v[200:203]\n" \
"v_mfma_f32_16x16x32_bf16 v[136:139], a[196:199], a[100:103], v[136:139]\n" \
"s_cbranch_vccz Lg6_%=\n" \
"s_nop 0\n" \
"buffer_store_dwordx4 v[8:11], %[oq0], %[srdz], s60 offen offset:1024 sc1\n" \
"Lg6_%=:\n" \
"s_waitcnt vmcnt(13) lgkmcnt(15)\n" \
"s_nop 0\n" \
"s_bitcmp1_b32 0xe0, s48\n" \
"s_cbranch_scc1 Lcw2_%=\n" \
"s_waitcnt vmcnt(11)\n" \
"Lcw2_%=:\n" \
"v_mfma_f32_16x16x32_bf16 v[140:143], a[236:239], a[112:115], v[204:207]\n" \
"ds_read_b64_tr_b16 a[216:217], v227 offset:40960\n" \
"v_mfma_f32_16x16x32_bf16 v[140:143], a[196:199], a[116:119], v[140:143]\n" \
"s_waitcnt vmcnt(11) lgkmcnt(5)\n" \
"s_nop 0\n" \
"s_bitcmp1_b32 0xe0, s48\n" \
"s_cbranch_scc1 Lcw3_%=\n" \
"s_waitcnt vmcnt(9)\n" \
"Lcw3_%=:\n" \
"v_mfma_f32_16x16x32_bf16 v[128:131], a[200:203], a[72:75], v[128:131]\n" \
"ds_read_b64_tr_b16 a[218:219], v227 offset:43008\n" \
"v_mfma_f32_16x16x32_bf16 v[128:131], a[204:207], a[76:79], v[128:131]\n" \
"ds_read_b64_tr_b16 a[220:221], v227 offset:45056\n" \
"buffer_load_dwordx4 a[12:15], %[oq0], %[srdz], s50 offen offset:3072 sc1\n" \
"s_waitcnt vmcnt(10) lgkmcnt(15)\n" \
"s_nop 0\n" \
"s_bitcmp1_b32 0xe0, s48\n" \
"s_cbranch_scc1 Lcw4_%=\n" \
"s_waitcnt vmcnt(8)\n" \
"Lcw4_%=:\n" \
"v_mfma_f32_16x16x32_bf16 v[132:135], a[200:203], a[88:91], v[132:135]\n" \
"ds_read_b64_tr_b16 a[222:223], v227 offset:47104\n" \
"v_mfma_f32_16x16x32_bf16 v[132:135], a[204:207], a[92:95], v[132:135]\n" \
"buffer_load_dwordx4 a[16:19], v208, %[srdz], s50 offen offset:0 sc1\n" \
"s_waitcnt vmcnt(7) lgkmcnt(14)\n" \
"s_nop 0\n" \
"s_bitcmp1_b32 0xe0, s48\n" \
"s_cbranch_scc1 Lcw5_%=\n" \
"s_waitcnt vmcnt(5)\n" \
"Lcw5_%=:\n" \
"v_mfma_f32_16x16x32_bf16 v[136:139], a[200:203], a[104:107], v[136:139]\n" \
"v_mfma_f32_16x16x32_bf16 v[136:139], a[204:207], a[108:111], v[136:139]\n" \
"v_mfma_f32_16x16x32_bf16 v[140:143], a[200:203], a[120:123], v[140:143]\n" \
"ds_read_b64_tr_b16 a[228:229], v215 offset:33280\n" \
"v_mfma_f32_16x16x32_bf16 v[140:143], a[204:207], a[124:127], v[140:143]\n" \
"ds_read_b64_tr_b16 a[230:231], v215 offset:35328\n" \
"v_mfma_f32_16x16x32_bf16 v[28:31], a[224:227], v[16:19], v[28:31]\n" \
"v_mfma_f32_16x16x32_bf16 v[28:31], a[232:235], v[24:27], v[28:31]\n" \
"s_cbranch_vccz Lg7_%=\n" \
"s_nop 0\n" \
"buffer_store_dwordx4 v[16:19], %[oq0], %[srdz], s60 offen offset:2048 sc1\n" \
"Lg7_%=:\n" \
"v_mfma_f32_16x16x32_bf16 v[60:63], a[224:227], v[48:51], v[60:63]\n" \
"v_mfma_f32_16x16x32_bf16 v[60:63], a[232:235], v[56:59], v[60:63]\n" \
"buffer_load_dwordx4 a[20:23], v208, %[srdz], s50 offen offset:1024 sc1\n" \
"v_mfma_f32_16x16x32_bf16 v[92:95], a[224:227], v[80:83], v[92:95]\n" \
"v_mfma_f32_16x16x32_bf16 v[92:95], a[232:235], v[88:91], v[92:95]\n" \
"v_cmp_u_f32_e64 s[44:45], v128, v132\n" \
"buffer_load_dwordx4 a[24:27], v208, %[srdz], s50 offen offset:2048 sc1\n" \
"v_mfma_f32_16x16x32_bf16 v[124:127], a[224:227], v[112:115], v[124:127]\n" \
"v_mfma_f32_16x16x32_bf16 v[124:127], a[232:235], v[120:123], v[124:127]\n" \
"v_cmp_u_f32_e64 s[56:57], v136, v140\n" \
"s_or_b64 s[56:57], s[56:57], s[44:45]\n" \
"s_cbranch_scc1 Lsp1_%=\n" \
"Lbk1_%=:\n" \
"s_waitcnt vmcnt(63) lgkmcnt(2)\n" \
"s_nop 0\n" \
"v_mfma_f32_16x16x32_bf16 v[144:147], a[208:211], a[64:67], v[144:147]\n" \
"ds_read_b64_tr_b16 a[224:225], v215 offset:37376\n" \
"v_mfma_f32_16x16x32_bf16 v[144:147], a[212:215], a[68:71], v[144:147]\n" \
"ds_read_b64_tr_b16 a[226:227], v215 offset:39424\n" \
"buffer_load_dwordx4 a[28:31], v208, %[srdz], s50 offen offset:3072 sc1\n" \
"v_mfma_f32_16x16x32_bf16 v[148:151], a[208:211], a[80:83], v[148:151]\n" \
"ds_read_b64_tr_b16 a[196:197], v215 offset:41472\n" \
"v_mfma_f32_16x16x32_bf16 v[148:151], a[212:215], a[84:87], v[148:151]\n" \
"ds_read_b64_tr_b16 a[198:199], v215 offset:43520\n" \
"v_mfma_f32_16x16x32_bf16 v[152:155], a[208:211], a[96:99], v[152:155]\n" \
"ds_read_b64_tr_b16 a[200:201], v215 offset:45568\n" \
"s_cbranch_vccz Lg8_%=\n" \
"s_nop 0\n" \
"buffer_store_dwordx4 v[24:27], %[oq0], %[srdz], s60 offen offset:3072 sc1\n" \
"Lg8_%=:\n" \
"v_mfma_f32_16x16x32_bf16 v[152:155], a[212:215], a[100:103], v[152:155]\n" \
"ds_read_b64_tr_b16 a[202:203], v215 offset:47616\n" \
"v_mfma_f32_16x16x32_bf16 v[156:159], a[208:211], a[112:115], v[156:159]\n" \
"ds_read_b64_tr_b16 a[204:205], v227 offset:33280\n" \
"buffer_load_dwordx4 a[32:35], v209, %[srdz], s50 offen offset:0 sc1\n" \
"v_mfma_f32_16x16x32_bf16 v[156:159], a[212:215], a[116:119], v[156:159]\n" \
"ds_read_b64_tr_b16 a[206:207], v227 offset:35328\n" \
"v_mfma_f32_16x16x32_bf16 v[144:147], a[216:219], a[72:75], v[144:147]\n" \
"ds_read_b64_tr_b16 a[232:233], v227 offset:37376\n" \
"v_mfma_f32_16x16x32_bf16 v[144:147], a[220:223], a[76:79], v[144:147]\n" \
"ds_read_b64_tr_b16 a[234:235], v227 offset:39424\n" \
"v_mfma_f32_16x16x32_bf16 v[148:151], a[216:219], a[88:91], v[148:151]\n" \
"buffer_load_dwordx4 a[36:39], v209, %[srdz], s50 offen offset:1024 sc1\n" \
"v_mfma_f32_16x16x32_bf16 v[148:151], a[220:223], a[92:95], v[148:151]\n" \
"v_mfma_f32_16x16x32_bf16 v[152:155], a[216:219], a[104:107], v[152:155]\n" \
"buffer_load_dwordx4 a[40:43], v209, %[srdz], s50 offen offset:2048 sc1\n" \
"v_mfma_f32_16x16x32_bf16 v[152:155], a[220:223], a[108:111], v[152:155]\n" \
"v_mfma_f32_16x16x32_bf16 v[156:159], a[216:219], a[120:123], v[156:159]\n" \
"ds_read_b64_tr_b16 a[236:237], v227 offset:41472\n" \
"v_mfma_f32_16x16x32_bf16 v[156:159], a[220:223], a[124:127], v[156:159]\n" \
"s_waitcnt vmcnt(63) lgkmcnt(5)\n" \
"s_nop 0\n" \
"v_mfma_f32_16x16x32_bf16 v[160:163], a[228:231], a[64:67], v[160:163]\n" \
"ds_read_b64_tr_b16 a[238:239], v227 offset:43520\n" \
"s_cbranch_vccz Lg9_%=\n" \
"s_nop 0\n" \
"buffer_store_dwordx4 v[32:35], v208, %[srdz], s60 offen offset:0 sc1\n" \
"Lg9_%=:\n" \
"v_mfma_f32_16x16x32_bf16 v[160:163], a[224:227], a[68:71], v[160:163]\n" \
"ds_read_b64_tr_b16 a[208:209], v227 offset:45568\n" \
"v_mfma_f32_16x16x32_bf16 v[164:167], a[228:231], a[80:83], v[164:167]\n" \
"ds_read_b64_tr_b16 a[210:211], v227 offset:47616\n" \
"buffer_load_dwordx4 a[44:47], v209, %[srdz], s50 offen offset:3072 sc1\n" \
"v_mfma_f32_16x16x32_bf16 v[164:167], a[224:227], a[84:87], v[164:167]\n" \
"v_mfma_f32_16x16x32_bf16 v[168:171], a[228:231], a[96:99], v[168:171]\n" \
"buffer_load_dwordx4 a[48:51], v210, %[srdz], s50 offen offset:0 sc1\n" \
"v_mfma_f32_16x16x32_bf16 v[168:171], a[224:227], a[100:103], v[168:171]\n" \
"v_mfma_f32_16x16x32_bf16 v[172:175], a[228:231], a[112:115], v[172:175]\n" \
"v_mfma_f32_16x16x32_bf16 v[172:175], a[224:227], a[116:119], v[172:175]\n" \
"buffer_load_dwordx4 a[52:55], v210, %[srdz], s50 offen offset:1024 sc1\n" \
"v_mfma_f32_16x16x32_bf16 v[160:163], a[196:199], a[72:75], v[160:163]\n" \
"v_mfma_f32_16x16x32_bf16 v[160:163], a[200:203], a[76:79], v[160:163]\n" \
"v_mfma_f32_16x16x32_bf16 v[164:167], a[196:199], a[88:91], v[164:167]\n" \
"v_mfma_f32_16x16x32_bf16 v[164:167], a[200:203], a[92:95], v[164:167]\n" \
"buffer_load_dwordx4 a[56:59], v210, %[srdz], s50 offen offset:2048 sc1\n" \
"v_mfma_f32_16x16x32_bf16 v[168:171], a[196:199], a[104:107], v[168:171]\n" \
"s_waitcnt vmcnt(55) lgkmcnt(0)\n" \
"s_bitcmp1_b32 0x1f, s48\n" \
"s_cbranch_scc1 Lcw6_%=\n" \
"s_waitcnt vmcnt(44)\n" \
"s_bitcmp1_b32 0xe0, s48\n" \
"s_cbranch_scc1 Lcw6_%=\n" \
"s_waitcnt vmcnt(39)\n" \
"Lcw6_%=:\n" \
"s_barrier\n" \
"s_nop 0\n" \
"v_mfma_f32_16x16x32_bf16 v[168:171], a[200:203], a[108:111], v[168:171]\n" \
"ds_read_b64_tr_b16 a[212:213], v220 offset:32768\n" \
"s_cbranch_vccz Lg10_%=\n" \
"s_nop 0\n" \
"buffer_store_dwordx4 v[40:43], v208, %[srdz], s60 offen offset:1024 sc1\n" \
"Lg10_%=:\n" \
"v_mfma_f32_16x16x32_bf16 v[172:175], a[196:199], a[120:123], v[172:175]\n" \
"ds_read_b64_tr_b16 a[214:215], v220 offset:34816\n" \
"v_mfma_f32_16x16x32_bf16 v[172:175], a[200:203], a[124:127], v[172:175]\n" \
"s_waitcnt vmcnt(63) lgkmcnt(2)\n" \
"s_nop 0\n" \
"v_mfma_f32_16x16x32_bf16 v[176:179], a[204:207], a[64:67], v[176:179]\n" \
"ds_read_b64_tr_b16 a[216:217], v220 offset:36864\n" \
"v_mfma_f32_16x16x32_bf16 v[176:179], a[232:235], a[68:71], v[176:179]\n" \
"ds_read_b64_tr_b16 a[218:219], v220 offset:38912\n" \
"v_mfma_f32_16x16x32_bf16 v[180:183], a[204:207], a[80:83], v[180:183]\n" \
"ds_read_b64_tr_b16 a[220:221], v220 offset:40960\n" \
"buffer_load_dwordx4 a[60:63], v210, %[srdz], s50 offen offset:3072 sc1\n" \
"v_mfma_f32_16x16x32_bf16 v[180:183], a[232:235], a[84:87], v[180:183]\n" \
"ds_read_b64_tr_b16 a[222:223], v220 offset:43008\n" \
"v_mfma_f32_16x16x32_bf16 v[184:187], a[204:207], a[96:99], v[184:187]\n" \
"ds_read_b64_tr_b16 a[228:229], v220 offset:45056\n" \
"v_mfma_f32_16x16x32_bf16 v[184:187], a[232:235], a[100:103], v[184:187]\n" \
"ds_read_b64_tr_b16 a[230:231], v220 offset:47104\n" \
"v_mfma_f32_16x16x32_bf16 v[188:191], a[204:207], a[112:115], v[188:191]\n" \
"ds_read_b64_tr_b16 a[224:225], v221 offset:32768\n" \
"v_mfma_f32_16x16x32_bf16 v[188:191], a[232:235], a[116:119], v[188:191]\n" \
"ds_read_b64_tr_b16 a[226:227], v221 offset:34816\n" \
"s_cbranch_vccz Lg11_%=\n" \
"s_nop 0\n" \
"buffer_store_dwordx4 v[48:51], v208, %[srdz], s60 offen offset:2048 sc1\n" \
"Lg11_%=:\n" \
"v_mfma_f32_16x16x32_bf16 v[176:179], a[236:239], a[72:75], v[176:179]\n" \
"ds_read_b64_tr_b16 a[196:197], v221 offset:36864\n" \
"v_mfma_f32_16x16x32_bf16 v[176:179], a[208:211], a[76:79], v[176:179]\n" \
"ds_read_b64_tr_b16 a[198:199], v221 offset:38912\n" \
"v_mfma_f32_16x16x32_bf16 v[180:183], a[236:239], a[88:91], v[180:183]\n" \
"s_cmp_gt_u32 s48, 2\n" \
"s_cselect_b32 s50, 2, 3\n" \
"s_mul_i32 s50, s50, 262144\n" \
"buffer_load_dwordx4 a[128:131], %[oq0], %[srdz], s50 offen offset:0 sc1\n" \
"v_mfma_f32_16x16x32_bf16 v[180:183], a[208:211], a[92:95], v[180:183]\n" \
"v_mfma_f32_16x16x32_bf16 v[184:187], a[236:239], a[104:107], v[184:187]\n" \
"v_mfma_f32_16x16x32_bf16 v[184:187], a[208:211], a[108:111], v[184:187]\n" \
"v_mfma_f32_16x16x32_bf16 v[188:191], a[236:239], a[120:123], v[188:191]\n" \
"ds_read_b64_tr_b16 a[200:201], v221 offset:40960\n" \
"v_mfma_f32_16x16x32_bf16 v[188:191], a[208:211], a[124:127], v[188:191]\n" \
"s_waitcnt vmcnt(63) lgkmcnt(5)\n" \
"s_nop 0\n" \
"v_mfma_f32_16x16x32_bf16 v[4:7], a[212:215], a[64:67], v[4:7]\n" \
"ds_read_b64_tr_b16 a[202:203], v221 offset:43008\n" \
"v_mfma_f32_16x16x32_bf16 v[4:7], a[216:219], a[68:71], v[4:7]\n" \
"ds_read_b64_tr_b16 a[204:205], v221 offset:45056\n" \
"s_add_u32 m0, %[m0b], 0\n" \
"s_cmp_gt_u32 s48, 2\n" \
"s_cselect_b32 s49, 2, 3\n" \
"s_lshl_b32 s49, s49, 18\n" \
"s_or_b32 s49, s49, s58\n" \
"s_nop 0\n" \
"buffer_load_dwordx4 %[wo0], %[srdt], s49 offen offset:0 lds\n" \
"v_mfma_f32_16x16x32_bf16 v[36:39], a[212:215], a[80:83], v[36:39]\n" \
"ds_read_b64_tr_b16 a[206:207], v221 offset:47104\n" \
"v_mfma_f32_16x16x32_bf16 v[36:39], a[216:219], a[84:87], v[36:39]\n" \
"s_cbranch_vccz Lg12_%=\n" \
"s_nop 0\n" \
"buffer_store_dwordx4 v[56:59], v208, %[srdz], s60 offen offset:3072 sc1\n" \
"Lg12_%=:\n" \
"v_mfma_f32_16x16x32_bf16 v[68:71], a[212:215], a[96:99], v[68:71]\n" \
"v_mfma_f32_16x16x32_bf16 v[68:71], a[216:219], a[100:103], v[68:71]\n" \
"buffer_load_dwordx4 a[132:135], %[oq0], %[srdz], s50 offen offset:1024 sc1\n" \
"v_mfma_f32_16x16x32_bf16 v[100:103], a[212:215], a[112:115], v[100:103]\n" \
"ds_read_b64_tr_b16 a[232:233], v220 offset:33280\n" \
"v_mfma_f32_16x16x32_bf16 v[100:103], a[216:219], a[116:119], v[100:103]\n" \
"ds_read_b64_tr_b16 a[234:235], v220 offset:35328\n" \
"v_mfma_f32_16x16x32_bf16 v[4:7], a[220:223], a[72:75], v[4:7]\n" \
"ds_read_b64_tr_b16 a[236:237], v220 offset:37376\n" \
"v_mfma_f32_16x16x32_bf16 v[4:7], a[228:231], a[76:79], v[4:7]\n" \
"ds_read_b64_tr_b16 a[238:239], v220 offset:39424\n" \
"buffer_load_dwordx4 a[136:139], %[oq0], %[srdz], s50 offen offset:2048 sc1\n" \
"v_mfma_f32_16x16x32_bf16 v[36:39], a[220:223], a[88:91], v[36:39]\n" \
"v_mfma_f32_16x16x32_bf16 v[36:39], a[228:231], a[92:95], v[36:39]\n" \
"v_mfma_f32_16x16x32_bf16 v[68:71], a[220:223], a[104:107], v[68:71]\n" \
"v_mfma_f32_16x16x32_bf16 v[68:71], a[228:231], a[108:111], v[68:71]\n" \
"buffer_load_dwordx4 a[140:143], %[oq0], %[srdz], s50 offen offset:3072 sc1\n" \
"v_mfma_f32_16x16x32_bf16 v[100:103], a[220:223], a[120:123], v[100:103]\n" \
"ds_read_b64_tr_b16 a[208:209], v220 offset:41472\n" \
"v_mfma_f32_16x16x32_bf16 v[100:103], a[228:231], a[124:127], v[100:103]\n" \
"s_waitcnt vmcnt(63) lgkmcnt(5)\n" \
"s_nop 0\n" \
"v_mfma_f32_16x16x32_bf16 v[12:15], a[224:227], a[64:67], v[12:15]\n" \
".if . - adaln_body != 45056\n" \
".error \"size model\"\n" \
".endif\n" \
"Lk10_%=:\n" \
"s_cbranch_execz Lk11_%=\n" \
"s_nop 0\n" \
"ds_read_b64_tr_b16 a[210:211], v220 offset:43520\n" \
"s_cbranch_vccz Lg13_%=\n" \
"s_nop 0\n" \
"buffer_store_dwordx4 v[64:67], v209, %[srdz], s60 offen offset:0 sc1\n" \
"Lg13_%=:\n" \
"v_mfma_f32_16x16x32_bf16 v[12:15], a[196:199], a[68:71], v[12:15]\n" \
"ds_read_b64_tr_b16 a[212:213], v220 offset:45568\n" \
"v_mfma_f32_16x16x32_bf16 v[44:47], a[224:227], a[80:83], v[44:47]\n" \
"ds_read_b64_tr_b16 a[214:215], v220 offset:47616\n" \
"buffer_load_dwordx4 v212, %[srdt], s49 offen offset:1024 lds\n" \
"v_mfma_f32_16x16x32_bf16 v[44:47], a[196:199], a[84:87], v[44:47]\n" \
"v_mfma_f32_16x16x32_bf16 v[76:79], a[224:227], a[96:99], v[76:79]\n" \
"v_mfma_f32_16x16x32_bf16 v[76:79], a[196:199], a[100:103], v[76:79]\n" \
"buffer_load_dwordx4 a[144:147], v208, %[srdz], s50 offen offset:0 sc1\n" \
"v_mfma_f32_16x16x32_bf16 v[108:111], a[224:227], a[112:115], v[108:111]\n" \
"ds_read_b64_tr_b16 a[216:217], v221 offset:33280\n" \
"v_mfma_f32_16x16x32_bf16 v[108:111], a[196:199], a[116:119], v[108:111]\n" \
"ds_read_b64_tr_b16 a[218:219], v221 offset:35328\n" \
"v_mfma_f32_16x16x32_bf16 v[12:15], a[200:203], a[72:75], v[12:15]\n" \
"ds_read_b64_tr_b16 a[220:221], v221 offset:41472\n" \
"v_mfma_f32_16x16x32_bf16 v[12:15], a[204:207], a[76:79], v[12:15]\n" \
"ds_read_b64_tr_b16 a[222:223], v221 offset:43520\n" \
"v_mfma_f32_16x16x32_bf16 v[44:47], a[200:203], a[88:91], v[44:47]\n" \
"ds_read_b64_tr_b16 a[228:229], v221 offset:37376\n" \
"s_cbranch_vccz Lg14_%=\n" \
"s_nop 0\n" \
"buffer_store_dwordx4 v[72:75], v209, %[srdz], s60 offen offset:1024 sc1\n" \
"Lg14_%=:\n" \
"v_mfma_f32_16x16x32_bf16 v[44:47], a[204:207], a[92:95], v[44:47]\n" \
"ds_read_b64_tr_b16 a[230:231], v221 offset:39424\n" \
"v_mfma_f32_16x16x32_bf16 v[76:79], a[200:203], a[104:107], v[76:79]\n" \
"ds_read_b64_tr_b16 a[224:225], v221 offset:45568\n" \
"v_mfma_f32_16x16x32_bf16 v[76:79], a[204:207], a[108:111], v[76:79]\n" \
"ds_read_b64_tr_b16 a[226:227], v221 offset:47616\n" \
"v_mfma_f32_16x16x32_bf16 v[108:111], a[200:203], a[120:123], v[108:111]\n" \
"buffer_load_dwordx4 a[148:151], v208, %[srdz], s50 offen offset:1024 sc1\n" \
"v_mfma_f32_16x16x32_bf16 v[108:111], a[204:207], a[124:127], v[108:111]\n" \
"s_waitcnt vmcnt(63) lgkmcnt(12)\n" \
"s_nop 0\n" \
"v_mfma_f32_16x16x32_bf16 v[20:23], a[232:235], a[64:67], v[20:23]\n" \
"v_mfma_f32_16x16x32_bf16 v[20:23], a[236:239], a[68:71], v[20:23]\n" \
"buffer_load_dwordx4 v213, %[srdt], s49 offen offset:2048 lds\n" \
"v_mfma_f32_16x16x32_bf16 v[52:55], a[232:235], a[80:83], v[52:55]\n" \
"v_mfma_f32_16x16x32_bf16 v[52:55], a[236:239], a[84:87], v[52:55]\n" \
"buffer_load_dwordx4 a[152:155], v208, %[srdz], s50 offen offset:2048 sc1\n" \
"v_mfma_f32_16x16x32_bf16 v[84:87], a[232:235], a[96:99], v[84:87]\n" \
"s_waitcnt vmcnt(56) lgkmcnt(0)\n" \
"s_nop 0\n" \
"s_bitcmp1_b32 0xe0, s48\n" \
"s_cbranch_scc1 Lcw7_%=\n" \
"s_waitcnt vmcnt(46)\n" \
"Lcw7_%=:\n" \
"s_barrier\n" \
"s_nop 0\n" \
"v_mfma_f32_16x16x32_bf16 v[84:87], a[236:239], a[100:103], v[84:87]\n" \
"ds_read_b64_tr_b16 a[196:197], v220 offset:49152\n" \
"v_mfma_f32_16x16x32_bf16 v[116:119], a[232:235], a[112:115], v[116:119]\n" \
"ds_read_b64_tr_b16 a[198:199], v220 offset:51200\n" \
"s_cbranch_vccz Lg15_%=\n" \
"s_nop 0\n" \
"buffer_store_dwordx4 v[80:83], v209, %[srdz], s60 offen offset:2048 sc1\n" \
"Lg15_%=:\n" \
"v_mfma_f32_16x16x32_bf16 v[116:119], a[236:239], a[116:119], v[116:119]\n" \
"ds_read_b64_tr_b16 a[200:201], v220 offset:53248\n" \
"v_mfma_f32_16x16x32_bf16 v[20:23], a[208:211], a[72:75], v[20:23]\n" \
"ds_read_b64_tr_b16 a[202:203], v220 offset:55296\n" \
"v_mfma_f32_16x16x32_bf16 v[20:23], a[212:215], a[76:79], v[20:23]\n" \
"ds_read_b64_tr_b16 a[204:205], v220 offset:57344\n" \
"v_mfma_f32_16x16x32_bf16 v[52:55], a[208:211], a[88:91], v[52:55]\n" \
"ds_read_b64_tr_b16 a[206:207], v220 offset:59392\n" \
"v_mfma_f32_16x16x32_bf16 v[52:55], a[212:215], a[92:95], v[52:55]\n" \
"ds_read_b64_tr_b16 a[232:233], v220 offset:61440\n" \
"v_mfma_f32_16x16x32_bf16 v[84:87], a[208:211], a[104:107], v[84:87]\n" \
"ds_read_b64_tr_b16 a[234:235], v220 offset:63488\n" \
"v_mfma_f32_16x16x32_bf16 v[84:87], a[212:215], a[108:111], v[84:87]\n" \
"buffer_load_dwordx4 a[156:159], v208, %[srdz], s50 offen offset:3072 sc1\n" \
"v_mfma_f32_16x16x32_bf16 v[116:119], a[208:211], a[120:123], v[116:119]\n" \
"v_mfma_f32_16x16x32_bf16 v[116:119], a[212:215], a[124:127], v[116:119]\n" \
"s_waitcnt vmcnt(19) lgkmcnt(4)\n" \
"s_nop 0\n" \
"s_bitcmp1_b32 0xe0, s48\n" \
"s_cbranch_scc1 Lcw8_%=\n" \
"s_waitcnt vmcnt(13)\n" \
"Lcw8_%=:\n" \
"v_mfma_f32_16x16x32_bf16 v[28:31], a[216:219], a[64:67], v[28:31]\n" \
"v_mfma_f32_16x16x32_bf16 v[28:31], a[228:231], a[68:71], v[28:31]\n" \
"s_cbranch_vccz Lg16_%=\n" \
"s_nop 0\n" \
"buffer_store_dwordx4 v[88:91], v209, %[srdz], s60 offen offset:3072 sc1\n" \
"Lg16_%=:\n" \
"v_mfma_f32_16x16x32_bf16 v[60:63], a[216:219], a[80:83], v[60:63]\n" \
"v_mfma_f32_16x16x32_bf16 v[60:63], a[228:231], a[84:87], v[60:63]\n" \
"buffer_load_dwordx4 v214, %[srdt], s49 offen offset:3072 lds\n" \
"v_mfma_f32_16x16x32_bf16 v[92:95], a[216:219], a[96:99], v[92:95]\n" \
"v_mfma_f32_16x16x32_bf16 v[92:95], a[228:231], a[100:103], v[92:95]\n" \
"buffer_load_dwordx4 a[160:163], v209, %[srdz], s50 offen offset:0 sc1\n" \
"v_mfma_f32_16x16x32_bf16 v[124:127], a[216:219], a[112:115], v[124:127]\n" \
"ds_read_b64_tr_b16 a[236:237], v221 offset:49152\n" \
"v_mfma_f32_16x16x32_bf16 v[124:127], a[228:231], a[116:119], v[124:127]\n" \
"ds_read_b64_tr_b16 a[238:239], v221 offset:51200\n" \
"v_mfma_f32_16x16x32_bf16 v[192:195], a[196:199], a[0:3], v[128:131]\n" \
"ds_read_b64_tr_b16 a[208:209], v221 offset:53248\n" \
"v_mfma_f32_16x16x32_bf16 v[192:195], a[200:203], a[4:7], v[192:195]\n" \
"ds_read_b64_tr_b16 a[210:211], v221 offset:55296\n" \
"v_mfma_f32_16x16x32_bf16 v[196:199], a[196:199], a[16:19], v[132:135]\n" \
"buffer_load_dwordx4 a[164:167], v209, %[srdz], s50 offen offset:1024 sc1\n" \
"v_mfma_f32_16x16x32_bf16 v[196:199], a[200:203], a[20:23], v[196:199]\n" \
"v_mfma_f32_16x16x32_bf16 v[200:203], a[196:199], a[32:35], v[136:139]\n" \
"v_mfma_f32_16x16x32_bf16 v[200:203], a[200:203], a[36:39], v[200:203]\n" \
"s_cbranch_vccz Lg17_%=\n" \
"s_nop 0\n" \
"buffer_store_dwordx4 v[96:99], v210, %[srdz], s60 offen offset:0 sc1\n" \
"Lg17_%=:\n" \
"v_mfma_f32_16x16x32_bf16 v[204:207], a[196:199], a[48:51], v[140:143]\n" \
"ds_read_b64_tr_b16 a[212:213], v221 offset:57344\n" \
"v_mfma_f32_16x16x32_bf16 v[204:207], a[200:203], a[52:55], v[204:207]\n" \
"s_waitcnt vmcnt(21) lgkmcnt(5)\n" \
"s_nop 0\n" \
"s_bitcmp1_b32 0xe0, s48\n" \
"s_cbranch_scc1 Lcw9_%=\n" \
"s_waitcnt vmcnt(14)\n" \
"Lcw9_%=:\n" \
"v_mfma_f32_16x16x32_bf16 v[192:195], a[204:207], a[8:11], v[192:195]\n" \
"ds_read_b64_tr_b16 a[214:215], v221 offset:59392\n" \
"v_mfma_f32_16x16x32_bf16 v[192:195], a[232:235], a[12:15], v[192:195]\n" \
"ds_read_b64_tr_b16 a[216:217], v221 offset:61440\n" \
"v_mfma_f32_16x16x32_bf16 v[196:199], a[204:207], a[24:27], v[196:199]\n" \
"ds_read_b64_tr_b16 a[218:219], v221 offset:63488\n" \
"v_mfma_f32_16x16x32_bf16 v[196:199], a[232:235], a[28:31], v[196:199]\n" \
"buffer_load_dwordx4 a[168:171], v209, %[srdz], s50 offen offset:2048 sc1\n" \
"v_mfma_f32_16x16x32_bf16 v[200:203], a[204:207], a[40:43], v[200:203]\n" \
"v_mfma_f32_16x16x32_bf16 v[200:203], a[232:235], a[44:47], v[200:203]\n" \
"v_mfma_f32_16x16x32_bf16 v[204:207], a[204:207], a[56:59], v[204:207]\n" \
"ds_read_b64_tr_b16 a[228:229], v220 offset:49664\n" \
"v_mfma_f32_16x16x32_bf16 v[204:207], a[232:235], a[60:63], v[204:207]\n" \
"ds_read_b64_tr_b16 a[230:231], v220 offset:51712\n" \
"v_mfma_f32_16x16x32_bf16 v[28:31], a[220:223], a[72:75], v[28:31]\n" \
"v_mfma_f32_16x16x32_bf16 v[28:31], a[224:227], a[76:79], v[28:31]\n" \
"s_cbranch_vccz Lg18_%=\n" \
"s_nop 0\n" \
"buffer_store_dwordx4 v[104:107], v210, %[srdz], s60 offen offset:1024 sc1\n" \
"Lg18_%=:\n" \
"v_mfma_f32_16x16x32_bf16 v[60:63], a[220:223], a[88:91], v[60:63]\n" \
"v_mfma_f32_16x16x32_bf16 v[60:63], a[224:227], a[92:95], v[60:63]\n" \
"buffer_load_dwordx4 a[172:175], v209, %[srdz], s50 offen offset:3072 sc1\n" \
"v_mfma_f32_16x16x32_bf16 v[92:95], a[220:223], a[104:107], v[92:95]\n" \
"v_mfma_f32_16x16x32_bf16 v[92:95], a[224:227], a[108:111], v[92:95]\n" \
"v_cmp_u_f32_e64 s[44:45], v192, v196\n" \
"v_mfma_f32_16x16x32_bf16 v[124:127], a[220:223], a[120:123], v[124:127]\n" \
"s_add_u32 m0, %[m0b], 16384\n" \
"s_cmp_gt_u32 s48, 2\n" \
"s_cselect_b32 s49, 2, 3\n" \
"s_lshl_b32 s49, s49, 18\n" \
"s_or_b32 s49, s49, s59\n" \
"buffer_load_dwordx4 %[wo0], %[srdt], s49 offen offset:0 lds\n" \
"v_mfma_f32_16x16x32_bf16 v[124:127], a[224:227], a[124:127], v[124:127]\n" \
"v_cmp_u_f32_e64 s[56:57], v200, v204\n" \
"s_or_b64 s[56:57], s[56:57], s[44:45]\n" \
"s_cbranch_scc1 Lsp2_%=\n" \
"Lbk2_%=:\n" \
"s_waitcnt vmcnt(63) lgkmcnt(2)\n" \
"s_nop 0\n" \
"v_mfma_f32_16x16x32_bf16 v[144:147], a[236:239], a[0:3], v[144:147]\n" \
"ds_read_b64_tr_b16 a[220:221], v220 offset:53760\n" \
"v_mfma_f32_16x16x32_bf16 v[144:147], a[208:211], a[4:7], v[144:147]\n" \
"ds_read_b64_tr_b16 a[222:223], v220 offset:55808\n" \
"v_mfma_f32_16x16x32_bf16 v[148:151], a[236:239], a[16:19], v[148:151]\n" \
"ds_read_b64_tr_b16 a[196:197], v220 offset:57856\n" \
"buffer_load_dwordx4 a[176:179], v210, %[srdz], s50 offen offset:0 sc1\n" \
"v_mfma_f32_16x16x32_bf16 v[148:151], a[208:211], a[20:23], v[148:151]\n" \
"ds_read_b64_tr_b16 a[198:199], v220 offset:59904\n" \
"v_mfma_f32_16x16x32_bf16 v[152:155], a[236:239], a[32:35], v[152:155]\n" \
"ds_read_b64_tr_b16 a[200:201], v220 offset:61952\n" \
"s_cbranch_vccz Lg19_%=\n" \
"s_nop 0\n" \
"buffer_store_dwordx4 v[112:115], v210, %[srdz], s60 offen offset:2048 sc1\n" \
"Lg19_%=:\n" \
"v_mfma_f32_16x16x32_bf16 v[152:155], a[208:211], a[36:39], v[152:155]\n" \
"ds_read_b64_tr_b16 a[202:203], v220 offset:64000\n" \
"v_mfma_f32_16x16x32_bf16 v[156:159], a[236:239], a[48:51], v[156:159]\n" \
"ds_read_b64_tr_b16 a[204:205], v221 offset:49664\n" \
"v_mfma_f32_16x16x32_bf16 v[156:159], a[208:211], a[52:55], v[156:159]\n" \
"ds_read_b64_tr_b16 a[206:207], v221 offset:51712\n" \
"v_mfma_f32_16x16x32_bf16 v[144:147], a[212:215], a[8:11], v[144:147]\n" \
"ds_read_b64_tr_b16 a[224:225], v221 offset:53760\n" \
"v_mfma_f32_16x16x32_bf16 v[144:147], a[216:219], a[12:15], v[144:147]\n" \
"ds_read_b64_tr_b16 a[226:227], v221 offset:55808\n" \
"buffer_load_dwordx4 a[180:183], v210, %[srdz], s50 offen offset:1024 sc1\n" \
"v_mfma_f32_16x16x32_bf16 v[148:151], a[212:215], a[24:27], v[148:151]\n" \
"v_mfma_f32_16x16x32_bf16 v[148:151], a[216:219], a[28:31], v[148:151]\n" \
"v_mfma_f32_16x16x32_bf16 v[152:155], a[212:215], a[40:43], v[152:155]\n" \
"v_mfma_f32_16x16x32_bf16 v[152:155], a[216:219], a[44:47], v[152:155]\n" \
"buffer_load_dwordx4 v212, %[srdt], s49 offen offset:1024 lds\n" \
"v_mfma_f32_16x16x32_bf16 v[156:159], a[212:215], a[56:59], v[156:159]\n" \
"ds_read_b64_tr_b16 a[232:233], v221 offset:57856\n" \
"v_mfma_f32_16x16x32_bf16 v[156:159], a[216:219], a[60:63], v[156:159]\n" \
"s_waitcnt vmcnt(63) lgkmcnt(5)\n" \
"s_nop 0\n" \
"v_mfma_f32_16x16x32_bf16 v[160:163], a[228:231], a[0:3], v[160:163]\n" \
"ds_read_b64_tr_b16 a[234:235], v221 offset:59904\n" \
"s_cbranch_vccz Lg20_%=\n" \
"s_nop 0\n" \
"buffer_store_dwordx4 v[120:123], v210, %[srdz], s60 offen offset:3072 sc1\n" \
"Lg20_%=:\n" \
"v_mfma_f32_16x16x32_bf16 v[160:163], a[220:223], a[4:7], v[160:163]\n" \
"ds_read_b64_tr_b16 a[236:237], v221 offset:61952\n" \
"v_mfma_f32_16x16x32_bf16 v[164:167], a[228:231], a[16:19], v[164:167]\n" \
"ds_read_b64_tr_b16 a[238:239], v221 offset:64000\n" \
"buffer_load_dwordx4 a[184:187], v210, %[srdz], s50 offen offset:2048 sc1\n" \
"v_mfma_f32_16x16x32_bf16 v[164:167], a[220:223], a[20:23], v[164:167]\n" \
"v_mfma_f32_16x16x32_bf16 v[168:171], a[228:231], a[32:35], v[168:171]\n" \
"buffer_load_dwordx4 a[188:191], v210, %[srdz], s50 offen offset:3072 sc1\n" \
"v_mfma_f32_16x16x32_bf16 v[168:171], a[220:223], a[36:39], v[168:171]\n" \
"v_mfma_f32_16x16x32_bf16 v[172:175], a[228:231], a[48:51], v[172:175]\n" \
"v_mfma_f32_16x16x32_bf16 v[172:175], a[220:223], a[52:55], v[172:175]\n" \
"v_mfma_f32_16x16x32_bf16 v[160:163], a[196:199], a[8:11], v[160:163]\n" \
"v_mfma_f32_16x16x32_bf16 v[160:163], a[200:203], a[12:15], v[160:163]\n" \
"v_mfma_f32_16x16x32_bf16 v[164:167], a[196:199], a[24:27], v[164:167]\n" \
"v_mfma_f32_16x16x32_bf16 v[164:167], a[200:203], a[28:31], v[164:167]\n" \
"v_mfma_f32_16x16x32_bf16 v[168:171], a[196:199], a[40:43], v[168:171]\n" \
"s_waitcnt vmcnt(63) lgkmcnt(0)\n" \
"s_nop 0\n" \
"s_bitcmp1_b32 0xe0, s48\n" \
"s_cbranch_scc1 Lcw10_%=\n" \
"s_waitcnt vmcnt(54)\n" \
"Lcw10_%=:\n" \
"s_barrier\n" \
"s_nop 0\n" \
"v_mfma_f32_16x16x32_bf16 v[168:171], a[200:203], a[44:47], v[168:171]\n" \
"ds_read_b64_tr_b16 a[208:209], v215 offset:49152\n" \
"v_mfma_f32_16x16x32_bf16 v[172:175], a[196:199], a[56:59], v[172:175]\n" \
"ds_read_b64_tr_b16 a[210:211], v215 offset:51200\n" \
"v_mfma_f32_16x16x32_bf16 v[172:175], a[200:203], a[60:63], v[172:175]\n" \
"s_waitcnt vmcnt(63) lgkmcnt(2)\n" \
"s_nop 0\n" \
"v_mfma_f32_16x16x32_bf16 v[176:179], a[204:207], a[0:3], v[176:179]\n" \
"ds_read_b64_tr_b16 a[212:213], v215 offset:53248\n" \
"global_load_dword v237, %[vct], %[pct]\n" \
"v_mfma_f32_16x16x32_bf16 v[176:179], a[224:227], a[4:7], v[176:179]\n" \
"ds_read_b64_tr_b16 a[214:215], v215 offset:55296\n" \
"v_mfma_f32_16x16x32_bf16 v[180:183], a[204:207], a[16:19], v[180:183]\n" \
"ds_read_b64_tr_b16 a[216:217], v215 offset:57344\n" \
"buffer_load_dwordx4 v213, %[srdt], s49 offen offset:2048 lds\n" \
"v_mfma_f32_16x16x32_bf16 v[180:183], a[224:227], a[20:23], v[180:183]\n" \
"ds_read_b64_tr_b16 a[218:219], v215 offset:59392\n" \
"v_mfma_f32_16x16x32_bf16 v[184:187], a[204:207], a[32:35], v[184:187]\n" \
"ds_read_b64_tr_b16 a[228:229], v215 offset:61440\n" \
"v_mfma_f32_16x16x32_bf16 v[184:187], a[224:227], a[36:39], v[184:187]\n" \
"ds_read_b64_tr_b16 a[230:231], v215 offset:63488\n" \
"v_mfma_f32_16x16x32_bf16 v[188:191], a[204:207], a[48:51], v[188:191]\n" \
"ds_read_b64_tr_b16 a[220:221], v227 offset:49152\n" \
"v_mfma_f32_16x16x32_bf16 v[188:191], a[224:227], a[52:55], v[188:191]\n" \
"ds_read_b64_tr_b16 a[222:223], v227 offset:51200\n" \
"v_mfma_f32_16x16x32_bf16 v[176:179], a[232:235], a[8:11], v[176:179]\n" \
"ds_read_b64_tr_b16 a[196:197], v227 offset:53248\n" \
"v_mfma_f32_16x16x32_bf16 v[176:179], a[236:239], a[12:15], v[176:179]\n" \
"ds_read_b64_tr_b16 a[198:199], v227 offset:55296\n" \
"s_cmp_gt_u32 s48, 3\n" \
"s_cselect_b32 s50, 3, 4\n" \
"s_mul_i32 s50, s50, 262144\n" \
"buffer_load_dwordx4 a[64:67], %[oq0], %[srdz], s50 offen offset:0 sc1\n" \
"v_mfma_f32_16x16x32_bf16 v[180:183], a[232:235], a[24:27], v[180:183]\n" \
"v_mfma_f32_16x16x32_bf16 v[180:183], a[236:239], a[28:31], v[180:183]\n" \
"buffer_load_dwordx4 v214, %[srdt], s49 offen offset:3072 lds\n" \
"v_mfma_f32_16x16x32_bf16 v[184:187], a[232:235], a[40:43], v[184:187]\n" \
"v_mfma_f32_16x16x32_bf16 v[184:187], a[236:239], a[44:47], v[184:187]\n" \
"buffer_load_dwordx4 a[68:71], %[oq0], %[srdz], s50 offen offset:1024 sc1\n" \
"v_mfma_f32_16x16x32_bf16 v[188:191], a[232:235], a[56:59], v[188:191]\n" \
"ds_read_b64_tr_b16 a[200:201], v227 offset:57344\n" \
"v_mfma_f32_16x16x32_bf16 v[188:191], a[236:239], a[60:63], v[188:191]\n" \
"s_waitcnt vmcnt(63) lgkmcnt(5)\n" \
"s_nop 0\n" \
"v_mfma_f32_16x16x32_bf16 v[4:7], a[208:211], a[0:3], v[4:7]\n" \
"ds_read_b64_tr_b16 a[202:203], v227 offset:59392\n" \
"v_mfma_f32_16x16x32_bf16 v[4:7], a[212:215], a[4:7], v[4:7]\n" \
"ds_read_b64_tr_b16 a[204:205], v227 offset:61440\n" \
"v_mfma_f32_16x16x32_bf16 v[36:39], a[208:211], a[16:19], v[36:39]\n" \
"ds_read_b64_tr_b16 a[206:207], v227 offset:63488\n" \
"v_mfma_f32_16x16x32_bf16 v[36:39], a[212:215], a[20:23], v[36:39]\n" \
"v_mfma_f32_16x16x32_bf16 v[68:71], a[208:211], a[32:35], v[68:71]\n" \
"buffer_load_dwordx4 a[72:75], %[oq0], %[srdz], s50 offen offset:2048 sc1\n" \
"v_mfma_f32_16x16x32_bf16 v[68:71], a[212:215], a[36:39], v[68:71]\n" \
"v_mfma_f32_16x16x32_bf16 v[100:103], a[208:211], a[48:51], v[100:103]\n" \
"ds_read_b64_tr_b16 a[224:225], v215 offset:49664\n" \
"v_mfma_f32_16x16x32_bf16 v[100:103], a[212:215], a[52:55], v[100:103]\n" \
"ds_read_b64_tr_b16 a[226:227], v215 offset:51712\n" \
"v_mfma_f32_16x16x32_bf16 v[4:7], a[216:219], a[8:11], v[4:7]\n" \
"ds_read_b64_tr_b16 a[232:233], v215 offset:53760\n" \
"v_mfma_f32_16x16x32_bf16 v[4:7], a[228:231], a[12:15], v[4:7]\n" \
"ds_read_b64_tr_b16 a[234:235], v215 offset:55808\n" \
"v_mfma_f32_16x16x32_bf16 v[36:39], a[216:219], a[24:27], v[36:39]\n" \
"v_mfma_f32_16x16x32_bf16 v[36:39], a[228:231], a[28:31], v[36:39]\n" \
"buffer_load_dwordx4 a[76:79], %[oq0], %[srdz], s50 offen offset:3072 sc1\n" \
"v_mfma_f32_16x16x32_bf16 v[68:71], a[216:219], a[40:43], v[68:71]\n" \
"v_mfma_f32_16x16x32_bf16 v[68:71], a[228:231], a[44:47], v[68:71]\n" \
"v_mfma_f32_16x16x32_bf16 v[100:103], a[216:219], a[56:59], v[100:103]\n" \
"ds_read_b64_tr_b16 a[236:237], v215 offset:57856\n" \
"v_mfma_f32_16x16x32_bf16 v[100:103], a[228:231], a[60:63], v[100:103]\n" \
"s_waitcnt vmcnt(63) lgkmcnt(5)\n" \
"s_nop 0\n" \
"v_mfma_f32_16x16x32_bf16 v[12:15], a[220:223], a[0:3], v[12:15]\n" \
"ds_read_b64_tr_b16 a[238:239], v215 offset:59904\n" \
"v_mfma_f32_16x16x32_bf16 v[12:15], a[196:199], a[4:7], v[12:15]\n" \
"ds_read_b64_tr_b16 a[208:209], v215 offset:61952\n" \
"v_mfma_f32_16x16x32_bf16 v[44:47], a[220:223], a[16:19], v[44:47]\n" \
"ds_read_b64_tr_b16 a[210:211], v215 offset:64000\n" \
"v_mfma_f32_16x16x32_bf16 v[44:47], a[196:199], a[20:23], v[44:47]\n" \
"buffer_load_dwordx4 a[80:83], v208, %[srdz], s50 offen offset:0 sc1\n" \
"v_mfma_f32_16x16x32_bf16 v[76:79], a[220:223], a[32:35], v[76:79]\n" \
"v_mfma_f32_16x16x32_bf16 v[76:79], a[196:199], a[36:39], v[76:79]\n" \
"s_add_u32 m0, %[m0b], 32768\n" \
"s_cmp_gt_u32 s48, 3\n" \
"s_cselect_b32 s49, 3, 4\n" \
"s_lshl_b32 s49, s49, 18\n" \
"s_or_b32 s49, s49, s58\n" \
"buffer_load_dwordx4 %[wo0], %[srdt], s49 offen offset:0 lds\n" \
"v_mfma_f32_16x16x32_bf16 v[108:111], a[220:223], a[48:51], v[108:111]\n" \
"ds_read_b64_tr_b16 a[212:213], v227 offset:49664\n" \
"v_mfma_f32_16x16x32_bf16 v[108:111], a[196:199], a[52:55], v[108:111]\n" \
"ds_read_b64_tr_b16 a[214:215], v227 offset:51712\n" \
"v_mfma_f32_16x16x32_bf16 v[12:15], a[200:203], a[8:11], v[12:15]\n" \
"ds_read_b64_tr_b16 a[216:217], v227 offset:57856\n" \
"v_mfma_f32_16x16x32_bf16 v[12:15], a[204:207], a[12:15], v[12:15]\n" \
"ds_read_b64_tr_b16 a[218:219], v227 offset:59904\n" \
"v_mfma_f32_16x16x32_bf16 v[44:47], a[200:203], a[24:27], v[44:47]\n" \
"ds_read_b64_tr_b16 a[228:229], v227 offset:53760\n" \
"v_mfma_f32_16x16x32_bf16 v[44:47], a[204:207], a[28:31], v[44:47]\n" \
"ds_read_b64_tr_b16 a[230:231], v227 offset:55808\n" \
"v_mfma_f32_16x16x32_bf16 v[76:79], a[200:203], a[40:43], v[76:79]\n" \
"ds_read_b64_tr_b16 a[220:221], v227 offset:61952\n" \
"buffer_load_dwordx4 a[84:87], v208, %[srdz], s50 offen offset:1024 sc1\n" \
"v_mfma_f32_16x16x32_bf16 v[76:79], a[204:207], a[44:47], v[76:79]\n" \
"ds_read_b64_tr_b16 a[222:223], v227 offset:64000\n" \
"v_mfma_f32_16x16x32_bf16 v[108:111], a[200:203], a[56:59], v[108:111]\n" \
"v_mfma_f32_16x16x32_bf16 v[108:111], a[204:207], a[60:63], v[108:111]\n" \
"s_waitcnt vmcnt(63) lgkmcnt(12)\n" \
"s_nop 0\n" \
"v_mfma_f32_16x16x32_bf16 v[20:23], a[224:227], a[0:3], v[20:23]\n" \
"v_mfma_f32_16x16x32_bf16 v[20:23], a[232:235], a[4:7], v[20:23]\n" \
"buffer_load_dwordx4 a[88:91], v208, %[srdz], s50 offen offset:2048 sc1\n" \
"v_mfma_f32_16x16x32_bf16 v[52:55], a[224:227], a[16:19], v[52:55]\n" \
"v_mfma_f32_16x16x32_bf16 v[52:55], a[232:235], a[20:23], v[52:55]\n" \
"buffer_load_dwordx4 v212, %[srdt], s49 offen offset:1024 lds\n" \
"v_mfma_f32_16x16x32_bf16 v[84:87], a[224:227], a[32:35], v[84:87]\n" \
"s_waitcnt vmcnt(26) lgkmcnt(0)\n" \
"s_nop 0\n" \
"s_bitcmp1_b32 0xe0, s48\n" \
"s_cbranch_scc1 Lcw11_%=\n" \
"s_waitcnt vmcnt(22)\n" \
"Lcw11_%=:\n" \
"s_barrier\n" \
"s_nop 0\n" \
"v_mfma_f32_16x16x32_bf16 v[84:87], a[232:235], a[36:39], v[84:87]\n" \
"ds_read_b64_tr_b16 a[196:197], v215 offset:0\n" \
"v_mfma_f32_16x16x32_bf16 v[116:119], a[224:227], a[48:51], v[116:119]\n" \
"ds_read_b64_tr_b16 a[198:199], v215 offset:2048\n" \
"v_mfma_f32_16x16x32_bf16 v[116:119], a[232:235], a[52:55], v[116:119]\n" \
"ds_read_b64_tr_b16 a[200:201], v215 offset:4096\n" \
"v_mfma_f32_16x16x32_bf16 v[20:23], a[236:239], a[8:11], v[20:23]\n" \
"ds_read_b64_tr_b16 a[202:203], v215 offset:6144\n" \
"v_mfma_f32_16x16x32_bf16 v[20:23], a[208:211], a[12:15], v[20:23]\n" \
"ds_read_b64_tr_b16 a[204:205], v215 offset:8192\n" \
"v_mfma_f32_16x16x32_bf16 v[52:55], a[236:239], a[24:27], v[52:55]\n" \
"ds_read_b64_tr_b16 a[206:207], v215 offset:10240\n" \
"v_mfma_f32_16x16x32_bf16 v[52:55], a[208:211], a[28:31], v[52:55]\n" \
"ds_read_b64_tr_b16 a[224:225], v215 offset:12288\n" \
"buffer_load_dwordx4 a[92:95], v208, %[srdz], s50 offen offset:3072 sc1\n" \
"v_mfma_f32_16x16x32_bf16 v[84:87], a[236:239], a[40:43], v[84:87]\n" \
"ds_read_b64_tr_b16 a[226:227], v215 offset:14336\n" \
"v_mfma_f32_16x16x32_bf16 v[84:87], a[208:211], a[44:47], v[84:87]\n" \
"v_mfma_f32_16x16x32_bf16 v[116:119], a[236:239], a[56:59], v[116:119]\n" \
"v_mfma_f32_16x16x32_bf16 v[116:119], a[208:211], a[60:63], v[116:119]\n" \
"s_waitcnt vmcnt(16) lgkmcnt(4)\n" \
"s_nop 0\n" \
"v_mfma_f32_16x16x32_bf16 v[28:31], a[212:215], a[0:3], v[28:31]\n" \
"buffer_load_dwordx4 a[96:99], v209, %[srdz], s50 offen offset:0 sc1\n" \
"v_mfma_f32_16x16x32_bf16 v[28:31], a[228:231], a[4:7], v[28:31]\n" \
"v_mfma_f32_16x16x32_bf16 v[60:63], a[212:215], a[16:19], v[60:63]\n" \
"buffer_load_dwordx4 v213, %[srdt], s49 offen offset:2048 lds\n" \
"v_mfma_f32_16x16x32_bf16 v[60:63], a[228:231], a[20:23], v[60:63]\n" \
"v_mfma_f32_16x16x32_bf16 v[92:95], a[212:215], a[32:35], v[92:95]\n" \
"v_mfma_f32_16x16x32_bf16 v[92:95], a[228:231], a[36:39], v[92:95]\n" \
"v_mfma_f32_16x16x32_bf16 v[124:127], a[212:215], a[48:51], v[124:127]\n" \
"ds_read_b64_tr_b16 a[232:233], v227 offset:0\n" \
"v_mfma_f32_16x16x32_bf16 v[124:127], a[228:231], a[52:55], v[124:127]\n" \
"ds_read_b64_tr_b16 a[234:235], v227 offset:2048\n" \
"v_mfma_f32_16x16x32_bf16 v[128:131], a[196:199], a[128:131], v[192:195]\n" \
"ds_read_b64_tr_b16 a[236:237], v227 offset:4096\n" \
"v_mfma_f32_16x16x32_bf16 v[128:131], a[200:203], a[132:135], v[128:131]\n" \
"ds_read_b64_tr_b16 a[238:239], v227 offset:6144\n" \
"buffer_load_dwordx4 a[100:103], v209, %[srdz], s50 offen offset:1024 sc1\n" \
"v_mfma_f32_16x16x32_bf16 v[132:135], a[196:199], a[144:147], v[196:199]\n" \
"v_mfma_f32_16x16x32_bf16 v[132:135], a[200:203], a[148:151], v[132:135]\n" \
"v_mfma_f32_16x16x32_bf16 v[136:139], a[196:199], a[160:163], v[200:203]\n" \
"v_mfma_f32_16x16x32_bf16 v[136:139], a[200:203], a[164:167], v[136:139]\n" \
"buffer_load_dwordx4 a[104:107], v209, %[srdz], s50 offen offset:2048 sc1\n" \
"v_mfma_f32_16x16x32_bf16 v[140:143], a[196:199], a[176:179], v[204:207]\n" \
"ds_read_b64_tr_b16 a[208:209], v227 offset:8192\n" \
"v_mfma_f32_16x16x32_bf16 v[140:143], a[200:203], a[180:183], v[140:143]\n" \
"s_waitcnt vmcnt(17) lgkmcnt(5)\n" \
"s_nop 0\n" \
"v_mfma_f32_16x16x32_bf16 v[128:131], a[204:207], a[136:139], v[128:131]\n" \
"ds_read_b64_tr_b16 a[210:211], v227 offset:10240\n" \
"v_mfma_f32_16x16x32_bf16 v[128:131], a[224:227], a[140:143], v[128:131]\n" \
"ds_read_b64_tr_b16 a[212:213], v227 offset:12288\n" \
"v_mfma_f32_16x16x32_bf16 v[132:135], a[204:207], a[152:155], v[132:135]\n" \
"ds_read_b64_tr_b16 a[214:215], v227 offset:14336\n" \
"v_mfma_f32_16x16x32_bf16 v[132:135], a[224:227], a[156:159], v[132:135]\n" \
"buffer_load_dwordx4 v214, %[srdt], s49 offen offset:3072 lds\n" \
"v_mfma_f32_16x16x32_bf16 v[136:139], a[204:207], a[168:171], v[136:139]\n" \
"v_mfma_f32_16x16x32_bf16 v[136:139], a[224:227], a[172:175], v[136:139]\n" \
"buffer_load_dwordx4 a[108:111], v209, %[srdz], s50 offen offset:3072 sc1\n" \
"v_mfma_f32_16x16x32_bf16 v[140:143], a[204:207], a[184:187], v[140:143]\n" \
"ds_read_b64_tr_b16 a[228:229], v215 offset:512\n" \
"v_mfma_f32_16x16x32_bf16 v[140:143], a[224:227], a[188:191], v[140:143]\n" \
"ds_read_b64_tr_b16 a[230:231], v215 offset:2560\n" \
"v_mfma_f32_16x16x32_bf16 v[28:31], a[216:219], a[8:11], v[28:31]\n" \
"v_mfma_f32_16x16x32_bf16 v[28:31], a[220:223], a[12:15], v[28:31]\n" \
"v_mfma_f32_16x16x32_bf16 v[60:63], a[216:219], a[24:27], v[60:63]\n" \
"v_mfma_f32_16x16x32_bf16 v[60:63], a[220:223], a[28:31], v[60:63]\n" \
"v_mfma_f32_16x16x32_bf16 v[92:95], a[216:219], a[40:43], v[92:95]\n" \
"buffer_load_dwordx4 a[112:115], v210, %[srdz], s50 offen offset:0 sc1\n" \
"v_mfma_f32_16x16x32_bf16 v[92:95], a[220:223], a[44:47], v[92:95]\n" \
"v_cmp_u_f32_e64 s[44:45], v128, v132\n" \
"v_mfma_f32_16x16x32_bf16 v[124:127], a[216:219], a[56:59], v[124:127]\n" \
"v_mfma_f32_16x16x32_bf16 v[124:127], a[220:223], a[60:63], v[124:127]\n" \
"v_cmp_u_f32_e64 s[56:57], v136, v140\n" \
"s_or_b64 s[56:57], s[56:57], s[44:45]\n" \
"s_cbranch_scc1 Lsp3_%=\n" \
"Lbk3_%=:\n" \
"s_waitcnt vmcnt(63) lgkmcnt(2)\n" \
"s_nop 0\n" \
"v_mfma_f32_16x16x32_bf16 v[144:147], a[232:235], a[128:131], v[144:147]\n" \
"ds_read_b64_tr_b16 a[216:217], v215 offset:4608\n" \
"v_mfma_f32_16x16x32_bf16 v[144:147], a[236:239], a[132:135], v[144:147]\n" \
"ds_read_b64_tr_b16 a[218:219], v215 offset:6656\n" \
"v_mfma_f32_16x16x32_bf16 v[148:151], a[232:235], a[144:147], v[148:151]\n" \
"ds_read_b64_tr_b16 a[196:197], v215 offset:8704\n" \
"v_mfma_f32_16x16x32_bf16 v[148:151], a[236:239], a[148:151], v[148:151]\n" \
"ds_read_b64_tr_b16 a[198:199], v215 offset:10752\n" \
"v_mfma_f32_16x16x32_bf16 v[152:155], a[232:235], a[160:163], v[152:155]\n" \
"ds_read_b64_tr_b16 a[200:201], v215 offset:12800\n" \
"v_mfma_f32_16x16x32_bf16 v[152:155], a[236:239], a[164:167], v[152:155]\n" \
"ds_read_b64_tr_b16 a[202:203], v215 offset:14848\n" \
"v_mfma_f32_16x16x32_bf16 v[156:159], a[232:235], a[176:179], v[156:159]\n" \
"ds_read_b64_tr_b16 a[204:205], v227 offset:512\n" \
"buffer_load_dwordx4 a[116:119], v210, %[srdz], s50 offen offset:1024 sc1\n" \
"v_mfma_f32_16x16x32_bf16 v[156:159], a[236:239], a[180:183], v[156:159]\n" \
"ds_read_b64_tr_b16 a[206:207], v227 offset:2560\n" \
"v_mfma_f32_16x16x32_bf16 v[144:147], a[208:211], a[136:139], v[144:147]\n" \
"ds_read_b64_tr_b16 a[220:221], v227 offset:4608\n" \
"v_mfma_f32_16x16x32_bf16 v[144:147], a[212:215], a[140:143], v[144:147]\n" \
"ds_read_b64_tr_b16 a[222:223], v227 offset:6656\n" \
"v_mfma_f32_16x16x32_bf16 v[148:151], a[208:211], a[152:155], v[148:151]\n" \
"s_add_u32 m0, %[m0b], 98304\n" \
"s_cmp_gt_u32 s48, 4\n" \
"s_cselect_b32 s49, 4, 5\n" \
"s_lshl_b32 s49, s49, 18\n" \
"s_or_b32 s49, s49, s58\n" \
"buffer_load_dwordx4 %[wo0], %[srdt], s49 offen offset:0 lds\n" \
"v_mfma_f32_16x16x32_bf16 v[148:151], a[212:215], a[156:159], v[148:151]\n" \
"v_mfma_f32_16x16x32_bf16 v[152:155], a[208:211], a[168:171], v[152:155]\n" \
"buffer_load_dwordx4 a[120:123], v210, %[srdz], s50 offen offset:2048 sc1\n" \
"v_mfma_f32_16x16x32_bf16 v[152:155], a[212:215], a[172:175], v[152:155]\n" \
"v_mfma_f32_16x16x32_bf16 v[156:159], a[208:211], a[184:187], v[156:159]\n" \
"ds_read_b64_tr_b16 a[224:225], v227 offset:8704\n" \
"v_mfma_f32_16x16x32_bf16 v[156:159], a[212:215], a[188:191], v[156:159]\n" \
"s_waitcnt vmcnt(63) lgkmcnt(5)\n" \
"s_nop 0\n" \
"v_mfma_f32_16x16x32_bf16 v[160:163], a[228:231], a[128:131], v[160:163]\n" \
"ds_read_b64_tr_b16 a[226:227], v227 offset:10752\n" \
"v_mfma_f32_16x16x32_bf16 v[160:163], a[216:219], a[132:135], v[160:163]\n" \
"ds_read_b64_tr_b16 a[232:233], v227 offset:12800\n" \
"v_mfma_f32_16x16x32_bf16 v[164:167], a[228:231], a[144:147], v[164:167]\n" \
"ds_read_b64_tr_b16 a[234:235], v227 offset:14848\n" \
"v_mfma_f32_16x16x32_bf16 v[164:167], a[216:219], a[148:151], v[164:167]\n" \
"buffer_load_dwordx4 a[124:127], v210, %[srdz], s50 offen offset:3072 sc1\n" \
"v_mfma_f32_16x16x32_bf16 v[168:171], a[228:231], a[160:163], v[168:171]\n" \
"v_mfma_f32_16x16x32_bf16 v[168:171], a[216:219], a[164:167], v[168:171]\n" \
"v_mfma_f32_16x16x32_bf16 v[172:175], a[228:231], a[176:179], v[172:175]\n" \
"v_mfma_f32_16x16x32_bf16 v[172:175], a[216:219], a[180:183], v[172:175]\n" \
"v_mfma_f32_16x16x32_bf16 v[160:163], a[196:199], a[136:139], v[160:163]\n" \
"v_mfma_f32_16x16x32_bf16 v[160:163], a[200:203], a[140:143], v[160:163]\n" \
"buffer_load_dwordx4 v212, %[srdt], s49 offen offset:1024 lds\n" \
"v_mfma_f32_16x16x32_bf16 v[164:167], a[196:199], a[152:155], v[164:167]\n" \
"v_mfma_f32_16x16x32_bf16 v[164:167], a[200:203], a[156:159], v[164:167]\n" \
"v_mfma_f32_16x16x32_bf16 v[168:171], a[196:199], a[168:171], v[168:171]\n" \
"s_waitcnt vmcnt(21) lgkmcnt(0)\n" \
"s_barrier\n" \
"v_mfma_f32_16x16x32_bf16 v[168:171], a[200:203], a[172:175], v[168:171]\n" \
"ds_read_b64_tr_b16 a[236:237], v215 offset:16384\n" \
"v_mfma_f32_16x16x32_bf16 v[172:175], a[196:199], a[184:187], v[172:175]\n" \
"ds_read_b64_tr_b16 a[238:239], v215 offset:18432\n" \
"v_mfma_f32_16x16x32_bf16 v[172:175], a[200:203], a[188:191], v[172:175]\n" \
"s_waitcnt vmcnt(63) lgkmcnt(2)\n" \
"s_nop 0\n" \
"v_mfma_f32_16x16x32_bf16 v[176:179], a[204:207], a[128:131], v[176:179]\n" \
"ds_read_b64_tr_b16 a[208:209], v215 offset:20480\n" \
"s_cmp_gt_u32 s48, 4\n" \
"s_cselect_b32 s50, 4, 5\n" \
"s_mul_i32 s50, s50, 262144\n" \
"buffer_load_dwordx4 v[0:3], %[oq0], %[srdz], s50 offen offset:0 sc1\n" \
"v_mfma_f32_16x16x32_bf16 v[176:179], a[220:223], a[132:135], v[176:179]\n" \
"ds_read_b64_tr_b16 a[210:211], v215 offset:22528\n" \
"v_mfma_f32_16x16x32_bf16 v[180:183], a[204:207], a[144:147], v[180:183]\n" \
"ds_read_b64_tr_b16 a[212:213], v215 offset:24576\n" \
"v_mfma_f32_16x16x32_bf16 v[180:183], a[220:223], a[148:151], v[180:183]\n" \
"ds_read_b64_tr_b16 a[214:215], v215 offset:26624\n" \
"v_mfma_f32_16x16x32_bf16 v[184:187], a[204:207], a[160:163], v[184:187]\n" \
"ds_read_b64_tr_b16 a[228:229], v215 offset:28672\n" \
"buffer_load_dwordx4 v[8:11], %[oq0], %[srdz], s50 offen offset:1024 sc1\n" \
"v_mfma_f32_16x16x32_bf16 v[184:187], a[220:223], a[164:167], v[184:187]\n" \
"ds_read_b64_tr_b16 a[230:231], v215 offset:30720\n" \
"v_mfma_f32_16x16x32_bf16 v[188:191], a[204:207], a[176:179], v[188:191]\n" \
"ds_read_b64_tr_b16 a[216:217], v227 offset:16384\n" \
"v_mfma_f32_16x16x32_bf16 v[188:191], a[220:223], a[180:183], v[188:191]\n" \
"ds_read_b64_tr_b16 a[218:219], v227 offset:18432\n" \
"v_mfma_f32_16x16x32_bf16 v[176:179], a[224:227], a[136:139], v[176:179]\n" \
"ds_read_b64_tr_b16 a[196:197], v227 offset:20480\n" \
"buffer_load_dwordx4 v[16:19], %[oq0], %[srdz], s50 offen offset:2048 sc1\n" \
"v_mfma_f32_16x16x32_bf16 v[176:179], a[232:235], a[140:143], v[176:179]\n" \
"ds_read_b64_tr_b16 a[198:199], v227 offset:22528\n" \
"v_mfma_f32_16x16x32_bf16 v[180:183], a[224:227], a[152:155], v[180:183]\n" \
"buffer_load_dwordx4 v213, %[srdt], s49 offen offset:2048 lds\n" \
"v_mfma_f32_16x16x32_bf16 v[180:183], a[232:235], a[156:159], v[180:183]\n" \
"v_mfma_f32_16x16x32_bf16 v[184:187], a[224:227], a[168:171], v[184:187]\n" \
"buffer_load_dwordx4 v[24:27], %[oq0], %[srdz], s50 offen offset:3072 sc1\n" \
"v_mfma_f32_16x16x32_bf16 v[184:187], a[232:235], a[172:175], v[184:187]\n" \
"v_mfma_f32_16x16x32_bf16 v[188:191], a[224:227], a[184:187], v[188:191]\n" \
"ds_read_b64_tr_b16 a[200:201], v227 offset:24576\n" \
"v_mfma_f32_16x16x32_bf16 v[188:191], a[232:235], a[188:191], v[188:191]\n" \
"s_waitcnt vmcnt(63) lgkmcnt(5)\n" \
"s_nop 0\n" \
"v_mfma_f32_16x16x32_bf16 v[4:7], a[236:239], a[128:131], v[4:7]\n" \
"ds_read_b64_tr_b16 a[202:203], v227 offset:26624\n" \
"buffer_load_dwordx4 v[32:35], v208, %[srdz], s50 offen offset:0 sc1\n" \
"v_mfma_f32_16x16x32_bf16 v[4:7], a[208:211], a[132:135], v[4:7]\n" \
"ds_read_b64_tr_b16 a[204:205], v227 offset:28672\n" \
"v_mfma_f32_16x16x32_bf16 v[36:39], a[236:239], a[144:147], v[36:39]\n" \
"ds_read_b64_tr_b16 a[206:207], v227 offset:30720\n" \
"v_mfma_f32_16x16x32_bf16 v[36:39], a[208:211], a[148:151], v[36:39]\n" \
"buffer_load_dwordx4 v[40:43], v208, %[srdz], s50 offen offset:1024 sc1\n" \
"v_mfma_f32_16x16x32_bf16 v[68:71], a[236:239], a[160:163], v[68:71]\n" \
"v_mfma_f32_16x16x32_bf16 v[68:71], a[208:211], a[164:167], v[68:71]\n" \
"buffer_load_dwordx4 v[48:51], v208, %[srdz], s50 offen offset:2048 sc1\n" \
"v_mfma_f32_16x16x32_bf16 v[100:103], a[236:239], a[176:179], v[100:103]\n" \
"ds_read_b64_tr_b16 a[220:221], v215 offset:16896\n" \
"v_mfma_f32_16x16x32_bf16 v[100:103], a[208:211], a[180:183], v[100:103]\n" \
"ds_read_b64_tr_b16 a[222:223], v215 offset:18944\n" \
"v_mfma_f32_16x16x32_bf16 v[4:7], a[212:215], a[136:139], v[4:7]\n" \
"ds_read_b64_tr_b16 a[224:225], v215 offset:20992\n" \
"v_mfma_f32_16x16x32_bf16 v[4:7], a[228:231], a[140:143], v[4:7]\n" \
"ds_read_b64_tr_b16 a[226:227], v215 offset:23040\n" \
"v_mfma_f32_16x16x32_bf16 v[36:39], a[212:215], a[152:155], v[36:39]\n" \
"buffer_load_dwordx4 v214, %[srdt], s49 offen offset:3072 lds\n" \
"v_mfma_f32_16x16x32_bf16 v[36:39], a[228:231], a[156:159], v[36:39]\n" \
"v_mfma_f32_16x16x32_bf16 v[68:71], a[212:215], a[168:171], v[68:71]\n" \
"buffer_load_dwordx4 v[56:59], v208, %[srdz], s50 offen offset:3072 sc1\n" \
"v_mfma_f32_16x16x32_bf16 v[68:71], a[228:231], a[172:175], v[68:71]\n" \
"v_mfma_f32_16x16x32_bf16 v[100:103], a[212:215], a[184:187], v[100:103]\n" \
"ds_read_b64_tr_b16 a[232:233], v215 offset:25088\n" \
"v_mfma_f32_16x16x32_bf16 v[100:103], a[228:231], a[188:191], v[100:103]\n" \
"s_waitcnt vmcnt(63) lgkmcnt(5)\n" \
"s_nop 0\n" \
"v_mfma_f32_16x16x32_bf16 v[12:15], a[216:219], a[128:131], v[12:15]\n" \
"ds_read_b64_tr_b16 a[234:235], v215 offset:27136\n" \
"buffer_load_dwordx4 v[64:67], v209, %[srdz], s50 offen offset:0 sc1\n" \
"v_mfma_f32_16x16x32_bf16 v[12:15], a[196:199], a[132:135], v[12:15]\n" \
"ds_read_b64_tr_b16 a[236:237], v215 offset:29184\n" \
"v_mfma_f32_16x16x32_bf16 v[44:47], a[216:219], a[144:147], v[44:47]\n" \
"ds_read_b64_tr_b16 a[238:239], v215 offset:31232\n" \
"v_mfma_f32_16x16x32_bf16 v[44:47], a[196:199], a[148:151], v[44:47]\n" \
".if . - adaln_body != 49152\n" \
".error \"size model\"\n" \
".endif\n" \
"Lk11_%=:\n" \
"s_cbranch_execz adaln_kr\n" \
"s_nop 0\n" \
"buffer_load_dwordx4 v[72:75], v209, %[srdz], s50 offen offset:1024 sc1\n" \
"v_mfma_f32_16x16x32_bf16 v[76:79], a[216:219], a[160:163], v[76:79]\n" \
"v_mfma_f32_16x16x32_bf16 v[76:79], a[196:199], a[164:167], v[76:79]\n" \
"buffer_load_dwordx4 v[80:83], v209, %[srdz], s50 offen offset:2048 sc1\n" \
"v_mfma_f32_16x16x32_bf16 v[108:111], a[216:219], a[176:179], v[108:111]\n" \
"ds_read_b64_tr_b16 a[208:209], v227 offset:16896\n" \
"v_mfma_f32_16x16x32_bf16 v[108:111], a[196:199], a[180:183], v[108:111]\n" \
"ds_read_b64_tr_b16 a[210:211], v227 offset:18944\n" \
"v_mfma_f32_16x16x32_bf16 v[12:15], a[200:203], a[136:139], v[12:15]\n" \
"ds_read_b64_tr_b16 a[212:213], v227 offset:25088\n" \
"v_mfma_f32_16x16x32_bf16 v[12:15], a[204:207], a[140:143], v[12:15]\n" \
"ds_read_b64_tr_b16 a[214:215], v227 offset:27136\n" \
"v_mfma_f32_16x16x32_bf16 v[44:47], a[200:203], a[152:155], v[44:47]\n" \
"ds_read_b64_tr_b16 a[228:229], v227 offset:20992\n" \
"v_mfma_f32_16x16x32_bf16 v[44:47], a[204:207], a[156:159], v[44:47]\n" \
"ds_read_b64_tr_b16 a[230:231], v227 offset:23040\n" \
"buffer_load_dwordx4 v[88:91], v209, %[srdz], s50 offen offset:3072 sc1\n" \
"v_mfma_f32_16x16x32_bf16 v[76:79], a[200:203], a[168:171], v[76:79]\n" \
"ds_read_b64_tr_b16 a[216:217], v227 offset:29184\n" \
"v_mfma_f32_16x16x32_bf16 v[76:79], a[204:207], a[172:175], v[76:79]\n" \
"ds_read_b64_tr_b16 a[218:219], v227 offset:31232\n" \
"v_mfma_f32_16x16x32_bf16 v[108:111], a[200:203], a[184:187], v[108:111]\n" \
"buffer_load_dwordx4 v[96:99], v210, %[srdz], s50 offen offset:0 sc1\n" \
"v_mfma_f32_16x16x32_bf16 v[108:111], a[204:207], a[188:191], v[108:111]\n" \
"s_waitcnt vmcnt(63) lgkmcnt(12)\n" \
"s_nop 0\n" \
"v_mfma_f32_16x16x32_bf16 v[20:23], a[220:223], a[128:131], v[20:23]\n" \
"s_add_u32 m0, %[m0b], 114688\n" \
"s_cmp_gt_u32 s48, 5\n" \
"s_cselect_b32 s49, 5, 6\n" \
"s_lshl_b32 s49, s49, 18\n" \
"s_or_b32 s49, s49, s58\n" \
"buffer_load_dwordx4 %[wo0], %[srdt], s49 offen offset:0 lds\n" \
"v_mfma_f32_16x16x32_bf16 v[20:23], a[224:227], a[132:135], v[20:23]\n" \
"v_mfma_f32_16x16x32_bf16 v[52:55], a[220:223], a[144:147], v[52:55]\n" \
"buffer_load_dwordx4 v[104:107], v210, %[srdz], s50 offen offset:1024 sc1\n" \
"v_mfma_f32_16x16x32_bf16 v[52:55], a[224:227], a[148:151], v[52:55]\n" \
"v_mfma_f32_16x16x32_bf16 v[84:87], a[220:223], a[160:163], v[84:87]\n" \
"s_waitcnt vmcnt(24) lgkmcnt(0)\n" \
"s_barrier\n" \
"v_mfma_f32_16x16x32_bf16 v[84:87], a[224:227], a[164:167], v[84:87]\n" \
"ds_read_b64_tr_b16 a[196:197], v215 offset:32768\n" \
"v_mfma_f32_16x16x32_bf16 v[116:119], a[220:223], a[176:179], v[116:119]\n" \
"ds_read_b64_tr_b16 a[198:199], v215 offset:34816\n" \
"v_mfma_f32_16x16x32_bf16 v[116:119], a[224:227], a[180:183], v[116:119]\n" \
"ds_read_b64_tr_b16 a[200:201], v215 offset:36864\n" \
"buffer_load_dwordx4 v[112:115], v210, %[srdz], s50 offen offset:2048 sc1\n" \
"v_mfma_f32_16x16x32_bf16 v[20:23], a[232:235], a[136:139], v[20:23]\n" \
"ds_read_b64_tr_b16 a[202:203], v215 offset:38912\n" \
"v_mfma_f32_16x16x32_bf16 v[20:23], a[236:239], a[140:143], v[20:23]\n" \
"ds_read_b64_tr_b16 a[204:205], v215 offset:40960\n" \
"v_mfma_f32_16x16x32_bf16 v[52:55], a[232:235], a[152:155], v[52:55]\n" \
"ds_read_b64_tr_b16 a[206:207], v215 offset:43008\n" \
"v_mfma_f32_16x16x32_bf16 v[52:55], a[236:239], a[156:159], v[52:55]\n" \
"ds_read_b64_tr_b16 a[220:221], v215 offset:45056\n" \
"buffer_load_dwordx4 v[120:123], v210, %[srdz], s50 offen offset:3072 sc1\n" \
"v_mfma_f32_16x16x32_bf16 v[84:87], a[232:235], a[168:171], v[84:87]\n" \
"ds_read_b64_tr_b16 a[222:223], v215 offset:47104\n" \
"v_readlane_b32 s54, %[thi], 16\n" \
"v_mfma_f32_16x16x32_bf16 v[84:87], a[236:239], a[172:175], v[84:87]\n" \
"v_mfma_f32_16x16x32_bf16 v[116:119], a[232:235], a[184:187], v[116:119]\n" \
"v_mfma_f32_16x16x32_bf16 v[116:119], a[236:239], a[188:191], v[116:119]\n" \
"s_waitcnt vmcnt(23) lgkmcnt(4)\n" \
"s_nop 0\n" \
"v_mfma_f32_16x16x32_bf16 v[28:31], a[208:211], a[128:131], v[28:31]\n" \
"buffer_load_dwordx4 v212, %[srdt], s49 offen offset:1024 lds\n" \
"v_mfma_f32_16x16x32_bf16 v[28:31], a[228:231], a[132:135], v[28:31]\n" \
"v_mfma_f32_16x16x32_bf16 v[60:63], a[208:211], a[144:147], v[60:63]\n" \
"s_cmp_gt_u32 s48, 5\n" \
"s_cselect_b32 s50, 5, 6\n" \
"s_mul_i32 s50, s50, 262144\n" \
"buffer_load_dwordx4 a[0:3], %[oq0], %[srdz], s50 offen offset:0 sc1\n" \
"v_mfma_f32_16x16x32_bf16 v[60:63], a[228:231], a[148:151], v[60:63]\n" \
"v_mfma_f32_16x16x32_bf16 v[92:95], a[208:211], a[160:163], v[92:95]\n" \
"buffer_load_dword v232, v226, %[srdz], s54 offen sc1\n" \
"v_mfma_f32_16x16x32_bf16 v[92:95], a[228:231], a[164:167], v[92:95]\n" \
"v_mfma_f32_16x16x32_bf16 v[124:127], a[208:211], a[176:179], v[124:127]\n" \
"ds_read_b64_tr_b16 a[224:225], v227 offset:32768\n" \
"buffer_load_dwordx4 a[4:7], %[oq0], %[srdz], s50 offen offset:1024 sc1\n" \
"v_mfma_f32_16x16x32_bf16 v[124:127], a[228:231], a[180:183], v[124:127]\n" \
"ds_read_b64_tr_b16 a[226:227], v227 offset:34816\n" \
"v_mfma_f32_16x16x32_bf16 v[192:195], a[196:199], a[64:67], v[128:131]\n" \
"ds_read_b64_tr_b16 a[232:233], v227 offset:36864\n" \
"v_mfma_f32_16x16x32_bf16 v[192:195], a[200:203], a[68:71], v[192:195]\n" \
"ds_read_b64_tr_b16 a[234:235], v227 offset:38912\n" \
"v_mfma_f32_16x16x32_bf16 v[196:199], a[196:199], a[80:83], v[132:135]\n" \
"buffer_load_dwordx4 a[8:11], %[oq0], %[srdz], s50 offen offset:2048 sc1\n" \
"v_mfma_f32_16x16x32_bf16 v[196:199], a[200:203], a[84:87], v[196:199]\n" \
"v_mfma_f32_16x16x32_bf16 v[200:203], a[196:199], a[96:99], v[136:139]\n" \
"buffer_load_dwordx4 a[12:15], %[oq0], %[srdz], s50 offen offset:3072 sc1\n" \
"v_mfma_f32_16x16x32_bf16 v[200:203], a[200:203], a[100:103], v[200:203]\n" \
"v_mfma_f32_16x16x32_bf16 v[204:207], a[196:199], a[112:115], v[140:143]\n" \
"ds_read_b64_tr_b16 a[236:237], v227 offset:40960\n" \
"v_mfma_f32_16x16x32_bf16 v[204:207], a[200:203], a[116:119], v[204:207]\n" \
"s_waitcnt vmcnt(26) lgkmcnt(5)\n" \
"s_nop 0\n" \
"v_mfma_f32_16x16x32_bf16 v[192:195], a[204:207], a[72:75], v[192:195]\n" \
"ds_read_b64_tr_b16 a[238:239], v227 offset:43008\n" \
"v_mfma_f32_16x16x32_bf16 v[192:195], a[220:223], a[76:79], v[192:195]\n" \
"ds_read_b64_tr_b16 a[208:209], v227 offset:45056\n" \
"buffer_load_dwordx4 v213, %[srdt], s49 offen offset:2048 lds\n" \
"v_mfma_f32_16x16x32_bf16 v[196:199], a[204:207], a[88:91], v[196:199]\n" \
"ds_read_b64_tr_b16 a[210:211], v227 offset:47104\n" \
"v_mfma_f32_16x16x32_bf16 v[196:199], a[220:223], a[92:95], v[196:199]\n" \
"buffer_load_dwordx4 a[16:19], v208, %[srdz], s50 offen offset:0 sc1\n" \
"v_mfma_f32_16x16x32_bf16 v[200:203], a[204:207], a[104:107], v[200:203]\n" \
"v_mfma_f32_16x16x32_bf16 v[200:203], a[220:223], a[108:111], v[200:203]\n" \
"buffer_load_dwordx4 a[20:23], v208, %[srdz], s50 offen offset:1024 sc1\n" \
"v_mfma_f32_16x16x32_bf16 v[204:207], a[204:207], a[120:123], v[204:207]\n" \
"ds_read_b64_tr_b16 a[228:229], v215 offset:33280\n" \
"v_mfma_f32_16x16x32_bf16 v[204:207], a[220:223], a[124:127], v[204:207]\n" \
"ds_read_b64_tr_b16 a[230:231], v215 offset:35328\n" \
"v_mfma_f32_16x16x32_bf16 v[28:31], a[212:215], a[136:139], v[28:31]\n" \
"buffer_load_dwordx4 a[24:27], v208, %[srdz], s50 offen offset:2048 sc1\n" \
"v_mfma_f32_16x16x32_bf16 v[28:31], a[216:219], a[140:143], v[28:31]\n" \
"v_mfma_f32_16x16x32_bf16 v[60:63], a[212:215], a[152:155], v[60:63]\n" \
"buffer_load_dwordx4 a[28:31], v208, %[srdz], s50 offen offset:3072 sc1\n" \
"v_mfma_f32_16x16x32_bf16 v[60:63], a[216:219], a[156:159], v[60:63]\n" \
"v_mfma_f32_16x16x32_bf16 v[92:95], a[212:215], a[168:171], v[92:95]\n" \
"v_mfma_f32_16x16x32_bf16 v[92:95], a[216:219], a[172:175], v[92:95]\n" \
"v_cmp_u_f32_e64 s[44:45], v192, v196\n" \
"buffer_load_dwordx4 v214, %[srdt], s49 offen offset:3072 lds\n" \
"v_mfma_f32_16x16x32_bf16 v[124:127], a[212:215], a[184:187], v[124:127]\n" \
"v_mfma_f32_16x16x32_bf16 v[124:127], a[216:219], a[188:191], v[124:127]\n" \
"v_cmp_u_f32_e64 s[56:57], v200, v204\n" \
"s_or_b64 s[56:57], s[56:57], s[44:45]\n" \
"s_cbranch_scc1 Lsp4_%=\n" \
"Lbk4_%=:\n" \
"s_waitcnt vmcnt(63) lgkmcnt(2)\n" \
"s_nop 0\n" \
"v_mfma_f32_16x16x32_bf16 v[144:147], a[224:227], a[64:67], v[144:147]\n" \
"ds_read_b64_tr_b16 a[212:213], v215 offset:37376\n" \
"v_mfma_f32_16x16x32_bf16 v[144:147], a[232:235], a[68:71], v[144:147]\n" \
"ds_read_b64_tr_b16 a[214:215], v215 offset:39424\n" \
"buffer_load_dwordx4 a[32:35], v209, %[srdz], s50 offen offset:0 sc1\n" \
"v_mfma_f32_16x16x32_bf16 v[148:151], a[224:227], a[80:83], v[148:151]\n" \
"ds_read_b64_tr_b16 a[196:197], v215 offset:41472\n" \
"v_mfma_f32_16x16x32_bf16 v[148:151], a[232:235], a[84:87], v[148:151]\n" \
"ds_read_b64_tr_b16 a[198:199], v215 offset:43520\n" \
"v_mfma_f32_16x16x32_bf16 v[152:155], a[224:227], a[96:99], v[152:155]\n" \
"ds_read_b64_tr_b16 a[200:201], v215 offset:45568\n" \
"v_mfma_f32_16x16x32_bf16 v[152:155], a[232:235], a[100:103], v[152:155]\n" \
"ds_read_b64_tr_b16 a[202:203], v215 offset:47616\n" \
"buffer_load_dwordx4 a[36:39], v209, %[srdz], s50 offen offset:1024 sc1\n" \
"v_mfma_f32_16x16x32_bf16 v[156:159], a[224:227], a[112:115], v[156:159]\n" \
"ds_read_b64_tr_b16 a[204:205], v227 offset:33280\n" \
"v_mfma_f32_16x16x32_bf16 v[156:159], a[232:235], a[116:119], v[156:159]\n" \
"ds_read_b64_tr_b16 a[206:207], v227 offset:35328\n" \
"v_mfma_f32_16x16x32_bf16 v[144:147], a[236:239], a[72:75], v[144:147]\n" \
"ds_read_b64_tr_b16 a[216:217], v227 offset:41472\n" \
"v_mfma_f32_16x16x32_bf16 v[144:147], a[208:211], a[76:79], v[144:147]\n" \
"ds_read_b64_tr_b16 a[218:219], v227 offset:43520\n" \
"buffer_load_dwordx4 a[40:43], v209, %[srdz], s50 offen offset:2048 sc1\n" \
"v_mfma_f32_16x16x32_bf16 v[148:151], a[236:239], a[88:91], v[148:151]\n" \
"ds_read_b64_tr_b16 a[220:221], v227 offset:37376\n" \
"v_mfma_f32_16x16x32_bf16 v[148:151], a[208:211], a[92:95], v[148:151]\n" \
"ds_read_b64_tr_b16 a[222:223], v227 offset:39424\n" \
"v_mfma_f32_16x16x32_bf16 v[152:155], a[236:239], a[104:107], v[152:155]\n" \
"ds_read_b64_tr_b16 a[224:225], v227 offset:45568\n" \
"v_mfma_f32_16x16x32_bf16 v[152:155], a[208:211], a[108:111], v[152:155]\n" \
"ds_read_b64_tr_b16 a[226:227], v227 offset:47616\n" \
"buffer_load_dwordx4 a[44:47], v209, %[srdz], s50 offen offset:3072 sc1\n" \
"v_mfma_f32_16x16x32_bf16 v[156:159], a[236:239], a[120:123], v[156:159]\n" \
"v_mfma_f32_16x16x32_bf16 v[156:159], a[208:211], a[124:127], v[156:159]\n" \
"s_waitcnt vmcnt(63) lgkmcnt(12)\n" \
"s_nop 0\n" \
"v_mfma_f32_16x16x32_bf16 v[160:163], a[228:231], a[64:67], v[160:163]\n" \
"buffer_load_dwordx4 a[48:51], v210, %[srdz], s50 offen offset:0 sc1\n" \
"v_mfma_f32_16x16x32_bf16 v[160:163], a[212:215], a[68:71], v[160:163]\n" \
"v_mfma_f32_16x16x32_bf16 v[164:167], a[228:231], a[80:83], v[164:167]\n" \
"buffer_load_dwordx4 a[52:55], v210, %[srdz], s50 offen offset:1024 sc1\n" \
"v_mfma_f32_16x16x32_bf16 v[164:167], a[212:215], a[84:87], v[164:167]\n" \
"v_mfma_f32_16x16x32_bf16 v[168:171], a[228:231], a[96:99], v[168:171]\n" \
"s_waitcnt vmcnt(28) lgkmcnt(0)\n" \
"s_barrier\n" \
"v_mfma_f32_16x16x32_bf16 v[168:171], a[212:215], a[100:103], v[168:171]\n" \
"ds_read_b64_tr_b16 a[232:233], v220 offset:32768\n" \
"v_mfma_f32_16x16x32_bf16 v[172:175], a[228:231], a[112:115], v[172:175]\n" \
"ds_read_b64_tr_b16 a[234:235], v220 offset:34816\n" \
"v_mfma_f32_16x16x32_bf16 v[172:175], a[212:215], a[116:119], v[172:175]\n" \
"ds_read_b64_tr_b16 a[236:237], v220 offset:36864\n" \
"v_mfma_f32_16x16x32_bf16 v[160:163], a[196:199], a[72:75], v[160:163]\n" \
"ds_read_b64_tr_b16 a[238:239], v220 offset:38912\n" \
"v_mfma_f32_16x16x32_bf16 v[160:163], a[200:203], a[76:79], v[160:163]\n" \
"ds_read_b64_tr_b16 a[208:209], v220 offset:40960\n" \
"s_add_u32 m0, %[m0b], 49152\n" \
"s_cmp_gt_u32 s48, 6\n" \
"s_cselect_b32 s49, 6, 7\n" \
"s_lshl_b32 s49, s49, 18\n" \
"s_or_b32 s49, s49, s58\n" \
"buffer_load_dwordx4 %[wo0], %[srdt], s49 offen offset:0 lds\n" \
"v_mfma_f32_16x16x32_bf16 v[164:167], a[196:199], a[88:91], v[164:167]\n" \
"ds_read_b64_tr_b16 a[210:211], v220 offset:43008\n" \
"v_mfma_f32_16x16x32_bf16 v[164:167], a[200:203], a[92:95], v[164:167]\n" \
"ds_read_b64_tr_b16 a[228:229], v220 offset:45056\n" \
"buffer_load_dwordx4 a[56:59], v210, %[srdz], s50 offen offset:2048 sc1\n" \
"v_mfma_f32_16x16x32_bf16 v[168:171], a[196:199], a[104:107], v[168:171]\n" \
"ds_read_b64_tr_b16 a[230:231], v220 offset:47104\n" \
"v_mfma_f32_16x16x32_bf16 v[168:171], a[200:203], a[108:111], v[168:171]\n" \
"buffer_load_dwordx4 a[60:63], v210, %[srdz], s50 offen offset:3072 sc1\n" \
"v_mfma_f32_16x16x32_bf16 v[172:175], a[196:199], a[120:123], v[172:175]\n" \
"v_mfma_f32_16x16x32_bf16 v[172:175], a[200:203], a[124:127], v[172:175]\n" \
"s_waitcnt vmcnt(23) lgkmcnt(4)\n" \
"s_nop 0\n" \
"v_mfma_f32_16x16x32_bf16 v[176:179], a[204:207], a[64:67], v[176:179]\n" \
"v_mfma_f32_16x16x32_bf16 v[176:179], a[220:223], a[68:71], v[176:179]\n" \
"s_cmp_gt_u32 s48, 6\n" \
"s_cselect_b32 s50, 6, 7\n" \
"s_mul_i32 s50, s50, 262144\n" \
"buffer_load_dwordx4 a[128:131], %[oq0], %[srdz], s50 offen offset:0 sc1\n" \
"v_mfma_f32_16x16x32_bf16 v[180:183], a[204:207], a[80:83], v[180:183]\n" \
"v_mfma_f32_16x16x32_bf16 v[180:183], a[220:223], a[84:87], v[180:183]\n" \
"v_mfma_f32_16x16x32_bf16 v[184:187], a[204:207], a[96:99], v[184:187]\n" \
"v_mfma_f32_16x16x32_bf16 v[184:187], a[220:223], a[100:103], v[184:187]\n" \
"buffer_load_dwordx4 v212, %[srdt], s49 offen offset:1024 lds\n" \
"v_mfma_f32_16x16x32_bf16 v[188:191], a[204:207], a[112:115], v[188:191]\n" \
"ds_read_b64_tr_b16 a[212:213], v221 offset:32768\n" \
"v_mfma_f32_16x16x32_bf16 v[188:191], a[220:223], a[116:119], v[188:191]\n" \
"ds_read_b64_tr_b16 a[214:215], v221 offset:34816\n" \
"v_mfma_f32_16x16x32_bf16 v[128:131], a[232:235], v[0:3], v[192:195]\n" \
"ds_read_b64_tr_b16 a[196:197], v221 offset:36864\n" \
"buffer_load_dwordx4 a[132:135], %[oq0], %[srdz], s50 offen offset:1024 sc1\n" \
"v_mfma_f32_16x16x32_bf16 v[128:131], a[236:239], v[8:11], v[128:131]\n" \
"ds_read_b64_tr_b16 a[198:199], v221 offset:38912\n" \
"v_mfma_f32_16x16x32_bf16 v[132:135], a[232:235], v[32:35], v[196:199]\n" \
"buffer_load_dwordx4 a[136:139], %[oq0], %[srdz], s50 offen offset:2048 sc1\n" \
"v_mfma_f32_16x16x32_bf16 v[132:135], a[236:239], v[40:43], v[132:135]\n" \
"v_mfma_f32_16x16x32_bf16 v[136:139], a[232:235], v[64:67], v[200:203]\n" \
"v_mfma_f32_16x16x32_bf16 v[136:139], a[236:239], v[72:75], v[136:139]\n" \
"buffer_load_dwordx4 a[140:143], %[oq0], %[srdz], s50 offen offset:3072 sc1\n" \
"v_mfma_f32_16x16x32_bf16 v[140:143], a[232:235], v[96:99], v[204:207]\n" \
"ds_read_b64_tr_b16 a[200:201], v221 offset:40960\n" \
"v_mfma_f32_16x16x32_bf16 v[140:143], a[236:239], v[104:107], v[140:143]\n" \
"s_waitcnt vmcnt(26) lgkmcnt(5)\n" \
"s_nop 0\n" \
"v_mfma_f32_16x16x32_bf16 v[128:131], a[208:211], v[16:19], v[128:131]\n" \
"ds_read_b64_tr_b16 a[202:203], v221 offset:43008\n" \
"v_mfma_f32_16x16x32_bf16 v[128:131], a[228:231], v[24:27], v[128:131]\n" \
"ds_read_b64_tr_b16 a[204:205], v221 offset:45056\n" \
"v_mfma_f32_16x16x32_bf16 v[132:135], a[208:211], v[48:51], v[132:135]\n" \
"ds_read_b64_tr_b16 a[206:207], v221 offset:47104\n" \
"v_mfma_f32_16x16x32_bf16 v[132:135], a[228:231], v[56:59], v[132:135]\n" \
"buffer_load_dwordx4 a[144:147], v208, %[srdz], s50 offen offset:0 sc1\n" \
"v_mfma_f32_16x16x32_bf16 v[136:139], a[208:211], v[80:83], v[136:139]\n" \
"v_mfma_f32_16x16x32_bf16 v[136:139], a[228:231], v[88:91], v[136:139]\n" \
"buffer_load_dwordx4 v213, %[srdt], s49 offen offset:2048 lds\n" \
"v_mfma_f32_16x16x32_bf16 v[140:143], a[208:211], v[112:115], v[140:143]\n" \
"ds_read_b64_tr_b16 a[220:221], v220 offset:33280\n" \
"v_mfma_f32_16x16x32_bf16 v[140:143], a[228:231], v[120:123], v[140:143]\n" \
"ds_read_b64_tr_b16 a[222:223], v220 offset:35328\n" \
"v_mfma_f32_16x16x32_bf16 v[176:179], a[216:219], a[72:75], v[176:179]\n" \
"buffer_load_dwordx4 a[148:151], v208, %[srdz], s50 offen offset:1024 sc1\n" \
"v_mfma_f32_16x16x32_bf16 v[176:179], a[224:227], a[76:79], v[176:179]\n" \
"v_mfma_f32_16x16x32_bf16 v[180:183], a[216:219], a[88:91], v[180:183]\n" \
"buffer_load_dwordx4 a[152:155], v208, %[srdz], s50 offen offset:2048 sc1\n" \
"v_mfma_f32_16x16x32_bf16 v[180:183], a[224:227], a[92:95], v[180:183]\n" \
"v_mfma_f32_16x16x32_bf16 v[184:187], a[216:219], a[104:107], v[184:187]\n" \
"v_mfma_f32_16x16x32_bf16 v[184:187], a[224:227], a[108:111], v[184:187]\n" \
"v_cmp_u_f32_e64 s[44:45], v128, v132\n" \
"buffer_load_dwordx4 a[156:159], v208, %[srdz], s50 offen offset:3072 sc1\n" \
"v_mfma_f32_16x16x32_bf16 v[188:191], a[216:219], a[120:123], v[188:191]\n" \
"v_mfma_f32_16x16x32_bf16 v[188:191], a[224:227], a[124:127], v[188:191]\n" \
"v_cmp_u_f32_e64 s[56:57], v136, v140\n" \
"s_or_b64 s[56:57], s[56:57], s[44:45]\n" \
"s_cbranch_scc1 Lsp5_%=\n" \
"Lbk5_%=:\n" \
"s_waitcnt vmcnt(63) lgkmcnt(2)\n" \
"s_nop 0\n" \
"v_mfma_f32_16x16x32_bf16 v[144:147], a[212:215], v[0:3], v[144:147]\n" \
"ds_read_b64_tr_b16 a[216:217], v220 offset:37376\n" \
"v_mfma_f32_16x16x32_bf16 v[144:147], a[196:199], v[8:11], v[144:147]\n" \
"ds_read_b64_tr_b16 a[218:219], v220 offset:39424\n" \
"v_mfma_f32_16x16x32_bf16 v[148:151], a[212:215], v[32:35], v[148:151]\n" \
"ds_read_b64_tr_b16 a[208:209], v220 offset:41472\n" \
"v_mfma_f32_16x16x32_bf16 v[148:151], a[196:199], v[40:43], v[148:151]\n" \
"ds_read_b64_tr_b16 a[210:211], v220 offset:43520\n" \
"v_mfma_f32_16x16x32_bf16 v[152:155], a[212:215], v[64:67], v[152:155]\n" \
"ds_read_b64_tr_b16 a[224:225], v220 offset:45568\n" \
"buffer_load_dwordx4 a[160:163], v209, %[srdz], s50 offen offset:0 sc1\n" \
"v_mfma_f32_16x16x32_bf16 v[152:155], a[196:199], v[72:75], v[152:155]\n" \
"ds_read_b64_tr_b16 a[226:227], v220 offset:47616\n" \
"v_mfma_f32_16x16x32_bf16 v[156:159], a[212:215], v[96:99], v[156:159]\n" \
"ds_read_b64_tr_b16 a[228:229], v221 offset:33280\n" \
"buffer_load_dwordx4 v214, %[srdt], s49 offen offset:3072 lds\n" \
"v_mfma_f32_16x16x32_bf16 v[156:159], a[196:199], v[104:107], v[156:159]\n" \
"ds_read_b64_tr_b16 a[230:231], v221 offset:35328\n" \
"v_mfma_f32_16x16x32_bf16 v[144:147], a[200:203], v[16:19], v[144:147]\n" \
"ds_read_b64_tr_b16 a[232:233], v221 offset:41472\n" \
"buffer_load_dwordx4 a[164:167], v209, %[srdz], s50 offen offset:1024 sc1\n" \
"v_mfma_f32_16x16x32_bf16 v[144:147], a[204:207], v[24:27], v[144:147]\n" \
"ds_read_b64_tr_b16 a[234:235], v221 offset:43520\n" \
"v_mfma_f32_16x16x32_bf16 v[148:151], a[200:203], v[48:51], v[148:151]\n" \
"ds_read_b64_tr_b16 a[236:237], v221 offset:37376\n" \
"v_mfma_f32_16x16x32_bf16 v[148:151], a[204:207], v[56:59], v[148:151]\n" \
"ds_read_b64_tr_b16 a[238:239], v221 offset:39424\n" \
"v_mfma_f32_16x16x32_bf16 v[152:155], a[200:203], v[80:83], v[152:155]\n" \
"ds_read_b64_tr_b16 a[212:213], v221 offset:45568\n" \
"buffer_load_dwordx4 a[168:171], v209, %[srdz], s50 offen offset:2048 sc1\n" \
"v_mfma_f32_16x16x32_bf16 v[152:155], a[204:207], v[88:91], v[152:155]\n" \
"ds_read_b64_tr_b16 a[214:215], v221 offset:47616\n" \
"v_mfma_f32_16x16x32_bf16 v[156:159], a[200:203], v[112:115], v[156:159]\n" \
"buffer_load_dwordx4 a[172:175], v209, %[srdz], s50 offen offset:3072 sc1\n" \
"v_mfma_f32_16x16x32_bf16 v[156:159], a[204:207], v[120:123], v[156:159]\n" \
"s_waitcnt vmcnt(63) lgkmcnt(12)\n" \
"s_nop 0\n" \
"v_mfma_f32_16x16x32_bf16 v[160:163], a[220:223], v[0:3], v[160:163]\n" \
"v_mfma_f32_16x16x32_bf16 v[160:163], a[216:219], v[8:11], v[160:163]\n" \
"buffer_load_dwordx4 a[176:179], v210, %[srdz], s50 offen offset:0 sc1\n" \
"v_mfma_f32_16x16x32_bf16 v[164:167], a[220:223], v[32:35], v[164:167]\n" \
"v_mfma_f32_16x16x32_bf16 v[164:167], a[216:219], v[40:43], v[164:167]\n" \
"v_mfma_f32_16x16x32_bf16 v[168:171], a[220:223], v[64:67], v[168:171]\n" \
"s_waitcnt vmcnt(25) lgkmcnt(0)\n" \
"s_barrier\n" \
"v_mfma_f32_16x16x32_bf16 v[168:171], a[216:219], v[72:75], v[168:171]\n" \
"ds_read_b64_tr_b16 a[196:197], v220 offset:49152\n" \
"v_mfma_f32_16x16x32_bf16 v[172:175], a[220:223], v[96:99], v[172:175]\n" \
"ds_read_b64_tr_b16 a[198:199], v220 offset:51200\n" \
"v_mfma_f32_16x16x32_bf16 v[172:175], a[216:219], v[104:107], v[172:175]\n" \
"ds_read_b64_tr_b16 a[200:201], v220 offset:53248\n" \
"v_mfma_f32_16x16x32_bf16 v[160:163], a[208:211], v[16:19], v[160:163]\n" \
"ds_read_b64_tr_b16 a[202:203], v220 offset:55296\n" \
"buffer_load_dwordx4 a[180:183], v210, %[srdz], s50 offen offset:1024 sc1\n" \
"v_mfma_f32_16x16x32_bf16 v[160:163], a[224:227], v[24:27], v[160:163]\n" \
"ds_read_b64_tr_b16 a[204:205], v220 offset:57344\n" \
"v_mfma_f32_16x16x32_bf16 v[164:167], a[208:211], v[48:51], v[164:167]\n" \
"ds_read_b64_tr_b16 a[206:207], v220 offset:59392\n" \
"v_mfma_f32_16x16x32_bf16 v[164:167], a[224:227], v[56:59], v[164:167]\n" \
"ds_read_b64_tr_b16 a[220:221], v220 offset:61440\n" \
"v_mfma_f32_16x16x32_bf16 v[168:171], a[208:211], v[80:83], v[168:171]\n" \
"ds_read_b64_tr_b16 a[222:223], v220 offset:63488\n" \
"buffer_load_dwordx4 a[184:187], v210, %[srdz], s50 offen offset:2048 sc1\n" \
"v_mfma_f32_16x16x32_bf16 v[168:171], a[224:227], v[88:91], v[168:171]\n" \
"v_mfma_f32_16x16x32_bf16 v[172:175], a[208:211], v[112:115], v[172:175]\n" \
"buffer_load_dwordx4 a[188:191], v210, %[srdz], s50 offen offset:3072 sc1\n" \
"v_mfma_f32_16x16x32_bf16 v[172:175], a[224:227], v[120:123], v[172:175]\n" \
"s_waitcnt vmcnt(63) lgkmcnt(4)\n" \
"s_nop 0\n" \
"v_mfma_f32_16x16x32_bf16 v[176:179], a[228:231], v[0:3], v[176:179]\n" \
"v_mfma_f32_16x16x32_bf16 v[176:179], a[236:239], v[8:11], v[176:179]\n" \
"v_mfma_f32_16x16x32_bf16 v[180:183], a[228:231], v[32:35], v[180:183]\n" \
"v_mfma_f32_16x16x32_bf16 v[180:183], a[236:239], v[40:43], v[180:183]\n" \
"v_mfma_f32_16x16x32_bf16 v[184:187], a[228:231], v[64:67], v[184:187]\n" \
"v_mfma_f32_16x16x32_bf16 v[184:187], a[236:239], v[72:75], v[184:187]\n" \
"v_mfma_f32_16x16x32_bf16 v[188:191], a[228:231], v[96:99], v[188:191]\n" \
"ds_read_b64_tr_b16 a[216:217], v221 offset:49152\n" \
"s_waitcnt vmcnt(22)\n" \
"v_cmp_eq_u32_e32 vcc, 0, v232\n" \
"s_cbranch_vccnz Lspp_%=\n" \
"Lbkp_%=:\n" \
"s_nop 0\n" \
"v_mfma_f32_16x16x32_bf16 v[188:191], a[236:239], v[104:107], v[188:191]\n" \
"ds_read_b64_tr_b16 a[218:219], v221 offset:51200\n" \
"v_mfma_f32_16x16x32_bf16 v[192:195], a[196:199], a[0:3], v[128:131]\n" \
"ds_read_b64_tr_b16 a[208:209], v221 offset:53248\n" \
"v_mfma_f32_16x16x32_bf16 v[192:195], a[200:203], a[4:7], v[192:195]\n" \
"ds_read_b64_tr_b16 a[210:211], v221 offset:55296\n" \
"s_add_u32 m0, %[m0b], 0\n" \
"s_cmp_gt_u32 s48, 3\n" \
"s_cselect_b32 s49, 3, 4\n" \
"s_lshl_b32 s49, s49, 18\n" \
"s_or_b32 s49, s49, s59\n" \
"s_nop 0\n" \
"buffer_load_dwordx4 %[wo0], %[srdt], s49 offen offset:0 lds\n" \
"v_mfma_f32_16x16x32_bf16 v[196:199], a[196:199], a[16:19], v[132:135]\n" \
"v_mfma_f32_16x16x32_bf16 v[196:199], a[200:203], a[20:23], v[196:199]\n" \
"v_mfma_f32_16x16x32_bf16 v[200:203], a[196:199], a[32:35], v[136:139]\n" \
"v_mfma_f32_16x16x32_bf16 v[200:203], a[200:203], a[36:39], v[200:203]\n" \
"v_mfma_f32_16x16x32_bf16 v[204:207], a[196:199], a[48:51], v[140:143]\n" \
"ds_read_b64_tr_b16 a[224:225], v221 offset:57344\n" \
"v_mfma_f32_16x16x32_bf16 v[204:207], a[200:203], a[52:55], v[204:207]\n" \
"s_waitcnt vmcnt(20) lgkmcnt(5)\n" \
"s_nop 0\n" \
"v_mfma_f32_16x16x32_bf16 v[192:195], a[204:207], a[8:11], v[192:195]\n" \
"ds_read_b64_tr_b16 a[226:227], v221 offset:59392\n" \
"v_mfma_f32_16x16x32_bf16 v[192:195], a[220:223], a[12:15], v[192:195]\n" \
"ds_read_b64_tr_b16 a[228:229], v221 offset:61440\n" \
"v_mfma_f32_16x16x32_bf16 v[196:199], a[204:207], a[24:27], v[196:199]\n" \
"ds_read_b64_tr_b16 a[230:231], v221 offset:63488\n" \
"v_mfma_f32_16x16x32_bf16 v[196:199], a[220:223], a[28:31], v[196:199]\n" \
"buffer_load_dwordx4 v212, %[srdt], s49 offen offset:1024 lds\n" \
"v_mfma_f32_16x16x32_bf16 v[200:203], a[204:207], a[40:43], v[200:203]\n" \
"v_mfma_f32_16x16x32_bf16 v[200:203], a[220:223], a[44:47], v[200:203]\n" \
"v_mfma_f32_16x16x32_bf16 v[204:207], a[204:207], a[56:59], v[204:207]\n" \
"ds_read_b64_tr_b16 a[236:237], v220 offset:49664\n" \
"v_mfma_f32_16x16x32_bf16 v[204:207], a[220:223], a[60:63], v[204:207]\n" \
"ds_read_b64_tr_b16 a[238:239], v220 offset:51712\n" \
"v_mfma_f32_16x16x32_bf16 v[176:179], a[232:235], v[16:19], v[176:179]\n" \
"v_mfma_f32_16x16x32_bf16 v[176:179], a[212:215], v[24:27], v[176:179]\n" \
"v_mfma_f32_16x16x32_bf16 v[180:183], a[232:235], v[48:51], v[180:183]\n" \
"buffer_load_dwordx4 v213, %[srdt], s49 offen offset:2048 lds\n" \
"v_mfma_f32_16x16x32_bf16 v[180:183], a[212:215], v[56:59], v[180:183]\n" \
"v_mfma_f32_16x16x32_bf16 v[184:187], a[232:235], v[80:83], v[184:187]\n" \
"s_add_u32 m0, %[m0b], 98304\n" \
"s_mov_b32 s61, 0\n" \
"s_nop 0\n" \
"buffer_load_dwordx4 %[vdn], s[64:67], s61 offen offset:0 lds\n" \
"v_mfma_f32_16x16x32_bf16 v[184:187], a[212:215], v[88:91], v[184:187]\n" \
"v_cmp_u_f32_e64 s[44:45], v192, v196\n" \
"v_mfma_f32_16x16x32_bf16 v[188:191], a[232:235], v[112:115], v[188:191]\n" \
"v_mfma_f32_16x16x32_bf16 v[188:191], a[212:215], v[120:123], v[188:191]\n" \
"v_cmp_u_f32_e64 s[56:57], v200, v204\n" \
"s_or_b64 s[56:57], s[56:57], s[44:45]\n" \
"s_cbranch_scc1 Lsp6_%=\n" \
"Lbk6_%=:\n" \
"s_waitcnt vmcnt(63) lgkmcnt(2)\n" \
"s_nop 0\n" \
"v_mfma_f32_16x16x32_bf16 v[144:147], a[216:219], a[0:3], v[144:147]\n" \
"ds_read_b64_tr_b16 a[232:233], v220 offset:53760\n" \
"v_mfma_f32_16x16x32_bf16 v[144:147], a[208:211], a[4:7], v[144:147]\n" \
"ds_read_b64_tr_b16 a[234:235], v220 offset:55808\n" \
"v_mfma_f32_16x16x32_bf16 v[148:151], a[216:219], a[16:19], v[148:151]\n" \
"ds_read_b64_tr_b16 a[196:197], v220 offset:57856\n" \
"v_mfma_f32_16x16x32_bf16 v[148:151], a[208:211], a[20:23], v[148:151]\n" \
"ds_read_b64_tr_b16 a[198:199], v220 offset:59904\n" \
"v_mfma_f32_16x16x32_bf16 v[152:155], a[216:219], a[32:35], v[152:155]\n" \
"ds_read_b64_tr_b16 a[200:201], v220 offset:61952\n" \
"v_mfma_f32_16x16x32_bf16 v[152:155], a[208:211], a[36:39], v[152:155]\n" \
"ds_read_b64_tr_b16 a[202:203], v220 offset:64000\n" \
"v_mfma_f32_16x16x32_bf16 v[156:159], a[216:219], a[48:51], v[156:159]\n" \
"ds_read_b64_tr_b16 a[204:205], v221 offset:49664\n" \
"v_mfma_f32_16x16x32_bf16 v[156:159], a[208:211], a[52:55], v[156:159]\n" \
"ds_read_b64_tr_b16 a[206:207], v221 offset:51712\n" \
"s_add_u32 m0, %[m0b], 0\n" \
"s_nop 0\n" \
"buffer_load_dwordx4 v214, %[srdt], s49 offen offset:3072 lds\n" \
"v_mfma_f32_16x16x32_bf16 v[144:147], a[224:227], a[8:11], v[144:147]\n" \
"ds_read_b64_tr_b16 a[212:213], v221 offset:57856\n" \
"v_mfma_f32_16x16x32_bf16 v[144:147], a[228:231], a[12:15], v[144:147]\n" \
"ds_read_b64_tr_b16 a[214:215], v221 offset:59904\n" \
"v_mfma_f32_16x16x32_bf16 v[148:151], a[224:227], a[24:27], v[148:151]\n" \
"ds_read_b64_tr_b16 a[220:221], v221 offset:53760\n" \
"v_mfma_f32_16x16x32_bf16 v[148:151], a[228:231], a[28:31], v[148:151]\n" \
"ds_read_b64_tr_b16 a[222:223], v221 offset:55808\n" \
"v_mfma_f32_16x16x32_bf16 v[152:155], a[224:227], a[40:43], v[152:155]\n" \
"ds_read_b64_tr_b16 a[216:217], v221 offset:61952\n" \
"v_mfma_f32_16x16x32_bf16 v[152:155], a[228:231], a[44:47], v[152:155]\n" \
"ds_read_b64_tr_b16 a[218:219], v221 offset:64000\n" \
"v_mfma_f32_16x16x32_bf16 v[156:159], a[224:227], a[56:59], v[156:159]\n" \
"s_add_u32 m0, %[m0b], 99328\n" \
"s_mov_b32 s61, 4096\n" \
"buffer_load_dwordx4 %[vdn], s[64:67], s61 offen offset:0 lds\n" \
"v_mfma_f32_16x16x32_bf16 v[156:159], a[228:231], a[60:63], v[156:159]\n" \
"s_waitcnt vmcnt(63) lgkmcnt(12)\n" \
"s_nop 0\n" \
"v_mfma_f32_16x16x32_bf16 v[160:163], a[236:239], a[0:3], v[160:163]\n" \
"v_mfma_f32_16x16x32_bf16 v[160:163], a[232:235], a[4:7], v[160:163]\n" \
"v_mfma_f32_16x16x32_bf16 v[164:167], a[236:239], a[16:19], v[164:167]\n" \
"v_mfma_f32_16x16x32_bf16 v[164:167], a[232:235], a[20:23], v[164:167]\n" \
"s_add_u32 m0, %[m0b], 16384\n" \
"s_cmp_gt_u32 s48, 4\n" \
"s_cselect_b32 s49, 4, 5\n" \
"s_lshl_b32 s49, s49, 18\n" \
"s_or_b32 s49, s49, s59\n" \
"buffer_load_dwordx4 %[wo0], %[srdt], s49 offen offset:0 lds\n" \
"v_mfma_f32_16x16x32_bf16 v[168:171], a[236:239], a[32:35], v[168:171]\n" \
"s_waitcnt vmcnt(14) lgkmcnt(0)\n" \
"s_barrier\n" \
"v_mfma_f32_16x16x32_bf16 v[168:171], a[232:235], a[36:39], v[168:171]\n" \
"ds_read_b64_tr_b16 a[208:209], v215 offset:49152\n" \
"v_mfma_f32_16x16x32_bf16 v[172:175], a[236:239], a[48:51], v[172:175]\n" \
"ds_read_b64_tr_b16 a[210:211], v215 offset:51200\n" \
"v_mfma_f32_16x16x32_bf16 v[172:175], a[232:235], a[52:55], v[172:175]\n" \
"ds_read_b64_tr_b16 a[224:225], v215 offset:53248\n" \
"v_mfma_f32_16x16x32_bf16 v[160:163], a[196:199], a[8:11], v[160:163]\n" \
"ds_read_b64_tr_b16 a[226:227], v215 offset:55296\n" \
"v_mfma_f32_16x16x32_bf16 v[160:163], a[200:203], a[12:15], v[160:163]\n" \
"ds_read_b64_tr_b16 a[228:229], v215 offset:57344\n" \
"v_mfma_f32_16x16x32_bf16 v[164:167], a[196:199], a[24:27], v[164:167]\n" \
"ds_read_b64_tr_b16 a[230:231], v215 offset:59392\n" \
"v_mfma_f32_16x16x32_bf16 v[164:167], a[200:203], a[28:31], v[164:167]\n" \
"ds_read_b64_tr_b16 a[236:237], v215 offset:61440\n" \
"v_mfma_f32_16x16x32_bf16 v[168:171], a[196:199], a[40:43], v[168:171]\n" \
"ds_read_b64_tr_b16 a[238:239], v215 offset:63488\n" \
"v_mfma_f32_16x16x32_bf16 v[168:171], a[200:203], a[44:47], v[168:171]\n" \
"buffer_load_dwordx4 v212, %[srdt], s49 offen offset:1024 lds\n" \
"v_mfma_f32_16x16x32_bf16 v[172:175], a[196:199], a[56:59], v[172:175]\n" \
"v_mfma_f32_16x16x32_bf16 v[172:175], a[200:203], a[60:63], v[172:175]\n" \
"s_waitcnt vmcnt(10) lgkmcnt(4)\n" \
"s_nop 0\n" \
"v_mfma_f32_16x16x32_bf16 v[176:179], a[204:207], a[0:3], v[176:179]\n" \
"s_add_u32 m0, %[m0b], 100352\n" \
"s_mov_b32 s61, 8192\n" \
"buffer_load_dwordx4 %[vdn], s[64:67], s61 offen offset:0 lds\n" \
"v_mfma_f32_16x16x32_bf16 v[176:179], a[220:223], a[4:7], v[176:179]\n" \
"v_mfma_f32_16x16x32_bf16 v[180:183], a[204:207], a[16:19], v[180:183]\n" \
"v_mfma_f32_16x16x32_bf16 v[180:183], a[220:223], a[20:23], v[180:183]\n" \
"v_mfma_f32_16x16x32_bf16 v[184:187], a[204:207], a[32:35], v[184:187]\n" \
"v_mfma_f32_16x16x32_bf16 v[184:187], a[220:223], a[36:39], v[184:187]\n" \
"v_mfma_f32_16x16x32_bf16 v[188:191], a[204:207], a[48:51], v[188:191]\n" \
"ds_read_b64_tr_b16 a[232:233], v227 offset:49152\n" \
"v_mfma_f32_16x16x32_bf16 v[188:191], a[220:223], a[52:55], v[188:191]\n" \
"ds_read_b64_tr_b16 a[234:235], v227 offset:51200\n" \
"v_mfma_f32_16x16x32_bf16 v[128:131], a[208:211], a[128:131], v[192:195]\n" \
"ds_read_b64_tr_b16 a[196:197], v227 offset:53248\n" \
"v_mfma_f32_16x16x32_bf16 v[128:131], a[224:227], a[132:135], v[128:131]\n" \
"ds_read_b64_tr_b16 a[198:199], v227 offset:55296\n" \
"v_mfma_f32_16x16x32_bf16 v[132:135], a[208:211], a[144:147], v[196:199]\n" \
"s_add_u32 m0, %[m0b], 16384\n" \
"s_nop 0\n" \
"s_nop 0\n" \
"buffer_load_dwordx4 v213, %[srdt], s49 offen offset:2048 lds\n" \
"v_mfma_f32_16x16x32_bf16 v[132:135], a[224:227], a[148:151], v[132:135]\n" \
"v_mfma_f32_16x16x32_bf16 v[136:139], a[208:211], a[160:163], v[200:203]\n" \
"v_mfma_f32_16x16x32_bf16 v[136:139], a[224:227], a[164:167], v[136:139]\n" \
"v_mfma_f32_16x16x32_bf16 v[140:143], a[208:211], a[176:179], v[204:207]\n" \
"ds_read_b64_tr_b16 a[200:201], v227 offset:57344\n" \
"v_mfma_f32_16x16x32_bf16 v[140:143], a[224:227], a[180:183], v[140:143]\n" \
"s_waitcnt vmcnt(10) lgkmcnt(5)\n" \
"s_nop 0\n" \
"v_mfma_f32_16x16x32_bf16 v[128:131], a[228:231], a[136:139], v[128:131]\n" \
"ds_read_b64_tr_b16 a[202:203], v227 offset:59392\n" \
"v_mfma_f32_16x16x32_bf16 v[128:131], a[236:239], a[140:143], v[128:131]\n" \
"ds_read_b64_tr_b16 a[204:205], v227 offset:61440\n" \
"s_add_u32 m0, %[m0b], 101376\n" \
"s_mov_b32 s61, 12288\n" \
"buffer_load_dwordx4 %[vdn], s[64:67], s61 offen offset:0 lds\n" \
"v_mfma_f32_16x16x32_bf16 v[132:135], a[228:231], a[152:155], v[132:135]\n" \
"ds_read_b64_tr_b16 a[206:207], v227 offset:63488\n" \
"v_mfma_f32_16x16x32_bf16 v[132:135], a[236:239], a[156:159], v[132:135]\n" \
"s_add_u32 m0, %[m0b], 16384\n" \
"s_nop 0\n" \
"s_nop 0\n" \
"buffer_load_dwordx4 v214, %[srdt], s49 offen offset:3072 lds\n" \
"v_mfma_f32_16x16x32_bf16 v[136:139], a[228:231], a[168:171], v[136:139]\n" \
"v_mfma_f32_16x16x32_bf16 v[136:139], a[236:239], a[172:175], v[136:139]\n" \
"v_mfma_f32_16x16x32_bf16 v[140:143], a[228:231], a[184:187], v[140:143]\n" \
"ds_read_b64_tr_b16 a[220:221], v215 offset:49664\n" \
"v_mfma_f32_16x16x32_bf16 v[140:143], a[236:239], a[188:191], v[140:143]\n" \
"ds_read_b64_tr_b16 a[222:223], v215 offset:51712\n" \
"v_mfma_f32_16x16x32_bf16 v[176:179], a[212:215], a[8:11], v[176:179]\n" \
"v_mfma_f32_16x16x32_bf16 v[176:179], a[216:219], a[12:15], v[176:179]\n" \
"v_mfma_f32_16x16x32_bf16 v[180:183], a[212:215], a[24:27], v[180:183]\n" \
"v_mfma_f32_16x16x32_bf16 v[180:183], a[216:219], a[28:31], v[180:183]\n" \
"v_mfma_f32_16x16x32_bf16 v[184:187], a[212:215], a[40:43], v[184:187]\n" \
"v_mfma_f32_16x16x32_bf16 v[184:187], a[216:219], a[44:47], v[184:187]\n" \
"v_cmp_u_f32_e64 s[44:45], v128, v132\n" \
"v_mfma_f32_16x16x32_bf16 v[188:191], a[212:215], a[56:59], v[188:191]\n" \
"v_mfma_f32_16x16x32_bf16 v[188:191], a[216:219], a[60:63], v[188:191]\n" \
"v_cmp_u_f32_e64 s[56:57], v136, v140\n" \
"s_or_b64 s[56:57], s[56:57], s[44:45]\n" \
"s_cbranch_scc1 Lsp7_%=\n" \
"Lbk7_%=:\n" \
"s_waitcnt vmcnt(63) lgkmcnt(2)\n" \
"s_nop 0\n" \
"v_mfma_f32_16x16x32_bf16 v[144:147], a[232:235], a[128:131], v[144:147]\n" \
"ds_read_b64_tr_b16 a[212:213], v215 offset:53760\n" \
"v_mfma_f32_16x16x32_bf16 v[144:147], a[196:199], a[132:135], v[144:147]\n" \
"ds_read_b64_tr_b16 a[214:215], v215 offset:55808\n" \
"v_mfma_f32_16x16x32_bf16 v[148:151], a[232:235], a[144:147], v[148:151]\n" \
"ds_read_b64_tr_b16 a[208:209], v215 offset:57856\n" \
"v_mfma_f32_16x16x32_bf16 v[148:151], a[196:199], a[148:151], v[148:151]\n" \
"ds_read_b64_tr_b16 a[210:211], v215 offset:59904\n" \
"s_lshl_b32 s61, %[wid], 10\n" \
"v_lshrrev_b32_e32 v231, 3, v224\n" \
"v_add_u32_e64 v231, s61, v231\n" \
"s_add_u32 m0, %[m0b], 32768\n" \
"s_cmp_gt_u32 s48, 5\n" \
"s_cselect_b32 s49, 5, 6\n" \
"s_lshl_b32 s49, s49, 18\n" \
"s_or_b32 s49, s49, s59\n" \
".if . - adaln_body != 53248\n" \
".error \"size model\"\n" \
".endif\n" \
"Lk12_%=:\n" \
"s_cbranch_execz Lk13_%=\n" \
"s_nop 0\n" \
"buffer_load_dwordx4 %[wo0], %[srdt], s49 offen offset:0 lds\n" \
"v_mfma_f32_16x16x32_bf16 v[152:155], a[232:235], a[160:163], v[152:155]\n" \
"ds_read_b64_tr_b16 a[216:217], v215 offset:61952\n" \
"v_readlane_b32 s44, %[tlo], 20\n" \
"v_readlane_b32 s45, %[thi], 20\n" \
"v_lshrrev_b32_e64 v236, 1, v231\n" \
"v_mfma_f32_16x16x32_bf16 v[152:155], a[196:199], a[164:167], v[152:155]\n" \
"ds_read_b64_tr_b16 a[218:219], v215 offset:64000\n" \
"v_mfma_f32_16x16x32_bf16 v[156:159], a[232:235], a[176:179], v[156:159]\n" \
"ds_read_b64_tr_b16 a[224:225], v227 offset:49664\n" \
"v_mfma_f32_16x16x32_bf16 v[156:159], a[196:199], a[180:183], v[156:159]\n" \
"ds_read_b64_tr_b16 a[226:227], v227 offset:51712\n" \
"v_mfma_f32_16x16x32_bf16 v[144:147], a[200:203], a[136:139], v[144:147]\n" \
"ds_read_b64_tr_b16 a[228:229], v227 offset:53760\n" \
"v_mfma_f32_16x16x32_bf16 v[144:147], a[204:207], a[140:143], v[144:147]\n" \
"ds_read_b64_tr_b16 a[230:231], v227 offset:55808\n" \
"v_mfma_f32_16x16x32_bf16 v[148:151], a[200:203], a[152:155], v[148:151]\n" \
"s_mov_b32 s61, 262144\n" \
"buffer_load_dwordx4 v[192:195], v231, %[srdz], s61 offen offset:0 sc1\n" \
"v_mfma_f32_16x16x32_bf16 v[148:151], a[204:207], a[156:159], v[148:151]\n" \
"v_mfma_f32_16x16x32_bf16 v[152:155], a[200:203], a[168:171], v[152:155]\n" \
"buffer_load_dwordx4 v212, %[srdt], s49 offen offset:1024 lds\n" \
"v_mfma_f32_16x16x32_bf16 v[152:155], a[204:207], a[172:175], v[152:155]\n" \
"v_mfma_f32_16x16x32_bf16 v[156:159], a[200:203], a[184:187], v[156:159]\n" \
"ds_read_b64_tr_b16 a[236:237], v227 offset:57856\n" \
"v_mfma_f32_16x16x32_bf16 v[156:159], a[204:207], a[188:191], v[156:159]\n" \
"s_waitcnt vmcnt(63) lgkmcnt(5)\n" \
"s_nop 0\n" \
"v_mfma_f32_16x16x32_bf16 v[160:163], a[220:223], a[128:131], v[160:163]\n" \
"ds_read_b64_tr_b16 a[238:239], v227 offset:59904\n" \
"v_mfma_f32_16x16x32_bf16 v[160:163], a[212:215], a[132:135], v[160:163]\n" \
"ds_read_b64_tr_b16 a[232:233], v227 offset:61952\n" \
"v_mfma_f32_16x16x32_bf16 v[164:167], a[220:223], a[144:147], v[164:167]\n" \
"ds_read_b64_tr_b16 a[234:235], v227 offset:64000\n" \
"s_mov_b32 s61, 266240\n" \
"buffer_load_dwordx4 v[196:199], v231, %[srdz], s61 offen offset:0 sc1\n" \
"v_mfma_f32_16x16x32_bf16 v[164:167], a[212:215], a[148:151], v[164:167]\n" \
"v_mfma_f32_16x16x32_bf16 v[168:171], a[220:223], a[160:163], v[168:171]\n" \
"v_cvt_pk_bf16_f32 v128, v128, v129\n" \
"buffer_load_dwordx4 v213, %[srdt], s49 offen offset:2048 lds\n" \
"v_mfma_f32_16x16x32_bf16 v[168:171], a[212:215], a[164:167], v[168:171]\n" \
"v_cvt_pk_bf16_f32 v129, v130, v131\n" \
"v_mfma_f32_16x16x32_bf16 v[172:175], a[220:223], a[176:179], v[172:175]\n" \
"v_cvt_pk_bf16_f32 v130, v144, v145\n" \
"v_mfma_f32_16x16x32_bf16 v[172:175], a[212:215], a[180:183], v[172:175]\n" \
"v_cvt_pk_bf16_f32 v131, v146, v147\n" \
"v_mfma_f32_16x16x32_bf16 v[160:163], a[208:211], a[136:139], v[160:163]\n" \
"s_add_u32 s55, s46, 0\n" \
"s_nop 0\n" \
"buffer_store_dwordx4 v[128:131], %[vxs], %[srdz], s55 offen offset:0 sc1\n" \
"v_mfma_f32_16x16x32_bf16 v[160:163], a[216:219], a[140:143], v[160:163]\n" \
"v_mfma_f32_16x16x32_bf16 v[164:167], a[208:211], a[152:155], v[164:167]\n" \
"v_cvt_pk_bf16_f32 v132, v132, v133\n" \
"s_mov_b32 s61, 270336\n" \
"buffer_load_dwordx4 v[200:203], v231, %[srdz], s61 offen offset:0 sc1\n" \
"v_mfma_f32_16x16x32_bf16 v[164:167], a[216:219], a[156:159], v[164:167]\n" \
"v_cvt_pk_bf16_f32 v133, v134, v135\n" \
"v_mfma_f32_16x16x32_bf16 v[168:171], a[208:211], a[168:171], v[168:171]\n" \
"s_waitcnt vmcnt(14) lgkmcnt(0)\n" \
"s_barrier\n" \
"v_mfma_f32_16x16x32_bf16 v[168:171], a[216:219], a[172:175], v[168:171]\n" \
"ds_read_b64_tr_b16 a[196:197], v215 offset:0\n" \
"v_cvt_pk_bf16_f32 v134, v148, v149\n" \
"v_mfma_f32_16x16x32_bf16 v[172:175], a[208:211], a[184:187], v[172:175]\n" \
"ds_read_b64_tr_b16 a[198:199], v215 offset:2048\n" \
"v_cvt_pk_bf16_f32 v135, v150, v151\n" \
"v_mfma_f32_16x16x32_bf16 v[172:175], a[216:219], a[188:191], v[172:175]\n" \
"s_waitcnt vmcnt(63) lgkmcnt(2)\n" \
"s_nop 0\n" \
"v_mfma_f32_16x16x32_bf16 v[176:179], a[224:227], a[128:131], v[176:179]\n" \
"ds_read_b64_tr_b16 a[200:201], v215 offset:4096\n" \
"v_cvt_pk_bf16_f32 v136, v136, v137\n" \
"v_mfma_f32_16x16x32_bf16 v[176:179], a[228:231], a[132:135], v[176:179]\n" \
"ds_read_b64_tr_b16 a[202:203], v215 offset:6144\n" \
"v_cvt_pk_bf16_f32 v137, v138, v139\n" \
"v_mfma_f32_16x16x32_bf16 v[180:183], a[224:227], a[144:147], v[180:183]\n" \
"ds_read_b64_tr_b16 a[204:205], v215 offset:8192\n" \
"s_add_u32 s55, s46, 524288\n" \
"buffer_store_dwordx4 v[132:135], %[vxs], %[srdz], s55 offen offset:0 sc1\n" \
"v_mfma_f32_16x16x32_bf16 v[180:183], a[228:231], a[148:151], v[180:183]\n" \
"ds_read_b64_tr_b16 a[206:207], v215 offset:10240\n" \
"v_cvt_pk_bf16_f32 v138, v152, v153\n" \
"v_mfma_f32_16x16x32_bf16 v[184:187], a[224:227], a[160:163], v[184:187]\n" \
"ds_read_b64_tr_b16 a[220:221], v215 offset:12288\n" \
"v_cvt_pk_bf16_f32 v139, v154, v155\n" \
"s_mov_b32 s61, 274432\n" \
"buffer_load_dwordx4 v[204:207], v231, %[srdz], s61 offen offset:0 sc1\n" \
"v_mfma_f32_16x16x32_bf16 v[184:187], a[228:231], a[164:167], v[184:187]\n" \
"ds_read_b64_tr_b16 a[222:223], v215 offset:14336\n" \
"v_mfma_f32_16x16x32_bf16 v[188:191], a[224:227], a[176:179], v[188:191]\n" \
"ds_read_b64_tr_b16 a[212:213], v227 offset:0\n" \
"v_cvt_pk_bf16_f32 v140, v140, v141\n" \
"buffer_load_dwordx4 v214, %[srdt], s49 offen offset:3072 lds\n" \
"v_mfma_f32_16x16x32_bf16 v[188:191], a[228:231], a[180:183], v[188:191]\n" \
"ds_read_b64_tr_b16 a[214:215], v227 offset:2048\n" \
"v_cvt_pk_bf16_f32 v141, v142, v143\n" \
"v_mfma_f32_16x16x32_bf16 v[176:179], a[236:239], a[136:139], v[176:179]\n" \
"ds_read_b64_tr_b16 a[208:209], v227 offset:4096\n" \
"s_add_u32 s55, s46, 1048576\n" \
"buffer_store_dwordx4 v[136:139], %[vxs], %[srdz], s55 offen offset:0 sc1\n" \
"v_mfma_f32_16x16x32_bf16 v[176:179], a[232:235], a[140:143], v[176:179]\n" \
"ds_read_b64_tr_b16 a[210:211], v227 offset:6144\n" \
"v_cvt_pk_bf16_f32 v142, v156, v157\n" \
"v_mfma_f32_16x16x32_bf16 v[180:183], a[236:239], a[152:155], v[180:183]\n" \
"v_cvt_pk_bf16_f32 v143, v158, v159\n" \
"v_mfma_f32_16x16x32_bf16 v[180:183], a[232:235], a[156:159], v[180:183]\n" \
"s_add_u32 s55, s46, 1572864\n" \
"buffer_store_dwordx4 v[140:143], %[vxs], %[srdz], s55 offen offset:0 sc1\n" \
"v_mfma_f32_16x16x32_bf16 v[184:187], a[236:239], a[168:171], v[184:187]\n" \
"v_mfma_f32_16x16x32_bf16 v[184:187], a[232:235], a[172:175], v[184:187]\n" \
"global_load_dwordx2 v[232:233], v236, s[44:45]\n" \
"v_mfma_f32_16x16x32_bf16 v[188:191], a[236:239], a[184:187], v[188:191]\n" \
"ds_read_b64_tr_b16 a[216:217], v227 offset:8192\n" \
"v_mfma_f32_16x16x32_bf16 v[188:191], a[232:235], a[188:191], v[188:191]\n" \
"s_waitcnt vmcnt(63) lgkmcnt(5)\n" \
"s_nop 0\n" \
"v_mfma_f32_16x16x32_bf16 v[4:7], a[196:199], a[64:67], v[4:7]\n" \
"ds_read_b64_tr_b16 a[218:219], v227 offset:10240\n" \
"v_mfma_f32_16x16x32_bf16 v[4:7], a[200:203], a[68:71], v[4:7]\n" \
"ds_read_b64_tr_b16 a[224:225], v227 offset:12288\n" \
"v_mfma_f32_16x16x32_bf16 v[36:39], a[196:199], a[80:83], v[36:39]\n" \
"ds_read_b64_tr_b16 a[226:227], v227 offset:14336\n" \
"v_mfma_f32_16x16x32_bf16 v[36:39], a[200:203], a[84:87], v[36:39]\n" \
"v_mfma_f32_16x16x32_bf16 v[68:71], a[196:199], a[96:99], v[68:71]\n" \
"v_cvt_pk_bf16_f32 v160, v160, v161\n" \
"v_mfma_f32_16x16x32_bf16 v[68:71], a[200:203], a[100:103], v[68:71]\n" \
"v_cvt_pk_bf16_f32 v161, v162, v163\n" \
"v_mfma_f32_16x16x32_bf16 v[100:103], a[196:199], a[112:115], v[100:103]\n" \
"ds_read_b64_tr_b16 a[228:229], v215 offset:512\n" \
"v_cvt_pk_bf16_f32 v162, v176, v177\n" \
"v_mfma_f32_16x16x32_bf16 v[100:103], a[200:203], a[116:119], v[100:103]\n" \
"ds_read_b64_tr_b16 a[230:231], v215 offset:2560\n" \
"v_cvt_pk_bf16_f32 v163, v178, v179\n" \
"v_mfma_f32_16x16x32_bf16 v[4:7], a[204:207], a[72:75], v[4:7]\n" \
"ds_read_b64_tr_b16 a[236:237], v215 offset:4608\n" \
"v_mfma_f32_16x16x32_bf16 v[4:7], a[220:223], a[76:79], v[4:7]\n" \
"ds_read_b64_tr_b16 a[238:239], v215 offset:6656\n" \
"v_mfma_f32_16x16x32_bf16 v[36:39], a[204:207], a[88:91], v[36:39]\n" \
"s_add_u32 s55, s46, 4096\n" \
"buffer_store_dwordx4 v[160:163], %[vxs], %[srdz], s55 offen offset:0 sc1\n" \
"v_mfma_f32_16x16x32_bf16 v[36:39], a[220:223], a[92:95], v[36:39]\n" \
"v_cvt_pk_bf16_f32 v164, v164, v165\n" \
"v_cvt_pk_bf16_f32 v165, v166, v167\n" \
"v_mfma_f32_16x16x32_bf16 v[68:71], a[204:207], a[104:107], v[68:71]\n" \
"v_cvt_pk_bf16_f32 v166, v180, v181\n" \
"s_add_u32 m0, %[m0b], 49152\n" \
"s_cmp_gt_u32 s48, 6\n" \
"s_cselect_b32 s49, 6, 7\n" \
"s_lshl_b32 s49, s49, 18\n" \
"s_or_b32 s49, s49, s59\n" \
"buffer_load_dwordx4 %[wo0], %[srdt], s49 offen offset:0 lds\n" \
"v_mfma_f32_16x16x32_bf16 v[68:71], a[220:223], a[108:111], v[68:71]\n" \
"v_cvt_pk_bf16_f32 v167, v182, v183\n" \
"v_mfma_f32_16x16x32_bf16 v[100:103], a[204:207], a[120:123], v[100:103]\n" \
"ds_read_b64_tr_b16 a[232:233], v215 offset:8704\n" \
"v_mfma_f32_16x16x32_bf16 v[100:103], a[220:223], a[124:127], v[100:103]\n" \
"s_waitcnt vmcnt(63) lgkmcnt(5)\n" \
"s_nop 0\n" \
"v_mfma_f32_16x16x32_bf16 v[12:15], a[212:215], a[64:67], v[12:15]\n" \
"ds_read_b64_tr_b16 a[234:235], v215 offset:10752\n" \
"v_cvt_pk_bf16_f32 v168, v168, v169\n" \
"v_mfma_f32_16x16x32_bf16 v[12:15], a[208:211], a[68:71], v[12:15]\n" \
"ds_read_b64_tr_b16 a[196:197], v215 offset:12800\n" \
"s_add_u32 s55, s46, 528384\n" \
"buffer_store_dwordx4 v[164:167], %[vxs], %[srdz], s55 offen offset:0 sc1\n" \
"v_mfma_f32_16x16x32_bf16 v[44:47], a[212:215], a[80:83], v[44:47]\n" \
"ds_read_b64_tr_b16 a[198:199], v215 offset:14848\n" \
"v_cvt_pk_bf16_f32 v169, v170, v171\n" \
"v_mfma_f32_16x16x32_bf16 v[44:47], a[208:211], a[84:87], v[44:47]\n" \
"v_cvt_pk_bf16_f32 v170, v184, v185\n" \
"v_cvt_pk_bf16_f32 v171, v186, v187\n" \
"v_mfma_f32_16x16x32_bf16 v[76:79], a[212:215], a[96:99], v[76:79]\n" \
"s_add_u32 s55, s46, 1052672\n" \
"buffer_store_dwordx4 v[168:171], %[vxs], %[srdz], s55 offen offset:0 sc1\n" \
"v_mfma_f32_16x16x32_bf16 v[76:79], a[208:211], a[100:103], v[76:79]\n" \
"v_mfma_f32_16x16x32_bf16 v[108:111], a[212:215], a[112:115], v[108:111]\n" \
"ds_read_b64_tr_b16 a[200:201], v227 offset:512\n" \
"v_cvt_pk_bf16_f32 v172, v172, v173\n" \
"buffer_load_dwordx4 v212, %[srdt], s49 offen offset:1024 lds\n" \
"v_mfma_f32_16x16x32_bf16 v[108:111], a[208:211], a[116:119], v[108:111]\n" \
"ds_read_b64_tr_b16 a[202:203], v227 offset:2560\n" \
"v_cvt_pk_bf16_f32 v173, v174, v175\n" \
"v_mfma_f32_16x16x32_bf16 v[12:15], a[216:219], a[72:75], v[12:15]\n" \
"ds_read_b64_tr_b16 a[204:205], v227 offset:4608\n" \
"v_cvt_pk_bf16_f32 v174, v188, v189\n" \
"v_mfma_f32_16x16x32_bf16 v[12:15], a[224:227], a[76:79], v[12:15]\n" \
"ds_read_b64_tr_b16 a[206:207], v227 offset:6656\n" \
"v_cvt_pk_bf16_f32 v175, v190, v191\n" \
"v_mfma_f32_16x16x32_bf16 v[44:47], a[216:219], a[88:91], v[44:47]\n" \
"s_add_u32 s55, s46, 1576960\n" \
"buffer_store_dwordx4 v[172:175], %[vxs], %[srdz], s55 offen offset:0 sc1\n" \
"v_mfma_f32_16x16x32_bf16 v[44:47], a[224:227], a[92:95], v[44:47]\n" \
"v_mfma_f32_16x16x32_bf16 v[76:79], a[216:219], a[104:107], v[76:79]\n" \
"buffer_load_dwordx4 v213, %[srdt], s49 offen offset:2048 lds\n" \
"v_mfma_f32_16x16x32_bf16 v[76:79], a[224:227], a[108:111], v[76:79]\n" \
"v_mfma_f32_16x16x32_bf16 v[108:111], a[216:219], a[120:123], v[108:111]\n" \
"ds_read_b64_tr_b16 a[220:221], v227 offset:8704\n" \
"v_accvgpr_read_b32 v129, a240\n" \
"v_mfma_f32_16x16x32_bf16 v[108:111], a[224:227], a[124:127], v[108:111]\n" \
"s_waitcnt vmcnt(63) lgkmcnt(5)\n" \
"s_nop 0\n" \
"v_mfma_f32_16x16x32_bf16 v[20:23], a[228:231], a[64:67], v[20:23]\n" \
"ds_read_b64_tr_b16 a[222:223], v227 offset:10752\n" \
"v_lshlrev_b32_e64 v128, 16, v129\n" \
"v_mfma_f32_16x16x32_bf16 v[20:23], a[236:239], a[68:71], v[20:23]\n" \
"ds_read_b64_tr_b16 a[212:213], v227 offset:12800\n" \
"v_and_b32_e32 v129, 0xffff0000, v129\n" \
"v_mfma_f32_16x16x32_bf16 v[52:55], a[228:231], a[80:83], v[52:55]\n" \
"ds_read_b64_tr_b16 a[214:215], v227 offset:14848\n" \
"v_accvgpr_read_b32 v131, a241\n" \
"v_mfma_f32_16x16x32_bf16 v[52:55], a[236:239], a[84:87], v[52:55]\n" \
"v_lshlrev_b32_e64 v130, 16, v131\n" \
"v_and_b32_e32 v131, 0xffff0000, v131\n" \
"global_load_dword v237, %[vct], %[pct]\n" \
"v_mfma_f32_16x16x32_bf16 v[84:87], a[228:231], a[96:99], v[84:87]\n" \
"v_accvgpr_read_b32 v133, a242\n" \
"v_mfma_f32_16x16x32_bf16 v[84:87], a[236:239], a[100:103], v[84:87]\n" \
"v_lshlrev_b32_e64 v132, 16, v133\n" \
"s_add_u32 m0, %[m0b], 114688\n" \
"s_mov_b32 s61, 16384\n" \
"buffer_load_dwordx4 %[vdn], s[64:67], s61 offen offset:0 lds\n" \
"v_mfma_f32_16x16x32_bf16 v[116:119], a[228:231], a[112:115], v[116:119]\n" \
"v_and_b32_e32 v133, 0xffff0000, v133\n" \
"v_mfma_f32_16x16x32_bf16 v[116:119], a[236:239], a[116:119], v[116:119]\n" \
"v_accvgpr_read_b32 v135, a243\n" \
"s_add_u32 m0, %[m0b], 49152\n" \
"s_nop 0\n" \
"s_nop 0\n" \
"buffer_load_dwordx4 v214, %[srdt], s49 offen offset:3072 lds\n" \
"v_mfma_f32_16x16x32_bf16 v[20:23], a[232:235], a[72:75], v[20:23]\n" \
"v_lshlrev_b32_e64 v134, 16, v135\n" \
"v_mfma_f32_16x16x32_bf16 v[20:23], a[196:199], a[76:79], v[20:23]\n" \
"v_and_b32_e32 v135, 0xffff0000, v135\n" \
"v_mfma_f32_16x16x32_bf16 v[52:55], a[232:235], a[88:91], v[52:55]\n" \
"v_accvgpr_read_b32 v137, a244\n" \
"v_mfma_f32_16x16x32_bf16 v[52:55], a[196:199], a[92:95], v[52:55]\n" \
"v_lshlrev_b32_e64 v136, 16, v137\n" \
"v_mfma_f32_16x16x32_bf16 v[84:87], a[232:235], a[104:107], v[84:87]\n" \
"s_waitcnt vmcnt(23) lgkmcnt(0)\n" \
"s_barrier\n" \
"v_mfma_f32_16x16x32_bf16 v[84:87], a[196:199], a[108:111], v[84:87]\n" \
"ds_read_b64_tr_b16 a[208:209], v215 offset:16384\n" \
"v_and_b32_e32 v137, 0xffff0000, v137\n" \
"v_mfma_f32_16x16x32_bf16 v[116:119], a[232:235], a[120:123], v[116:119]\n" \
"ds_read_b64_tr_b16 a[210:211], v215 offset:18432\n" \
"v_accvgpr_read_b32 v139, a245\n" \
"v_mfma_f32_16x16x32_bf16 v[116:119], a[196:199], a[124:127], v[116:119]\n" \
"s_waitcnt vmcnt(63) lgkmcnt(2)\n" \
"s_nop 0\n" \
"v_mfma_f32_16x16x32_bf16 v[28:31], a[200:203], a[64:67], v[28:31]\n" \
"ds_read_b64_tr_b16 a[216:217], v215 offset:20480\n" \
"v_lshlrev_b32_e64 v138, 16, v139\n" \
"v_mfma_f32_16x16x32_bf16 v[28:31], a[204:207], a[68:71], v[28:31]\n" \
"ds_read_b64_tr_b16 a[218:219], v215 offset:22528\n" \
"v_and_b32_e32 v139, 0xffff0000, v139\n" \
"v_mfma_f32_16x16x32_bf16 v[60:63], a[200:203], a[80:83], v[60:63]\n" \
"ds_read_b64_tr_b16 a[224:225], v215 offset:24576\n" \
"v_accvgpr_read_b32 v141, a246\n" \
"v_mfma_f32_16x16x32_bf16 v[60:63], a[204:207], a[84:87], v[60:63]\n" \
"ds_read_b64_tr_b16 a[226:227], v215 offset:26624\n" \
"v_lshlrev_b32_e64 v140, 16, v141\n" \
"v_mfma_f32_16x16x32_bf16 v[92:95], a[200:203], a[96:99], v[92:95]\n" \
"ds_read_b64_tr_b16 a[228:229], v215 offset:28672\n" \
"v_and_b32_e32 v141, 0xffff0000, v141\n" \
"s_add_u32 m0, %[m0b], 115712\n" \
"s_mov_b32 s61, 20480\n" \
"buffer_load_dwordx4 %[vdn], s[64:67], s61 offen offset:0 lds\n" \
"v_mfma_f32_16x16x32_bf16 v[92:95], a[204:207], a[100:103], v[92:95]\n" \
"ds_read_b64_tr_b16 a[230:231], v215 offset:30720\n" \
"v_accvgpr_read_b32 v143, a247\n" \
"v_mfma_f32_16x16x32_bf16 v[124:127], a[200:203], a[112:115], v[124:127]\n" \
"ds_read_b64_tr_b16 a[236:237], v227 offset:16384\n" \
"v_lshlrev_b32_e64 v142, 16, v143\n" \
"v_mfma_f32_16x16x32_bf16 v[124:127], a[204:207], a[116:119], v[124:127]\n" \
"ds_read_b64_tr_b16 a[238:239], v227 offset:18432\n" \
"v_and_b32_e32 v143, 0xffff0000, v143\n" \
"v_mfma_f32_16x16x32_bf16 v[28:31], a[220:223], a[72:75], v[28:31]\n" \
"ds_read_b64_tr_b16 a[232:233], v227 offset:20480\n" \
"v_accvgpr_read_b32 v145, a248\n" \
"v_mfma_f32_16x16x32_bf16 v[28:31], a[212:215], a[76:79], v[28:31]\n" \
"ds_read_b64_tr_b16 a[234:235], v227 offset:22528\n" \
"v_lshlrev_b32_e64 v144, 16, v145\n" \
"v_mfma_f32_16x16x32_bf16 v[60:63], a[220:223], a[88:91], v[60:63]\n" \
"v_and_b32_e32 v145, 0xffff0000, v145\n" \
"v_accvgpr_read_b32 v147, a249\n" \
"v_mfma_f32_16x16x32_bf16 v[60:63], a[212:215], a[92:95], v[60:63]\n" \
"v_lshlrev_b32_e64 v146, 16, v147\n" \
"v_and_b32_e32 v147, 0xffff0000, v147\n" \
"v_mfma_f32_16x16x32_bf16 v[92:95], a[220:223], a[104:107], v[92:95]\n" \
"v_accvgpr_read_b32 v149, a250\n" \
"v_mfma_f32_16x16x32_bf16 v[92:95], a[212:215], a[108:111], v[92:95]\n" \
"v_lshlrev_b32_e64 v148, 16, v149\n" \
"v_mfma_f32_16x16x32_bf16 v[124:127], a[220:223], a[120:123], v[124:127]\n" \
"ds_read_b64_tr_b16 a[196:197], v227 offset:24576\n" \
"v_and_b32_e32 v149, 0xffff0000, v149\n" \
"v_mfma_f32_16x16x32_bf16 v[124:127], a[212:215], a[124:127], v[124:127]\n" \
"s_waitcnt vmcnt(63) lgkmcnt(5)\n" \
"s_nop 0\n" \
"v_mfma_f32_16x16x32_bf16 v[4:7], a[208:211], v[0:3], v[4:7]\n" \
"ds_read_b64_tr_b16 a[198:199], v227 offset:26624\n" \
"v_accvgpr_read_b32 v151, a251\n" \
"v_mfma_f32_16x16x32_bf16 v[4:7], a[216:219], v[8:11], v[4:7]\n" \
"ds_read_b64_tr_b16 a[200:201], v227 offset:28672\n" \
"v_lshlrev_b32_e64 v150, 16, v151\n" \
"v_mfma_f32_16x16x32_bf16 v[36:39], a[208:211], v[32:35], v[36:39]\n" \
"ds_read_b64_tr_b16 a[202:203], v227 offset:30720\n" \
"v_and_b32_e32 v151, 0xffff0000, v151\n" \
"v_mfma_f32_16x16x32_bf16 v[36:39], a[216:219], v[40:43], v[36:39]\n" \
"v_accvgpr_read_b32 v153, a252\n" \
"v_lshlrev_b32_e64 v152, 16, v153\n" \
"s_add_u32 m0, %[m0b], 116736\n" \
"s_mov_b32 s61, 24576\n" \
"buffer_load_dwordx4 %[vdn], s[64:67], s61 offen offset:0 lds\n" \
"v_mfma_f32_16x16x32_bf16 v[68:71], a[208:211], v[64:67], v[68:71]\n" \
"v_and_b32_e32 v153, 0xffff0000, v153\n" \
"v_mfma_f32_16x16x32_bf16 v[68:71], a[216:219], v[72:75], v[68:71]\n" \
"v_accvgpr_read_b32 v155, a253\n" \
"v_mfma_f32_16x16x32_bf16 v[100:103], a[208:211], v[96:99], v[100:103]\n" \
"v_lshlrev_b32_e64 v154, 16, v155\n" \
"v_mfma_f32_16x16x32_bf16 v[100:103], a[216:219], v[104:107], v[100:103]\n" \
"v_and_b32_e32 v155, 0xffff0000, v155\n" \
"v_mfma_f32_16x16x32_bf16 v[4:7], a[224:227], v[16:19], v[4:7]\n" \
"v_accvgpr_read_b32 v157, a254\n" \
"v_mfma_f32_16x16x32_bf16 v[4:7], a[228:231], v[24:27], v[4:7]\n" \
"v_lshlrev_b32_e64 v156, 16, v157\n" \
"v_mfma_f32_16x16x32_bf16 v[36:39], a[224:227], v[48:51], v[36:39]\n" \
"v_and_b32_e32 v157, 0xffff0000, v157\n" \
"v_mfma_f32_16x16x32_bf16 v[36:39], a[228:231], v[56:59], v[36:39]\n" \
"v_accvgpr_read_b32 v159, a255\n" \
"v_mfma_f32_16x16x32_bf16 v[68:71], a[224:227], v[80:83], v[68:71]\n" \
"s_waitcnt vmcnt(15) lgkmcnt(15)\n" \
"s_barrier\n" \
"v_mfma_f32_16x16x32_bf16 v[68:71], a[228:231], v[88:91], v[68:71]\n" \
"ds_read_b64_tr_b16 a[204:205], v215 offset:32768\n" \
"v_lshlrev_b32_e64 v158, 16, v159\n" \
"v_mfma_f32_16x16x32_bf16 v[100:103], a[224:227], v[112:115], v[100:103]\n" \
"ds_read_b64_tr_b16 a[206:207], v215 offset:34816\n" \
"v_and_b32_e32 v159, 0xffff0000, v159\n" \
"v_mfma_f32_16x16x32_bf16 v[100:103], a[228:231], v[120:123], v[100:103]\n" \
"s_waitcnt vmcnt(63) lgkmcnt(2)\n" \
"s_nop 0\n" \
"v_mfma_f32_16x16x32_bf16 v[12:15], a[236:239], v[0:3], v[12:15]\n" \
"ds_read_b64_tr_b16 a[220:221], v215 offset:36864\n" \
"v_and_b32_e32 v208, 0x7ff, %[vx0]\n" \
"v_mfma_f32_16x16x32_bf16 v[12:15], a[232:235], v[8:11], v[12:15]\n" \
"ds_read_b64_tr_b16 a[222:223], v215 offset:38912\n" \
"v_lshlrev_b32_e64 v208, 1, v208\n" \
"v_mfma_f32_16x16x32_bf16 v[44:47], a[236:239], v[32:35], v[44:47]\n" \
"ds_read_b64_tr_b16 a[212:213], v215 offset:40960\n" \
"v_xor_b32_e32 v209, 0x100, v208\n" \
"v_mfma_f32_16x16x32_bf16 v[44:47], a[232:235], v[40:43], v[44:47]\n" \
"ds_read_b64_tr_b16 a[214:215], v215 offset:43008\n" \
"v_lshrrev_b32_e64 v210, 3, v224\n" \
"v_mfma_f32_16x16x32_bf16 v[76:79], a[236:239], v[64:67], v[76:79]\n" \
"ds_read_b64_tr_b16 a[208:209], v215 offset:45056\n" \
"v_and_b32_e64 v210, 48, v210\n" \
"v_mfma_f32_16x16x32_bf16 v[76:79], a[232:235], v[72:75], v[76:79]\n" \
"ds_read_b64_tr_b16 a[210:211], v215 offset:47104\n" \
"s_lshl_b32 s61, %[wid], 12\n" \
"s_sub_u32 s61, %[m0b], s61\n" \
"v_add_u32_e32 v208, s61, v208\n" \
"v_add_u32_e32 v209, s61, v209\n" \
"v_add_u32_e64 v210, s61, v210\n" \
"v_mfma_f32_16x16x32_bf16 v[108:111], a[236:239], v[96:99], v[108:111]\n" \
"ds_read_b64_tr_b16 a[216:217], v227 offset:32768\n" \
"v_mfma_f32_16x16x32_bf16 v[108:111], a[232:235], v[104:107], v[108:111]\n" \
"ds_read_b64_tr_b16 a[218:219], v227 offset:34816\n" \
"s_waitcnt vmcnt(12)\n" \
"s_nop 0\n" \
"v_and_or_b32 v236, v192, v193, 0\n" \
"s_add_u32 m0, %[m0b], 117760\n" \
"s_mov_b32 s61, 28672\n" \
"buffer_load_dwordx4 %[vdn], s[64:67], s61 offen offset:0 lds\n" \
"v_mfma_f32_16x16x32_bf16 v[12:15], a[196:199], v[16:19], v[12:15]\n" \
"ds_read_b64_tr_b16 a[224:225], v227 offset:36864\n" \
"v_bitop3_b32 v236, v236, v194, v195 bitop3:0x80\n" \
"v_mfma_f32_16x16x32_bf16 v[12:15], a[200:203], v[24:27], v[12:15]\n" \
"ds_read_b64_tr_b16 a[226:227], v227 offset:38912\n" \
"v_bitop3_b32 v236, v236, v196, v197 bitop3:0x80\n" \
"v_mfma_f32_16x16x32_bf16 v[44:47], a[196:199], v[48:51], v[44:47]\n" \
"v_bitop3_b32 v236, v236, v198, v199 bitop3:0x80\n" \
"v_mfma_f32_16x16x32_bf16 v[44:47], a[200:203], v[56:59], v[44:47]\n" \
"v_bitop3_b32 v236, v236, v200, v201 bitop3:0x80\n" \
"v_mfma_f32_16x16x32_bf16 v[76:79], a[196:199], v[80:83], v[76:79]\n" \
"v_bitop3_b32 v236, v236, v202, v203 bitop3:0x80\n" \
"v_mfma_f32_16x16x32_bf16 v[76:79], a[200:203], v[88:91], v[76:79]\n" \
"v_bitop3_b32 v236, v236, v204, v205 bitop3:0x80\n" \
"v_mfma_f32_16x16x32_bf16 v[108:111], a[196:199], v[112:115], v[108:111]\n" \
"ds_read_b64_tr_b16 a[228:229], v227 offset:40960\n" \
"v_bitop3_b32 v236, v236, v206, v207 bitop3:0x80\n" \
"v_mfma_f32_16x16x32_bf16 v[108:111], a[200:203], v[120:123], v[108:111]\n" \
"s_waitcnt vmcnt(63) lgkmcnt(5)\n" \
"s_nop 0\n" \
"v_mfma_f32_16x16x32_bf16 v[4:7], a[204:207], a[0:3], v[4:7]\n" \
"ds_read_b64_tr_b16 a[230:231], v227 offset:43008\n" \
"s_mov_b32 s54, 0x07060302\n" \
"v_and_b32_e64 v236, 1, v236\n" \
"v_mfma_f32_16x16x32_bf16 v[4:7], a[220:223], a[4:7], v[4:7]\n" \
"ds_read_b64_tr_b16 a[236:237], v227 offset:45056\n" \
"v_lshrrev_b32_e64 v218, 1, v231\n" \
"v_mfma_f32_16x16x32_bf16 v[36:39], a[204:207], a[16:19], v[36:39]\n" \
"ds_read_b64_tr_b16 a[238:239], v227 offset:47104\n" \
"v_cmp_eq_u32_e64 s[56:57], 0, v236\n" \
"v_mfma_f32_16x16x32_bf16 v[36:39], a[220:223], a[20:23], v[36:39]\n" \
"s_waitcnt vmcnt(7)\n" \
"v_mov_b32_e32 v230, 1\n" \
"buffer_store_dword v230, v225, %[srdz], s62 offen offset:68 sc1\n" \
"v_mfma_f32_16x16x32_bf16 v[68:71], a[204:207], a[32:35], v[68:71]\n" \
"s_lshl_b32 s61, %[wid], 12\n" \
"s_sub_u32 s61, %[m0b], s61\n" \
"v_add_u32_e32 v231, s61, v231\n" \
"v_add_u32_e32 v218, s61, v218\n" \
"v_mov_b32_e64 v219, s61\n" \
"ds_write_b128 v231, v[192:195] offset:0\n" \
"v_mfma_f32_16x16x32_bf16 v[68:71], a[220:223], a[36:39], v[68:71]\n" \
"v_lshlrev_b32_e64 v234, 16, v233\n" \
"v_and_b32_e32 v235, 0xffff0000, v233\n" \
"v_mfma_f32_16x16x32_bf16 v[100:103], a[204:207], a[48:51], v[100:103]\n" \
"v_and_b32_e32 v233, 0xffff0000, v232\n" \
"v_mfma_f32_16x16x32_bf16 v[100:103], a[220:223], a[52:55], v[100:103]\n" \
"v_lshlrev_b32_e64 v232, 16, v232\n" \
"v_mfma_f32_16x16x32_bf16 v[4:7], a[212:215], a[8:11], v[4:7]\n" \
"v_mul_f32_e64 v234, v202, v234\n" \
"v_mfma_f32_16x16x32_bf16 v[4:7], a[208:211], a[12:15], v[4:7]\n" \
"v_mul_f32_e64 v235, v203, v235\n" \
"v_mfma_f32_16x16x32_bf16 v[36:39], a[212:215], a[24:27], v[36:39]\n" \
"v_mul_f32_e64 v233, v201, v233\n" \
"v_mfma_f32_16x16x32_bf16 v[36:39], a[208:211], a[28:31], v[36:39]\n" \
"v_mul_f32_e64 v232, v200, v232\n" \
"v_mfma_f32_16x16x32_bf16 v[68:71], a[212:215], a[40:43], v[68:71]\n" \
"s_waitcnt vmcnt(4) lgkmcnt(15)\n" \
"s_barrier\n" \
"v_mfma_f32_16x16x32_bf16 v[68:71], a[208:211], a[44:47], v[68:71]\n" \
"ds_read_b64_tr_b16 a[232:233], v215 offset:49152\n" \
"v_add_f32_e64 v223, v234, v235\n" \
"v_mfma_f32_16x16x32_bf16 v[100:103], a[212:215], a[56:59], v[100:103]\n" \
"ds_read_b64_tr_b16 a[234:235], v215 offset:51200\n" \
"v_add_f32_e64 v222, v232, v233\n" \
"v_mfma_f32_16x16x32_bf16 v[100:103], a[208:211], a[60:63], v[100:103]\n" \
"s_waitcnt vmcnt(63) lgkmcnt(3)\n" \
"s_nop 0\n" \
"v_mfma_f32_16x16x32_bf16 v[12:15], a[216:219], a[0:3], v[12:15]\n" \
"ds_read_b64_tr_b16 a[196:197], v215 offset:53248\n" \
"ds_write_b128 v231, v[232:235] offset:4096\n" \
"v_mfma_f32_16x16x32_bf16 v[12:15], a[224:227], a[4:7], v[12:15]\n" \
"ds_read_b64_tr_b16 a[198:199], v215 offset:55296\n" \
"v_add_f32_e64 v222, v222, v223\n" \
"v_mfma_f32_16x16x32_bf16 v[44:47], a[216:219], a[16:19], v[44:47]\n" \
"ds_read_b64_tr_b16 a[200:201], v215 offset:57344\n" \
"v_perm_b32 v196, v197, v196, s54\n" \
"v_mfma_f32_16x16x32_bf16 v[44:47], a[224:227], a[20:23], v[44:47]\n" \
"ds_read_b64_tr_b16 a[202:203], v215 offset:59392\n" \
"v_add_f32_dpp v222, v222, v222 quad_perm:[1,0,3,2] row_mask:0xf bank_mask:0xf\n" \
"v_mfma_f32_16x16x32_bf16 v[76:79], a[216:219], a[32:35], v[76:79]\n" \
"ds_read_b64_tr_b16 a[204:205], v215 offset:61440\n" \
"v_perm_b32 v197, v199, v198, s54\n" \
"v_mfma_f32_16x16x32_bf16 v[76:79], a[224:227], a[36:39], v[76:79]\n" \
"ds_read_b64_tr_b16 a[206:207], v215 offset:63488\n" \
"v_add_f32_dpp v222, v222, v222 quad_perm:[2,3,0,1] row_mask:0xf bank_mask:0xf\n" \
"v_mfma_f32_16x16x32_bf16 v[108:111], a[216:219], a[48:51], v[108:111]\n" \
"ds_read_b64_tr_b16 a[220:221], v227 offset:49152\n" \
"ds_write_b64 v218, v[196:197] offset:8192\n" \
"v_mfma_f32_16x16x32_bf16 v[108:111], a[224:227], a[52:55], v[108:111]\n" \
"ds_read_b64_tr_b16 a[222:223], v227 offset:51200\n" \
"v_add_f32_dpp v222, v222, v222 row_ror:4 row_mask:0xf bank_mask:0xf\n" \
"v_mfma_f32_16x16x32_bf16 v[12:15], a[228:231], a[8:11], v[12:15]\n" \
"ds_read_b64_tr_b16 a[212:213], v227 offset:53248\n" \
"v_perm_b32 v200, v201, v200, s54\n" \
"v_mfma_f32_16x16x32_bf16 v[12:15], a[236:239], a[12:15], v[12:15]\n" \
"ds_read_b64_tr_b16 a[214:215], v227 offset:55296\n" \
"v_add_f32_dpp v222, v222, v222 row_ror:8 row_mask:0xf bank_mask:0xf\n" \
"v_mfma_f32_16x16x32_bf16 v[44:47], a[228:231], a[24:27], v[44:47]\n" \
"v_perm_b32 v201, v203, v202, s54\n" \
"v_mov_b32_e64 v223, v222\n" \
"v_mfma_f32_16x16x32_bf16 v[44:47], a[236:239], a[28:31], v[44:47]\n" \
"ds_write_b64 v218, v[200:201] offset:10240\n" \
"s_nop 0\n" \
"v_permlane16_swap_b32_e32 v222, v223\n" \
"v_mfma_f32_16x16x32_bf16 v[76:79], a[228:231], a[40:43], v[76:79]\n" \
"v_perm_b32 v204, v205, v204, s54\n" \
"v_mfma_f32_16x16x32_bf16 v[76:79], a[236:239], a[44:47], v[76:79]\n" \
"v_add_f32_e64 v222, v222, v223\n" \
"v_mfma_f32_16x16x32_bf16 v[108:111], a[228:231], a[56:59], v[108:111]\n" \
"ds_read_b64_tr_b16 a[208:209], v227 offset:57344\n" \
"v_perm_b32 v205, v207, v206, s54\n" \
"v_mfma_f32_16x16x32_bf16 v[108:111], a[236:239], a[60:63], v[108:111]\n" \
"s_waitcnt vmcnt(63) lgkmcnt(7)\n" \
"s_nop 0\n" \
"v_mfma_f32_16x16x32_bf16 v[4:7], a[232:235], a[128:131], v[4:7]\n" \
"ds_read_b64_tr_b16 a[210:211], v227 offset:59392\n" \
"v_mov_b32_e64 v223, v222\n" \
"v_mfma_f32_16x16x32_bf16 v[4:7], a[196:199], a[132:135], v[4:7]\n" \
"ds_read_b64_tr_b16 a[216:217], v227 offset:61440\n" \
"ds_write_b64 v218, v[204:205] offset:14336\n" \
"v_mfma_f32_16x16x32_bf16 v[36:39], a[232:235], a[144:147], v[36:39]\n" \
"ds_read_b64_tr_b16 a[218:219], v227 offset:63488\n" \
"s_nop 0\n" \
"v_permlane32_swap_b32_e32 v222, v223\n" \
"v_mfma_f32_16x16x32_bf16 v[36:39], a[196:199], a[148:151], v[36:39]\n" \
"v_lshl_add_u32 v219, %[wid], 4, v219\n" \
"v_add_f32_e64 v222, v222, v223\n" \
"v_mfma_f32_16x16x32_bf16 v[68:71], a[232:235], a[160:163], v[68:71]\n" \
"s_cmp_lg_u64 s[56:57], 0\n" \
"s_cselect_b32 s61, 1, 0\n" \
"v_mov_b32_e64 v192, s61\n" \
"v_mfma_f32_16x16x32_bf16 v[68:71], a[196:199], a[164:167], v[68:71]\n" \
"v_mov_b32_e64 v193, v192\n" \
"v_mfma_f32_16x16x32_bf16 v[100:103], a[232:235], a[176:179], v[100:103]\n" \
"ds_read_b64_tr_b16 a[224:225], v215 offset:16896\n" \
"ds_write_b32 v219, v222 offset:12424\n" \
"v_mfma_f32_16x16x32_bf16 v[100:103], a[196:199], a[180:183], v[100:103]\n" \
"ds_read_b64_tr_b16 a[226:227], v215 offset:18944\n" \
"ds_write_b64 v219, v[192:193] offset:12416\n" \
"v_mfma_f32_16x16x32_bf16 v[4:7], a[200:203], a[136:139], v[4:7]\n" \
"ds_read_b64_tr_b16 a[228:229], v215 offset:20992\n" \
"v_mfma_f32_16x16x32_bf16 v[4:7], a[204:207], a[140:143], v[4:7]\n" \
"ds_read_b64_tr_b16 a[230:231], v215 offset:23040\n" \
"v_mfma_f32_16x16x32_bf16 v[36:39], a[200:203], a[152:155], v[36:39]\n" \
"v_mfma_f32_16x16x32_bf16 v[36:39], a[204:207], a[156:159], v[36:39]\n" \
"v_mfma_f32_16x16x32_bf16 v[68:71], a[200:203], a[168:171], v[68:71]\n" \
"v_mfma_f32_16x16x32_bf16 v[68:71], a[204:207], a[172:175], v[68:71]\n" \
"v_mfma_f32_16x16x32_bf16 v[100:103], a[200:203], a[184:187], v[100:103]\n" \
"ds_read_b64_tr_b16 a[236:237], v215 offset:25088\n" \
"v_mfma_f32_16x16x32_bf16 v[100:103], a[204:207], a[188:191], v[100:103]\n" \
"s_waitcnt vmcnt(63) lgkmcnt(3)\n" \
"s_barrier\n" \
"v_mfma_f32_16x16x32_bf16 v[12:15], a[220:223], a[128:131], v[12:15]\n" \
"ds_read_b64_tr_b16 a[238:239], v215 offset:27136\n" \
"ds_read_b128 v[204:207], v210 offset:12416\n" \
"v_mfma_f32_16x16x32_bf16 v[12:15], a[212:215], a[132:135], v[12:15]\n" \
"ds_read_b64_tr_b16 a[232:233], v215 offset:29184\n" \
"v_lshrrev_b32_e64 v200, 3, v224\n" \
"v_mfma_f32_16x16x32_bf16 v[44:47], a[220:223], a[144:147], v[44:47]\n" \
"ds_read_b64_tr_b16 a[234:235], v215 offset:31232\n" \
"ds_read_b128 v[160:163], v208 offset:0\n" \
"v_mfma_f32_16x16x32_bf16 v[44:47], a[212:215], a[148:151], v[44:47]\n" \
"v_bfe_u32 v201, v224, 8, 3\n" \
"v_and_b32_e64 v200, 16, v200\n" \
"v_mfma_f32_16x16x32_bf16 v[76:79], a[220:223], a[160:163], v[76:79]\n" \
"ds_read_b128 v[164:167], v208 offset:2048\n" \
".if . - adaln_body != 57344\n" \
".error \"size model\"\n" \
".endif\n" \
"Lk13_%=:\n" \
"s_cbranch_execz adaln_pg0\n" \
"s_nop 0\n" \
"v_lshlrev_b32_e64 v201, v201, 1\n" \
"v_mfma_f32_16x16x32_bf16 v[76:79], a[212:215], a[164:167], v[76:79]\n" \
"s_movk_i32 s61, 0x3f80\n" \
"s_nop 0\n" \
"v_lshlrev_b32_e64 v200, v200, s61\n" \
"v_readlane_b32 s68, %[thi], 18\n" \
"v_and_b32_e64 v216, 64, v226\n" \
"v_mfma_f32_16x16x32_bf16 v[108:111], a[220:223], a[176:179], v[108:111]\n" \
"ds_read_b64_tr_b16 a[196:197], v227 offset:16896\n" \
"ds_read_b128 v[168:171], v208 offset:128\n" \
"v_mfma_f32_16x16x32_bf16 v[108:111], a[212:215], a[180:183], v[108:111]\n" \
"ds_read_b64_tr_b16 a[198:199], v227 offset:18944\n" \
"v_bfe_i32 v192, v201, 0, 1\n" \
"v_add_u32_e64 v226, v216, v226\n" \
"v_mfma_f32_16x16x32_bf16 v[12:15], a[208:211], a[136:139], v[12:15]\n" \
"ds_read_b64_tr_b16 a[200:201], v227 offset:20992\n" \
"ds_read_b128 v[172:175], v208 offset:2176\n" \
"v_mfma_f32_16x16x32_bf16 v[12:15], a[216:219], a[140:143], v[12:15]\n" \
"ds_read_b64_tr_b16 a[202:203], v227 offset:23040\n" \
"v_and_b32_e32 v192, v200, v192\n" \
"v_lshlrev_b32_e32 v226, 7, v226\n" \
"v_mfma_f32_16x16x32_bf16 v[44:47], a[208:211], a[152:155], v[44:47]\n" \
"ds_read_b128 v[176:179], v209 offset:0\n" \
"v_bfe_i32 v193, v201, 1, 1\n" \
"v_mfma_f32_16x16x32_bf16 v[44:47], a[216:219], a[156:159], v[44:47]\n" \
"v_and_b32_e64 v193, v200, v193\n" \
"v_bfe_i32 v194, v201, 2, 1\n" \
"v_mfma_f32_16x16x32_bf16 v[76:79], a[208:211], a[168:171], v[76:79]\n" \
"ds_read_b128 v[180:183], v209 offset:2048\n" \
"v_and_b32_e64 v194, v200, v194\n" \
"v_mfma_f32_16x16x32_bf16 v[76:79], a[216:219], a[172:175], v[76:79]\n" \
"v_bfe_i32 v195, v201, 3, 1\n" \
"v_and_b32_e64 v195, v200, v195\n" \
"buffer_load_dword v216, v226, %[srdz], s68 offen offset:68 sc1\n" \
"v_mfma_f32_16x16x32_bf16 v[108:111], a[208:211], a[184:187], v[108:111]\n" \
"ds_read_b64_tr_b16 a[204:205], v227 offset:25088\n" \
"ds_read_b128 v[184:187], v209 offset:128\n" \
"v_lshrrev_b32_e64 v217, 3, v224\n" \
"s_add_u32 s51, %[sin], 4096\n" \
"v_mfma_f32_16x16x32_bf16 v[108:111], a[216:219], a[188:191], v[108:111]\n" \
"s_waitcnt vmcnt(63) lgkmcnt(12)\n" \
"s_nop 0\n" \
"v_mfma_f32_16x16x32_bf16 v[20:23], a[224:227], v[0:3], v[20:23]\n" \
"ds_read_b64_tr_b16 a[206:207], v227 offset:27136\n" \
"ds_read_b128 v[188:191], v209 offset:2176\n" \
"v_mfma_f32_16x16x32_bf16 v[20:23], a[228:231], v[8:11], v[20:23]\n" \
"ds_read_b64_tr_b16 a[220:221], v227 offset:29184\n" \
"v_bfe_i32 v196, v201, 4, 1\n" \
"v_mfma_f32_16x16x32_bf16 v[52:55], a[224:227], v[32:35], v[52:55]\n" \
"ds_read_b64_tr_b16 a[222:223], v227 offset:31232\n" \
"v_and_b32_e64 v196, v200, v196\n" \
"v_mfma_f32_16x16x32_bf16 v[52:55], a[228:231], v[40:43], v[52:55]\n" \
"v_bfe_i32 v197, v201, 5, 1\n" \
"v_and_b32_e64 v197, v200, v197\n" \
"v_mfma_f32_16x16x32_bf16 v[84:87], a[224:227], v[64:67], v[84:87]\n" \
"v_cvt_pk_bf16_f32 v4, v4, v5\n" \
"v_bfe_i32 v198, v201, 6, 1\n" \
"v_mfma_f32_16x16x32_bf16 v[84:87], a[228:231], v[72:75], v[84:87]\n" \
"v_cvt_pk_bf16_f32 v5, v6, v7\n" \
"v_and_b32_e64 v198, v200, v198\n" \
"v_mfma_f32_16x16x32_bf16 v[116:119], a[224:227], v[96:99], v[116:119]\n" \
"ds_read_b64_tr_b16 a[212:213], v215 offset:33280\n" \
"v_cvt_pk_bf16_f32 v6, v12, v13\n" \
"v_mfma_f32_16x16x32_bf16 v[116:119], a[228:231], v[104:107], v[116:119]\n" \
"ds_read_b64_tr_b16 a[214:215], v215 offset:35328\n" \
"v_cvt_pk_bf16_f32 v7, v14, v15\n" \
"v_mfma_f32_16x16x32_bf16 v[20:23], a[236:239], v[16:19], v[20:23]\n" \
"ds_read_b64_tr_b16 a[208:209], v215 offset:37376\n" \
"v_bfe_i32 v199, v201, 7, 1\n" \
"v_mfma_f32_16x16x32_bf16 v[20:23], a[232:235], v[24:27], v[20:23]\n" \
"ds_read_b64_tr_b16 a[210:211], v215 offset:39424\n" \
"v_and_b32_e64 v199, v200, v199\n" \
"v_mfma_f32_16x16x32_bf16 v[52:55], a[236:239], v[48:51], v[52:55]\n" \
"s_add_u32 s55, s46, 16384\n" \
"buffer_store_dwordx4 v[4:7], %[vxs], %[srdz], s55 offen offset:0 sc1\n" \
"v_mfma_f32_16x16x32_bf16 v[52:55], a[232:235], v[56:59], v[52:55]\n" \
"v_cvt_pk_bf16_f32 v36, v36, v37\n" \
"v_cvt_pk_bf16_f32 v37, v38, v39\n" \
"v_mfma_f32_16x16x32_bf16 v[84:87], a[236:239], v[80:83], v[84:87]\n" \
"v_cvt_pk_bf16_f32 v38, v44, v45\n" \
"s_waitcnt lgkmcnt(14)\n" \
"s_nop 0\n" \
"v_add_f32_dpp v206, v206, v206 quad_perm:[1,0,3,2] row_mask:0xf bank_mask:0xf\n" \
"v_mfma_f32_16x16x32_bf16 v[84:87], a[232:235], v[88:91], v[84:87]\n" \
"v_cvt_pk_bf16_f32 v39, v46, v47\n" \
"v_mfma_f32_16x16x32_bf16 v[116:119], a[236:239], v[112:115], v[116:119]\n" \
"ds_read_b64_tr_b16 a[216:217], v215 offset:41472\n" \
"v_add_f32_dpp v206, v206, v206 quad_perm:[2,3,0,1] row_mask:0xf bank_mask:0xf\n" \
"v_mfma_f32_16x16x32_bf16 v[116:119], a[232:235], v[120:123], v[116:119]\n" \
"s_waitcnt vmcnt(63) lgkmcnt(5)\n" \
"s_nop 0\n" \
"v_mfma_f32_16x16x32_bf16 v[28:31], a[196:199], v[0:3], v[28:31]\n" \
"ds_read_b64_tr_b16 a[218:219], v215 offset:43520\n" \
"v_cvt_pk_bf16_f32 v68, v68, v69\n" \
"v_mfma_f32_16x16x32_bf16 v[28:31], a[200:203], v[8:11], v[28:31]\n" \
"ds_read_b64_tr_b16 a[224:225], v215 offset:45568\n" \
"s_add_u32 s55, s46, 540672\n" \
"buffer_store_dwordx4 v[36:39], %[vxs], %[srdz], s55 offen offset:0 sc1\n" \
"v_mfma_f32_16x16x32_bf16 v[60:63], a[196:199], v[32:35], v[60:63]\n" \
"ds_read_b64_tr_b16 a[226:227], v215 offset:47616\n" \
"v_cvt_pk_bf16_f32 v69, v70, v71\n" \
"v_mfma_f32_16x16x32_bf16 v[60:63], a[200:203], v[40:43], v[60:63]\n" \
"v_cvt_pk_bf16_f32 v70, v76, v77\n" \
"v_cvt_pk_bf16_f32 v71, v78, v79\n" \
"v_mfma_f32_16x16x32_bf16 v[92:95], a[196:199], v[64:67], v[92:95]\n" \
"s_add_u32 s55, s46, 1064960\n" \
"buffer_store_dwordx4 v[68:71], %[vxs], %[srdz], s55 offen offset:0 sc1\n" \
"v_mfma_f32_16x16x32_bf16 v[92:95], a[200:203], v[72:75], v[92:95]\n" \
"v_mfma_f32_16x16x32_bf16 v[124:127], a[196:199], v[96:99], v[124:127]\n" \
"ds_read_b64_tr_b16 a[228:229], v227 offset:33280\n" \
"v_cvt_pk_bf16_f32 v100, v100, v101\n" \
"v_mfma_f32_16x16x32_bf16 v[124:127], a[200:203], v[104:107], v[124:127]\n" \
"ds_read_b64_tr_b16 a[230:231], v227 offset:35328\n" \
"v_cvt_pk_bf16_f32 v101, v102, v103\n" \
"v_mfma_f32_16x16x32_bf16 v[28:31], a[204:207], v[16:19], v[28:31]\n" \
"ds_read_b64_tr_b16 a[236:237], v227 offset:37376\n" \
"v_cvt_pk_bf16_f32 v102, v108, v109\n" \
"v_mfma_f32_16x16x32_bf16 v[28:31], a[220:223], v[24:27], v[28:31]\n" \
"ds_read_b64_tr_b16 a[238:239], v227 offset:39424\n" \
"v_cvt_pk_bf16_f32 v103, v110, v111\n" \
"v_mfma_f32_16x16x32_bf16 v[60:63], a[204:207], v[48:51], v[60:63]\n" \
"s_add_u32 s55, s46, 1589248\n" \
"buffer_store_dwordx4 v[100:103], %[vxs], %[srdz], s55 offen offset:0 sc1\n" \
"v_mfma_f32_16x16x32_bf16 v[60:63], a[220:223], v[56:59], v[60:63]\n" \
"v_mfma_f32_16x16x32_bf16 v[92:95], a[204:207], v[80:83], v[92:95]\n" \
"v_mov_b32_e64 v230, 1\n" \
"buffer_store_dword v230, v225, %[srdz], s62 offen offset:64 sc1\n" \
"v_mfma_f32_16x16x32_bf16 v[92:95], a[220:223], v[88:91], v[92:95]\n" \
"ds_read_b128 v[4:7], v208 offset:4096\n" \
"v_mfma_f32_16x16x32_bf16 v[124:127], a[204:207], v[112:115], v[124:127]\n" \
"ds_read_b64_tr_b16 a[232:233], v227 offset:41472\n" \
"v_mfma_f32_16x16x32_bf16 v[124:127], a[220:223], v[120:123], v[124:127]\n" \
"s_waitcnt vmcnt(63) lgkmcnt(6)\n" \
"s_nop 0\n" \
"v_mfma_f32_16x16x32_bf16 v[20:23], a[212:215], a[0:3], v[20:23]\n" \
"ds_read_b64_tr_b16 a[234:235], v227 offset:43520\n" \
"ds_read_b128 v[12:15], v208 offset:6144\n" \
"v_mfma_f32_16x16x32_bf16 v[20:23], a[208:211], a[4:7], v[20:23]\n" \
"ds_read_b64_tr_b16 a[196:197], v227 offset:45568\n" \
"v_mfma_f32_16x16x32_bf16 v[52:55], a[212:215], a[16:19], v[52:55]\n" \
"ds_read_b64_tr_b16 a[198:199], v227 offset:47616\n" \
"v_mfma_f32_16x16x32_bf16 v[52:55], a[208:211], a[20:23], v[52:55]\n" \
"ds_read_b128 v[36:39], v208 offset:4224\n" \
"v_mfma_f32_16x16x32_bf16 v[84:87], a[212:215], a[32:35], v[84:87]\n" \
"v_mfma_f32_16x16x32_bf16 v[84:87], a[208:211], a[36:39], v[84:87]\n" \
"v_mfma_f32_16x16x32_bf16 v[116:119], a[212:215], a[48:51], v[116:119]\n" \
"ds_read_b64_tr_b16 a[200:201], v215 offset:49664\n" \
"ds_read_b128 v[44:47], v208 offset:6272\n" \
"v_mfma_f32_16x16x32_bf16 v[116:119], a[208:211], a[52:55], v[116:119]\n" \
"ds_read_b64_tr_b16 a[202:203], v215 offset:51712\n" \
"v_mfma_f32_16x16x32_bf16 v[20:23], a[216:219], a[8:11], v[20:23]\n" \
"ds_read_b64_tr_b16 a[204:205], v215 offset:53760\n" \
"v_mfma_f32_16x16x32_bf16 v[20:23], a[224:227], a[12:15], v[20:23]\n" \
"ds_read_b64_tr_b16 a[206:207], v215 offset:55808\n" \
"ds_read_b128 v[68:71], v209 offset:4096\n" \
"v_mfma_f32_16x16x32_bf16 v[52:55], a[216:219], a[24:27], v[52:55]\n" \
"ds_read_b64_tr_b16 a[220:221], v215 offset:57856\n" \
"v_mfma_f32_16x16x32_bf16 v[52:55], a[224:227], a[28:31], v[52:55]\n" \
"ds_read_b64_tr_b16 a[222:223], v215 offset:59904\n" \
"v_mfma_f32_16x16x32_bf16 v[84:87], a[216:219], a[40:43], v[84:87]\n" \
"ds_read_b64_tr_b16 a[212:213], v215 offset:61952\n" \
"ds_read_b128 v[76:79], v209 offset:6144\n" \
"v_mfma_f32_16x16x32_bf16 v[84:87], a[224:227], a[44:47], v[84:87]\n" \
"ds_read_b64_tr_b16 a[214:215], v215 offset:64000\n" \
"v_mfma_f32_16x16x32_bf16 v[116:119], a[216:219], a[56:59], v[116:119]\n" \
"ds_read_b64_tr_b16 a[208:209], v227 offset:49664\n" \
"v_mfma_f32_16x16x32_bf16 v[116:119], a[224:227], a[60:63], v[116:119]\n" \
"s_waitcnt vmcnt(63) lgkmcnt(13)\n" \
"s_nop 0\n" \
"v_mfma_f32_16x16x32_bf16 v[28:31], a[228:231], a[0:3], v[28:31]\n" \
"ds_read_b64_tr_b16 a[210:211], v227 offset:51712\n" \
"ds_read_b128 v[100:103], v209 offset:4224\n" \
"v_mfma_f32_16x16x32_bf16 v[28:31], a[236:239], a[4:7], v[28:31]\n" \
"ds_read_b64_tr_b16 a[216:217], v227 offset:53760\n" \
"v_mfma_f32_16x16x32_bf16 v[60:63], a[228:231], a[16:19], v[60:63]\n" \
"ds_read_b64_tr_b16 a[218:219], v227 offset:55808\n" \
"ds_read_b128 v[108:111], v209 offset:6272\n" \
"v_mfma_f32_16x16x32_bf16 v[60:63], a[236:239], a[20:23], v[60:63]\n" \
"ds_read_b64_tr_b16 a[224:225], v227 offset:57856\n" \
"v_mfma_f32_16x16x32_bf16 v[92:95], a[228:231], a[32:35], v[92:95]\n" \
"ds_read_b64_tr_b16 a[226:227], v227 offset:59904\n" \
"v_mfma_f32_16x16x32_bf16 v[92:95], a[236:239], a[36:39], v[92:95]\n" \
"v_mfma_f32_16x16x32_bf16 v[124:127], a[228:231], a[48:51], v[124:127]\n" \
"v_mfma_f32_16x16x32_bf16 v[124:127], a[236:239], a[52:55], v[124:127]\n" \
"ds_read_b64_tr_b16 a[228:229], v227 offset:61952\n" \
"v_mfma_f32_16x16x32_bf16 v[28:31], a[232:235], a[8:11], v[28:31]\n" \
"ds_read_b64_tr_b16 a[230:231], v227 offset:64000\n" \
"s_waitcnt vmcnt(5)\n" \
"v_cmp_eq_u32_e32 vcc, 0, v216\n" \
"s_nop 0\n" \
"s_cmp_eq_u64 vcc, 0\n" \
"s_cselect_b32 %[got], 1, 0\n" \
"s_nop 0\n" \
"s_cselect_b32 s54, %[sin], 0x80000000\n" \
"s_cselect_b32 s51, s51, 0x80000000\n" \
"v_mfma_f32_16x16x32_bf16 v[28:31], a[196:199], a[12:15], v[28:31]\n" \
"v_mfma_f32_16x16x32_bf16 v[60:63], a[232:235], a[24:27], v[60:63]\n" \
"buffer_load_dwordx4 a[64:67], v217, %[srdz], s54 offen offset:0 sc1\n" \
"v_mfma_f32_16x16x32_bf16 v[60:63], a[196:199], a[28:31], v[60:63]\n" \
"v_mfma_f32_16x16x32_bf16 v[92:95], a[232:235], a[40:43], v[92:95]\n" \
"buffer_load_dwordx4 a[68:71], v217, %[srdz], s54 offen offset:1024 sc1\n" \
"v_mfma_f32_16x16x32_bf16 v[92:95], a[196:199], a[44:47], v[92:95]\n" \
"v_mfma_f32_16x16x32_bf16 v[124:127], a[232:235], a[56:59], v[124:127]\n" \
"buffer_load_dwordx4 a[72:75], v217, %[srdz], s54 offen offset:2048 sc1\n" \
"v_mfma_f32_16x16x32_bf16 v[124:127], a[196:199], a[60:63], v[124:127]\n" \
"s_waitcnt vmcnt(63) lgkmcnt(0)\n" \
"s_nop 0\n" \
"v_mfma_f32_16x16x32_bf16 v[20:23], a[200:203], a[128:131], v[20:23]\n" \
"buffer_load_dwordx4 a[76:79], v217, %[srdz], s54 offen offset:3072 sc1\n" \
"v_mfma_f32_16x16x32_bf16 v[20:23], a[204:207], a[132:135], v[20:23]\n" \
"v_mfma_f32_16x16x32_bf16 v[20:23], a[220:223], a[136:139], v[20:23]\n" \
"buffer_load_dwordx4 a[80:83], v217, %[srdz], s51 offen offset:0 sc1\n" \
"v_mfma_f32_16x16x32_bf16 v[20:23], a[212:215], a[140:143], v[20:23]\n" \
"v_mfma_f32_16x16x32_bf16 v[28:31], a[208:211], a[128:131], v[28:31]\n" \
"buffer_load_dwordx4 a[84:87], v217, %[srdz], s51 offen offset:1024 sc1\n" \
"v_mfma_f32_16x16x32_bf16 v[28:31], a[216:219], a[132:135], v[28:31]\n" \
"v_mfma_f32_16x16x32_bf16 v[28:31], a[224:227], a[136:139], v[28:31]\n" \
"buffer_load_dwordx4 a[88:91], v217, %[srdz], s51 offen offset:2048 sc1\n" \
"v_mfma_f32_16x16x32_bf16 v[28:31], a[228:231], a[140:143], v[28:31]\n" \
"v_mfma_f32_16x16x32_bf16 v[52:55], a[200:203], a[144:147], v[52:55]\n" \
"buffer_load_dwordx4 a[92:95], v217, %[srdz], s51 offen offset:3072 sc1\n" \
"v_mfma_f32_16x16x32_bf16 v[52:55], a[204:207], a[148:151], v[52:55]\n" \
"v_mfma_f32_16x16x32_bf16 v[52:55], a[220:223], a[152:155], v[52:55]\n" \
"v_mfma_f32_16x16x32_bf16 v[52:55], a[212:215], a[156:159], v[52:55]\n" \
"v_cvt_pk_bf16_f32 v20, v20, v21\n" \
"v_cvt_pk_bf16_f32 v21, v22, v23\n" \
"v_mfma_f32_16x16x32_bf16 v[60:63], a[208:211], a[144:147], v[60:63]\n" \
"buffer_load_dwordx2 v[228:229], v226, %[srdz], s68 offen offset:64 sc1\n" \
"v_mfma_f32_16x16x32_bf16 v[60:63], a[216:219], a[148:151], v[60:63]\n" \
"v_cvt_pk_bf16_f32 v22, v28, v29\n" \
"v_cvt_pk_bf16_f32 v23, v30, v31\n" \
"v_mfma_f32_16x16x32_bf16 v[60:63], a[224:227], a[152:155], v[60:63]\n" \
"s_add_u32 s55, s46, 20480\n" \
"buffer_store_dwordx4 v[20:23], %[vxs], %[srdz], s55 offen offset:0 sc1\n" \
"v_mfma_f32_16x16x32_bf16 v[60:63], a[228:231], a[156:159], v[60:63]\n" \
"v_mfma_f32_16x16x32_bf16 v[84:87], a[200:203], a[160:163], v[84:87]\n" \
"v_mfma_f32_16x16x32_bf16 v[84:87], a[204:207], a[164:167], v[84:87]\n" \
"v_mfma_f32_16x16x32_bf16 v[84:87], a[220:223], a[168:171], v[84:87]\n" \
"v_mfma_f32_16x16x32_bf16 v[84:87], a[212:215], a[172:175], v[84:87]\n" \
"v_cvt_pk_bf16_f32 v52, v52, v53\n" \
"v_cvt_pk_bf16_f32 v53, v54, v55\n" \
"v_mfma_f32_16x16x32_bf16 v[92:95], a[208:211], a[160:163], v[92:95]\n" \
"v_mfma_f32_16x16x32_bf16 v[92:95], a[216:219], a[164:167], v[92:95]\n" \
"v_cvt_pk_bf16_f32 v54, v60, v61\n" \
"v_cvt_pk_bf16_f32 v55, v62, v63\n" \
"v_mfma_f32_16x16x32_bf16 v[92:95], a[224:227], a[168:171], v[92:95]\n" \
"s_add_u32 s55, s46, 544768\n" \
"buffer_store_dwordx4 v[52:55], %[vxs], %[srdz], s55 offen offset:0 sc1\n" \
"v_mfma_f32_16x16x32_bf16 v[92:95], a[228:231], a[172:175], v[92:95]\n" \
"v_mfma_f32_16x16x32_bf16 v[116:119], a[200:203], a[176:179], v[116:119]\n" \
"v_mfma_f32_16x16x32_bf16 v[116:119], a[204:207], a[180:183], v[116:119]\n" \
"v_mfma_f32_16x16x32_bf16 v[116:119], a[220:223], a[184:187], v[116:119]\n" \
"v_mfma_f32_16x16x32_bf16 v[116:119], a[212:215], a[188:191], v[116:119]\n" \
"v_cvt_pk_bf16_f32 v84, v84, v85\n" \
"v_cvt_pk_bf16_f32 v85, v86, v87\n" \
"v_mfma_f32_16x16x32_bf16 v[124:127], a[208:211], a[176:179], v[124:127]\n" \
"v_mfma_f32_16x16x32_bf16 v[124:127], a[216:219], a[180:183], v[124:127]\n" \
"v_cvt_pk_bf16_f32 v86, v92, v93\n" \
"v_cvt_pk_bf16_f32 v87, v94, v95\n" \
"v_mfma_f32_16x16x32_bf16 v[124:127], a[224:227], a[184:187], v[124:127]\n" \
"s_add_u32 s55, s46, 1069056\n" \
"buffer_store_dwordx4 v[84:87], %[vxs], %[srdz], s55 offen offset:0 sc1\n" \
"v_mfma_f32_16x16x32_bf16 v[124:127], a[228:231], a[188:191], v[124:127]\n" \
"v_cvt_pk_bf16_f32 v116, v116, v117\n" \
"v_cvt_pk_bf16_f32 v117, v118, v119\n" \
"s_waitcnt lgkmcnt(0)\n" \
"s_nop 4\n" \
"v_cvt_pk_bf16_f32 v118, v124, v125\n" \
"v_cvt_pk_bf16_f32 v119, v126, v127\n" \
"s_add_u32 s55, s46, 1593344\n" \
"buffer_store_dwordx4 v[116:119], %[vxs], %[srdz], s55 offen offset:0 sc1\n" \
"s_branch Lend_%=\n" \
"s_nop 0\n" \
"Lsp1_%=:\n" \
"s_movk_i32 s51, 0x1000\n" \
"s_cmp_gt_u32 s48, 0\n" \
"s_cselect_b32 s61, 0, 1\n" \
"s_nop 0\n" \
"s_mul_i32 s61, s61, 262144\n" \
"Lsq1_%=:\n" \
"buffer_load_dwordx4 a[64:67], %[oq0], %[srdz], s61 offen offset:0 sc1\n" \
"buffer_load_dwordx4 a[68:71], %[oq0], %[srdz], s61 offen offset:1024 sc1\n" \
"buffer_load_dwordx4 a[72:75], %[oq0], %[srdz], s61 offen offset:2048 sc1\n" \
"buffer_load_dwordx4 a[76:79], %[oq0], %[srdz], s61 offen offset:3072 sc1\n" \
"buffer_load_dwordx4 a[80:83], v208, %[srdz], s61 offen offset:0 sc1\n" \
"buffer_load_dwordx4 a[84:87], v208, %[srdz], s61 offen offset:1024 sc1\n" \
"buffer_load_dwordx4 a[88:91], v208, %[srdz], s61 offen offset:2048 sc1\n" \
"buffer_load_dwordx4 a[92:95], v208, %[srdz], s61 offen offset:3072 sc1\n" \
"buffer_load_dwordx4 a[96:99], v209, %[srdz], s61 offen offset:0 sc1\n" \
"buffer_load_dwordx4 a[100:103], v209, %[srdz], s61 offen offset:1024 sc1\n" \
"buffer_load_dwordx4 a[104:107], v209, %[srdz], s61 offen offset:2048 sc1\n" \
"buffer_load_dwordx4 a[108:111], v209, %[srdz], s61 offen offset:3072 sc1\n" \
"buffer_load_dwordx4 a[112:115], v210, %[srdz], s61 offen offset:0 sc1\n" \
"buffer_load_dwordx4 a[116:119], v210, %[srdz], s61 offen offset:1024 sc1\n" \
"buffer_load_dwordx4 a[120:123], v210, %[srdz], s61 offen offset:2048 sc1\n" \
"buffer_load_dwordx4 a[124:127], v210, %[srdz], s61 offen offset:3072 sc1\n" \
"s_waitcnt vmcnt(0)\n" \
"s_nop 0\n" \
"v_mfma_f32_16x16x32_bf16 v[128:131], a[236:239], a[64:67], v[192:195]\n" \
"v_mfma_f32_16x16x32_bf16 v[128:131], a[196:199], a[68:71], v[128:131]\n" \
"v_mfma_f32_16x16x32_bf16 v[128:131], a[200:203], a[72:75], v[128:131]\n" \
"v_mfma_f32_16x16x32_bf16 v[128:131], a[204:207], a[76:79], v[128:131]\n" \
"v_mfma_f32_16x16x32_bf16 v[132:135], a[236:239], a[80:83], v[196:199]\n" \
"v_mfma_f32_16x16x32_bf16 v[132:135], a[196:199], a[84:87], v[132:135]\n" \
"v_mfma_f32_16x16x32_bf16 v[132:135], a[200:203], a[88:91], v[132:135]\n" \
"v_mfma_f32_16x16x32_bf16 v[132:135], a[204:207], a[92:95], v[132:135]\n" \
"v_mfma_f32_16x16x32_bf16 v[136:139], a[236:239], a[96:99], v[200:203]\n" \
"v_mfma_f32_16x16x32_bf16 v[136:139], a[196:199], a[100:103], v[136:139]\n" \
"v_mfma_f32_16x16x32_bf16 v[136:139], a[200:203], a[104:107], v[136:139]\n" \
"v_mfma_f32_16x16x32_bf16 v[136:139], a[204:207], a[108:111], v[136:139]\n" \
"v_mfma_f32_16x16x32_bf16 v[140:143], a[236:239], a[112:115], v[204:207]\n" \
"v_mfma_f32_16x16x32_bf16 v[140:143], a[196:199], a[116:119], v[140:143]\n" \
"v_mfma_f32_16x16x32_bf16 v[140:143], a[200:203], a[120:123], v[140:143]\n" \
"v_mfma_f32_16x16x32_bf16 v[140:143], a[204:207], a[124:127], v[140:143]\n" \
"s_nop 7\n" \
"s_nop 0\n" \
"v_cmp_u_f32_e64 s[44:45], v128, v132\n" \
"v_cmp_u_f32_e64 s[56:57], v136, v140\n" \
"s_or_b64 s[56:57], s[56:57], s[44:45]\n" \
"s_cbranch_scc0 Lbk1_%=\n" \
"s_sub_u32 s51, s51, 1\n" \
"s_cbranch_scc0 Lsq1_%=\n" \
"s_branch Lbk1_%=\n" \
"Lsp2_%=:\n" \
"s_movk_i32 s51, 0x1000\n" \
"s_cmp_gt_u32 s48, 1\n" \
"s_cselect_b32 s61, 1, 2\n" \
"s_mul_i32 s61, s61, 262144\n" \
"Lsq2_%=:\n" \
"buffer_load_dwordx4 a[0:3], %[oq0], %[srdz], s61 offen offset:0 sc1\n" \
"buffer_load_dwordx4 a[4:7], %[oq0], %[srdz], s61 offen offset:1024 sc1\n" \
"buffer_load_dwordx4 a[8:11], %[oq0], %[srdz], s61 offen offset:2048 sc1\n" \
"buffer_load_dwordx4 a[12:15], %[oq0], %[srdz], s61 offen offset:3072 sc1\n" \
"buffer_load_dwordx4 a[16:19], v208, %[srdz], s61 offen offset:0 sc1\n" \
"buffer_load_dwordx4 a[20:23], v208, %[srdz], s61 offen offset:1024 sc1\n" \
"buffer_load_dwordx4 a[24:27], v208, %[srdz], s61 offen offset:2048 sc1\n" \
"buffer_load_dwordx4 a[28:31], v208, %[srdz], s61 offen offset:3072 sc1\n" \
"buffer_load_dwordx4 a[32:35], v209, %[srdz], s61 offen offset:0 sc1\n" \
"buffer_load_dwordx4 a[36:39], v209, %[srdz], s61 offen offset:1024 sc1\n" \
"buffer_load_dwordx4 a[40:43], v209, %[srdz], s61 offen offset:2048 sc1\n" \
"buffer_load_dwordx4 a[44:47], v209, %[srdz], s61 offen offset:3072 sc1\n" \
"buffer_load_dwordx4 a[48:51], v210, %[srdz], s61 offen offset:0 sc1\n" \
"buffer_load_dwordx4 a[52:55], v210, %[srdz], s61 offen offset:1024 sc1\n" \
"buffer_load_dwordx4 a[56:59], v210, %[srdz], s61 offen offset:2048 sc1\n" \
"buffer_load_dwordx4 a[60:63], v210, %[srdz], s61 offen offset:3072 sc1\n" \
"s_waitcnt vmcnt(0)\n" \
"s_nop 0\n" \
"v_mfma_f32_16x16x32_bf16 v[192:195], a[196:199], a[0:3], v[128:131]\n" \
"v_mfma_f32_16x16x32_bf16 v[192:195], a[200:203], a[4:7], v[192:195]\n" \
"v_mfma_f32_16x16x32_bf16 v[192:195], a[204:207], a[8:11], v[192:195]\n" \
"v_mfma_f32_16x16x32_bf16 v[192:195], a[232:235], a[12:15], v[192:195]\n" \
"v_mfma_f32_16x16x32_bf16 v[196:199], a[196:199], a[16:19], v[132:135]\n" \
"v_mfma_f32_16x16x32_bf16 v[196:199], a[200:203], a[20:23], v[196:199]\n" \
"v_mfma_f32_16x16x32_bf16 v[196:199], a[204:207], a[24:27], v[196:199]\n" \
"v_mfma_f32_16x16x32_bf16 v[196:199], a[232:235], a[28:31], v[196:199]\n" \
"v_mfma_f32_16x16x32_bf16 v[200:203], a[196:199], a[32:35], v[136:139]\n" \
"v_mfma_f32_16x16x32_bf16 v[200:203], a[200:203], a[36:39], v[200:203]\n" \
"v_mfma_f32_16x16x32_bf16 v[200:203], a[204:207], a[40:43], v[200:203]\n" \
"v_mfma_f32_16x16x32_bf16 v[200:203], a[232:235], a[44:47], v[200:203]\n" \
"v_mfma_f32_16x16x32_bf16 v[204:207], a[196:199], a[48:51], v[140:143]\n" \
"v_mfma_f32_16x16x32_bf16 v[204:207], a[200:203], a[52:55], v[204:207]\n" \
"v_mfma_f32_16x16x32_bf16 v[204:207], a[204:207], a[56:59], v[204:207]\n" \
"v_mfma_f32_16x16x32_bf16 v[204:207], a[232:235], a[60:63], v[204:207]\n" \
"s_nop 7\n" \
"s_nop 0\n" \
"v_cmp_u_f32_e64 s[44:45], v192, v196\n" \
"v_cmp_u_f32_e64 s[56:57], v200, v204\n" \
"s_or_b64 s[56:57], s[56:57], s[44:45]\n" \
"s_cbranch_scc0 Lbk2_%=\n" \
"s_sub_u32 s51, s51, 1\n" \
"s_cbranch_scc0 Lsq2_%=\n" \
"s_branch Lbk2_%=\n" \
"Lsp3_%=:\n" \
"s_movk_i32 s51, 0x1000\n" \
"s_cmp_gt_u32 s48, 2\n" \
"s_cselect_b32 s61, 2, 3\n" \
"s_mul_i32 s61, s61, 262144\n" \
"Lsq3_%=:\n" \
"buffer_load_dwordx4 a[128:131], %[oq0], %[srdz], s61 offen offset:0 sc1\n" \
"buffer_load_dwordx4 a[132:135], %[oq0], %[srdz], s61 offen offset:1024 sc1\n" \
"buffer_load_dwordx4 a[136:139], %[oq0], %[srdz], s61 offen offset:2048 sc1\n" \
"buffer_load_dwordx4 a[140:143], %[oq0], %[srdz], s61 offen offset:3072 sc1\n" \
"buffer_load_dwordx4 a[144:147], v208, %[srdz], s61 offen offset:0 sc1\n" \
"buffer_load_dwordx4 a[148:151], v208, %[srdz], s61 offen offset:1024 sc1\n" \
"buffer_load_dwordx4 a[152:155], v208, %[srdz], s61 offen offset:2048 sc1\n" \
"buffer_load_dwordx4 a[156:159], v208, %[srdz], s61 offen offset:3072 sc1\n" \
"buffer_load_dwordx4 a[160:163], v209, %[srdz], s61 offen offset:0 sc1\n" \
"buffer_load_dwordx4 a[164:167], v209, %[srdz], s61 offen offset:1024 sc1\n" \
"buffer_load_dwordx4 a[168:171], v209, %[srdz], s61 offen offset:2048 sc1\n" \
"buffer_load_dwordx4 a[172:175], v209, %[srdz], s61 offen offset:3072 sc1\n" \
"buffer_load_dwordx4 a[176:179], v210, %[srdz], s61 offen offset:0 sc1\n" \
"buffer_load_dwordx4 a[180:183], v210, %[srdz], s61 offen offset:1024 sc1\n" \
"buffer_load_dwordx4 a[184:187], v210, %[srdz], s61 offen offset:2048 sc1\n" \
"buffer_load_dwordx4 a[188:191], v210, %[srdz], s61 offen offset:3072 sc1\n" \
"s_waitcnt vmcnt(0)\n" \
"s_nop 0\n" \
"v_mfma_f32_16x16x32_bf16 v[128:131], a[196:199], a[128:131], v[192:195]\n" \
"v_mfma_f32_16x16x32_bf16 v[128:131], a[200:203], a[132:135], v[128:131]\n" \
"v_mfma_f32_16x16x32_bf16 v[128:131], a[204:207], a[136:139], v[128:131]\n" \
"v_mfma_f32_16x16x32_bf16 v[128:131], a[224:227], a[140:143], v[128:131]\n" \
"v_mfma_f32_16x16x32_bf16 v[132:135], a[196:199], a[144:147], v[196:199]\n" \
"v_mfma_f32_16x16x32_bf16 v[132:135], a[200:203], a[148:151], v[132:135]\n" \
"v_mfma_f32_16x16x32_bf16 v[132:135], a[204:207], a[152:155], v[132:135]\n" \
"v_mfma_f32_16x16x32_bf16 v[132:135], a[224:227], a[156:159], v[132:135]\n" \
"v_mfma_f32_16x16x32_bf16 v[136:139], a[196:199], a[160:163], v[200:203]\n" \
"v_mfma_f32_16x16x32_bf16 v[136:139], a[200:203], a[164:167], v[136:139]\n" \
"v_mfma_f32_16x16x32_bf16 v[136:139], a[204:207], a[168:171], v[136:139]\n" \
"v_mfma_f32_16x16x32_bf16 v[136:139], a[224:227], a[172:175], v[136:139]\n" \
"v_mfma_f32_16x16x32_bf16 v[140:143], a[196:199], a[176:179], v[204:207]\n" \
"v_mfma_f32_16x16x32_bf16 v[140:143], a[200:203], a[180:183], v[140:143]\n" \
"v_mfma_f32_16x16x32_bf16 v[140:143], a[204:207], a[184:187], v[140:143]\n" \
"v_mfma_f32_16x16x32_bf16 v[140:143], a[224:227], a[188:191], v[140:143]\n" \
"s_nop 7\n" \
"s_nop 0\n" \
"v_cmp_u_f32_e64 s[44:45], v128, v132\n" \
"v_cmp_u_f32_e64 s[56:57], v136, v140\n" \
"s_or_b64 s[56:57], s[56:57], s[44:45]\n" \
"s_cbranch_scc0 Lbk3_%=\n" \
"s_sub_u32 s51, s51, 1\n" \
"s_cbranch_scc0 Lsq3_%=\n" \
"s_branch Lbk3_%=\n" \
"Lsp4_%=:\n" \
"s_movk_i32 s51, 0x1000\n" \
"s_cmp_gt_u32 s48, 3\n" \
"s_cselect_b32 s61, 3, 4\n" \
"s_mul_i32 s61, s61, 262144\n" \
"Lsq4_%=:\n" \
"buffer_load_dwordx4 a[64:67], %[oq0], %[srdz], s61 offen offset:0 sc1\n" \
"buffer_load_dwordx4 a[68:71], %[oq0], %[srdz], s61 offen offset:1024 sc1\n" \
"buffer_load_dwordx4 a[72:75], %[oq0], %[srdz], s61 offen offset:2048 sc1\n" \
"buffer_load_dwordx4 a[76:79], %[oq0], %[srdz], s61 offen offset:3072 sc1\n" \
"buffer_load_dwordx4 a[80:83], v208, %[srdz], s61 offen offset:0 sc1\n" \
"buffer_load_dwordx4 a[84:87], v208, %[srdz], s61 offen offset:1024 sc1\n" \
"buffer_load_dwordx4 a[88:91], v208, %[srdz], s61 offen offset:2048 sc1\n" \
"buffer_load_dwordx4 a[92:95], v208, %[srdz], s61 offen offset:3072 sc1\n" \
"buffer_load_dwordx4 a[96:99], v209, %[srdz], s61 offen offset:0 sc1\n" \
"buffer_load_dwordx4 a[100:103], v209, %[srdz], s61 offen offset:1024 sc1\n" \
"buffer_load_dwordx4 a[104:107], v209, %[srdz], s61 offen offset:2048 sc1\n" \
"buffer_load_dwordx4 a[108:111], v209, %[srdz], s61 offen offset:3072 sc1\n" \
"buffer_load_dwordx4 a[112:115], v210, %[srdz], s61 offen offset:0 sc1\n" \
"buffer_load_dwordx4 a[116:119], v210, %[srdz], s61 offen offset:1024 sc1\n" \
"buffer_load_dwordx4 a[120:123], v210, %[srdz], s61 offen offset:2048 sc1\n" \
"buffer_load_dwordx4 a[124:127], v210, %[srdz], s61 offen offset:3072 sc1\n" \
"s_waitcnt vmcnt(0)\n" \
"s_nop 0\n" \
"v_mfma_f32_16x16x32_bf16 v[192:195], a[196:199], a[64:67], v[128:131]\n" \
"v_mfma_f32_16x16x32_bf16 v[192:195], a[200:203], a[68:71], v[192:195]\n" \
"v_mfma_f32_16x16x32_bf16 v[192:195], a[204:207], a[72:75], v[192:195]\n" \
"v_mfma_f32_16x16x32_bf16 v[192:195], a[220:223], a[76:79], v[192:195]\n" \
"v_mfma_f32_16x16x32_bf16 v[196:199], a[196:199], a[80:83], v[132:135]\n" \
"v_mfma_f32_16x16x32_bf16 v[196:199], a[200:203], a[84:87], v[196:199]\n" \
"v_mfma_f32_16x16x32_bf16 v[196:199], a[204:207], a[88:91], v[196:199]\n" \
"v_mfma_f32_16x16x32_bf16 v[196:199], a[220:223], a[92:95], v[196:199]\n" \
"v_mfma_f32_16x16x32_bf16 v[200:203], a[196:199], a[96:99], v[136:139]\n" \
"v_mfma_f32_16x16x32_bf16 v[200:203], a[200:203], a[100:103], v[200:203]\n" \
"v_mfma_f32_16x16x32_bf16 v[200:203], a[204:207], a[104:107], v[200:203]\n" \
"v_mfma_f32_16x16x32_bf16 v[200:203], a[220:223], a[108:111], v[200:203]\n" \
"v_mfma_f32_16x16x32_bf16 v[204:207], a[196:199], a[112:115], v[140:143]\n" \
"v_mfma_f32_16x16x32_bf16 v[204:207], a[200:203], a[116:119], v[204:207]\n" \
"v_mfma_f32_16x16x32_bf16 v[204:207], a[204:207], a[120:123], v[204:207]\n" \
"v_mfma_f32_16x16x32_bf16 v[204:207], a[220:223], a[124:127], v[204:207]\n" \
"s_nop 7\n" \
"s_nop 0\n" \
"v_cmp_u_f32_e64 s[44:45], v192, v196\n" \
"v_cmp_u_f32_e64 s[56:57], v200, v204\n" \
"s_or_b64 s[56:57], s[56:57], s[44:45]\n" \
"s_cbranch_scc0 Lbk4_%=\n" \
"s_sub_u32 s51, s51, 1\n" \
"s_cbranch_scc0 Lsq4_%=\n" \
"s_branch Lbk4_%=\n" \
"Lsp5_%=:\n" \
"s_movk_i32 s51, 0x1000\n" \
"s_cmp_gt_u32 s48, 4\n" \
"s_cselect_b32 s61, 4, 5\n" \
"s_mul_i32 s61, s61, 262144\n" \
"Lsq5_%=:\n" \
"buffer_load_dwordx4 v[0:3], %[oq0], %[srdz], s61 offen offset:0 sc1\n" \
"buffer_load_dwordx4 v[8:11], %[oq0], %[srdz], s61 offen offset:1024 sc1\n" \
"buffer_load_dwordx4 v[16:19], %[oq0], %[srdz], s61 offen offset:2048 sc1\n" \
"buffer_load_dwordx4 v[24:27], %[oq0], %[srdz], s61 offen offset:3072 sc1\n" \
"buffer_load_dwordx4 v[32:35], v208, %[srdz], s61 offen offset:0 sc1\n" \
"buffer_load_dwordx4 v[40:43], v208, %[srdz], s61 offen offset:1024 sc1\n" \
"buffer_load_dwordx4 v[48:51], v208, %[srdz], s61 offen offset:2048 sc1\n" \
"buffer_load_dwordx4 v[56:59], v208, %[srdz], s61 offen offset:3072 sc1\n" \
"buffer_load_dwordx4 v[64:67], v209, %[srdz], s61 offen offset:0 sc1\n" \
"buffer_load_dwordx4 v[72:75], v209, %[srdz], s61 offen offset:1024 sc1\n" \
"buffer_load_dwordx4 v[80:83], v209, %[srdz], s61 offen offset:2048 sc1\n" \
"buffer_load_dwordx4 v[88:91], v209, %[srdz], s61 offen offset:3072 sc1\n" \
"buffer_load_dwordx4 v[96:99], v210, %[srdz], s61 offen offset:0 sc1\n" \
"buffer_load_dwordx4 v[104:107], v210, %[srdz], s61 offen offset:1024 sc1\n" \
"buffer_load_dwordx4 v[112:115], v210, %[srdz], s61 offen offset:2048 sc1\n" \
"buffer_load_dwordx4 v[120:123], v210, %[srdz], s61 offen offset:3072 sc1\n" \
"s_waitcnt vmcnt(0)\n" \
"s_nop 0\n" \
"v_mfma_f32_16x16x32_bf16 v[128:131], a[232:235], v[0:3], v[192:195]\n" \
"v_mfma_f32_16x16x32_bf16 v[128:131], a[236:239], v[8:11], v[128:131]\n" \
"v_mfma_f32_16x16x32_bf16 v[128:131], a[208:211], v[16:19], v[128:131]\n" \
"v_mfma_f32_16x16x32_bf16 v[128:131], a[228:231], v[24:27], v[128:131]\n" \
"v_mfma_f32_16x16x32_bf16 v[132:135], a[232:235], v[32:35], v[196:199]\n" \
"v_mfma_f32_16x16x32_bf16 v[132:135], a[236:239], v[40:43], v[132:135]\n" \
"v_mfma_f32_16x16x32_bf16 v[132:135], a[208:211], v[48:51], v[132:135]\n" \
"v_mfma_f32_16x16x32_bf16 v[132:135], a[228:231], v[56:59], v[132:135]\n" \
"v_mfma_f32_16x16x32_bf16 v[136:139], a[232:235], v[64:67], v[200:203]\n" \
"v_mfma_f32_16x16x32_bf16 v[136:139], a[236:239], v[72:75], v[136:139]\n" \
"v_mfma_f32_16x16x32_bf16 v[136:139], a[208:211], v[80:83], v[136:139]\n" \
"v_mfma_f32_16x16x32_bf16 v[136:139], a[228:231], v[88:91], v[136:139]\n" \
"v_mfma_f32_16x16x32_bf16 v[140:143], a[232:235], v[96:99], v[204:207]\n" \
"v_mfma_f32_16x16x32_bf16 v[140:143], a[236:239], v[104:107], v[140:143]\n" \
"v_mfma_f32_16x16x32_bf16 v[140:143], a[208:211], v[112:115], v[140:143]\n" \
"v_mfma_f32_16x16x32_bf16 v[140:143], a[228:231], v[120:123], v[140:143]\n" \
"s_nop 7\n" \
"s_nop 0\n" \
"v_cmp_u_f32_e64 s[44:45], v128, v132\n" \
"v_cmp_u_f32_e64 s[56:57], v136, v140\n" \
"s_or_b64 s[56:57], s[56:57], s[44:45]\n" \
"s_cbranch_scc0 Lbk5_%=\n" \
"s_sub_u32 s51, s51, 1\n" \
"s_cbranch_scc0 Lsq5_%=\n" \
"s_branch Lbk5_%=\n" \
"Lsp6_%=:\n" \
"s_movk_i32 s51, 0x1000\n" \
"s_cmp_gt_u32 s48, 5\n" \
"s_cselect_b32 s61, 5, 6\n" \
"s_mul_i32 s61, s61, 262144\n" \
"Lsq6_%=:\n" \
"buffer_load_dwordx4 a[0:3], %[oq0], %[srdz], s61 offen offset:0 sc1\n" \
"buffer_load_dwordx4 a[4:7], %[oq0], %[srdz], s61 offen offset:1024 sc1\n" \
"buffer_load_dwordx4 a[8:11], %[oq0], %[srdz], s61 offen offset:2048 sc1\n" \
"buffer_load_dwordx4 a[12:15], %[oq0], %[srdz], s61 offen offset:3072 sc1\n" \
"buffer_load_dwordx4 a[16:19], v208, %[srdz], s61 offen offset:0 sc1\n" \
"buffer_load_dwordx4 a[20:23], v208, %[srdz], s61 offen offset:1024 sc1\n" \
"buffer_load_dwordx4 a[24:27], v208, %[srdz], s61 offen offset:2048 sc1\n" \
"buffer_load_dwordx4 a[28:31], v208, %[srdz], s61 offen offset:3072 sc1\n" \
"buffer_load_dwordx4 a[32:35], v209, %[srdz], s61 offen offset:0 sc1\n" \
"buffer_load_dwordx4 a[36:39], v209, %[srdz], s61 offen offset:1024 sc1\n" \
"buffer_load_dwordx4 a[40:43], v209, %[srdz], s61 offen offset:2048 sc1\n" \
"buffer_load_dwordx4 a[44:47], v209, %[srdz], s61 offen offset:3072 sc1\n" \
"buffer_load_dwordx4 a[48:51], v210, %[srdz], s61 offen offset:0 sc1\n" \
"buffer_load_dwordx4 a[52:55], v210, %[srdz], s61 offen offset:1024 sc1\n" \
"buffer_load_dwordx4 a[56:59], v210, %[srdz], s61 offen offset:2048 sc1\n" \
"buffer_load_dwordx4 a[60:63], v210, %[srdz], s61 offen offset:3072 sc1\n" \
"s_waitcnt vmcnt(0)\n" \
"s_nop 0\n" \
"v_mfma_f32_16x16x32_bf16 v[192:195], a[196:199], a[0:3], v[128:131]\n" \
"v_mfma_f32_16x16x32_bf16 v[192:195], a[200:203], a[4:7], v[192:195]\n" \
"v_mfma_f32_16x16x32_bf16 v[192:195], a[204:207], a[8:11], v[192:195]\n" \
"v_mfma_f32_16x16x32_bf16 v[192:195], a[220:223], a[12:15], v[192:195]\n" \
"v_mfma_f32_16x16x32_bf16 v[196:199], a[196:199], a[16:19], v[132:135]\n" \
"v_mfma_f32_16x16x32_bf16 v[196:199], a[200:203], a[20:23], v[196:199]\n" \
"v_mfma_f32_16x16x32_bf16 v[196:199], a[204:207], a[24:27], v[196:199]\n" \
"v_mfma_f32_16x16x32_bf16 v[196:199], a[220:223], a[28:31], v[196:199]\n" \
"v_mfma_f32_16x16x32_bf16 v[200:203], a[196:199], a[32:35], v[136:139]\n" \
"v_mfma_f32_16x16x32_bf16 v[200:203], a[200:203], a[36:39], v[200:203]\n" \
"v_mfma_f32_16x16x32_bf16 v[200:203], a[204:207], a[40:43], v[200:203]\n" \
"v_mfma_f32_16x16x32_bf16 v[200:203], a[220:223], a[44:47], v[200:203]\n" \
"v_mfma_f32_16x16x32_bf16 v[204:207], a[196:199], a[48:51], v[140:143]\n" \
"v_mfma_f32_16x16x32_bf16 v[204:207], a[200:203], a[52:55], v[204:207]\n" \
"v_mfma_f32_16x16x32_bf16 v[204:207], a[204:207], a[56:59], v[204:207]\n" \
"v_mfma_f32_16x16x32_bf16 v[204:207], a[220:223], a[60:63], v[204:207]\n" \
"s_nop 7\n" \
"s_nop 0\n" \
"v_cmp_u_f32_e64 s[44:45], v192, v196\n" \
"v_cmp_u_f32_e64 s[56:57], v200, v204\n" \
"s_or_b64 s[56:57], s[56:57], s[44:45]\n" \
"s_cbranch_scc0 Lbk6_%=\n" \
"s_sub_u32 s51, s51, 1\n" \
"s_cbranch_scc0 Lsq6_%=\n" \
"s_branch Lbk6_%=\n" \
"Lsp7_%=:\n" \
"s_movk_i32 s51, 0x1000\n" \
"s_cmp_gt_u32 s48, 6\n" \
"s_cselect_b32 s61, 6, 7\n" \
"s_mul_i32 s61, s61, 262144\n" \
"Lsq7_%=:\n" \
"buffer_load_dwordx4 a[128:131], %[oq0], %[srdz], s61 offen offset:0 sc1\n" \
"buffer_load_dwordx4 a[132:135], %[oq0], %[srdz], s61 offen offset:1024 sc1\n" \
"buffer_load_dwordx4 a[136:139], %[oq0], %[srdz], s61 offen offset:2048 sc1\n" \
"buffer_load_dwordx4 a[140:143], %[oq0], %[srdz], s61 offen offset:3072 sc1\n" \
"buffer_load_dwordx4 a[144:147], v208, %[srdz], s61 offen offset:0 sc1\n" \
"buffer_load_dwordx4 a[148:151], v208, %[srdz], s61 offen offset:1024 sc1\n" \
"buffer_load_dwordx4 a[152:155], v208, %[srdz], s61 offen offset:2048 sc1\n" \
"buffer_load_dwordx4 a[156:159], v208, %[srdz], s61 offen offset:3072 sc1\n" \
"buffer_load_dwordx4 a[160:163], v209, %[srdz], s61 offen offset:0 sc1\n" \
"buffer_load_dwordx4 a[164:167], v209, %[srdz], s61 offen offset:1024 sc1\n" \
"buffer_load_dwordx4 a[168:171], v209, %[srdz], s61 offen offset:2048 sc1\n" \
"buffer_load_dwordx4 a[172:175], v209, %[srdz], s61 offen offset:3072 sc1\n" \
"buffer_load_dwordx4 a[176:179], v210, %[srdz], s61 offen offset:0 sc1\n" \
"buffer_load_dwordx4 a[180:183], v210, %[srdz], s61 offen offset:1024 sc1\n" \
"buffer_load_dwordx4 a[184:187], v210, %[srdz], s61 offen offset:2048 sc1\n" \
"buffer_load_dwordx4 a[188:191], v210, %[srdz], s61 offen offset:3072 sc1\n" \
"s_waitcnt vmcnt(0)\n" \
"s_nop 0\n" \
"v_mfma_f32_16x16x32_bf16 v[128:131], a[208:211], a[128:131], v[192:195]\n" \
"v_mfma_f32_16x16x32_bf16 v[128:131], a[224:227], a[132:135], v[128:131]\n" \
"v_mfma_f32_16x16x32_bf16 v[128:131], a[228:231], a[136:139], v[128:131]\n" \
"v_mfma_f32_16x16x32_bf16 v[128:131], a[236:239], a[140:143], v[128:131]\n" \
"v_mfma_f32_16x16x32_bf16 v[132:135], a[208:211], a[144:147], v[196:199]\n" \
"v_mfma_f32_16x16x32_bf16 v[132:135], a[224:227], a[148:151], v[132:135]\n" \
"v_mfma_f32_16x16x32_bf16 v[132:135], a[228:231], a[152:155], v[132:135]\n" \
"v_mfma_f32_16x16x32_bf16 v[132:135], a[236:239], a[156:159], v[132:135]\n" \
"v_mfma_f32_16x16x32_bf16 v[136:139], a[208:211], a[160:163], v[200:203]\n" \
"v_mfma_f32_16x16x32_bf16 v[136:139], a[224:227], a[164:167], v[136:139]\n" \
"v_mfma_f32_16x16x32_bf16 v[136:139], a[228:231], a[168:171], v[136:139]\n" \
"v_mfma_f32_16x16x32_bf16 v[136:139], a[236:239], a[172:175], v[136:139]\n" \
"v_mfma_f32_16x16x32_bf16 v[140:143], a[208:211], a[176:179], v[204:207]\n" \
"v_mfma_f32_16x16x32_bf16 v[140:143], a[224:227], a[180:183], v[140:143]\n" \
"v_mfma_f32_16x16x32_bf16 v[140:143], a[228:231], a[184:187], v[140:143]\n" \
"v_mfma_f32_16x16x32_bf16 v[140:143], a[236:239], a[188:191], v[140:143]\n" \
"s_nop 7\n" \
"s_nop 0\n" \
"v_cmp_u_f32_e64 s[44:45], v128, v132\n" \
"v_cmp_u_f32_e64 s[56:57], v136, v140\n" \
"s_or_b64 s[56:57], s[56:57], s[44:45]\n" \
"s_cbranch_scc0 Lbk7_%=\n" \
"s_sub_u32 s51, s51, 1\n" \
"s_cbranch_scc0 Lsq7_%=\n" \
"s_branch Lbk7_%=\n" \
"Lspk_%=:\n" \
"s_nop 0\n" \
"s_mov_b32 s51, 0x10000\n" \
"Lsqk_%=:\n" \
"buffer_load_dword v229, v226, %[srdz], s50 offen sc1\n" \
"s_waitcnt vmcnt(0)\n" \
"s_nop 0\n" \
"v_cmp_eq_u32_e64 s[56:57], 0, v229\n" \
"s_cmp_lg_u64 s[56:57], 0\n" \
"s_cbranch_scc0 Lbkk_%=\n" \
"s_sub_u32 s51, s51, 1\n" \
"s_cbranch_scc0 Lsqk_%=\n" \
"s_branch Lbkk_%=\n" \
"Lspp_%=:\n" \
"s_nop 0\n" \
"s_mov_b32 s51, 0x10000\n" \
"Lsqp_%=:\n" \
"buffer_load_dword v232, v226, %[srdz], s54 offen sc1\n" \
"s_waitcnt vmcnt(0)\n" \
"v_cmp_eq_u32_e32 vcc, 0, v232\n" \
"s_cbranch_vccz Lbkp_%=\n" \
"s_sub_u32 s51, s51, 1\n" \
"s_cbranch_scc0 Lsqp_%=\n" \
"s_branch Lbkp_%=\n" \
"Lprobe_%=:\n" \
"s_mov_b32 s62, -1\n" \
"s_mov_b32 s63, -1\n" \
"s_memtime s[56:57]\n" \
"s_waitcnt lgkmcnt(0)\n" \
"s_nop 0\n" \
"s_load_dword s61, %[pcnt], 0x29900\n" \
"s_waitcnt lgkmcnt(0)\n" \
"s_nop 0\n" \
"s_memtime s[58:59]\n" \
"s_waitcnt lgkmcnt(0)\n" \
"s_sub_u32 s58, s58, s56\n" \
"s_min_u32 s62, s62, s58\n" \
"s_nop 0\n" \
"s_memtime s[56:57]\n" \
"s_waitcnt lgkmcnt(0)\n" \
"s_nop 0\n" \
"s_load_dword s61, %[pcnt], 0x2b900\n" \
"s_waitcnt lgkmcnt(0)\n" \
"s_nop 0\n" \
"s_memtime s[58:59]\n" \
"s_waitcnt lgkmcnt(0)\n" \
"s_sub_u32 s58, s58, s56\n" \
"s_min_u32 s63, s63, s58\n" \
"s_nop 0\n" \
"s_memtime s[56:57]\n" \
"s_waitcnt lgkmcnt(0)\n" \
"s_nop 0\n" \
"s_load_dword s61, %[pcnt], 0x2dd80\n" \
"s_waitcnt lgkmcnt(0)\n" \
"s_nop 0\n" \
"s_memtime s[58:59]\n" \
"s_waitcnt lgkmcnt(0)\n" \
"s_sub_u32 s58, s58, s56\n" \
"s_min_u32 s62, s62, s58\n" \
"s_nop 0\n" \
"s_memtime s[56:57]\n" \
"s_waitcnt lgkmcnt(0)\n" \
"s_nop 0\n" \
"s_load_dword s61, %[pcnt], 0x2fd80\n" \
"s_waitcnt lgkmcnt(0)\n" \
"s_nop 0\n" \
"s_memtime s[58:59]\n" \
"s_waitcnt lgkmcnt(0)\n" \
"s_sub_u32 s58, s58, s56\n" \
"s_min_u32 s63, s63, s58\n" \
"s_nop 0\n" \
"s_memtime s[56:57]\n" \
"s_waitcnt lgkmcnt(0)\n" \
"s_nop 0\n" \
"s_load_dword s61, %[pcnt], 0x2a200\n" \
"s_waitcnt lgkmcnt(0)\n" \
"s_nop 0\n" \
"s_memtime s[58:59]\n" \
"s_waitcnt lgkmcnt(0)\n" \
"s_sub_u32 s58, s58, s56\n" \
"s_min_u32 s62, s62, s58\n" \
"s_nop 0\n" \
"s_memtime s[56:57]\n" \
"s_waitcnt lgkmcnt(0)\n" \
"s_nop 0\n" \
"s_load_dword s61, %[pcnt], 0x2c200\n" \
"s_waitcnt lgkmcnt(0)\n" \
"s_nop 0\n" \
"s_memtime s[58:59]\n" \
"s_waitcnt lgkmcnt(0)\n" \
"s_sub_u32 s58, s58, s56\n" \
"s_min_u32 s63, s63, s58\n" \
"s_cmp_lt_u32 s62, s63\n" \
"s_cselect_b32 s61, 8, 0\n" \
"s_mov_b64 exec, 1\n" \
"buffer_store_dwordx2 v[234:235], off, %[srdz], s61 offset:128 sc1\n" \
"s_mov_b64 exec, -1\n" \
"s_branch Lgo_%=\n" \
"Lnsp_%=:\n" \
"s_mov_b32 s61, 0x10000\n" \
"Lnsq_%=:\n" \
"buffer_load_dwordx4 v[230:233], off, %[srdz], 0 offset:128 sc1\n" \
"s_waitcnt vmcnt(0)\n" \
"v_cmp_eq_u64_e32 vcc, v[230:231], v[234:235]\n" \
"v_cmp_eq_u64_e64 s[56:57], v[232:233], v[234:235]\n" \
"s_nop 3\n" \
"s_or_b64 s[58:59], vcc, s[56:57]\n" \
"s_cbranch_scc1 Lnbk_%=\n" \
"s_sub_u32 s61, s61, 1\n" \
"s_cbranch_scc0 Lnsq_%=\n" \
"s_branch Lnbk_%=\n" \
"Lwalk_%=:\n" \
"s_mov_b64 exec, 0\n" \
"s_cmp_eq_u32 %[wid], 1\n" \
"s_cbranch_scc1 Lk0_%=\n" \
"s_cmp_eq_u32 %[wid], 2\n" \
"s_cbranch_scc1 Lk6_%=\n" \
"s_branch Lk12_%=\n" \
"adaln_kr:\n" \
"s_mov_b64 exec, -1\n" \
"s_branch Lgo_%=\n" \
"Lslow_%=:\n" \
"s_mov_b32 s52, 1\n" \
"s_mov_b32 s48, 0\n" \
"s_mov_b32 s51, 32\n" \
"s_nop 0\n" \
"s_mov_b32 s62, 0x80000000\n" \
"s_mov_b32 s63, 0x80000000\n" \
"s_waitcnt vmcnt(0)\n" \
"s_nop 0\n" \
"v_readlane_b32 s64, %[tlo], 9\n" \
"v_readlane_b32 s65, %[thi], 9\n" \
"s_mov_b32 s66, %[wbytes]\n" \
"v_mov_b32_e32 v229, %[vg0]\n" \
"s_mov_b32 s44, %[m0t]\n" \
"s_mov_b32 s45, 0\n" \
"v_xor_b32_e64 v208, 64, %[kb0]\n" \
"v_xor_b32_e32 v209, 0x80, %[kb0]\n" \
"v_xor_b32_e32 v210, 0xc0, %[kb0]\n" \
"v_xor_b32_e64 v212, 64, %[kd0]\n" \
"v_xor_b32_e32 v213, 0x80, %[kd0]\n" \
"v_xor_b32_e32 v214, 0xc0, %[kd0]\n" \
"v_xor_b32_e64 v211, 32, %[vvb]\n" \
"v_add_u32_e32 v215, 0xfffffc80, %[vd0]\n" \
"s_mov_b32 s66, 0\n" \
"s_add_u32 s40, %[m0b], 0\n" \
"s_add_u32 s41, %[m0b], 16384\n" \
"s_add_u32 s42, %[m0b], 32768\n" \
"s_add_u32 s43, %[m0b], 49152\n" \
"v_mov_b32_e32 v225, 0xff800000\n" \
"v_mov_b32_e32 v226, 0xff800000\n" \
"v_mov_b32_e32 v227, 0xff800000\n" \
"v_mov_b32_e32 v228, 0xff800000\n" \
"s_waitcnt vmcnt(0) lgkmcnt(0)\n" \
"s_barrier\n" \
"Lsl_%=:\n" \
"s_mov_b32 m0, s40\n" \
"s_nop 0\n" \
"buffer_load_dwordx4 %[kd0], %[srdk], s48 offen offset:0 lds\n" \
"buffer_load_dwordx4 v212, %[srdk], s48 offen offset:1024 lds\n" \
"buffer_load_dwordx4 v213, %[srdk], s48 offen offset:2048 lds\n" \
"buffer_load_dwordx4 v214, %[srdk], s48 offen offset:3072 lds\n" \
"s_waitcnt vmcnt(0)\n" \
"s_barrier\n" \
"ds_read_b128 a[64:67], %[kb0] offset:0\n" \
"ds_read_b128 a[68:71], v208 offset:0\n" \
"ds_read_b128 a[72:75], v209 offset:0\n" \
"ds_read_b128 a[76:79], v210 offset:0\n" \
"ds_read_b128 a[92:95], v210 offset:4096\n" \
"ds_read_b128 a[88:91], v209 offset:4096\n" \
"ds_read_b128 a[84:87], v208 offset:4096\n" \
"ds_read_b128 a[80:83], %[kb0] offset:4096\n" \
"ds_read_b128 a[96:99], %[kb0] offset:8192\n" \
"ds_read_b128 a[100:103], v208 offset:8192\n" \
"ds_read_b128 a[104:107], v209 offset:8192\n" \
"ds_read_b128 a[108:111], v210 offset:8192\n" \
"ds_read_b128 a[124:127], v210 offset:12288\n" \
"ds_read_b128 a[120:123], v209 offset:12288\n" \
"ds_read_b128 a[116:119], v208 offset:12288\n" \
"ds_read_b128 a[112:115], %[kb0] offset:12288\n" \
"s_waitcnt lgkmcnt(0)\n" \
"s_barrier\n" \
"v_mfma_f32_16x16x32_bf16 v[128:131], a[64:67], a[0:3], 0\n" \
"v_mfma_f32_16x16x32_bf16 v[128:131], a[68:71], a[4:7], v[128:131]\n" \
"v_mfma_f32_16x16x32_bf16 v[128:131], a[72:75], a[8:11], v[128:131]\n" \
"v_mfma_f32_16x16x32_bf16 v[128:131], a[76:79], a[12:15], v[128:131]\n" \
"v_mfma_f32_16x16x32_bf16 v[132:135], a[92:95], a[12:15], 0\n" \
"v_mfma_f32_16x16x32_bf16 v[132:135], a[88:91], a[8:11], v[132:135]\n" \
"v_mfma_f32_16x16x32_bf16 v[132:135], a[84:87], a[4:7], v[132:135]\n" \
"v_mfma_f32_16x16x32_bf16 v[132:135], a[80:83], a[0:3], v[132:135]\n" \
"v_mfma_f32_16x16x32_bf16 v[136:139], a[64:67], a[16:19], 0\n" \
"v_mfma_f32_16x16x32_bf16 v[136:139], a[68:71], a[20:23], v[136:139]\n" \
"v_mfma_f32_16x16x32_bf16 v[136:139], a[72:75], a[24:27], v[136:139]\n" \
"v_mfma_f32_16x16x32_bf16 v[136:139], a[76:79], a[28:31], v[136:139]\n" \
"v_mfma_f32_16x16x32_bf16 v[140:143], a[92:95], a[28:31], 0\n" \
"v_mfma_f32_16x16x32_bf16 v[140:143], a[88:91], a[24:27], v[140:143]\n" \
"v_mfma_f32_16x16x32_bf16 v[140:143], a[84:87], a[20:23], v[140:143]\n" \
"v_mfma_f32_16x16x32_bf16 v[140:143], a[80:83], a[16:19], v[140:143]\n" \
"v_mfma_f32_16x16x32_bf16 v[144:147], a[64:67], a[32:35], 0\n" \
"v_mfma_f32_16x16x32_bf16 v[144:147], a[68:71], a[36:39], v[144:147]\n" \
"v_mfma_f32_16x16x32_bf16 v[144:147], a[72:75], a[40:43], v[144:147]\n" \
"v_mfma_f32_16x16x32_bf16 v[144:147], a[76:79], a[44:47], v[144:147]\n" \
"v_mfma_f32_16x16x32_bf16 v[148:151], a[92:95], a[44:47], 0\n" \
"v_mfma_f32_16x16x32_bf16 v[148:151], a[88:91], a[40:43], v[148:151]\n" \
"v_mfma_f32_16x16x32_bf16 v[148:151], a[84:87], a[36:39], v[148:151]\n" \
"v_mfma_f32_16x16x32_bf16 v[148:151], a[80:83], a[32:35], v[148:151]\n" \
"v_mfma_f32_16x16x32_bf16 v[152:155], a[64:67], a[48:51], 0\n" \
"v_mfma_f32_16x16x32_bf16 v[152:155], a[68:71], a[52:55], v[152:155]\n" \
"v_mfma_f32_16x16x32_bf16 v[152:155], a[72:75], a[56:59], v[152:155]\n" \
"v_mfma_f32_16x16x32_bf16 v[152:155], a[76:79], a[60:63], v[152:155]\n" \
"v_mfma_f32_16x16x32_bf16 v[156:159], a[92:95], a[60:63], 0\n" \
"v_mfma_f32_16x16x32_bf16 v[156:159], a[88:91], a[56:59], v[156:159]\n" \
"v_mfma_f32_16x16x32_bf16 v[156:159], a[84:87], a[52:55], v[156:159]\n" \
"v_mfma_f32_16x16x32_bf16 v[156:159], a[80:83], a[48:51], v[156:159]\n" \
"s_nop 7\n" \
"s_nop 0\n" \
"v_max3_f32 v230, v128, v129, v130\n" \
"v_max3_f32 v231, v131, v132, v133\n" \
"v_max3_f32 v230, v230, v134, v135\n" \
"v_max3_f32 v225, v230, v231, v225\n" \
"v_max3_f32 v230, v136, v137, v138\n" \
"v_max3_f32 v231, v139, v140, v141\n" \
"v_max3_f32 v230, v230, v142, v143\n" \
"v_max3_f32 v226, v230, v231, v226\n" \
"v_max3_f32 v230, v144, v145, v146\n" \
"v_max3_f32 v231, v147, v148, v149\n" \
"v_max3_f32 v230, v230, v150, v151\n" \
"v_max3_f32 v227, v230, v231, v227\n" \
"v_max3_f32 v230, v152, v153, v154\n" \
"v_max3_f32 v231, v155, v156, v157\n" \
"v_max3_f32 v230, v230, v158, v159\n" \
"v_max3_f32 v228, v230, v231, v228\n" \
"s_nop 3\n" \
"s_nop 0\n" \
"v_mfma_f32_16x16x32_bf16 v[128:131], a[96:99], a[0:3], 0\n" \
"v_mfma_f32_16x16x32_bf16 v[128:131], a[100:103], a[4:7], v[128:131]\n" \
"v_mfma_f32_16x16x32_bf16 v[128:131], a[104:107], a[8:11], v[128:131]\n" \
"v_mfma_f32_16x16x32_bf16 v[128:131], a[108:111], a[12:15], v[128:131]\n" \
"v_mfma_f32_16x16x32_bf16 v[132:135], a[124:127], a[12:15], 0\n" \
"v_mfma_f32_16x16x32_bf16 v[132:135], a[120:123], a[8:11], v[132:135]\n" \
"v_mfma_f32_16x16x32_bf16 v[132:135], a[116:119], a[4:7], v[132:135]\n" \
"v_mfma_f32_16x16x32_bf16 v[132:135], a[112:115], a[0:3], v[132:135]\n" \
"v_mfma_f32_16x16x32_bf16 v[136:139], a[96:99], a[16:19], 0\n" \
"v_mfma_f32_16x16x32_bf16 v[136:139], a[100:103], a[20:23], v[136:139]\n" \
"v_mfma_f32_16x16x32_bf16 v[136:139], a[104:107], a[24:27], v[136:139]\n" \
"v_mfma_f32_16x16x32_bf16 v[136:139], a[108:111], a[28:31], v[136:139]\n" \
"v_mfma_f32_16x16x32_bf16 v[140:143], a[124:127], a[28:31], 0\n" \
"v_mfma_f32_16x16x32_bf16 v[140:143], a[120:123], a[24:27], v[140:143]\n" \
"v_mfma_f32_16x16x32_bf16 v[140:143], a[116:119], a[20:23], v[140:143]\n" \
"v_mfma_f32_16x16x32_bf16 v[140:143], a[112:115], a[16:19], v[140:143]\n" \
"v_mfma_f32_16x16x32_bf16 v[144:147], a[96:99], a[32:35], 0\n" \
"v_mfma_f32_16x16x32_bf16 v[144:147], a[100:103], a[36:39], v[144:147]\n" \
"v_mfma_f32_16x16x32_bf16 v[144:147], a[104:107], a[40:43], v[144:147]\n" \
"v_mfma_f32_16x16x32_bf16 v[144:147], a[108:111], a[44:47], v[144:147]\n" \
"v_mfma_f32_16x16x32_bf16 v[148:151], a[124:127], a[44:47], 0\n" \
"v_mfma_f32_16x16x32_bf16 v[148:151], a[120:123], a[40:43], v[148:151]\n" \
"v_mfma_f32_16x16x32_bf16 v[148:151], a[116:119], a[36:39], v[148:151]\n" \
"v_mfma_f32_16x16x32_bf16 v[148:151], a[112:115], a[32:35], v[148:151]\n" \
"v_mfma_f32_16x16x32_bf16 v[152:155], a[96:99], a[48:51], 0\n" \
"v_mfma_f32_16x16x32_bf16 v[152:155], a[100:103], a[52:55], v[152:155]\n" \
"v_mfma_f32_16x16x32_bf16 v[152:155], a[104:107], a[56:59], v[152:155]\n" \
"v_mfma_f32_16x16x32_bf16 v[152:155], a[108:111], a[60:63], v[152:155]\n" \
"v_mfma_f32_16x16x32_bf16 v[156:159], a[124:127], a[60:63], 0\n" \
"v_mfma_f32_16x16x32_bf16 v[156:159], a[120:123], a[56:59], v[156:159]\n" \
"v_mfma_f32_16x16x32_bf16 v[156:159], a[116:119], a[52:55], v[156:159]\n" \
"v_mfma_f32_16x16x32_bf16 v[156:159], a[112:115], a[48:51], v[156:159]\n" \
"s_nop 7\n" \
"s_nop 0\n" \
"v_max3_f32 v230, v128, v129, v130\n" \
"v_max3_f32 v231, v131, v132, v133\n" \
"v_max3_f32 v230, v230, v134, v135\n" \
"v_max3_f32 v225, v230, v231, v225\n" \
"v_max3_f32 v230, v136, v137, v138\n" \
"v_max3_f32 v231, v139, v140, v141\n" \
"v_max3_f32 v230, v230, v142, v143\n" \
"v_max3_f32 v226, v230, v231, v226\n" \
"v_max3_f32 v230, v144, v145, v146\n" \
"v_max3_f32 v231, v147, v148, v149\n" \
"v_max3_f32 v230, v230, v150, v151\n" \
"v_max3_f32 v227, v230, v231, v227\n" \
"v_max3_f32 v230, v152, v153, v154\n" \
"v_max3_f32 v231, v155, v156, v157\n" \
"v_max3_f32 v230, v230, v158, v159\n" \
"v_max3_f32 v228, v230, v231, v228\n" \
"s_nop 3\n" \
"s_add_u32 s48, s48, s54\n" \
"s_sub_u32 s51, s51, 1\n" \
"s_cmp_lg_u32 s51, 0\n" \
"s_cbranch_scc1 Lsl_%=\n" \
"s_nop 0\n" \
"v_mov_b32_e32 v232, v225\n" \
"v_mov_b32_e32 v233, v226\n" \
"v_mov_b32_e32 v234, v227\n" \
"v_mov_b32_e32 v235, v228\n" \
"s_nop 0\n" \
"v_permlane16_swap_b32_e32 v225, v232\n" \
"v_permlane16_swap_b32_e32 v226, v233\n" \
"v_permlane16_swap_b32_e32 v227, v234\n" \
"v_permlane16_swap_b32_e32 v228, v235\n" \
"v_max_f32_e32 v225, v225, v232\n" \
"v_max_f32_e32 v226, v226, v233\n" \
"v_max_f32_e32 v227, v227, v234\n" \
"v_max_f32_e32 v228, v228, v235\n" \
"v_mov_b32_e32 v232, v225\n" \
"v_mov_b32_e32 v233, v226\n" \
"v_mov_b32_e32 v234, v227\n" \
"v_mov_b32_e32 v235, v228\n" \
"s_nop 0\n" \
"v_permlane32_swap_b32_e32 v225, v232\n" \
"v_permlane32_swap_b32_e32 v226, v233\n" \
"v_permlane32_swap_b32_e32 v227, v234\n" \
"v_permlane32_swap_b32_e32 v228, v235\n" \
"v_max_f32_e32 v225, v225, v232\n" \
"v_max_f32_e32 v226, v226, v233\n" \
"v_max_f32_e32 v227, v227, v234\n" \
"v_max_f32_e32 v228, v228, v235\n" \
"s_cmp_lg_u32 s69, 0\n" \
"s_cbranch_scc1 Lnewm_%=\n" \
"v_sub_f32_e32 v225, 0, v160\n" \
"v_sub_f32_e32 v226, 0, v164\n" \
"v_sub_f32_e64 v227, 0, v168\n" \
"v_sub_f32_e32 v228, 0, v172\n" \
"Lnewm_%=:\n" \
"s_mov_b32 s48, 0\n" \
"s_mov_b32 m0, s40\n" \
"s_nop 0\n" \
"buffer_load_dwordx4 %[kd0], %[srdk], s48 offen offset:0 lds\n" \
"buffer_load_dwordx4 v212, %[srdk], s48 offen offset:1024 lds\n" \
"buffer_load_dwordx4 v213, %[srdk], s48 offen offset:2048 lds\n" \
"buffer_load_dwordx4 v214, %[srdk], s48 offen offset:3072 lds\n" \
"s_mov_b32 s48, 16384\n" \
"s_mov_b32 m0, s41\n" \
"s_nop 0\n" \
"buffer_load_dwordx4 %[kd0], %[srdk], s48 offen offset:0 lds\n" \
"buffer_load_dwordx4 v212, %[srdk], s48 offen offset:1024 lds\n" \
"buffer_load_dwordx4 v213, %[srdk], s48 offen offset:2048 lds\n" \
"buffer_load_dwordx4 v214, %[srdk], s48 offen offset:3072 lds\n" \
"s_add_u32 m0, %[m0b], 65536\n" \
"s_nop 0\n" \
"s_nop 0\n" \
"buffer_load_dwordx4 %[vd0], %[srdv], s48 offen offset:0 lds\n" \
"buffer_load_dwordx4 v215, %[srdv], s48 offen offset:1024 lds\n" \
"buffer_load_dwordx4 %[vd0], %[srdv], s48 offen offset:2048 lds\n" \
"buffer_load_dwordx4 v215, %[srdv], s48 offen offset:3072 lds\n" \
"s_mov_b32 s48, 32768\n" \
"s_mov_b32 m0, s42\n" \
"s_nop 0\n" \
"buffer_load_dwordx4 %[kd0], %[srdk], s48 offen offset:0 lds\n" \
"buffer_load_dwordx4 v212, %[srdk], s48 offen offset:1024 lds\n" \
"buffer_load_dwordx4 v213, %[srdk], s48 offen offset:2048 lds\n" \
"buffer_load_dwordx4 v214, %[srdk], s48 offen offset:3072 lds\n" \
"s_add_u32 m0, %[m0b], 81920\n" \
"s_nop 0\n" \
"s_nop 0\n" \
"buffer_load_dwordx4 %[vd0], %[srdv], s48 offen offset:0 lds\n" \
"buffer_load_dwordx4 v215, %[srdv], s48 offen offset:1024 lds\n" \
"buffer_load_dwordx4 %[vd0], %[srdv], s48 offen offset:2048 lds\n" \
"buffer_load_dwordx4 v215, %[srdv], s48 offen offset:3072 lds\n" \
"s_mov_b32 s48, 49152\n" \
"s_mov_b32 m0, s43\n" \
"s_nop 0\n" \
"buffer_load_dwordx4 %[kd0], %[srdk], s48 offen offset:0 lds\n" \
"buffer_load_dwordx4 v212, %[srdk], s48 offen offset:1024 lds\n" \
"buffer_load_dwordx4 v213, %[srdk], s48 offen offset:2048 lds\n" \
"buffer_load_dwordx4 v214, %[srdk], s48 offen offset:3072 lds\n" \
"s_add_u32 m0, %[m0b], 98304\n" \
"s_nop 0\n" \
"s_nop 0\n" \
"buffer_load_dwordx4 %[vd0], %[srdv], s48 offen offset:0 lds\n" \
"buffer_load_dwordx4 v215, %[srdv], s48 offen offset:1024 lds\n" \
"buffer_load_dwordx4 %[vd0], %[srdv], s48 offen offset:2048 lds\n" \
"buffer_load_dwordx4 v215, %[srdv], s48 offen offset:3072 lds\n" \
"s_waitcnt vmcnt(0)\n" \
"s_barrier\n" \
"ds_read_b128 a[64:67], %[kb0] offset:0\n" \
"ds_read_b128 a[68:71], v208 offset:0\n" \
"ds_read_b128 a[72:75], v209 offset:0\n" \
"ds_read_b128 a[76:79], v210 offset:0\n" \
"ds_read_b128 a[92:95], v210 offset:4096\n" \
"ds_read_b128 a[88:91], v209 offset:4096\n" \
"ds_read_b128 a[84:87], v208 offset:4096\n" \
"ds_read_b128 a[80:83], %[kb0] offset:4096\n" \
"s_mov_b32 s48, 65536\n" \
"s_mov_b32 s60, 0\n" \
"s_waitcnt lgkmcnt(0)\n" \
"v_mfma_f32_16x16x32_bf16 v[128:131], a[64:67], a[0:3], 0\n" \
"ds_read_b128 a[96:99], %[kb0] offset:8192\n" \
"v_mfma_f32_16x16x32_bf16 v[128:131], a[68:71], a[4:7], v[128:131]\n" \
"ds_read_b128 a[100:103], v208 offset:8192\n" \
"v_mfma_f32_16x16x32_bf16 v[128:131], a[72:75], a[8:11], v[128:131]\n" \
"ds_read_b128 a[104:107], v209 offset:8192\n" \
"v_mfma_f32_16x16x32_bf16 v[128:131], a[76:79], a[12:15], v[128:131]\n" \
"ds_read_b128 a[108:111], v210 offset:8192\n" \
"v_mfma_f32_16x16x32_bf16 v[132:135], a[92:95], a[12:15], 0\n" \
"ds_read_b128 a[124:127], v210 offset:12288\n" \
"v_mfma_f32_16x16x32_bf16 v[132:135], a[88:91], a[8:11], v[132:135]\n" \
"ds_read_b128 a[120:123], v209 offset:12288\n" \
"v_mfma_f32_16x16x32_bf16 v[132:135], a[84:87], a[4:7], v[132:135]\n" \
"ds_read_b128 a[116:119], v208 offset:12288\n" \
"v_mfma_f32_16x16x32_bf16 v[132:135], a[80:83], a[0:3], v[132:135]\n" \
"ds_read_b128 a[112:115], %[kb0] offset:12288\n" \
"v_mfma_f32_16x16x32_bf16 v[136:139], a[64:67], a[16:19], 0\n" \
"ds_read_b64_tr_b16 a[128:129], %[vvb] offset:0\n" \
"v_mfma_f32_16x16x32_bf16 v[136:139], a[68:71], a[20:23], v[136:139]\n" \
"ds_read_b64_tr_b16 a[130:131], %[vvb] offset:4096\n" \
"v_mfma_f32_16x16x32_bf16 v[136:139], a[72:75], a[24:27], v[136:139]\n" \
"ds_read_b64_tr_b16 a[132:133], v211 offset:0\n" \
"v_mfma_f32_16x16x32_bf16 v[136:139], a[76:79], a[28:31], v[136:139]\n" \
"ds_read_b64_tr_b16 a[134:135], v211 offset:4096\n" \
"v_mfma_f32_16x16x32_bf16 v[140:143], a[92:95], a[28:31], 0\n" \
"ds_read_b64_tr_b16 a[136:137], %[vvb] offset:512\n" \
"v_mfma_f32_16x16x32_bf16 v[140:143], a[88:91], a[24:27], v[140:143]\n" \
"ds_read_b64_tr_b16 a[138:139], %[vvb] offset:4608\n" \
"v_mfma_f32_16x16x32_bf16 v[140:143], a[84:87], a[20:23], v[140:143]\n" \
"ds_read_b64_tr_b16 a[140:141], v211 offset:512\n" \
"v_mfma_f32_16x16x32_bf16 v[140:143], a[80:83], a[16:19], v[140:143]\n" \
"ds_read_b64_tr_b16 a[142:143], v211 offset:4608\n" \
"v_mfma_f32_16x16x32_bf16 v[144:147], a[64:67], a[32:35], 0\n" \
"ds_read_b64_tr_b16 a[144:145], %[vvb] offset:1024\n" \
"v_mfma_f32_16x16x32_bf16 v[144:147], a[68:71], a[36:39], v[144:147]\n" \
"ds_read_b64_tr_b16 a[146:147], %[vvb] offset:5120\n" \
"v_mfma_f32_16x16x32_bf16 v[144:147], a[72:75], a[40:43], v[144:147]\n" \
"ds_read_b64_tr_b16 a[148:149], v211 offset:1024\n" \
"v_mfma_f32_16x16x32_bf16 v[144:147], a[76:79], a[44:47], v[144:147]\n" \
"ds_read_b64_tr_b16 a[150:151], v211 offset:5120\n" \
"v_mfma_f32_16x16x32_bf16 v[148:151], a[92:95], a[44:47], 0\n" \
"ds_read_b64_tr_b16 a[152:153], %[vvb] offset:1536\n" \
"v_mfma_f32_16x16x32_bf16 v[148:151], a[88:91], a[40:43], v[148:151]\n" \
"ds_read_b64_tr_b16 a[154:155], %[vvb] offset:5632\n" \
"v_mfma_f32_16x16x32_bf16 v[148:151], a[84:87], a[36:39], v[148:151]\n" \
"ds_read_b64_tr_b16 a[156:157], v211 offset:1536\n" \
"v_mfma_f32_16x16x32_bf16 v[148:151], a[80:83], a[32:35], v[148:151]\n" \
"ds_read_b64_tr_b16 a[158:159], v211 offset:5632\n" \
"v_mfma_f32_16x16x32_bf16 v[152:155], a[64:67], a[48:51], 0\n" \
"v_mfma_f32_16x16x32_bf16 v[152:155], a[68:71], a[52:55], v[152:155]\n" \
"v_mfma_f32_16x16x32_bf16 v[152:155], a[72:75], a[56:59], v[152:155]\n" \
"v_mfma_f32_16x16x32_bf16 v[152:155], a[76:79], a[60:63], v[152:155]\n" \
"v_mfma_f32_16x16x32_bf16 v[156:159], a[92:95], a[60:63], 0\n" \
"v_mfma_f32_16x16x32_bf16 v[156:159], a[88:91], a[56:59], v[156:159]\n" \
"v_mfma_f32_16x16x32_bf16 v[156:159], a[84:87], a[52:55], v[156:159]\n" \
"v_mfma_f32_16x16x32_bf16 v[156:159], a[80:83], a[48:51], v[156:159]\n" \
"s_branch Lhavem_%=\n" \
"s_nop 0\n" \
".p2align 12\n" \
"adaln_pg0:\n" \
"s_cbranch_execz adaln_pg1\n" \
"Lend_%=:\n" \
""

#define SRD_WORD3 0x00020000
DI void top2_merge(unsigned& a1, unsigned& a2, unsigned b1, unsigned b2) {
  const unsigned n1 = max(a1, b1), n2 = max(min(a1, b1), max(a2, b2)); a1 = n1; a2 = n2;
}
template <int CTRL> DI void top2_dpp(unsigned& k1, unsigned& k2) {
  const unsigned p1 = __builtin_amdgcn_update_dpp(0u, k1, CTRL, 0xf, 0xf, true), p2 = __builtin_amdgcn_update_dpp(0u, k2, CTRL, 0xf, 0xf, true); top2_merge(k1, k2, p1, p2);
}
DI float row_sum(float v) {
  v = dpp_add<DPP_XOR1>(v); v = dpp_add<DPP_XOR2>(v); v = dpp_add<DPP_ROR8>(v);
  v = swap16_add(v, v); return swap32_add(v, v);
}
#define PAGE(n, nx) asm volatile("s_branch adaln_pe" #n "\n.p2align 12\nadaln_pg" #n ":\n\ts_cbranch_execz " nx "\nadaln_pe" #n ":")
#define STR_(x) #x
#define STR(x) STR_(x)
constexpr int MOE_WGS = DFF / 16;
constexpr unsigned RETRY_CAP = 4096;
#define ASLEEP 2
#define KLOOK 6
#define FSLEEP 14
#define KSLEEP 3
namespace fa {
constexpr unsigned LDS_BYTES = 163840;
constexpr float QSCALE = 0.12751743082f;
struct Args { const u16 *x, *wmod, *wq, *kc, *vc, *wo, *wg, *wu, *wd, *c, *wr; char* ws; u16* out;
};
__global__ __launch_bounds__(256, 1) void fattn_k(const Args p) {
  extern __shared__ __attribute__((aligned(16))) char lds[];
  const unsigned wid = __builtin_amdgcn_readfirstlane(threadIdx.x >> 6);
  const unsigned b = blockIdx.x, idx = b >> 3, kg = b & 3u, m = idx & 7u, hd = 8u * kg + m, y = (b & 4u) + (idx >> 3);
  const unsigned ldsb = (unsigned)(size_t)lds;
  const unsigned m0b = ldsb + 4096u * wid, m0t = ldsb + 131072u + 8192u * wid;
  const __amdgpu_buffer_rsrc_t srdz = __builtin_amdgcn_make_buffer_rsrc((void*)p.ws, (short)0, (int)WS_END, SRD_WORD3);
  const bool moe = b < MOE_WGS;
  const unsigned sl = 4u * ((b & 7u) * 7u + (idx >> 2)) + (idx & 3u), yp2 = (y & 1u) * 2u;
  unsigned hx = (y >> 2) * 8192u;
  unsigned cx = 0, sin = (unsigned)WS_XIN + (((y & 3u) * 32u + hd) * 4u + wid) * BOX + (hx ^ ((~kg & 1u) * 8192u));
  unsigned sfl = (unsigned)WS_OFL + (y & 3u) * 32768u + hx + kg * 128u, spart = (unsigned)WS_PART + (b >> 1) * 16384u + ((wid >> 1) ^ 1u) * 8192u + (b & 1u) * 4096u + (wid & 1u) * 2048u;
  unsigned fl, sd0, got, got2; unsigned long long tag; u16* pout;
  f32x2 xf[16]; f32x4 gaf[8], aw[8]; u32x4 alo, ahi; unsigned late; float arow;
#define FQ(i)
#define TQ(i)
  {
    const unsigned tid = threadIdx.x, lane = tid & 63u, l16 = lane & 15u, g = lane >> 4, hi = lane >> 5, r8 = (lane >> 2) & 7u;
    unsigned kb0 = ldsb + 256u * l16 + 16u * (g ^ l16);
    unsigned vvb = ldsb + 65536u + 2048u * hi + 288u * (g & 1u) + 64u * ((lane >> 2) & 3u) + 8u * (lane & 3u);
    unsigned vwb = ldsb + 65536u + 2048u * g + 64u * ((lane >> 2) & 3u) + 16u * (lane & 3u);
    unsigned kd0 = 4096u * wid + 256u * g + 16u * (l16 ^ g);
    unsigned vd0 = 4096u * wid + 256u * r8 + 64u * hi + 16u * ((lane & 3u) ^ (2u * (r8 >> 2))) + 1024u;
    unsigned wd0 = 8192u * (16u * wid + r8) + 64u * hi + 16u * (lane & 3u) + 4096u;
    unsigned hq0 = (unsigned)WS_H + (16u * y + 4u * wid) * 32768u + 16u * lane;
    unsigned oq0 = (unsigned)WS_XIN + (((y & 3u) * 32u + kg * 8u) * 4u + wid) * BOX + (hx ^ ((~kg & 1u) * 8192u)) + 32768u + 16u * lane;
    unsigned flag = ldsb + 49152u + 4u * wid, wbytes = moe ? DM * DFF * 2 : 0;
    const __amdgpu_buffer_rsrc_t srdw = __builtin_amdgcn_make_buffer_rsrc((void*)((const char*)p.wq + 256u * hd - 4096), (short)0, (int)(DM * DQ * 2 - 256u * hd + 4096u), SRD_WORD3);
    __amdgpu_buffer_rsrc_t srdk = __builtin_amdgcn_make_buffer_rsrc((void*)((const char*)p.kc + (size_t)hd * (SEQ * 256)), (short)0, SEQ * 256, SRD_WORD3);
    __amdgpu_buffer_rsrc_t srdv = __builtin_amdgcn_make_buffer_rsrc((void*)((const char*)p.vc + (size_t)hd * (SEQ * 256) - 17408), (short)0, SEQ * 256 + 17408, SRD_WORD3);
    unsigned long long pct; unsigned clen;
    asm volatile("s_getpc_b64 %0\n\ts_mov_b32 %1, adaln_text_end@rel32@lo+4" : "=s"(pct), "=s"(clen));
    const unsigned vct = min(((idx * 4u + wid) * 6u + min(lane, 5u)) * 128u, (clen - 4u) & ~3u);
      const unsigned gr = 128u * (b & 7u) + 4u * (tid >> 3), gc = 64u * (b >> 3) + 8u * (tid & 7u);
      u32x4 w[4], xs[4]; u32x2 ep, cv;
#define XV(i) ({ u32x4 v_; asm volatile("v_accvgpr_read_b32 %0, a[%4]\n\tv_accvgpr_read_b32 %1, a[%4 + 1]\n\tv_accvgpr_read_b32 %2, a[%4 + 2]\n\tv_accvgpr_read_b32 %3, a[%4 + 3]" : "=v"(v_[0]), "=v"(v_[1]), "=v"(v_[2]), "=v"(v_[3]) : "n"(200 + 4 * (i))); v_; })
      asm volatile("global_load_dwordx2 %0, %1, %2" : "=v"(cv) : "v"(gr * 2u), "s"(p.c) : "memory");
      #pragma unroll
      for (int j = 0; j < 4; ++j) asm volatile("global_load_dwordx4 %0, %1, %2 nt" : "=v"(w[j]) : "v"(((gr + j) * NMOD + gc) * 2u), "s"(p.wmod) : "memory");
      #pragma unroll
      for (int q = 0; q < 4; ++q) asm volatile("global_load_dwordx4 %0, %1, %2 offset:%3 nt" : "=v"(xs[q]) : "v"((8u * (y * 32u + hd) + 2u * wid) * 2048u + 16u * lane), "s"(p.x), "n"(1024 * q) : "memory");
      asm volatile("global_load_dwordx2 %0, %1, %2 sc1" : "=v"(ep) : "v"(0u), "s"(p.ws + WS_EP) : "memory");
      asm volatile("buffer_store_dwordx4 %0, %1, %2, 0 offen sc1\n\ts_nop 1" :: "v"(u32x4{0u, 0u, 0u, 0u}), "v"(tid < Z_SHARE / 16u ? (unsigned)WS_Z + b * Z_SHARE + tid * 16u : 0x80000000u), "s"(srdz) : "memory");
      FQ(6);
      asm volatile("global_load_dword a255, %0, %1" :: "v"(vct), "s"(pct) : "memory");
      asm volatile("s_mov_b64 exec, 0\n\ts_cmp_eq_u32 %0, 1\n\ts_cbranch_scc1 adaln_pg5\n\ts_cmp_eq_u32 %0, 2\n\ts_cbranch_scc1 adaln_pg6\n\ts_cmp_eq_u32 %0, 3\n\ts_cbranch_scc1 adaln_pg7\nadaln_fr:\n\ts_mov_b64 exec, -1" :: "s"(wid) : "scc");
      FQ(7);
      unsigned tq0 = (idx * 8u + wid * 2u + ((lane >> 3) & 1u)) * 8192u + (b & 4u) * 256u + (lane & 7u) * 128u + 4096u - 256u * m;
      const unsigned rb0 = 16u * y + 4u * (hd >> 3), ch = 16u * (hd & 7u) + (tid >> 4);
      unsigned vrw = ldsb + 135168u + wid * 256u + 32u * (tid & 7u) + 8u * g, vrr = ldsb + 135168u + 4u * lane;
      unsigned vmo = wid ? 0x80000000u : (unsigned)WS_MOD + b * 512u + lane * 8u, vst = (lane & 31u) ? 0x80000000u : (unsigned)WS_RST + (8u * (y * 32u + hd) + 2u * wid + hi) * 16u;
      unsigned vx2 = (rb0 * 16u + l16) * 2048u + ch * 16u;
      pout = p.out;
      asm volatile("s_waitcnt vmcnt(11)" : "+v"(cv), "+v"(tq0), "+v"(vrw), "+v"(vrr), "+v"(vmo), "+v"(vst), "+v"(vx2), "+s"(pout) :: "memory");
      auto silu2 = [](unsigned v) { const f32x2 f = unpk(v); return cvtpk(f[0] * __builtin_amdgcn_rcpf(1.f + __builtin_amdgcn_exp2f(f[0] * -1.44269504089f)), f[1] * __builtin_amdgcn_rcpf(1.f + __builtin_amdgcn_exp2f(f[1] * -1.44269504089f))); };
      auto dot2 = [](unsigned x, unsigned z, float c) { return __builtin_amdgcn_fdot2_f32_bf16(__builtin_bit_cast(bf16x2, x), __builtin_bit_cast(bf16x2, z), c, false); };
      unsigned s01 = silu2(cv[0]), s23 = silu2(cv[1]);
      asm volatile("s_waitcnt vmcnt(7)" : "+v"(s01), "+v"(s23), "+v"(w[0]), "+v"(w[1]), "+v"(w[2]), "+v"(w[3]) :: "memory");
      FQ(4);
      asm volatile("buffer_load_dword a255, %0, %1, 0 offen" :: "v"(tq0), "s"(srdw) : "memory");
      {
        float acc[8];
        #pragma unroll
        for (int e = 0; e < 4; ++e) {
          acc[2 * e]     = dot2(__builtin_amdgcn_perm(w[3][e], w[2][e], 0x05040100u), s23, dot2(__builtin_amdgcn_perm(w[1][e], w[0][e], 0x05040100u), s01, 0.f));
          acc[2 * e + 1] = dot2(__builtin_amdgcn_perm(w[3][e], w[2][e], 0x07060302u), s23, dot2(__builtin_amdgcn_perm(w[1][e], w[0][e], 0x07060302u), s01, 0.f));
        }
        float t[4], u[2];
        #pragma unroll
        for (int e = 0; e < 4; ++e) t[e] = swap32_add(acc[e], acc[e + 4]);
        #pragma unroll
        for (int e = 0; e < 2; ++e) u[e] = dpp_add<DPP_ROR8>(swap16_add(t[e], t[e + 2]));
        asm volatile("ds_write_b64 %5, %6\n\ts_waitcnt vmcnt(3)" : "+v"(xs[0]), "+v"(xs[1]), "+v"(xs[2]), "+v"(xs[3]), "+v"(ep) : "v"(vrw), "v"(f32x2{u[0], u[1]}) : "memory");
      }
      tag = (((unsigned long long)(unsigned)__builtin_amdgcn_readfirstlane(ep[1]) << 32) | (unsigned)__builtin_amdgcn_readfirstlane(ep[0])) + 1ull;
      const unsigned tl = (unsigned)tag & 0xffu, th = (unsigned)(tag >> 8);
      u32x4 sg;
      {
        float t4[4] = {0.f, 0.f, 0.f, 0.f};
        #pragma unroll
        for (int e = 0; e < 16; ++e) { const unsigned xw = xs[e >> 2][e & 3]; t4[2 * (e >> 3)] = dot2(xw, 0x3f803f80u, t4[2 * (e >> 3)]); t4[2 * (e >> 3) + 1] = dot2(xw, xw, t4[2 * (e >> 3) + 1]); }
        float u = swap16_add(swap32_add(t4[0], t4[2]), swap32_add(t4[1], t4[3]));
        u = dpp_add<DPP_XOR1>(u); u = dpp_add<DPP_XOR2>(u); u = dpp_add<DPP_ROR4>(u); u = dpp_add<DPP_ROR8>(u);
        const auto r16 = __builtin_amdgcn_permlane16_swap(__float_as_uint(u), __float_as_uint(u), false, false);
        const float mean = __uint_as_float(r16[0]) * (1.f / DM), m2 = mean * mean;
        float var = fmaf(__uint_as_float(r16[1]), 1.f / DM, -m2);
        if (__builtin_expect(__any(!(m2 <= var)), 0)) {
          const f32x2 mm = {__uint_as_float(__builtin_amdgcn_readlane(__float_as_uint(mean), 0)), __uint_as_float(__builtin_amdgcn_readlane(__float_as_uint(mean), 32))};
          f32x2 sq = {0.f, 0.f};
          #pragma unroll
          for (int e = 0; e < 8; ++e) {
            const f32x2 d0 = f32x2{lo16(xs[e >> 2][e & 3]), lo16(xs[2 + (e >> 2)][e & 3])} - mm, d1 = f32x2{hi16(xs[e >> 2][e & 3]), hi16(xs[2 + (e >> 2)][e & 3])} - mm;
            sq = __builtin_elementwise_fma(d1, d1, __builtin_elementwise_fma(d0, d0, sq));
          }
          const float v0 = wave_sum(sq[0]), v1 = wave_sum(sq[1]);
          var = (hi ? v1 : v0) * (1.f / DM);
        }
        sg = u32x4{cvtpk(0.f, mean) | tl, th, cvtpk(0.f, __builtin_amdgcn_sqrtf(rbh(rbh(var) + 1e-5f))) | tl, th};
      }
      {
        f32x2 ra, rc;
        asm volatile("s_waitcnt vmcnt(2) lgkmcnt(0)\n\ts_barrier\n\tds_read2st64_b32 %0, %2 offset1:1\n\tds_read2st64_b32 %1, %2 offset0:2 offset1:3\n\t"
                     "buffer_store_dwordx4 %3, %4, %5, 0 offen sc1\n\ts_waitcnt lgkmcnt(0)" : "=&v"(ra), "=&v"(rc) : "v"(vrr), "v"(sg), "v"(vst), "s"(srdz) : "memory");
        ra += rc;
        asm volatile("buffer_store_dwordx2 %0, %1, %2, 0 offen sc1" :: "v"(u32x2{((__float_as_uint(ra[0] + ra[1]) + 0x80u) & 0xffffff00u) | tl, th}), "v"(vmo), "s"(srdz) : "memory");
      }
      FQ(5);
      asm volatile("global_load_dwordx2 a[216:217], %0, %1" :: "v"(tid * 8u), "s"(p.c) : "memory");
      #pragma unroll
      for (int i = 0; i < 4; ++i) asm volatile("global_load_dwordx4 a[%2:%2 + 3], %0, %1" :: "v"(vx2 + i * 32768u), "s"(p.x), "n"(200 + 4 * i) : "memory");
      PAGE(5, "adaln_fr");
      unsigned lane2 = lane; asm volatile("" : "+v"(lane2));
#define lane lane2
    unsigned wo0 = (32u * wid + r8) * 2048u + m * 256u + 64u * hi + 16u * ((lane & 3u) ^ (2u * (r8 >> 2)));
    __amdgpu_buffer_rsrc_t srdt = __builtin_amdgcn_make_buffer_rsrc((void*)((const char*)p.wo + (size_t)kg * (DM * DM * 2)), (short)0, DM * DM * 2, SRD_WORD3);
    __amdgpu_buffer_rsrc_t srdp = __builtin_amdgcn_make_buffer_rsrc((void*)((const char*)(y < 4u ? p.kc : p.vc) + (size_t)hd * (SEQ * 256) + (y & 3u) * 4096u), (short)0, (int)(SEQ * 256 - (y & 3u) * 4096u), SRD_WORD3);
    unsigned vg0 = (256u * wid + (lane >> 1)) * (DFF * 2u) + sl * 32u + 16u * (lane & 1u);
    unsigned vdn = (16u * sl + hi) * 2048u + 512u * wid + 16u * (lane & 31u);
    const unsigned mc0 = 2u * DM + 64u * ((idx >> 2) * 8u + (b & 7u)) + 16u * (idx & 3u);
    const unsigned md0 = (256u * wid + (lane >> 1)) * (NMOD * 2u) + mc0 * 2u + 16u * (lane & 1u);
    sd0 = ldsb + 131072u + 8192u + 512u * wid + 16u * g;
    fl = md0;
    unsigned rd0 = ldsb + 131072u + 7168u + 8192u * ((lane >> 2) & 3u) + 256u * wid + 4u * (lane & 3u);
    unsigned ta0 = m0t + 256u * g + 32u * ((lane >> 2) & 3u) + 8u * (lane & 3u);
    const unsigned c8 = (b * 4u + wid) * 8192u, cd = moe ? sl * 32768u + wid * 8192u : 0u, cw = (idx * 4u + wid) * 16384u;
    const unsigned cm = moe ? ((sl & 3u) * 4u + wid) * 64u * (DFF * 2u) + (sl >> 2) * 128u : 0u;
    unsigned vxs = BOX * (l16 >> 1) + 64u * (l16 & 1u) + 16u * g;
    const unsigned sxs = (unsigned)WS_XIN + ((y & 3u) * 128u + wid * 32u) * BOX + (hx ^ ((~wid & 1u) * 8192u)) + (m >> 1) * 1024u + kg * 256u + (m & 1u) * 128u;
    unsigned vx0 = ((256u * y + 8u * hd + 2u * wid + ((lane >> 2) & 1u)) * DM + 256u * hi + 128u * (l16 >> 3) + 16u * (g & 1u) + 4u * (lane & 3u) + 32u * yp2) * 2u;
    unsigned tlo = 0, thi = 0;
#define tab(ln, lo, hi2) asm("v_writelane_b32 %0, %2, " #ln "\n\tv_writelane_b32 %1, %3, " #ln : "+v"(tlo), "+v"(thi) : "s"(lo), "s"(hi2))
#define tabp(ln, q, off) do { const unsigned long long a_ = (size_t)(q) + (off); tab(ln, (unsigned)a_, (unsigned)(a_ >> 32)); } while (0)
    tabp(1, p.x, ((y * 32u + hd) * 2u + (wid & 1u)) * 8192u); tabp(2, p.wu, moe ? c8 : 0u); tabp(3, p.wo, c8); tabp(4, p.wd, cd);
    tabp(5, p.wo, kg * 2097152u + cw); tabp(6, p.wo, kg * 2097152u + cw + 8192u); tabp(7, p.wg, cm);
    tabp(17, p.wmod, 0u); tab(18, sin + 32768u, sfl);
    tab(19, (unsigned)WS_OFL + (y & 3u) * 32768u + (hd >> 4) * 16384u + hx + (hd & 15u) * 512u + wid * 128u, 0u);
    tabp(8, p.wg, 0u); tabp(9, p.wu, 0u); tabp(10, p.wd, 0u);
    tab(11, m, m * 4u * BOX); tab(12, (y & 1u) * 128u, 128u - (y & 1u) * 128u);
    tab(13, b, (unsigned)WS_PFL + (y * 32u + hd) * 512u + kg * 128u); tab(14, sxs, 0u);
    tab(15, (unsigned)WS_MODT + (mc0 - 2u * DM + 4u * wid) * 4u, (mc0 >> 10) == 4u ? 0x3f800000u : 0u);
    tab(16, PPAR && (b & 1u) ? 0x80000000u : (unsigned)WS_PFL + y * 16384u + (hd * 4u + wid) * 4u, (unsigned)WS_PFL + (y * 32u + hd) * 512u + wid * 128u);
    const char* psig = p.ws + WS_CNT + y * 8192u + hd * 8u;
    const char* pcnt = p.ws + WS_CNT + y * 8192u + hd * 256u;
    tabp(20, p.wr, 0u); tab(21, spart, (unsigned)WS_ROWS + (y * 32u + hd) * 8192u + wid * 1024u);
      asm volatile("" : "+v"(tlo), "+v"(thi), "+v"(wo0), "+v"(vg0), "+v"(vdn), "+v"(ta0), "+v"(vxs), "+v"(vx0), "+v"(rd0), "+v"(fl), "+v"(sd0), "+v"(kb0), "+v"(vvb), "+v"(vwb), "+v"(kd0), "+v"(vd0), "+v"(wd0), "+v"(hq0), "+v"(oq0));
      asm volatile("" : "+s"(srdk), "+s"(srdv), "+s"(srdt), "+s"(srdp), "+s"(psig), "+s"(pcnt), "+s"(flag), "+s"(wbytes));
#undef lane
      u32x2 mp[8]; u32x4 rs;
      unsigned vlm = ((wid >> 1) * 16u + 2u * (hd & 7u) + (wid & 1u)) * 4096u + lane * 8u, vlr = (unsigned)WS_RST + ((rb0 + wid) * 16u + l16) * 16u;
#define LOOK9 "buffer_load_dwordx2 %0, %[vlm], %[srdz], %[som] offen sc1\n\tbuffer_load_dwordx2 %1, %[vlm], %[srdz], %[som] offen offset:512 sc1\n\tbuffer_load_dwordx2 %2, %[vlm], %[srdz], %[som] offen offset:1024 sc1\n\t" \
              "buffer_load_dwordx2 %3, %[vlm], %[srdz], %[som] offen offset:1536 sc1\n\tbuffer_load_dwordx2 %4, %[vlm], %[srdz], %[som] offen offset:2048 sc1\n\tbuffer_load_dwordx2 %5, %[vlm], %[srdz], %[som] offen offset:2560 sc1\n\t" \
              "buffer_load_dwordx2 %6, %[vlm], %[srdz], %[som] offen offset:3072 sc1\n\tbuffer_load_dwordx2 %7, %[vlm], %[srdz], %[som] offen offset:3584 sc1\n\tbuffer_load_dwordx4 %8, %[vlr], %[srdz], 0 offen sc1\n\t"
#define LREG(c) c "{v[0:1]}"(mp[0]), c "{v[2:3]}"(mp[1]), c "{v[4:5]}"(mp[2]), c "{v[6:7]}"(mp[3]), c "{v[8:9]}"(mp[4]), c "{v[10:11]}"(mp[5]), c "{v[12:13]}"(mp[6]), c "{v[14:15]}"(mp[7]), c "{v[16:19]}"(rs)
      asm volatile(LOOK9 : LREG("=&") : [vlm] "v"(vlm), [vlr] "v"(vlr), [srdz] "s"(srdz), [som] "s"((unsigned)WS_MOD) : "memory");
      #pragma unroll
      for (int q = 0; q < 14; ++q)
        asm volatile("s_mov_b32 m0, %0\n\ts_nop 0\n\tbuffer_load_dwordx4 %1, %2, %3 offen lds" :: "s"(m0b + 65536u + 16384u * (q >> 2) + 1024u * (q & 3)), "v"(wd0), "s"(srdw),
                     "s"(0x80000u * (q >> 2) + 65536u * ((q >> 1) & 1) + 128u * (q & 1)) : "m0", "memory");
      asm volatile("s_waitcnt vmcnt(23)" ::: "memory");
      { const u32x4 cs = XV(4); *(u32x2*)(lds + 131072 + 8192 + tid * 8u) = u32x2{silu2(cs[0]), silu2(cs[1])}; }
      f32x2 xu[16];
      #pragma unroll
      for (int i = 0; i < 4; ++i) { const u32x4 o = XV(i);
        #pragma unroll
        for (int e = 0; e < 4; ++e) { xu[4 * i + e] = unpk(o[e]); asm volatile("" : "+v"(xu[4 * i + e])); } }
      unsigned vpw = ldsb + 131072u + 4u * tid, vpr = ldsb + 131072u + 1024u + 32u * l16 + 8u * wid, vpc = ldsb + 131072u + 32u * (tid >> 4), vpq = ldsb + 131072u + 1024u + 32u * l16;
      unsigned vh = (unsigned)WS_H + rb0 * 32768u + ch * 256u + 16u * l16; float one = wid >> 1 ? 1.f : 0.f;
      {
        unsigned bl, bh, cnt = SPIN_CAP;
#define XO(d, r, t) "v_bitop3_b32 %[" #d "], v" #r ", %[" #t "], %[" #d "] bitop3:0xbe\n\t"
        asm volatile(
          "s_waitcnt vmcnt(14)\n\t"
          "s_branch Lm1_%=\n"
          "Lm0_%=:\n\t"
          "s_sleep " STR(ASLEEP) "\n\t"
          LOOK9
          "s_waitcnt vmcnt(0)\n"
          "Lm1_%=:\n\t"
          "v_bitop3_b32 %[bl], v0, v2, %[tl] bitop3:0x7e\n\t"
          "v_bitop3_b32 %[bh], v1, v3, %[th] bitop3:0x7e\n\t"
          XO(bl, 4, tl) XO(bh, 5, th) XO(bl, 6, tl) XO(bh, 7, th) XO(bl, 8, tl) XO(bh, 9, th) XO(bl, 10, tl) XO(bh, 11, th) XO(bl, 12, tl) XO(bh, 13, th) XO(bl, 14, tl) XO(bh, 15, th)
          XO(bl, 16, tl) XO(bh, 17, th) XO(bl, 18, tl) XO(bh, 19, th)
          "v_and_or_b32 %[bl], %[bl], %[ff], %[bh]\n\t"
          "v_cmp_ne_u32 vcc, 0, %[bl]\n\t"
          "s_sub_u32 %[cnt], %[cnt], 1\n\t"
          "s_cbranch_scc1 Lm2_%=\n\t"
          "s_cbranch_vccnz Lm0_%=\n"
          "Lm2_%=:"
          : LREG("+"), [bl] "=&v"(bl), [bh] "=&v"(bh), [cnt] "+s"(cnt), "+v"(vpw), "+v"(vpr), "+v"(vpc), "+v"(vpq), "+v"(vh), "+v"(one)
          : [vlm] "v"(vlm), [vlr] "v"(vlr), [srdz] "s"(srdz), [som] "s"((unsigned)WS_MOD), [tl] "s"(tl), [th] "s"(th), [ff] "v"(0xffu) : "vcc", "scc", "memory");
      }
      f32x4 a0, a1, c0, c1, t0, t1;
      {
        auto val = [&](int k) { return __uint_as_float(mp[k][0] & 0xffffff00u); };
        const float v = rbh(one + rbh(((val(0) + val(1)) + (val(2) + val(3))) + ((val(4) + val(5)) + (val(6) + val(7)))));
        const float rd = __builtin_amdgcn_rcpf(hi16(rs[2]));
        asm volatile("ds_write_b32 %0, %1\n\tds_write_b64 %2, %3" :: "v"(vpw), "v"(v * QSCALE), "v"(vpr), "v"(f32x2{-hi16(rs[0]) * rd, rd}) : "memory");
      }
      PAGE(6, "adaln_fr");
      asm volatile("s_waitcnt lgkmcnt(0)\n\ts_barrier\n\tds_read_b128 %4, %7\n\tds_read_b128 %0, %6\n\tds_read_b128 %2, %6 offset:512\n\tds_read_b128 %1, %6 offset:16\n\tds_read_b128 %3, %6 offset:528\n\tds_read_b128 %5, %7 offset:16\n\t"
                   "buffer_load_dword a255, %8, %9, %10 offen\n\ts_waitcnt lgkmcnt(0)"
                   : "=&v"(a0), "=&v"(a1), "=&v"(c0), "=&v"(c1), "=&v"(t0), "=&v"(t1) : "v"(vpc), "v"(vpq), "v"(tq0), "s"(srdw), "s"(0x200000u) : "memory");
      FQ(0);
      #pragma unroll
      for (int i = 0; i < 4; ++i) {
        const float mn = i < 2 ? t0[2 * i] : t1[2 * i - 4], rd = i < 2 ? t0[2 * i + 1] : t1[2 * i - 3];
        u32x4 o;
        #pragma unroll
        for (int e = 0; e < 4; ++e) {
          const f32x2 sc = e < 2 ? f32x2{c0[2 * e], c0[2 * e + 1]} : f32x2{c1[2 * e - 4], c1[2 * e - 3]}, sh = e < 2 ? f32x2{a0[2 * e], a0[2 * e + 1]} : f32x2{a1[2 * e - 4], a1[2 * e - 3]};
          o[e] = cvtpk2(__builtin_elementwise_fma(__builtin_elementwise_fma(xu[4 * i + e], f32x2{rd, rd}, f32x2{mn, mn}), sc, sh));
        }
        asm volatile("buffer_store_dwordx4 %0, %1, %2, %3 offen sc1\n\ts_nop 1" :: "v"(o), "v"(vh), "s"(srdz), "s"(32768u * i) : "memory");
      }
      FQ(1);
    asm volatile(FA_BODY
      : FA_OUTS, [sin] "+s"(sin), [oq0] "+v"(oq0), [tlo] "+v"(tlo), [thi] "+v"(thi), [cx] "+s"(cx)
      : [kb0] "v"(kb0), [vvb] "v"(vvb), [kd0] "v"(kd0), [vd0] "v"(vd0),
        [m0b] "s"(m0b), [flag] "s"(flag), [srdz] "s"(srdz), [srdw] "s"(srdw), [srdk] "s"(srdk), [srdv] "s"(srdv), [m0t] "s"(m0t), [srdt] "s"(srdt), [srdp] "s"(srdp),
        [tag] "s"(tag), [pcnt] "s"(pcnt), [psig] "s"(psig), [wid] "s"(wid), [rd0] "v"(rd0), [wo0] "v"(wo0), [vg0] "v"(vg0), [vx0] "v"(vx0), [px] "s"(p.x), [vct] "v"(vct), [pct] "s"(pct), [vdn] "v"(vdn), [ta0] "v"(ta0), [vxs] "v"(vxs),
        [wbytes] "s"(wbytes)
      : FA_CLOBBERS);
    cx = __builtin_amdgcn_readfirstlane(cx); hx ^= cx; sfl ^= cx; spart ^= cx;
  }
#define RDELAYN 16
#define RDELAY2 4
#define RDELAY3 8
#define RCHKN 5
  unsigned lane; asm volatile("v_mbcnt_lo_u32_b32 %0, -1, 0\n\tv_mbcnt_hi_u32_b32 %0, -1, %0" : "=v"(lane));
  const unsigned tid = wid * 64u + lane, n = lane & 15u, g = lane >> 4;
  const unsigned r0 = 256u * y + 8u * hd + 2u * wid, row = r0 + ((n >> 2) & 1u);
  const unsigned colb = 256u * (g >> 1) + 128u * (n >> 3) + 16u * (g & 1u) + 4u * (n & 3u);
  char* par = lds;
  const unsigned cq0 = colb + 32u * yp2, cq1 = colb + 32u * (2u ^ yp2);
  auto parq = [&](int o, int k) { return *(const u32x2*)(par + o + (k >> 2 ? cq1 : cq0) * 2u + 64 * ((k >> 1) & 1) + 1024 * (k & 1)); };
  auto parf = [&](int o, int k) { return *(const f32x4*)(par + o + (k >> 2 ? cq1 : cq0) * 4u + 128 * ((k >> 1) & 1) + 2048 * (k & 1)); };
  if (__builtin_expect(__any(late != 0u), 0)) {
    u32x4 mt[4]; u32x2 wr;
    asm volatile("s_barrier" ::: "memory");
    for (unsigned spins = 0; spins <= SPIN_CAP; ++spins) {
      asm volatile("s_nop 4\n\tglobal_load_dwordx2 %0, %1, %2" : "=v"(wr) : "v"(tid * 8u), "s"(p.wr) : "memory");
      #pragma unroll
      for (int k = 0; k < 4; ++k) asm volatile("buffer_load_dwordx4 %0, %1, %2, %3 offen sc1" : "=v"(mt[k]) : "v"(tid * 16u), "s"(srdz), "s"((unsigned)WS_MODT + 4096u * k) : "memory");
      asm volatile("s_waitcnt vmcnt(0)" : "+v"(mt[0]), "+v"(mt[1]), "+v"(mt[2]), "+v"(mt[3]), "+v"(wr) :: "memory");
      const u32x4 t = mt[0] & mt[1] & mt[2] & mt[3];
      if (__all((t[0] & t[1] & t[2] & t[3] & 1u) != 0u)) break;
    }
    const f32x4 a = __builtin_bit_cast(f32x4, mt[2]) * f32x4{lo16(wr[0]), hi16(wr[0]), lo16(wr[1]), hi16(wr[1])};
    *(u32x4*)(par + tid * 16u) = mt[0]; *(f32x4*)(par + 4096 + tid * 16u) = a;
    #pragma unroll
    for (int k = 1; k < 4; ++k) *(u32x2*)(par + (k == 3 ? 14336 : 6144 + 2048 * k) + tid * 8u) = u32x2{__builtin_amdgcn_perm(mt[k][1], mt[k][0], 0x07060302u), __builtin_amdgcn_perm(mt[k][3], mt[k][2], 0x07060302u)};
    *(float*)(par + 12424 + 16u * wid) = wave_sum((a[0] + a[1]) + (a[2] + a[3]));
    asm volatile("s_waitcnt lgkmcnt(0)\n\ts_barrier" ::: "memory");
    #pragma unroll
    for (int k = 0; k < 8; ++k) { gaf[k] = parf(0, k); aw[k] = parf(4096, k); }
    arow = dpp_add<DPP_XOR2>(dpp_add<DPP_XOR1>(*(const float*)(par + 12424 + 16u * (lane & 3u))));
  }
  unsigned vin = lane * 16u, vin3 = lane * 16u + 20480u, vin2 = lane * 16u + 16384u, kp = (unsigned)WS_KEYS + (b & 3u) * 2048u + hx + 32u * lane;
  TQ(0);
  f32x2 x1[16], s2, d2, q2, sbf[16], gbf[16], c1f[16];
  float mrow, rdrow, drow;
  unsigned vkey, vkb, vkr, nrow, vpub, ta, tu, tb; char* pk;
  {
#define H2(v, h) f32x2{v[2 * h], v[2 * h + 1]}
#define PART(R, c) do { f32x4 t0, t1; \
      asm volatile("v_mfma_f32_16x16x32_bf16 %0, %2, a[" #R ":" #R "+3], 0\n\tv_mfma_f32_16x16x32_bf16 %1, %2, a[" #R "+8:" #R "+11], 0\n\t" \
                   "v_mfma_f32_16x16x32_bf16 %0, %3, a[" #R "+4:" #R "+7], %0\n\tv_mfma_f32_16x16x32_bf16 %1, %3, a[" #R "+12:" #R "+15], %1\n\ts_nop 6" : "=&v"(t0), "=&v"(t1) : "v"(alo), "v"(ahi)); \
      f32x2 u0 = H2(t0, 0), u1 = H2(t0, 1), u2 = H2(t1, 0), u3 = H2(t1, 1); \
      asm volatile("v_pk_fma_f32 %0, %0, %4, %8\n\tv_pk_fma_f32 %1, %1, %5, %9\n\tv_pk_fma_f32 %2, %2, %6, %10\n\tv_pk_fma_f32 %3, %3, %7, %11" : "+v"(u0), "+v"(u1), "+v"(u2), "+v"(u3) \
                   : "v"(H2(gaf[2 * c], 0)), "v"(H2(gaf[2 * c], 1)), "v"(H2(gaf[2 * c + 1], 0)), "v"(H2(gaf[2 * c + 1], 1)), "v"(xf[4 * c]), "v"(xf[4 * c + 1]), "v"(xf[4 * c + 2]), "v"(xf[4 * c + 3])); \
      x1[4 * c] = rb2(u0); x1[4 * c + 1] = rb2(u1); x1[4 * c + 2] = rb2(u2); x1[4 * c + 3] = rb2(u3); } while (0)
#define AC3(k) "v_pk_add_f32 %0, %0, %[y" #k "]\n\tv_pk_fma_f32 %1, %[y" #k "], %[w" #k "], %1\n\tv_pk_fma_f32 %2, %[y" #k "], %[y" #k "], %2\n\t"
    auto part_ = [&](int c2, f32x2& sa, f32x2& da, f32x2& qa) __attribute__((always_inline)) {
      if (c2 == 0) PART(64, 0); else if (c2 == 1) PART(80, 1); else if (c2 == 2) PART(96, 2); else PART(112, 3);
      asm volatile(AC3(0) AC3(1) AC3(2) AC3(3) : "+v"(sa), "+v"(da), "+v"(qa) : [y0] "v"(x1[4 * c2]), [y1] "v"(x1[4 * c2 + 1]), [y2] "v"(x1[4 * c2 + 2]), [y3] "v"(x1[4 * c2 + 3]),
                   [w0] "v"(H2(aw[2 * c2], 0)), [w1] "v"(H2(aw[2 * c2], 1)), [w2] "v"(H2(aw[2 * c2 + 1], 0)), [w3] "v"(H2(aw[2 * c2 + 1], 1)));
    };
#define RCV4(o, a, b, c, d) \
      "buffer_load_dwordx4 " #a ", %[vin" #o "], %[srdz], %[sin] offen sc1\n\t" \
      "buffer_load_dwordx4 " #b ", %[vin" #o "], %[srdz], %[sin] offen offset:1024 sc1\n\t" \
      "buffer_load_dwordx4 " #c ", %[vin" #o "], %[srdz], %[sin] offen offset:2048 sc1\n\t" \
      "buffer_load_dwordx4 " #d ", %[vin" #o "], %[srdz], %[sin] offen offset:3072 sc1\n\t"
#define RWAIT(lv, w) \
      "Lrq" #lv "_%=:\n\t" \
      "v_mov_b32 " w ", 0\n\t" \
      "s_mov_b32 %[sc], " STR(RCHKN) "\n\t" \
      LOOK2 \
      "Lrs" #lv "_%=:\n\t" \
      "ds_read_b32 %[tmp], %[vsh]\n\t" \
      "s_waitcnt lgkmcnt(0)\n\t" \
      "v_and_or_b32 %[tmp], %[tmp], " #lv ", " w "\n\t" \
      "v_cmp_eq_u32 vcc, 0, %[tmp]\n\t" \
      "s_cbranch_vccz Lrd" #lv "_%=\n\t" \
      "s_sub_u32 %[sc], %[sc], 1\n\t" \
      "s_cbranch_scc0 Lrs" #lv "_%=\n\t" \
      "s_sub_u32 %[cnt], %[cnt], 1\n\t" \
      "s_cselect_b32 %[cnt], 0, %[cnt]\n\t" \
      "s_waitcnt vmcnt(0)\n\t" \
      "v_cmp_eq_u32 vcc, 0, " w "\n\t" \
      "s_cbranch_scc1 Lrl" #lv "_%=\n\t" \
      "s_cbranch_vccnz Lrq" #lv "_%=\n" \
      "Lrd" #lv "_%=:\n\t" \
      "v_mov_b32 %[tmp], " #lv "\n\t" \
      "s_mov_b64 exec, 1\n\t" \
      "ds_or_b32 %[vsh], %[tmp]\n\t" \
      "s_mov_b64 exec, -1\n" \
      "Lrl" #lv "_%=:\n\t"
#define RCV8A RCV4(0, a[64:67], a[68:71], a[72:75], a[76:79]) RCV4(1, a[80:83], a[84:87], a[88:91], a[92:95])
#define RCV4C RCV4(2, a[96:99], a[100:103], a[104:107], a[108:111])
#define RCV4D RCV4(3, a[112:115], a[116:119], a[120:123], a[124:127])
#define RCV8B RCV4C RCV4D
#define LOOK2 "buffer_load_dwordx2 v[228:229], %[vfl], %[srdz], %[sfl] offen offset:64 sc1\n\t"
    unsigned cnt, tmp, sc;
    u32x2 sbq[8], gbq[8], c1q[8];
    #pragma unroll
    for (int k = 0; k < 8; ++k) { sbq[k] = parq(8192, k); c1q[k] = parq(10240, k); gbq[k] = parq(14336, k); }
    #pragma unroll
    for (int i = 0; i < 16; ++i) { sbf[i] = unpk(sbq[i >> 1][i & 1]); c1f[i] = unpk(c1q[i >> 1][i & 1]); asm volatile("" : "+v"(sbf[i]), "+v"(c1f[i])); }
    unsigned vfl = (lane & 15u) * 512u + (lane & 16u) * 1024u, vin1 = lane * 16u + 4096u;
    PAGE(1, "adaln_pg2");
    asm volatile(
      "s_mov_b32 %[go], 0\n\t"
      "s_cmp_lg_u32 %[got], 0\n\t"
      "s_waitcnt vmcnt(4)\n\t"
      "s_cbranch_scc1 Lrf_%=\n\t"
      "v_cmp_eq_u32 vcc, 0, v229\n\t"
      "s_cbranch_vccnz Lrk_%=\n\t"
      "s_mov_b32 %[got], 2\n\t"
      RCV8A
      "Lrf_%=:\n\t"
      "v_cmp_eq_u32 vcc, 0, v228\n\t"
      "s_cbranch_vccnz Lrk_%=\n\t"
      "s_mov_b32 %[go], 1\n\t"
      RCV4C
      "s_branch Lrn_%=\n"
      "Lrk_%=:\n\t"
      LOOK2
      "Lrn_%=:"
      : "+{v228}"(sd0), "+{v229}"(fl), [got] "+s"(got), [go] "=&s"(got2), [vfl] "+v"(vfl), [vin1] "+v"(vin1), "+v"(gbq[0]), "+v"(gbq[1]), "+v"(gbq[2]), "+v"(gbq[3]), "+v"(gbq[4]), "+v"(gbq[5]), "+v"(gbq[6]), "+v"(gbq[7])
      : [srdz] "s"(srdz), [sfl] "s"(sfl), [sin] "s"(sin), [vin0] "v"(vin), [vin2] "v"(vin2) : "vcc", "scc", "memory");
#define NRE
    #pragma unroll
    for (int i = 0; i < 16; ++i) { gbf[i] = unpk(gbq[i >> 1][i & 1]); asm volatile("" : "+v"(gbf[i])); }
    vkey = lane * 2048u + b * 8u; vkb = ldsb + 12288u + 8u * wid + (lane & 4u); vkr = ldsb + 12288u + 4u * (lane & 7u); nrow = 2047u - row;
    vpub = (unsigned)WS_ROWS + (y * 32u + hd) * 8192u + (g >> 1) * 512u + ((n >> 3) * 8u + (g & 1u) * 4u + (n & 3u)) * 16u;
    ta = m0b + 65536u + 8u * lane; tu = m0t + 8u * lane; tb = m0b + 98304u + 16384u * (g >> 1) + 2048u * (g & 1u) + 512u * ((lane >> 2) & 3u) + 8u * (lane & 3u);
    pk = p.ws + WS_KEYS;
    s2 = f32x2{0.f, 0.f}; d2 = s2; q2 = s2;
    f32x2 s3, d3, q3;
    asm volatile(
      "s_mov_b32 %[cnt], 0x10000\n\t"
      "s_cmp_lg_u32 %[go], 0\n\t"
      "s_cbranch_scc1 Lro_%=\n\t"
      "s_cmp_lg_u32 %[got], 0\n\t"
      "s_waitcnt vmcnt(0)\n\t"
      "s_cbranch_scc1 Lrf_%=\n\t"
      "v_cmp_eq_u32 vcc, 0, v229\n\t"
      "s_cbranch_vccz Lrl2_%=\n\t"
      RWAIT(2, "v229")
      RCV8A
      "Lrf_%=:\n\t"
      "v_cmp_eq_u32 vcc, 0, v228\n\t"
      "s_cbranch_vccz Lrm_%=\n\t"
      RWAIT(4, "v228")
      RCV4C
      "s_sleep " STR(RDELAYN) "\n\t"
      "s_branch Lrp_%=\n"
      "Lrm_%=:\n\t"
      RCV4C
      "s_sleep " STR(RDELAY3) "\n\t"
      "s_branch Lrp_%=\n"
      "Lro_%=:\n\t"
      "s_sleep " STR(RDELAY2) "\n"
      "Lrp_%=:\n\t"
      RCV4D
      "s_cmp_eq_u32 %[got], 1\n\t"
      "s_cbranch_scc1 Lrz_%=\n\t"
      "s_waitcnt vmcnt(8)\n"
      "Lrz_%=:"
      : "+{v228}"(sd0), "+{v229}"(fl), [cnt] "=&s"(cnt), [tmp] "=&v"(tmp), [sc] "=&s"(sc), "+v"(s2),
        "+v"(vkey), "+v"(vkb), "+v"(vkr), "+v"(nrow), "+v"(vpub), "+v"(ta), "+v"(tu), "+v"(tb), "+s"(pk)
      : [srdz] "s"(srdz), [got] "s"(got), [go] "s"(got2), [vfl] "v"(vfl), [sfl] "s"(sfl), [sin] "s"(sin), [vin0] "v"(vin), [vin1] "v"(vin1), [vin2] "v"(vin2), [vin3] "v"(vin3), [vsh] "v"(ldsb + 12420u)
      : "vcc", "scc", "memory");
    part_(0, s2, d2, q2); part_(1, s2, d2, q2);
    TQ(2);
    s3 = s2; d3 = d2; q3 = q2;
    asm volatile("s_waitcnt vmcnt(4)" : "+{v228}"(sd0), "+{v229}"(fl), "+v"(s2), "+v"(d2), "+v"(q2), "+v"(s3), "+v"(d3), "+v"(q3) :: "memory");
    part_(2, s3, d3, q3);
    asm volatile("s_waitcnt vmcnt(0)" : "+v"(s3), "+v"(d3), "+v"(q3) :: "memory");
    TQ(3);
    part_(3, s3, d3, q3);
    if (__builtin_expect(__any(s3[0] + s3[1] != s3[0] + s3[1]), 0)) {
      unsigned spins = 0;
      do {
        asm volatile(RCV8B "s_waitcnt vmcnt(0)" :: [srdz] "s"(srdz), [sin] "s"(sin), [vin2] "v"(vin2), [vin3] "v"(vin3) : "memory"); NRE;
        s3 = s2; d3 = d2; q3 = q2;
        part_(2, s3, d3, q3); part_(3, s3, d3, q3);
      } while (__any(s3[0] + s3[1] != s3[0] + s3[1]) && ++spins < RETRY_CAP);
    }
    asm volatile("; all in");
    const float sv = s3[0] + s3[1], dv = d3[0] + d3[1], qv = q3[0] + q3[1];
    float u = swap16_add(swap32_add(sv, dv), swap32_add(qv, qv));
    u = dpp_add<DPP_XOR1>(u); u = dpp_add<DPP_XOR2>(u); u = dpp_add<DPP_ROR8>(u);
    const auto r16 = __builtin_amdgcn_permlane16_swap(__float_as_uint(u), __float_as_uint(u), false, false);
    const auto r32 = __builtin_amdgcn_permlane32_swap(r16[0], r16[0], false, false);
    const float mean = __uint_as_float(r32[0]) * (1.f / DM), m2v = mean * mean;
    drow = __uint_as_float(r32[1]);
    float var = fmaf(__uint_as_float(r16[1]), 1.f / DM, -m2v);
    if (__builtin_expect(__any(!(m2v <= var)), 0)) {
      f32x2 qq = {0.f, 0.f};
      #pragma unroll
      for (int e = 0; e < 16; ++e) { const f32x2 t = x1[e] - mean; qq = __builtin_elementwise_fma(t, t, qq); }
      var = row_sum(qq[0] + qq[1]) * (1.f / DM);
    }
    mrow = rbh(mean); rdrow = __builtin_amdgcn_rcpf(rbh(__builtin_amdgcn_sqrtf(rbh(rbh(var) + 1e-5f))));
  }
  unsigned* kb = (unsigned*)(lds + 12288);
  unsigned mykey, k1w;
  { const unsigned bb = __float_as_uint(fmaf(-mrow, arow, drow) * rdrow); mykey = max((bb ^ ((unsigned)((int)bb >> 31) | 0x80000000u)) & 0xfffff800u, 0x800u) | nrow; }
  asm volatile("ds_write_b32 %1, %2\n\ts_waitcnt lgkmcnt(0)\n\ts_barrier\n\tds_read_b32 %0, %3\n\ts_waitcnt lgkmcnt(0)" : "=&v"(k1w) : "v"(vkb), "v"(mykey), "v"(vkr) : "memory");
  bool cand;
  u32x4 ka, kc;
  {
    const unsigned o1 = __builtin_amdgcn_update_dpp(0u, k1w, DPP_XOR1, 0xf, 0xf, true);
    unsigned k1 = max(k1w, o1), k2 = min(k1w, o1);
    top2_dpp<DPP_XOR2>(k1, k2); top2_dpp<0x141>(k1, k2);
    asm volatile("s_cmp_lg_u32 %[wid], 0\n\ts_cbranch_scc1 Lka_%=\n\ts_mov_b64 exec, %[mk]\n\tglobal_atomic_or_x2 %[va], %[kk], %[pk] sc1\n\ts_mov_b64 exec, -1\nLka_%=:"
                 :: [wid] "s"(wid), [mk] "n"((1u << KEY_REP) - 1u), [va] "v"(vkey), [kk] "v"(u32x2{k2, k1}), [pk] "s"(pk) : "scc", "memory");
    cand = mykey >= k2;
    vpub += mykey == k1 ? 0u : 2048u;
  }
  TQ(4);
  PAGE(2, "adaln_pg3");
#define TRA(r, a, o) asm volatile("ds_read_b64_tr_b16 a[%0:%0 + 1], %1 offset:%2" :: "n"(r), "v"(a), "n"(o) : "memory")
#define FRAGS(k) do { \
      TRA(128 + 8 * (k), ta, 2048 * ((k) >> 2) + 512 * ((k) & 1) + 16384 * (((k) >> 1) & 1)); TRA(130 + 8 * (k), ta, 2048 * ((k) >> 2) + 512 * ((k) & 1) + 16384 * (((k) >> 1) & 1) + 1024); \
      TRA(132 + 8 * (k), tu, 2048 * ((k) >> 2) + 512 * ((k) & 1) + 4096 * (((k) >> 1) & 1)); TRA(134 + 8 * (k), tu, 2048 * ((k) >> 2) + 512 * ((k) & 1) + 4096 * (((k) >> 1) & 1) + 1024); \
      TRA(192 + 4 * (k), tb, 64 * (k)); TRA(194 + 4 * (k), tb, 64 * (k) + 32); } while (0)
  f32x2 h2[16];
  {
    asm volatile("" : "+v"(mrow));
    const float nm = -mrow * rdrow;
    #pragma unroll
    for (int k = 0; k < 8; ++k) {
      #pragma unroll
      for (int e = 0; e < 2; ++e) { const int i = 2 * k + e; h2[i] = __builtin_elementwise_fma(__builtin_elementwise_fma(x1[i], f32x2{rdrow, rdrow}, f32x2{nm, nm}), c1f[i], sbf[i]); }
      if (k & 1) { asm volatile("" : "+v"(h2[2 * k]), "+v"(h2[2 * k + 1])); FRAGS(k >> 1); }
    }
  }
  const unsigned cw = __builtin_amdgcn_readfirstlane((unsigned)__ballot(cand) & 0x11u);
  auto publish = [&](const f32x2* v, unsigned off) __attribute__((always_inline)) {
    #pragma unroll
    for (int k = 0; k < 4; ++k) { const int q = 4 * (k >> 1) + (k & 1);
      asm volatile("buffer_store_dwordx4 %0, %1, %2, %3 offen sc1\n\ts_nop 1" :: "v"(u32x4{cvtpk2(v[2 * q]), cvtpk2(v[2 * q + 1]), cvtpk2(v[2 * q + 4]), cvtpk2(v[2 * q + 5])}), "v"(vpub), "s"(srdz), "s"(off + (k & 1) * 1024u + (((k >> 1) * 2u) ^ yp2) * 128u) : "memory"); }
  };
  if (cand) publish(h2, 0u);
  char* st = lds + 32768 + 4096u * wid;
  auto look1 = [&]() __attribute__((always_inline)) {
    asm volatile("s_cmp_lg_u32 %2, 0\n\ts_cbranch_scc1 Lkl_%=\n\tbuffer_load_dwordx4 %0, %3, %4, 0 offen sc1\n\tbuffer_load_dwordx4 %1, %3, %4, 0 offen offset:16 sc1\nLkl_%=:" : "=&{v[240:243]}"(ka), "=&{v[244:247]}"(kc) : "s"(wid), "v"(kp), "s"(srdz) : "scc", "memory");
  };
  if (KLOOK == 0) look1();
  #pragma unroll
  for (int k = 0; k < 8; ++k) {
    u32x2 yq;
    #pragma unroll
    for (int e = 0; e < 2; ++e) yq[e] = cvtpk2(__builtin_elementwise_fma(gbf[2 * k + e], h2[2 * k + e], x1[2 * k + e]));
    *(u32x2*)(st + (row & 1u) * 2048u + (colb + 32u * ((k >> 2) * 2u ^ yp2)) * 2u + 64 * ((k >> 1) & 1) + 1024 * (k & 1)) = yq;
    if (k & 1) FRAGS(4 + (k >> 1));
    if (k == KLOOK - 1) look1();
  }
  auto out_rows = [&](unsigned which) __attribute__((always_inline)) {
    #pragma unroll
    for (int r = 0; r < 2; ++r) {
      if ((which >> (4 * r)) & 1u) {
        const u32x4 a = *(const u32x4*)(st + 2048 * r + 16u * lane), c = *(const u32x4*)(st + 2048 * r + 1024 + 16u * lane);
        asm volatile("s_nop 4\n\tglobal_store_dwordx4 %0, %1, %3 offset:%4 sc1\n\tglobal_store_dwordx4 %0, %2, %3 offset:%5 sc1\n\ts_nop 1" :: "v"(r0 * 2048u + 16u * lane), "v"(a), "v"(c), "s"(pout), "n"(2048 * r), "n"(2048 * r + 1024) : "memory");
      }
    }
  };
  const unsigned un = tid & 127u, ca = 256u * (un >> 5) + 64u * ((un >> 4) & 1u) + 128u * ((un >> 3) & 1u) + 16u * ((un >> 2) & 1u) + 4u * (un & 3u);
  unsigned vrow = (unsigned)WS_ROWS + (moe ? wid * 512u + 16u * g : un * 16u), vsel = ldsb + 12288u + 40u + 4u * (moe ? lane & 1u : wid >> 1);
  unsigned mk[8];
  #pragma unroll
  for (int t = 0; t < 8; ++t) { mk[t] = (unsigned)__builtin_amdgcn_sbfe((int)(1u << (n >> 1)), t, 1); asm volatile("" : "+v"(mk[t])); }
  unsigned vred = ldsb + 24576u + wid * 256u + (lane & 1u) * 64u + 16u * g, vrf = ldsb + 24576u + ((lane & 1u) * 4u + g) * 16u + ((lane >> 1) & 3u) * 4u, vpt = spart + lane * 16u, vkk = ldsb + 12288u + 32u, vkw = vkk + (lane < 48u ? 64u : 0u);
  unsigned long long m2 = 0x0003000300030003ull;
  asm volatile("" : "+v"(vrow), "+v"(vsel), "+v"(vred), "+v"(vrf), "+v"(vpt), "+v"(vkk), "+v"(vkw), "+s"(m2));
  const unsigned later = __builtin_amdgcn_readfirstlane(wid ? cw : 0x11u);
  out_rows(~later);
  TQ(1);
  PAGE(3, "adaln_pg8");
  if (wid == 0) {
    unsigned cnt = SPIN_CAP, t;
    asm volatile(
      "s_waitcnt vmcnt(0)\n"
      "Lk0_%=:\n\t"
      "v_min3_u32 %[t], v240, v242, v244\n\t"
      "v_min_u32 %[t], %[t], v246\n\t"
      "v_cmp_eq_u32 vcc, 0, %[t]\n\t"
      "s_cbranch_vccz Lk1_%=\n\t"
      "s_sleep " STR(KSLEEP) "\n\t"
      "buffer_load_dwordx4 v[240:243], %[kp], %[srdz], 0 offen sc1\n\t"
      "buffer_load_dwordx4 v[244:247], %[kp], %[srdz], 0 offen offset:16 sc1\n\t"
      "s_sub_u32 %[cnt], %[cnt], 1\n\t"
      "s_waitcnt vmcnt(0)\n\t"
      "s_cbranch_scc0 Lk0_%=\n"
      "Lk1_%=:"
      : "+{v[240:243]}"(ka), "+{v[244:247]}"(kc), [t] "=&v"(t), [cnt] "+s"(cnt) : [kp] "v"(kp), [srdz] "s"(srdz) : "vcc", "scc", "memory");
    unsigned k1 = ka[1], k2 = ka[0];
    unsigned l1 = kc[1], l2 = kc[0];
    top2_merge(k1, k2, ka[3], ka[2]); top2_merge(l1, l2, kc[3], kc[2]); top2_merge(k1, k2, l1, l2);
    top2_dpp<DPP_XOR1>(k1, k2); top2_dpp<DPP_XOR2>(k1, k2); top2_dpp<0x141>(k1, k2); top2_dpp<0x140>(k1, k2);
    top2_merge(k1, k2, __builtin_amdgcn_update_dpp(0u, k1, 0x142, 0xa, 0xf, false), __builtin_amdgcn_update_dpp(0u, k2, 0x142, 0xa, 0xf, false));
    top2_merge(k1, k2, __builtin_amdgcn_update_dpp(0u, k1, 0x143, 0xc, 0xf, false), __builtin_amdgcn_update_dpp(0u, k2, 0x143, 0xc, 0xf, false));
    k1 = ~k1 & 2047u; k2 = ~k2 & 2047u;
    *(LAS u32x4*)vkw = u32x4{k1, k2, (k1 >> 3) * 8192u, (k2 >> 3) * 8192u + ((k1 ^ k2) < 8u ? 2048u : 0u)};
  }
  u32x2 kk; unsigned vo;
  asm volatile("s_waitcnt lgkmcnt(0)\n\ts_barrier\n\tds_read_b32 %0, %3\n\tds_read_b64 %1, %4\n\ts_waitcnt lgkmcnt(0)\n\tv_add_u32 %2, %2, %0" : "=&v"(vo), "=&v"(kk), "+v"(vrow) : "v"(vsel), "v"(vkk) : "memory");
  TQ(5);
  unsigned k1, k2, rest;
  auto routed = [&]() __attribute__((always_inline)) {
    asm volatile("" : "+v"(kk));
    k1 = __builtin_amdgcn_readfirstlane(kk[0]); k2 = __builtin_amdgcn_readfirstlane(kk[1]);
    rest = __builtin_amdgcn_readfirstlane(later & ((r0 != k1 && r0 != k2 ? 1u : 0u) | (r0 + 1u != k1 && r0 + 1u != k2 ? 16u : 0u)));
    if (__builtin_expect(rest != later, 0)) { if (row == k1 || row == k2) publish(x1, 4096u); }
  };
  if (__builtin_expect(!moe, 0)) {
    asm volatile("adaln_pg8:\n\ts_cbranch_execz adaln_pg4");
    PAGE(4, "adaln_kr");
    u32x4 ha; u32x2 w0, w1;
    asm volatile("s_nop 4\n\tglobal_load_dwordx2 %0, %2, %3\n\tglobal_load_dwordx2 %1, %2, %3 offset:64" : "=&v"(w0), "=&v"(w1) : "v"(ca * 2u), "s"(p.wr) : "memory");
    asm volatile("buffer_load_dwordx4 %0, %3, %4, 0 offen sc1\n\ts_waitcnt vmcnt(0)" : "=&v"(ha), "+v"(w0), "+v"(w1) : "v"(vrow), "s"(srdz) : "memory");
    for (unsigned spins = 0; __builtin_expect(__any(max(max(ha[0], ha[1]), max(ha[2], ha[3])) == 0xffffffffu), 0) && spins < RETRY_CAP; ++spins)
      asm volatile("buffer_load_dwordx4 %0, %1, %2, 0 offen sc1\n\ts_waitcnt vmcnt(0)" : "=&v"(ha) : "v"(vrow), "s"(srdz) : "memory");
    routed();
    if (b == 255u && tid == 0u) __hip_atomic_store((gu64*)(p.ws + WS_EP), tag, RLX_AGENT);
    char* rows = lds + 16384 + (tid >> 7) * 2048u + ca * 2u;
    *(u32x2*)rows = u32x2{ha[0], ha[1]}; *(u32x2*)(rows + 64) = u32x2{ha[2], ha[3]};
    {
      float dot = 0.f;
      #pragma unroll
      for (int e = 0; e < 4; ++e) { const unsigned w = e < 2 ? w0[e] : w1[e - 2]; dot = fmaf(hi16(ha[e]), hi16(w), fmaf(lo16(ha[e]), lo16(w), dot)); }
      dot = wave_sum(dot);
      if (lane == 0) kb[16u + wid] = __float_as_uint(dot);
    }
    asm volatile("s_waitcnt lgkmcnt(0)\n\ts_barrier" ::: "memory");
    TQ(6);
    const unsigned zj = (b & 4u) * 4u + (idx & 3u) * 4u + (b & 3u), pp = zj * 64u + lane, sq = pp & 1u, nn = 128u * (pp >> 8) + 16u * (2u * ((pp >> 6) & 3u) + ((pp >> 1) & 1u)) + ((pp >> 2) & 15u);
    float gbv = bf2f(*(const u16*)(par + 14336 + nn * 2u)), hv = bf2f(*(const u16*)(lds + 16384 + sq * 2048u + nn * 2u));
    float rsel = rbf(__builtin_amdgcn_rcpf(1.f + __builtin_amdgcn_exp2f(rbf(__uint_as_float(kb[16u + 2u * sq]) + __uint_as_float(kb[17u + 2u * sq])) * -1.44269504089f)));
    u16* vout = pout + (sq ? k2 : k1) * DM + nn;
    unsigned vp = (unsigned)WS_PART + (tid >> 5) * 16384u + (hx ^ 8192u) + ((tid >> 4) & 1u) * 4096u + (zj & 15u) * 256u + (tid & 15u) * 16u;
    unsigned vw = ldsb + 24576u + wid * 256u + (lane & 15u) * 16u + 4u * g, vr = ldsb + 24576u + 4u * lane;
    unsigned vx = (unsigned)WS_ROWS + 4096u + kb[10u + sq] + (nn >> 9) * 1024u + (32u * ((nn >> 8) & 1u) + 16u * ((nn >> 6) & 1u) + 8u * ((nn >> 7) & 1u) + 4u * ((nn >> 4) & 1u) + ((nn >> 2) & 3u)) * 16u + ((nn >> 5) & 1u) * 8u + (nn & 3u) * 2u;
    asm volatile("" : "+v"(gbv), "+v"(hv), "+v"(rsel), "+v"(vout), "+v"(vp), "+v"(vw), "+v"(vr), "+v"(vx));
    out_rows(rest);
    TQ(7);
    __builtin_amdgcn_s_sleep(FSLEEP);
    float u; unsigned xw;
    for (unsigned spins = 0;; ++spins) {
      f32x4 v[14];
      asm volatile("buffer_load_ushort %0, %1, %2, 0 offen sc1" : "=v"(xw) : "v"(vx), "s"(srdz) : "memory");
      #pragma unroll
      for (int k = 0; k < 14; ++k) asm volatile("buffer_load_dwordx4 %0, %1, %2, %3 offen sc1" : "=v"(v[k]) : "v"(vp), "s"(srdz), "s"(131072u * k) : "memory");
      asm volatile("s_waitcnt vmcnt(12)" : "+v"(v[0]), "+v"(v[1]), "+v"(xw) :: "memory");
      f32x4 sum = v[0] + v[1];
#define NEXT2(k) asm volatile("s_waitcnt vmcnt(%3)" : "+v"(v[k]), "+v"(v[k + 1]), "+v"(sum) : "n"(12 - k) : "memory"); sum += v[k]; sum += v[k + 1]
      NEXT2(2); NEXT2(4); NEXT2(6); NEXT2(8); NEXT2(10); NEXT2(12);
      u = swap16_add(swap32_add(sum[0], sum[2]), swap32_add(sum[1], sum[3]));
      if (!__any(u != u || xw == 0xffffu) || spins > RETRY_CAP) break;
    }
    TQ(1);
    f32x2 ra, rc;
    asm volatile("ds_write_b32 %0, %1\n\ts_waitcnt lgkmcnt(0)\n\ts_barrier" :: "v"(vw), "v"(u) : "memory");
    if (wid == 0) {
      asm volatile("ds_read2st64_b32 %0, %2 offset1:1\n\tds_read2st64_b32 %1, %2 offset0:2 offset1:3\n\ts_waitcnt lgkmcnt(0)" : "=&v"(ra), "=&v"(rc) : "v"(vr) : "memory");
      ra += rc;
      const unsigned ov = cvtpk(fmaf(gbv, fmaf(ra[0] + ra[1], rsel, hv), lo16(xw)), 0.f);
      asm volatile("global_store_short %0, %1, off sc1" :: "v"(vout), "v"(ov) : "memory");
    }
  } else {
    f32x4 dg, du;
    bf16x8 hb[8];
    auto fetch = [&]() __attribute__((always_inline)) {
      asm volatile("s_mov_b64 exec, %8\n\t"
                   "buffer_load_dwordx4 %0, %9, %10, 0 offen sc1\n\tbuffer_load_dwordx4 %1, %9, %10, 0 offen offset:64 sc1\n\tbuffer_load_dwordx4 %2, %9, %10, 0 offen offset:128 sc1\n\tbuffer_load_dwordx4 %3, %9, %10, 0 offen offset:192 sc1\n\t"
                   "buffer_load_dwordx4 %4, %9, %10, 0 offen offset:256 sc1\n\tbuffer_load_dwordx4 %5, %9, %10, 0 offen offset:320 sc1\n\tbuffer_load_dwordx4 %6, %9, %10, 0 offen offset:384 sc1\n\tbuffer_load_dwordx4 %7, %9, %10, 0 offen offset:448 sc1\n\t"
                   "s_mov_b64 exec, -1"
                   : "=&v"(hb[0]), "=&v"(hb[1]), "=&v"(hb[2]), "=&v"(hb[3]), "=&v"(hb[4]), "=&v"(hb[5]), "=&v"(hb[6]), "=&v"(hb[7]) : "s"(m2), "v"(vrow), "s"(srdz) : "memory");
    };
#define MM2(a, h, c0, c1) "v_mfma_f32_16x16x32_bf16 %0, a[" #a ":" #a "+3], %" #h ", " c0 "\n\tv_mfma_f32_16x16x32_bf16 %1, a[" #a "+4:" #a "+7], %" #h ", " c1 "\n\t"
#define MM16 MM2(128, 2, "0", "0") MM2(136, 3, "%0", "%1") MM2(144, 4, "%0", "%1") MM2(152, 5, "%0", "%1") MM2(160, 6, "%0", "%1") MM2(168, 7, "%0", "%1") MM2(176, 8, "%0", "%1") MM2(184, 9, "%0", "%1") "s_nop 7"
#define HB "+v"(hb[0]), "+v"(hb[1]), "+v"(hb[2]), "+v"(hb[3]), "+v"(hb[4]), "+v"(hb[5]), "+v"(hb[6]), "+v"(hb[7])
    fetch();
    routed();
    asm volatile("s_cmp_lg_u32 %10, %11\n\ts_waitcnt vmcnt(4)\n\ts_cbranch_scc1 Lh_%=\n\ts_waitcnt vmcnt(0)\nLh_%=:\n\t" MM16 : "=&v"(dg), "=&v"(du), HB : "s"(rest), "s"(later) : "scc", "memory");
    TQ(6);
    for (unsigned spins = 0; __builtin_expect((__ballot(dg[0] != dg[0]) & m2) != 0ull, 0) && spins < RETRY_CAP; ++spins) {
      fetch();
      asm volatile("s_waitcnt vmcnt(0)\n\t" MM16 : "=&v"(dg), "=&v"(du), HB :: "memory");
    }
    f32x2 ra, rc, rd, re;
    asm volatile("s_mov_b64 exec, %7\n\tds_write_b128 %4, %5\n\tds_write_b128 %4, %6 offset:128\n\ts_mov_b64 exec, -1\n\ts_waitcnt lgkmcnt(0)\n\ts_barrier\n\t"
                 "ds_read2_b32 %0, %8 offset1:32\n\tds_read2_b32 %1, %8 offset0:64 offset1:96\n\tds_read2_b32 %2, %8 offset0:128 offset1:160\n\tds_read2_b32 %3, %8 offset0:192 offset1:224\n\ts_waitcnt lgkmcnt(0)"
                 : "=&v"(ra), "=&v"(rc), "=&v"(rd), "=&v"(re) : "v"(vred), "v"(dg), "v"(du), "s"(m2), "v"(vrf) : "memory");
    u32x2 actp;
    {
      const f32x2 gu = rb2((ra + rc) + (rd + re));
      const float sg = __uint_as_float(cvtpk(0.f, gu[0] * __builtin_amdgcn_rcpf(1.f + __builtin_amdgcn_exp2f(gu[0] * -1.44269504089f))));
      const unsigned a = cvtpk(0.f, sg * gu[1]);
      const unsigned o2 = __builtin_amdgcn_update_dpp(0u, a, DPP_XOR2, 0xf, 0xf, true);
      const unsigned pr = (lane & 2u) ? ((o2 >> 16) | a) : ((a >> 16) | o2);
      const unsigned o4 = __builtin_amdgcn_update_dpp(0u, pr, DPP_ROR4, 0xf, 0xf, true);
      actp = (lane & 4u) ? u32x2{o4, pr} : u32x2{pr, o4};
    }
    f32x4 yv[2];
#define ABD(t) "v"(u32x4{actp[0] & mk[t], actp[1] & mk[t], actp[0] & mk[t + 1], actp[1] & mk[t + 1]})
#define MM3(a, w, c0, c1) "v_mfma_f32_16x16x32_bf16 %0, %" #a ", a[" #w ":" #w "+3], " c0 "\n\tv_mfma_f32_16x16x32_bf16 %1, %" #a ", a[" #w "+16:" #w "+19], " c1 "\n\t"
    asm volatile("s_nop 1\n\t" MM3(2, 192, "0", "0") MM3(3, 196, "%0", "%1") MM3(4, 200, "%0", "%1") MM3(5, 204, "%0", "%1") "s_nop 6\n\t"
                 "buffer_store_dwordx4 %0, %6, %7, 0 offen sc1\n\tbuffer_store_dwordx4 %1, %6, %7, 0 offen offset:1024 sc1\n\ts_nop 1"
                 : "=&v"(yv[0]), "=&v"(yv[1]) : ABD(0), ABD(2), ABD(4), ABD(6), "v"(vpt), "s"(srdz) : "memory");
    TQ(7);
    out_rows(rest);
  }
  asm volatile("adaln_text_end:");
}
}

extern "C" void kernel_launch(void* const* d_in, const int* in_sizes, int n_in, void* d_out, int out_size, void* d_ws, size_t ws_size, hipStream_t stream) {
  (void)in_sizes; (void)n_in; (void)out_size;
  if (ws_size < WS_END) return;
  const u16* x    = (const u16*)d_in[0];
  const u16* kc   = (const u16*)d_in[1];
  const u16* vc   = (const u16*)d_in[2];
  const u16* wq   = (const u16*)d_in[3];
  const u16* wo   = (const u16*)d_in[4];
  const u16* wr   = (const u16*)d_in[6];
  const u16* wg   = (const u16*)d_in[7];
  const u16* wu   = (const u16*)d_in[8];
  const u16* wd   = (const u16*)d_in[9];
  const u16* c    = (const u16*)d_in[10];
  const u16* wmod = (const u16*)d_in[11];
  u16* out = (u16*)d_out;
  char* ws = (char*)d_ws;
  fa::Args fr;
  fr.x = x; fr.wmod = wmod; fr.wq = wq; fr.kc = kc; fr.vc = vc; fr.wo = wo; fr.wg = wg; fr.wu = wu; fr.wd = wd; fr.ws = ws; fr.out = out; fr.c = c; fr.wr = wr;
  hipFuncSetAttribute((const void*)fa::fattn_k, hipFuncAttributeMaxDynamicSharedMemorySize, (int)fa::LDS_BYTES);
  hipLaunchKernelGGL(fa::fattn_k, dim3(256), dim3(256), fa::LDS_BYTES, stream, fr);
}
```

```cpp
#include <hip/hip_runtime.h>
#include <hip/hip_bf16.h>
#include <cstdint>

#pragma clang fp contract(off)
typedef float    f32x2  __attribute__((ext_vector_type(2)));
typedef float    f32x4  __attribute__((ext_vector_type(4)));
typedef __bf16   bf16x8 __attribute__((ext_vector_type(8)));
typedef __bf16   bf16x4 __attribute__((ext_vector_type(4)));
typedef __bf16   bf16x2 __attribute__((ext_vector_type(2)));
typedef unsigned u32x4  __attribute__((ext_vector_type(4)));
typedef unsigned u32x2  __attribute__((ext_vector_type(2)));
typedef unsigned short u16;
#define DI __device__ __forceinline__
constexpr int SEQ = 2048, DM = 1024, NHEAD = 32, DHEAD = 128, DQ = NHEAD * DHEAD, DFF = 3584, NMOD = 6 * DM;
constexpr size_t WS_EP    = 0;
constexpr size_t WS_RST   = 32768;
constexpr size_t WS_CNT   = 65536;
constexpr size_t WS_MOD   = 131072;
constexpr size_t WS_Z     = 262144;
constexpr size_t WS_MODT  = WS_Z;
constexpr unsigned KEY_REP = 8, Z_SHARE = 1280;
constexpr size_t WS_KEYS  = WS_Z + 16384;
static_assert(Z_SHARE * 256 == 327680, "zeroed area");
constexpr size_t WS_PFL   = WS_Z + 65536;
constexpr size_t WS_OFL   = WS_Z + 196608;
constexpr size_t WS_H     = 1048576;
constexpr size_t WS_PART  = 6u << 20;
constexpr size_t WS_ROWS  = 8u << 20;
constexpr size_t WS_XIN   = 10u << 20;
constexpr unsigned BOX    = 65536;
constexpr size_t WS_END   = WS_XIN + (size_t)BOX * (SEQ / 4);

DI float bf2f(u16 b) { return __uint_as_float((unsigned)b << 16); }
DI float rbf(float x) { return (float)(__bf16)x; }
DI float lo16(unsigned w) { return __uint_as_float(w << 16); }
DI float hi16(unsigned w) { return __uint_as_float(w & 0xffff0000u); }
DI unsigned cvtpk(float lo, float hi) {
  f32x2 v = {lo, hi}; bf16x2 b = __builtin_convertvector(v, bf16x2); return __builtin_bit_cast(unsigned, b);
}
template <int CTRL> DI float dpp_add(float v) { return v + __uint_as_float(__builtin_amdgcn_update_dpp(0u, __float_as_uint(v), CTRL, 0xf, 0xf, true)); }
DI float swap32_add(float a, float b) {
  auto r = __builtin_amdgcn_permlane32_swap(__float_as_uint(a), __float_as_uint(b), false, false); return __uint_as_float(r[0]) + __uint_as_float(r[1]);
}
DI float swap16_add(float a, float b) {
  auto r = __builtin_amdgcn_permlane16_swap(__float_as_uint(a), __float_as_uint(b), false, false); return __uint_as_float(r[0]) + __uint_as_float(r[1]);
}
constexpr int DPP_XOR1 = 0xB1, DPP_XOR2 = 0x4E, DPP_ROR4 = 0x124, DPP_ROR8 = 0x128;
DI float wave_sum(float v) {
  v = dpp_add<DPP_XOR1>(v); v = dpp_add<DPP_XOR2>(v); v = dpp_add<DPP_ROR4>(v); v = dpp_add<DPP_ROR8>(v);
  v = swap16_add(v, v); return swap32_add(v, v);
}

DI f32x2 unpk(unsigned w) { return f32x2{lo16(w), hi16(w)}; }
DI unsigned cvtpk2(f32x2 v) { return __builtin_bit_cast(unsigned, __builtin_convertvector(v, bf16x2)); }
DI float rbh(float v) { return __uint_as_float(cvtpk(0.f, v)); }
DI f32x2 rb2(f32x2 v) { return f32x2{__uint_as_float(cvtpk(0.f, v[0])), __uint_as_float(cvtpk(0.f, v[1]))}; }
#define GAS __attribute__((address_space(1)))
#define LAS __attribute__((address_space(3)))
typedef GAS unsigned long long gu64;
#define RLX_AGENT __ATOMIC_RELAXED, __HIP_MEMORY_SCOPE_AGENT
constexpr unsigned SPIN_CAP = 1u << 16;

static_assert(BOX == 65536, "box size");
#define NP_FILL 8
#define GOT_WAIT "8"
#define FA_CLOBBERS "v0", "v1", "v2", "v3", "v8", "v9", "v10", "v11", "v16", "v17", "v18", "v19", "v20", "v21", "v22", "v23", "v24", "v25", "v26", "v27", "v28", "v29", "v30", "v31", "v32", "v33", "v34", "v35", "v40", "v41", "v42", "v43", "v48", "v49", "v50", "v51", "v52", "v53", "v54", "v55", "v56", "v57", "v58", "v59", "v60", "v61", "v62", "v63", "v64", "v65", "v66", "v67", "v72", "v73", "v74", "v75", "v80", "v81", "v82", "v83", "v84", "v85", "v86", "v87", "v88", "v89", "v90", "v91", "v92", "v93", "v94", "v95", "v96", "v97", "v98", "v99", "v104", "v105", "v106", "v107", "v112", "v113", "v114", "v115", "v116", "v117", "v118", "v119", "v120", "v121", "v122", "v123", "v124", "v125", "v126", "v127", "v200", "v201", "v202", "v203", "v205", "v207", "v208", "v209", "v210", "v211", "v212", "v213", "v214", "v215", "v216", "v217", "v218", "v219", "v220", "v221", "v222", "v223", "v224", "v230", "v231", "v232", "v233", "v234", "v235", "v237", "a0", "a1", "a2", "a3", "a4", "a5", "a6", "a7", "a8", "a9", "a10", "a11", "a12", "a13", "a14", "a15", "a16", "a17", "a18", "a19", "a20", "a21", "a22", "a23", "a24", "a25", "a26", "a27", "a28", "a29", "a30", "a31", "a32", "a33", "a34", "a35", "a36", "a37", "a38", "a39", "a40", "a41", "a42", "a43", "a44", "a45", "a46", "a47", "a48", "a49", "a50", "a51", "a52", "a53", "a54", "a55", "a56", "a57", "a58", "a59", "a60", "a61", "a62", "a63", "a64", "a65", "a66", "a67", "a68", "a69", "a70", "a71", "a72", "a73", "a74", "a75", "a76", "a77", "a78", "a79", "a80", "a81", "a82", "a83", "a84", "a85", "a86", "a87", "a88", "a89", "a90", "a91", "a92", "a93", "a94", "a95", "a96", "a97", "a98", "a99", "a100", "a101", "a102", "a103", "a104", "a105", "a106", "a107", "a108", "a109", "a110", "a111", "a112", "a113", "a114", "a115", "a116", "a117", "a118", "a119", "a120", "a121", "a122", "a123", "a124", "a125", "a126", "a127", "a128", "a129", "a130", "a131", "a132", "a133", "a134", "a135", "a136", "a137", "a138", "a139", "a140", "a141", "a142", "a143", "a144", "a145", "a146", "a147", "a148", "a149", "a150", "a151", "a152", "a153", "a154", "a155", "a156", "a157", "a158", "a159", "a160", "a161", "a162", "a163", "a164", "a165", "a166", "a167", "a168", "a169", "a170", "a171", "a172", "a173", "a174", "a175", "a176", "a177", "a178", "a179", "a180", "a181", "a182", "a183", "a184", "a185", "a186", "a187", "a188", "a189", "a190", "a191", "a192", "a193", "a194", "a195", "a196", "a197", "a198", "a199", "a200", "a201", "a202", "a203", "a204", "a205", "a206", "a207", "a208", "a209", "a210", "a211", "a212", "a213", "a214", "a215", "a216", "a217", "a218", "a219", "a220", "a221", "a222", "a223", "a224", "a225", "a226", "a227", "a228", "a229", "a230", "a231", "a232", "a233", "a234", "a235", "a236", "a237", "a238", "a239", "a240", "a241", "a242", "a243", "a244", "a245", "a246", "a247", "a248", "a249", "a250", "a251", "a252", "a253", "a254", "a255", "s40", "s41", "s42", "s43", "s44", "s45", "s46", "s47", "s48", "s49", "s50", "s51", "s52", "s53", "s54", "s55", "s56", "s57", "s58", "s59", "s60", "s61", "s62", "s63", "s64", "s65", "s66", "s67", "s68", "s69", "vcc", "m0", "scc", "memory"
#define EARLY2 0
#define SHW 1
#define QROT 0
#define SKV 0
#define KVPAR 0
#define QT2 0
#define QSTAG 0
#define TLATE 0
#define NTV 5
#define BPERM 0
#define NEAR 3
#define PPAR 1
#define FA_OUTS [got] "=&s"(got), "=&{v[128:129]}"(xf[0]), "=&{v[130:131]}"(xf[1]), "=&{v[132:133]}"(xf[2]), "=&{v[134:135]}"(xf[3]), "=&{v[136:137]}"(xf[4]), "=&{v[138:139]}"(xf[5]), "=&{v[140:141]}"(xf[6]), "=&{v[142:143]}"(xf[7]), "=&{v[144:145]}"(xf[8]), "=&{v[146:147]}"(xf[9]), "=&{v[148:149]}"(xf[10]), "=&{v[150:151]}"(xf[11]), "=&{v[152:153]}"(xf[12]), "=&{v[154:155]}"(xf[13]), "=&{v[156:157]}"(xf[14]), "=&{v[158:159]}"(xf[15]), "=&{v[160:163]}"(gaf[0]), "=&{v[164:167]}"(gaf[1]), "=&{v[168:171]}"(gaf[2]), "=&{v[172:175]}"(gaf[3]), "=&{v[176:179]}"(gaf[4]), "=&{v[180:183]}"(gaf[5]), "=&{v[184:187]}"(gaf[6]), "=&{v[188:191]}"(gaf[7]), "=&{v[4:7]}"(aw[0]), "=&{v[12:15]}"(aw[1]), "=&{v[36:39]}"(aw[2]), "=&{v[44:47]}"(aw[3]), "=&{v[68:71]}"(aw[4]), "=&{v[76:79]}"(aw[5]), "=&{v[100:103]}"(aw[6]), "=&{v[108:111]}"(aw[7]), "=&{v[192:195]}"(alo), "=&{v[196:199]}"(ahi), "=&{v204}"(late), "=&{v206}"(arow), "+{v229}"(fl), "+{v225}"(vwb), "+{v226}"(wd0), "+{v227}"(hq0), "+{v228}"(sd0), "+{v236}"(tq0)
#define FA_BODY \
"s_branch adaln_body\n" \
".p2align 12\n" \
"adaln_pg7:\n" \
"adaln_body:\n" \
"s_cbranch_execz adaln_fr\n" \
"s_nop 0\n" \
"s_mov_b32 s55, 0x80000\n" \
"v_add_u32_e32 v219, 0x8000, v227\n" \
"v_add_u32_e32 v220, 0x10000, v227\n" \
"v_add_u32_e32 v221, 0x18000, v227\n" \
"s_mov_b32 s50, 0\n" \
"s_movk_i32 s53, 0x1000\n" \
"s_mov_b32 s52, 0\n" \
"s_nop 0\n" \
"s_mov_b32 s49, 1572864\n" \
"v_add_u32_e32 v216, 0xfffffc80, v226\n" \
"v_add_u32_e32 v217, 0xf800, v226\n" \
"v_add_u32_e32 v218, 0xf480, v226\n" \
"s_add_u32 s40, %[m0b], 0\n" \
"s_nop 0\n" \
"s_add_u32 s44, %[m0b], 65536\n" \
"s_add_u32 s41, %[m0b], 16384\n" \
"s_add_u32 s45, %[m0b], 81920\n" \
"s_add_u32 s42, %[m0b], 32768\n" \
"s_add_u32 s46, %[m0b], 98304\n" \
"s_add_u32 s43, %[m0b], 49152\n" \
"s_add_u32 s47, %[m0b], 114688\n" \
"v_xor_b32_e64 v208, 64, %[kb0]\n" \
"v_xor_b32_e32 v209, 0x80, %[kb0]\n" \
"v_xor_b32_e32 v210, 0xc0, %[kb0]\n" \
"v_xor_b32_e64 v212, 64, %[kd0]\n" \
"v_xor_b32_e32 v213, 0x80, %[kd0]\n" \
"v_xor_b32_e32 v214, 0xc0, %[kd0]\n" \
"v_xor_b32_e64 v211, 32, %[vvb]\n" \
"v_add_u32_e32 v215, 0xfffffc80, %[vd0]\n" \
"v_readlane_b32 s64, %[tlo], 17\n" \
"v_readlane_b32 s65, %[thi], 17\n" \
"s_mov_b32 s66, 12582912\n" \
"s_mov_b32 s67, 0x20000\n" \
"v_mov_b64_e32 v[0:1], 0\n" \
"v_mov_b64_e32 v[2:3], 0\n" \
"v_mov_b64_e32 v[4:5], 0\n" \
"v_mov_b64_e32 v[6:7], 0\n" \
"v_mov_b64_e32 v[8:9], 0\n" \
"v_mov_b64_e32 v[10:11], 0\n" \
"v_mov_b64_e32 v[12:13], 0\n" \
"v_mov_b64_e32 v[14:15], 0\n" \
"v_mov_b64_e32 v[16:17], 0\n" \
"v_mov_b64_e32 v[18:19], 0\n" \
"v_mov_b64_e32 v[20:21], 0\n" \
"v_mov_b64_e32 v[22:23], 0\n" \
"v_mov_b64_e32 v[24:25], 0\n" \
"v_mov_b64_e32 v[26:27], 0\n" \
"v_mov_b64_e32 v[28:29], 0\n" \
"v_mov_b64_e32 v[30:31], 0\n" \
"v_mov_b64_e32 v[32:33], 0\n" \
"v_mov_b64_e32 v[34:35], 0\n" \
"v_mov_b64_e32 v[36:37], 0\n" \
"v_mov_b64_e32 v[38:39], 0\n" \
"v_mov_b64_e32 v[40:41], 0\n" \
"v_mov_b64_e32 v[42:43], 0\n" \
"v_mov_b64_e32 v[44:45], 0\n" \
"v_mov_b64_e32 v[46:47], 0\n" \
"v_mov_b64_e32 v[48:49], 0\n" \
"v_mov_b64_e32 v[50:51], 0\n" \
"v_mov_b64_e32 v[52:53], 0\n" \
"v_mov_b64_e32 v[54:55], 0\n" \
"v_mov_b64_e32 v[56:57], 0\n" \
"v_mov_b64_e32 v[58:59], 0\n" \
"v_mov_b64_e32 v[60:61], 0\n" \
"v_mov_b64_e32 v[62:63], 0\n" \
"v_mov_b64_e32 v[64:65], 0\n" \
"v_mov_b64_e32 v[66:67], 0\n" \
"v_mov_b64_e32 v[68:69], 0\n" \
"v_mov_b64_e32 v[70:71], 0\n" \
"v_mov_b64_e32 v[72:73], 0\n" \
"v_mov_b64_e32 v[74:75], 0\n" \
"v_mov_b64_e32 v[76:77], 0\n" \
"v_mov_b64_e32 v[78:79], 0\n" \
"v_mov_b64_e32 v[80:81], 0\n" \
"v_mov_b64_e32 v[82:83], 0\n" \
"v_mov_b64_e32 v[84:85], 0\n" \
"v_mov_b64_e32 v[86:87], 0\n" \
"v_mov_b64_e32 v[88:89], 0\n" \
"v_mov_b64_e32 v[90:91], 0\n" \
"v_mov_b64_e32 v[92:93], 0\n" \
"v_mov_b64_e32 v[94:95], 0\n" \
"v_mov_b64_e32 v[96:97], 0\n" \
"v_mov_b64_e32 v[98:99], 0\n" \
"v_mov_b64_e32 v[100:101], 0\n" \
"v_mov_b64_e32 v[102:103], 0\n" \
"v_mov_b64_e32 v[104:105], 0\n" \
"v_mov_b64_e32 v[106:107], 0\n" \
"v_mov_b64_e32 v[108:109], 0\n" \
"v_mov_b64_e32 v[110:111], 0\n" \
"v_mov_b64_e32 v[112:113], 0\n" \
"v_mov_b64_e32 v[114:115], 0\n" \
"v_mov_b64_e32 v[116:117], 0\n" \
"v_mov_b64_e32 v[118:119], 0\n" \
"v_mov_b64_e32 v[120:121], 0\n" \
"v_mov_b64_e32 v[122:123], 0\n" \
"v_mov_b64_e32 v[124:125], 0\n" \
"v_mov_b64_e32 v[126:127], 0\n" \
"s_mov_b32 s51, 0x10000\n" \
"v_mbcnt_lo_u32_b32 v232, -1, 0\n" \
"v_mov_b64_e32 v[234:235], %[tag]\n" \
"v_lshlrev_b32_e32 v233, 3, v232\n" \
"v_lshlrev_b32_e32 v232, 8, v232\n" \
"s_waitcnt vmcnt(0)\n" \
"s_barrier\n" \
"s_cmp_lg_u32 %[wid], 0\n" \
"s_cbranch_scc1 Lwalk_%=\n" \
"s_mov_b32 exec_hi, 0\n" \
"global_store_dwordx2 v232, v[234:235], %[psig] sc1\n" \
"s_mov_b32 exec_hi, -1\n" \
"s_nop 0\n" \
"v_readlane_b32 s62, %[tlo], 13\n" \
"s_nop 3\n" \
"s_cmpk_eq_u32 s62, 252\n" \
"s_cbranch_scc1 Lprobe_%=\n" \
"Lgo_%=:\n" \
"s_nop 0\n" \
"ds_read_b64_tr_b16 v[128:129], v225 offset:0\n" \
"ds_read_b64_tr_b16 v[130:131], v225 offset:256\n" \
"ds_read_b64_tr_b16 v[132:133], v225 offset:8192\n" \
"ds_read_b64_tr_b16 v[134:135], v225 offset:8448\n" \
"ds_read_b64_tr_b16 v[136:137], v225 offset:8\n" \
"ds_read_b64_tr_b16 v[138:139], v225 offset:264\n" \
"ds_read_b64_tr_b16 v[140:141], v225 offset:8200\n" \
"ds_read_b64_tr_b16 v[142:143], v225 offset:8456\n" \
"ds_read_b64_tr_b16 v[144:145], v225 offset:512\n" \
"ds_read_b64_tr_b16 v[146:147], v225 offset:768\n" \
"ds_read_b64_tr_b16 v[148:149], v225 offset:8704\n" \
"ds_read_b64_tr_b16 v[150:151], v225 offset:8960\n" \
"ds_read_b64_tr_b16 v[152:153], v225 offset:520\n" \
"ds_read_b64_tr_b16 v[154:155], v225 offset:776\n" \
"ds_read_b64_tr_b16 v[156:157], v225 offset:8712\n" \
"ds_read_b64_tr_b16 v[158:159], v225 offset:8968\n" \
"s_mov_b32 s60, 4194304\n" \
"v_and_b32_e32 v233, 56, v233\n" \
"s_lshl_b32 s61, %[wid], 6\n" \
"v_add_u32_e64 v233, s61, v233\n" \
"Lpoll_%=:\n" \
"global_load_dwordx2 v[230:231], v233, %[pcnt] sc1\n" \
"s_waitcnt vmcnt(0)\n" \
"v_cmp_ne_u64_e32 vcc, v[230:231], v[234:235]\n" \
"s_sub_u32 s51, s51, 1\n" \
"s_cbranch_scc1 Lpd_%=\n" \
"s_nop 3\n" \
"s_cbranch_vccnz Lpoll_%=\n" \
"Lpd_%=:\n" \
"buffer_load_dwordx4 a[128:131], v227, %[srdz], s50 offen offset:0 sc1\n" \
"buffer_load_dwordx4 a[132:135], v219, %[srdz], s50 offen offset:0 sc1\n" \
"buffer_load_dwordx4 a[136:139], v220, %[srdz], s50 offen offset:0 sc1\n" \
"buffer_load_dwordx4 a[140:143], v221, %[srdz], s50 offen offset:0 sc1\n" \
"buffer_load_dwordx4 a[144:147], v227, %[srdz], s50 offen offset:1024 sc1\n" \
"buffer_load_dwordx4 a[148:151], v219, %[srdz], s50 offen offset:1024 sc1\n" \
"buffer_load_dwordx4 a[152:155], v220, %[srdz], s50 offen offset:1024 sc1\n" \
"buffer_load_dwordx4 a[156:159], v221, %[srdz], s50 offen offset:1024 sc1\n" \
"buffer_load_dwordx4 a[160:163], v227, %[srdz], s50 offen offset:2048 sc1\n" \
"buffer_load_dwordx4 a[164:167], v219, %[srdz], s50 offen offset:2048 sc1\n" \
"buffer_load_dwordx4 a[168:171], v220, %[srdz], s50 offen offset:2048 sc1\n" \
"buffer_load_dwordx4 a[172:175], v221, %[srdz], s50 offen offset:2048 sc1\n" \
"buffer_load_dwordx4 a[176:179], v227, %[srdz], s50 offen offset:3072 sc1\n" \
"buffer_load_dwordx4 a[180:183], v219, %[srdz], s50 offen offset:3072 sc1\n" \
"buffer_load_dwordx4 a[184:187], v220, %[srdz], s50 offen offset:3072 sc1\n" \
"buffer_load_dwordx4 a[188:191], v221, %[srdz], s50 offen offset:3072 sc1\n" \
"s_add_u32 s50, s50, s53\n" \
"s_nop 0\n" \
"buffer_load_dwordx4 a[192:195], v227, %[srdz], s50 offen offset:0 sc1\n" \
"buffer_load_dwordx4 a[196:199], v219, %[srdz], s50 offen offset:0 sc1\n" \
"buffer_load_dwordx4 a[200:203], v220, %[srdz], s50 offen offset:0 sc1\n" \
"buffer_load_dwordx4 a[204:207], v221, %[srdz], s50 offen offset:0 sc1\n" \
"buffer_load_dwordx4 a[208:211], v227, %[srdz], s50 offen offset:1024 sc1\n" \
"buffer_load_dwordx4 a[212:215], v219, %[srdz], s50 offen offset:1024 sc1\n" \
"buffer_load_dwordx4 a[216:219], v220, %[srdz], s50 offen offset:1024 sc1\n" \
"buffer_load_dwordx4 a[220:223], v221, %[srdz], s50 offen offset:1024 sc1\n" \
"s_mov_b32 s51, 3\n" \
"s_waitcnt vmcnt(16) lgkmcnt(0)\n" \
"Lq_%=:\n" \
"v_mfma_f32_16x16x32_bf16 v[0:3], v[128:131], a[128:131], v[0:3]\n" \
"ds_read_b64_tr_b16 v[160:161], v225 offset:1024\n" \
"v_mfma_f32_16x16x32_bf16 v[0:3], v[132:135], a[144:147], v[0:3]\n" \
"ds_read_b64_tr_b16 v[162:163], v225 offset:1280\n" \
"v_mfma_f32_16x16x32_bf16 v[32:35], v[128:131], a[132:135], v[32:35]\n" \
"s_mov_b32 m0, s47\n" \
"s_nop 0\n" \
"buffer_load_dwordx4 v217, %[srdw], s49 offen offset:2048 lds\n" \
"v_mfma_f32_16x16x32_bf16 v[32:35], v[132:135], a[148:151], v[32:35]\n" \
"ds_read_b64_tr_b16 v[164:165], v225 offset:9216\n" \
"v_mfma_f32_16x16x32_bf16 v[64:67], v[128:131], a[136:139], v[64:67]\n" \
"ds_read_b64_tr_b16 v[166:167], v225 offset:9472\n" \
"v_mfma_f32_16x16x32_bf16 v[64:67], v[132:135], a[152:155], v[64:67]\n" \
"v_mfma_f32_16x16x32_bf16 v[96:99], v[128:131], a[140:143], v[96:99]\n" \
"ds_read_b64_tr_b16 v[168:169], v225 offset:1032\n" \
"v_mfma_f32_16x16x32_bf16 v[96:99], v[132:135], a[156:159], v[96:99]\n" \
"ds_read_b64_tr_b16 v[170:171], v225 offset:1288\n" \
"v_mfma_f32_16x16x32_bf16 v[4:7], v[136:139], a[128:131], v[4:7]\n" \
"buffer_load_dwordx4 v218, %[srdw], s49 offen offset:3072 lds\n" \
"v_mfma_f32_16x16x32_bf16 v[4:7], v[140:143], a[144:147], v[4:7]\n" \
"ds_read_b64_tr_b16 v[172:173], v225 offset:9224\n" \
"v_mfma_f32_16x16x32_bf16 v[36:39], v[136:139], a[132:135], v[36:39]\n" \
"ds_read_b64_tr_b16 v[174:175], v225 offset:9480\n" \
"v_mfma_f32_16x16x32_bf16 v[36:39], v[140:143], a[148:151], v[36:39]\n" \
"v_mfma_f32_16x16x32_bf16 v[68:71], v[136:139], a[136:139], v[68:71]\n" \
"ds_read_b64_tr_b16 v[176:177], v225 offset:1536\n" \
"v_mfma_f32_16x16x32_bf16 v[68:71], v[140:143], a[152:155], v[68:71]\n" \
"ds_read_b64_tr_b16 v[178:179], v225 offset:1792\n" \
"v_mfma_f32_16x16x32_bf16 v[100:103], v[136:139], a[140:143], v[100:103]\n" \
"buffer_load_dwordx4 a[224:227], v227, %[srdz], s50 offen offset:2048 sc1\n" \
"v_mfma_f32_16x16x32_bf16 v[100:103], v[140:143], a[156:159], v[100:103]\n" \
"ds_read_b64_tr_b16 v[180:181], v225 offset:9728\n" \
"v_mfma_f32_16x16x32_bf16 v[8:11], v[144:147], a[128:131], v[8:11]\n" \
"ds_read_b64_tr_b16 v[182:183], v225 offset:9984\n" \
"v_mfma_f32_16x16x32_bf16 v[8:11], v[148:151], a[144:147], v[8:11]\n" \
"v_mfma_f32_16x16x32_bf16 v[40:43], v[144:147], a[132:135], v[40:43]\n" \
"ds_read_b64_tr_b16 v[184:185], v225 offset:1544\n" \
"v_mfma_f32_16x16x32_bf16 v[40:43], v[148:151], a[148:151], v[40:43]\n" \
"ds_read_b64_tr_b16 v[186:187], v225 offset:1800\n" \
"v_mfma_f32_16x16x32_bf16 v[72:75], v[144:147], a[136:139], v[72:75]\n" \
"buffer_load_dwordx4 a[228:231], v219, %[srdz], s50 offen offset:2048 sc1\n" \
"v_mfma_f32_16x16x32_bf16 v[72:75], v[148:151], a[152:155], v[72:75]\n" \
"ds_read_b64_tr_b16 v[188:189], v225 offset:9736\n" \
"v_mfma_f32_16x16x32_bf16 v[104:107], v[144:147], a[140:143], v[104:107]\n" \
"ds_read_b64_tr_b16 v[190:191], v225 offset:9992\n" \
"v_mfma_f32_16x16x32_bf16 v[104:107], v[148:151], a[156:159], v[104:107]\n" \
"v_mfma_f32_16x16x32_bf16 v[12:15], v[152:155], a[128:131], v[12:15]\n" \
"buffer_load_dwordx4 a[232:235], v220, %[srdz], s50 offen offset:2048 sc1\n" \
"v_mfma_f32_16x16x32_bf16 v[12:15], v[156:159], a[144:147], v[12:15]\n" \
"v_mfma_f32_16x16x32_bf16 v[44:47], v[152:155], a[132:135], v[44:47]\n" \
"v_mfma_f32_16x16x32_bf16 v[44:47], v[156:159], a[148:151], v[44:47]\n" \
"v_mfma_f32_16x16x32_bf16 v[76:79], v[152:155], a[136:139], v[76:79]\n" \
"buffer_load_dwordx4 a[236:239], v221, %[srdz], s50 offen offset:2048 sc1\n" \
"v_mfma_f32_16x16x32_bf16 v[76:79], v[156:159], a[152:155], v[76:79]\n" \
"v_mfma_f32_16x16x32_bf16 v[108:111], v[152:155], a[140:143], v[108:111]\n" \
"v_mfma_f32_16x16x32_bf16 v[108:111], v[156:159], a[156:159], v[108:111]\n" \
"s_waitcnt vmcnt(29) lgkmcnt(0)\n" \
"s_barrier\n" \
"v_mfma_f32_16x16x32_bf16 v[16:19], v[160:163], a[128:131], v[16:19]\n" \
"ds_read_b64_tr_b16 v[128:129], v225 offset:16384\n" \
"v_mfma_f32_16x16x32_bf16 v[16:19], v[164:167], a[144:147], v[16:19]\n" \
"ds_read_b64_tr_b16 v[130:131], v225 offset:16640\n" \
"v_mfma_f32_16x16x32_bf16 v[48:51], v[160:163], a[132:135], v[48:51]\n" \
"s_add_u32 s49, s49, s55\n" \
"s_mov_b32 m0, s44\n" \
"s_nop 0\n" \
"buffer_load_dwordx4 v226, %[srdw], s49 offen offset:0 lds\n" \
"v_mfma_f32_16x16x32_bf16 v[48:51], v[164:167], a[148:151], v[48:51]\n" \
"ds_read_b64_tr_b16 v[132:133], v225 offset:24576\n" \
"v_mfma_f32_16x16x32_bf16 v[80:83], v[160:163], a[136:139], v[80:83]\n" \
"ds_read_b64_tr_b16 v[134:135], v225 offset:24832\n" \
"v_mfma_f32_16x16x32_bf16 v[80:83], v[164:167], a[152:155], v[80:83]\n" \
"v_mfma_f32_16x16x32_bf16 v[112:115], v[160:163], a[140:143], v[112:115]\n" \
"ds_read_b64_tr_b16 v[136:137], v225 offset:16392\n" \
"v_mfma_f32_16x16x32_bf16 v[112:115], v[164:167], a[156:159], v[112:115]\n" \
"ds_read_b64_tr_b16 v[138:139], v225 offset:16648\n" \
"v_mfma_f32_16x16x32_bf16 v[20:23], v[168:171], a[128:131], v[20:23]\n" \
"buffer_load_dwordx4 v216, %[srdw], s49 offen offset:1024 lds\n" \
"v_mfma_f32_16x16x32_bf16 v[20:23], v[172:175], a[144:147], v[20:23]\n" \
"ds_read_b64_tr_b16 v[140:141], v225 offset:24584\n" \
"v_mfma_f32_16x16x32_bf16 v[52:55], v[168:171], a[132:135], v[52:55]\n" \
"ds_read_b64_tr_b16 v[142:143], v225 offset:24840\n" \
"v_mfma_f32_16x16x32_bf16 v[52:55], v[172:175], a[148:151], v[52:55]\n" \
"v_mfma_f32_16x16x32_bf16 v[84:87], v[168:171], a[136:139], v[84:87]\n" \
"ds_read_b64_tr_b16 v[144:145], v225 offset:16896\n" \
"v_mfma_f32_16x16x32_bf16 v[84:87], v[172:175], a[152:155], v[84:87]\n" \
"ds_read_b64_tr_b16 v[146:147], v225 offset:17152\n" \
"v_mfma_f32_16x16x32_bf16 v[116:119], v[168:171], a[140:143], v[116:119]\n" \
"buffer_load_dwordx4 a[240:243], v227, %[srdz], s50 offen offset:3072 sc1\n" \
"v_mfma_f32_16x16x32_bf16 v[116:119], v[172:175], a[156:159], v[116:119]\n" \
"ds_read_b64_tr_b16 v[148:149], v225 offset:25088\n" \
"v_mfma_f32_16x16x32_bf16 v[24:27], v[176:179], a[128:131], v[24:27]\n" \
"ds_read_b64_tr_b16 v[150:151], v225 offset:25344\n" \
"v_mfma_f32_16x16x32_bf16 v[24:27], v[180:183], a[144:147], v[24:27]\n" \
"v_mfma_f32_16x16x32_bf16 v[56:59], v[176:179], a[132:135], v[56:59]\n" \
"ds_read_b64_tr_b16 v[152:153], v225 offset:16904\n" \
"v_mfma_f32_16x16x32_bf16 v[56:59], v[180:183], a[148:151], v[56:59]\n" \
"ds_read_b64_tr_b16 v[154:155], v225 offset:17160\n" \
"v_mfma_f32_16x16x32_bf16 v[88:91], v[176:179], a[136:139], v[88:91]\n" \
"buffer_load_dwordx4 a[244:247], v219, %[srdz], s50 offen offset:3072 sc1\n" \
"v_mfma_f32_16x16x32_bf16 v[88:91], v[180:183], a[152:155], v[88:91]\n" \
"ds_read_b64_tr_b16 v[156:157], v225 offset:25096\n" \
"v_mfma_f32_16x16x32_bf16 v[120:123], v[176:179], a[140:143], v[120:123]\n" \
"ds_read_b64_tr_b16 v[158:159], v225 offset:25352\n" \
"v_mfma_f32_16x16x32_bf16 v[120:123], v[180:183], a[156:159], v[120:123]\n" \
"v_mfma_f32_16x16x32_bf16 v[28:31], v[184:187], a[128:131], v[28:31]\n" \
"buffer_load_dwordx4 a[248:251], v220, %[srdz], s50 offen offset:3072 sc1\n" \
"v_mfma_f32_16x16x32_bf16 v[28:31], v[188:191], a[144:147], v[28:31]\n" \
"v_mfma_f32_16x16x32_bf16 v[60:63], v[184:187], a[132:135], v[60:63]\n" \
"v_mfma_f32_16x16x32_bf16 v[60:63], v[188:191], a[148:151], v[60:63]\n" \
"v_mfma_f32_16x16x32_bf16 v[92:95], v[184:187], a[136:139], v[92:95]\n" \
"buffer_load_dwordx4 a[252:255], v221, %[srdz], s50 offen offset:3072 sc1\n" \
"v_mfma_f32_16x16x32_bf16 v[92:95], v[188:191], a[152:155], v[92:95]\n" \
"v_mfma_f32_16x16x32_bf16 v[124:127], v[184:187], a[140:143], v[124:127]\n" \
"v_mfma_f32_16x16x32_bf16 v[124:127], v[188:191], a[156:159], v[124:127]\n" \
"s_waitcnt vmcnt(20) lgkmcnt(0)\n" \
"v_mfma_f32_16x16x32_bf16 v[0:3], v[128:131], a[160:163], v[0:3]\n" \
"ds_read_b64_tr_b16 v[160:161], v225 offset:17408\n" \
"v_mfma_f32_16x16x32_bf16 v[0:3], v[132:135], a[176:179], v[0:3]\n" \
"ds_read_b64_tr_b16 v[162:163], v225 offset:17664\n" \
"v_mfma_f32_16x16x32_bf16 v[32:35], v[128:131], a[164:167], v[32:35]\n" \
"s_mov_b32 m0, s44\n" \
"s_nop 0\n" \
"buffer_load_dwordx4 v217, %[srdw], s49 offen offset:2048 lds\n" \
"v_mfma_f32_16x16x32_bf16 v[32:35], v[132:135], a[180:183], v[32:35]\n" \
"ds_read_b64_tr_b16 v[164:165], v225 offset:25600\n" \
"v_mfma_f32_16x16x32_bf16 v[64:67], v[128:131], a[168:171], v[64:67]\n" \
"ds_read_b64_tr_b16 v[166:167], v225 offset:25856\n" \
"v_mfma_f32_16x16x32_bf16 v[64:67], v[132:135], a[184:187], v[64:67]\n" \
"v_mfma_f32_16x16x32_bf16 v[96:99], v[128:131], a[172:175], v[96:99]\n" \
"ds_read_b64_tr_b16 v[168:169], v225 offset:17416\n" \
"v_mfma_f32_16x16x32_bf16 v[96:99], v[132:135], a[188:191], v[96:99]\n" \
"ds_read_b64_tr_b16 v[170:171], v225 offset:17672\n" \
"v_mfma_f32_16x16x32_bf16 v[4:7], v[136:139], a[160:163], v[4:7]\n" \
"buffer_load_dwordx4 v218, %[srdw], s49 offen offset:3072 lds\n" \
"v_mfma_f32_16x16x32_bf16 v[4:7], v[140:143], a[176:179], v[4:7]\n" \
"ds_read_b64_tr_b16 v[172:173], v225 offset:25608\n" \
"v_mfma_f32_16x16x32_bf16 v[36:39], v[136:139], a[164:167], v[36:39]\n" \
"ds_read_b64_tr_b16 v[174:175], v225 offset:25864\n" \
"v_mfma_f32_16x16x32_bf16 v[36:39], v[140:143], a[180:183], v[36:39]\n" \
"v_mfma_f32_16x16x32_bf16 v[68:71], v[136:139], a[168:171], v[68:71]\n" \
"ds_read_b64_tr_b16 v[176:177], v225 offset:17920\n" \
"v_mfma_f32_16x16x32_bf16 v[68:71], v[140:143], a[184:187], v[68:71]\n" \
"ds_read_b64_tr_b16 v[178:179], v225 offset:18176\n" \
"v_mfma_f32_16x16x32_bf16 v[100:103], v[136:139], a[172:175], v[100:103]\n" \
"s_add_u32 s50, s50, s53\n" \
"buffer_load_dwordx4 a[128:131], v227, %[srdz], s50 offen offset:0 sc1\n" \
"v_mfma_f32_16x16x32_bf16 v[100:103], v[140:143], a[188:191], v[100:103]\n" \
"ds_read_b64_tr_b16 v[180:181], v225 offset:26112\n" \
"v_mfma_f32_16x16x32_bf16 v[8:11], v[144:147], a[160:163], v[8:11]\n" \
"ds_read_b64_tr_b16 v[182:183], v225 offset:26368\n" \
"v_mfma_f32_16x16x32_bf16 v[8:11], v[148:151], a[176:179], v[8:11]\n" \
"v_mfma_f32_16x16x32_bf16 v[40:43], v[144:147], a[164:167], v[40:43]\n" \
"ds_read_b64_tr_b16 v[184:185], v225 offset:17928\n" \
"v_mfma_f32_16x16x32_bf16 v[40:43], v[148:151], a[180:183], v[40:43]\n" \
"ds_read_b64_tr_b16 v[186:187], v225 offset:18184\n" \
"v_mfma_f32_16x16x32_bf16 v[72:75], v[144:147], a[168:171], v[72:75]\n" \
"buffer_load_dwordx4 a[132:135], v219, %[srdz], s50 offen offset:0 sc1\n" \
"v_mfma_f32_16x16x32_bf16 v[72:75], v[148:151], a[184:187], v[72:75]\n" \
"ds_read_b64_tr_b16 v[188:189], v225 offset:26120\n" \
"v_mfma_f32_16x16x32_bf16 v[104:107], v[144:147], a[172:175], v[104:107]\n" \
"ds_read_b64_tr_b16 v[190:191], v225 offset:26376\n" \
"v_mfma_f32_16x16x32_bf16 v[104:107], v[148:151], a[188:191], v[104:107]\n" \
"v_mfma_f32_16x16x32_bf16 v[12:15], v[152:155], a[160:163], v[12:15]\n" \
"buffer_load_dwordx4 a[136:139], v220, %[srdz], s50 offen offset:0 sc1\n" \
"v_mfma_f32_16x16x32_bf16 v[12:15], v[156:159], a[176:179], v[12:15]\n" \
"v_mfma_f32_16x16x32_bf16 v[44:47], v[152:155], a[164:167], v[44:47]\n" \
"v_mfma_f32_16x16x32_bf16 v[44:47], v[156:159], a[180:183], v[44:47]\n" \
"v_mfma_f32_16x16x32_bf16 v[76:79], v[152:155], a[168:171], v[76:79]\n" \
"buffer_load_dwordx4 a[140:143], v221, %[srdz], s50 offen offset:0 sc1\n" \
"v_mfma_f32_16x16x32_bf16 v[76:79], v[156:159], a[184:187], v[76:79]\n" \
"v_mfma_f32_16x16x32_bf16 v[108:111], v[152:155], a[172:175], v[108:111]\n" \
"v_mfma_f32_16x16x32_bf16 v[108:111], v[156:159], a[188:191], v[108:111]\n" \
"s_waitcnt vmcnt(29) lgkmcnt(0)\n" \
"s_barrier\n" \
"v_mfma_f32_16x16x32_bf16 v[16:19], v[160:163], a[160:163], v[16:19]\n" \
"ds_read_b64_tr_b16 v[128:129], v225 offset:32768\n" \
"v_mfma_f32_16x16x32_bf16 v[16:19], v[164:167], a[176:179], v[16:19]\n" \
"ds_read_b64_tr_b16 v[130:131], v225 offset:33024\n" \
"v_mfma_f32_16x16x32_bf16 v[48:51], v[160:163], a[164:167], v[48:51]\n" \
"s_add_u32 s49, s49, s55\n" \
"s_mov_b32 m0, s45\n" \
"s_nop 0\n" \
"buffer_load_dwordx4 v226, %[srdw], s49 offen offset:0 lds\n" \
"v_mfma_f32_16x16x32_bf16 v[48:51], v[164:167], a[180:183], v[48:51]\n" \
"ds_read_b64_tr_b16 v[132:133], v225 offset:40960\n" \
"v_mfma_f32_16x16x32_bf16 v[80:83], v[160:163], a[168:171], v[80:83]\n" \
"ds_read_b64_tr_b16 v[134:135], v225 offset:41216\n" \
"v_mfma_f32_16x16x32_bf16 v[80:83], v[164:167], a[184:187], v[80:83]\n" \
"v_mfma_f32_16x16x32_bf16 v[112:115], v[160:163], a[172:175], v[112:115]\n" \
"ds_read_b64_tr_b16 v[136:137], v225 offset:32776\n" \
"v_mfma_f32_16x16x32_bf16 v[112:115], v[164:167], a[188:191], v[112:115]\n" \
"ds_read_b64_tr_b16 v[138:139], v225 offset:33032\n" \
"v_mfma_f32_16x16x32_bf16 v[20:23], v[168:171], a[160:163], v[20:23]\n" \
"buffer_load_dwordx4 v216, %[srdw], s49 offen offset:1024 lds\n" \
"v_mfma_f32_16x16x32_bf16 v[20:23], v[172:175], a[176:179], v[20:23]\n" \
"ds_read_b64_tr_b16 v[140:141], v225 offset:40968\n" \
"v_mfma_f32_16x16x32_bf16 v[52:55], v[168:171], a[164:167], v[52:55]\n" \
"ds_read_b64_tr_b16 v[142:143], v225 offset:41224\n" \
"v_mfma_f32_16x16x32_bf16 v[52:55], v[172:175], a[180:183], v[52:55]\n" \
"v_mfma_f32_16x16x32_bf16 v[84:87], v[168:171], a[168:171], v[84:87]\n" \
"ds_read_b64_tr_b16 v[144:145], v225 offset:33280\n" \
"v_mfma_f32_16x16x32_bf16 v[84:87], v[172:175], a[184:187], v[84:87]\n" \
"ds_read_b64_tr_b16 v[146:147], v225 offset:33536\n" \
"v_mfma_f32_16x16x32_bf16 v[116:119], v[168:171], a[172:175], v[116:119]\n" \
"buffer_load_dwordx4 a[144:147], v227, %[srdz], s50 offen offset:1024 sc1\n" \
"v_mfma_f32_16x16x32_bf16 v[116:119], v[172:175], a[188:191], v[116:119]\n" \
"ds_read_b64_tr_b16 v[148:149], v225 offset:41472\n" \
"v_mfma_f32_16x16x32_bf16 v[24:27], v[176:179], a[160:163], v[24:27]\n" \
"ds_read_b64_tr_b16 v[150:151], v225 offset:41728\n" \
"v_mfma_f32_16x16x32_bf16 v[24:27], v[180:183], a[176:179], v[24:27]\n" \
"v_mfma_f32_16x16x32_bf16 v[56:59], v[176:179], a[164:167], v[56:59]\n" \
"ds_read_b64_tr_b16 v[152:153], v225 offset:33288\n" \
"v_mfma_f32_16x16x32_bf16 v[56:59], v[180:183], a[180:183], v[56:59]\n" \
"ds_read_b64_tr_b16 v[154:155], v225 offset:33544\n" \
"v_mfma_f32_16x16x32_bf16 v[88:91], v[176:179], a[168:171], v[88:91]\n" \
"buffer_load_dwordx4 a[148:151], v219, %[srdz], s50 offen offset:1024 sc1\n" \
"v_mfma_f32_16x16x32_bf16 v[88:91], v[180:183], a[184:187], v[88:91]\n" \
"ds_read_b64_tr_b16 v[156:157], v225 offset:41480\n" \
"v_mfma_f32_16x16x32_bf16 v[120:123], v[176:179], a[172:175], v[120:123]\n" \
"ds_read_b64_tr_b16 v[158:159], v225 offset:41736\n" \
"v_mfma_f32_16x16x32_bf16 v[120:123], v[180:183], a[188:191], v[120:123]\n" \
"v_mfma_f32_16x16x32_bf16 v[28:31], v[184:187], a[160:163], v[28:31]\n" \
"buffer_load_dwordx4 a[152:155], v220, %[srdz], s50 offen offset:1024 sc1\n" \
"v_mfma_f32_16x16x32_bf16 v[28:31], v[188:191], a[176:179], v[28:31]\n" \
"v_mfma_f32_16x16x32_bf16 v[60:63], v[184:187], a[164:167], v[60:63]\n" \
"v_mfma_f32_16x16x32_bf16 v[60:63], v[188:191], a[180:183], v[60:63]\n" \
"v_mfma_f32_16x16x32_bf16 v[92:95], v[184:187], a[168:171], v[92:95]\n" \
"buffer_load_dwordx4 a[156:159], v221, %[srdz], s50 offen offset:1024 sc1\n" \
"v_mfma_f32_16x16x32_bf16 v[92:95], v[188:191], a[184:187], v[92:95]\n" \
"v_mfma_f32_16x16x32_bf16 v[124:127], v[184:187], a[172:175], v[124:127]\n" \
"v_mfma_f32_16x16x32_bf16 v[124:127], v[188:191], a[188:191], v[124:127]\n" \
"s_waitcnt vmcnt(24) lgkmcnt(0)\n" \
"v_mfma_f32_16x16x32_bf16 v[0:3], v[128:131], a[192:195], v[0:3]\n" \
"ds_read_b64_tr_b16 v[160:161], v225 offset:33792\n" \
"v_mfma_f32_16x16x32_bf16 v[0:3], v[132:135], a[208:211], v[0:3]\n" \
"ds_read_b64_tr_b16 v[162:163], v225 offset:34048\n" \
"v_mfma_f32_16x16x32_bf16 v[32:35], v[128:131], a[196:199], v[32:35]\n" \
"s_mov_b32 m0, s45\n" \
"s_nop 0\n" \
"buffer_load_dwordx4 v217, %[srdw], s49 offen offset:2048 lds\n" \
"v_mfma_f32_16x16x32_bf16 v[32:35], v[132:135], a[212:215], v[32:35]\n" \
"ds_read_b64_tr_b16 v[164:165], v225 offset:41984\n" \
"v_mfma_f32_16x16x32_bf16 v[64:67], v[128:131], a[200:203], v[64:67]\n" \
"ds_read_b64_tr_b16 v[166:167], v225 offset:42240\n" \
"v_mfma_f32_16x16x32_bf16 v[64:67], v[132:135], a[216:219], v[64:67]\n" \
"v_mfma_f32_16x16x32_bf16 v[96:99], v[128:131], a[204:207], v[96:99]\n" \
"ds_read_b64_tr_b16 v[168:169], v225 offset:33800\n" \
"v_mfma_f32_16x16x32_bf16 v[96:99], v[132:135], a[220:223], v[96:99]\n" \
"ds_read_b64_tr_b16 v[170:171], v225 offset:34056\n" \
"v_mfma_f32_16x16x32_bf16 v[4:7], v[136:139], a[192:195], v[4:7]\n" \
"buffer_load_dwordx4 v218, %[srdw], s49 offen offset:3072 lds\n" \
"v_mfma_f32_16x16x32_bf16 v[4:7], v[140:143], a[208:211], v[4:7]\n" \
"ds_read_b64_tr_b16 v[172:173], v225 offset:41992\n" \
"v_mfma_f32_16x16x32_bf16 v[36:39], v[136:139], a[196:199], v[36:39]\n" \
"ds_read_b64_tr_b16 v[174:175], v225 offset:42248\n" \
"v_mfma_f32_16x16x32_bf16 v[36:39], v[140:143], a[212:215], v[36:39]\n" \
"v_mfma_f32_16x16x32_bf16 v[68:71], v[136:139], a[200:203], v[68:71]\n" \
"ds_read_b64_tr_b16 v[176:177], v225 offset:34304\n" \
"v_mfma_f32_16x16x32_bf16 v[68:71], v[140:143], a[216:219], v[68:71]\n" \
"ds_read_b64_tr_b16 v[178:179], v225 offset:34560\n" \
"v_mfma_f32_16x16x32_bf16 v[100:103], v[136:139], a[204:207], v[100:103]\n" \
"buffer_load_dwordx4 a[160:163], v227, %[srdz], s50 offen offset:2048 sc1\n" \
"v_mfma_f32_16x16x32_bf16 v[100:103], v[140:143], a[220:223], v[100:103]\n" \
"ds_read_b64_tr_b16 v[180:181], v225 offset:42496\n" \
"v_mfma_f32_16x16x32_bf16 v[8:11], v[144:147], a[192:195], v[8:11]\n" \
"ds_read_b64_tr_b16 v[182:183], v225 offset:42752\n" \
"v_mfma_f32_16x16x32_bf16 v[8:11], v[148:151], a[208:211], v[8:11]\n" \
"v_mfma_f32_16x16x32_bf16 v[40:43], v[144:147], a[196:199], v[40:43]\n" \
"ds_read_b64_tr_b16 v[184:185], v225 offset:34312\n" \
"v_mfma_f32_16x16x32_bf16 v[40:43], v[148:151], a[212:215], v[40:43]\n" \
"ds_read_b64_tr_b16 v[186:187], v225 offset:34568\n" \
"v_mfma_f32_16x16x32_bf16 v[72:75], v[144:147], a[200:203], v[72:75]\n" \
"buffer_load_dwordx4 a[164:167], v219, %[srdz], s50 offen offset:2048 sc1\n" \
"v_mfma_f32_16x16x32_bf16 v[72:75], v[148:151], a[216:219], v[72:75]\n" \
"ds_read_b64_tr_b16 v[188:189], v225 offset:42504\n" \
"v_mfma_f32_16x16x32_bf16 v[104:107], v[144:147], a[204:207], v[104:107]\n" \
"ds_read_b64_tr_b16 v[190:191], v225 offset:42760\n" \
"v_mfma_f32_16x16x32_bf16 v[104:107], v[148:151], a[220:223], v[104:107]\n" \
"v_mfma_f32_16x16x32_bf16 v[12:15], v[152:155], a[192:195], v[12:15]\n" \
"buffer_load_dwordx4 a[168:171], v220, %[srdz], s50 offen offset:2048 sc1\n" \
"v_mfma_f32_16x16x32_bf16 v[12:15], v[156:159], a[208:211], v[12:15]\n" \
"v_mfma_f32_16x16x32_bf16 v[44:47], v[152:155], a[196:199], v[44:47]\n" \
"v_mfma_f32_16x16x32_bf16 v[44:47], v[156:159], a[212:215], v[44:47]\n" \
"v_mfma_f32_16x16x32_bf16 v[76:79], v[152:155], a[200:203], v[76:79]\n" \
"buffer_load_dwordx4 a[172:175], v221, %[srdz], s50 offen offset:2048 sc1\n" \
"v_mfma_f32_16x16x32_bf16 v[76:79], v[156:159], a[216:219], v[76:79]\n" \
"v_mfma_f32_16x16x32_bf16 v[108:111], v[152:155], a[204:207], v[108:111]\n" \
"v_mfma_f32_16x16x32_bf16 v[108:111], v[156:159], a[220:223], v[108:111]\n" \
"s_waitcnt vmcnt(28) lgkmcnt(0)\n" \
"s_barrier\n" \
"v_mfma_f32_16x16x32_bf16 v[16:19], v[160:163], a[192:195], v[16:19]\n" \
"ds_read_b64_tr_b16 v[128:129], v225 offset:49152\n" \
"v_mfma_f32_16x16x32_bf16 v[16:19], v[164:167], a[208:211], v[16:19]\n" \
"ds_read_b64_tr_b16 v[130:131], v225 offset:49408\n" \
"v_mfma_f32_16x16x32_bf16 v[48:51], v[160:163], a[196:199], v[48:51]\n" \
"s_add_u32 s49, s49, s55\n" \
"s_mov_b32 m0, s46\n" \
"s_nop 0\n" \
"buffer_load_dwordx4 v226, %[srdw], s49 offen offset:0 lds\n" \
"v_mfma_f32_16x16x32_bf16 v[48:51], v[164:167], a[212:215], v[48:51]\n" \
"ds_read_b64_tr_b16 v[132:133], v225 offset:57344\n" \
"v_mfma_f32_16x16x32_bf16 v[80:83], v[160:163], a[200:203], v[80:83]\n" \
"ds_read_b64_tr_b16 v[134:135], v225 offset:57600\n" \
"v_mfma_f32_16x16x32_bf16 v[80:83], v[164:167], a[216:219], v[80:83]\n" \
"v_mfma_f32_16x16x32_bf16 v[112:115], v[160:163], a[204:207], v[112:115]\n" \
"ds_read_b64_tr_b16 v[136:137], v225 offset:49160\n" \
"v_mfma_f32_16x16x32_bf16 v[112:115], v[164:167], a[220:223], v[112:115]\n" \
"ds_read_b64_tr_b16 v[138:139], v225 offset:49416\n" \
"v_mfma_f32_16x16x32_bf16 v[20:23], v[168:171], a[192:195], v[20:23]\n" \
"buffer_load_dwordx4 v216, %[srdw], s49 offen offset:1024 lds\n" \
"v_mfma_f32_16x16x32_bf16 v[20:23], v[172:175], a[208:211], v[20:23]\n" \
"ds_read_b64_tr_b16 v[140:141], v225 offset:57352\n" \
"v_mfma_f32_16x16x32_bf16 v[52:55], v[168:171], a[196:199], v[52:55]\n" \
"ds_read_b64_tr_b16 v[142:143], v225 offset:57608\n" \
"v_mfma_f32_16x16x32_bf16 v[52:55], v[172:175], a[212:215], v[52:55]\n" \
"v_mfma_f32_16x16x32_bf16 v[84:87], v[168:171], a[200:203], v[84:87]\n" \
"ds_read_b64_tr_b16 v[144:145], v225 offset:49664\n" \
"v_mfma_f32_16x16x32_bf16 v[84:87], v[172:175], a[216:219], v[84:87]\n" \
"ds_read_b64_tr_b16 v[146:147], v225 offset:49920\n" \
"v_mfma_f32_16x16x32_bf16 v[116:119], v[168:171], a[204:207], v[116:119]\n" \
"buffer_load_dwordx4 a[176:179], v227, %[srdz], s50 offen offset:3072 sc1\n" \
"v_mfma_f32_16x16x32_bf16 v[116:119], v[172:175], a[220:223], v[116:119]\n" \
"ds_read_b64_tr_b16 v[148:149], v225 offset:57856\n" \
"v_mfma_f32_16x16x32_bf16 v[24:27], v[176:179], a[192:195], v[24:27]\n" \
"ds_read_b64_tr_b16 v[150:151], v225 offset:58112\n" \
"v_mfma_f32_16x16x32_bf16 v[24:27], v[180:183], a[208:211], v[24:27]\n" \
"v_mfma_f32_16x16x32_bf16 v[56:59], v[176:179], a[196:199], v[56:59]\n" \
"ds_read_b64_tr_b16 v[152:153], v225 offset:49672\n" \
"v_mfma_f32_16x16x32_bf16 v[56:59], v[180:183], a[212:215], v[56:59]\n" \
"ds_read_b64_tr_b16 v[154:155], v225 offset:49928\n" \
"v_mfma_f32_16x16x32_bf16 v[88:91], v[176:179], a[200:203], v[88:91]\n" \
"buffer_load_dwordx4 a[180:183], v219, %[srdz], s50 offen offset:3072 sc1\n" \
"v_mfma_f32_16x16x32_bf16 v[88:91], v[180:183], a[216:219], v[88:91]\n" \
"ds_read_b64_tr_b16 v[156:157], v225 offset:57864\n" \
"v_mfma_f32_16x16x32_bf16 v[120:123], v[176:179], a[204:207], v[120:123]\n" \
"ds_read_b64_tr_b16 v[158:159], v225 offset:58120\n" \
"v_mfma_f32_16x16x32_bf16 v[120:123], v[180:183], a[220:223], v[120:123]\n" \
"v_mfma_f32_16x16x32_bf16 v[28:31], v[184:187], a[192:195], v[28:31]\n" \
"buffer_load_dwordx4 a[184:187], v220, %[srdz], s50 offen offset:3072 sc1\n" \
"v_mfma_f32_16x16x32_bf16 v[28:31], v[188:191], a[208:211], v[28:31]\n" \
"v_mfma_f32_16x16x32_bf16 v[60:63], v[184:187], a[196:199], v[60:63]\n" \
"v_mfma_f32_16x16x32_bf16 v[60:63], v[188:191], a[212:215], v[60:63]\n" \
"v_mfma_f32_16x16x32_bf16 v[92:95], v[184:187], a[200:203], v[92:95]\n" \
"buffer_load_dwordx4 a[188:191], v221, %[srdz], s50 offen offset:3072 sc1\n" \
"v_mfma_f32_16x16x32_bf16 v[92:95], v[188:191], a[216:219], v[92:95]\n" \
"v_mfma_f32_16x16x32_bf16 v[124:127], v[184:187], a[204:207], v[124:127]\n" \
"v_mfma_f32_16x16x32_bf16 v[124:127], v[188:191], a[220:223], v[124:127]\n" \
"s_waitcnt vmcnt(24) lgkmcnt(0)\n" \
"v_mfma_f32_16x16x32_bf16 v[0:3], v[128:131], a[224:227], v[0:3]\n" \
"ds_read_b64_tr_b16 v[160:161], v225 offset:50176\n" \
"v_mfma_f32_16x16x32_bf16 v[0:3], v[132:135], a[240:243], v[0:3]\n" \
"ds_read_b64_tr_b16 v[162:163], v225 offset:50432\n" \
"v_mfma_f32_16x16x32_bf16 v[32:35], v[128:131], a[228:231], v[32:35]\n" \
"s_mov_b32 m0, s46\n" \
"s_nop 0\n" \
"buffer_load_dwordx4 v217, %[srdw], s49 offen offset:2048 lds\n" \
"v_mfma_f32_16x16x32_bf16 v[32:35], v[132:135], a[244:247], v[32:35]\n" \
"ds_read_b64_tr_b16 v[164:165], v225 offset:58368\n" \
"v_mfma_f32_16x16x32_bf16 v[64:67], v[128:131], a[232:235], v[64:67]\n" \
"ds_read_b64_tr_b16 v[166:167], v225 offset:58624\n" \
"v_mfma_f32_16x16x32_bf16 v[64:67], v[132:135], a[248:251], v[64:67]\n" \
"v_mfma_f32_16x16x32_bf16 v[96:99], v[128:131], a[236:239], v[96:99]\n" \
"ds_read_b64_tr_b16 v[168:169], v225 offset:50184\n" \
"v_mfma_f32_16x16x32_bf16 v[96:99], v[132:135], a[252:255], v[96:99]\n" \
"ds_read_b64_tr_b16 v[170:171], v225 offset:50440\n" \
"v_mfma_f32_16x16x32_bf16 v[4:7], v[136:139], a[224:227], v[4:7]\n" \
"buffer_load_dwordx4 v218, %[srdw], s49 offen offset:3072 lds\n" \
"v_mfma_f32_16x16x32_bf16 v[4:7], v[140:143], a[240:243], v[4:7]\n" \
"ds_read_b64_tr_b16 v[172:173], v225 offset:58376\n" \
"v_mfma_f32_16x16x32_bf16 v[36:39], v[136:139], a[228:231], v[36:39]\n" \
"ds_read_b64_tr_b16 v[174:175], v225 offset:58632\n" \
"v_mfma_f32_16x16x32_bf16 v[36:39], v[140:143], a[244:247], v[36:39]\n" \
"v_mfma_f32_16x16x32_bf16 v[68:71], v[136:139], a[232:235], v[68:71]\n" \
"ds_read_b64_tr_b16 v[176:177], v225 offset:50688\n" \
"v_mfma_f32_16x16x32_bf16 v[68:71], v[140:143], a[248:251], v[68:71]\n" \
"ds_read_b64_tr_b16 v[178:179], v225 offset:50944\n" \
"v_mfma_f32_16x16x32_bf16 v[100:103], v[136:139], a[236:239], v[100:103]\n" \
"s_add_u32 s50, s50, s53\n" \
"buffer_load_dwordx4 a[192:195], v227, %[srdz], s50 offen offset:0 sc1\n" \
"v_mfma_f32_16x16x32_bf16 v[100:103], v[140:143], a[252:255], v[100:103]\n" \
"ds_read_b64_tr_b16 v[180:181], v225 offset:58880\n" \
"v_mfma_f32_16x16x32_bf16 v[8:11], v[144:147], a[224:227], v[8:11]\n" \
"ds_read_b64_tr_b16 v[182:183], v225 offset:59136\n" \
"v_mfma_f32_16x16x32_bf16 v[8:11], v[148:151], a[240:243], v[8:11]\n" \
"v_mfma_f32_16x16x32_bf16 v[40:43], v[144:147], a[228:231], v[40:43]\n" \
"ds_read_b64_tr_b16 v[184:185], v225 offset:50696\n" \
"v_mfma_f32_16x16x32_bf16 v[40:43], v[148:151], a[244:247], v[40:43]\n" \
"ds_read_b64_tr_b16 v[186:187], v225 offset:50952\n" \
"v_mfma_f32_16x16x32_bf16 v[72:75], v[144:147], a[232:235], v[72:75]\n" \
"buffer_load_dwordx4 a[196:199], v219, %[srdz], s50 offen offset:0 sc1\n" \
"v_mfma_f32_16x16x32_bf16 v[72:75], v[148:151], a[248:251], v[72:75]\n" \
"ds_read_b64_tr_b16 v[188:189], v225 offset:58888\n" \
"v_mfma_f32_16x16x32_bf16 v[104:107], v[144:147], a[236:239], v[104:107]\n" \
"ds_read_b64_tr_b16 v[190:191], v225 offset:59144\n" \
"v_mfma_f32_16x16x32_bf16 v[104:107], v[148:151], a[252:255], v[104:107]\n" \
"v_mfma_f32_16x16x32_bf16 v[12:15], v[152:155], a[224:227], v[12:15]\n" \
"buffer_load_dwordx4 a[200:203], v220, %[srdz], s50 offen offset:0 sc1\n" \
"v_mfma_f32_16x16x32_bf16 v[12:15], v[156:159], a[240:243], v[12:15]\n" \
"v_mfma_f32_16x16x32_bf16 v[44:47], v[152:155], a[228:231], v[44:47]\n" \
"buffer_load_dword v237, v236, %[srdw], s60 offen\n" \
"s_add_u32 s60, s60, 0x200000\n" \
"v_mfma_f32_16x16x32_bf16 v[44:47], v[156:159], a[244:247], v[44:47]\n" \
".if . - adaln_body != 4096\n" \
".error \"size model\"\n" \
".endif\n" \
"Lk0_%=:\n" \
"s_cbranch_execz Lk1_%=\n" \
"s_nop 0\n" \
"v_mfma_f32_16x16x32_bf16 v[76:79], v[152:155], a[232:235], v[76:79]\n" \
"buffer_load_dwordx4 a[204:207], v221, %[srdz], s50 offen offset:0 sc1\n" \
"v_mfma_f32_16x16x32_bf16 v[76:79], v[156:159], a[248:251], v[76:79]\n" \
"v_mfma_f32_16x16x32_bf16 v[108:111], v[152:155], a[236:239], v[108:111]\n" \
"v_mfma_f32_16x16x32_bf16 v[108:111], v[156:159], a[252:255], v[108:111]\n" \
"s_waitcnt vmcnt(29) lgkmcnt(0)\n" \
"s_barrier\n" \
"v_mfma_f32_16x16x32_bf16 v[16:19], v[160:163], a[224:227], v[16:19]\n" \
"ds_read_b64_tr_b16 v[128:129], v225 offset:0\n" \
"v_mfma_f32_16x16x32_bf16 v[16:19], v[164:167], a[240:243], v[16:19]\n" \
"ds_read_b64_tr_b16 v[130:131], v225 offset:256\n" \
"v_mfma_f32_16x16x32_bf16 v[48:51], v[160:163], a[228:231], v[48:51]\n" \
"s_add_u32 s49, s49, s55\n" \
"s_mov_b32 m0, s47\n" \
"s_nop 0\n" \
"buffer_load_dwordx4 v226, %[srdw], s49 offen offset:0 lds\n" \
"v_mfma_f32_16x16x32_bf16 v[48:51], v[164:167], a[244:247], v[48:51]\n" \
"ds_read_b64_tr_b16 v[132:133], v225 offset:8192\n" \
"v_mfma_f32_16x16x32_bf16 v[80:83], v[160:163], a[232:235], v[80:83]\n" \
"ds_read_b64_tr_b16 v[134:135], v225 offset:8448\n" \
"v_mfma_f32_16x16x32_bf16 v[80:83], v[164:167], a[248:251], v[80:83]\n" \
"v_mfma_f32_16x16x32_bf16 v[112:115], v[160:163], a[236:239], v[112:115]\n" \
"ds_read_b64_tr_b16 v[136:137], v225 offset:8\n" \
"v_mfma_f32_16x16x32_bf16 v[112:115], v[164:167], a[252:255], v[112:115]\n" \
"ds_read_b64_tr_b16 v[138:139], v225 offset:264\n" \
"v_mfma_f32_16x16x32_bf16 v[20:23], v[168:171], a[224:227], v[20:23]\n" \
"buffer_load_dwordx4 v216, %[srdw], s49 offen offset:1024 lds\n" \
"v_mfma_f32_16x16x32_bf16 v[20:23], v[172:175], a[240:243], v[20:23]\n" \
"ds_read_b64_tr_b16 v[140:141], v225 offset:8200\n" \
"v_mfma_f32_16x16x32_bf16 v[52:55], v[168:171], a[228:231], v[52:55]\n" \
"ds_read_b64_tr_b16 v[142:143], v225 offset:8456\n" \
"v_mfma_f32_16x16x32_bf16 v[52:55], v[172:175], a[244:247], v[52:55]\n" \
"v_mfma_f32_16x16x32_bf16 v[84:87], v[168:171], a[232:235], v[84:87]\n" \
"ds_read_b64_tr_b16 v[144:145], v225 offset:512\n" \
"v_mfma_f32_16x16x32_bf16 v[84:87], v[172:175], a[248:251], v[84:87]\n" \
"ds_read_b64_tr_b16 v[146:147], v225 offset:768\n" \
"v_mfma_f32_16x16x32_bf16 v[116:119], v[168:171], a[236:239], v[116:119]\n" \
"buffer_load_dwordx4 a[208:211], v227, %[srdz], s50 offen offset:1024 sc1\n" \
"v_mfma_f32_16x16x32_bf16 v[116:119], v[172:175], a[252:255], v[116:119]\n" \
"ds_read_b64_tr_b16 v[148:149], v225 offset:8704\n" \
"v_mfma_f32_16x16x32_bf16 v[24:27], v[176:179], a[224:227], v[24:27]\n" \
"ds_read_b64_tr_b16 v[150:151], v225 offset:8960\n" \
"v_mfma_f32_16x16x32_bf16 v[24:27], v[180:183], a[240:243], v[24:27]\n" \
"v_mfma_f32_16x16x32_bf16 v[56:59], v[176:179], a[228:231], v[56:59]\n" \
"ds_read_b64_tr_b16 v[152:153], v225 offset:520\n" \
"v_mfma_f32_16x16x32_bf16 v[56:59], v[180:183], a[244:247], v[56:59]\n" \
"ds_read_b64_tr_b16 v[154:155], v225 offset:776\n" \
"v_mfma_f32_16x16x32_bf16 v[88:91], v[176:179], a[232:235], v[88:91]\n" \
"buffer_load_dwordx4 a[212:215], v219, %[srdz], s50 offen offset:1024 sc1\n" \
"v_mfma_f32_16x16x32_bf16 v[88:91], v[180:183], a[248:251], v[88:91]\n" \
"ds_read_b64_tr_b16 v[156:157], v225 offset:8712\n" \
"v_mfma_f32_16x16x32_bf16 v[120:123], v[176:179], a[236:239], v[120:123]\n" \
"ds_read_b64_tr_b16 v[158:159], v225 offset:8968\n" \
"v_mfma_f32_16x16x32_bf16 v[120:123], v[180:183], a[252:255], v[120:123]\n" \
"v_mfma_f32_16x16x32_bf16 v[28:31], v[184:187], a[224:227], v[28:31]\n" \
"buffer_load_dwordx4 a[216:219], v220, %[srdz], s50 offen offset:1024 sc1\n" \
"v_mfma_f32_16x16x32_bf16 v[28:31], v[188:191], a[240:243], v[28:31]\n" \
"v_mfma_f32_16x16x32_bf16 v[60:63], v[184:187], a[228:231], v[60:63]\n" \
"v_mfma_f32_16x16x32_bf16 v[60:63], v[188:191], a[244:247], v[60:63]\n" \
"v_mfma_f32_16x16x32_bf16 v[92:95], v[184:187], a[232:235], v[92:95]\n" \
"buffer_load_dwordx4 a[220:223], v221, %[srdz], s50 offen offset:1024 sc1\n" \
"v_mfma_f32_16x16x32_bf16 v[92:95], v[188:191], a[248:251], v[92:95]\n" \
"v_mfma_f32_16x16x32_bf16 v[124:127], v[184:187], a[236:239], v[124:127]\n" \
"v_mfma_f32_16x16x32_bf16 v[124:127], v[188:191], a[252:255], v[124:127]\n" \
"s_waitcnt vmcnt(25) lgkmcnt(0)\n" \
"s_sub_u32 s51, s51, 1\n" \
"s_cmp_lg_u32 s51, 0\n" \
"s_cbranch_scc1 Lq_%=\n" \
"s_nop 0\n" \
"v_mfma_f32_16x16x32_bf16 v[0:3], v[128:131], a[128:131], v[0:3]\n" \
"ds_read_b64_tr_b16 v[160:161], v225 offset:1024\n" \
"v_mfma_f32_16x16x32_bf16 v[0:3], v[132:135], a[144:147], v[0:3]\n" \
"ds_read_b64_tr_b16 v[162:163], v225 offset:1280\n" \
"v_mfma_f32_16x16x32_bf16 v[32:35], v[128:131], a[132:135], v[32:35]\n" \
"s_mov_b32 m0, s47\n" \
"s_nop 0\n" \
"buffer_load_dwordx4 v217, %[srdw], s49 offen offset:2048 lds\n" \
"v_mfma_f32_16x16x32_bf16 v[32:35], v[132:135], a[148:151], v[32:35]\n" \
"ds_read_b64_tr_b16 v[164:165], v225 offset:9216\n" \
"v_mfma_f32_16x16x32_bf16 v[64:67], v[128:131], a[136:139], v[64:67]\n" \
"ds_read_b64_tr_b16 v[166:167], v225 offset:9472\n" \
"v_mfma_f32_16x16x32_bf16 v[64:67], v[132:135], a[152:155], v[64:67]\n" \
"v_mfma_f32_16x16x32_bf16 v[96:99], v[128:131], a[140:143], v[96:99]\n" \
"ds_read_b64_tr_b16 v[168:169], v225 offset:1032\n" \
"v_mfma_f32_16x16x32_bf16 v[96:99], v[132:135], a[156:159], v[96:99]\n" \
"ds_read_b64_tr_b16 v[170:171], v225 offset:1288\n" \
"v_mfma_f32_16x16x32_bf16 v[4:7], v[136:139], a[128:131], v[4:7]\n" \
"buffer_load_dwordx4 v218, %[srdw], s49 offen offset:3072 lds\n" \
"v_mfma_f32_16x16x32_bf16 v[4:7], v[140:143], a[144:147], v[4:7]\n" \
"ds_read_b64_tr_b16 v[172:173], v225 offset:9224\n" \
"v_mfma_f32_16x16x32_bf16 v[36:39], v[136:139], a[132:135], v[36:39]\n" \
"ds_read_b64_tr_b16 v[174:175], v225 offset:9480\n" \
"v_mfma_f32_16x16x32_bf16 v[36:39], v[140:143], a[148:151], v[36:39]\n" \
"v_mfma_f32_16x16x32_bf16 v[68:71], v[136:139], a[136:139], v[68:71]\n" \
"ds_read_b64_tr_b16 v[176:177], v225 offset:1536\n" \
"v_mfma_f32_16x16x32_bf16 v[68:71], v[140:143], a[152:155], v[68:71]\n" \
"ds_read_b64_tr_b16 v[178:179], v225 offset:1792\n" \
"v_mfma_f32_16x16x32_bf16 v[100:103], v[136:139], a[140:143], v[100:103]\n" \
"buffer_load_dwordx4 a[224:227], v227, %[srdz], s50 offen offset:2048 sc1\n" \
"v_mfma_f32_16x16x32_bf16 v[100:103], v[140:143], a[156:159], v[100:103]\n" \
"ds_read_b64_tr_b16 v[180:181], v225 offset:9728\n" \
"v_mfma_f32_16x16x32_bf16 v[8:11], v[144:147], a[128:131], v[8:11]\n" \
"ds_read_b64_tr_b16 v[182:183], v225 offset:9984\n" \
"v_mfma_f32_16x16x32_bf16 v[8:11], v[148:151], a[144:147], v[8:11]\n" \
"buffer_load_dwordx4 v[230:233], off, %[srdz], 0 offset:128 sc1\n" \
"v_mfma_f32_16x16x32_bf16 v[40:43], v[144:147], a[132:135], v[40:43]\n" \
"ds_read_b64_tr_b16 v[184:185], v225 offset:1544\n" \
"v_mfma_f32_16x16x32_bf16 v[40:43], v[148:151], a[148:151], v[40:43]\n" \
"ds_read_b64_tr_b16 v[186:187], v225 offset:1800\n" \
"v_mfma_f32_16x16x32_bf16 v[72:75], v[144:147], a[136:139], v[72:75]\n" \
"buffer_load_dwordx4 a[228:231], v219, %[srdz], s50 offen offset:2048 sc1\n" \
"v_mfma_f32_16x16x32_bf16 v[72:75], v[148:151], a[152:155], v[72:75]\n" \
"ds_read_b64_tr_b16 v[188:189], v225 offset:9736\n" \
"v_mfma_f32_16x16x32_bf16 v[104:107], v[144:147], a[140:143], v[104:107]\n" \
"ds_read_b64_tr_b16 v[190:191], v225 offset:9992\n" \
"v_mfma_f32_16x16x32_bf16 v[104:107], v[148:151], a[156:159], v[104:107]\n" \
"v_mfma_f32_16x16x32_bf16 v[12:15], v[152:155], a[128:131], v[12:15]\n" \
"buffer_load_dwordx4 a[232:235], v220, %[srdz], s50 offen offset:2048 sc1\n" \
"v_mfma_f32_16x16x32_bf16 v[12:15], v[156:159], a[144:147], v[12:15]\n" \
"v_mfma_f32_16x16x32_bf16 v[44:47], v[152:155], a[132:135], v[44:47]\n" \
"v_mfma_f32_16x16x32_bf16 v[44:47], v[156:159], a[148:151], v[44:47]\n" \
"v_mfma_f32_16x16x32_bf16 v[76:79], v[152:155], a[136:139], v[76:79]\n" \
"buffer_load_dwordx4 a[236:239], v221, %[srdz], s50 offen offset:2048 sc1\n" \
"v_mfma_f32_16x16x32_bf16 v[76:79], v[156:159], a[152:155], v[76:79]\n" \
"v_mfma_f32_16x16x32_bf16 v[108:111], v[152:155], a[140:143], v[108:111]\n" \
"v_mfma_f32_16x16x32_bf16 v[108:111], v[156:159], a[156:159], v[108:111]\n" \
"s_waitcnt vmcnt(30) lgkmcnt(0)\n" \
"s_barrier\n" \
"v_mfma_f32_16x16x32_bf16 v[16:19], v[160:163], a[128:131], v[16:19]\n" \
"ds_read_b64_tr_b16 v[128:129], v225 offset:16384\n" \
"v_mfma_f32_16x16x32_bf16 v[16:19], v[164:167], a[144:147], v[16:19]\n" \
"ds_read_b64_tr_b16 v[130:131], v225 offset:16640\n" \
"v_mfma_f32_16x16x32_bf16 v[48:51], v[160:163], a[132:135], v[48:51]\n" \
"s_mov_b32 s48, 16384\n" \
"s_mov_b32 m0, s44\n" \
"s_nop 0\n" \
"buffer_load_dwordx4 %[vd0], %[srdv], s48 offen offset:0 lds\n" \
"v_mfma_f32_16x16x32_bf16 v[48:51], v[164:167], a[148:151], v[48:51]\n" \
"ds_read_b64_tr_b16 v[132:133], v225 offset:24576\n" \
"v_mfma_f32_16x16x32_bf16 v[80:83], v[160:163], a[136:139], v[80:83]\n" \
"ds_read_b64_tr_b16 v[134:135], v225 offset:24832\n" \
"v_mfma_f32_16x16x32_bf16 v[80:83], v[164:167], a[152:155], v[80:83]\n" \
"v_mfma_f32_16x16x32_bf16 v[112:115], v[160:163], a[140:143], v[112:115]\n" \
"ds_read_b64_tr_b16 v[136:137], v225 offset:16392\n" \
"v_mfma_f32_16x16x32_bf16 v[112:115], v[164:167], a[156:159], v[112:115]\n" \
"ds_read_b64_tr_b16 v[138:139], v225 offset:16648\n" \
"v_mfma_f32_16x16x32_bf16 v[20:23], v[168:171], a[128:131], v[20:23]\n" \
"buffer_load_dwordx4 v215, %[srdv], s48 offen offset:1024 lds\n" \
"v_mfma_f32_16x16x32_bf16 v[20:23], v[172:175], a[144:147], v[20:23]\n" \
"ds_read_b64_tr_b16 v[140:141], v225 offset:24584\n" \
"v_mfma_f32_16x16x32_bf16 v[52:55], v[168:171], a[132:135], v[52:55]\n" \
"ds_read_b64_tr_b16 v[142:143], v225 offset:24840\n" \
"v_mfma_f32_16x16x32_bf16 v[52:55], v[172:175], a[148:151], v[52:55]\n" \
"v_mfma_f32_16x16x32_bf16 v[84:87], v[168:171], a[136:139], v[84:87]\n" \
"ds_read_b64_tr_b16 v[144:145], v225 offset:16896\n" \
"v_mfma_f32_16x16x32_bf16 v[84:87], v[172:175], a[152:155], v[84:87]\n" \
"ds_read_b64_tr_b16 v[146:147], v225 offset:17152\n" \
"v_mfma_f32_16x16x32_bf16 v[116:119], v[168:171], a[140:143], v[116:119]\n" \
"buffer_load_dwordx4 a[240:243], v227, %[srdz], s50 offen offset:3072 sc1\n" \
"v_mfma_f32_16x16x32_bf16 v[116:119], v[172:175], a[156:159], v[116:119]\n" \
"ds_read_b64_tr_b16 v[148:149], v225 offset:25088\n" \
"v_mfma_f32_16x16x32_bf16 v[24:27], v[176:179], a[128:131], v[24:27]\n" \
"ds_read_b64_tr_b16 v[150:151], v225 offset:25344\n" \
"v_mfma_f32_16x16x32_bf16 v[24:27], v[180:183], a[144:147], v[24:27]\n" \
"v_mfma_f32_16x16x32_bf16 v[56:59], v[176:179], a[132:135], v[56:59]\n" \
"ds_read_b64_tr_b16 v[152:153], v225 offset:16904\n" \
"v_mfma_f32_16x16x32_bf16 v[56:59], v[180:183], a[148:151], v[56:59]\n" \
"ds_read_b64_tr_b16 v[154:155], v225 offset:17160\n" \
"v_mfma_f32_16x16x32_bf16 v[88:91], v[176:179], a[136:139], v[88:91]\n" \
"buffer_load_dwordx4 a[244:247], v219, %[srdz], s50 offen offset:3072 sc1\n" \
"v_mfma_f32_16x16x32_bf16 v[88:91], v[180:183], a[152:155], v[88:91]\n" \
"ds_read_b64_tr_b16 v[156:157], v225 offset:25096\n" \
"v_mfma_f32_16x16x32_bf16 v[120:123], v[176:179], a[140:143], v[120:123]\n" \
"ds_read_b64_tr_b16 v[158:159], v225 offset:25352\n" \
"v_mfma_f32_16x16x32_bf16 v[120:123], v[180:183], a[156:159], v[120:123]\n" \
"v_mfma_f32_16x16x32_bf16 v[28:31], v[184:187], a[128:131], v[28:31]\n" \
"buffer_load_dwordx4 a[248:251], v220, %[srdz], s50 offen offset:3072 sc1\n" \
"v_mfma_f32_16x16x32_bf16 v[28:31], v[188:191], a[144:147], v[28:31]\n" \
"v_mfma_f32_16x16x32_bf16 v[60:63], v[184:187], a[132:135], v[60:63]\n" \
"v_mfma_f32_16x16x32_bf16 v[60:63], v[188:191], a[148:151], v[60:63]\n" \
"v_mfma_f32_16x16x32_bf16 v[92:95], v[184:187], a[136:139], v[92:95]\n" \
"buffer_load_dwordx4 a[252:255], v221, %[srdz], s50 offen offset:3072 sc1\n" \
"v_mfma_f32_16x16x32_bf16 v[92:95], v[188:191], a[152:155], v[92:95]\n" \
"v_mfma_f32_16x16x32_bf16 v[124:127], v[184:187], a[140:143], v[124:127]\n" \
"v_mfma_f32_16x16x32_bf16 v[124:127], v[188:191], a[156:159], v[124:127]\n" \
"s_waitcnt vmcnt(26) lgkmcnt(0)\n" \
"v_mfma_f32_16x16x32_bf16 v[0:3], v[128:131], a[160:163], v[0:3]\n" \
"ds_read_b64_tr_b16 v[160:161], v225 offset:17408\n" \
"v_mfma_f32_16x16x32_bf16 v[0:3], v[132:135], a[176:179], v[0:3]\n" \
"ds_read_b64_tr_b16 v[162:163], v225 offset:17664\n" \
"v_mfma_f32_16x16x32_bf16 v[32:35], v[128:131], a[164:167], v[32:35]\n" \
"s_mov_b32 s48, 16384\n" \
"s_mov_b32 m0, s44\n" \
"s_nop 0\n" \
"buffer_load_dwordx4 %[vd0], %[srdv], s48 offen offset:2048 lds\n" \
"v_mfma_f32_16x16x32_bf16 v[32:35], v[132:135], a[180:183], v[32:35]\n" \
"ds_read_b64_tr_b16 v[164:165], v225 offset:25600\n" \
"v_mfma_f32_16x16x32_bf16 v[64:67], v[128:131], a[168:171], v[64:67]\n" \
"ds_read_b64_tr_b16 v[166:167], v225 offset:25856\n" \
"v_mfma_f32_16x16x32_bf16 v[64:67], v[132:135], a[184:187], v[64:67]\n" \
"v_mfma_f32_16x16x32_bf16 v[96:99], v[128:131], a[172:175], v[96:99]\n" \
"ds_read_b64_tr_b16 v[168:169], v225 offset:17416\n" \
"v_mfma_f32_16x16x32_bf16 v[96:99], v[132:135], a[188:191], v[96:99]\n" \
"ds_read_b64_tr_b16 v[170:171], v225 offset:17672\n" \
"v_mfma_f32_16x16x32_bf16 v[4:7], v[136:139], a[160:163], v[4:7]\n" \
"buffer_load_dwordx4 v215, %[srdv], s48 offen offset:3072 lds\n" \
"v_mfma_f32_16x16x32_bf16 v[4:7], v[140:143], a[176:179], v[4:7]\n" \
"ds_read_b64_tr_b16 v[172:173], v225 offset:25608\n" \
"v_mfma_f32_16x16x32_bf16 v[36:39], v[136:139], a[164:167], v[36:39]\n" \
"ds_read_b64_tr_b16 v[174:175], v225 offset:25864\n" \
"v_mfma_f32_16x16x32_bf16 v[36:39], v[140:143], a[180:183], v[36:39]\n" \
"v_mfma_f32_16x16x32_bf16 v[68:71], v[136:139], a[168:171], v[68:71]\n" \
"ds_read_b64_tr_b16 v[176:177], v225 offset:17920\n" \
"v_mfma_f32_16x16x32_bf16 v[68:71], v[140:143], a[184:187], v[68:71]\n" \
"ds_read_b64_tr_b16 v[178:179], v225 offset:18176\n" \
"v_mfma_f32_16x16x32_bf16 v[100:103], v[136:139], a[172:175], v[100:103]\n" \
"s_mov_b32 s48, 0\n" \
"s_mov_b32 m0, s40\n" \
"s_nop 0\n" \
"buffer_load_dwordx4 %[kd0], %[srdk], s48 offen offset:0 lds\n" \
"v_mfma_f32_16x16x32_bf16 v[100:103], v[140:143], a[188:191], v[100:103]\n" \
"ds_read_b64_tr_b16 v[180:181], v225 offset:26112\n" \
"v_mfma_f32_16x16x32_bf16 v[8:11], v[144:147], a[160:163], v[8:11]\n" \
"ds_read_b64_tr_b16 v[182:183], v225 offset:26368\n" \
"v_mfma_f32_16x16x32_bf16 v[8:11], v[148:151], a[176:179], v[8:11]\n" \
"v_mfma_f32_16x16x32_bf16 v[40:43], v[144:147], a[164:167], v[40:43]\n" \
"ds_read_b64_tr_b16 v[184:185], v225 offset:17928\n" \
"v_mfma_f32_16x16x32_bf16 v[40:43], v[148:151], a[180:183], v[40:43]\n" \
"ds_read_b64_tr_b16 v[186:187], v225 offset:18184\n" \
"v_mfma_f32_16x16x32_bf16 v[72:75], v[144:147], a[168:171], v[72:75]\n" \
"buffer_load_dwordx4 v212, %[srdk], s48 offen offset:1024 lds\n" \
"v_mfma_f32_16x16x32_bf16 v[72:75], v[148:151], a[184:187], v[72:75]\n" \
"ds_read_b64_tr_b16 v[188:189], v225 offset:26120\n" \
"v_mfma_f32_16x16x32_bf16 v[104:107], v[144:147], a[172:175], v[104:107]\n" \
"ds_read_b64_tr_b16 v[190:191], v225 offset:26376\n" \
"v_mfma_f32_16x16x32_bf16 v[104:107], v[148:151], a[188:191], v[104:107]\n" \
"v_mfma_f32_16x16x32_bf16 v[12:15], v[152:155], a[160:163], v[12:15]\n" \
"buffer_load_dwordx4 v213, %[srdk], s48 offen offset:2048 lds\n" \
"v_mfma_f32_16x16x32_bf16 v[12:15], v[156:159], a[176:179], v[12:15]\n" \
"v_mfma_f32_16x16x32_bf16 v[44:47], v[152:155], a[164:167], v[44:47]\n" \
"v_mfma_f32_16x16x32_bf16 v[44:47], v[156:159], a[180:183], v[44:47]\n" \
"v_mfma_f32_16x16x32_bf16 v[76:79], v[152:155], a[168:171], v[76:79]\n" \
"buffer_load_dwordx4 v214, %[srdk], s48 offen offset:3072 lds\n" \
"v_mfma_f32_16x16x32_bf16 v[76:79], v[156:159], a[184:187], v[76:79]\n" \
"v_mfma_f32_16x16x32_bf16 v[108:111], v[152:155], a[172:175], v[108:111]\n" \
"v_mfma_f32_16x16x32_bf16 v[108:111], v[156:159], a[188:191], v[108:111]\n" \
"s_waitcnt vmcnt(30) lgkmcnt(0)\n" \
"s_barrier\n" \
"v_mfma_f32_16x16x32_bf16 v[16:19], v[160:163], a[160:163], v[16:19]\n" \
"ds_read_b64_tr_b16 v[128:129], v225 offset:32768\n" \
"v_mfma_f32_16x16x32_bf16 v[16:19], v[164:167], a[176:179], v[16:19]\n" \
"ds_read_b64_tr_b16 v[130:131], v225 offset:33024\n" \
"v_mfma_f32_16x16x32_bf16 v[48:51], v[160:163], a[164:167], v[48:51]\n" \
"s_mov_b32 s48, 32768\n" \
"s_mov_b32 m0, s45\n" \
"s_nop 0\n" \
"buffer_load_dwordx4 %[vd0], %[srdv], s48 offen offset:0 lds\n" \
"v_mfma_f32_16x16x32_bf16 v[48:51], v[164:167], a[180:183], v[48:51]\n" \
"ds_read_b64_tr_b16 v[132:133], v225 offset:40960\n" \
"v_mfma_f32_16x16x32_bf16 v[80:83], v[160:163], a[168:171], v[80:83]\n" \
"ds_read_b64_tr_b16 v[134:135], v225 offset:41216\n" \
"v_mfma_f32_16x16x32_bf16 v[80:83], v[164:167], a[184:187], v[80:83]\n" \
"v_mfma_f32_16x16x32_bf16 v[112:115], v[160:163], a[172:175], v[112:115]\n" \
"ds_read_b64_tr_b16 v[136:137], v225 offset:32776\n" \
"v_mfma_f32_16x16x32_bf16 v[112:115], v[164:167], a[188:191], v[112:115]\n" \
"ds_read_b64_tr_b16 v[138:139], v225 offset:33032\n" \
"v_mfma_f32_16x16x32_bf16 v[20:23], v[168:171], a[160:163], v[20:23]\n" \
"buffer_load_dwordx4 v215, %[srdv], s48 offen offset:1024 lds\n" \
"v_mfma_f32_16x16x32_bf16 v[20:23], v[172:175], a[176:179], v[20:23]\n" \
"ds_read_b64_tr_b16 v[140:141], v225 offset:40968\n" \
"v_mfma_f32_16x16x32_bf16 v[52:55], v[168:171], a[164:167], v[52:55]\n" \
"ds_read_b64_tr_b16 v[142:143], v225 offset:41224\n" \
"v_mfma_f32_16x16x32_bf16 v[52:55], v[172:175], a[180:183], v[52:55]\n" \
"v_mfma_f32_16x16x32_bf16 v[84:87], v[168:171], a[168:171], v[84:87]\n" \
"ds_read_b64_tr_b16 v[144:145], v225 offset:33280\n" \
"v_mfma_f32_16x16x32_bf16 v[84:87], v[172:175], a[184:187], v[84:87]\n" \
"ds_read_b64_tr_b16 v[146:147], v225 offset:33536\n" \
"v_mfma_f32_16x16x32_bf16 v[116:119], v[168:171], a[172:175], v[116:119]\n" \
"s_mov_b32 s48, 16384\n" \
"s_mov_b32 m0, s41\n" \
"s_nop 0\n" \
"buffer_load_dwordx4 %[kd0], %[srdk], s48 offen offset:0 lds\n" \
"v_mfma_f32_16x16x32_bf16 v[116:119], v[172:175], a[188:191], v[116:119]\n" \
"ds_read_b64_tr_b16 v[148:149], v225 offset:41472\n" \
"v_mfma_f32_16x16x32_bf16 v[24:27], v[176:179], a[160:163], v[24:27]\n" \
"ds_read_b64_tr_b16 v[150:151], v225 offset:41728\n" \
"v_mfma_f32_16x16x32_bf16 v[24:27], v[180:183], a[176:179], v[24:27]\n" \
"s_waitcnt vmcnt(18)\n" \
"v_cmp_eq_u64_e32 vcc, v[230:231], v[234:235]\n" \
"v_cmp_eq_u64_e64 s[56:57], v[232:233], v[234:235]\n" \
"s_nop 3\n" \
"s_or_b64 s[58:59], vcc, s[56:57]\n" \
"s_cbranch_scc0 Lnsp_%=\n" \
"Lnbk_%=:\n" \
"v_mfma_f32_16x16x32_bf16 v[56:59], v[176:179], a[164:167], v[56:59]\n" \
"ds_read_b64_tr_b16 v[152:153], v225 offset:33288\n" \
"v_mfma_f32_16x16x32_bf16 v[56:59], v[180:183], a[180:183], v[56:59]\n" \
"ds_read_b64_tr_b16 v[154:155], v225 offset:33544\n" \
"s_cmp_lg_u64 s[56:57], 0\n" \
"s_cselect_b32 %[cx], 0x2000, 0\n" \
"s_xor_b32 %[sin], %[sin], %[cx]\n" \
"v_mfma_f32_16x16x32_bf16 v[88:91], v[176:179], a[168:171], v[88:91]\n" \
"buffer_load_dwordx4 v212, %[srdk], s48 offen offset:1024 lds\n" \
"v_mfma_f32_16x16x32_bf16 v[88:91], v[180:183], a[184:187], v[88:91]\n" \
"ds_read_b64_tr_b16 v[156:157], v225 offset:41480\n" \
"v_mfma_f32_16x16x32_bf16 v[120:123], v[176:179], a[172:175], v[120:123]\n" \
"ds_read_b64_tr_b16 v[158:159], v225 offset:41736\n" \
"v_mfma_f32_16x16x32_bf16 v[120:123], v[180:183], a[188:191], v[120:123]\n" \
"s_mov_b64 exec, 0x2c4000\n" \
"v_xor_b32_e32 %[tlo], %[cx], %[tlo]\n" \
"s_mov_b64 exec, 0x40000\n" \
"v_xor_b32_e32 %[thi], %[cx], %[thi]\n" \
"s_mov_b64 exec, -1\n" \
"v_mfma_f32_16x16x32_bf16 v[28:31], v[184:187], a[160:163], v[28:31]\n" \
"buffer_load_dwordx4 v213, %[srdk], s48 offen offset:2048 lds\n" \
"v_mfma_f32_16x16x32_bf16 v[28:31], v[188:191], a[176:179], v[28:31]\n" \
"v_xor_b32_e32 %[oq0], %[cx], %[oq0]\n" \
"v_mfma_f32_16x16x32_bf16 v[60:63], v[184:187], a[164:167], v[60:63]\n" \
"v_mfma_f32_16x16x32_bf16 v[60:63], v[188:191], a[180:183], v[60:63]\n" \
"v_mfma_f32_16x16x32_bf16 v[92:95], v[184:187], a[168:171], v[92:95]\n" \
"buffer_load_dwordx4 v214, %[srdk], s48 offen offset:3072 lds\n" \
"v_mfma_f32_16x16x32_bf16 v[92:95], v[188:191], a[184:187], v[92:95]\n" \
"v_mfma_f32_16x16x32_bf16 v[124:127], v[184:187], a[172:175], v[124:127]\n" \
"v_mfma_f32_16x16x32_bf16 v[124:127], v[188:191], a[188:191], v[124:127]\n" \
"s_waitcnt vmcnt(25) lgkmcnt(0)\n" \
"v_mfma_f32_16x16x32_bf16 v[0:3], v[128:131], a[192:195], v[0:3]\n" \
"ds_read_b64_tr_b16 v[160:161], v225 offset:33792\n" \
"v_mfma_f32_16x16x32_bf16 v[0:3], v[132:135], a[208:211], v[0:3]\n" \
"ds_read_b64_tr_b16 v[162:163], v225 offset:34048\n" \
"v_mfma_f32_16x16x32_bf16 v[32:35], v[128:131], a[196:199], v[32:35]\n" \
"s_mov_b32 s48, 32768\n" \
"s_mov_b32 m0, s45\n" \
"s_nop 0\n" \
"buffer_load_dwordx4 %[vd0], %[srdv], s48 offen offset:2048 lds\n" \
"v_mfma_f32_16x16x32_bf16 v[32:35], v[132:135], a[212:215], v[32:35]\n" \
"ds_read_b64_tr_b16 v[164:165], v225 offset:41984\n" \
"v_mfma_f32_16x16x32_bf16 v[64:67], v[128:131], a[200:203], v[64:67]\n" \
"ds_read_b64_tr_b16 v[166:167], v225 offset:42240\n" \
"v_mfma_f32_16x16x32_bf16 v[64:67], v[132:135], a[216:219], v[64:67]\n" \
"v_mfma_f32_16x16x32_bf16 v[96:99], v[128:131], a[204:207], v[96:99]\n" \
"ds_read_b64_tr_b16 v[168:169], v225 offset:33800\n" \
"v_mfma_f32_16x16x32_bf16 v[96:99], v[132:135], a[220:223], v[96:99]\n" \
"ds_read_b64_tr_b16 v[170:171], v225 offset:34056\n" \
"v_mfma_f32_16x16x32_bf16 v[4:7], v[136:139], a[192:195], v[4:7]\n" \
"buffer_load_dwordx4 v215, %[srdv], s48 offen offset:3072 lds\n" \
"v_mfma_f32_16x16x32_bf16 v[4:7], v[140:143], a[208:211], v[4:7]\n" \
"ds_read_b64_tr_b16 v[172:173], v225 offset:41992\n" \
"v_mfma_f32_16x16x32_bf16 v[36:39], v[136:139], a[196:199], v[36:39]\n" \
"ds_read_b64_tr_b16 v[174:175], v225 offset:42248\n" \
"v_mfma_f32_16x16x32_bf16 v[36:39], v[140:143], a[212:215], v[36:39]\n" \
"v_mfma_f32_16x16x32_bf16 v[68:71], v[136:139], a[200:203], v[68:71]\n" \
"ds_read_b64_tr_b16 v[176:177], v225 offset:34304\n" \
"v_mfma_f32_16x16x32_bf16 v[68:71], v[140:143], a[216:219], v[68:71]\n" \
"ds_read_b64_tr_b16 v[178:179], v225 offset:34560\n" \
"v_mfma_f32_16x16x32_bf16 v[100:103], v[136:139], a[204:207], v[100:103]\n" \
"s_mov_b32 s48, 32768\n" \
"s_mov_b32 m0, s42\n" \
"s_nop 0\n" \
"buffer_load_dwordx4 %[kd0], %[srdk], s48 offen offset:0 lds\n" \
"v_mfma_f32_16x16x32_bf16 v[100:103], v[140:143], a[220:223], v[100:103]\n" \
"ds_read_b64_tr_b16 v[180:181], v225 offset:42496\n" \
"v_mfma_f32_16x16x32_bf16 v[8:11], v[144:147], a[192:195], v[8:11]\n" \
"ds_read_b64_tr_b16 v[182:183], v225 offset:42752\n" \
"v_mfma_f32_16x16x32_bf16 v[8:11], v[148:151], a[208:211], v[8:11]\n" \
"v_mfma_f32_16x16x32_bf16 v[40:43], v[144:147], a[196:199], v[40:43]\n" \
"ds_read_b64_tr_b16 v[184:185], v225 offset:34312\n" \
"v_mfma_f32_16x16x32_bf16 v[40:43], v[148:151], a[212:215], v[40:43]\n" \
"ds_read_b64_tr_b16 v[186:187], v225 offset:34568\n" \
"v_mfma_f32_16x16x32_bf16 v[72:75], v[144:147], a[200:203], v[72:75]\n" \
"buffer_load_dwordx4 v212, %[srdk], s48 offen offset:1024 lds\n" \
"v_mfma_f32_16x16x32_bf16 v[72:75], v[148:151], a[216:219], v[72:75]\n" \
"ds_read_b64_tr_b16 v[188:189], v225 offset:42504\n" \
"v_mfma_f32_16x16x32_bf16 v[104:107], v[144:147], a[204:207], v[104:107]\n" \
"ds_read_b64_tr_b16 v[190:191], v225 offset:42760\n" \
"v_mfma_f32_16x16x32_bf16 v[104:107], v[148:151], a[220:223], v[104:107]\n" \
"v_mfma_f32_16x16x32_bf16 v[12:15], v[152:155], a[192:195], v[12:15]\n" \
"buffer_load_dwordx4 v213, %[srdk], s48 offen offset:2048 lds\n" \
"v_mfma_f32_16x16x32_bf16 v[12:15], v[156:159], a[208:211], v[12:15]\n" \
"v_mfma_f32_16x16x32_bf16 v[44:47], v[152:155], a[196:199], v[44:47]\n" \
"v_mfma_f32_16x16x32_bf16 v[44:47], v[156:159], a[212:215], v[44:47]\n" \
"v_mfma_f32_16x16x32_bf16 v[76:79], v[152:155], a[200:203], v[76:79]\n" \
"buffer_load_dwordx4 v214, %[srdk], s48 offen offset:3072 lds\n" \
"v_mfma_f32_16x16x32_bf16 v[76:79], v[156:159], a[216:219], v[76:79]\n" \
"v_mfma_f32_16x16x32_bf16 v[108:111], v[152:155], a[204:207], v[108:111]\n" \
"v_mfma_f32_16x16x32_bf16 v[108:111], v[156:159], a[220:223], v[108:111]\n" \
"s_waitcnt vmcnt(29) lgkmcnt(0)\n" \
"s_barrier\n" \
"v_mfma_f32_16x16x32_bf16 v[16:19], v[160:163], a[192:195], v[16:19]\n" \
"ds_read_b64_tr_b16 v[128:129], v225 offset:49152\n" \
"s_mov_b32 s54, 0x4000\n" \
"v_mfma_f32_16x16x32_bf16 v[16:19], v[164:167], a[208:211], v[16:19]\n" \
"ds_read_b64_tr_b16 v[130:131], v225 offset:49408\n" \
"v_mfma_f32_16x16x32_bf16 v[48:51], v[160:163], a[196:199], v[48:51]\n" \
"s_mov_b32 s48, 49152\n" \
"s_mov_b32 m0, s46\n" \
"s_nop 0\n" \
"buffer_load_dwordx4 %[vd0], %[srdv], s48 offen offset:0 lds\n" \
"v_mbcnt_lo_u32_b32 v231, -1, 0\n" \
"v_mfma_f32_16x16x32_bf16 v[48:51], v[164:167], a[212:215], v[48:51]\n" \
"ds_read_b64_tr_b16 v[132:133], v225 offset:57344\n" \
"v_mfma_f32_16x16x32_bf16 v[80:83], v[160:163], a[200:203], v[80:83]\n" \
"ds_read_b64_tr_b16 v[134:135], v225 offset:57600\n" \
"v_mfma_f32_16x16x32_bf16 v[80:83], v[164:167], a[216:219], v[80:83]\n" \
"v_mbcnt_hi_u32_b32 v231, -1, v231\n" \
"v_mfma_f32_16x16x32_bf16 v[112:115], v[160:163], a[204:207], v[112:115]\n" \
"ds_read_b64_tr_b16 v[136:137], v225 offset:49160\n" \
"v_mfma_f32_16x16x32_bf16 v[112:115], v[164:167], a[220:223], v[112:115]\n" \
"ds_read_b64_tr_b16 v[138:139], v225 offset:49416\n" \
"v_mfma_f32_16x16x32_bf16 v[20:23], v[168:171], a[192:195], v[20:23]\n" \
"buffer_load_dwordx4 v215, %[srdv], s48 offen offset:1024 lds\n" \
"v_lshrrev_b32_e64 v232, 4, v231\n" \
"v_mfma_f32_16x16x32_bf16 v[20:23], v[172:175], a[208:211], v[20:23]\n" \
"ds_read_b64_tr_b16 v[140:141], v225 offset:57352\n" \
"v_mfma_f32_16x16x32_bf16 v[52:55], v[168:171], a[196:199], v[52:55]\n" \
"ds_read_b64_tr_b16 v[142:143], v225 offset:57608\n" \
"v_xor_b32_e64 v232, v232, v231\n" \
"v_mfma_f32_16x16x32_bf16 v[52:55], v[172:175], a[212:215], v[52:55]\n" \
"v_mfma_f32_16x16x32_bf16 v[84:87], v[168:171], a[200:203], v[84:87]\n" \
"ds_read_b64_tr_b16 v[144:145], v225 offset:49664\n" \
"v_mfma_f32_16x16x32_bf16 v[84:87], v[172:175], a[216:219], v[84:87]\n" \
"ds_read_b64_tr_b16 v[146:147], v225 offset:49920\n" \
"v_bfe_i32 v232, v232, 1, 1\n" \
"v_mfma_f32_16x16x32_bf16 v[116:119], v[168:171], a[204:207], v[116:119]\n" \
"s_mov_b32 s48, 49152\n" \
"s_mov_b32 m0, s43\n" \
"s_nop 0\n" \
"buffer_load_dwordx4 %[kd0], %[srdk], s48 offen offset:0 lds\n" \
"v_mfma_f32_16x16x32_bf16 v[116:119], v[172:175], a[220:223], v[116:119]\n" \
"ds_read_b64_tr_b16 v[148:149], v225 offset:57856\n" \
"v_mfma_f32_16x16x32_bf16 v[24:27], v[176:179], a[192:195], v[24:27]\n" \
"ds_read_b64_tr_b16 v[150:151], v225 offset:58112\n" \
"v_not_b32_e64 v232, v232\n" \
"v_mfma_f32_16x16x32_bf16 v[24:27], v[180:183], a[208:211], v[24:27]\n" \
"v_mfma_f32_16x16x32_bf16 v[56:59], v[176:179], a[196:199], v[56:59]\n" \
"ds_read_b64_tr_b16 v[152:153], v225 offset:49672\n" \
"v_mfma_f32_16x16x32_bf16 v[56:59], v[180:183], a[212:215], v[56:59]\n" \
"ds_read_b64_tr_b16 v[154:155], v225 offset:49928\n" \
"v_and_b32_e32 v230, 0x3f803f80, v232\n" \
"v_mfma_f32_16x16x32_bf16 v[88:91], v[176:179], a[200:203], v[88:91]\n" \
"buffer_load_dwordx4 v212, %[srdk], s48 offen offset:1024 lds\n" \
"v_mfma_f32_16x16x32_bf16 v[88:91], v[180:183], a[216:219], v[88:91]\n" \
"ds_read_b64_tr_b16 v[156:157], v225 offset:57864\n" \
"v_readlane_b32 s62, %[tlo], 18\n" \
"v_mfma_f32_16x16x32_bf16 v[120:123], v[176:179], a[204:207], v[120:123]\n" \
"ds_read_b64_tr_b16 v[158:159], v225 offset:58120\n" \
"v_mfma_f32_16x16x32_bf16 v[120:123], v[180:183], a[220:223], v[120:123]\n" \
"v_mfma_f32_16x16x32_bf16 v[28:31], v[184:187], a[192:195], v[28:31]\n" \
"buffer_load_dwordx4 v213, %[srdk], s48 offen offset:2048 lds\n" \
"v_readlane_b32 s61, %[tlo], 21\n" \
"v_mfma_f32_16x16x32_bf16 v[28:31], v[188:191], a[208:211], v[28:31]\n" \
"v_mfma_f32_16x16x32_bf16 v[60:63], v[184:187], a[196:199], v[60:63]\n" \
"v_mfma_f32_16x16x32_bf16 v[60:63], v[188:191], a[212:215], v[60:63]\n" \
"v_lshlrev_b32_e64 v224, 7, v231\n" \
"v_mfma_f32_16x16x32_bf16 v[92:95], v[184:187], a[200:203], v[92:95]\n" \
"buffer_load_dwordx4 v214, %[srdk], s48 offen offset:3072 lds\n" \
"v_mfma_f32_16x16x32_bf16 v[92:95], v[188:191], a[216:219], v[92:95]\n" \
"v_mfma_f32_16x16x32_bf16 v[124:127], v[184:187], a[204:207], v[124:127]\n" \
"v_mfma_f32_16x16x32_bf16 v[124:127], v[188:191], a[220:223], v[124:127]\n" \
"s_waitcnt vmcnt(24) lgkmcnt(0)\n" \
"v_mfma_f32_16x16x32_bf16 v[0:3], v[128:131], a[224:227], v[0:3]\n" \
"ds_read_b64_tr_b16 v[160:161], v225 offset:50176\n" \
"v_accvgpr_write_b32 a192, v230\n" \
"v_mfma_f32_16x16x32_bf16 v[0:3], v[132:135], a[240:243], v[0:3]\n" \
"ds_read_b64_tr_b16 v[162:163], v225 offset:50432\n" \
"v_mfma_f32_16x16x32_bf16 v[32:35], v[128:131], a[228:231], v[32:35]\n" \
"s_mov_b32 s48, 49152\n" \
"s_mov_b32 m0, s46\n" \
"s_nop 0\n" \
"buffer_load_dwordx4 %[vd0], %[srdv], s48 offen offset:2048 lds\n" \
"v_accvgpr_write_b32 a193, v230\n" \
"v_mfma_f32_16x16x32_bf16 v[32:35], v[132:135], a[244:247], v[32:35]\n" \
"ds_read_b64_tr_b16 v[164:165], v225 offset:58368\n" \
"v_mfma_f32_16x16x32_bf16 v[64:67], v[128:131], a[232:235], v[64:67]\n" \
"ds_read_b64_tr_b16 v[166:167], v225 offset:58624\n" \
"v_accvgpr_write_b32 a194, v230\n" \
"v_mfma_f32_16x16x32_bf16 v[64:67], v[132:135], a[248:251], v[64:67]\n" \
"v_mfma_f32_16x16x32_bf16 v[96:99], v[128:131], a[236:239], v[96:99]\n" \
"ds_read_b64_tr_b16 v[168:169], v225 offset:50184\n" \
"v_accvgpr_write_b32 a195, v230\n" \
"v_mfma_f32_16x16x32_bf16 v[96:99], v[132:135], a[252:255], v[96:99]\n" \
"ds_read_b64_tr_b16 v[170:171], v225 offset:50440\n" \
"v_mfma_f32_16x16x32_bf16 v[4:7], v[136:139], a[224:227], v[4:7]\n" \
"buffer_load_dwordx4 v215, %[srdv], s48 offen offset:3072 lds\n" \
"v_accvgpr_write_b32 a196, -1\n" \
"v_mfma_f32_16x16x32_bf16 v[4:7], v[140:143], a[240:243], v[4:7]\n" \
"ds_read_b64_tr_b16 v[172:173], v225 offset:58376\n" \
"v_mfma_f32_16x16x32_bf16 v[36:39], v[136:139], a[228:231], v[36:39]\n" \
"ds_read_b64_tr_b16 v[174:175], v225 offset:58632\n" \
"v_accvgpr_write_b32 a197, -1\n" \
"v_mfma_f32_16x16x32_bf16 v[36:39], v[140:143], a[244:247], v[36:39]\n" \
"v_mfma_f32_16x16x32_bf16 v[68:71], v[136:139], a[232:235], v[68:71]\n" \
"ds_read_b64_tr_b16 v[176:177], v225 offset:50688\n" \
"v_accvgpr_write_b32 a198, -1\n" \
"v_mfma_f32_16x16x32_bf16 v[68:71], v[140:143], a[248:251], v[68:71]\n" \
"ds_read_b64_tr_b16 v[178:179], v225 offset:50944\n" \
"v_mfma_f32_16x16x32_bf16 v[100:103], v[136:139], a[236:239], v[100:103]\n" \
"v_accvgpr_write_b32 a199, -1\n" \
"v_mfma_f32_16x16x32_bf16 v[100:103], v[140:143], a[252:255], v[100:103]\n" \
"ds_read_b64_tr_b16 v[180:181], v225 offset:58880\n" \
"v_mfma_f32_16x16x32_bf16 v[8:11], v[144:147], a[224:227], v[8:11]\n" \
"ds_read_b64_tr_b16 v[182:183], v225 offset:59136\n" \
"v_lshrrev_b32_e32 v231, 3, v224\n" \
"v_mfma_f32_16x16x32_bf16 v[8:11], v[148:151], a[240:243], v[8:11]\n" \
"v_mfma_f32_16x16x32_bf16 v[40:43], v[144:147], a[228:231], v[40:43]\n" \
"ds_read_b64_tr_b16 v[184:185], v225 offset:50696\n" \
"v_mov_b32_e64 v232, -1\n" \
"v_mfma_f32_16x16x32_bf16 v[40:43], v[148:151], a[244:247], v[40:43]\n" \
"ds_read_b64_tr_b16 v[186:187], v225 offset:50952\n" \
"v_mfma_f32_16x16x32_bf16 v[72:75], v[144:147], a[232:235], v[72:75]\n" \
"v_mov_b32_e64 v233, -1\n" \
"v_mfma_f32_16x16x32_bf16 v[72:75], v[148:151], a[248:251], v[72:75]\n" \
"ds_read_b64_tr_b16 v[188:189], v225 offset:58888\n" \
"v_mfma_f32_16x16x32_bf16 v[104:107], v[144:147], a[236:239], v[104:107]\n" \
"ds_read_b64_tr_b16 v[190:191], v225 offset:59144\n" \
"v_mov_b32_e64 v234, -1\n" \
"v_mfma_f32_16x16x32_bf16 v[104:107], v[148:151], a[252:255], v[104:107]\n" \
"v_mfma_f32_16x16x32_bf16 v[12:15], v[152:155], a[224:227], v[12:15]\n" \
"v_mov_b32_e64 v235, -1\n" \
"v_mfma_f32_16x16x32_bf16 v[12:15], v[156:159], a[240:243], v[12:15]\n" \
"v_mfma_f32_16x16x32_bf16 v[44:47], v[152:155], a[228:231], v[44:47]\n" \
"buffer_store_dwordx4 v[232:235], v231, %[srdz], s61 offen offset:0 sc1\n" \
"v_mfma_f32_16x16x32_bf16 v[44:47], v[156:159], a[244:247], v[44:47]\n" \
"v_mfma_f32_16x16x32_bf16 v[76:79], v[152:155], a[232:235], v[76:79]\n" \
"buffer_store_dwordx4 v[232:235], v231, %[srdz], s61 offen offset:1024 sc1\n" \
"v_mfma_f32_16x16x32_bf16 v[76:79], v[156:159], a[248:251], v[76:79]\n" \
"v_mfma_f32_16x16x32_bf16 v[108:111], v[152:155], a[236:239], v[108:111]\n" \
"v_mfma_f32_16x16x32_bf16 v[108:111], v[156:159], a[252:255], v[108:111]\n" \
"s_waitcnt vmcnt(22) lgkmcnt(0)\n" \
"s_barrier\n" \
"v_mfma_f32_16x16x32_bf16 v[16:19], v[160:163], a[224:227], v[16:19]\n" \
"ds_read_b128 a[64:67], %[kb0] offset:0\n" \
"v_mfma_f32_16x16x32_bf16 v[16:19], v[164:167], a[240:243], v[16:19]\n" \
"v_mfma_f32_16x16x32_bf16 v[20:23], v[168:171], a[224:227], v[20:23]\n" \
"v_cvt_pk_bf16_f32 v230, v0, v1\n" \
"v_cvt_pk_bf16_f32 v231, v2, v3\n" \
"v_cvt_pk_bf16_f32 v232, v4, v5\n" \
"ds_read_b128 a[68:71], v208 offset:0\n" \
"v_mfma_f32_16x16x32_bf16 v[20:23], v[172:175], a[240:243], v[20:23]\n" \
"v_cvt_pk_bf16_f32 v233, v6, v7\n" \
"v_accvgpr_write_b32 a0, v230\n" \
".if . - adaln_body != 8192\n" \
".error \"size model\"\n" \
".endif\n" \
"Lk1_%=:\n" \
"s_cbranch_execz Lk2_%=\n" \
"s_nop 0\n" \
"v_accvgpr_write_b32 a1, v231\n" \
"v_mfma_f32_16x16x32_bf16 v[24:27], v[176:179], a[224:227], v[24:27]\n" \
"v_accvgpr_write_b32 a2, v232\n" \
"v_accvgpr_write_b32 a3, v233\n" \
"v_cvt_pk_bf16_f32 v234, v8, v9\n" \
"ds_read_b128 a[72:75], v209 offset:0\n" \
"v_mfma_f32_16x16x32_bf16 v[24:27], v[180:183], a[240:243], v[24:27]\n" \
"v_cvt_pk_bf16_f32 v235, v10, v11\n" \
"v_cvt_pk_bf16_f32 v236, v12, v13\n" \
"v_cvt_pk_bf16_f32 v237, v14, v15\n" \
"v_mfma_f32_16x16x32_bf16 v[28:31], v[184:187], a[224:227], v[28:31]\n" \
"v_accvgpr_write_b32 a4, v234\n" \
"v_accvgpr_write_b32 a5, v235\n" \
"v_accvgpr_write_b32 a6, v236\n" \
"ds_read_b128 a[76:79], v210 offset:0\n" \
"v_mfma_f32_16x16x32_bf16 v[28:31], v[188:191], a[240:243], v[28:31]\n" \
"v_accvgpr_write_b32 a7, v237\n" \
"v_cvt_pk_bf16_f32 v230, v32, v33\n" \
"v_cvt_pk_bf16_f32 v231, v34, v35\n" \
"v_mfma_f32_16x16x32_bf16 v[48:51], v[160:163], a[228:231], v[48:51]\n" \
"v_cvt_pk_bf16_f32 v232, v36, v37\n" \
"v_cvt_pk_bf16_f32 v233, v38, v39\n" \
"v_accvgpr_write_b32 a16, v230\n" \
"ds_read_b128 a[92:95], v210 offset:4096\n" \
"v_mfma_f32_16x16x32_bf16 v[48:51], v[164:167], a[244:247], v[48:51]\n" \
"v_accvgpr_write_b32 a17, v231\n" \
"v_accvgpr_write_b32 a18, v232\n" \
"v_accvgpr_write_b32 a19, v233\n" \
"v_mfma_f32_16x16x32_bf16 v[52:55], v[168:171], a[228:231], v[52:55]\n" \
"v_cvt_pk_bf16_f32 v234, v40, v41\n" \
"v_cvt_pk_bf16_f32 v235, v42, v43\n" \
"v_cvt_pk_bf16_f32 v236, v44, v45\n" \
"ds_read_b128 a[88:91], v209 offset:4096\n" \
"v_mfma_f32_16x16x32_bf16 v[52:55], v[172:175], a[244:247], v[52:55]\n" \
"v_cvt_pk_bf16_f32 v237, v46, v47\n" \
"v_accvgpr_write_b32 a20, v234\n" \
"v_accvgpr_write_b32 a21, v235\n" \
"v_mfma_f32_16x16x32_bf16 v[56:59], v[176:179], a[228:231], v[56:59]\n" \
"v_accvgpr_write_b32 a22, v236\n" \
"v_accvgpr_write_b32 a23, v237\n" \
"v_cvt_pk_bf16_f32 v230, v16, v17\n" \
"ds_read_b128 a[84:87], v208 offset:4096\n" \
"v_mfma_f32_16x16x32_bf16 v[56:59], v[180:183], a[244:247], v[56:59]\n" \
"v_cvt_pk_bf16_f32 v231, v18, v19\n" \
"v_cvt_pk_bf16_f32 v232, v20, v21\n" \
"v_cvt_pk_bf16_f32 v233, v22, v23\n" \
"v_mfma_f32_16x16x32_bf16 v[60:63], v[184:187], a[228:231], v[60:63]\n" \
"v_accvgpr_write_b32 a8, v230\n" \
"v_accvgpr_write_b32 a9, v231\n" \
"v_accvgpr_write_b32 a10, v232\n" \
"ds_read_b128 a[80:83], %[kb0] offset:4096\n" \
"v_mfma_f32_16x16x32_bf16 v[60:63], v[188:191], a[244:247], v[60:63]\n" \
"v_accvgpr_write_b32 a11, v233\n" \
"v_cvt_pk_bf16_f32 v234, v24, v25\n" \
"v_cvt_pk_bf16_f32 v235, v26, v27\n" \
"v_mfma_f32_16x16x32_bf16 v[80:83], v[160:163], a[232:235], v[80:83]\n" \
"v_cvt_pk_bf16_f32 v236, v28, v29\n" \
"v_cvt_pk_bf16_f32 v237, v30, v31\n" \
"v_accvgpr_write_b32 a12, v234\n" \
"v_mfma_f32_16x16x32_bf16 v[80:83], v[164:167], a[248:251], v[80:83]\n" \
"v_accvgpr_write_b32 a13, v235\n" \
"v_accvgpr_write_b32 a14, v236\n" \
"v_accvgpr_write_b32 a15, v237\n" \
"v_mfma_f32_16x16x32_bf16 v[84:87], v[168:171], a[232:235], v[84:87]\n" \
"v_cvt_pk_bf16_f32 v230, v64, v65\n" \
"v_cvt_pk_bf16_f32 v231, v66, v67\n" \
"v_cvt_pk_bf16_f32 v232, v68, v69\n" \
"v_mfma_f32_16x16x32_bf16 v[84:87], v[172:175], a[248:251], v[84:87]\n" \
"v_cvt_pk_bf16_f32 v233, v70, v71\n" \
"v_accvgpr_write_b32 a32, v230\n" \
"v_accvgpr_write_b32 a33, v231\n" \
"v_mfma_f32_16x16x32_bf16 v[88:91], v[176:179], a[232:235], v[88:91]\n" \
"v_accvgpr_write_b32 a34, v232\n" \
"v_accvgpr_write_b32 a35, v233\n" \
"v_cvt_pk_bf16_f32 v234, v72, v73\n" \
"v_mfma_f32_16x16x32_bf16 v[88:91], v[180:183], a[248:251], v[88:91]\n" \
"v_cvt_pk_bf16_f32 v235, v74, v75\n" \
"v_cvt_pk_bf16_f32 v236, v76, v77\n" \
"v_cvt_pk_bf16_f32 v237, v78, v79\n" \
"v_mfma_f32_16x16x32_bf16 v[92:95], v[184:187], a[232:235], v[92:95]\n" \
"v_accvgpr_write_b32 a36, v234\n" \
"v_accvgpr_write_b32 a37, v235\n" \
"v_accvgpr_write_b32 a38, v236\n" \
"v_mfma_f32_16x16x32_bf16 v[92:95], v[188:191], a[248:251], v[92:95]\n" \
"v_accvgpr_write_b32 a39, v237\n" \
"v_cvt_pk_bf16_f32 v230, v48, v49\n" \
"v_cvt_pk_bf16_f32 v231, v50, v51\n" \
"v_mfma_f32_16x16x32_bf16 v[112:115], v[160:163], a[236:239], v[112:115]\n" \
"v_cvt_pk_bf16_f32 v232, v52, v53\n" \
"v_cvt_pk_bf16_f32 v233, v54, v55\n" \
"v_accvgpr_write_b32 a24, v230\n" \
"v_mfma_f32_16x16x32_bf16 v[112:115], v[164:167], a[252:255], v[112:115]\n" \
"v_accvgpr_write_b32 a25, v231\n" \
"v_accvgpr_write_b32 a26, v232\n" \
"v_accvgpr_write_b32 a27, v233\n" \
"v_mfma_f32_16x16x32_bf16 v[116:119], v[168:171], a[236:239], v[116:119]\n" \
"v_cvt_pk_bf16_f32 v234, v56, v57\n" \
"v_cvt_pk_bf16_f32 v235, v58, v59\n" \
"v_cvt_pk_bf16_f32 v236, v60, v61\n" \
"v_mfma_f32_16x16x32_bf16 v[116:119], v[172:175], a[252:255], v[116:119]\n" \
"v_cvt_pk_bf16_f32 v237, v62, v63\n" \
"v_accvgpr_write_b32 a28, v234\n" \
"v_accvgpr_write_b32 a29, v235\n" \
"v_mfma_f32_16x16x32_bf16 v[120:123], v[176:179], a[236:239], v[120:123]\n" \
"v_accvgpr_write_b32 a30, v236\n" \
"v_accvgpr_write_b32 a31, v237\n" \
"v_cvt_pk_bf16_f32 v230, v96, v97\n" \
"v_mfma_f32_16x16x32_bf16 v[120:123], v[180:183], a[252:255], v[120:123]\n" \
"v_cvt_pk_bf16_f32 v231, v98, v99\n" \
"v_cvt_pk_bf16_f32 v232, v100, v101\n" \
"v_cvt_pk_bf16_f32 v233, v102, v103\n" \
"v_mfma_f32_16x16x32_bf16 v[124:127], v[184:187], a[236:239], v[124:127]\n" \
"v_accvgpr_write_b32 a48, v230\n" \
"v_accvgpr_write_b32 a49, v231\n" \
"v_accvgpr_write_b32 a50, v232\n" \
"v_mfma_f32_16x16x32_bf16 v[124:127], v[188:191], a[252:255], v[124:127]\n" \
"v_accvgpr_write_b32 a51, v233\n" \
"s_waitcnt vmcnt(63) lgkmcnt(0)\n" \
"s_nop 0\n" \
"s_mov_b32 s48, 65536\n" \
"s_mov_b32 s60, 0\n" \
"s_waitcnt lgkmcnt(0)\n" \
"v_mfma_f32_16x16x32_bf16 v[128:131], a[64:67], a[0:3], 0\n" \
"ds_read_b128 a[96:99], %[kb0] offset:8192\n" \
"v_cvt_pk_bf16_f32 v234, v80, v81\n" \
"v_cvt_pk_bf16_f32 v235, v82, v83\n" \
"v_mfma_f32_16x16x32_bf16 v[128:131], a[68:71], a[4:7], v[128:131]\n" \
"ds_read_b128 a[100:103], v208 offset:8192\n" \
"v_cvt_pk_bf16_f32 v236, v84, v85\n" \
"v_cvt_pk_bf16_f32 v237, v86, v87\n" \
"v_mfma_f32_16x16x32_bf16 v[128:131], a[72:75], a[8:11], v[128:131]\n" \
"ds_read_b128 a[104:107], v209 offset:8192\n" \
"v_accvgpr_write_b32 a40, v234\n" \
"v_accvgpr_write_b32 a41, v235\n" \
"v_mfma_f32_16x16x32_bf16 v[128:131], a[76:79], a[12:15], v[128:131]\n" \
"ds_read_b128 a[108:111], v210 offset:8192\n" \
"v_accvgpr_write_b32 a42, v236\n" \
"v_accvgpr_write_b32 a43, v237\n" \
"v_mfma_f32_16x16x32_bf16 v[132:135], a[92:95], a[12:15], 0\n" \
"ds_read_b128 a[124:127], v210 offset:12288\n" \
"v_cvt_pk_bf16_f32 v230, v88, v89\n" \
"v_cvt_pk_bf16_f32 v231, v90, v91\n" \
"v_mfma_f32_16x16x32_bf16 v[132:135], a[88:91], a[8:11], v[132:135]\n" \
"ds_read_b128 a[120:123], v209 offset:12288\n" \
"v_cvt_pk_bf16_f32 v232, v92, v93\n" \
"v_cvt_pk_bf16_f32 v233, v94, v95\n" \
"v_mfma_f32_16x16x32_bf16 v[132:135], a[84:87], a[4:7], v[132:135]\n" \
"ds_read_b128 a[116:119], v208 offset:12288\n" \
"v_accvgpr_write_b32 a44, v230\n" \
"v_accvgpr_write_b32 a45, v231\n" \
"v_mfma_f32_16x16x32_bf16 v[132:135], a[80:83], a[0:3], v[132:135]\n" \
"ds_read_b128 a[112:115], %[kb0] offset:12288\n" \
"v_accvgpr_write_b32 a46, v232\n" \
"v_accvgpr_write_b32 a47, v233\n" \
"v_mfma_f32_16x16x32_bf16 v[136:139], a[64:67], a[16:19], 0\n" \
"ds_read_b64_tr_b16 a[128:129], %[vvb] offset:0\n" \
"v_cvt_pk_bf16_f32 v234, v104, v105\n" \
"v_cvt_pk_bf16_f32 v235, v106, v107\n" \
"v_mfma_f32_16x16x32_bf16 v[136:139], a[68:71], a[20:23], v[136:139]\n" \
"ds_read_b64_tr_b16 a[130:131], %[vvb] offset:4096\n" \
"v_cvt_pk_bf16_f32 v236, v108, v109\n" \
"v_cvt_pk_bf16_f32 v237, v110, v111\n" \
"v_mfma_f32_16x16x32_bf16 v[136:139], a[72:75], a[24:27], v[136:139]\n" \
"ds_read_b64_tr_b16 a[132:133], v211 offset:0\n" \
"v_accvgpr_write_b32 a52, v234\n" \
"v_accvgpr_write_b32 a53, v235\n" \
"v_mfma_f32_16x16x32_bf16 v[136:139], a[76:79], a[28:31], v[136:139]\n" \
"ds_read_b64_tr_b16 a[134:135], v211 offset:4096\n" \
"v_accvgpr_write_b32 a54, v236\n" \
"v_accvgpr_write_b32 a55, v237\n" \
"v_mfma_f32_16x16x32_bf16 v[140:143], a[92:95], a[28:31], 0\n" \
"ds_read_b64_tr_b16 a[136:137], %[vvb] offset:512\n" \
"v_cvt_pk_bf16_f32 v230, v112, v113\n" \
"v_cvt_pk_bf16_f32 v231, v114, v115\n" \
"v_mfma_f32_16x16x32_bf16 v[140:143], a[88:91], a[24:27], v[140:143]\n" \
"ds_read_b64_tr_b16 a[138:139], %[vvb] offset:4608\n" \
"v_cvt_pk_bf16_f32 v232, v116, v117\n" \
"v_cvt_pk_bf16_f32 v233, v118, v119\n" \
"v_mfma_f32_16x16x32_bf16 v[140:143], a[84:87], a[20:23], v[140:143]\n" \
"ds_read_b64_tr_b16 a[140:141], v211 offset:512\n" \
"v_accvgpr_write_b32 a56, v230\n" \
"v_accvgpr_write_b32 a57, v231\n" \
"v_mfma_f32_16x16x32_bf16 v[140:143], a[80:83], a[16:19], v[140:143]\n" \
"ds_read_b64_tr_b16 a[142:143], v211 offset:4608\n" \
"v_accvgpr_write_b32 a58, v232\n" \
"v_accvgpr_write_b32 a59, v233\n" \
"v_mfma_f32_16x16x32_bf16 v[144:147], a[64:67], a[32:35], 0\n" \
"ds_read_b64_tr_b16 a[144:145], %[vvb] offset:1024\n" \
"v_cvt_pk_bf16_f32 v234, v120, v121\n" \
"v_cvt_pk_bf16_f32 v235, v122, v123\n" \
"v_mfma_f32_16x16x32_bf16 v[144:147], a[68:71], a[36:39], v[144:147]\n" \
"ds_read_b64_tr_b16 a[146:147], %[vvb] offset:5120\n" \
"v_cvt_pk_bf16_f32 v236, v124, v125\n" \
"v_cvt_pk_bf16_f32 v237, v126, v127\n" \
"v_mfma_f32_16x16x32_bf16 v[144:147], a[72:75], a[40:43], v[144:147]\n" \
"ds_read_b64_tr_b16 a[148:149], v211 offset:1024\n" \
"v_accvgpr_write_b32 a60, v234\n" \
"v_accvgpr_write_b32 a61, v235\n" \
"v_mfma_f32_16x16x32_bf16 v[144:147], a[76:79], a[44:47], v[144:147]\n" \
"ds_read_b64_tr_b16 a[150:151], v211 offset:5120\n" \
"v_accvgpr_write_b32 a62, v236\n" \
"v_accvgpr_write_b32 a63, v237\n" \
"v_mfma_f32_16x16x32_bf16 v[148:151], a[92:95], a[44:47], 0\n" \
"ds_read_b64_tr_b16 a[152:153], %[vvb] offset:1536\n" \
"v_max3_f32 v192, v128, v129, v130\n" \
"v_max3_f32 v193, v131, v132, v133\n" \
"v_mfma_f32_16x16x32_bf16 v[148:151], a[88:91], a[40:43], v[148:151]\n" \
"ds_read_b64_tr_b16 a[154:155], %[vvb] offset:5632\n" \
"v_max3_f32 v192, v192, v134, v135\n" \
"v_max_f32_e64 v225, v192, v193\n" \
"v_mfma_f32_16x16x32_bf16 v[148:151], a[84:87], a[36:39], v[148:151]\n" \
"ds_read_b64_tr_b16 a[156:157], v211 offset:1536\n" \
"v_max3_f32 v192, v136, v137, v138\n" \
"v_max3_f32 v193, v139, v140, v141\n" \
"v_mfma_f32_16x16x32_bf16 v[148:151], a[80:83], a[32:35], v[148:151]\n" \
"ds_read_b64_tr_b16 a[158:159], v211 offset:5632\n" \
"v_max3_f32 v192, v192, v142, v143\n" \
"v_max_f32_e64 v226, v192, v193\n" \
"v_mfma_f32_16x16x32_bf16 v[152:155], a[64:67], a[48:51], 0\n" \
"ds_read_b128 a[200:203], v228 offset:0\n" \
"s_mov_b32 s44, %[m0t]\n" \
"s_mov_b32 s45, 0\n" \
"v_mfma_f32_16x16x32_bf16 v[152:155], a[68:71], a[52:55], v[152:155]\n" \
"ds_read_b128 a[204:207], v228 offset:64\n" \
"v_mfma_f32_16x16x32_bf16 v[152:155], a[72:75], a[56:59], v[152:155]\n" \
"ds_read_b128 a[208:211], v228 offset:128\n" \
"v_mfma_f32_16x16x32_bf16 v[152:155], a[76:79], a[60:63], v[152:155]\n" \
"ds_read_b128 a[212:215], v228 offset:192\n" \
"v_mfma_f32_16x16x32_bf16 v[156:159], a[92:95], a[60:63], 0\n" \
"ds_read_b128 a[216:219], v228 offset:256\n" \
"v_mfma_f32_16x16x32_bf16 v[156:159], a[88:91], a[56:59], v[156:159]\n" \
"ds_read_b128 a[220:223], v228 offset:320\n" \
"v_mfma_f32_16x16x32_bf16 v[156:159], a[84:87], a[52:55], v[156:159]\n" \
"ds_read_b128 a[224:227], v228 offset:384\n" \
"v_max3_f32 v192, v144, v145, v146\n" \
"v_max3_f32 v193, v147, v148, v149\n" \
"v_mfma_f32_16x16x32_bf16 v[156:159], a[80:83], a[48:51], v[156:159]\n" \
"ds_read_b128 a[228:231], v228 offset:448\n" \
"v_max3_f32 v192, v192, v150, v151\n" \
"v_max_f32_e64 v227, v192, v193\n" \
"s_nop 3\n" \
"s_nop 0\n" \
"v_max3_f32 v230, v152, v153, v154\n" \
"v_max3_f32 v231, v155, v156, v157\n" \
"v_max3_f32 v230, v230, v158, v159\n" \
"v_max_f32_e32 v228, v230, v231\n" \
"v_mov_b32_e32 v232, v225\n" \
"v_mov_b32_e32 v233, v226\n" \
"v_mov_b32_e32 v234, v227\n" \
"v_mov_b32_e32 v235, v228\n" \
"s_nop 0\n" \
"v_permlane16_swap_b32_e32 v225, v232\n" \
"v_permlane16_swap_b32_e32 v226, v233\n" \
"v_permlane16_swap_b32_e32 v227, v234\n" \
"v_permlane16_swap_b32_e32 v228, v235\n" \
"v_max_f32_e32 v225, v225, v232\n" \
"v_max_f32_e32 v226, v226, v233\n" \
"v_max_f32_e32 v227, v227, v234\n" \
"v_max_f32_e32 v228, v228, v235\n" \
"v_mov_b32_e32 v232, v225\n" \
"v_mov_b32_e32 v233, v226\n" \
"v_mov_b32_e32 v234, v227\n" \
"v_mov_b32_e32 v235, v228\n" \
"s_nop 0\n" \
"v_permlane32_swap_b32_e32 v225, v232\n" \
"v_permlane32_swap_b32_e32 v226, v233\n" \
"v_permlane32_swap_b32_e32 v227, v234\n" \
"v_permlane32_swap_b32_e32 v228, v235\n" \
"v_max_f32_e32 v225, v225, v232\n" \
"v_max_f32_e32 v226, v226, v233\n" \
"v_max_f32_e32 v227, v227, v234\n" \
"v_max_f32_e64 v228, v228, v235\n" \
"v_add_f32_e32 v225, 0x41800000, v225\n" \
"v_add_f32_e32 v226, 0x41800000, v226\n" \
"v_add_f32_e32 v227, 0x41800000, v227\n" \
"v_add_f32_e32 v228, 0x41800000, v228\n" \
"Lhavem_%=:\n" \
"s_nop 3\n" \
"v_mov_b64_e32 v[176:177], 0\n" \
"v_mov_b64_e32 v[178:179], 0\n" \
"v_mov_b64_e32 v[180:181], 0\n" \
"v_mov_b64_e32 v[182:183], 0\n" \
"s_and_b32 s68, s52, s69\n" \
"s_cselect_b32 s68, 0, 0x39400000\n" \
"s_waitcnt lgkmcnt(14)\n" \
"v_mfma_f32_16x16x32_bf16 v[230:233], a[128:131], a[128:131], 0\n" \
"v_sub_f32_e32 v160, 0, v225\n" \
"v_sub_f32_e32 v161, 0, v225\n" \
"v_sub_f32_e32 v162, 0, v225\n" \
"v_sub_f32_e32 v163, 0, v225\n" \
"v_sub_f32_e32 v164, 0, v226\n" \
"v_sub_f32_e32 v165, 0, v226\n" \
"v_sub_f32_e32 v166, 0, v226\n" \
"v_sub_f32_e32 v167, 0, v226\n" \
"v_sub_f32_e32 v168, 0, v227\n" \
"v_sub_f32_e32 v169, 0, v227\n" \
"v_sub_f32_e32 v170, 0, v227\n" \
"v_sub_f32_e32 v171, 0, v227\n" \
"v_sub_f32_e32 v172, 0, v228\n" \
"v_sub_f32_e32 v173, 0, v228\n" \
"v_sub_f32_e32 v174, 0, v228\n" \
"v_sub_f32_e32 v175, 0, v228\n" \
"v_sub_f32_e32 v128, v128, v225\n" \
"v_sub_f32_e32 v129, v129, v225\n" \
"v_sub_f32_e32 v130, v130, v225\n" \
"v_sub_f32_e32 v131, v131, v225\n" \
"v_sub_f32_e32 v132, v132, v225\n" \
"v_sub_f32_e32 v133, v133, v225\n" \
"v_sub_f32_e32 v134, v134, v225\n" \
"v_sub_f32_e32 v135, v135, v225\n" \
"v_sub_f32_e32 v136, v136, v226\n" \
"v_sub_f32_e32 v137, v137, v226\n" \
"v_sub_f32_e32 v138, v138, v226\n" \
"v_sub_f32_e32 v139, v139, v226\n" \
"v_sub_f32_e32 v140, v140, v226\n" \
"v_sub_f32_e32 v141, v141, v226\n" \
"v_sub_f32_e32 v142, v142, v226\n" \
"v_sub_f32_e32 v143, v143, v226\n" \
"v_sub_f32_e32 v144, v144, v227\n" \
"v_sub_f32_e32 v145, v145, v227\n" \
"v_sub_f32_e32 v146, v146, v227\n" \
"v_sub_f32_e32 v147, v147, v227\n" \
"v_sub_f32_e32 v148, v148, v227\n" \
"v_sub_f32_e32 v149, v149, v227\n" \
"v_sub_f32_e32 v150, v150, v227\n" \
"v_sub_f32_e32 v151, v151, v227\n" \
"v_sub_f32_e32 v152, v152, v228\n" \
"v_sub_f32_e32 v153, v153, v228\n" \
"v_sub_f32_e32 v154, v154, v228\n" \
"v_sub_f32_e32 v155, v155, v228\n" \
"v_sub_f32_e32 v156, v156, v228\n" \
"v_sub_f32_e32 v157, v157, v228\n" \
"v_sub_f32_e32 v158, v158, v228\n" \
"v_sub_f32_e32 v159, v159, v228\n" \
"v_exp_f32_e32 v128, v128\n" \
"v_exp_f32_e32 v129, v129\n" \
"v_exp_f32_e32 v130, v130\n" \
"v_exp_f32_e32 v131, v131\n" \
"v_exp_f32_e32 v132, v132\n" \
"v_exp_f32_e32 v133, v133\n" \
"v_exp_f32_e32 v134, v134\n" \
"v_exp_f32_e32 v135, v135\n" \
"v_exp_f32_e32 v136, v136\n" \
"v_exp_f32_e32 v137, v137\n" \
"v_exp_f32_e32 v138, v138\n" \
"v_exp_f32_e32 v139, v139\n" \
"v_exp_f32_e32 v140, v140\n" \
"v_exp_f32_e32 v141, v141\n" \
"v_exp_f32_e32 v142, v142\n" \
"v_exp_f32_e32 v143, v143\n" \
"v_exp_f32_e32 v144, v144\n" \
"v_exp_f32_e32 v145, v145\n" \
"v_exp_f32_e32 v146, v146\n" \
"v_exp_f32_e32 v147, v147\n" \
"v_exp_f32_e32 v148, v148\n" \
"v_exp_f32_e32 v149, v149\n" \
"v_exp_f32_e32 v150, v150\n" \
"v_exp_f32_e32 v151, v151\n" \
"v_exp_f32_e32 v152, v152\n" \
"v_exp_f32_e32 v153, v153\n" \
"v_exp_f32_e32 v154, v154\n" \
"v_exp_f32_e64 v155, v155\n" \
"v_bfe_i32 v228, v224, 7, 1\n" \
"v_and_b32_e32 v228, 0xaaaa, v228\n" \
"v_or_b32_e32 v228, 0x4444, v228\n" \
"v_cvt_pk_bf16_f32 v184, v128, v129\n" \
"v_cvt_pk_bf16_f32 v185, v130, v131\n" \
"v_cvt_pk_bf16_f32 v186, v132, v133\n" \
"v_cvt_pk_bf16_f32 v187, v134, v135\n" \
"v_cvt_pk_bf16_f32 v188, v136, v137\n" \
"v_cvt_pk_bf16_f32 v189, v138, v139\n" \
"v_cvt_pk_bf16_f32 v190, v140, v141\n" \
"v_cvt_pk_bf16_f32 v191, v142, v143\n" \
"v_cvt_pk_bf16_f32 v216, v144, v145\n" \
"v_cvt_pk_bf16_f32 v217, v146, v147\n" \
"v_cvt_pk_bf16_f32 v218, v148, v149\n" \
"v_cvt_pk_bf16_f32 v219, v150, v151\n" \
"v_max3_f32 v230, |v230|, |v231|, |v232|\n" \
"v_max_f32_e64 v230, v230, |v233|\n" \
"v_sqrt_f32_e32 v230, v230\n" \
"s_nop 0\n" \
"v_mul_legacy_f32 v225, s68, v230\n" \
"v_mov_b32_e32 v230, v225\n" \
"v_mov_b32_e32 v231, v225\n" \
"v_mov_b32_e32 v232, v225\n" \
"v_mov_b32_e32 v233, v225\n" \
"v_lshrrev_b32_e64 v235, 3, v224\n" \
"v_readlane_b32 s68, %[tlo], 13\n" \
"s_xor_b32 s61, %[sin], 0x2000\n" \
"s_sub_u32 s61, s61, %[sin]\n" \
"s_add_u32 s61, s61, 0x200000\n" \
"v_add_u32_e32 v226, s61, v235\n" \
"s_bitcmp0_b32 s68, 0\n" \
"s_cselect_b64 vcc, -1, 0\n" \
"s_waitcnt vmcnt(2) lgkmcnt(0)\n" \
"s_barrier\n" \
"v_mfma_f32_16x16x32_bf16 v[128:131], a[96:99], a[0:3], v[160:163]\n" \
"v_exp_f32_e64 v156, v156\n" \
"v_mfma_f32_16x16x32_bf16 v[128:131], a[100:103], a[4:7], v[128:131]\n" \
"s_add_u32 m0, %[m0b], 114688\n" \
"v_mfma_f32_16x16x32_bf16 v[128:131], a[104:107], a[8:11], v[128:131]\n" \
"v_exp_f32_e64 v157, v157\n" \
"v_mfma_f32_16x16x32_bf16 v[128:131], a[108:111], a[12:15], v[128:131]\n" \
"v_cvt_pk_bf16_f32 v220, v152, v153\n" \
"v_cvt_pk_bf16_f32 v221, v154, v155\n" \
"v_mfma_f32_16x16x32_bf16 v[132:135], a[124:127], a[12:15], v[160:163]\n" \
"v_exp_f32_e64 v158, v158\n" \
"v_mfma_f32_16x16x32_bf16 v[132:135], a[120:123], a[8:11], v[132:135]\n" \
"v_mfma_f32_16x16x32_bf16 v[132:135], a[116:119], a[4:7], v[132:135]\n" \
"buffer_load_dwordx4 %[vd0], %[srdv], s48 offen offset:0 lds\n" \
"v_mfma_f32_16x16x32_bf16 v[132:135], a[112:115], a[0:3], v[132:135]\n" \
"v_exp_f32_e64 v159, v159\n" \
"v_mfma_f32_16x16x32_bf16 v[0:3], a[128:131], v[184:187], v[230:233]\n" \
"ds_read_b128 a[64:67], %[kb0] offset:16384\n" \
"v_mfma_f32_16x16x32_bf16 v[32:35], a[128:131], v[188:191], v[230:233]\n" \
"v_exp_f32_e64 v128, v128\n" \
"v_mfma_f32_16x16x32_bf16 v[4:7], a[132:135], v[184:187], v[230:233]\n" \
"v_cvt_pk_bf16_f32 v222, v156, v157\n" \
"v_cvt_pk_bf16_f32 v223, v158, v159\n" \
"v_mfma_f32_16x16x32_bf16 v[36:39], a[132:135], v[188:191], v[230:233]\n" \
"v_exp_f32_e64 v129, v129\n" \
"v_mfma_f32_16x16x32_bf16 v[8:11], a[136:139], v[184:187], v[230:233]\n" \
"ds_read_b128 a[68:71], v208 offset:16384\n" \
"v_mfma_f32_16x16x32_bf16 v[40:43], a[136:139], v[188:191], v[230:233]\n" \
"v_exp_f32_e64 v130, v130\n" \
"v_mfma_f32_16x16x32_bf16 v[12:15], a[140:143], v[184:187], v[230:233]\n" \
"ds_read_b128 a[72:75], v209 offset:16384\n" \
"v_mfma_f32_16x16x32_bf16 v[44:47], a[140:143], v[188:191], v[230:233]\n" \
"v_exp_f32_e64 v131, v131\n" \
"v_smfmac_f32_16x16x64_bf16 v[176:179], a[192:195], v[184:191], v228\n" \
"v_mfma_f32_16x16x32_bf16 v[136:139], a[96:99], a[16:19], v[164:167]\n" \
"v_exp_f32_e64 v132, v132\n" \
"v_mfma_f32_16x16x32_bf16 v[136:139], a[100:103], a[20:23], v[136:139]\n" \
"ds_read_b128 a[76:79], v210 offset:16384\n" \
"v_mfma_f32_16x16x32_bf16 v[136:139], a[104:107], a[24:27], v[136:139]\n" \
"v_exp_f32_e64 v133, v133\n" \
"v_mfma_f32_16x16x32_bf16 v[136:139], a[108:111], a[28:31], v[136:139]\n" \
"v_cvt_pk_bf16_f32 v192, v128, v129\n" \
"v_cvt_pk_bf16_f32 v193, v130, v131\n" \
"v_mfma_f32_16x16x32_bf16 v[140:143], a[124:127], a[28:31], v[164:167]\n" \
"v_exp_f32_e64 v134, v134\n" \
"v_mfma_f32_16x16x32_bf16 v[140:143], a[120:123], a[24:27], v[140:143]\n" \
"ds_read_b128 a[92:95], v210 offset:20480\n" \
"v_mfma_f32_16x16x32_bf16 v[140:143], a[116:119], a[20:23], v[140:143]\n" \
"buffer_load_dwordx4 v215, %[srdv], s48 offen offset:1024 lds\n" \
"v_mfma_f32_16x16x32_bf16 v[140:143], a[112:115], a[16:19], v[140:143]\n" \
"v_exp_f32_e64 v135, v135\n" \
"v_mfma_f32_16x16x32_bf16 v[16:19], a[144:147], v[184:187], v[230:233]\n" \
"ds_read_b128 a[88:91], v209 offset:20480\n" \
"v_mfma_f32_16x16x32_bf16 v[48:51], a[144:147], v[188:191], v[230:233]\n" \
"v_exp_f32_e64 v136, v136\n" \
"v_mfma_f32_16x16x32_bf16 v[20:23], a[148:151], v[184:187], v[230:233]\n" \
"v_cvt_pk_bf16_f32 v194, v132, v133\n" \
"v_cvt_pk_bf16_f32 v195, v134, v135\n" \
"v_mfma_f32_16x16x32_bf16 v[52:55], a[148:151], v[188:191], v[230:233]\n" \
"v_exp_f32_e64 v137, v137\n" \
"v_mfma_f32_16x16x32_bf16 v[24:27], a[152:155], v[184:187], v[230:233]\n" \
"v_exp_f32_e64 v138, v138\n" \
"v_mfma_f32_16x16x32_bf16 v[56:59], a[152:155], v[188:191], v[230:233]\n" \
"ds_read_b128 a[84:87], v208 offset:20480\n" \
"v_mfma_f32_16x16x32_bf16 v[28:31], a[156:159], v[184:187], v[230:233]\n" \
"v_exp_f32_e64 v139, v139\n" \
"v_mfma_f32_16x16x32_bf16 v[60:63], a[156:159], v[188:191], v[230:233]\n" \
"v_mfma_f32_16x16x32_bf16 v[144:147], a[96:99], a[32:35], v[168:171]\n" \
"v_exp_f32_e64 v140, v140\n" \
"v_mfma_f32_16x16x32_bf16 v[144:147], a[100:103], a[36:39], v[144:147]\n" \
"ds_read_b128 a[80:83], %[kb0] offset:20480\n" \
"v_mfma_f32_16x16x32_bf16 v[144:147], a[104:107], a[40:43], v[144:147]\n" \
"v_exp_f32_e64 v141, v141\n" \
"v_mfma_f32_16x16x32_bf16 v[144:147], a[108:111], a[44:47], v[144:147]\n" \
"v_cvt_pk_bf16_f32 v196, v136, v137\n" \
"v_cvt_pk_bf16_f32 v197, v138, v139\n" \
"v_mfma_f32_16x16x32_bf16 v[148:151], a[124:127], a[44:47], v[168:171]\n" \
"v_exp_f32_e64 v142, v142\n" \
"v_mfma_f32_16x16x32_bf16 v[148:151], a[120:123], a[40:43], v[148:151]\n" \
"v_mfma_f32_16x16x32_bf16 v[148:151], a[116:119], a[36:39], v[148:151]\n" \
"buffer_load_dwordx4 %[vd0], %[srdv], s48 offen offset:2048 lds\n" \
"v_mfma_f32_16x16x32_bf16 v[148:151], a[112:115], a[32:35], v[148:151]\n" \
"v_exp_f32_e64 v143, v143\n" \
"v_mfma_f32_16x16x32_bf16 v[64:67], a[128:131], v[216:219], v[230:233]\n" \
"ds_read_b64_tr_b16 a[160:161], %[vvb] offset:8192\n" \
"v_mfma_f32_16x16x32_bf16 v[96:99], a[128:131], v[220:223], v[230:233]\n" \
"v_exp_f32_e64 v144, v144\n" \
"v_mfma_f32_16x16x32_bf16 v[68:71], a[132:135], v[216:219], v[230:233]\n" \
"v_cvt_pk_bf16_f32 v198, v140, v141\n" \
"v_cvt_pk_bf16_f32 v199, v142, v143\n" \
"v_mfma_f32_16x16x32_bf16 v[100:103], a[132:135], v[220:223], v[230:233]\n" \
"v_exp_f32_e64 v145, v145\n" \
"v_mfma_f32_16x16x32_bf16 v[72:75], a[136:139], v[216:219], v[230:233]\n" \
"ds_read_b64_tr_b16 a[162:163], %[vvb] offset:12288\n" \
"ds_read_b64_tr_b16 a[164:165], v211 offset:8192\n" \
"v_mfma_f32_16x16x32_bf16 v[104:107], a[136:139], v[220:223], v[230:233]\n" \
"v_exp_f32_e64 v146, v146\n" \
"v_mfma_f32_16x16x32_bf16 v[76:79], a[140:143], v[216:219], v[230:233]\n" \
"ds_read_b64_tr_b16 a[166:167], v211 offset:12288\n" \
"v_mfma_f32_16x16x32_bf16 v[108:111], a[140:143], v[220:223], v[230:233]\n" \
"v_exp_f32_e64 v147, v147\n" \
"v_smfmac_f32_16x16x64_bf16 v[180:183], a[192:195], v[216:223], v228\n" \
"ds_read_b64_tr_b16 a[168:169], %[vvb] offset:8704\n" \
"v_mfma_f32_16x16x32_bf16 v[152:155], a[96:99], a[48:51], v[172:175]\n" \
"v_exp_f32_e64 v148, v148\n" \
"v_mfma_f32_16x16x32_bf16 v[152:155], a[100:103], a[52:55], v[152:155]\n" \
"ds_read_b64_tr_b16 a[170:171], %[vvb] offset:12800\n" \
"v_mfma_f32_16x16x32_bf16 v[152:155], a[104:107], a[56:59], v[152:155]\n" \
"v_exp_f32_e64 v149, v149\n" \
"v_mfma_f32_16x16x32_bf16 v[152:155], a[108:111], a[60:63], v[152:155]\n" \
"v_cvt_pk_bf16_f32 v200, v144, v145\n" \
"v_cvt_pk_bf16_f32 v201, v146, v147\n" \
"v_mfma_f32_16x16x32_bf16 v[156:159], a[124:127], a[60:63], v[172:175]\n" \
"v_exp_f32_e64 v150, v150\n" \
"v_mfma_f32_16x16x32_bf16 v[156:159], a[120:123], a[56:59], v[156:159]\n" \
"ds_read_b64_tr_b16 a[172:173], v211 offset:8704\n" \
"v_mfma_f32_16x16x32_bf16 v[156:159], a[116:119], a[52:55], v[156:159]\n" \
"buffer_load_dwordx4 v215, %[srdv], s48 offen offset:3072 lds\n" \
"v_mfma_f32_16x16x32_bf16 v[156:159], a[112:115], a[48:51], v[156:159]\n" \
"v_exp_f32_e64 v151, v151\n" \
"v_mfma_f32_16x16x32_bf16 v[80:83], a[144:147], v[216:219], v[230:233]\n" \
"ds_read_b64_tr_b16 a[174:175], v211 offset:12800\n" \
"ds_read_b64_tr_b16 a[176:177], %[vvb] offset:9216\n" \
"v_mfma_f32_16x16x32_bf16 v[112:115], a[144:147], v[220:223], v[230:233]\n" \
"v_exp_f32_e64 v152, v152\n" \
"v_mfma_f32_16x16x32_bf16 v[84:87], a[148:151], v[216:219], v[230:233]\n" \
"v_cvt_pk_bf16_f32 v202, v148, v149\n" \
"v_cvt_pk_bf16_f32 v203, v150, v151\n" \
"v_mfma_f32_16x16x32_bf16 v[116:119], a[148:151], v[220:223], v[230:233]\n" \
"v_exp_f32_e64 v153, v153\n" \
"v_mfma_f32_16x16x32_bf16 v[88:91], a[152:155], v[216:219], v[230:233]\n" \
"v_exp_f32_e64 v154, v154\n" \
"v_mfma_f32_16x16x32_bf16 v[120:123], a[152:155], v[220:223], v[230:233]\n" \
"ds_read_b64_tr_b16 a[178:179], %[vvb] offset:13312\n" \
"v_mfma_f32_16x16x32_bf16 v[92:95], a[156:159], v[216:219], v[230:233]\n" \
"v_exp_f32_e64 v155, v155\n" \
"v_mov_b64_e32 v[216:217], 0\n" \
"v_mov_b64_e32 v[218:219], 0\n" \
"v_mfma_f32_16x16x32_bf16 v[124:127], a[156:159], v[220:223], v[230:233]\n" \
"ds_read_b64_tr_b16 a[180:181], v211 offset:9216\n" \
"s_waitcnt lgkmcnt(13)\n" \
"s_nop 0\n" \
"LmA_%=:\n" \
"v_mfma_f32_16x16x32_bf16 v[128:131], a[64:67], a[0:3], v[160:163]\n" \
"v_exp_f32_e64 v156, v156\n" \
"v_mfma_f32_16x16x32_bf16 v[128:131], a[68:71], a[4:7], v[128:131]\n" \
"ds_read_b64_tr_b16 a[182:183], v211 offset:13312\n" \
"s_add_u32 m0, %[m0b], 0\n" \
"v_mfma_f32_16x16x32_bf16 v[128:131], a[72:75], a[8:11], v[128:131]\n" \
"v_exp_f32_e32 v157, v157\n" \
"v_mfma_f32_16x16x32_bf16 v[128:131], a[76:79], a[12:15], v[128:131]\n" \
"v_cvt_pk_bf16_f32 v204, v152, v153\n" \
"v_cvt_pk_bf16_f32 v205, v154, v155\n" \
"v_mfma_f32_16x16x32_bf16 v[132:135], a[92:95], a[12:15], v[160:163]\n" \
"v_exp_f32_e64 v158, v158\n" \
"v_mfma_f32_16x16x32_bf16 v[132:135], a[88:91], a[8:11], v[132:135]\n" \
"ds_read_b64_tr_b16 a[184:185], %[vvb] offset:9728\n" \
"s_waitcnt lgkmcnt(5)\n" \
"v_mfma_f32_16x16x32_bf16 v[132:135], a[84:87], a[4:7], v[132:135]\n" \
"buffer_load_dwordx4 %[kd0], %[srdk], s48 offen offset:0 lds\n" \
"v_mfma_f32_16x16x32_bf16 v[132:135], a[80:83], a[0:3], v[132:135]\n" \
"v_exp_f32_e32 v159, v159\n" \
"v_mfma_f32_16x16x32_bf16 v[64:67], a[128:131], v[216:219], v[64:67]\n" \
"ds_read_b64_tr_b16 a[186:187], %[vvb] offset:13824\n" \
"ds_read_b64_tr_b16 a[188:189], v211 offset:9728\n" \
"v_mfma_f32_16x16x32_bf16 v[64:67], a[160:163], v[200:203], v[64:67]\n" \
"v_exp_f32_e64 v128, v128\n" \
"v_mfma_f32_16x16x32_bf16 v[68:71], a[132:135], v[216:219], v[68:71]\n" \
"v_cvt_pk_bf16_f32 v206, v156, v157\n" \
"v_cvt_pk_bf16_f32 v207, v158, v159\n" \
"v_mfma_f32_16x16x32_bf16 v[68:71], a[164:167], v[200:203], v[68:71]\n" \
"v_exp_f32_e64 v129, v129\n" \
"v_mfma_f32_16x16x32_bf16 v[72:75], a[136:139], v[216:219], v[72:75]\n" \
"ds_read_b64_tr_b16 a[190:191], v211 offset:13824\n" \
"ds_read_b64_tr_b16 a[128:129], %[vvb] offset:16384\n" \
"s_add_u32 s60, s60, 1\n" \
"s_nop 0\n" \
"v_readlane_b32 s58, %[tlo], s60\n" \
"v_mfma_f32_16x16x32_bf16 v[72:75], a[168:171], v[200:203], v[72:75]\n" \
"v_exp_f32_e64 v130, v130\n" \
"v_mfma_f32_16x16x32_bf16 v[76:79], a[140:143], v[216:219], v[76:79]\n" \
"ds_read_b64_tr_b16 a[130:131], %[vvb] offset:20480\n" \
"ds_read_b64_tr_b16 a[132:133], v211 offset:16384\n" \
"v_readlane_b32 s59, %[thi], s60\n" \
"v_mfma_f32_16x16x32_bf16 v[76:79], a[172:175], v[200:203], v[76:79]\n" \
"v_exp_f32_e64 v131, v131\n" \
".if . - adaln_body != 12288\n" \
".error \"size model\"\n" \
".endif\n" \
"Lk2_%=:\n" \
"s_cbranch_execz Lk3_%=\n" \
"s_nop 0\n" \
"v_smfmac_f32_16x16x64_bf16 v[176:179], a[192:195], v[192:199], v228\n" \
"ds_read_b64_tr_b16 a[134:135], v211 offset:20480\n" \
"ds_read_b64_tr_b16 a[136:137], %[vvb] offset:16896\n" \
"v_mfma_f32_16x16x32_bf16 v[136:139], a[64:67], a[16:19], v[164:167]\n" \
"v_exp_f32_e64 v132, v132\n" \
"v_mfma_f32_16x16x32_bf16 v[136:139], a[68:71], a[20:23], v[136:139]\n" \
"ds_read_b64_tr_b16 a[138:139], %[vvb] offset:20992\n" \
"ds_read_b64_tr_b16 a[140:141], v211 offset:16896\n" \
"v_mfma_f32_16x16x32_bf16 v[136:139], a[72:75], a[24:27], v[136:139]\n" \
"v_exp_f32_e64 v133, v133\n" \
"v_mfma_f32_16x16x32_bf16 v[136:139], a[76:79], a[28:31], v[136:139]\n" \
"v_cvt_pk_bf16_f32 v184, v128, v129\n" \
"v_cvt_pk_bf16_f32 v185, v130, v131\n" \
"v_mfma_f32_16x16x32_bf16 v[140:143], a[92:95], a[28:31], v[164:167]\n" \
"v_exp_f32_e64 v134, v134\n" \
"v_mfma_f32_16x16x32_bf16 v[140:143], a[88:91], a[24:27], v[140:143]\n" \
"ds_read_b64_tr_b16 a[142:143], v211 offset:20992\n" \
"s_waitcnt lgkmcnt(8)\n" \
"v_mfma_f32_16x16x32_bf16 v[140:143], a[84:87], a[20:23], v[140:143]\n" \
"buffer_load_dwordx4 v212, %[srdk], s48 offen offset:1024 lds\n" \
"v_mfma_f32_16x16x32_bf16 v[140:143], a[80:83], a[16:19], v[140:143]\n" \
"v_exp_f32_e32 v135, v135\n" \
"v_mfma_f32_16x16x32_bf16 v[80:83], a[144:147], v[216:219], v[80:83]\n" \
"ds_read_b64_tr_b16 a[144:145], %[vvb] offset:17408\n" \
"ds_read_b64_tr_b16 a[146:147], %[vvb] offset:21504\n" \
"v_mfma_f32_16x16x32_bf16 v[80:83], a[176:179], v[200:203], v[80:83]\n" \
"v_exp_f32_e64 v136, v136\n" \
"v_mfma_f32_16x16x32_bf16 v[84:87], a[148:151], v[216:219], v[84:87]\n" \
"v_cvt_pk_bf16_f32 v186, v132, v133\n" \
"v_cvt_pk_bf16_f32 v187, v134, v135\n" \
"v_mfma_f32_16x16x32_bf16 v[84:87], a[180:183], v[200:203], v[84:87]\n" \
"v_exp_f32_e64 v137, v137\n" \
"v_mfma_f32_16x16x32_bf16 v[88:91], a[152:155], v[216:219], v[88:91]\n" \
"v_exp_f32_e64 v138, v138\n" \
"v_mfma_f32_16x16x32_bf16 v[88:91], a[184:187], v[200:203], v[88:91]\n" \
"ds_read_b64_tr_b16 a[148:149], v211 offset:17408\n" \
"ds_read_b64_tr_b16 a[150:151], v211 offset:21504\n" \
"v_mfma_f32_16x16x32_bf16 v[92:95], a[156:159], v[216:219], v[92:95]\n" \
"v_exp_f32_e64 v139, v139\n" \
"v_mfma_f32_16x16x32_bf16 v[92:95], a[188:191], v[200:203], v[92:95]\n" \
"ds_read_b64_tr_b16 a[152:153], %[vvb] offset:17920\n" \
"v_mfma_f32_16x16x32_bf16 v[144:147], a[64:67], a[32:35], v[168:171]\n" \
"v_exp_f32_e64 v140, v140\n" \
"v_mfma_f32_16x16x32_bf16 v[144:147], a[68:71], a[36:39], v[144:147]\n" \
"ds_read_b64_tr_b16 a[154:155], %[vvb] offset:22016\n" \
"ds_read_b64_tr_b16 a[156:157], v211 offset:17920\n" \
"v_mfma_f32_16x16x32_bf16 v[144:147], a[72:75], a[40:43], v[144:147]\n" \
"v_exp_f32_e64 v141, v141\n" \
"v_mfma_f32_16x16x32_bf16 v[144:147], a[76:79], a[44:47], v[144:147]\n" \
"v_cvt_pk_bf16_f32 v188, v136, v137\n" \
"v_cvt_pk_bf16_f32 v189, v138, v139\n" \
"v_mfma_f32_16x16x32_bf16 v[148:151], a[92:95], a[44:47], v[168:171]\n" \
"v_exp_f32_e64 v142, v142\n" \
"v_mfma_f32_16x16x32_bf16 v[148:151], a[88:91], a[40:43], v[148:151]\n" \
"ds_read_b64_tr_b16 a[158:159], v211 offset:22016\n" \
"s_waitcnt lgkmcnt(8)\n" \
"v_mfma_f32_16x16x32_bf16 v[148:151], a[84:87], a[36:39], v[148:151]\n" \
"buffer_load_dwordx4 v213, %[srdk], s48 offen offset:2048 lds\n" \
"v_mfma_f32_16x16x32_bf16 v[148:151], a[80:83], a[32:35], v[148:151]\n" \
"v_exp_f32_e32 v143, v143\n" \
"v_mfma_f32_16x16x32_bf16 v[0:3], a[160:163], v[192:195], v[0:3]\n" \
"ds_read_b128 a[96:99], %[kb0] offset:24576\n" \
"v_mfma_f32_16x16x32_bf16 v[0:3], a[128:131], v[184:187], v[0:3]\n" \
"v_exp_f32_e64 v144, v144\n" \
"v_mfma_f32_16x16x32_bf16 v[4:7], a[164:167], v[192:195], v[4:7]\n" \
"v_cvt_pk_bf16_f32 v190, v140, v141\n" \
"v_cvt_pk_bf16_f32 v191, v142, v143\n" \
"v_mfma_f32_16x16x32_bf16 v[4:7], a[132:135], v[184:187], v[4:7]\n" \
"v_exp_f32_e64 v145, v145\n" \
"v_mfma_f32_16x16x32_bf16 v[8:11], a[168:171], v[192:195], v[8:11]\n" \
"ds_read_b128 a[100:103], v208 offset:24576\n" \
"v_mfma_f32_16x16x32_bf16 v[8:11], a[136:139], v[184:187], v[8:11]\n" \
"v_exp_f32_e64 v146, v146\n" \
"v_mfma_f32_16x16x32_bf16 v[12:15], a[172:175], v[192:195], v[12:15]\n" \
"ds_read_b128 a[104:107], v209 offset:24576\n" \
"v_mfma_f32_16x16x32_bf16 v[12:15], a[140:143], v[184:187], v[12:15]\n" \
"v_exp_f32_e64 v147, v147\n" \
"v_smfmac_f32_16x16x64_bf16 v[180:183], a[192:195], v[200:207], v228\n" \
"ds_read_b128 a[108:111], v210 offset:24576\n" \
"v_mfma_f32_16x16x32_bf16 v[152:155], a[64:67], a[48:51], v[172:175]\n" \
"v_exp_f32_e64 v148, v148\n" \
"v_mfma_f32_16x16x32_bf16 v[152:155], a[68:71], a[52:55], v[152:155]\n" \
"ds_read_b128 a[124:127], v210 offset:28672\n" \
"v_mfma_f32_16x16x32_bf16 v[152:155], a[72:75], a[56:59], v[152:155]\n" \
"v_exp_f32_e64 v149, v149\n" \
"v_mfma_f32_16x16x32_bf16 v[152:155], a[76:79], a[60:63], v[152:155]\n" \
"v_cvt_pk_bf16_f32 v216, v144, v145\n" \
"v_cvt_pk_bf16_f32 v217, v146, v147\n" \
"v_mfma_f32_16x16x32_bf16 v[156:159], a[92:95], a[60:63], v[172:175]\n" \
"v_exp_f32_e64 v150, v150\n" \
"v_mfma_f32_16x16x32_bf16 v[156:159], a[88:91], a[56:59], v[156:159]\n" \
"ds_read_b128 a[120:123], v209 offset:28672\n" \
"s_waitcnt lgkmcnt(6)\n" \
"v_mfma_f32_16x16x32_bf16 v[156:159], a[84:87], a[52:55], v[156:159]\n" \
"buffer_load_dwordx4 v214, %[srdk], s48 offen offset:3072 lds\n" \
"v_mfma_f32_16x16x32_bf16 v[156:159], a[80:83], a[48:51], v[156:159]\n" \
"v_exp_f32_e32 v151, v151\n" \
"v_mfma_f32_16x16x32_bf16 v[16:19], a[176:179], v[192:195], v[16:19]\n" \
"ds_read_b128 a[116:119], v208 offset:28672\n" \
"v_mfma_f32_16x16x32_bf16 v[16:19], a[144:147], v[184:187], v[16:19]\n" \
"v_exp_f32_e64 v152, v152\n" \
"v_mfma_f32_16x16x32_bf16 v[20:23], a[180:183], v[192:195], v[20:23]\n" \
"v_cvt_pk_bf16_f32 v218, v148, v149\n" \
"v_cvt_pk_bf16_f32 v219, v150, v151\n" \
"v_mfma_f32_16x16x32_bf16 v[20:23], a[148:151], v[184:187], v[20:23]\n" \
"v_exp_f32_e64 v153, v153\n" \
"v_mfma_f32_16x16x32_bf16 v[24:27], a[184:187], v[192:195], v[24:27]\n" \
"v_exp_f32_e64 v154, v154\n" \
"v_mfma_f32_16x16x32_bf16 v[24:27], a[152:155], v[184:187], v[24:27]\n" \
"ds_read_b128 a[112:115], %[kb0] offset:28672\n" \
"s_add_u32 s48, s48, 0x4000\n" \
"s_mov_b32 m0, s44\n" \
"s_addk_i32 s44, 0x400\n" \
"buffer_load_dwordx4 v229, s[64:67], s45 offen offset:0 nt lds\n" \
"s_add_u32 s45, s45, 393216\n" \
"v_mfma_f32_16x16x32_bf16 v[28:31], a[188:191], v[192:195], v[28:31]\n" \
"v_exp_f32_e64 v155, v155\n" \
"v_mfma_f32_16x16x32_bf16 v[28:31], a[156:159], v[184:187], v[28:31]\n" \
"s_waitcnt vmcnt(13) lgkmcnt(2)\n" \
"s_barrier\n" \
"v_mfma_f32_16x16x32_bf16 v[128:131], a[96:99], a[0:3], v[160:163]\n" \
"v_exp_f32_e64 v156, v156\n" \
"v_mfma_f32_16x16x32_bf16 v[128:131], a[100:103], a[4:7], v[128:131]\n" \
"s_add_u32 m0, %[m0b], 65536\n" \
"v_mfma_f32_16x16x32_bf16 v[128:131], a[104:107], a[8:11], v[128:131]\n" \
"v_exp_f32_e64 v157, v157\n" \
"v_mfma_f32_16x16x32_bf16 v[128:131], a[108:111], a[12:15], v[128:131]\n" \
"v_cvt_pk_bf16_f32 v220, v152, v153\n" \
"v_cvt_pk_bf16_f32 v221, v154, v155\n" \
"v_mfma_f32_16x16x32_bf16 v[132:135], a[124:127], a[12:15], v[160:163]\n" \
"v_exp_f32_e64 v158, v158\n" \
"v_mfma_f32_16x16x32_bf16 v[132:135], a[120:123], a[8:11], v[132:135]\n" \
"s_waitcnt lgkmcnt(0)\n" \
"v_mfma_f32_16x16x32_bf16 v[132:135], a[116:119], a[4:7], v[132:135]\n" \
"buffer_load_dwordx4 %[vd0], %[srdv], s48 offen offset:0 lds\n" \
"v_mfma_f32_16x16x32_bf16 v[132:135], a[112:115], a[0:3], v[132:135]\n" \
"v_exp_f32_e32 v159, v159\n" \
"v_mfma_f32_16x16x32_bf16 v[32:35], a[160:163], v[196:199], v[32:35]\n" \
"ds_read_b128 a[64:67], %[kb0] offset:32768\n" \
"v_mfma_f32_16x16x32_bf16 v[32:35], a[128:131], v[188:191], v[32:35]\n" \
"v_exp_f32_e64 v128, v128\n" \
"v_mfma_f32_16x16x32_bf16 v[36:39], a[164:167], v[196:199], v[36:39]\n" \
"v_cvt_pk_bf16_f32 v222, v156, v157\n" \
"v_cvt_pk_bf16_f32 v223, v158, v159\n" \
"v_mfma_f32_16x16x32_bf16 v[36:39], a[132:135], v[188:191], v[36:39]\n" \
"v_exp_f32_e64 v129, v129\n" \
"v_mfma_f32_16x16x32_bf16 v[40:43], a[168:171], v[196:199], v[40:43]\n" \
"ds_read_b128 a[68:71], v208 offset:32768\n" \
"v_mfma_f32_16x16x32_bf16 v[40:43], a[136:139], v[188:191], v[40:43]\n" \
"v_exp_f32_e64 v130, v130\n" \
"v_mfma_f32_16x16x32_bf16 v[44:47], a[172:175], v[196:199], v[44:47]\n" \
"ds_read_b128 a[72:75], v209 offset:32768\n" \
"v_mfma_f32_16x16x32_bf16 v[44:47], a[140:143], v[188:191], v[44:47]\n" \
"v_exp_f32_e64 v131, v131\n" \
"v_smfmac_f32_16x16x64_bf16 v[176:179], a[192:195], v[184:191], v228\n" \
"v_mfma_f32_16x16x32_bf16 v[136:139], a[96:99], a[16:19], v[164:167]\n" \
"v_exp_f32_e64 v132, v132\n" \
"v_mfma_f32_16x16x32_bf16 v[136:139], a[100:103], a[20:23], v[136:139]\n" \
"ds_read_b128 a[76:79], v210 offset:32768\n" \
"v_mfma_f32_16x16x32_bf16 v[136:139], a[104:107], a[24:27], v[136:139]\n" \
"v_exp_f32_e64 v133, v133\n" \
"v_mfma_f32_16x16x32_bf16 v[136:139], a[108:111], a[28:31], v[136:139]\n" \
"v_cvt_pk_bf16_f32 v192, v128, v129\n" \
"v_cvt_pk_bf16_f32 v193, v130, v131\n" \
"v_mfma_f32_16x16x32_bf16 v[140:143], a[124:127], a[28:31], v[164:167]\n" \
"v_exp_f32_e64 v134, v134\n" \
"v_mfma_f32_16x16x32_bf16 v[140:143], a[120:123], a[24:27], v[140:143]\n" \
"ds_read_b128 a[92:95], v210 offset:36864\n" \
"v_mfma_f32_16x16x32_bf16 v[140:143], a[116:119], a[20:23], v[140:143]\n" \
"buffer_load_dwordx4 v215, %[srdv], s48 offen offset:1024 lds\n" \
"v_mfma_f32_16x16x32_bf16 v[140:143], a[112:115], a[16:19], v[140:143]\n" \
"v_exp_f32_e64 v135, v135\n" \
"v_mfma_f32_16x16x32_bf16 v[48:51], a[176:179], v[196:199], v[48:51]\n" \
"ds_read_b128 a[88:91], v209 offset:36864\n" \
"v_mfma_f32_16x16x32_bf16 v[48:51], a[144:147], v[188:191], v[48:51]\n" \
"v_exp_f32_e64 v136, v136\n" \
"v_mfma_f32_16x16x32_bf16 v[52:55], a[180:183], v[196:199], v[52:55]\n" \
"v_cvt_pk_bf16_f32 v194, v132, v133\n" \
"v_cvt_pk_bf16_f32 v195, v134, v135\n" \
"v_mfma_f32_16x16x32_bf16 v[52:55], a[148:151], v[188:191], v[52:55]\n" \
"v_exp_f32_e64 v137, v137\n" \
"v_mfma_f32_16x16x32_bf16 v[56:59], a[184:187], v[196:199], v[56:59]\n" \
"v_exp_f32_e64 v138, v138\n" \
"v_mfma_f32_16x16x32_bf16 v[56:59], a[152:155], v[188:191], v[56:59]\n" \
"ds_read_b128 a[84:87], v208 offset:36864\n" \
"v_mfma_f32_16x16x32_bf16 v[60:63], a[188:191], v[196:199], v[60:63]\n" \
"v_exp_f32_e64 v139, v139\n" \
"v_mfma_f32_16x16x32_bf16 v[60:63], a[156:159], v[188:191], v[60:63]\n" \
"s_cmp_lg_u32 %[wid], 0\n" \
"s_cbranch_scc1 LtA03_%=\n" \
"global_load_dword v237, v224, s[58:59]\n" \
"s_add_u32 s61, s48, 49152\n" \
"s_mov_b32 exec_hi, 0\n" \
"s_nop 0\n" \
"buffer_load_dword v237, v224, %[srdp], s61 offen\n" \
"s_mov_b32 exec_hi, -1\n" \
"s_nop 0\n" \
"LtA03_%=:\n" \
"v_mfma_f32_16x16x32_bf16 v[144:147], a[96:99], a[32:35], v[168:171]\n" \
"v_exp_f32_e64 v140, v140\n" \
"v_mfma_f32_16x16x32_bf16 v[144:147], a[100:103], a[36:39], v[144:147]\n" \
"ds_read_b128 a[80:83], %[kb0] offset:36864\n" \
"v_mfma_f32_16x16x32_bf16 v[144:147], a[104:107], a[40:43], v[144:147]\n" \
"v_exp_f32_e64 v141, v141\n" \
"v_mfma_f32_16x16x32_bf16 v[144:147], a[108:111], a[44:47], v[144:147]\n" \
"v_cvt_pk_bf16_f32 v196, v136, v137\n" \
"v_cvt_pk_bf16_f32 v197, v138, v139\n" \
"v_mfma_f32_16x16x32_bf16 v[148:151], a[124:127], a[44:47], v[168:171]\n" \
"v_exp_f32_e64 v142, v142\n" \
"v_mfma_f32_16x16x32_bf16 v[148:151], a[120:123], a[40:43], v[148:151]\n" \
"v_mfma_f32_16x16x32_bf16 v[148:151], a[116:119], a[36:39], v[148:151]\n" \
"buffer_load_dwordx4 %[vd0], %[srdv], s48 offen offset:2048 lds\n" \
"v_mfma_f32_16x16x32_bf16 v[148:151], a[112:115], a[32:35], v[148:151]\n" \
"v_exp_f32_e64 v143, v143\n" \
"v_mfma_f32_16x16x32_bf16 v[96:99], a[160:163], v[204:207], v[96:99]\n" \
"ds_read_b64_tr_b16 a[160:161], %[vvb] offset:24576\n" \
"v_mfma_f32_16x16x32_bf16 v[96:99], a[128:131], v[220:223], v[96:99]\n" \
"v_exp_f32_e64 v144, v144\n" \
"v_mfma_f32_16x16x32_bf16 v[100:103], a[164:167], v[204:207], v[100:103]\n" \
"v_cvt_pk_bf16_f32 v198, v140, v141\n" \
"v_cvt_pk_bf16_f32 v199, v142, v143\n" \
"v_mfma_f32_16x16x32_bf16 v[100:103], a[132:135], v[220:223], v[100:103]\n" \
"v_exp_f32_e64 v145, v145\n" \
"v_mfma_f32_16x16x32_bf16 v[104:107], a[168:171], v[204:207], v[104:107]\n" \
"ds_read_b64_tr_b16 a[162:163], %[vvb] offset:28672\n" \
"ds_read_b64_tr_b16 a[164:165], v211 offset:24576\n" \
"v_mfma_f32_16x16x32_bf16 v[104:107], a[136:139], v[220:223], v[104:107]\n" \
"v_exp_f32_e64 v146, v146\n" \
"v_mfma_f32_16x16x32_bf16 v[108:111], a[172:175], v[204:207], v[108:111]\n" \
"ds_read_b64_tr_b16 a[166:167], v211 offset:28672\n" \
"v_mfma_f32_16x16x32_bf16 v[108:111], a[140:143], v[220:223], v[108:111]\n" \
"v_exp_f32_e64 v147, v147\n" \
"v_smfmac_f32_16x16x64_bf16 v[180:183], a[192:195], v[216:223], v228\n" \
"ds_read_b64_tr_b16 a[168:169], %[vvb] offset:25088\n" \
"v_mfma_f32_16x16x32_bf16 v[152:155], a[96:99], a[48:51], v[172:175]\n" \
"v_exp_f32_e64 v148, v148\n" \
"v_mfma_f32_16x16x32_bf16 v[152:155], a[100:103], a[52:55], v[152:155]\n" \
"ds_read_b64_tr_b16 a[170:171], %[vvb] offset:29184\n" \
"v_mfma_f32_16x16x32_bf16 v[152:155], a[104:107], a[56:59], v[152:155]\n" \
"v_exp_f32_e64 v149, v149\n" \
"v_mfma_f32_16x16x32_bf16 v[152:155], a[108:111], a[60:63], v[152:155]\n" \
"v_cvt_pk_bf16_f32 v200, v144, v145\n" \
"v_cvt_pk_bf16_f32 v201, v146, v147\n" \
"v_mfma_f32_16x16x32_bf16 v[156:159], a[124:127], a[60:63], v[172:175]\n" \
"v_exp_f32_e64 v150, v150\n" \
"v_mfma_f32_16x16x32_bf16 v[156:159], a[120:123], a[56:59], v[156:159]\n" \
"ds_read_b64_tr_b16 a[172:173], v211 offset:25088\n" \
"v_mfma_f32_16x16x32_bf16 v[156:159], a[116:119], a[52:55], v[156:159]\n" \
"buffer_load_dwordx4 v215, %[srdv], s48 offen offset:3072 lds\n" \
"v_mfma_f32_16x16x32_bf16 v[156:159], a[112:115], a[48:51], v[156:159]\n" \
"v_exp_f32_e64 v151, v151\n" \
"v_mfma_f32_16x16x32_bf16 v[112:115], a[176:179], v[204:207], v[112:115]\n" \
"ds_read_b64_tr_b16 a[174:175], v211 offset:29184\n" \
"ds_read_b64_tr_b16 a[176:177], %[vvb] offset:25600\n" \
"v_mfma_f32_16x16x32_bf16 v[112:115], a[144:147], v[220:223], v[112:115]\n" \
"v_exp_f32_e64 v152, v152\n" \
"v_mfma_f32_16x16x32_bf16 v[116:119], a[180:183], v[204:207], v[116:119]\n" \
"v_cvt_pk_bf16_f32 v202, v148, v149\n" \
"v_cvt_pk_bf16_f32 v203, v150, v151\n" \
"v_mfma_f32_16x16x32_bf16 v[116:119], a[148:151], v[220:223], v[116:119]\n" \
"v_exp_f32_e64 v153, v153\n" \
"v_mfma_f32_16x16x32_bf16 v[120:123], a[184:187], v[204:207], v[120:123]\n" \
"v_exp_f32_e64 v154, v154\n" \
"v_mfma_f32_16x16x32_bf16 v[120:123], a[152:155], v[220:223], v[120:123]\n" \
"ds_read_b64_tr_b16 a[178:179], %[vvb] offset:29696\n" \
"v_mfma_f32_16x16x32_bf16 v[124:127], a[188:191], v[204:207], v[124:127]\n" \
"v_exp_f32_e64 v155, v155\n" \
"v_mfma_f32_16x16x32_bf16 v[124:127], a[156:159], v[220:223], v[124:127]\n" \
"ds_read_b64_tr_b16 a[180:181], v211 offset:25600\n" \
"s_waitcnt lgkmcnt(13)\n" \
"v_mfma_f32_16x16x32_bf16 v[128:131], a[64:67], a[0:3], v[160:163]\n" \
"v_exp_f32_e32 v156, v156\n" \
"v_mfma_f32_16x16x32_bf16 v[128:131], a[68:71], a[4:7], v[128:131]\n" \
"ds_read_b64_tr_b16 a[182:183], v211 offset:29696\n" \
"s_add_u32 m0, %[m0b], 16384\n" \
"v_mfma_f32_16x16x32_bf16 v[128:131], a[72:75], a[8:11], v[128:131]\n" \
"v_exp_f32_e64 v157, v157\n" \
"v_mfma_f32_16x16x32_bf16 v[128:131], a[76:79], a[12:15], v[128:131]\n" \
"v_cvt_pk_bf16_f32 v204, v152, v153\n" \
"v_cvt_pk_bf16_f32 v205, v154, v155\n" \
"v_mfma_f32_16x16x32_bf16 v[132:135], a[92:95], a[12:15], v[160:163]\n" \
"v_exp_f32_e64 v158, v158\n" \
"v_mfma_f32_16x16x32_bf16 v[132:135], a[88:91], a[8:11], v[132:135]\n" \
"ds_read_b64_tr_b16 a[184:185], %[vvb] offset:26112\n" \
"s_waitcnt lgkmcnt(5)\n" \
"v_mfma_f32_16x16x32_bf16 v[132:135], a[84:87], a[4:7], v[132:135]\n" \
"buffer_load_dwordx4 %[kd0], %[srdk], s48 offen offset:0 lds\n" \
"v_mfma_f32_16x16x32_bf16 v[132:135], a[80:83], a[0:3], v[132:135]\n" \
"v_exp_f32_e32 v159, v159\n" \
"v_mfma_f32_16x16x32_bf16 v[64:67], a[128:131], v[216:219], v[64:67]\n" \
"ds_read_b64_tr_b16 a[186:187], %[vvb] offset:30208\n" \
"ds_read_b64_tr_b16 a[188:189], v211 offset:26112\n" \
"v_mfma_f32_16x16x32_bf16 v[64:67], a[160:163], v[200:203], v[64:67]\n" \
"v_exp_f32_e64 v128, v128\n" \
"v_mfma_f32_16x16x32_bf16 v[68:71], a[132:135], v[216:219], v[68:71]\n" \
"v_cvt_pk_bf16_f32 v206, v156, v157\n" \
"v_cvt_pk_bf16_f32 v207, v158, v159\n" \
"v_mfma_f32_16x16x32_bf16 v[68:71], a[164:167], v[200:203], v[68:71]\n" \
"v_exp_f32_e64 v129, v129\n" \
"v_mfma_f32_16x16x32_bf16 v[72:75], a[136:139], v[216:219], v[72:75]\n" \
"ds_read_b64_tr_b16 a[190:191], v211 offset:30208\n" \
"ds_read_b64_tr_b16 a[128:129], %[vvb] offset:32768\n" \
"v_mfma_f32_16x16x32_bf16 v[72:75], a[168:171], v[200:203], v[72:75]\n" \
"v_exp_f32_e64 v130, v130\n" \
"v_mfma_f32_16x16x32_bf16 v[76:79], a[140:143], v[216:219], v[76:79]\n" \
"ds_read_b64_tr_b16 a[130:131], %[vvb] offset:36864\n" \
"ds_read_b64_tr_b16 a[132:133], v211 offset:32768\n" \
"v_mfma_f32_16x16x32_bf16 v[76:79], a[172:175], v[200:203], v[76:79]\n" \
"v_exp_f32_e64 v131, v131\n" \
"v_smfmac_f32_16x16x64_bf16 v[176:179], a[192:195], v[192:199], v228\n" \
"ds_read_b64_tr_b16 a[134:135], v211 offset:36864\n" \
"ds_read_b64_tr_b16 a[136:137], %[vvb] offset:33280\n" \
"v_mfma_f32_16x16x32_bf16 v[136:139], a[64:67], a[16:19], v[164:167]\n" \
"v_exp_f32_e64 v132, v132\n" \
"v_mfma_f32_16x16x32_bf16 v[136:139], a[68:71], a[20:23], v[136:139]\n" \
"ds_read_b64_tr_b16 a[138:139], %[vvb] offset:37376\n" \
"ds_read_b64_tr_b16 a[140:141], v211 offset:33280\n" \
"v_mfma_f32_16x16x32_bf16 v[136:139], a[72:75], a[24:27], v[136:139]\n" \
"v_exp_f32_e64 v133, v133\n" \
"v_mfma_f32_16x16x32_bf16 v[136:139], a[76:79], a[28:31], v[136:139]\n" \
"v_cvt_pk_bf16_f32 v184, v128, v129\n" \
"v_cvt_pk_bf16_f32 v185, v130, v131\n" \
"v_mfma_f32_16x16x32_bf16 v[140:143], a[92:95], a[28:31], v[164:167]\n" \
"v_exp_f32_e64 v134, v134\n" \
"v_mfma_f32_16x16x32_bf16 v[140:143], a[88:91], a[24:27], v[140:143]\n" \
"ds_read_b64_tr_b16 a[142:143], v211 offset:37376\n" \
"s_waitcnt lgkmcnt(8)\n" \
"v_mfma_f32_16x16x32_bf16 v[140:143], a[84:87], a[20:23], v[140:143]\n" \
"buffer_load_dwordx4 v212, %[srdk], s48 offen offset:1024 lds\n" \
"v_mfma_f32_16x16x32_bf16 v[140:143], a[80:83], a[16:19], v[140:143]\n" \
"v_exp_f32_e32 v135, v135\n" \
"v_mfma_f32_16x16x32_bf16 v[80:83], a[144:147], v[216:219], v[80:83]\n" \
"ds_read_b64_tr_b16 a[144:145], %[vvb] offset:33792\n" \
"ds_read_b64_tr_b16 a[146:147], %[vvb] offset:37888\n" \
"v_mfma_f32_16x16x32_bf16 v[80:83], a[176:179], v[200:203], v[80:83]\n" \
"v_exp_f32_e64 v136, v136\n" \
"v_mfma_f32_16x16x32_bf16 v[84:87], a[148:151], v[216:219], v[84:87]\n" \
"v_cvt_pk_bf16_f32 v186, v132, v133\n" \
"v_cvt_pk_bf16_f32 v187, v134, v135\n" \
"v_mfma_f32_16x16x32_bf16 v[84:87], a[180:183], v[200:203], v[84:87]\n" \
"v_exp_f32_e64 v137, v137\n" \
"v_mfma_f32_16x16x32_bf16 v[88:91], a[152:155], v[216:219], v[88:91]\n" \
"v_exp_f32_e64 v138, v138\n" \
"v_mfma_f32_16x16x32_bf16 v[88:91], a[184:187], v[200:203], v[88:91]\n" \
"ds_read_b64_tr_b16 a[148:149], v211 offset:33792\n" \
"ds_read_b64_tr_b16 a[150:151], v211 offset:37888\n" \
"v_mfma_f32_16x16x32_bf16 v[92:95], a[156:159], v[216:219], v[92:95]\n" \
"v_exp_f32_e64 v139, v139\n" \
"v_mfma_f32_16x16x32_bf16 v[92:95], a[188:191], v[200:203], v[92:95]\n" \
"ds_read_b64_tr_b16 a[152:153], %[vvb] offset:34304\n" \
"s_cbranch_vccz Lpp1_%=\n" \
"buffer_store_dwordx4 a[196:199], v235, %[srdz], s62 offen offset:0 sc1\n" \
"buffer_store_dwordx4 a[196:199], v226, %[srdz], s62 offen offset:0 sc1\n" \
"Lpp1_%=:\n" \
"v_mfma_f32_16x16x32_bf16 v[144:147], a[64:67], a[32:35], v[168:171]\n" \
"v_exp_f32_e32 v140, v140\n" \
"v_mfma_f32_16x16x32_bf16 v[144:147], a[68:71], a[36:39], v[144:147]\n" \
"ds_read_b64_tr_b16 a[154:155], %[vvb] offset:38400\n" \
"ds_read_b64_tr_b16 a[156:157], v211 offset:34304\n" \
"v_mfma_f32_16x16x32_bf16 v[144:147], a[72:75], a[40:43], v[144:147]\n" \
"v_exp_f32_e64 v141, v141\n" \
"v_mfma_f32_16x16x32_bf16 v[144:147], a[76:79], a[44:47], v[144:147]\n" \
"v_cvt_pk_bf16_f32 v188, v136, v137\n" \
"v_cvt_pk_bf16_f32 v189, v138, v139\n" \
"v_mfma_f32_16x16x32_bf16 v[148:151], a[92:95], a[44:47], v[168:171]\n" \
"v_exp_f32_e64 v142, v142\n" \
"v_mfma_f32_16x16x32_bf16 v[148:151], a[88:91], a[40:43], v[148:151]\n" \
"ds_read_b64_tr_b16 a[158:159], v211 offset:38400\n" \
"s_waitcnt lgkmcnt(8)\n" \
"v_mfma_f32_16x16x32_bf16 v[148:151], a[84:87], a[36:39], v[148:151]\n" \
"buffer_load_dwordx4 v213, %[srdk], s48 offen offset:2048 lds\n" \
"v_mfma_f32_16x16x32_bf16 v[148:151], a[80:83], a[32:35], v[148:151]\n" \
"v_exp_f32_e32 v143, v143\n" \
"v_mfma_f32_16x16x32_bf16 v[0:3], a[160:163], v[192:195], v[0:3]\n" \
"ds_read_b128 a[96:99], %[kb0] offset:40960\n" \
"v_mfma_f32_16x16x32_bf16 v[0:3], a[128:131], v[184:187], v[0:3]\n" \
"v_exp_f32_e64 v144, v144\n" \
"v_mfma_f32_16x16x32_bf16 v[4:7], a[164:167], v[192:195], v[4:7]\n" \
"v_cvt_pk_bf16_f32 v190, v140, v141\n" \
"v_cvt_pk_bf16_f32 v191, v142, v143\n" \
"v_mfma_f32_16x16x32_bf16 v[4:7], a[132:135], v[184:187], v[4:7]\n" \
"v_exp_f32_e64 v145, v145\n" \
"v_mfma_f32_16x16x32_bf16 v[8:11], a[168:171], v[192:195], v[8:11]\n" \
"ds_read_b128 a[100:103], v208 offset:40960\n" \
"v_mfma_f32_16x16x32_bf16 v[8:11], a[136:139], v[184:187], v[8:11]\n" \
"v_exp_f32_e64 v146, v146\n" \
"v_mfma_f32_16x16x32_bf16 v[12:15], a[172:175], v[192:195], v[12:15]\n" \
"ds_read_b128 a[104:107], v209 offset:40960\n" \
"v_mfma_f32_16x16x32_bf16 v[12:15], a[140:143], v[184:187], v[12:15]\n" \
"v_exp_f32_e64 v147, v147\n" \
"v_smfmac_f32_16x16x64_bf16 v[180:183], a[192:195], v[200:207], v228\n" \
"ds_read_b128 a[108:111], v210 offset:40960\n" \
"v_mfma_f32_16x16x32_bf16 v[152:155], a[64:67], a[48:51], v[172:175]\n" \
"v_exp_f32_e64 v148, v148\n" \
"v_mfma_f32_16x16x32_bf16 v[152:155], a[68:71], a[52:55], v[152:155]\n" \
"ds_read_b128 a[124:127], v210 offset:45056\n" \
"v_mfma_f32_16x16x32_bf16 v[152:155], a[72:75], a[56:59], v[152:155]\n" \
"v_exp_f32_e64 v149, v149\n" \
"v_mfma_f32_16x16x32_bf16 v[152:155], a[76:79], a[60:63], v[152:155]\n" \
"v_cvt_pk_bf16_f32 v216, v144, v145\n" \
"v_cvt_pk_bf16_f32 v217, v146, v147\n" \
"v_mfma_f32_16x16x32_bf16 v[156:159], a[92:95], a[60:63], v[172:175]\n" \
"v_exp_f32_e64 v150, v150\n" \
"v_mfma_f32_16x16x32_bf16 v[156:159], a[88:91], a[56:59], v[156:159]\n" \
"ds_read_b128 a[120:123], v209 offset:45056\n" \
"s_waitcnt lgkmcnt(6)\n" \
"v_mfma_f32_16x16x32_bf16 v[156:159], a[84:87], a[52:55], v[156:159]\n" \
"buffer_load_dwordx4 v214, %[srdk], s48 offen offset:3072 lds\n" \
"v_mfma_f32_16x16x32_bf16 v[156:159], a[80:83], a[48:51], v[156:159]\n" \
"v_exp_f32_e32 v151, v151\n" \
"v_mfma_f32_16x16x32_bf16 v[16:19], a[176:179], v[192:195], v[16:19]\n" \
"ds_read_b128 a[116:119], v208 offset:45056\n" \
"v_mfma_f32_16x16x32_bf16 v[16:19], a[144:147], v[184:187], v[16:19]\n" \
"v_exp_f32_e64 v152, v152\n" \
"v_mfma_f32_16x16x32_bf16 v[20:23], a[180:183], v[192:195], v[20:23]\n" \
"v_cvt_pk_bf16_f32 v218, v148, v149\n" \
"v_cvt_pk_bf16_f32 v219, v150, v151\n" \
"v_mfma_f32_16x16x32_bf16 v[20:23], a[148:151], v[184:187], v[20:23]\n" \
"v_exp_f32_e64 v153, v153\n" \
"v_mfma_f32_16x16x32_bf16 v[24:27], a[184:187], v[192:195], v[24:27]\n" \
"v_exp_f32_e64 v154, v154\n" \
"v_mfma_f32_16x16x32_bf16 v[24:27], a[152:155], v[184:187], v[24:27]\n" \
"ds_read_b128 a[112:115], %[kb0] offset:45056\n" \
"s_add_u32 s48, s48, 0x4000\n" \
"v_mfma_f32_16x16x32_bf16 v[28:31], a[188:191], v[192:195], v[28:31]\n" \
"v_exp_f32_e64 v155, v155\n" \
"v_mfma_f32_16x16x32_bf16 v[28:31], a[156:159], v[184:187], v[28:31]\n" \
"s_waitcnt vmcnt(13) lgkmcnt(2)\n" \
"s_barrier\n" \
"v_mfma_f32_16x16x32_bf16 v[128:131], a[96:99], a[0:3], v[160:163]\n" \
"v_exp_f32_e64 v156, v156\n" \
"v_mfma_f32_16x16x32_bf16 v[128:131], a[100:103], a[4:7], v[128:131]\n" \
"s_add_u32 m0, %[m0b], 81920\n" \
"v_mfma_f32_16x16x32_bf16 v[128:131], a[104:107], a[8:11], v[128:131]\n" \
"v_exp_f32_e64 v157, v157\n" \
"v_mfma_f32_16x16x32_bf16 v[128:131], a[108:111], a[12:15], v[128:131]\n" \
"v_cvt_pk_bf16_f32 v220, v152, v153\n" \
"v_cvt_pk_bf16_f32 v221, v154, v155\n" \
"v_mfma_f32_16x16x32_bf16 v[132:135], a[124:127], a[12:15], v[160:163]\n" \
"v_exp_f32_e64 v158, v158\n" \
"v_mfma_f32_16x16x32_bf16 v[132:135], a[120:123], a[8:11], v[132:135]\n" \
"s_waitcnt lgkmcnt(0)\n" \
"v_mfma_f32_16x16x32_bf16 v[132:135], a[116:119], a[4:7], v[132:135]\n" \
"buffer_load_dwordx4 %[vd0], %[srdv], s48 offen offset:0 lds\n" \
"v_mfma_f32_16x16x32_bf16 v[132:135], a[112:115], a[0:3], v[132:135]\n" \
"v_exp_f32_e32 v159, v159\n" \
"v_mfma_f32_16x16x32_bf16 v[32:35], a[160:163], v[196:199], v[32:35]\n" \
"ds_read_b128 a[64:67], %[kb0] offset:49152\n" \
"v_mfma_f32_16x16x32_bf16 v[32:35], a[128:131], v[188:191], v[32:35]\n" \
"v_exp_f32_e64 v128, v128\n" \
"v_mfma_f32_16x16x32_bf16 v[36:39], a[164:167], v[196:199], v[36:39]\n" \
"v_cvt_pk_bf16_f32 v222, v156, v157\n" \
"v_cvt_pk_bf16_f32 v223, v158, v159\n" \
"v_mfma_f32_16x16x32_bf16 v[36:39], a[132:135], v[188:191], v[36:39]\n" \
"v_exp_f32_e64 v129, v129\n" \
"v_mfma_f32_16x16x32_bf16 v[40:43], a[168:171], v[196:199], v[40:43]\n" \
"ds_read_b128 a[68:71], v208 offset:49152\n" \
"v_mfma_f32_16x16x32_bf16 v[40:43], a[136:139], v[188:191], v[40:43]\n" \
"v_exp_f32_e64 v130, v130\n" \
"v_mfma_f32_16x16x32_bf16 v[44:47], a[172:175], v[196:199], v[44:47]\n" \
"ds_read_b128 a[72:75], v209 offset:49152\n" \
"v_mfma_f32_16x16x32_bf16 v[44:47], a[140:143], v[188:191], v[44:47]\n" \
"v_exp_f32_e64 v131, v131\n" \
"v_smfmac_f32_16x16x64_bf16 v[176:179], a[192:195], v[184:191], v228\n" \
"v_mfma_f32_16x16x32_bf16 v[136:139], a[96:99], a[16:19], v[164:167]\n" \
"v_exp_f32_e64 v132, v132\n" \
"v_mfma_f32_16x16x32_bf16 v[136:139], a[100:103], a[20:23], v[136:139]\n" \
"ds_read_b128 a[76:79], v210 offset:49152\n" \
"v_mfma_f32_16x16x32_bf16 v[136:139], a[104:107], a[24:27], v[136:139]\n" \
"v_exp_f32_e64 v133, v133\n" \
"v_mfma_f32_16x16x32_bf16 v[136:139], a[108:111], a[28:31], v[136:139]\n" \
"v_cvt_pk_bf16_f32 v192, v128, v129\n" \
"v_cvt_pk_bf16_f32 v193, v130, v131\n" \
"v_mfma_f32_16x16x32_bf16 v[140:143], a[124:127], a[28:31], v[164:167]\n" \
"v_exp_f32_e64 v134, v134\n" \
"v_mfma_f32_16x16x32_bf16 v[140:143], a[120:123], a[24:27], v[140:143]\n" \
"ds_read_b128 a[92:95], v210 offset:53248\n" \
"v_mfma_f32_16x16x32_bf16 v[140:143], a[116:119], a[20:23], v[140:143]\n" \
"buffer_load_dwordx4 v215, %[srdv], s48 offen offset:1024 lds\n" \
"v_mfma_f32_16x16x32_bf16 v[140:143], a[112:115], a[16:19], v[140:143]\n" \
"v_exp_f32_e64 v135, v135\n" \
"v_mfma_f32_16x16x32_bf16 v[48:51], a[176:179], v[196:199], v[48:51]\n" \
"ds_read_b128 a[88:91], v209 offset:53248\n" \
"v_mfma_f32_16x16x32_bf16 v[48:51], a[144:147], v[188:191], v[48:51]\n" \
"v_exp_f32_e64 v136, v136\n" \
"v_mfma_f32_16x16x32_bf16 v[52:55], a[180:183], v[196:199], v[52:55]\n" \
"v_cvt_pk_bf16_f32 v194, v132, v133\n" \
"v_cvt_pk_bf16_f32 v195, v134, v135\n" \
"v_mfma_f32_16x16x32_bf16 v[52:55], a[148:151], v[188:191], v[52:55]\n" \
"v_exp_f32_e64 v137, v137\n" \
"v_mfma_f32_16x16x32_bf16 v[56:59], a[184:187], v[196:199], v[56:59]\n" \
"v_exp_f32_e64 v138, v138\n" \
"v_mfma_f32_16x16x32_bf16 v[56:59], a[152:155], v[188:191], v[56:59]\n" \
"ds_read_b128 a[84:87], v208 offset:53248\n" \
"v_mfma_f32_16x16x32_bf16 v[60:63], a[188:191], v[196:199], v[60:63]\n" \
"v_exp_f32_e64 v139, v139\n" \
"v_mfma_f32_16x16x32_bf16 v[60:63], a[156:159], v[188:191], v[60:63]\n" \
"s_cmp_lg_u32 %[wid], 1\n" \
"s_cbranch_scc1 LtA05_%=\n" \
"global_load_dword v237, v224, s[58:59]\n" \
"s_add_u32 s61, s48, 49152\n" \
"s_mov_b32 exec_hi, 0\n" \
"s_nop 0\n" \
"buffer_load_dword v237, v224, %[srdp], s61 offen\n" \
"s_mov_b32 exec_hi, -1\n" \
"s_nop 0\n" \
"LtA05_%=:\n" \
"v_mfma_f32_16x16x32_bf16 v[144:147], a[96:99], a[32:35], v[168:171]\n" \
"v_exp_f32_e64 v140, v140\n" \
"v_mfma_f32_16x16x32_bf16 v[144:147], a[100:103], a[36:39], v[144:147]\n" \
"ds_read_b128 a[80:83], %[kb0] offset:53248\n" \
"v_mfma_f32_16x16x32_bf16 v[144:147], a[104:107], a[40:43], v[144:147]\n" \
"v_exp_f32_e64 v141, v141\n" \
"v_mfma_f32_16x16x32_bf16 v[144:147], a[108:111], a[44:47], v[144:147]\n" \
"v_cvt_pk_bf16_f32 v196, v136, v137\n" \
"v_cvt_pk_bf16_f32 v197, v138, v139\n" \
"v_mfma_f32_16x16x32_bf16 v[148:151], a[124:127], a[44:47], v[168:171]\n" \
"v_exp_f32_e64 v142, v142\n" \
"v_mfma_f32_16x16x32_bf16 v[148:151], a[120:123], a[40:43], v[148:151]\n" \
"v_mfma_f32_16x16x32_bf16 v[148:151], a[116:119], a[36:39], v[148:151]\n" \
"buffer_load_dwordx4 %[vd0], %[srdv], s48 offen offset:2048 lds\n" \
"v_mfma_f32_16x16x32_bf16 v[148:151], a[112:115], a[32:35], v[148:151]\n" \
"v_exp_f32_e64 v143, v143\n" \
"v_mfma_f32_16x16x32_bf16 v[96:99], a[160:163], v[204:207], v[96:99]\n" \
"ds_read_b64_tr_b16 a[160:161], %[vvb] offset:40960\n" \
"v_mfma_f32_16x16x32_bf16 v[96:99], a[128:131], v[220:223], v[96:99]\n" \
"v_exp_f32_e64 v144, v144\n" \
"v_mfma_f32_16x16x32_bf16 v[100:103], a[164:167], v[204:207], v[100:103]\n" \
"v_cvt_pk_bf16_f32 v198, v140, v141\n" \
"v_cvt_pk_bf16_f32 v199, v142, v143\n" \
"v_mfma_f32_16x16x32_bf16 v[100:103], a[132:135], v[220:223], v[100:103]\n" \
"v_exp_f32_e64 v145, v145\n" \
"v_mfma_f32_16x16x32_bf16 v[104:107], a[168:171], v[204:207], v[104:107]\n" \
"ds_read_b64_tr_b16 a[162:163], %[vvb] offset:45056\n" \
".if . - adaln_body != 16384\n" \
".error \"size model\"\n" \
".endif\n" \
"Lk3_%=:\n" \
"s_cbranch_execz Lk4_%=\n" \
"s_nop 0\n" \
"ds_read_b64_tr_b16 a[164:165], v211 offset:40960\n" \
"v_mfma_f32_16x16x32_bf16 v[104:107], a[136:139], v[220:223], v[104:107]\n" \
"v_exp_f32_e64 v146, v146\n" \
"v_mfma_f32_16x16x32_bf16 v[108:111], a[172:175], v[204:207], v[108:111]\n" \
"ds_read_b64_tr_b16 a[166:167], v211 offset:45056\n" \
"v_mfma_f32_16x16x32_bf16 v[108:111], a[140:143], v[220:223], v[108:111]\n" \
"v_exp_f32_e64 v147, v147\n" \
"v_smfmac_f32_16x16x64_bf16 v[180:183], a[192:195], v[216:223], v228\n" \
"ds_read_b64_tr_b16 a[168:169], %[vvb] offset:41472\n" \
"v_mfma_f32_16x16x32_bf16 v[152:155], a[96:99], a[48:51], v[172:175]\n" \
"v_exp_f32_e64 v148, v148\n" \
"v_mfma_f32_16x16x32_bf16 v[152:155], a[100:103], a[52:55], v[152:155]\n" \
"ds_read_b64_tr_b16 a[170:171], %[vvb] offset:45568\n" \
"v_mfma_f32_16x16x32_bf16 v[152:155], a[104:107], a[56:59], v[152:155]\n" \
"v_exp_f32_e64 v149, v149\n" \
"v_mfma_f32_16x16x32_bf16 v[152:155], a[108:111], a[60:63], v[152:155]\n" \
"v_cvt_pk_bf16_f32 v200, v144, v145\n" \
"v_cvt_pk_bf16_f32 v201, v146, v147\n" \
"v_mfma_f32_16x16x32_bf16 v[156:159], a[124:127], a[60:63], v[172:175]\n" \
"v_exp_f32_e64 v150, v150\n" \
"v_mfma_f32_16x16x32_bf16 v[156:159], a[120:123], a[56:59], v[156:159]\n" \
"ds_read_b64_tr_b16 a[172:173], v211 offset:41472\n" \
"v_mfma_f32_16x16x32_bf16 v[156:159], a[116:119], a[52:55], v[156:159]\n" \
"buffer_load_dwordx4 v215, %[srdv], s48 offen offset:3072 lds\n" \
"v_mfma_f32_16x16x32_bf16 v[156:159], a[112:115], a[48:51], v[156:159]\n" \
"v_exp_f32_e64 v151, v151\n" \
"v_mfma_f32_16x16x32_bf16 v[112:115], a[176:179], v[204:207], v[112:115]\n" \
"ds_read_b64_tr_b16 a[174:175], v211 offset:45568\n" \
"ds_read_b64_tr_b16 a[176:177], %[vvb] offset:41984\n" \
"v_mfma_f32_16x16x32_bf16 v[112:115], a[144:147], v[220:223], v[112:115]\n" \
"v_exp_f32_e64 v152, v152\n" \
"v_mfma_f32_16x16x32_bf16 v[116:119], a[180:183], v[204:207], v[116:119]\n" \
"v_cvt_pk_bf16_f32 v202, v148, v149\n" \
"v_cvt_pk_bf16_f32 v203, v150, v151\n" \
"v_mfma_f32_16x16x32_bf16 v[116:119], a[148:151], v[220:223], v[116:119]\n" \
"v_exp_f32_e64 v153, v153\n" \
"v_mfma_f32_16x16x32_bf16 v[120:123], a[184:187], v[204:207], v[120:123]\n" \
"v_exp_f32_e64 v154, v154\n" \
"v_mfma_f32_16x16x32_bf16 v[120:123], a[152:155], v[220:223], v[120:123]\n" \
"ds_read_b64_tr_b16 a[178:179], %[vvb] offset:46080\n" \
"v_mfma_f32_16x16x32_bf16 v[124:127], a[188:191], v[204:207], v[124:127]\n" \
"v_exp_f32_e64 v155, v155\n" \
"v_mfma_f32_16x16x32_bf16 v[124:127], a[156:159], v[220:223], v[124:127]\n" \
"ds_read_b64_tr_b16 a[180:181], v211 offset:41984\n" \
"s_waitcnt lgkmcnt(13)\n" \
"v_mfma_f32_16x16x32_bf16 v[128:131], a[64:67], a[0:3], v[160:163]\n" \
"v_exp_f32_e32 v156, v156\n" \
"v_mfma_f32_16x16x32_bf16 v[128:131], a[68:71], a[4:7], v[128:131]\n" \
"ds_read_b64_tr_b16 a[182:183], v211 offset:46080\n" \
"s_add_u32 m0, %[m0b], 32768\n" \
"v_mfma_f32_16x16x32_bf16 v[128:131], a[72:75], a[8:11], v[128:131]\n" \
"v_exp_f32_e64 v157, v157\n" \
"v_mfma_f32_16x16x32_bf16 v[128:131], a[76:79], a[12:15], v[128:131]\n" \
"v_cvt_pk_bf16_f32 v204, v152, v153\n" \
"v_cvt_pk_bf16_f32 v205, v154, v155\n" \
"v_mfma_f32_16x16x32_bf16 v[132:135], a[92:95], a[12:15], v[160:163]\n" \
"v_exp_f32_e64 v158, v158\n" \
"v_mfma_f32_16x16x32_bf16 v[132:135], a[88:91], a[8:11], v[132:135]\n" \
"ds_read_b64_tr_b16 a[184:185], %[vvb] offset:42496\n" \
"s_waitcnt lgkmcnt(5)\n" \
"v_mfma_f32_16x16x32_bf16 v[132:135], a[84:87], a[4:7], v[132:135]\n" \
"buffer_load_dwordx4 %[kd0], %[srdk], s48 offen offset:0 lds\n" \
"v_mfma_f32_16x16x32_bf16 v[132:135], a[80:83], a[0:3], v[132:135]\n" \
"v_exp_f32_e32 v159, v159\n" \
"v_mfma_f32_16x16x32_bf16 v[64:67], a[128:131], v[216:219], v[64:67]\n" \
"ds_read_b64_tr_b16 a[186:187], %[vvb] offset:46592\n" \
"ds_read_b64_tr_b16 a[188:189], v211 offset:42496\n" \
"v_mfma_f32_16x16x32_bf16 v[64:67], a[160:163], v[200:203], v[64:67]\n" \
"v_exp_f32_e64 v128, v128\n" \
"v_mfma_f32_16x16x32_bf16 v[68:71], a[132:135], v[216:219], v[68:71]\n" \
"v_cvt_pk_bf16_f32 v206, v156, v157\n" \
"v_cvt_pk_bf16_f32 v207, v158, v159\n" \
"v_mfma_f32_16x16x32_bf16 v[68:71], a[164:167], v[200:203], v[68:71]\n" \
"v_exp_f32_e64 v129, v129\n" \
"v_mfma_f32_16x16x32_bf16 v[72:75], a[136:139], v[216:219], v[72:75]\n" \
"ds_read_b64_tr_b16 a[190:191], v211 offset:46592\n" \
"ds_read_b64_tr_b16 a[128:129], %[vvb] offset:49152\n" \
"v_mfma_f32_16x16x32_bf16 v[72:75], a[168:171], v[200:203], v[72:75]\n" \
"v_exp_f32_e64 v130, v130\n" \
"v_mfma_f32_16x16x32_bf16 v[76:79], a[140:143], v[216:219], v[76:79]\n" \
"ds_read_b64_tr_b16 a[130:131], %[vvb] offset:53248\n" \
"ds_read_b64_tr_b16 a[132:133], v211 offset:49152\n" \
"v_mfma_f32_16x16x32_bf16 v[76:79], a[172:175], v[200:203], v[76:79]\n" \
"v_exp_f32_e64 v131, v131\n" \
"v_smfmac_f32_16x16x64_bf16 v[176:179], a[192:195], v[192:199], v228\n" \
"ds_read_b64_tr_b16 a[134:135], v211 offset:53248\n" \
"ds_read_b64_tr_b16 a[136:137], %[vvb] offset:49664\n" \
"v_mfma_f32_16x16x32_bf16 v[136:139], a[64:67], a[16:19], v[164:167]\n" \
"v_exp_f32_e64 v132, v132\n" \
"v_mfma_f32_16x16x32_bf16 v[136:139], a[68:71], a[20:23], v[136:139]\n" \
"ds_read_b64_tr_b16 a[138:139], %[vvb] offset:53760\n" \
"ds_read_b64_tr_b16 a[140:141], v211 offset:49664\n" \
"v_mfma_f32_16x16x32_bf16 v[136:139], a[72:75], a[24:27], v[136:139]\n" \
"v_exp_f32_e64 v133, v133\n" \
"v_mfma_f32_16x16x32_bf16 v[136:139], a[76:79], a[28:31], v[136:139]\n" \
"v_cvt_pk_bf16_f32 v184, v128, v129\n" \
"v_cvt_pk_bf16_f32 v185, v130, v131\n" \
"v_mfma_f32_16x16x32_bf16 v[140:143], a[92:95], a[28:31], v[164:167]\n" \
"v_exp_f32_e64 v134, v134\n" \
"v_mfma_f32_16x16x32_bf16 v[140:143], a[88:91], a[24:27], v[140:143]\n" \
"ds_read_b64_tr_b16 a[142:143], v211 offset:53760\n" \
"s_waitcnt lgkmcnt(8)\n" \
"v_mfma_f32_16x16x32_bf16 v[140:143], a[84:87], a[20:23], v[140:143]\n" \
"buffer_load_dwordx4 v212, %[srdk], s48 offen offset:1024 lds\n" \
"v_mfma_f32_16x16x32_bf16 v[140:143], a[80:83], a[16:19], v[140:143]\n" \
"v_exp_f32_e32 v135, v135\n" \
"v_mfma_f32_16x16x32_bf16 v[80:83], a[144:147], v[216:219], v[80:83]\n" \
"ds_read_b64_tr_b16 a[144:145], %[vvb] offset:50176\n" \
"ds_read_b64_tr_b16 a[146:147], %[vvb] offset:54272\n" \
"v_mfma_f32_16x16x32_bf16 v[80:83], a[176:179], v[200:203], v[80:83]\n" \
"v_exp_f32_e64 v136, v136\n" \
"v_mfma_f32_16x16x32_bf16 v[84:87], a[148:151], v[216:219], v[84:87]\n" \
"v_cvt_pk_bf16_f32 v186, v132, v133\n" \
"v_cvt_pk_bf16_f32 v187, v134, v135\n" \
"v_mfma_f32_16x16x32_bf16 v[84:87], a[180:183], v[200:203], v[84:87]\n" \
"v_exp_f32_e64 v137, v137\n" \
"v_mfma_f32_16x16x32_bf16 v[88:91], a[152:155], v[216:219], v[88:91]\n" \
"v_exp_f32_e64 v138, v138\n" \
"v_mfma_f32_16x16x32_bf16 v[88:91], a[184:187], v[200:203], v[88:91]\n" \
"ds_read_b64_tr_b16 a[148:149], v211 offset:50176\n" \
"ds_read_b64_tr_b16 a[150:151], v211 offset:54272\n" \
"v_mfma_f32_16x16x32_bf16 v[92:95], a[156:159], v[216:219], v[92:95]\n" \
"v_exp_f32_e64 v139, v139\n" \
"v_mfma_f32_16x16x32_bf16 v[92:95], a[188:191], v[200:203], v[92:95]\n" \
"ds_read_b64_tr_b16 a[152:153], %[vvb] offset:50688\n" \
"global_load_dword v237, %[vct], %[pct]\n" \
"v_mfma_f32_16x16x32_bf16 v[144:147], a[64:67], a[32:35], v[168:171]\n" \
"v_exp_f32_e64 v140, v140\n" \
"v_mfma_f32_16x16x32_bf16 v[144:147], a[68:71], a[36:39], v[144:147]\n" \
"ds_read_b64_tr_b16 a[154:155], %[vvb] offset:54784\n" \
"ds_read_b64_tr_b16 a[156:157], v211 offset:50688\n" \
"v_mfma_f32_16x16x32_bf16 v[144:147], a[72:75], a[40:43], v[144:147]\n" \
"v_exp_f32_e64 v141, v141\n" \
"v_mfma_f32_16x16x32_bf16 v[144:147], a[76:79], a[44:47], v[144:147]\n" \
"v_cvt_pk_bf16_f32 v188, v136, v137\n" \
"v_cvt_pk_bf16_f32 v189, v138, v139\n" \
"v_mfma_f32_16x16x32_bf16 v[148:151], a[92:95], a[44:47], v[168:171]\n" \
"v_exp_f32_e64 v142, v142\n" \
"v_mfma_f32_16x16x32_bf16 v[148:151], a[88:91], a[40:43], v[148:151]\n" \
"ds_read_b64_tr_b16 a[158:159], v211 offset:54784\n" \
"s_waitcnt lgkmcnt(8)\n" \
"v_mfma_f32_16x16x32_bf16 v[148:151], a[84:87], a[36:39], v[148:151]\n" \
"buffer_load_dwordx4 v213, %[srdk], s48 offen offset:2048 lds\n" \
"v_mfma_f32_16x16x32_bf16 v[148:151], a[80:83], a[32:35], v[148:151]\n" \
"v_exp_f32_e32 v143, v143\n" \
"v_mfma_f32_16x16x32_bf16 v[0:3], a[160:163], v[192:195], v[0:3]\n" \
"ds_read_b128 a[96:99], %[kb0] offset:57344\n" \
"v_mfma_f32_16x16x32_bf16 v[0:3], a[128:131], v[184:187], v[0:3]\n" \
"v_exp_f32_e64 v144, v144\n" \
"v_mfma_f32_16x16x32_bf16 v[4:7], a[164:167], v[192:195], v[4:7]\n" \
"v_cvt_pk_bf16_f32 v190, v140, v141\n" \
"v_cvt_pk_bf16_f32 v191, v142, v143\n" \
"v_mfma_f32_16x16x32_bf16 v[4:7], a[132:135], v[184:187], v[4:7]\n" \
"v_exp_f32_e64 v145, v145\n" \
"v_mfma_f32_16x16x32_bf16 v[8:11], a[168:171], v[192:195], v[8:11]\n" \
"ds_read_b128 a[100:103], v208 offset:57344\n" \
"v_mfma_f32_16x16x32_bf16 v[8:11], a[136:139], v[184:187], v[8:11]\n" \
"v_exp_f32_e64 v146, v146\n" \
"v_mfma_f32_16x16x32_bf16 v[12:15], a[172:175], v[192:195], v[12:15]\n" \
"ds_read_b128 a[104:107], v209 offset:57344\n" \
"v_mfma_f32_16x16x32_bf16 v[12:15], a[140:143], v[184:187], v[12:15]\n" \
"v_exp_f32_e64 v147, v147\n" \
"v_smfmac_f32_16x16x64_bf16 v[180:183], a[192:195], v[200:207], v228\n" \
"ds_read_b128 a[108:111], v210 offset:57344\n" \
"v_mfma_f32_16x16x32_bf16 v[152:155], a[64:67], a[48:51], v[172:175]\n" \
"v_exp_f32_e64 v148, v148\n" \
"v_mfma_f32_16x16x32_bf16 v[152:155], a[68:71], a[52:55], v[152:155]\n" \
"ds_read_b128 a[124:127], v210 offset:61440\n" \
"v_mfma_f32_16x16x32_bf16 v[152:155], a[72:75], a[56:59], v[152:155]\n" \
"v_exp_f32_e64 v149, v149\n" \
"v_mfma_f32_16x16x32_bf16 v[152:155], a[76:79], a[60:63], v[152:155]\n" \
"v_cvt_pk_bf16_f32 v216, v144, v145\n" \
"v_cvt_pk_bf16_f32 v217, v146, v147\n" \
"v_mfma_f32_16x16x32_bf16 v[156:159], a[92:95], a[60:63], v[172:175]\n" \
"v_exp_f32_e64 v150, v150\n" \
"v_mfma_f32_16x16x32_bf16 v[156:159], a[88:91], a[56:59], v[156:159]\n" \
"ds_read_b128 a[120:123], v209 offset:61440\n" \
"s_waitcnt lgkmcnt(6)\n" \
"v_mfma_f32_16x16x32_bf16 v[156:159], a[84:87], a[52:55], v[156:159]\n" \
"buffer_load_dwordx4 v214, %[srdk], s48 offen offset:3072 lds\n" \
"v_mfma_f32_16x16x32_bf16 v[156:159], a[80:83], a[48:51], v[156:159]\n" \
"v_exp_f32_e32 v151, v151\n" \
"v_mfma_f32_16x16x32_bf16 v[16:19], a[176:179], v[192:195], v[16:19]\n" \
"ds_read_b128 a[116:119], v208 offset:61440\n" \
"v_mfma_f32_16x16x32_bf16 v[16:19], a[144:147], v[184:187], v[16:19]\n" \
"v_exp_f32_e64 v152, v152\n" \
"v_mfma_f32_16x16x32_bf16 v[20:23], a[180:183], v[192:195], v[20:23]\n" \
"v_cvt_pk_bf16_f32 v218, v148, v149\n" \
"v_cvt_pk_bf16_f32 v219, v150, v151\n" \
"v_mfma_f32_16x16x32_bf16 v[20:23], a[148:151], v[184:187], v[20:23]\n" \
"v_exp_f32_e64 v153, v153\n" \
"v_mfma_f32_16x16x32_bf16 v[24:27], a[184:187], v[192:195], v[24:27]\n" \
"v_exp_f32_e64 v154, v154\n" \
"v_mfma_f32_16x16x32_bf16 v[24:27], a[152:155], v[184:187], v[24:27]\n" \
"ds_read_b128 a[112:115], %[kb0] offset:61440\n" \
"s_add_u32 s48, s48, 0x4000\n" \
"s_mov_b32 m0, s44\n" \
"s_addk_i32 s44, 0x400\n" \
"buffer_load_dwordx4 v229, s[64:67], s45 offen offset:0 nt lds\n" \
"s_add_u32 s45, s45, 393216\n" \
"v_mfma_f32_16x16x32_bf16 v[28:31], a[188:191], v[192:195], v[28:31]\n" \
"v_exp_f32_e64 v155, v155\n" \
"v_mfma_f32_16x16x32_bf16 v[28:31], a[156:159], v[184:187], v[28:31]\n" \
"s_waitcnt vmcnt(14) lgkmcnt(2)\n" \
"s_barrier\n" \
"v_mfma_f32_16x16x32_bf16 v[128:131], a[96:99], a[0:3], v[160:163]\n" \
"v_exp_f32_e64 v156, v156\n" \
"v_mfma_f32_16x16x32_bf16 v[128:131], a[100:103], a[4:7], v[128:131]\n" \
"s_add_u32 m0, %[m0b], 98304\n" \
"v_mfma_f32_16x16x32_bf16 v[128:131], a[104:107], a[8:11], v[128:131]\n" \
"v_exp_f32_e64 v157, v157\n" \
"v_mfma_f32_16x16x32_bf16 v[128:131], a[108:111], a[12:15], v[128:131]\n" \
"v_cvt_pk_bf16_f32 v220, v152, v153\n" \
"v_cvt_pk_bf16_f32 v221, v154, v155\n" \
"v_mfma_f32_16x16x32_bf16 v[132:135], a[124:127], a[12:15], v[160:163]\n" \
"v_exp_f32_e64 v158, v158\n" \
"v_mfma_f32_16x16x32_bf16 v[132:135], a[120:123], a[8:11], v[132:135]\n" \
"s_waitcnt lgkmcnt(0)\n" \
"v_mfma_f32_16x16x32_bf16 v[132:135], a[116:119], a[4:7], v[132:135]\n" \
"buffer_load_dwordx4 %[vd0], %[srdv], s48 offen offset:0 lds\n" \
"v_mfma_f32_16x16x32_bf16 v[132:135], a[112:115], a[0:3], v[132:135]\n" \
"v_exp_f32_e32 v159, v159\n" \
"v_mfma_f32_16x16x32_bf16 v[32:35], a[160:163], v[196:199], v[32:35]\n" \
"ds_read_b128 a[64:67], %[kb0] offset:0\n" \
"v_mfma_f32_16x16x32_bf16 v[32:35], a[128:131], v[188:191], v[32:35]\n" \
"v_exp_f32_e64 v128, v128\n" \
"v_mfma_f32_16x16x32_bf16 v[36:39], a[164:167], v[196:199], v[36:39]\n" \
"v_cvt_pk_bf16_f32 v222, v156, v157\n" \
"v_cvt_pk_bf16_f32 v223, v158, v159\n" \
"v_mfma_f32_16x16x32_bf16 v[36:39], a[132:135], v[188:191], v[36:39]\n" \
"v_exp_f32_e64 v129, v129\n" \
"v_mfma_f32_16x16x32_bf16 v[40:43], a[168:171], v[196:199], v[40:43]\n" \
"ds_read_b128 a[68:71], v208 offset:0\n" \
"v_mfma_f32_16x16x32_bf16 v[40:43], a[136:139], v[188:191], v[40:43]\n" \
"v_exp_f32_e64 v130, v130\n" \
"v_mfma_f32_16x16x32_bf16 v[44:47], a[172:175], v[196:199], v[44:47]\n" \
"ds_read_b128 a[72:75], v209 offset:0\n" \
"v_mfma_f32_16x16x32_bf16 v[44:47], a[140:143], v[188:191], v[44:47]\n" \
"v_exp_f32_e64 v131, v131\n" \
"v_smfmac_f32_16x16x64_bf16 v[176:179], a[192:195], v[184:191], v228\n" \
"v_mfma_f32_16x16x32_bf16 v[136:139], a[96:99], a[16:19], v[164:167]\n" \
"v_exp_f32_e64 v132, v132\n" \
"v_mfma_f32_16x16x32_bf16 v[136:139], a[100:103], a[20:23], v[136:139]\n" \
"ds_read_b128 a[76:79], v210 offset:0\n" \
"v_mfma_f32_16x16x32_bf16 v[136:139], a[104:107], a[24:27], v[136:139]\n" \
"v_exp_f32_e64 v133, v133\n" \
"v_mfma_f32_16x16x32_bf16 v[136:139], a[108:111], a[28:31], v[136:139]\n" \
"v_cvt_pk_bf16_f32 v192, v128, v129\n" \
"v_cvt_pk_bf16_f32 v193, v130, v131\n" \
"v_mfma_f32_16x16x32_bf16 v[140:143], a[124:127], a[28:31], v[164:167]\n" \
"v_exp_f32_e64 v134, v134\n" \
"v_mfma_f32_16x16x32_bf16 v[140:143], a[120:123], a[24:27], v[140:143]\n" \
"ds_read_b128 a[92:95], v210 offset:4096\n" \
"v_mfma_f32_16x16x32_bf16 v[140:143], a[116:119], a[20:23], v[140:143]\n" \
"buffer_load_dwordx4 v215, %[srdv], s48 offen offset:1024 lds\n" \
"v_mfma_f32_16x16x32_bf16 v[140:143], a[112:115], a[16:19], v[140:143]\n" \
"v_exp_f32_e64 v135, v135\n" \
"v_mfma_f32_16x16x32_bf16 v[48:51], a[176:179], v[196:199], v[48:51]\n" \
"ds_read_b128 a[88:91], v209 offset:4096\n" \
"v_mfma_f32_16x16x32_bf16 v[48:51], a[144:147], v[188:191], v[48:51]\n" \
"v_exp_f32_e64 v136, v136\n" \
"v_mfma_f32_16x16x32_bf16 v[52:55], a[180:183], v[196:199], v[52:55]\n" \
"v_cvt_pk_bf16_f32 v194, v132, v133\n" \
"v_cvt_pk_bf16_f32 v195, v134, v135\n" \
"v_mfma_f32_16x16x32_bf16 v[52:55], a[148:151], v[188:191], v[52:55]\n" \
"v_exp_f32_e64 v137, v137\n" \
"v_mfma_f32_16x16x32_bf16 v[56:59], a[184:187], v[196:199], v[56:59]\n" \
"v_exp_f32_e64 v138, v138\n" \
"v_mfma_f32_16x16x32_bf16 v[56:59], a[152:155], v[188:191], v[56:59]\n" \
"ds_read_b128 a[84:87], v208 offset:4096\n" \
"v_mfma_f32_16x16x32_bf16 v[60:63], a[188:191], v[196:199], v[60:63]\n" \
"v_exp_f32_e64 v139, v139\n" \
"v_mfma_f32_16x16x32_bf16 v[60:63], a[156:159], v[188:191], v[60:63]\n" \
"s_cmp_lg_u32 %[wid], 2\n" \
"s_cbranch_scc1 LtA07_%=\n" \
"global_load_dword v237, v224, s[58:59]\n" \
"s_add_u32 s61, s48, 49152\n" \
"s_mov_b32 exec_hi, 0\n" \
"s_nop 0\n" \
"buffer_load_dword v237, v224, %[srdp], s61 offen\n" \
"s_mov_b32 exec_hi, -1\n" \
"s_nop 0\n" \
"LtA07_%=:\n" \
"v_mfma_f32_16x16x32_bf16 v[144:147], a[96:99], a[32:35], v[168:171]\n" \
"v_exp_f32_e64 v140, v140\n" \
"v_mfma_f32_16x16x32_bf16 v[144:147], a[100:103], a[36:39], v[144:147]\n" \
"ds_read_b128 a[80:83], %[kb0] offset:4096\n" \
"v_mfma_f32_16x16x32_bf16 v[144:147], a[104:107], a[40:43], v[144:147]\n" \
"v_exp_f32_e64 v141, v141\n" \
"v_mfma_f32_16x16x32_bf16 v[144:147], a[108:111], a[44:47], v[144:147]\n" \
"v_cvt_pk_bf16_f32 v196, v136, v137\n" \
"v_cvt_pk_bf16_f32 v197, v138, v139\n" \
"v_mfma_f32_16x16x32_bf16 v[148:151], a[124:127], a[44:47], v[168:171]\n" \
"v_exp_f32_e64 v142, v142\n" \
"v_mfma_f32_16x16x32_bf16 v[148:151], a[120:123], a[40:43], v[148:151]\n" \
"v_mfma_f32_16x16x32_bf16 v[148:151], a[116:119], a[36:39], v[148:151]\n" \
"buffer_load_dwordx4 %[vd0], %[srdv], s48 offen offset:2048 lds\n" \
"v_mfma_f32_16x16x32_bf16 v[148:151], a[112:115], a[32:35], v[148:151]\n" \
"v_exp_f32_e64 v143, v143\n" \
"v_mfma_f32_16x16x32_bf16 v[96:99], a[160:163], v[204:207], v[96:99]\n" \
"ds_read_b64_tr_b16 a[160:161], %[vvb] offset:57344\n" \
"v_mfma_f32_16x16x32_bf16 v[96:99], a[128:131], v[220:223], v[96:99]\n" \
"v_exp_f32_e64 v144, v144\n" \
"v_mfma_f32_16x16x32_bf16 v[100:103], a[164:167], v[204:207], v[100:103]\n" \
"v_cvt_pk_bf16_f32 v198, v140, v141\n" \
"v_cvt_pk_bf16_f32 v199, v142, v143\n" \
"v_mfma_f32_16x16x32_bf16 v[100:103], a[132:135], v[220:223], v[100:103]\n" \
"v_exp_f32_e64 v145, v145\n" \
"v_mfma_f32_16x16x32_bf16 v[104:107], a[168:171], v[204:207], v[104:107]\n" \
"ds_read_b64_tr_b16 a[162:163], %[vvb] offset:61440\n" \
"ds_read_b64_tr_b16 a[164:165], v211 offset:57344\n" \
"v_mfma_f32_16x16x32_bf16 v[104:107], a[136:139], v[220:223], v[104:107]\n" \
"v_exp_f32_e64 v146, v146\n" \
"v_mfma_f32_16x16x32_bf16 v[108:111], a[172:175], v[204:207], v[108:111]\n" \
"ds_read_b64_tr_b16 a[166:167], v211 offset:61440\n" \
"v_mfma_f32_16x16x32_bf16 v[108:111], a[140:143], v[220:223], v[108:111]\n" \
"v_exp_f32_e64 v147, v147\n" \
"v_smfmac_f32_16x16x64_bf16 v[180:183], a[192:195], v[216:223], v228\n" \
"ds_read_b64_tr_b16 a[168:169], %[vvb] offset:57856\n" \
"v_mfma_f32_16x16x32_bf16 v[152:155], a[96:99], a[48:51], v[172:175]\n" \
"v_exp_f32_e64 v148, v148\n" \
"v_mfma_f32_16x16x32_bf16 v[152:155], a[100:103], a[52:55], v[152:155]\n" \
"ds_read_b64_tr_b16 a[170:171], %[vvb] offset:61952\n" \
"v_mfma_f32_16x16x32_bf16 v[152:155], a[104:107], a[56:59], v[152:155]\n" \
"v_exp_f32_e64 v149, v149\n" \
"v_mfma_f32_16x16x32_bf16 v[152:155], a[108:111], a[60:63], v[152:155]\n" \
"v_cvt_pk_bf16_f32 v200, v144, v145\n" \
"v_cvt_pk_bf16_f32 v201, v146, v147\n" \
"v_mfma_f32_16x16x32_bf16 v[156:159], a[124:127], a[60:63], v[172:175]\n" \
"v_exp_f32_e64 v150, v150\n" \
"v_mfma_f32_16x16x32_bf16 v[156:159], a[120:123], a[56:59], v[156:159]\n" \
"ds_read_b64_tr_b16 a[172:173], v211 offset:57856\n" \
"v_mfma_f32_16x16x32_bf16 v[156:159], a[116:119], a[52:55], v[156:159]\n" \
"buffer_load_dwordx4 v215, %[srdv], s48 offen offset:3072 lds\n" \
"v_mfma_f32_16x16x32_bf16 v[156:159], a[112:115], a[48:51], v[156:159]\n" \
"v_exp_f32_e64 v151, v151\n" \
"v_mfma_f32_16x16x32_bf16 v[112:115], a[176:179], v[204:207], v[112:115]\n" \
"ds_read_b64_tr_b16 a[174:175], v211 offset:61952\n" \
"ds_read_b64_tr_b16 a[176:177], %[vvb] offset:58368\n" \
"v_mfma_f32_16x16x32_bf16 v[112:115], a[144:147], v[220:223], v[112:115]\n" \
"v_exp_f32_e64 v152, v152\n" \
"v_mfma_f32_16x16x32_bf16 v[116:119], a[180:183], v[204:207], v[116:119]\n" \
"v_cvt_pk_bf16_f32 v202, v148, v149\n" \
"v_cvt_pk_bf16_f32 v203, v150, v151\n" \
"v_mfma_f32_16x16x32_bf16 v[116:119], a[148:151], v[220:223], v[116:119]\n" \
"v_exp_f32_e64 v153, v153\n" \
"v_mfma_f32_16x16x32_bf16 v[120:123], a[184:187], v[204:207], v[120:123]\n" \
"v_exp_f32_e64 v154, v154\n" \
"v_mfma_f32_16x16x32_bf16 v[120:123], a[152:155], v[220:223], v[120:123]\n" \
"ds_read_b64_tr_b16 a[178:179], %[vvb] offset:62464\n" \
"v_mfma_f32_16x16x32_bf16 v[124:127], a[188:191], v[204:207], v[124:127]\n" \
"v_exp_f32_e64 v155, v155\n" \
"v_mfma_f32_16x16x32_bf16 v[124:127], a[156:159], v[220:223], v[124:127]\n" \
"ds_read_b64_tr_b16 a[180:181], v211 offset:58368\n" \
"s_waitcnt lgkmcnt(13)\n" \
"v_mfma_f32_16x16x32_bf16 v[128:131], a[64:67], a[0:3], v[160:163]\n" \
"v_exp_f32_e32 v156, v156\n" \
"v_mfma_f32_16x16x32_bf16 v[128:131], a[68:71], a[4:7], v[128:131]\n" \
"ds_read_b64_tr_b16 a[182:183], v211 offset:62464\n" \
"s_add_u32 m0, %[m0b], 49152\n" \
"v_mfma_f32_16x16x32_bf16 v[128:131], a[72:75], a[8:11], v[128:131]\n" \
"v_exp_f32_e64 v157, v157\n" \
"v_mfma_f32_16x16x32_bf16 v[128:131], a[76:79], a[12:15], v[128:131]\n" \
"v_cvt_pk_bf16_f32 v204, v152, v153\n" \
"v_cvt_pk_bf16_f32 v205, v154, v155\n" \
"v_mfma_f32_16x16x32_bf16 v[132:135], a[92:95], a[12:15], v[160:163]\n" \
"v_exp_f32_e64 v158, v158\n" \
"v_mfma_f32_16x16x32_bf16 v[132:135], a[88:91], a[8:11], v[132:135]\n" \
"ds_read_b64_tr_b16 a[184:185], %[vvb] offset:58880\n" \
"s_waitcnt lgkmcnt(5)\n" \
"v_mfma_f32_16x16x32_bf16 v[132:135], a[84:87], a[4:7], v[132:135]\n" \
"buffer_load_dwordx4 %[kd0], %[srdk], s48 offen offset:0 lds\n" \
"v_mfma_f32_16x16x32_bf16 v[132:135], a[80:83], a[0:3], v[132:135]\n" \
"v_exp_f32_e32 v159, v159\n" \
"v_mfma_f32_16x16x32_bf16 v[64:67], a[128:131], v[216:219], v[64:67]\n" \
"ds_read_b64_tr_b16 a[186:187], %[vvb] offset:62976\n" \
"ds_read_b64_tr_b16 a[188:189], v211 offset:58880\n" \
"v_mfma_f32_16x16x32_bf16 v[64:67], a[160:163], v[200:203], v[64:67]\n" \
"v_exp_f32_e64 v128, v128\n" \
"v_mfma_f32_16x16x32_bf16 v[68:71], a[132:135], v[216:219], v[68:71]\n" \
"v_cvt_pk_bf16_f32 v206, v156, v157\n" \
"v_cvt_pk_bf16_f32 v207, v158, v159\n" \
"v_mfma_f32_16x16x32_bf16 v[68:71], a[164:167], v[200:203], v[68:71]\n" \
"v_exp_f32_e64 v129, v129\n" \
"v_mfma_f32_16x16x32_bf16 v[72:75], a[136:139], v[216:219], v[72:75]\n" \
"ds_read_b64_tr_b16 a[190:191], v211 offset:62976\n" \
"ds_read_b64_tr_b16 a[128:129], %[vvb] offset:0\n" \
"v_mfma_f32_16x16x32_bf16 v[72:75], a[168:171], v[200:203], v[72:75]\n" \
"v_exp_f32_e64 v130, v130\n" \
"v_mfma_f32_16x16x32_bf16 v[76:79], a[140:143], v[216:219], v[76:79]\n" \
"ds_read_b64_tr_b16 a[130:131], %[vvb] offset:4096\n" \
"ds_read_b64_tr_b16 a[132:133], v211 offset:0\n" \
"v_mfma_f32_16x16x32_bf16 v[76:79], a[172:175], v[200:203], v[76:79]\n" \
"v_exp_f32_e64 v131, v131\n" \
"v_smfmac_f32_16x16x64_bf16 v[176:179], a[192:195], v[192:199], v228\n" \
"ds_read_b64_tr_b16 a[134:135], v211 offset:4096\n" \
"ds_read_b64_tr_b16 a[136:137], %[vvb] offset:512\n" \
"v_mfma_f32_16x16x32_bf16 v[136:139], a[64:67], a[16:19], v[164:167]\n" \
"v_exp_f32_e64 v132, v132\n" \
"v_mfma_f32_16x16x32_bf16 v[136:139], a[68:71], a[20:23], v[136:139]\n" \
"ds_read_b64_tr_b16 a[138:139], %[vvb] offset:4608\n" \
"ds_read_b64_tr_b16 a[140:141], v211 offset:512\n" \
"v_mfma_f32_16x16x32_bf16 v[136:139], a[72:75], a[24:27], v[136:139]\n" \
"v_exp_f32_e64 v133, v133\n" \
"v_mfma_f32_16x16x32_bf16 v[136:139], a[76:79], a[28:31], v[136:139]\n" \
"v_cvt_pk_bf16_f32 v184, v128, v129\n" \
"v_cvt_pk_bf16_f32 v185, v130, v131\n" \
"v_mfma_f32_16x16x32_bf16 v[140:143], a[92:95], a[28:31], v[164:167]\n" \
"v_exp_f32_e64 v134, v134\n" \
"v_mfma_f32_16x16x32_bf16 v[140:143], a[88:91], a[24:27], v[140:143]\n" \
"ds_read_b64_tr_b16 a[142:143], v211 offset:4608\n" \
"s_waitcnt lgkmcnt(8)\n" \
"v_mfma_f32_16x16x32_bf16 v[140:143], a[84:87], a[20:23], v[140:143]\n" \
"buffer_load_dwordx4 v212, %[srdk], s48 offen offset:1024 lds\n" \
"v_mfma_f32_16x16x32_bf16 v[140:143], a[80:83], a[16:19], v[140:143]\n" \
"v_exp_f32_e32 v135, v135\n" \
"v_mfma_f32_16x16x32_bf16 v[80:83], a[144:147], v[216:219], v[80:83]\n" \
"ds_read_b64_tr_b16 a[144:145], %[vvb] offset:1024\n" \
"ds_read_b64_tr_b16 a[146:147], %[vvb] offset:5120\n" \
"v_mfma_f32_16x16x32_bf16 v[80:83], a[176:179], v[200:203], v[80:83]\n" \
"v_exp_f32_e64 v136, v136\n" \
"v_mfma_f32_16x16x32_bf16 v[84:87], a[148:151], v[216:219], v[84:87]\n" \
"v_cvt_pk_bf16_f32 v186, v132, v133\n" \
"v_cvt_pk_bf16_f32 v187, v134, v135\n" \
"v_mfma_f32_16x16x32_bf16 v[84:87], a[180:183], v[200:203], v[84:87]\n" \
"v_exp_f32_e64 v137, v137\n" \
"v_mfma_f32_16x16x32_bf16 v[88:91], a[152:155], v[216:219], v[88:91]\n" \
"v_exp_f32_e64 v138, v138\n" \
"v_mfma_f32_16x16x32_bf16 v[88:91], a[184:187], v[200:203], v[88:91]\n" \
"ds_read_b64_tr_b16 a[148:149], v211 offset:1024\n" \
"ds_read_b64_tr_b16 a[150:151], v211 offset:5120\n" \
"v_mfma_f32_16x16x32_bf16 v[92:95], a[156:159], v[216:219], v[92:95]\n" \
"v_exp_f32_e64 v139, v139\n" \
"v_mfma_f32_16x16x32_bf16 v[92:95], a[188:191], v[200:203], v[92:95]\n" \
"ds_read_b64_tr_b16 a[152:153], %[vvb] offset:1536\n" \
"s_cbranch_vccz Lpp2_%=\n" \
"buffer_store_dwordx4 a[196:199], v235, %[srdz], s62 offen offset:1024 sc1\n" \
"buffer_store_dwordx4 a[196:199], v226, %[srdz], s62 offen offset:1024 sc1\n" \
"Lpp2_%=:\n" \
"s_add_u32 s62, s62, 2048\n" \
"v_mfma_f32_16x16x32_bf16 v[144:147], a[64:67], a[32:35], v[168:171]\n" \
"v_exp_f32_e32 v140, v140\n" \
"v_mfma_f32_16x16x32_bf16 v[144:147], a[68:71], a[36:39], v[144:147]\n" \
"ds_read_b64_tr_b16 a[154:155], %[vvb] offset:5632\n" \
"ds_read_b64_tr_b16 a[156:157], v211 offset:1536\n" \
"v_mfma_f32_16x16x32_bf16 v[144:147], a[72:75], a[40:43], v[144:147]\n" \
"v_exp_f32_e64 v141, v141\n" \
"v_mfma_f32_16x16x32_bf16 v[144:147], a[76:79], a[44:47], v[144:147]\n" \
"v_cvt_pk_bf16_f32 v188, v136, v137\n" \
"v_cvt_pk_bf16_f32 v189, v138, v139\n" \
"v_mfma_f32_16x16x32_bf16 v[148:151], a[92:95], a[44:47], v[168:171]\n" \
"v_exp_f32_e64 v142, v142\n" \
"v_mfma_f32_16x16x32_bf16 v[148:151], a[88:91], a[40:43], v[148:151]\n" \
"ds_read_b64_tr_b16 a[158:159], v211 offset:5632\n" \
"s_waitcnt lgkmcnt(8)\n" \
"v_mfma_f32_16x16x32_bf16 v[148:151], a[84:87], a[36:39], v[148:151]\n" \
"buffer_load_dwordx4 v213, %[srdk], s48 offen offset:2048 lds\n" \
"v_mfma_f32_16x16x32_bf16 v[148:151], a[80:83], a[32:35], v[148:151]\n" \
"v_exp_f32_e32 v143, v143\n" \
"v_mfma_f32_16x16x32_bf16 v[0:3], a[160:163], v[192:195], v[0:3]\n" \
"ds_read_b128 a[96:99], %[kb0] offset:8192\n" \
"v_mfma_f32_16x16x32_bf16 v[0:3], a[128:131], v[184:187], v[0:3]\n" \
"v_exp_f32_e64 v144, v144\n" \
"v_mfma_f32_16x16x32_bf16 v[4:7], a[164:167], v[192:195], v[4:7]\n" \
"v_cvt_pk_bf16_f32 v190, v140, v141\n" \
"v_cvt_pk_bf16_f32 v191, v142, v143\n" \
"v_mfma_f32_16x16x32_bf16 v[4:7], a[132:135], v[184:187], v[4:7]\n" \
"v_exp_f32_e64 v145, v145\n" \
"v_mfma_f32_16x16x32_bf16 v[8:11], a[168:171], v[192:195], v[8:11]\n" \
"ds_read_b128 a[100:103], v208 offset:8192\n" \
"v_mfma_f32_16x16x32_bf16 v[8:11], a[136:139], v[184:187], v[8:11]\n" \
"v_exp_f32_e64 v146, v146\n" \
"v_mfma_f32_16x16x32_bf16 v[12:15], a[172:175], v[192:195], v[12:15]\n" \
"ds_read_b128 a[104:107], v209 offset:8192\n" \
"v_mfma_f32_16x16x32_bf16 v[12:15], a[140:143], v[184:187], v[12:15]\n" \
"v_exp_f32_e64 v147, v147\n" \
"v_smfmac_f32_16x16x64_bf16 v[180:183], a[192:195], v[200:207], v228\n" \
"ds_read_b128 a[108:111], v210 offset:8192\n" \
"v_mfma_f32_16x16x32_bf16 v[152:155], a[64:67], a[48:51], v[172:175]\n" \
"v_exp_f32_e64 v148, v148\n" \
"v_mfma_f32_16x16x32_bf16 v[152:155], a[68:71], a[52:55], v[152:155]\n" \
"ds_read_b128 a[124:127], v210 offset:12288\n" \
"v_mfma_f32_16x16x32_bf16 v[152:155], a[72:75], a[56:59], v[152:155]\n" \
"v_exp_f32_e64 v149, v149\n" \
"v_mfma_f32_16x16x32_bf16 v[152:155], a[76:79], a[60:63], v[152:155]\n" \
"v_cvt_pk_bf16_f32 v216, v144, v145\n" \
"v_cvt_pk_bf16_f32 v217, v146, v147\n" \
"v_mfma_f32_16x16x32_bf16 v[156:159], a[92:95], a[60:63], v[172:175]\n" \
"v_exp_f32_e64 v150, v150\n" \
"v_mfma_f32_16x16x32_bf16 v[156:159], a[88:91], a[56:59], v[156:159]\n" \
"ds_read_b128 a[120:123], v209 offset:12288\n" \
"s_waitcnt lgkmcnt(6)\n" \
"v_mfma_f32_16x16x32_bf16 v[156:159], a[84:87], a[52:55], v[156:159]\n" \
"buffer_load_dwordx4 v214, %[srdk], s48 offen offset:3072 lds\n" \
"v_mfma_f32_16x16x32_bf16 v[156:159], a[80:83], a[48:51], v[156:159]\n" \
"v_exp_f32_e32 v151, v151\n" \
"v_mfma_f32_16x16x32_bf16 v[16:19], a[176:179], v[192:195], v[16:19]\n" \
"ds_read_b128 a[116:119], v208 offset:12288\n" \
"v_mfma_f32_16x16x32_bf16 v[16:19], a[144:147], v[184:187], v[16:19]\n" \
"v_exp_f32_e64 v152, v152\n" \
"v_mfma_f32_16x16x32_bf16 v[20:23], a[180:183], v[192:195], v[20:23]\n" \
"v_cvt_pk_bf16_f32 v218, v148, v149\n" \
"v_cvt_pk_bf16_f32 v219, v150, v151\n" \
"v_mfma_f32_16x16x32_bf16 v[20:23], a[148:151], v[184:187], v[20:23]\n" \
"v_exp_f32_e64 v153, v153\n" \
"v_mfma_f32_16x16x32_bf16 v[24:27], a[184:187], v[192:195], v[24:27]\n" \
"v_exp_f32_e64 v154, v154\n" \
"v_mfma_f32_16x16x32_bf16 v[24:27], a[152:155], v[184:187], v[24:27]\n" \
"ds_read_b128 a[112:115], %[kb0] offset:12288\n" \
"s_add_u32 s48, s48, 0x4000\n" \
"v_mfma_f32_16x16x32_bf16 v[28:31], a[188:191], v[192:195], v[28:31]\n" \
"v_exp_f32_e64 v155, v155\n" \
"v_mfma_f32_16x16x32_bf16 v[28:31], a[156:159], v[184:187], v[28:31]\n" \
"s_waitcnt vmcnt(14) lgkmcnt(2)\n" \
"s_barrier\n" \
"v_mfma_f32_16x16x32_bf16 v[128:131], a[96:99], a[0:3], v[160:163]\n" \
"v_exp_f32_e64 v156, v156\n" \
"v_mfma_f32_16x16x32_bf16 v[128:131], a[100:103], a[4:7], v[128:131]\n" \
"s_add_u32 m0, %[m0b], 114688\n" \
"v_mfma_f32_16x16x32_bf16 v[128:131], a[104:107], a[8:11], v[128:131]\n" \
"v_exp_f32_e64 v157, v157\n" \
"v_mfma_f32_16x16x32_bf16 v[128:131], a[108:111], a[12:15], v[128:131]\n" \
"v_cvt_pk_bf16_f32 v220, v152, v153\n" \
"v_cvt_pk_bf16_f32 v221, v154, v155\n" \
"v_mfma_f32_16x16x32_bf16 v[132:135], a[124:127], a[12:15], v[160:163]\n" \
"v_exp_f32_e64 v158, v158\n" \
"v_mfma_f32_16x16x32_bf16 v[132:135], a[120:123], a[8:11], v[132:135]\n" \
"s_waitcnt lgkmcnt(0)\n" \
"v_mfma_f32_16x16x32_bf16 v[132:135], a[116:119], a[4:7], v[132:135]\n" \
"buffer_load_dwordx4 %[vd0], %[srdv], s48 offen offset:0 lds\n" \
"v_mfma_f32_16x16x32_bf16 v[132:135], a[112:115], a[0:3], v[132:135]\n" \
"v_exp_f32_e32 v159, v159\n" \
".if . - adaln_body != 20480\n" \
".error \"size model\"\n" \
".endif\n" \
"Lk4_%=:\n" \
"s_cbranch_execz Lk5_%=\n" \
"s_nop 0\n" \
"v_mfma_f32_16x16x32_bf16 v[32:35], a[160:163], v[196:199], v[32:35]\n" \
"ds_read_b128 a[64:67], %[kb0] offset:16384\n" \
"v_mfma_f32_16x16x32_bf16 v[32:35], a[128:131], v[188:191], v[32:35]\n" \
"v_exp_f32_e64 v128, v128\n" \
"v_mfma_f32_16x16x32_bf16 v[36:39], a[164:167], v[196:199], v[36:39]\n" \
"v_cvt_pk_bf16_f32 v222, v156, v157\n" \
"v_cvt_pk_bf16_f32 v223, v158, v159\n" \
"v_mfma_f32_16x16x32_bf16 v[36:39], a[132:135], v[188:191], v[36:39]\n" \
"v_exp_f32_e64 v129, v129\n" \
"v_mfma_f32_16x16x32_bf16 v[40:43], a[168:171], v[196:199], v[40:43]\n" \
"ds_read_b128 a[68:71], v208 offset:16384\n" \
"v_mfma_f32_16x16x32_bf16 v[40:43], a[136:139], v[188:191], v[40:43]\n" \
"v_exp_f32_e64 v130, v130\n" \
"v_mfma_f32_16x16x32_bf16 v[44:47], a[172:175], v[196:199], v[44:47]\n" \
"ds_read_b128 a[72:75], v209 offset:16384\n" \
"v_mfma_f32_16x16x32_bf16 v[44:47], a[140:143], v[188:191], v[44:47]\n" \
"v_exp_f32_e64 v131, v131\n" \
"v_smfmac_f32_16x16x64_bf16 v[176:179], a[192:195], v[184:191], v228\n" \
"v_mfma_f32_16x16x32_bf16 v[136:139], a[96:99], a[16:19], v[164:167]\n" \
"v_exp_f32_e64 v132, v132\n" \
"v_mfma_f32_16x16x32_bf16 v[136:139], a[100:103], a[20:23], v[136:139]\n" \
"ds_read_b128 a[76:79], v210 offset:16384\n" \
"v_mfma_f32_16x16x32_bf16 v[136:139], a[104:107], a[24:27], v[136:139]\n" \
"v_exp_f32_e64 v133, v133\n" \
"v_mfma_f32_16x16x32_bf16 v[136:139], a[108:111], a[28:31], v[136:139]\n" \
"v_cvt_pk_bf16_f32 v192, v128, v129\n" \
"v_cvt_pk_bf16_f32 v193, v130, v131\n" \
"v_mfma_f32_16x16x32_bf16 v[140:143], a[124:127], a[28:31], v[164:167]\n" \
"v_exp_f32_e64 v134, v134\n" \
"v_mfma_f32_16x16x32_bf16 v[140:143], a[120:123], a[24:27], v[140:143]\n" \
"ds_read_b128 a[92:95], v210 offset:20480\n" \
"v_mfma_f32_16x16x32_bf16 v[140:143], a[116:119], a[20:23], v[140:143]\n" \
"buffer_load_dwordx4 v215, %[srdv], s48 offen offset:1024 lds\n" \
"v_mfma_f32_16x16x32_bf16 v[140:143], a[112:115], a[16:19], v[140:143]\n" \
"v_exp_f32_e64 v135, v135\n" \
"v_mfma_f32_16x16x32_bf16 v[48:51], a[176:179], v[196:199], v[48:51]\n" \
"ds_read_b128 a[88:91], v209 offset:20480\n" \
"v_mfma_f32_16x16x32_bf16 v[48:51], a[144:147], v[188:191], v[48:51]\n" \
"v_exp_f32_e64 v136, v136\n" \
"v_mfma_f32_16x16x32_bf16 v[52:55], a[180:183], v[196:199], v[52:55]\n" \
"v_cvt_pk_bf16_f32 v194, v132, v133\n" \
"v_cvt_pk_bf16_f32 v195, v134, v135\n" \
"v_mfma_f32_16x16x32_bf16 v[52:55], a[148:151], v[188:191], v[52:55]\n" \
"v_exp_f32_e64 v137, v137\n" \
"v_mfma_f32_16x16x32_bf16 v[56:59], a[184:187], v[196:199], v[56:59]\n" \
"v_exp_f32_e64 v138, v138\n" \
"v_mfma_f32_16x16x32_bf16 v[56:59], a[152:155], v[188:191], v[56:59]\n" \
"ds_read_b128 a[84:87], v208 offset:20480\n" \
"v_mfma_f32_16x16x32_bf16 v[60:63], a[188:191], v[196:199], v[60:63]\n" \
"v_exp_f32_e64 v139, v139\n" \
"v_mfma_f32_16x16x32_bf16 v[60:63], a[156:159], v[188:191], v[60:63]\n" \
"s_cmp_lg_u32 %[wid], 3\n" \
"s_cbranch_scc1 LtA01_%=\n" \
"global_load_dword v237, v224, s[58:59]\n" \
"s_add_u32 s61, s48, 49152\n" \
"s_mov_b32 exec_hi, 0\n" \
"s_nop 0\n" \
"buffer_load_dword v237, v224, %[srdp], s61 offen\n" \
"s_mov_b32 exec_hi, -1\n" \
"s_nop 0\n" \
"LtA01_%=:\n" \
"v_mfma_f32_16x16x32_bf16 v[144:147], a[96:99], a[32:35], v[168:171]\n" \
"v_exp_f32_e64 v140, v140\n" \
"v_mfma_f32_16x16x32_bf16 v[144:147], a[100:103], a[36:39], v[144:147]\n" \
"ds_read_b128 a[80:83], %[kb0] offset:20480\n" \
"v_mfma_f32_16x16x32_bf16 v[144:147], a[104:107], a[40:43], v[144:147]\n" \
"v_exp_f32_e64 v141, v141\n" \
"v_mfma_f32_16x16x32_bf16 v[144:147], a[108:111], a[44:47], v[144:147]\n" \
"v_cvt_pk_bf16_f32 v196, v136, v137\n" \
"v_cvt_pk_bf16_f32 v197, v138, v139\n" \
"v_mfma_f32_16x16x32_bf16 v[148:151], a[124:127], a[44:47], v[168:171]\n" \
"v_exp_f32_e64 v142, v142\n" \
"v_mfma_f32_16x16x32_bf16 v[148:151], a[120:123], a[40:43], v[148:151]\n" \
"v_mfma_f32_16x16x32_bf16 v[148:151], a[116:119], a[36:39], v[148:151]\n" \
"buffer_load_dwordx4 %[vd0], %[srdv], s48 offen offset:2048 lds\n" \
"v_mfma_f32_16x16x32_bf16 v[148:151], a[112:115], a[32:35], v[148:151]\n" \
"v_exp_f32_e64 v143, v143\n" \
"v_mfma_f32_16x16x32_bf16 v[96:99], a[160:163], v[204:207], v[96:99]\n" \
"ds_read_b64_tr_b16 a[160:161], %[vvb] offset:8192\n" \
"v_mfma_f32_16x16x32_bf16 v[96:99], a[128:131], v[220:223], v[96:99]\n" \
"v_exp_f32_e64 v144, v144\n" \
"v_mfma_f32_16x16x32_bf16 v[100:103], a[164:167], v[204:207], v[100:103]\n" \
"v_cvt_pk_bf16_f32 v198, v140, v141\n" \
"v_cvt_pk_bf16_f32 v199, v142, v143\n" \
"v_mfma_f32_16x16x32_bf16 v[100:103], a[132:135], v[220:223], v[100:103]\n" \
"v_exp_f32_e64 v145, v145\n" \
"v_mfma_f32_16x16x32_bf16 v[104:107], a[168:171], v[204:207], v[104:107]\n" \
"ds_read_b64_tr_b16 a[162:163], %[vvb] offset:12288\n" \
"ds_read_b64_tr_b16 a[164:165], v211 offset:8192\n" \
"v_mfma_f32_16x16x32_bf16 v[104:107], a[136:139], v[220:223], v[104:107]\n" \
"v_exp_f32_e64 v146, v146\n" \
"v_mfma_f32_16x16x32_bf16 v[108:111], a[172:175], v[204:207], v[108:111]\n" \
"ds_read_b64_tr_b16 a[166:167], v211 offset:12288\n" \
"v_mfma_f32_16x16x32_bf16 v[108:111], a[140:143], v[220:223], v[108:111]\n" \
"v_exp_f32_e64 v147, v147\n" \
"v_smfmac_f32_16x16x64_bf16 v[180:183], a[192:195], v[216:223], v228\n" \
"ds_read_b64_tr_b16 a[168:169], %[vvb] offset:8704\n" \
"v_mfma_f32_16x16x32_bf16 v[152:155], a[96:99], a[48:51], v[172:175]\n" \
"v_exp_f32_e64 v148, v148\n" \
"v_mfma_f32_16x16x32_bf16 v[152:155], a[100:103], a[52:55], v[152:155]\n" \
"ds_read_b64_tr_b16 a[170:171], %[vvb] offset:12800\n" \
"v_mfma_f32_16x16x32_bf16 v[152:155], a[104:107], a[56:59], v[152:155]\n" \
"v_exp_f32_e64 v149, v149\n" \
"v_mfma_f32_16x16x32_bf16 v[152:155], a[108:111], a[60:63], v[152:155]\n" \
"v_cvt_pk_bf16_f32 v200, v144, v145\n" \
"v_cvt_pk_bf16_f32 v201, v146, v147\n" \
"v_mfma_f32_16x16x32_bf16 v[156:159], a[124:127], a[60:63], v[172:175]\n" \
"v_exp_f32_e64 v150, v150\n" \
"v_mfma_f32_16x16x32_bf16 v[156:159], a[120:123], a[56:59], v[156:159]\n" \
"ds_read_b64_tr_b16 a[172:173], v211 offset:8704\n" \
"v_mfma_f32_16x16x32_bf16 v[156:159], a[116:119], a[52:55], v[156:159]\n" \
"buffer_load_dwordx4 v215, %[srdv], s48 offen offset:3072 lds\n" \
"v_mfma_f32_16x16x32_bf16 v[156:159], a[112:115], a[48:51], v[156:159]\n" \
"v_exp_f32_e64 v151, v151\n" \
"v_mfma_f32_16x16x32_bf16 v[112:115], a[176:179], v[204:207], v[112:115]\n" \
"ds_read_b64_tr_b16 a[174:175], v211 offset:12800\n" \
"ds_read_b64_tr_b16 a[176:177], %[vvb] offset:9216\n" \
"v_mfma_f32_16x16x32_bf16 v[112:115], a[144:147], v[220:223], v[112:115]\n" \
"v_exp_f32_e64 v152, v152\n" \
"v_mfma_f32_16x16x32_bf16 v[116:119], a[180:183], v[204:207], v[116:119]\n" \
"v_cvt_pk_bf16_f32 v202, v148, v149\n" \
"v_cvt_pk_bf16_f32 v203, v150, v151\n" \
"v_mfma_f32_16x16x32_bf16 v[116:119], a[148:151], v[220:223], v[116:119]\n" \
"v_exp_f32_e64 v153, v153\n" \
"v_mfma_f32_16x16x32_bf16 v[120:123], a[184:187], v[204:207], v[120:123]\n" \
"v_exp_f32_e64 v154, v154\n" \
"v_mfma_f32_16x16x32_bf16 v[120:123], a[152:155], v[220:223], v[120:123]\n" \
"ds_read_b64_tr_b16 a[178:179], %[vvb] offset:13312\n" \
"v_mfma_f32_16x16x32_bf16 v[124:127], a[188:191], v[204:207], v[124:127]\n" \
"v_exp_f32_e64 v155, v155\n" \
"v_mfma_f32_16x16x32_bf16 v[124:127], a[156:159], v[220:223], v[124:127]\n" \
"ds_read_b64_tr_b16 a[180:181], v211 offset:9216\n" \
"s_waitcnt lgkmcnt(13)\n" \
"s_cmp_lg_u32 s60, 4\n" \
"s_cbranch_scc1 LmA_%=\n" \
"s_nop 0\n" \
"v_readlane_b32 s64, %[tlo], 9\n" \
"v_readlane_b32 s65, %[thi], 9\n" \
"s_mov_b32 s66, %[wbytes]\n" \
"v_mov_b32_e32 v229, %[vg0]\n" \
"s_mov_b32 s44, %[m0t]\n" \
"s_mov_b32 s45, 0\n" \
"s_cmp_lg_u32 s52, 0\n" \
"s_cbranch_scc1 LmB_%=\n" \
"v_readlane_b32 s61, %[thi], 21\n" \
"s_add_u32 s62, %[sin], 16384\n" \
"s_add_u32 s63, %[sin], 49152\n" \
"s_add_u32 s55, s61, 4096\n" \
"s_nop 1\n" \
"s_nop 0\n" \
"buffer_store_dwordx4 a[196:199], v235, %[srdz], s61 offen offset:0 sc1\n" \
"buffer_store_dwordx4 a[196:199], v235, %[srdz], s55 offen offset:0 sc1\n" \
"s_waitcnt vmcnt(14)\n" \
"s_nop 0\n" \
"ds_read_b64_tr_b16 a[232:233], %[ta0] offset:0\n" \
"ds_read_b64_tr_b16 a[234:235], %[ta0] offset:128\n" \
"ds_read_b64_tr_b16 a[236:237], %[ta0] offset:1024\n" \
"ds_read_b64_tr_b16 a[238:239], %[ta0] offset:1152\n" \
"ds_read_b64_tr_b16 a[240:241], %[ta0] offset:2048\n" \
"ds_read_b64_tr_b16 a[242:243], %[ta0] offset:2176\n" \
"ds_read_b64_tr_b16 a[244:245], %[ta0] offset:3072\n" \
"ds_read_b64_tr_b16 a[246:247], %[ta0] offset:3200\n" \
"ds_read_b64_tr_b16 a[248:249], %[ta0] offset:4096\n" \
"ds_read_b64_tr_b16 a[250:251], %[ta0] offset:4224\n" \
"ds_read_b64_tr_b16 a[252:253], %[ta0] offset:5120\n" \
"ds_read_b64_tr_b16 a[254:255], %[ta0] offset:5248\n" \
"v_add_u32_e32 v234, %[m0t], v235\n" \
"s_waitcnt lgkmcnt(8)\n" \
"v_mfma_f32_16x16x32_bf16 v[230:233], a[232:235], a[200:203], 0\n" \
"v_mfma_f32_16x16x32_bf16 v[230:233], a[236:239], a[204:207], v[230:233]\n" \
"ds_read_b64_tr_b16 a[232:233], %[ta0] offset:6144\n" \
"ds_read_b64_tr_b16 a[234:235], %[ta0] offset:6272\n" \
"ds_read_b64_tr_b16 a[236:237], %[ta0] offset:7168\n" \
"ds_read_b64_tr_b16 a[238:239], %[ta0] offset:7296\n" \
"s_waitcnt lgkmcnt(4)\n" \
"s_nop 0\n" \
"v_mfma_f32_16x16x32_bf16 v[230:233], a[240:243], a[208:211], v[230:233]\n" \
"v_mfma_f32_16x16x32_bf16 v[230:233], a[244:247], a[212:215], v[230:233]\n" \
"v_mfma_f32_16x16x32_bf16 v[230:233], a[248:251], a[216:219], v[230:233]\n" \
"v_mfma_f32_16x16x32_bf16 v[230:233], a[252:255], a[220:223], v[230:233]\n" \
"s_waitcnt lgkmcnt(0)\n" \
"s_nop 0\n" \
"v_mfma_f32_16x16x32_bf16 v[230:233], a[232:235], a[224:227], v[230:233]\n" \
"v_mfma_f32_16x16x32_bf16 v[230:233], a[236:239], a[228:231], v[230:233]\n" \
"s_nop 7\n" \
"s_nop 0\n" \
"ds_write_b128 v234, v[230:233] offset:7168\n" \
"LmB_%=:\n" \
"v_mfma_f32_16x16x32_bf16 v[128:131], a[64:67], a[0:3], v[160:163]\n" \
"v_exp_f32_e64 v156, v156\n" \
"v_mfma_f32_16x16x32_bf16 v[128:131], a[68:71], a[4:7], v[128:131]\n" \
"ds_read_b64_tr_b16 a[182:183], v211 offset:13312\n" \
"s_add_u32 m0, %[m0b], 0\n" \
"v_mfma_f32_16x16x32_bf16 v[128:131], a[72:75], a[8:11], v[128:131]\n" \
"v_exp_f32_e32 v157, v157\n" \
"v_mfma_f32_16x16x32_bf16 v[128:131], a[76:79], a[12:15], v[128:131]\n" \
"v_cvt_pk_bf16_f32 v204, v152, v153\n" \
"v_cvt_pk_bf16_f32 v205, v154, v155\n" \
"v_mfma_f32_16x16x32_bf16 v[132:135], a[92:95], a[12:15], v[160:163]\n" \
"v_exp_f32_e64 v158, v158\n" \
"v_mfma_f32_16x16x32_bf16 v[132:135], a[88:91], a[8:11], v[132:135]\n" \
"ds_read_b64_tr_b16 a[184:185], %[vvb] offset:9728\n" \
"s_waitcnt lgkmcnt(5)\n" \
"v_mfma_f32_16x16x32_bf16 v[132:135], a[84:87], a[4:7], v[132:135]\n" \
"buffer_load_dwordx4 %[kd0], %[srdk], s48 offen offset:0 lds\n" \
"v_mfma_f32_16x16x32_bf16 v[132:135], a[80:83], a[0:3], v[132:135]\n" \
"v_exp_f32_e32 v159, v159\n" \
"v_mfma_f32_16x16x32_bf16 v[64:67], a[128:131], v[216:219], v[64:67]\n" \
"ds_read_b64_tr_b16 a[186:187], %[vvb] offset:13824\n" \
"ds_read_b64_tr_b16 a[188:189], v211 offset:9728\n" \
"v_mfma_f32_16x16x32_bf16 v[64:67], a[160:163], v[200:203], v[64:67]\n" \
"v_exp_f32_e64 v128, v128\n" \
"v_mfma_f32_16x16x32_bf16 v[68:71], a[132:135], v[216:219], v[68:71]\n" \
"v_cvt_pk_bf16_f32 v206, v156, v157\n" \
"v_cvt_pk_bf16_f32 v207, v158, v159\n" \
"v_mfma_f32_16x16x32_bf16 v[68:71], a[164:167], v[200:203], v[68:71]\n" \
"v_exp_f32_e64 v129, v129\n" \
"v_mfma_f32_16x16x32_bf16 v[72:75], a[136:139], v[216:219], v[72:75]\n" \
"ds_read_b64_tr_b16 a[190:191], v211 offset:13824\n" \
"ds_read_b64_tr_b16 a[128:129], %[vvb] offset:16384\n" \
"s_add_u32 s60, s60, 1\n" \
"s_nop 0\n" \
"v_readlane_b32 s58, %[tlo], s60\n" \
"v_mfma_f32_16x16x32_bf16 v[72:75], a[168:171], v[200:203], v[72:75]\n" \
"v_exp_f32_e64 v130, v130\n" \
"v_mfma_f32_16x16x32_bf16 v[76:79], a[140:143], v[216:219], v[76:79]\n" \
"ds_read_b64_tr_b16 a[130:131], %[vvb] offset:20480\n" \
"ds_read_b64_tr_b16 a[132:133], v211 offset:16384\n" \
"v_readlane_b32 s59, %[thi], s60\n" \
"v_mfma_f32_16x16x32_bf16 v[76:79], a[172:175], v[200:203], v[76:79]\n" \
"v_exp_f32_e64 v131, v131\n" \
"v_smfmac_f32_16x16x64_bf16 v[176:179], a[192:195], v[192:199], v228\n" \
"ds_read_b64_tr_b16 a[134:135], v211 offset:20480\n" \
"ds_read_b64_tr_b16 a[136:137], %[vvb] offset:16896\n" \
"s_cmp_eq_u32 s60, 7\n" \
"s_cselect_b32 s61, 56, 1\n" \
"v_mul_u32_u24_e64 v236, s61, v224\n" \
"v_mfma_f32_16x16x32_bf16 v[136:139], a[64:67], a[16:19], v[164:167]\n" \
"v_exp_f32_e64 v132, v132\n" \
"v_mfma_f32_16x16x32_bf16 v[136:139], a[68:71], a[20:23], v[136:139]\n" \
"ds_read_b64_tr_b16 a[138:139], %[vvb] offset:20992\n" \
"ds_read_b64_tr_b16 a[140:141], v211 offset:16896\n" \
"v_mfma_f32_16x16x32_bf16 v[136:139], a[72:75], a[24:27], v[136:139]\n" \
"v_exp_f32_e64 v133, v133\n" \
"v_mfma_f32_16x16x32_bf16 v[136:139], a[76:79], a[28:31], v[136:139]\n" \
"v_cvt_pk_bf16_f32 v184, v128, v129\n" \
"v_cvt_pk_bf16_f32 v185, v130, v131\n" \
"v_mfma_f32_16x16x32_bf16 v[140:143], a[92:95], a[28:31], v[164:167]\n" \
"v_exp_f32_e64 v134, v134\n" \
"v_mfma_f32_16x16x32_bf16 v[140:143], a[88:91], a[24:27], v[140:143]\n" \
"ds_read_b64_tr_b16 a[142:143], v211 offset:20992\n" \
"s_waitcnt lgkmcnt(8)\n" \
"v_mfma_f32_16x16x32_bf16 v[140:143], a[84:87], a[20:23], v[140:143]\n" \
"buffer_load_dwordx4 v212, %[srdk], s48 offen offset:1024 lds\n" \
"v_mfma_f32_16x16x32_bf16 v[140:143], a[80:83], a[16:19], v[140:143]\n" \
"v_exp_f32_e32 v135, v135\n" \
"v_mfma_f32_16x16x32_bf16 v[80:83], a[144:147], v[216:219], v[80:83]\n" \
"ds_read_b64_tr_b16 a[144:145], %[vvb] offset:17408\n" \
"ds_read_b64_tr_b16 a[146:147], %[vvb] offset:21504\n" \
"v_mfma_f32_16x16x32_bf16 v[80:83], a[176:179], v[200:203], v[80:83]\n" \
"v_exp_f32_e64 v136, v136\n" \
"v_mfma_f32_16x16x32_bf16 v[84:87], a[148:151], v[216:219], v[84:87]\n" \
"v_cvt_pk_bf16_f32 v186, v132, v133\n" \
"v_cvt_pk_bf16_f32 v187, v134, v135\n" \
"v_mfma_f32_16x16x32_bf16 v[84:87], a[180:183], v[200:203], v[84:87]\n" \
"v_exp_f32_e64 v137, v137\n" \
"v_mfma_f32_16x16x32_bf16 v[88:91], a[152:155], v[216:219], v[88:91]\n" \
"v_exp_f32_e64 v138, v138\n" \
"v_mfma_f32_16x16x32_bf16 v[88:91], a[184:187], v[200:203], v[88:91]\n" \
"ds_read_b64_tr_b16 a[148:149], v211 offset:17408\n" \
"ds_read_b64_tr_b16 a[150:151], v211 offset:21504\n" \
"v_mfma_f32_16x16x32_bf16 v[92:95], a[156:159], v[216:219], v[92:95]\n" \
"v_exp_f32_e64 v139, v139\n" \
"v_mfma_f32_16x16x32_bf16 v[92:95], a[188:191], v[200:203], v[92:95]\n" \
"ds_read_b64_tr_b16 a[152:153], %[vvb] offset:17920\n" \
"s_cbranch_vccz Lpp3_%=\n" \
"buffer_store_dwordx4 a[196:199], v235, %[srdz], s63 offen offset:0 sc1\n" \
"buffer_store_dwordx4 a[196:199], v226, %[srdz], s63 offen offset:0 sc1\n" \
"Lpp3_%=:\n" \
"v_mfma_f32_16x16x32_bf16 v[144:147], a[64:67], a[32:35], v[168:171]\n" \
"v_exp_f32_e32 v140, v140\n" \
"v_mfma_f32_16x16x32_bf16 v[144:147], a[68:71], a[36:39], v[144:147]\n" \
"ds_read_b64_tr_b16 a[154:155], %[vvb] offset:22016\n" \
"ds_read_b64_tr_b16 a[156:157], v211 offset:17920\n" \
"v_mfma_f32_16x16x32_bf16 v[144:147], a[72:75], a[40:43], v[144:147]\n" \
"v_exp_f32_e64 v141, v141\n" \
"v_mfma_f32_16x16x32_bf16 v[144:147], a[76:79], a[44:47], v[144:147]\n" \
"v_cvt_pk_bf16_f32 v188, v136, v137\n" \
"v_cvt_pk_bf16_f32 v189, v138, v139\n" \
"v_mfma_f32_16x16x32_bf16 v[148:151], a[92:95], a[44:47], v[168:171]\n" \
"v_exp_f32_e64 v142, v142\n" \
"v_mfma_f32_16x16x32_bf16 v[148:151], a[88:91], a[40:43], v[148:151]\n" \
"ds_read_b64_tr_b16 a[158:159], v211 offset:22016\n" \
"s_waitcnt lgkmcnt(8)\n" \
"v_mfma_f32_16x16x32_bf16 v[148:151], a[84:87], a[36:39], v[148:151]\n" \
"buffer_load_dwordx4 v213, %[srdk], s48 offen offset:2048 lds\n" \
"v_mfma_f32_16x16x32_bf16 v[148:151], a[80:83], a[32:35], v[148:151]\n" \
"v_exp_f32_e32 v143, v143\n" \
"v_mfma_f32_16x16x32_bf16 v[0:3], a[160:163], v[192:195], v[0:3]\n" \
"ds_read_b128 a[96:99], %[kb0] offset:24576\n" \
"v_mfma_f32_16x16x32_bf16 v[0:3], a[128:131], v[184:187], v[0:3]\n" \
"v_exp_f32_e64 v144, v144\n" \
"v_mfma_f32_16x16x32_bf16 v[4:7], a[164:167], v[192:195], v[4:7]\n" \
"v_cvt_pk_bf16_f32 v190, v140, v141\n" \
"v_cvt_pk_bf16_f32 v191, v142, v143\n" \
"v_mfma_f32_16x16x32_bf16 v[4:7], a[132:135], v[184:187], v[4:7]\n" \
"v_exp_f32_e64 v145, v145\n" \
"v_mfma_f32_16x16x32_bf16 v[8:11], a[168:171], v[192:195], v[8:11]\n" \
"ds_read_b128 a[100:103], v208 offset:24576\n" \
"v_mfma_f32_16x16x32_bf16 v[8:11], a[136:139], v[184:187], v[8:11]\n" \
"v_exp_f32_e64 v146, v146\n" \
"v_mfma_f32_16x16x32_bf16 v[12:15], a[172:175], v[192:195], v[12:15]\n" \
"ds_read_b128 a[104:107], v209 offset:24576\n" \
"v_mfma_f32_16x16x32_bf16 v[12:15], a[140:143], v[184:187], v[12:15]\n" \
"v_exp_f32_e64 v147, v147\n" \
"v_smfmac_f32_16x16x64_bf16 v[180:183], a[192:195], v[200:207], v228\n" \
"ds_read_b128 a[108:111], v210 offset:24576\n" \
"v_mfma_f32_16x16x32_bf16 v[152:155], a[64:67], a[48:51], v[172:175]\n" \
"v_exp_f32_e64 v148, v148\n" \
"v_mfma_f32_16x16x32_bf16 v[152:155], a[68:71], a[52:55], v[152:155]\n" \
"ds_read_b128 a[124:127], v210 offset:28672\n" \
"v_mfma_f32_16x16x32_bf16 v[152:155], a[72:75], a[56:59], v[152:155]\n" \
"v_exp_f32_e64 v149, v149\n" \
"v_mfma_f32_16x16x32_bf16 v[152:155], a[76:79], a[60:63], v[152:155]\n" \
"v_cvt_pk_bf16_f32 v216, v144, v145\n" \
"v_cvt_pk_bf16_f32 v217, v146, v147\n" \
"v_mfma_f32_16x16x32_bf16 v[156:159], a[92:95], a[60:63], v[172:175]\n" \
"v_exp_f32_e64 v150, v150\n" \
"v_mfma_f32_16x16x32_bf16 v[156:159], a[88:91], a[56:59], v[156:159]\n" \
"ds_read_b128 a[120:123], v209 offset:28672\n" \
"s_waitcnt lgkmcnt(6)\n" \
"v_mfma_f32_16x16x32_bf16 v[156:159], a[84:87], a[52:55], v[156:159]\n" \
"buffer_load_dwordx4 v214, %[srdk], s48 offen offset:3072 lds\n" \
"v_mfma_f32_16x16x32_bf16 v[156:159], a[80:83], a[48:51], v[156:159]\n" \
"v_exp_f32_e32 v151, v151\n" \
"v_mfma_f32_16x16x32_bf16 v[16:19], a[176:179], v[192:195], v[16:19]\n" \
"ds_read_b128 a[116:119], v208 offset:28672\n" \
"v_mfma_f32_16x16x32_bf16 v[16:19], a[144:147], v[184:187], v[16:19]\n" \
"v_exp_f32_e64 v152, v152\n" \
"v_mfma_f32_16x16x32_bf16 v[20:23], a[180:183], v[192:195], v[20:23]\n" \
"v_cvt_pk_bf16_f32 v218, v148, v149\n" \
"v_cvt_pk_bf16_f32 v219, v150, v151\n" \
"v_mfma_f32_16x16x32_bf16 v[20:23], a[148:151], v[184:187], v[20:23]\n" \
"v_exp_f32_e64 v153, v153\n" \
"v_mfma_f32_16x16x32_bf16 v[24:27], a[184:187], v[192:195], v[24:27]\n" \
"v_exp_f32_e64 v154, v154\n" \
"v_mfma_f32_16x16x32_bf16 v[24:27], a[152:155], v[184:187], v[24:27]\n" \
"ds_read_b128 a[112:115], %[kb0] offset:28672\n" \
"s_add_u32 s48, s48, 0x4000\n" \
"s_mov_b32 m0, s44\n" \
"s_addk_i32 s44, 0x400\n" \
"buffer_load_dwordx4 v229, s[64:67], s45 offen offset:0 lds\n" \
"s_add_u32 s45, s45, 229376\n" \
"v_mfma_f32_16x16x32_bf16 v[28:31], a[188:191], v[192:195], v[28:31]\n" \
"v_exp_f32_e64 v155, v155\n" \
"v_mfma_f32_16x16x32_bf16 v[28:31], a[156:159], v[184:187], v[28:31]\n" \
"s_waitcnt vmcnt(13) lgkmcnt(2)\n" \
"s_barrier\n" \
"v_mfma_f32_16x16x32_bf16 v[128:131], a[96:99], a[0:3], v[160:163]\n" \
"v_exp_f32_e64 v156, v156\n" \
"v_mfma_f32_16x16x32_bf16 v[128:131], a[100:103], a[4:7], v[128:131]\n" \
"s_add_u32 m0, %[m0b], 65536\n" \
"v_mfma_f32_16x16x32_bf16 v[128:131], a[104:107], a[8:11], v[128:131]\n" \
"v_exp_f32_e64 v157, v157\n" \
"v_mfma_f32_16x16x32_bf16 v[128:131], a[108:111], a[12:15], v[128:131]\n" \
"v_cvt_pk_bf16_f32 v220, v152, v153\n" \
"v_cvt_pk_bf16_f32 v221, v154, v155\n" \
"v_mfma_f32_16x16x32_bf16 v[132:135], a[124:127], a[12:15], v[160:163]\n" \
"v_exp_f32_e64 v158, v158\n" \
"v_mfma_f32_16x16x32_bf16 v[132:135], a[120:123], a[8:11], v[132:135]\n" \
"s_waitcnt lgkmcnt(0)\n" \
"v_mfma_f32_16x16x32_bf16 v[132:135], a[116:119], a[4:7], v[132:135]\n" \
"buffer_load_dwordx4 %[vd0], %[srdv], s48 offen offset:0 lds\n" \
"v_mfma_f32_16x16x32_bf16 v[132:135], a[112:115], a[0:3], v[132:135]\n" \
"v_exp_f32_e32 v159, v159\n" \
"v_mfma_f32_16x16x32_bf16 v[32:35], a[160:163], v[196:199], v[32:35]\n" \
"ds_read_b128 a[64:67], %[kb0] offset:32768\n" \
"v_mfma_f32_16x16x32_bf16 v[32:35], a[128:131], v[188:191], v[32:35]\n" \
"v_exp_f32_e64 v128, v128\n" \
"v_mfma_f32_16x16x32_bf16 v[36:39], a[164:167], v[196:199], v[36:39]\n" \
"v_cvt_pk_bf16_f32 v222, v156, v157\n" \
"v_cvt_pk_bf16_f32 v223, v158, v159\n" \
"v_mfma_f32_16x16x32_bf16 v[36:39], a[132:135], v[188:191], v[36:39]\n" \
"v_exp_f32_e64 v129, v129\n" \
"v_mfma_f32_16x16x32_bf16 v[40:43], a[168:171], v[196:199], v[40:43]\n" \
"ds_read_b128 a[68:71], v208 offset:32768\n" \
"v_mfma_f32_16x16x32_bf16 v[40:43], a[136:139], v[188:191], v[40:43]\n" \
"v_exp_f32_e64 v130, v130\n" \
"v_mfma_f32_16x16x32_bf16 v[44:47], a[172:175], v[196:199], v[44:47]\n" \
"ds_read_b128 a[72:75], v209 offset:32768\n" \
"v_mfma_f32_16x16x32_bf16 v[44:47], a[140:143], v[188:191], v[44:47]\n" \
"v_exp_f32_e64 v131, v131\n" \
"v_smfmac_f32_16x16x64_bf16 v[176:179], a[192:195], v[184:191], v228\n" \
"v_mfma_f32_16x16x32_bf16 v[136:139], a[96:99], a[16:19], v[164:167]\n" \
"v_exp_f32_e64 v132, v132\n" \
"v_mfma_f32_16x16x32_bf16 v[136:139], a[100:103], a[20:23], v[136:139]\n" \
"ds_read_b128 a[76:79], v210 offset:32768\n" \
"v_mfma_f32_16x16x32_bf16 v[136:139], a[104:107], a[24:27], v[136:139]\n" \
"v_exp_f32_e64 v133, v133\n" \
"v_mfma_f32_16x16x32_bf16 v[136:139], a[108:111], a[28:31], v[136:139]\n" \
"v_cvt_pk_bf16_f32 v192, v128, v129\n" \
"v_cvt_pk_bf16_f32 v193, v130, v131\n" \
"v_mfma_f32_16x16x32_bf16 v[140:143], a[124:127], a[28:31], v[164:167]\n" \
"v_exp_f32_e64 v134, v134\n" \
"v_mfma_f32_16x16x32_bf16 v[140:143], a[120:123], a[24:27], v[140:143]\n" \
"ds_read_b128 a[92:95], v210 offset:36864\n" \
"v_mfma_f32_16x16x32_bf16 v[140:143], a[116:119], a[20:23], v[140:143]\n" \
"buffer_load_dwordx4 v215, %[srdv], s48 offen offset:1024 lds\n" \
"v_mfma_f32_16x16x32_bf16 v[140:143], a[112:115], a[16:19], v[140:143]\n" \
"v_exp_f32_e64 v135, v135\n" \
"v_mfma_f32_16x16x32_bf16 v[48:51], a[176:179], v[196:199], v[48:51]\n" \
"ds_read_b128 a[88:91], v209 offset:36864\n" \
"v_mfma_f32_16x16x32_bf16 v[48:51], a[144:147], v[188:191], v[48:51]\n" \
"v_exp_f32_e64 v136, v136\n" \
"v_mfma_f32_16x16x32_bf16 v[52:55], a[180:183], v[196:199], v[52:55]\n" \
"v_cvt_pk_bf16_f32 v194, v132, v133\n" \
"v_cvt_pk_bf16_f32 v195, v134, v135\n" \
"v_mfma_f32_16x16x32_bf16 v[52:55], a[148:151], v[188:191], v[52:55]\n" \
"v_exp_f32_e64 v137, v137\n" \
"v_mfma_f32_16x16x32_bf16 v[56:59], a[184:187], v[196:199], v[56:59]\n" \
"v_exp_f32_e64 v138, v138\n" \
"v_mfma_f32_16x16x32_bf16 v[56:59], a[152:155], v[188:191], v[56:59]\n" \
"ds_read_b128 a[84:87], v208 offset:36864\n" \
"v_mfma_f32_16x16x32_bf16 v[60:63], a[188:191], v[196:199], v[60:63]\n" \
"v_exp_f32_e64 v139, v139\n" \
"v_mfma_f32_16x16x32_bf16 v[60:63], a[156:159], v[188:191], v[60:63]\n" \
"s_cmp_lg_u32 %[wid], 0\n" \
"s_cbranch_scc1 LtB03_%=\n" \
"global_load_dword v237, v236, s[58:59]\n" \
"s_add_u32 s61, s48, 49152\n" \
"s_mov_b32 exec_hi, 0\n" \
"s_nop 0\n" \
"buffer_load_dword v237, v224, %[srdp], s61 offen\n" \
"s_mov_b32 exec_hi, -1\n" \
"s_nop 0\n" \
"LtB03_%=:\n" \
"v_mfma_f32_16x16x32_bf16 v[144:147], a[96:99], a[32:35], v[168:171]\n" \
"v_exp_f32_e64 v140, v140\n" \
"v_mfma_f32_16x16x32_bf16 v[144:147], a[100:103], a[36:39], v[144:147]\n" \
"ds_read_b128 a[80:83], %[kb0] offset:36864\n" \
"v_mfma_f32_16x16x32_bf16 v[144:147], a[104:107], a[40:43], v[144:147]\n" \
"v_exp_f32_e64 v141, v141\n" \
"v_mfma_f32_16x16x32_bf16 v[144:147], a[108:111], a[44:47], v[144:147]\n" \
"v_cvt_pk_bf16_f32 v196, v136, v137\n" \
"v_cvt_pk_bf16_f32 v197, v138, v139\n" \
"v_mfma_f32_16x16x32_bf16 v[148:151], a[124:127], a[44:47], v[168:171]\n" \
"v_exp_f32_e64 v142, v142\n" \
"v_mfma_f32_16x16x32_bf16 v[148:151], a[120:123], a[40:43], v[148:151]\n" \
"v_mfma_f32_16x16x32_bf16 v[148:151], a[116:119], a[36:39], v[148:151]\n" \
"buffer_load_dwordx4 %[vd0], %[srdv], s48 offen offset:2048 lds\n" \
"v_mfma_f32_16x16x32_bf16 v[148:151], a[112:115], a[32:35], v[148:151]\n" \
"v_exp_f32_e64 v143, v143\n" \
"v_mfma_f32_16x16x32_bf16 v[96:99], a[160:163], v[204:207], v[96:99]\n" \
"ds_read_b64_tr_b16 a[160:161], %[vvb] offset:24576\n" \
"v_mfma_f32_16x16x32_bf16 v[96:99], a[128:131], v[220:223], v[96:99]\n" \
"v_exp_f32_e64 v144, v144\n" \
"v_mfma_f32_16x16x32_bf16 v[100:103], a[164:167], v[204:207], v[100:103]\n" \
"v_cvt_pk_bf16_f32 v198, v140, v141\n" \
"v_cvt_pk_bf16_f32 v199, v142, v143\n" \
"v_mfma_f32_16x16x32_bf16 v[100:103], a[132:135], v[220:223], v[100:103]\n" \
"v_exp_f32_e64 v145, v145\n" \
"v_mfma_f32_16x16x32_bf16 v[104:107], a[168:171], v[204:207], v[104:107]\n" \
"ds_read_b64_tr_b16 a[162:163], %[vvb] offset:28672\n" \
"ds_read_b64_tr_b16 a[164:165], v211 offset:24576\n" \
"v_mfma_f32_16x16x32_bf16 v[104:107], a[136:139], v[220:223], v[104:107]\n" \
"v_exp_f32_e64 v146, v146\n" \
"v_mfma_f32_16x16x32_bf16 v[108:111], a[172:175], v[204:207], v[108:111]\n" \
"ds_read_b64_tr_b16 a[166:167], v211 offset:28672\n" \
"v_mfma_f32_16x16x32_bf16 v[108:111], a[140:143], v[220:223], v[108:111]\n" \
"v_exp_f32_e64 v147, v147\n" \
"v_smfmac_f32_16x16x64_bf16 v[180:183], a[192:195], v[216:223], v228\n" \
"ds_read_b64_tr_b16 a[168:169], %[vvb] offset:25088\n" \
"s_cbranch_vccz Lpp4_%=\n" \
"buffer_store_dwordx4 a[196:199], v235, %[srdz], s62 offen offset:0 sc1\n" \
"buffer_store_dwordx4 a[196:199], v226, %[srdz], s62 offen offset:0 sc1\n" \
"Lpp4_%=:\n" \
"v_mfma_f32_16x16x32_bf16 v[152:155], a[96:99], a[48:51], v[172:175]\n" \
"v_exp_f32_e32 v148, v148\n" \
"v_mfma_f32_16x16x32_bf16 v[152:155], a[100:103], a[52:55], v[152:155]\n" \
"ds_read_b64_tr_b16 a[170:171], %[vvb] offset:29184\n" \
"v_mfma_f32_16x16x32_bf16 v[152:155], a[104:107], a[56:59], v[152:155]\n" \
"v_exp_f32_e64 v149, v149\n" \
"v_mfma_f32_16x16x32_bf16 v[152:155], a[108:111], a[60:63], v[152:155]\n" \
"v_cvt_pk_bf16_f32 v200, v144, v145\n" \
"v_cvt_pk_bf16_f32 v201, v146, v147\n" \
"v_mfma_f32_16x16x32_bf16 v[156:159], a[124:127], a[60:63], v[172:175]\n" \
"v_exp_f32_e64 v150, v150\n" \
"v_mfma_f32_16x16x32_bf16 v[156:159], a[120:123], a[56:59], v[156:159]\n" \
"ds_read_b64_tr_b16 a[172:173], v211 offset:25088\n" \
"v_mfma_f32_16x16x32_bf16 v[156:159], a[116:119], a[52:55], v[156:159]\n" \
"buffer_load_dwordx4 v215, %[srdv], s48 offen offset:3072 lds\n" \
"v_mfma_f32_16x16x32_bf16 v[156:159], a[112:115], a[48:51], v[156:159]\n" \
"v_exp_f32_e64 v151, v151\n" \
"v_mfma_f32_16x16x32_bf16 v[112:115], a[176:179], v[204:207], v[112:115]\n" \
"ds_read_b64_tr_b16 a[174:175], v211 offset:29184\n" \
"ds_read_b64_tr_b16 a[176:177], %[vvb] offset:25600\n" \
"v_mfma_f32_16x16x32_bf16 v[112:115], a[144:147], v[220:223], v[112:115]\n" \
"v_exp_f32_e64 v152, v152\n" \
"v_mfma_f32_16x16x32_bf16 v[116:119], a[180:183], v[204:207], v[116:119]\n" \
"v_cvt_pk_bf16_f32 v202, v148, v149\n" \
"v_cvt_pk_bf16_f32 v203, v150, v151\n" \
"v_mfma_f32_16x16x32_bf16 v[116:119], a[148:151], v[220:223], v[116:119]\n" \
"v_exp_f32_e64 v153, v153\n" \
"v_mfma_f32_16x16x32_bf16 v[120:123], a[184:187], v[204:207], v[120:123]\n" \
"v_exp_f32_e64 v154, v154\n" \
"v_mfma_f32_16x16x32_bf16 v[120:123], a[152:155], v[220:223], v[120:123]\n" \
"ds_read_b64_tr_b16 a[178:179], %[vvb] offset:29696\n" \
"v_mfma_f32_16x16x32_bf16 v[124:127], a[188:191], v[204:207], v[124:127]\n" \
"v_exp_f32_e64 v155, v155\n" \
"v_mfma_f32_16x16x32_bf16 v[124:127], a[156:159], v[220:223], v[124:127]\n" \
"ds_read_b64_tr_b16 a[180:181], v211 offset:25600\n" \
"s_waitcnt lgkmcnt(13)\n" \
"v_mfma_f32_16x16x32_bf16 v[128:131], a[64:67], a[0:3], v[160:163]\n" \
"v_exp_f32_e32 v156, v156\n" \
"v_mfma_f32_16x16x32_bf16 v[128:131], a[68:71], a[4:7], v[128:131]\n" \
"ds_read_b64_tr_b16 a[182:183], v211 offset:29696\n" \
"s_add_u32 m0, %[m0b], 16384\n" \
"v_mfma_f32_16x16x32_bf16 v[128:131], a[72:75], a[8:11], v[128:131]\n" \
"v_exp_f32_e64 v157, v157\n" \
"v_mfma_f32_16x16x32_bf16 v[128:131], a[76:79], a[12:15], v[128:131]\n" \
"v_cvt_pk_bf16_f32 v204, v152, v153\n" \
"v_cvt_pk_bf16_f32 v205, v154, v155\n" \
"v_mfma_f32_16x16x32_bf16 v[132:135], a[92:95], a[12:15], v[160:163]\n" \
"v_exp_f32_e64 v158, v158\n" \
"v_mfma_f32_16x16x32_bf16 v[132:135], a[88:91], a[8:11], v[132:135]\n" \
"ds_read_b64_tr_b16 a[184:185], %[vvb] offset:26112\n" \
"s_waitcnt lgkmcnt(5)\n" \
"v_mfma_f32_16x16x32_bf16 v[132:135], a[84:87], a[4:7], v[132:135]\n" \
"buffer_load_dwordx4 %[kd0], %[srdk], s48 offen offset:0 lds\n" \
"v_mfma_f32_16x16x32_bf16 v[132:135], a[80:83], a[0:3], v[132:135]\n" \
"v_exp_f32_e32 v159, v159\n" \
"v_mfma_f32_16x16x32_bf16 v[64:67], a[128:131], v[216:219], v[64:67]\n" \
"ds_read_b64_tr_b16 a[186:187], %[vvb] offset:30208\n" \
"ds_read_b64_tr_b16 a[188:189], v211 offset:26112\n" \
"v_mfma_f32_16x16x32_bf16 v[64:67], a[160:163], v[200:203], v[64:67]\n" \
"v_exp_f32_e64 v128, v128\n" \
"v_mfma_f32_16x16x32_bf16 v[68:71], a[132:135], v[216:219], v[68:71]\n" \
"v_cvt_pk_bf16_f32 v206, v156, v157\n" \
"v_cvt_pk_bf16_f32 v207, v158, v159\n" \
"v_mfma_f32_16x16x32_bf16 v[68:71], a[164:167], v[200:203], v[68:71]\n" \
"v_exp_f32_e64 v129, v129\n" \
"v_mfma_f32_16x16x32_bf16 v[72:75], a[136:139], v[216:219], v[72:75]\n" \
"ds_read_b64_tr_b16 a[190:191], v211 offset:30208\n" \
"ds_read_b64_tr_b16 a[128:129], %[vvb] offset:32768\n" \
"v_mfma_f32_16x16x32_bf16 v[72:75], a[168:171], v[200:203], v[72:75]\n" \
"v_exp_f32_e64 v130, v130\n" \
"v_mfma_f32_16x16x32_bf16 v[76:79], a[140:143], v[216:219], v[76:79]\n" \
".if . - adaln_body != 24576\n" \
".error \"size model\"\n" \
".endif\n" \
"Lk5_%=:\n" \
"s_cbranch_execz adaln_kr\n" \
"s_nop 0\n" \
"ds_read_b64_tr_b16 a[130:131], %[vvb] offset:36864\n" \
"ds_read_b64_tr_b16 a[132:133], v211 offset:32768\n" \
"v_mfma_f32_16x16x32_bf16 v[76:79], a[172:175], v[200:203], v[76:79]\n" \
"v_exp_f32_e64 v131, v131\n" \
"v_smfmac_f32_16x16x64_bf16 v[176:179], a[192:195], v[192:199], v228\n" \
"ds_read_b64_tr_b16 a[134:135], v211 offset:36864\n" \
"ds_read_b64_tr_b16 a[136:137], %[vvb] offset:33280\n" \
"v_mfma_f32_16x16x32_bf16 v[136:139], a[64:67], a[16:19], v[164:167]\n" \
"v_exp_f32_e64 v132, v132\n" \
"v_mfma_f32_16x16x32_bf16 v[136:139], a[68:71], a[20:23], v[136:139]\n" \
"ds_read_b64_tr_b16 a[138:139], %[vvb] offset:37376\n" \
"ds_read_b64_tr_b16 a[140:141], v211 offset:33280\n" \
"v_mfma_f32_16x16x32_bf16 v[136:139], a[72:75], a[24:27], v[136:139]\n" \
"v_exp_f32_e64 v133, v133\n" \
"v_mfma_f32_16x16x32_bf16 v[136:139], a[76:79], a[28:31], v[136:139]\n" \
"v_cvt_pk_bf16_f32 v184, v128, v129\n" \
"v_cvt_pk_bf16_f32 v185, v130, v131\n" \
"v_mfma_f32_16x16x32_bf16 v[140:143], a[92:95], a[28:31], v[164:167]\n" \
"v_exp_f32_e64 v134, v134\n" \
"v_mfma_f32_16x16x32_bf16 v[140:143], a[88:91], a[24:27], v[140:143]\n" \
"ds_read_b64_tr_b16 a[142:143], v211 offset:37376\n" \
"s_waitcnt lgkmcnt(8)\n" \
"v_mfma_f32_16x16x32_bf16 v[140:143], a[84:87], a[20:23], v[140:143]\n" \
"buffer_load_dwordx4 v212, %[srdk], s48 offen offset:1024 lds\n" \
"v_mfma_f32_16x16x32_bf16 v[140:143], a[80:83], a[16:19], v[140:143]\n" \
"v_exp_f32_e32 v135, v135\n" \
"v_mfma_f32_16x16x32_bf16 v[80:83], a[144:147], v[216:219], v[80:83]\n" \
"ds_read_b64_tr_b16 a[144:145], %[vvb] offset:33792\n" \
"ds_read_b64_tr_b16 a[146:147], %[vvb] offset:37888\n" \
"v_mfma_f32_16x16x32_bf16 v[80:83], a[176:179], v[200:203], v[80:83]\n" \
"v_exp_f32_e64 v136, v136\n" \
"v_mfma_f32_16x16x32_bf16 v[84:87], a[148:151], v[216:219], v[84:87]\n" \
"v_cvt_pk_bf16_f32 v186, v132, v133\n" \
"v_cvt_pk_bf16_f32 v187, v134, v135\n" \
"v_mfma_f32_16x16x32_bf16 v[84:87], a[180:183], v[200:203], v[84:87]\n" \
"v_exp_f32_e64 v137, v137\n" \
"v_mfma_f32_16x16x32_bf16 v[88:91], a[152:155], v[216:219], v[88:91]\n" \
"v_exp_f32_e64 v138, v138\n" \
"v_mfma_f32_16x16x32_bf16 v[88:91], a[184:187], v[200:203], v[88:91]\n" \
"ds_read_b64_tr_b16 a[148:149], v211 offset:33792\n" \
"ds_read_b64_tr_b16 a[150:151], v211 offset:37888\n" \
"v_mfma_f32_16x16x32_bf16 v[92:95], a[156:159], v[216:219], v[92:95]\n" \
"v_exp_f32_e64 v139, v139\n" \
"v_mfma_f32_16x16x32_bf16 v[92:95], a[188:191], v[200:203], v[92:95]\n" \
"ds_read_b64_tr_b16 a[152:153], %[vvb] offset:34304\n" \
"s_cbranch_vccz Lpp5_%=\n" \
"buffer_store_dwordx4 a[196:199], v235, %[srdz], s63 offen offset:1024 sc1\n" \
"buffer_store_dwordx4 a[196:199], v226, %[srdz], s63 offen offset:1024 sc1\n" \
"Lpp5_%=:\n" \
"v_mfma_f32_16x16x32_bf16 v[144:147], a[64:67], a[32:35], v[168:171]\n" \
"v_exp_f32_e32 v140, v140\n" \
"v_mfma_f32_16x16x32_bf16 v[144:147], a[68:71], a[36:39], v[144:147]\n" \
"ds_read_b64_tr_b16 a[154:155], %[vvb] offset:38400\n" \
"ds_read_b64_tr_b16 a[156:157], v211 offset:34304\n" \
"v_mfma_f32_16x16x32_bf16 v[144:147], a[72:75], a[40:43], v[144:147]\n" \
"v_exp_f32_e64 v141, v141\n" \
"v_mfma_f32_16x16x32_bf16 v[144:147], a[76:79], a[44:47], v[144:147]\n" \
"v_cvt_pk_bf16_f32 v188, v136, v137\n" \
"v_cvt_pk_bf16_f32 v189, v138, v139\n" \
"v_mfma_f32_16x16x32_bf16 v[148:151], a[92:95], a[44:47], v[168:171]\n" \
"v_exp_f32_e64 v142, v142\n" \
"v_mfma_f32_16x16x32_bf16 v[148:151], a[88:91], a[40:43], v[148:151]\n" \
"ds_read_b64_tr_b16 a[158:159], v211 offset:38400\n" \
"s_waitcnt lgkmcnt(8)\n" \
"v_mfma_f32_16x16x32_bf16 v[148:151], a[84:87], a[36:39], v[148:151]\n" \
"buffer_load_dwordx4 v213, %[srdk], s48 offen offset:2048 lds\n" \
"v_mfma_f32_16x16x32_bf16 v[148:151], a[80:83], a[32:35], v[148:151]\n" \
"v_exp_f32_e32 v143, v143\n" \
"v_mfma_f32_16x16x32_bf16 v[0:3], a[160:163], v[192:195], v[0:3]\n" \
"ds_read_b128 a[96:99], %[kb0] offset:40960\n" \
"v_mfma_f32_16x16x32_bf16 v[0:3], a[128:131], v[184:187], v[0:3]\n" \
"v_exp_f32_e64 v144, v144\n" \
"v_mfma_f32_16x16x32_bf16 v[4:7], a[164:167], v[192:195], v[4:7]\n" \
"v_cvt_pk_bf16_f32 v190, v140, v141\n" \
"v_cvt_pk_bf16_f32 v191, v142, v143\n" \
"v_mfma_f32_16x16x32_bf16 v[4:7], a[132:135], v[184:187], v[4:7]\n" \
"v_exp_f32_e64 v145, v145\n" \
"v_mfma_f32_16x16x32_bf16 v[8:11], a[168:171], v[192:195], v[8:11]\n" \
"ds_read_b128 a[100:103], v208 offset:40960\n" \
"v_mfma_f32_16x16x32_bf16 v[8:11], a[136:139], v[184:187], v[8:11]\n" \
"v_exp_f32_e64 v146, v146\n" \
"v_mfma_f32_16x16x32_bf16 v[12:15], a[172:175], v[192:195], v[12:15]\n" \
"ds_read_b128 a[104:107], v209 offset:40960\n" \
"v_mfma_f32_16x16x32_bf16 v[12:15], a[140:143], v[184:187], v[12:15]\n" \
"v_exp_f32_e64 v147, v147\n" \
"v_smfmac_f32_16x16x64_bf16 v[180:183], a[192:195], v[200:207], v228\n" \
"ds_read_b128 a[108:111], v210 offset:40960\n" \
"v_mfma_f32_16x16x32_bf16 v[152:155], a[64:67], a[48:51], v[172:175]\n" \
"v_exp_f32_e64 v148, v148\n" \
"v_mfma_f32_16x16x32_bf16 v[152:155], a[68:71], a[52:55], v[152:155]\n" \
"ds_read_b128 a[124:127], v210 offset:45056\n" \
"v_mfma_f32_16x16x32_bf16 v[152:155], a[72:75], a[56:59], v[152:155]\n" \
"v_exp_f32_e64 v149, v149\n" \
"v_mfma_f32_16x16x32_bf16 v[152:155], a[76:79], a[60:63], v[152:155]\n" \
"v_cvt_pk_bf16_f32 v216, v144, v145\n" \
"v_cvt_pk_bf16_f32 v217, v146, v147\n" \
"v_mfma_f32_16x16x32_bf16 v[156:159], a[92:95], a[60:63], v[172:175]\n" \
"v_exp_f32_e64 v150, v150\n" \
"v_mfma_f32_16x16x32_bf16 v[156:159], a[88:91], a[56:59], v[156:159]\n" \
"ds_read_b128 a[120:123], v209 offset:45056\n" \
"s_waitcnt lgkmcnt(6)\n" \
"v_mfma_f32_16x16x32_bf16 v[156:159], a[84:87], a[52:55], v[156:159]\n" \
"buffer_load_dwordx4 v214, %[srdk], s48 offen offset:3072 lds\n" \
"v_mfma_f32_16x16x32_bf16 v[156:159], a[80:83], a[48:51], v[156:159]\n" \
"v_exp_f32_e32 v151, v151\n" \
"v_mfma_f32_16x16x32_bf16 v[16:19], a[176:179], v[192:195], v[16:19]\n" \
"ds_read_b128 a[116:119], v208 offset:45056\n" \
"v_mfma_f32_16x16x32_bf16 v[16:19], a[144:147], v[184:187], v[16:19]\n" \
"v_exp_f32_e64 v152, v152\n" \
"v_mfma_f32_16x16x32_bf16 v[20:23], a[180:183], v[192:195], v[20:23]\n" \
"v_cvt_pk_bf16_f32 v218, v148, v149\n" \
"v_cvt_pk_bf16_f32 v219, v150, v151\n" \
"v_mfma_f32_16x16x32_bf16 v[20:23], a[148:151], v[184:187], v[20:23]\n" \
"v_exp_f32_e64 v153, v153\n" \
"v_mfma_f32_16x16x32_bf16 v[24:27], a[184:187], v[192:195], v[24:27]\n" \
"v_exp_f32_e64 v154, v154\n" \
"v_mfma_f32_16x16x32_bf16 v[24:27], a[152:155], v[184:187], v[24:27]\n" \
"ds_read_b128 a[112:115], %[kb0] offset:45056\n" \
"s_add_u32 s48, s48, 0x4000\n" \
"v_mfma_f32_16x16x32_bf16 v[28:31], a[188:191], v[192:195], v[28:31]\n" \
"v_exp_f32_e64 v155, v155\n" \
"v_mfma_f32_16x16x32_bf16 v[28:31], a[156:159], v[184:187], v[28:31]\n" \
"s_waitcnt vmcnt(13) lgkmcnt(2)\n" \
"s_barrier\n" \
"v_mfma_f32_16x16x32_bf16 v[128:131], a[96:99], a[0:3], v[160:163]\n" \
"v_exp_f32_e64 v156, v156\n" \
"v_mfma_f32_16x16x32_bf16 v[128:131], a[100:103], a[4:7], v[128:131]\n" \
"s_add_u32 m0, %[m0b], 81920\n" \
"v_mfma_f32_16x16x32_bf16 v[128:131], a[104:107], a[8:11], v[128:131]\n" \
"v_exp_f32_e64 v157, v157\n" \
"v_mfma_f32_16x16x32_bf16 v[128:131], a[108:111], a[12:15], v[128:131]\n" \
"v_cvt_pk_bf16_f32 v220, v152, v153\n" \
"v_cvt_pk_bf16_f32 v221, v154, v155\n" \
"v_mfma_f32_16x16x32_bf16 v[132:135], a[124:127], a[12:15], v[160:163]\n" \
"v_exp_f32_e64 v158, v158\n" \
"v_mfma_f32_16x16x32_bf16 v[132:135], a[120:123], a[8:11], v[132:135]\n" \
"s_waitcnt lgkmcnt(0)\n" \
"v_mfma_f32_16x16x32_bf16 v[132:135], a[116:119], a[4:7], v[132:135]\n" \
"buffer_load_dwordx4 %[vd0], %[srdv], s48 offen offset:0 lds\n" \
"v_mfma_f32_16x16x32_bf16 v[132:135], a[112:115], a[0:3], v[132:135]\n" \
"v_exp_f32_e32 v159, v159\n" \
"v_mfma_f32_16x16x32_bf16 v[32:35], a[160:163], v[196:199], v[32:35]\n" \
"ds_read_b128 a[64:67], %[kb0] offset:49152\n" \
"v_mfma_f32_16x16x32_bf16 v[32:35], a[128:131], v[188:191], v[32:35]\n" \
"v_exp_f32_e64 v128, v128\n" \
"v_mfma_f32_16x16x32_bf16 v[36:39], a[164:167], v[196:199], v[36:39]\n" \
"v_cvt_pk_bf16_f32 v222, v156, v157\n" \
"v_cvt_pk_bf16_f32 v223, v158, v159\n" \
"v_mfma_f32_16x16x32_bf16 v[36:39], a[132:135], v[188:191], v[36:39]\n" \
"v_exp_f32_e64 v129, v129\n" \
"v_mfma_f32_16x16x32_bf16 v[40:43], a[168:171], v[196:199], v[40:43]\n" \
"ds_read_b128 a[68:71], v208 offset:49152\n" \
"v_mfma_f32_16x16x32_bf16 v[40:43], a[136:139], v[188:191], v[40:43]\n" \
"v_exp_f32_e64 v130, v130\n" \
"v_mfma_f32_16x16x32_bf16 v[44:47], a[172:175], v[196:199], v[44:47]\n" \
"ds_read_b128 a[72:75], v209 offset:49152\n" \
"v_mfma_f32_16x16x32_bf16 v[44:47], a[140:143], v[188:191], v[44:47]\n" \
"v_exp_f32_e64 v131, v131\n" \
"v_smfmac_f32_16x16x64_bf16 v[176:179], a[192:195], v[184:191], v228\n" \
"v_mfma_f32_16x16x32_bf16 v[136:139], a[96:99], a[16:19], v[164:167]\n" \
"v_exp_f32_e64 v132, v132\n" \
"v_mfma_f32_16x16x32_bf16 v[136:139], a[100:103], a[20:23], v[136:139]\n" \
"ds_read_b128 a[76:79], v210 offset:49152\n" \
"v_mfma_f32_16x16x32_bf16 v[136:139], a[104:107], a[24:27], v[136:139]\n" \
"v_exp_f32_e64 v133, v133\n" \
"v_mfma_f32_16x16x32_bf16 v[136:139], a[108:111], a[28:31], v[136:139]\n" \
"v_cvt_pk_bf16_f32 v192, v128, v129\n" \
"v_cvt_pk_bf16_f32 v193, v130, v131\n" \
"v_mfma_f32_16x16x32_bf16 v[140:143], a[124:127], a[28:31], v[164:167]\n" \
"v_exp_f32_e64 v134, v134\n" \
"v_mfma_f32_16x16x32_bf16 v[140:143], a[120:123], a[24:27], v[140:143]\n" \
"ds_read_b128 a[92:95], v210 offset:53248\n" \
"v_mfma_f32_16x16x32_bf16 v[140:143], a[116:119], a[20:23], v[140:143]\n" \
"buffer_load_dwordx4 v215, %[srdv], s48 offen offset:1024 lds\n" \
"v_mfma_f32_16x16x32_bf16 v[140:143], a[112:115], a[16:19], v[140:143]\n" \
"v_exp_f32_e64 v135, v135\n" \
"v_mfma_f32_16x16x32_bf16 v[48:51], a[176:179], v[196:199], v[48:51]\n" \
"ds_read_b128 a[88:91], v209 offset:53248\n" \
"v_mfma_f32_16x16x32_bf16 v[48:51], a[144:147], v[188:191], v[48:51]\n" \
"v_exp_f32_e64 v136, v136\n" \
"v_mfma_f32_16x16x32_bf16 v[52:55], a[180:183], v[196:199], v[52:55]\n" \
"v_cvt_pk_bf16_f32 v194, v132, v133\n" \
"v_cvt_pk_bf16_f32 v195, v134, v135\n" \
"v_mfma_f32_16x16x32_bf16 v[52:55], a[148:151], v[188:191], v[52:55]\n" \
"v_exp_f32_e64 v137, v137\n" \
"v_mfma_f32_16x16x32_bf16 v[56:59], a[184:187], v[196:199], v[56:59]\n" \
"v_exp_f32_e64 v138, v138\n" \
"v_mfma_f32_16x16x32_bf16 v[56:59], a[152:155], v[188:191], v[56:59]\n" \
"ds_read_b128 a[84:87], v208 offset:53248\n" \
"v_mfma_f32_16x16x32_bf16 v[60:63], a[188:191], v[196:199], v[60:63]\n" \
"v_exp_f32_e64 v139, v139\n" \
"v_mfma_f32_16x16x32_bf16 v[60:63], a[156:159], v[188:191], v[60:63]\n" \
"s_cmp_lg_u32 %[wid], 1\n" \
"s_cbranch_scc1 LtB05_%=\n" \
"global_load_dword v237, v236, s[58:59]\n" \
"s_add_u32 s61, s48, 49152\n" \
"s_mov_b32 exec_hi, 0\n" \
"s_nop 0\n" \
"buffer_load_dword v237, v224, %[srdp], s61 offen\n" \
"s_mov_b32 exec_hi, -1\n" \
"s_nop 0\n" \
"LtB05_%=:\n" \
"v_mfma_f32_16x16x32_bf16 v[144:147], a[96:99], a[32:35], v[168:171]\n" \
"v_exp_f32_e64 v140, v140\n" \
"v_mfma_f32_16x16x32_bf16 v[144:147], a[100:103], a[36:39], v[144:147]\n" \
"ds_read_b128 a[80:83], %[kb0] offset:53248\n" \
"v_mfma_f32_16x16x32_bf16 v[144:147], a[104:107], a[40:43], v[144:147]\n" \
"v_exp_f32_e64 v141, v141\n" \
"v_mfma_f32_16x16x32_bf16 v[144:147], a[108:111], a[44:47], v[144:147]\n" \
"v_cvt_pk_bf16_f32 v196, v136, v137\n" \
"v_cvt_pk_bf16_f32 v197, v138, v139\n" \
"v_mfma_f32_16x16x32_bf16 v[148:151], a[124:127], a[44:47], v[168:171]\n" \
"v_exp_f32_e64 v142, v142\n" \
"v_mfma_f32_16x16x32_bf16 v[148:151], a[120:123], a[40:43], v[148:151]\n" \
"v_mfma_f32_16x16x32_bf16 v[148:151], a[116:119], a[36:39], v[148:151]\n" \
"buffer_load_dwordx4 %[vd0], %[srdv], s48 offen offset:2048 lds\n" \
"v_mfma_f32_16x16x32_bf16 v[148:151], a[112:115], a[32:35], v[148:151]\n" \
"v_exp_f32_e64 v143, v143\n" \
"v_mfma_f32_16x16x32_bf16 v[96:99], a[160:163], v[204:207], v[96:99]\n" \
"ds_read_b64_tr_b16 a[160:161], %[vvb] offset:40960\n" \
"v_mfma_f32_16x16x32_bf16 v[96:99], a[128:131], v[220:223], v[96:99]\n" \
"v_exp_f32_e64 v144, v144\n" \
"v_mfma_f32_16x16x32_bf16 v[100:103], a[164:167], v[204:207], v[100:103]\n" \
"v_cvt_pk_bf16_f32 v198, v140, v141\n" \
"v_cvt_pk_bf16_f32 v199, v142, v143\n" \
"v_mfma_f32_16x16x32_bf16 v[100:103], a[132:135], v[220:223], v[100:103]\n" \
"v_exp_f32_e64 v145, v145\n" \
"v_mfma_f32_16x16x32_bf16 v[104:107], a[168:171], v[204:207], v[104:107]\n" \
"ds_read_b64_tr_b16 a[162:163], %[vvb] offset:45056\n" \
"ds_read_b64_tr_b16 a[164:165], v211 offset:40960\n" \
"v_mfma_f32_16x16x32_bf16 v[104:107], a[136:139], v[220:223], v[104:107]\n" \
"v_exp_f32_e64 v146, v146\n" \
"v_mfma_f32_16x16x32_bf16 v[108:111], a[172:175], v[204:207], v[108:111]\n" \
"ds_read_b64_tr_b16 a[166:167], v211 offset:45056\n" \
"v_mfma_f32_16x16x32_bf16 v[108:111], a[140:143], v[220:223], v[108:111]\n" \
"v_exp_f32_e64 v147, v147\n" \
"v_smfmac_f32_16x16x64_bf16 v[180:183], a[192:195], v[216:223], v228\n" \
"ds_read_b64_tr_b16 a[168:169], %[vvb] offset:41472\n" \
"v_mfma_f32_16x16x32_bf16 v[152:155], a[96:99], a[48:51], v[172:175]\n" \
"v_exp_f32_e64 v148, v148\n" \
"v_mfma_f32_16x16x32_bf16 v[152:155], a[100:103], a[52:55], v[152:155]\n" \
"ds_read_b64_tr_b16 a[170:171], %[vvb] offset:45568\n" \
"v_mfma_f32_16x16x32_bf16 v[152:155], a[104:107], a[56:59], v[152:155]\n" \
"v_exp_f32_e64 v149, v149\n" \
"v_mfma_f32_16x16x32_bf16 v[152:155], a[108:111], a[60:63], v[152:155]\n" \
"v_cvt_pk_bf16_f32 v200, v144, v145\n" \
"v_cvt_pk_bf16_f32 v201, v146, v147\n" \
"v_mfma_f32_16x16x32_bf16 v[156:159], a[124:127], a[60:63], v[172:175]\n" \
"v_exp_f32_e64 v150, v150\n" \
"v_mfma_f32_16x16x32_bf16 v[156:159], a[120:123], a[56:59], v[156:159]\n" \
"ds_read_b64_tr_b16 a[172:173], v211 offset:41472\n" \
"v_mfma_f32_16x16x32_bf16 v[156:159], a[116:119], a[52:55], v[156:159]\n" \
"buffer_load_dwordx4 v215, %[srdv], s48 offen offset:3072 lds\n" \
"v_mfma_f32_16x16x32_bf16 v[156:159], a[112:115], a[48:51], v[156:159]\n" \
"v_exp_f32_e64 v151, v151\n" \
"v_mfma_f32_16x16x32_bf16 v[112:115], a[176:179], v[204:207], v[112:115]\n" \
"ds_read_b64_tr_b16 a[174:175], v211 offset:45568\n" \
"ds_read_b64_tr_b16 a[176:177], %[vvb] offset:41984\n" \
"v_mfma_f32_16x16x32_bf16 v[112:115], a[144:147], v[220:223], v[112:115]\n" \
"v_exp_f32_e64 v152, v152\n" \
"v_mfma_f32_16x16x32_bf16 v[116:119], a[180:183], v[204:207], v[116:119]\n" \
"v_cvt_pk_bf16_f32 v202, v148, v149\n" \
"v_cvt_pk_bf16_f32 v203, v150, v151\n" \
"v_mfma_f32_16x16x32_bf16 v[116:119], a[148:151], v[220:223], v[116:119]\n" \
"v_exp_f32_e64 v153, v153\n" \
"v_mfma_f32_16x16x32_bf16 v[120:123], a[184:187], v[204:207], v[120:123]\n" \
"v_exp_f32_e64 v154, v154\n" \
"v_mfma_f32_16x16x32_bf16 v[120:123], a[152:155], v[220:223], v[120:123]\n" \
"ds_read_b64_tr_b16 a[178:179], %[vvb] offset:46080\n" \
"v_mfma_f32_16x16x32_bf16 v[124:127], a[188:191], v[204:207], v[124:127]\n" \
"v_exp_f32_e64 v155, v155\n" \
"v_mfma_f32_16x16x32_bf16 v[124:127], a[156:159], v[220:223], v[124:127]\n" \
"ds_read_b64_tr_b16 a[180:181], v211 offset:41984\n" \
"s_waitcnt lgkmcnt(13)\n" \
"v_mfma_f32_16x16x32_bf16 v[128:131], a[64:67], a[0:3], v[160:163]\n" \
"v_exp_f32_e32 v156, v156\n" \
"v_mfma_f32_16x16x32_bf16 v[128:131], a[68:71], a[4:7], v[128:131]\n" \
"ds_read_b64_tr_b16 a[182:183], v211 offset:46080\n" \
"s_add_u32 m0, %[m0b], 32768\n" \
"v_mfma_f32_16x16x32_bf16 v[128:131], a[72:75], a[8:11], v[128:131]\n" \
"v_exp_f32_e64 v157, v157\n" \
"v_mfma_f32_16x16x32_bf16 v[128:131], a[76:79], a[12:15], v[128:131]\n" \
"v_cvt_pk_bf16_f32 v204, v152, v153\n" \
"v_cvt_pk_bf16_f32 v205, v154, v155\n" \
"v_mfma_f32_16x16x32_bf16 v[132:135], a[92:95], a[12:15], v[160:163]\n" \
"v_exp_f32_e64 v158, v158\n" \
"v_mfma_f32_16x16x32_bf16 v[132:135], a[88:91], a[8:11], v[132:135]\n" \
"ds_read_b64_tr_b16 a[184:185], %[vvb] offset:42496\n" \
"s_waitcnt lgkmcnt(5)\n" \
"v_mfma_f32_16x16x32_bf16 v[132:135], a[84:87], a[4:7], v[132:135]\n" \
"buffer_load_dwordx4 %[kd0], %[srdk], s48 offen offset:0 lds\n" \
"v_mfma_f32_16x16x32_bf16 v[132:135], a[80:83], a[0:3], v[132:135]\n" \
"v_exp_f32_e32 v159, v159\n" \
"v_mfma_f32_16x16x32_bf16 v[64:67], a[128:131], v[216:219], v[64:67]\n" \
"ds_read_b64_tr_b16 a[186:187], %[vvb] offset:46592\n" \
"ds_read_b64_tr_b16 a[188:189], v211 offset:42496\n" \
"v_mfma_f32_16x16x32_bf16 v[64:67], a[160:163], v[200:203], v[64:67]\n" \
"v_exp_f32_e64 v128, v128\n" \
"v_mfma_f32_16x16x32_bf16 v[68:71], a[132:135], v[216:219], v[68:71]\n" \
"v_cvt_pk_bf16_f32 v206, v156, v157\n" \
"v_cvt_pk_bf16_f32 v207, v158, v159\n" \
"v_mfma_f32_16x16x32_bf16 v[68:71], a[164:167], v[200:203], v[68:71]\n" \
"v_exp_f32_e64 v129, v129\n" \
"v_mfma_f32_16x16x32_bf16 v[72:75], a[136:139], v[216:219], v[72:75]\n" \
"ds_read_b64_tr_b16 a[190:191], v211 offset:46592\n" \
"ds_read_b64_tr_b16 a[128:129], %[vvb] offset:49152\n" \
"v_mfma_f32_16x16x32_bf16 v[72:75], a[168:171], v[200:203], v[72:75]\n" \
"v_exp_f32_e64 v130, v130\n" \
"v_mfma_f32_16x16x32_bf16 v[76:79], a[140:143], v[216:219], v[76:79]\n" \
"ds_read_b64_tr_b16 a[130:131], %[vvb] offset:53248\n" \
"ds_read_b64_tr_b16 a[132:133], v211 offset:49152\n" \
"v_mfma_f32_16x16x32_bf16 v[76:79], a[172:175], v[200:203], v[76:79]\n" \
"v_exp_f32_e64 v131, v131\n" \
"v_smfmac_f32_16x16x64_bf16 v[176:179], a[192:195], v[192:199], v228\n" \
"ds_read_b64_tr_b16 a[134:135], v211 offset:53248\n" \
"ds_read_b64_tr_b16 a[136:137], %[vvb] offset:49664\n" \
"s_cbranch_vccz Lpp6_%=\n" \
"buffer_store_dwordx4 a[196:199], v235, %[srdz], s62 offen offset:1024 sc1\n" \
"buffer_store_dwordx4 a[196:199], v226, %[srdz], s62 offen offset:1024 sc1\n" \
"Lpp6_%=:\n" \
"v_mfma_f32_16x16x32_bf16 v[136:139], a[64:67], a[16:19], v[164:167]\n" \
"v_exp_f32_e32 v132, v132\n" \
"v_mfma_f32_16x16x32_bf16 v[136:139], a[68:71], a[20:23], v[136:139]\n" \
"ds_read_b64_tr_b16 a[138:139], %[vvb] offset:53760\n" \
"ds_read_b64_tr_b16 a[140:141], v211 offset:49664\n" \
"v_mfma_f32_16x16x32_bf16 v[136:139], a[72:75], a[24:27], v[136:139]\n" \
"v_exp_f32_e64 v133, v133\n" \
"v_mfma_f32_16x16x32_bf16 v[136:139], a[76:79], a[28:31], v[136:139]\n" \
"v_cvt_pk_bf16_f32 v184, v128, v129\n" \
"v_cvt_pk_bf16_f32 v185, v130, v131\n" \
"v_mfma_f32_16x16x32_bf16 v[140:143], a[92:95], a[28:31], v[164:167]\n" \
"v_exp_f32_e64 v134, v134\n" \
"v_mfma_f32_16x16x32_bf16 v[140:143], a[88:91], a[24:27], v[140:143]\n" \
"ds_read_b64_tr_b16 a[142:143], v211 offset:53760\n" \
"s_waitcnt lgkmcnt(8)\n" \
"v_mfma_f32_16x16x32_bf16 v[140:143], a[84:87], a[20:23], v[140:143]\n" \
"buffer_load_dwordx4 v212, %[srdk], s48 offen offset:1024 lds\n" \
"v_mfma_f32_16x16x32_bf16 v[140:143], a[80:83], a[16:19], v[140:143]\n" \
"v_exp_f32_e32 v135, v135\n" \
"v_mfma_f32_16x16x32_bf16 v[80:83], a[144:147], v[216:219], v[80:83]\n" \
"ds_read_b64_tr_b16 a[144:145], %[vvb] offset:50176\n" \
"ds_read_b64_tr_b16 a[146:147], %[vvb] offset:54272\n" \
"v_mfma_f32_16x16x32_bf16 v[80:83], a[176:179], v[200:203], v[80:83]\n" \
"v_exp_f32_e64 v136, v136\n" \
"v_mfma_f32_16x16x32_bf16 v[84:87], a[148:151], v[216:219], v[84:87]\n" \
"v_cvt_pk_bf16_f32 v186, v132, v133\n" \
"v_cvt_pk_bf16_f32 v187, v134, v135\n" \
"v_mfma_f32_16x16x32_bf16 v[84:87], a[180:183], v[200:203], v[84:87]\n" \
"v_exp_f32_e64 v137, v137\n" \
"v_mfma_f32_16x16x32_bf16 v[88:91], a[152:155], v[216:219], v[88:91]\n" \
"v_exp_f32_e64 v138, v138\n" \
"v_mfma_f32_16x16x32_bf16 v[88:91], a[184:187], v[200:203], v[88:91]\n" \
"ds_read_b64_tr_b16 a[148:149], v211 offset:50176\n" \
"ds_read_b64_tr_b16 a[150:151], v211 offset:54272\n" \
"v_mfma_f32_16x16x32_bf16 v[92:95], a[156:159], v[216:219], v[92:95]\n" \
"v_exp_f32_e64 v139, v139\n" \
"v_mfma_f32_16x16x32_bf16 v[92:95], a[188:191], v[200:203], v[92:95]\n" \
"ds_read_b64_tr_b16 a[152:153], %[vvb] offset:50688\n" \
"global_load_dword v237, %[vct], %[pct]\n" \
"v_mfma_f32_16x16x32_bf16 v[144:147], a[64:67], a[32:35], v[168:171]\n" \
"v_exp_f32_e64 v140, v140\n" \
"v_mfma_f32_16x16x32_bf16 v[144:147], a[68:71], a[36:39], v[144:147]\n" \
"ds_read_b64_tr_b16 a[154:155], %[vvb] offset:54784\n" \
"ds_read_b64_tr_b16 a[156:157], v211 offset:50688\n" \
"v_mfma_f32_16x16x32_bf16 v[144:147], a[72:75], a[40:43], v[144:147]\n" \
"v_exp_f32_e64 v141, v141\n" \
"v_mfma_f32_16x16x32_bf16 v[144:147], a[76:79], a[44:47], v[144:147]\n" \
"v_cvt_pk_bf16_f32 v188, v136, v137\n" \
"v_cvt_pk_bf16_f32 v189, v138, v139\n" \
"v_mfma_f32_16x16x32_bf16 v[148:151], a[92:95], a[44:47], v[168:171]\n" \
"v_exp_f32_e64 v142, v142\n" \
"v_mfma_f32_16x16x32_bf16 v[148:151], a[88:91], a[40:43], v[148:151]\n" \
"ds_read_b64_tr_b16 a[158:159], v211 offset:54784\n" \
"s_waitcnt lgkmcnt(8)\n" \
"v_mfma_f32_16x16x32_bf16 v[148:151], a[84:87], a[36:39], v[148:151]\n" \
"buffer_load_dwordx4 v213, %[srdk], s48 offen offset:2048 lds\n" \
"v_mfma_f32_16x16x32_bf16 v[148:151], a[80:83], a[32:35], v[148:151]\n" \
"v_exp_f32_e32 v143, v143\n" \
"v_mfma_f32_16x16x32_bf16 v[0:3], a[160:163], v[192:195], v[0:3]\n" \
"ds_read_b128 a[96:99], %[kb0] offset:57344\n" \
"v_mfma_f32_16x16x32_bf16 v[0:3], a[128:131], v[184:187], v[0:3]\n" \
"v_exp_f32_e64 v144, v144\n" \
"v_mfma_f32_16x16x32_bf16 v[4:7], a[164:167], v[192:195], v[4:7]\n" \
"v_cvt_pk_bf16_f32 v190, v140, v141\n" \
"v_cvt_pk_bf16_f32 v191, v142, v143\n" \
"v_mfma_f32_16x16x32_bf16 v[4:7], a[132:135], v[184:187], v[4:7]\n" \
"v_exp_f32_e64 v145, v145\n" \
"v_mfma_f32_16x16x32_bf16 v[8:11], a[168:171], v[192:195], v[8:11]\n" \
"ds_read_b128 a[100:103], v208 offset:57344\n" \
"v_mfma_f32_16x16x32_bf16 v[8:11], a[136:139], v[184:187], v[8:11]\n" \
"v_exp_f32_e64 v146, v146\n" \
"v_mfma_f32_16x16x32_bf16 v[12:15], a[172:175], v[192:195], v[12:15]\n" \
"ds_read_b128 a[104:107], v209 offset:57344\n" \
"v_mfma_f32_16x16x32_bf16 v[12:15], a[140:143], v[184:187], v[12:15]\n" \
"v_exp_f32_e64 v147, v147\n" \
"v_smfmac_f32_16x16x64_bf16 v[180:183], a[192:195], v[200:207], v228\n" \
"ds_read_b128 a[108:111], v210 offset:57344\n" \
"v_mfma_f32_16x16x32_bf16 v[152:155], a[64:67], a[48:51], v[172:175]\n" \
"v_exp_f32_e64 v148, v148\n" \
"v_mfma_f32_16x16x32_bf16 v[152:155], a[68:71], a[52:55], v[152:155]\n" \
"ds_read_b128 a[124:127], v210 offset:61440\n" \
"v_mfma_f32_16x16x32_bf16 v[152:155], a[72:75], a[56:59], v[152:155]\n" \
"v_exp_f32_e64 v149, v149\n" \
"v_mfma_f32_16x16x32_bf16 v[152:155], a[76:79], a[60:63], v[152:155]\n" \
"v_cvt_pk_bf16_f32 v216, v144, v145\n" \
"v_cvt_pk_bf16_f32 v217, v146, v147\n" \
"v_mfma_f32_16x16x32_bf16 v[156:159], a[92:95], a[60:63], v[172:175]\n" \
"v_exp_f32_e64 v150, v150\n" \
"v_mfma_f32_16x16x32_bf16 v[156:159], a[88:91], a[56:59], v[156:159]\n" \
"ds_read_b128 a[120:123], v209 offset:61440\n" \
"s_waitcnt lgkmcnt(6)\n" \
"v_mfma_f32_16x16x32_bf16 v[156:159], a[84:87], a[52:55], v[156:159]\n" \
"buffer_load_dwordx4 v214, %[srdk], s48 offen offset:3072 lds\n" \
"v_mfma_f32_16x16x32_bf16 v[156:159], a[80:83], a[48:51], v[156:159]\n" \
"v_exp_f32_e32 v151, v151\n" \
"v_mfma_f32_16x16x32_bf16 v[16:19], a[176:179], v[192:195], v[16:19]\n" \
"ds_read_b128 a[116:119], v208 offset:61440\n" \
"v_mfma_f32_16x16x32_bf16 v[16:19], a[144:147], v[184:187], v[16:19]\n" \
"v_exp_f32_e64 v152, v152\n" \
"v_mfma_f32_16x16x32_bf16 v[20:23], a[180:183], v[192:195], v[20:23]\n" \
"v_cvt_pk_bf16_f32 v218, v148, v149\n" \
"v_cvt_pk_bf16_f32 v219, v150, v151\n" \
"v_mfma_f32_16x16x32_bf16 v[20:23], a[148:151], v[184:187], v[20:23]\n" \
"v_exp_f32_e64 v153, v153\n" \
"v_mfma_f32_16x16x32_bf16 v[24:27], a[184:187], v[192:195], v[24:27]\n" \
"v_exp_f32_e64 v154, v154\n" \
"v_mfma_f32_16x16x32_bf16 v[24:27], a[152:155], v[184:187], v[24:27]\n" \
"ds_read_b128 a[112:115], %[kb0] offset:61440\n" \
"s_add_u32 s48, s48, 0x4000\n" \
"s_mov_b32 m0, s44\n" \
"s_addk_i32 s44, 0x400\n" \
"buffer_load_dwordx4 v229, s[64:67], s45 offen offset:0 lds\n" \
"s_add_u32 s45, s45, 229376\n" \
"v_mfma_f32_16x16x32_bf16 v[28:31], a[188:191], v[192:195], v[28:31]\n" \
"v_exp_f32_e64 v155, v155\n" \
"v_mfma_f32_16x16x32_bf16 v[28:31], a[156:159], v[184:187], v[28:31]\n" \
"s_waitcnt vmcnt(14) lgkmcnt(2)\n" \
"s_barrier\n" \
"v_mfma_f32_16x16x32_bf16 v[128:131], a[96:99], a[0:3], v[160:163]\n" \
"v_exp_f32_e64 v156, v156\n" \
"v_mfma_f32_16x16x32_bf16 v[128:131], a[100:103], a[4:7], v[128:131]\n" \
"s_add_u32 m0, %[m0b], 98304\n" \
"v_mfma_f32_16x16x32_bf16 v[128:131], a[104:107], a[8:11], v[128:131]\n" \
"v_exp_f32_e64 v157, v157\n" \
"v_mfma_f32_16x16x32_bf16 v[128:131], a[108:111], a[12:15], v[128:131]\n" \
"v_cvt_pk_bf16_f32 v220, v152, v153\n" \
"v_cvt_pk_bf16_f32 v221, v154, v155\n" \
"v_mfma_f32_16x16x32_bf16 v[132:135], a[124:127], a[12:15], v[160:163]\n" \
"v_exp_f32_e64 v158, v158\n" \
"v_mfma_f32_16x16x32_bf16 v[132:135], a[120:123], a[8:11], v[132:135]\n" \
"s_waitcnt lgkmcnt(0)\n" \
"v_mfma_f32_16x16x32_bf16 v[132:135], a[116:119], a[4:7], v[132:135]\n" \
"buffer_load_dwordx4 %[vd0], %[srdv], s48 offen offset:0 lds\n" \
"v_mfma_f32_16x16x32_bf16 v[132:135], a[112:115], a[0:3], v[132:135]\n" \
"v_exp_f32_e32 v159, v159\n" \
"v_mfma_f32_16x16x32_bf16 v[32:35], a[160:163], v[196:199], v[32:35]\n" \
"ds_read_b128 a[64:67], %[kb0] offset:0\n" \
"v_mfma_f32_16x16x32_bf16 v[32:35], a[128:131], v[188:191], v[32:35]\n" \
"v_exp_f32_e64 v128, v128\n" \
"v_mfma_f32_16x16x32_bf16 v[36:39], a[164:167], v[196:199], v[36:39]\n" \
"v_cvt_pk_bf16_f32 v222, v156, v157\n" \
"v_cvt_pk_bf16_f32 v223, v158, v159\n" \
"v_mfma_f32_16x16x32_bf16 v[36:39], a[132:135], v[188:191], v[36:39]\n" \
"v_exp_f32_e64 v129, v129\n" \
"v_mfma_f32_16x16x32_bf16 v[40:43], a[168:171], v[196:199], v[40:43]\n" \
"ds_read_b128 a[68:71], v208 offset:0\n" \
"v_mfma_f32_16x16x32_bf16 v[40:43], a[136:139], v[188:191], v[40:43]\n" \
"v_exp_f32_e64 v130, v130\n" \
"v_mfma_f32_16x16x32_bf16 v[44:47], a[172:175], v[196:199], v[44:47]\n" \
"ds_read_b128 a[72:75], v209 offset:0\n" \
"v_mfma_f32_16x16x32_bf16 v[44:47], a[140:143], v[188:191], v[44:47]\n" \
"v_exp_f32_e64 v131, v131\n" \
"v_smfmac_f32_16x16x64_bf16 v[176:179], a[192:195], v[184:191], v228\n" \
"s_cbranch_vccz Lpp7_%=\n" \
"buffer_store_dwordx4 a[196:199], v235, %[srdz], s63 offen offset:2048 sc1\n" \
"buffer_store_dwordx4 a[196:199], v226, %[srdz], s63 offen offset:2048 sc1\n" \
"Lpp7_%=:\n" \
"s_add_u32 s63, s63, 2560\n" \
"v_mfma_f32_16x16x32_bf16 v[136:139], a[96:99], a[16:19], v[164:167]\n" \
"v_exp_f32_e32 v132, v132\n" \
"v_mfma_f32_16x16x32_bf16 v[136:139], a[100:103], a[20:23], v[136:139]\n" \
"ds_read_b128 a[76:79], v210 offset:0\n" \
"v_mfma_f32_16x16x32_bf16 v[136:139], a[104:107], a[24:27], v[136:139]\n" \
"v_exp_f32_e64 v133, v133\n" \
"v_mfma_f32_16x16x32_bf16 v[136:139], a[108:111], a[28:31], v[136:139]\n" \
"v_cvt_pk_bf16_f32 v192, v128, v129\n" \
"v_cvt_pk_bf16_f32 v193, v130, v131\n" \
"v_mfma_f32_16x16x32_bf16 v[140:143], a[124:127], a[28:31], v[164:167]\n" \
"v_exp_f32_e64 v134, v134\n" \
"v_mfma_f32_16x16x32_bf16 v[140:143], a[120:123], a[24:27], v[140:143]\n" \
"ds_read_b128 a[92:95], v210 offset:4096\n" \
"v_mfma_f32_16x16x32_bf16 v[140:143], a[116:119], a[20:23], v[140:143]\n" \
"buffer_load_dwordx4 v215, %[srdv], s48 offen offset:1024 lds\n" \
"v_mfma_f32_16x16x32_bf16 v[140:143], a[112:115], a[16:19], v[140:143]\n" \
"v_exp_f32_e64 v135, v135\n" \
"v_mfma_f32_16x16x32_bf16 v[48:51], a[176:179], v[196:199], v[48:51]\n" \
"ds_read_b128 a[88:91], v209 offset:4096\n" \
"v_mfma_f32_16x16x32_bf16 v[48:51], a[144:147], v[188:191], v[48:51]\n" \
"v_exp_f32_e64 v136, v136\n" \
"v_mfma_f32_16x16x32_bf16 v[52:55], a[180:183], v[196:199], v[52:55]\n" \
"v_cvt_pk_bf16_f32 v194, v132, v133\n" \
"v_cvt_pk_bf16_f32 v195, v134, v135\n" \
"v_mfma_f32_16x16x32_bf16 v[52:55], a[148:151], v[188:191], v[52:55]\n" \
"v_exp_f32_e64 v137, v137\n" \
"v_mfma_f32_16x16x32_bf16 v[56:59], a[184:187], v[196:199], v[56:59]\n" \
"v_exp_f32_e64 v138, v138\n" \
"v_mfma_f32_16x16x32_bf16 v[56:59], a[152:155], v[188:191], v[56:59]\n" \
"ds_read_b128 a[84:87], v208 offset:4096\n" \
"v_mfma_f32_16x16x32_bf16 v[60:63], a[188:191], v[196:199], v[60:63]\n" \
"v_exp_f32_e64 v139, v139\n" \
"v_mfma_f32_16x16x32_bf16 v[60:63], a[156:159], v[188:191], v[60:63]\n" \
"s_cmp_lg_u32 %[wid], 2\n" \
"s_cbranch_scc1 LtB07_%=\n" \
"global_load_dword v237, v236, s[58:59]\n" \
"s_add_u32 s61, s48, 49152\n" \
"s_mov_b32 exec_hi, 0\n" \
"s_nop 0\n" \
"buffer_load_dword v237, v224, %[srdp], s61 offen\n" \
"s_mov_b32 exec_hi, -1\n" \
"s_nop 0\n" \
"LtB07_%=:\n" \
"v_mfma_f32_16x16x32_bf16 v[144:147], a[96:99], a[32:35], v[168:171]\n" \
"v_exp_f32_e64 v140, v140\n" \
"v_mfma_f32_16x16x32_bf16 v[144:147], a[100:103], a[36:39], v[144:147]\n" \
"ds_read_b128 a[80:83], %[kb0] offset:4096\n" \
"v_mfma_f32_16x16x32_bf16 v[144:147], a[104:107], a[40:43], v[144:147]\n" \
"v_exp_f32_e64 v141, v141\n" \
"v_mfma_f32_16x16x32_bf16 v[144:147], a[108:111], a[44:47], v[144:147]\n" \
"v_cvt_pk_bf16_f32 v196, v136, v137\n" \
"v_cvt_pk_bf16_f32 v197, v138, v139\n" \
"v_mfma_f32_16x16x32_bf16 v[148:151], a[124:127], a[44:47], v[168:171]\n" \
"v_exp_f32_e64 v142, v142\n" \
"v_mfma_f32_16x16x32_bf16 v[148:151], a[120:123], a[40:43], v[148:151]\n" \
"v_mfma_f32_16x16x32_bf16 v[148:151], a[116:119], a[36:39], v[148:151]\n" \
"buffer_load_dwordx4 %[vd0], %[srdv], s48 offen offset:2048 lds\n" \
"v_mfma_f32_16x16x32_bf16 v[148:151], a[112:115], a[32:35], v[148:151]\n" \
"v_exp_f32_e64 v143, v143\n" \
"v_mfma_f32_16x16x32_bf16 v[96:99], a[160:163], v[204:207], v[96:99]\n" \
".if . - adaln_body != 28672\n" \
".error \"size model\"\n" \
".endif\n" \
"Lk6_%=:\n" \
"s_cbranch_execz Lk7_%=\n" \
"s_nop 0\n" \
"ds_read_b64_tr_b16 a[160:161], %[vvb] offset:57344\n" \
"v_mfma_f32_16x16x32_bf16 v[96:99], a[128:131], v[220:223], v[96:99]\n" \
"v_exp_f32_e64 v144, v144\n" \
"v_mfma_f32_16x16x32_bf16 v[100:103], a[164:167], v[204:207], v[100:103]\n" \
"v_cvt_pk_bf16_f32 v198, v140, v141\n" \
"v_cvt_pk_bf16_f32 v199, v142, v143\n" \
"v_mfma_f32_16x16x32_bf16 v[100:103], a[132:135], v[220:223], v[100:103]\n" \
"v_exp_f32_e64 v145, v145\n" \
"v_mfma_f32_16x16x32_bf16 v[104:107], a[168:171], v[204:207], v[104:107]\n" \
"ds_read_b64_tr_b16 a[162:163], %[vvb] offset:61440\n" \
"ds_read_b64_tr_b16 a[164:165], v211 offset:57344\n" \
"v_mfma_f32_16x16x32_bf16 v[104:107], a[136:139], v[220:223], v[104:107]\n" \
"v_exp_f32_e64 v146, v146\n" \
"v_mfma_f32_16x16x32_bf16 v[108:111], a[172:175], v[204:207], v[108:111]\n" \
"ds_read_b64_tr_b16 a[166:167], v211 offset:61440\n" \
"v_mfma_f32_16x16x32_bf16 v[108:111], a[140:143], v[220:223], v[108:111]\n" \
"v_exp_f32_e64 v147, v147\n" \
"v_smfmac_f32_16x16x64_bf16 v[180:183], a[192:195], v[216:223], v228\n" \
"ds_read_b64_tr_b16 a[168:169], %[vvb] offset:57856\n" \
"v_mfma_f32_16x16x32_bf16 v[152:155], a[96:99], a[48:51], v[172:175]\n" \
"v_exp_f32_e64 v148, v148\n" \
"v_mfma_f32_16x16x32_bf16 v[152:155], a[100:103], a[52:55], v[152:155]\n" \
"ds_read_b64_tr_b16 a[170:171], %[vvb] offset:61952\n" \
"v_mfma_f32_16x16x32_bf16 v[152:155], a[104:107], a[56:59], v[152:155]\n" \
"v_exp_f32_e64 v149, v149\n" \
"v_mfma_f32_16x16x32_bf16 v[152:155], a[108:111], a[60:63], v[152:155]\n" \
"v_cvt_pk_bf16_f32 v200, v144, v145\n" \
"v_cvt_pk_bf16_f32 v201, v146, v147\n" \
"v_mfma_f32_16x16x32_bf16 v[156:159], a[124:127], a[60:63], v[172:175]\n" \
"v_exp_f32_e64 v150, v150\n" \
"v_mfma_f32_16x16x32_bf16 v[156:159], a[120:123], a[56:59], v[156:159]\n" \
"ds_read_b64_tr_b16 a[172:173], v211 offset:57856\n" \
"v_mfma_f32_16x16x32_bf16 v[156:159], a[116:119], a[52:55], v[156:159]\n" \
"buffer_load_dwordx4 v215, %[srdv], s48 offen offset:3072 lds\n" \
"v_mfma_f32_16x16x32_bf16 v[156:159], a[112:115], a[48:51], v[156:159]\n" \
"v_exp_f32_e64 v151, v151\n" \
"v_mfma_f32_16x16x32_bf16 v[112:115], a[176:179], v[204:207], v[112:115]\n" \
"ds_read_b64_tr_b16 a[174:175], v211 offset:61952\n" \
"ds_read_b64_tr_b16 a[176:177], %[vvb] offset:58368\n" \
"v_mfma_f32_16x16x32_bf16 v[112:115], a[144:147], v[220:223], v[112:115]\n" \
"v_exp_f32_e64 v152, v152\n" \
"v_mfma_f32_16x16x32_bf16 v[116:119], a[180:183], v[204:207], v[116:119]\n" \
"v_cvt_pk_bf16_f32 v202, v148, v149\n" \
"v_cvt_pk_bf16_f32 v203, v150, v151\n" \
"v_mfma_f32_16x16x32_bf16 v[116:119], a[148:151], v[220:223], v[116:119]\n" \
"v_exp_f32_e64 v153, v153\n" \
"v_mfma_f32_16x16x32_bf16 v[120:123], a[184:187], v[204:207], v[120:123]\n" \
"v_exp_f32_e64 v154, v154\n" \
"v_mfma_f32_16x16x32_bf16 v[120:123], a[152:155], v[220:223], v[120:123]\n" \
"ds_read_b64_tr_b16 a[178:179], %[vvb] offset:62464\n" \
"v_mfma_f32_16x16x32_bf16 v[124:127], a[188:191], v[204:207], v[124:127]\n" \
"v_exp_f32_e64 v155, v155\n" \
"v_mfma_f32_16x16x32_bf16 v[124:127], a[156:159], v[220:223], v[124:127]\n" \
"ds_read_b64_tr_b16 a[180:181], v211 offset:58368\n" \
"s_waitcnt lgkmcnt(13)\n" \
"v_mfma_f32_16x16x32_bf16 v[128:131], a[64:67], a[0:3], v[160:163]\n" \
"v_exp_f32_e32 v156, v156\n" \
"v_mfma_f32_16x16x32_bf16 v[128:131], a[68:71], a[4:7], v[128:131]\n" \
"ds_read_b64_tr_b16 a[182:183], v211 offset:62464\n" \
"s_add_u32 m0, %[m0b], 49152\n" \
"v_mfma_f32_16x16x32_bf16 v[128:131], a[72:75], a[8:11], v[128:131]\n" \
"v_exp_f32_e64 v157, v157\n" \
"v_mfma_f32_16x16x32_bf16 v[128:131], a[76:79], a[12:15], v[128:131]\n" \
"v_cvt_pk_bf16_f32 v204, v152, v153\n" \
"v_cvt_pk_bf16_f32 v205, v154, v155\n" \
"v_mfma_f32_16x16x32_bf16 v[132:135], a[92:95], a[12:15], v[160:163]\n" \
"v_exp_f32_e64 v158, v158\n" \
"v_mfma_f32_16x16x32_bf16 v[132:135], a[88:91], a[8:11], v[132:135]\n" \
"ds_read_b64_tr_b16 a[184:185], %[vvb] offset:58880\n" \
"s_waitcnt lgkmcnt(5)\n" \
"v_mfma_f32_16x16x32_bf16 v[132:135], a[84:87], a[4:7], v[132:135]\n" \
"buffer_load_dwordx4 %[kd0], %[srdk], s48 offen offset:0 lds\n" \
"v_mfma_f32_16x16x32_bf16 v[132:135], a[80:83], a[0:3], v[132:135]\n" \
"v_exp_f32_e32 v159, v159\n" \
"v_mfma_f32_16x16x32_bf16 v[64:67], a[128:131], v[216:219], v[64:67]\n" \
"ds_read_b64_tr_b16 a[186:187], %[vvb] offset:62976\n" \
"ds_read_b64_tr_b16 a[188:189], v211 offset:58880\n" \
"v_mfma_f32_16x16x32_bf16 v[64:67], a[160:163], v[200:203], v[64:67]\n" \
"v_exp_f32_e64 v128, v128\n" \
"v_mfma_f32_16x16x32_bf16 v[68:71], a[132:135], v[216:219], v[68:71]\n" \
"v_cvt_pk_bf16_f32 v206, v156, v157\n" \
"v_cvt_pk_bf16_f32 v207, v158, v159\n" \
"v_mfma_f32_16x16x32_bf16 v[68:71], a[164:167], v[200:203], v[68:71]\n" \
"v_exp_f32_e64 v129, v129\n" \
"v_mfma_f32_16x16x32_bf16 v[72:75], a[136:139], v[216:219], v[72:75]\n" \
"ds_read_b64_tr_b16 a[190:191], v211 offset:62976\n" \
"ds_read_b64_tr_b16 a[128:129], %[vvb] offset:0\n" \
"v_mfma_f32_16x16x32_bf16 v[72:75], a[168:171], v[200:203], v[72:75]\n" \
"v_exp_f32_e64 v130, v130\n" \
"v_mfma_f32_16x16x32_bf16 v[76:79], a[140:143], v[216:219], v[76:79]\n" \
"ds_read_b64_tr_b16 a[130:131], %[vvb] offset:4096\n" \
"ds_read_b64_tr_b16 a[132:133], v211 offset:0\n" \
"v_mfma_f32_16x16x32_bf16 v[76:79], a[172:175], v[200:203], v[76:79]\n" \
"v_exp_f32_e64 v131, v131\n" \
"v_smfmac_f32_16x16x64_bf16 v[176:179], a[192:195], v[192:199], v228\n" \
"ds_read_b64_tr_b16 a[134:135], v211 offset:4096\n" \
"ds_read_b64_tr_b16 a[136:137], %[vvb] offset:512\n" \
"v_mfma_f32_16x16x32_bf16 v[136:139], a[64:67], a[16:19], v[164:167]\n" \
"v_exp_f32_e64 v132, v132\n" \
"v_mfma_f32_16x16x32_bf16 v[136:139], a[68:71], a[20:23], v[136:139]\n" \
"ds_read_b64_tr_b16 a[138:139], %[vvb] offset:4608\n" \
"ds_read_b64_tr_b16 a[140:141], v211 offset:512\n" \
"v_mfma_f32_16x16x32_bf16 v[136:139], a[72:75], a[24:27], v[136:139]\n" \
"v_exp_f32_e64 v133, v133\n" \
"v_mfma_f32_16x16x32_bf16 v[136:139], a[76:79], a[28:31], v[136:139]\n" \
"v_cvt_pk_bf16_f32 v184, v128, v129\n" \
"v_cvt_pk_bf16_f32 v185, v130, v131\n" \
"v_mfma_f32_16x16x32_bf16 v[140:143], a[92:95], a[28:31], v[164:167]\n" \
"v_exp_f32_e64 v134, v134\n" \
"v_mfma_f32_16x16x32_bf16 v[140:143], a[88:91], a[24:27], v[140:143]\n" \
"ds_read_b64_tr_b16 a[142:143], v211 offset:4608\n" \
"s_waitcnt lgkmcnt(8)\n" \
"v_mfma_f32_16x16x32_bf16 v[140:143], a[84:87], a[20:23], v[140:143]\n" \
"buffer_load_dwordx4 v212, %[srdk], s48 offen offset:1024 lds\n" \
"v_mfma_f32_16x16x32_bf16 v[140:143], a[80:83], a[16:19], v[140:143]\n" \
"v_exp_f32_e32 v135, v135\n" \
"v_mfma_f32_16x16x32_bf16 v[80:83], a[144:147], v[216:219], v[80:83]\n" \
"ds_read_b64_tr_b16 a[144:145], %[vvb] offset:1024\n" \
"ds_read_b64_tr_b16 a[146:147], %[vvb] offset:5120\n" \
"v_mfma_f32_16x16x32_bf16 v[80:83], a[176:179], v[200:203], v[80:83]\n" \
"v_exp_f32_e64 v136, v136\n" \
"v_mfma_f32_16x16x32_bf16 v[84:87], a[148:151], v[216:219], v[84:87]\n" \
"v_cvt_pk_bf16_f32 v186, v132, v133\n" \
"v_cvt_pk_bf16_f32 v187, v134, v135\n" \
"v_mfma_f32_16x16x32_bf16 v[84:87], a[180:183], v[200:203], v[84:87]\n" \
"v_exp_f32_e64 v137, v137\n" \
"v_mfma_f32_16x16x32_bf16 v[88:91], a[152:155], v[216:219], v[88:91]\n" \
"v_exp_f32_e64 v138, v138\n" \
"v_mfma_f32_16x16x32_bf16 v[88:91], a[184:187], v[200:203], v[88:91]\n" \
"ds_read_b64_tr_b16 a[148:149], v211 offset:1024\n" \
"ds_read_b64_tr_b16 a[150:151], v211 offset:5120\n" \
"v_mfma_f32_16x16x32_bf16 v[92:95], a[156:159], v[216:219], v[92:95]\n" \
"v_exp_f32_e64 v139, v139\n" \
"v_mfma_f32_16x16x32_bf16 v[92:95], a[188:191], v[200:203], v[92:95]\n" \
"ds_read_b64_tr_b16 a[152:153], %[vvb] offset:1536\n" \
"s_cbranch_vccz Lpp8_%=\n" \
"buffer_store_dwordx4 a[196:199], v235, %[srdz], s62 offen offset:2048 sc1\n" \
"buffer_store_dwordx4 a[196:199], v226, %[srdz], s62 offen offset:2048 sc1\n" \
"Lpp8_%=:\n" \
"s_add_u32 s62, s62, 2560\n" \
"v_mfma_f32_16x16x32_bf16 v[144:147], a[64:67], a[32:35], v[168:171]\n" \
"v_exp_f32_e32 v140, v140\n" \
"v_mfma_f32_16x16x32_bf16 v[144:147], a[68:71], a[36:39], v[144:147]\n" \
"ds_read_b64_tr_b16 a[154:155], %[vvb] offset:5632\n" \
"ds_read_b64_tr_b16 a[156:157], v211 offset:1536\n" \
"v_mfma_f32_16x16x32_bf16 v[144:147], a[72:75], a[40:43], v[144:147]\n" \
"v_exp_f32_e64 v141, v141\n" \
"v_mfma_f32_16x16x32_bf16 v[144:147], a[76:79], a[44:47], v[144:147]\n" \
"v_cvt_pk_bf16_f32 v188, v136, v137\n" \
"v_cvt_pk_bf16_f32 v189, v138, v139\n" \
"v_mfma_f32_16x16x32_bf16 v[148:151], a[92:95], a[44:47], v[168:171]\n" \
"v_exp_f32_e64 v142, v142\n" \
"v_mfma_f32_16x16x32_bf16 v[148:151], a[88:91], a[40:43], v[148:151]\n" \
"ds_read_b64_tr_b16 a[158:159], v211 offset:5632\n" \
"s_waitcnt lgkmcnt(8)\n" \
"v_mfma_f32_16x16x32_bf16 v[148:151], a[84:87], a[36:39], v[148:151]\n" \
"buffer_load_dwordx4 v213, %[srdk], s48 offen offset:2048 lds\n" \
"v_mfma_f32_16x16x32_bf16 v[148:151], a[80:83], a[32:35], v[148:151]\n" \
"v_exp_f32_e32 v143, v143\n" \
"v_mfma_f32_16x16x32_bf16 v[0:3], a[160:163], v[192:195], v[0:3]\n" \
"ds_read_b128 a[96:99], %[kb0] offset:8192\n" \
"v_mfma_f32_16x16x32_bf16 v[0:3], a[128:131], v[184:187], v[0:3]\n" \
"v_exp_f32_e64 v144, v144\n" \
"v_mfma_f32_16x16x32_bf16 v[4:7], a[164:167], v[192:195], v[4:7]\n" \
"v_cvt_pk_bf16_f32 v190, v140, v141\n" \
"v_cvt_pk_bf16_f32 v191, v142, v143\n" \
"v_mfma_f32_16x16x32_bf16 v[4:7], a[132:135], v[184:187], v[4:7]\n" \
"v_exp_f32_e64 v145, v145\n" \
"v_mfma_f32_16x16x32_bf16 v[8:11], a[168:171], v[192:195], v[8:11]\n" \
"ds_read_b128 a[100:103], v208 offset:8192\n" \
"v_mfma_f32_16x16x32_bf16 v[8:11], a[136:139], v[184:187], v[8:11]\n" \
"v_exp_f32_e64 v146, v146\n" \
"v_mfma_f32_16x16x32_bf16 v[12:15], a[172:175], v[192:195], v[12:15]\n" \
"ds_read_b128 a[104:107], v209 offset:8192\n" \
"v_mfma_f32_16x16x32_bf16 v[12:15], a[140:143], v[184:187], v[12:15]\n" \
"v_exp_f32_e64 v147, v147\n" \
"v_smfmac_f32_16x16x64_bf16 v[180:183], a[192:195], v[200:207], v228\n" \
"ds_read_b128 a[108:111], v210 offset:8192\n" \
"v_mfma_f32_16x16x32_bf16 v[152:155], a[64:67], a[48:51], v[172:175]\n" \
"v_exp_f32_e64 v148, v148\n" \
"v_mfma_f32_16x16x32_bf16 v[152:155], a[68:71], a[52:55], v[152:155]\n" \
"ds_read_b128 a[124:127], v210 offset:12288\n" \
"v_mfma_f32_16x16x32_bf16 v[152:155], a[72:75], a[56:59], v[152:155]\n" \
"v_exp_f32_e64 v149, v149\n" \
"v_mfma_f32_16x16x32_bf16 v[152:155], a[76:79], a[60:63], v[152:155]\n" \
"v_cvt_pk_bf16_f32 v216, v144, v145\n" \
"v_cvt_pk_bf16_f32 v217, v146, v147\n" \
"v_mfma_f32_16x16x32_bf16 v[156:159], a[92:95], a[60:63], v[172:175]\n" \
"v_exp_f32_e64 v150, v150\n" \
"v_mfma_f32_16x16x32_bf16 v[156:159], a[88:91], a[56:59], v[156:159]\n" \
"ds_read_b128 a[120:123], v209 offset:12288\n" \
"s_waitcnt lgkmcnt(6)\n" \
"v_mfma_f32_16x16x32_bf16 v[156:159], a[84:87], a[52:55], v[156:159]\n" \
"buffer_load_dwordx4 v214, %[srdk], s48 offen offset:3072 lds\n" \
"v_mfma_f32_16x16x32_bf16 v[156:159], a[80:83], a[48:51], v[156:159]\n" \
"v_exp_f32_e32 v151, v151\n" \
"v_mfma_f32_16x16x32_bf16 v[16:19], a[176:179], v[192:195], v[16:19]\n" \
"ds_read_b128 a[116:119], v208 offset:12288\n" \
"v_mfma_f32_16x16x32_bf16 v[16:19], a[144:147], v[184:187], v[16:19]\n" \
"v_exp_f32_e64 v152, v152\n" \
"v_mfma_f32_16x16x32_bf16 v[20:23], a[180:183], v[192:195], v[20:23]\n" \
"v_cvt_pk_bf16_f32 v218, v148, v149\n" \
"v_cvt_pk_bf16_f32 v219, v150, v151\n" \
"v_mfma_f32_16x16x32_bf16 v[20:23], a[148:151], v[184:187], v[20:23]\n" \
"v_exp_f32_e64 v153, v153\n" \
"v_mfma_f32_16x16x32_bf16 v[24:27], a[184:187], v[192:195], v[24:27]\n" \
"v_exp_f32_e64 v154, v154\n" \
"v_mfma_f32_16x16x32_bf16 v[24:27], a[152:155], v[184:187], v[24:27]\n" \
"ds_read_b128 a[112:115], %[kb0] offset:12288\n" \
"s_add_u32 s48, s48, 0x4000\n" \
"v_mfma_f32_16x16x32_bf16 v[28:31], a[188:191], v[192:195], v[28:31]\n" \
"v_exp_f32_e64 v155, v155\n" \
"v_mfma_f32_16x16x32_bf16 v[28:31], a[156:159], v[184:187], v[28:31]\n" \
"s_waitcnt vmcnt(14) lgkmcnt(2)\n" \
"s_barrier\n" \
"v_mfma_f32_16x16x32_bf16 v[128:131], a[96:99], a[0:3], v[160:163]\n" \
"v_exp_f32_e64 v156, v156\n" \
"v_mfma_f32_16x16x32_bf16 v[128:131], a[100:103], a[4:7], v[128:131]\n" \
"s_add_u32 m0, %[m0b], 114688\n" \
"v_mfma_f32_16x16x32_bf16 v[128:131], a[104:107], a[8:11], v[128:131]\n" \
"v_exp_f32_e64 v157, v157\n" \
"v_mfma_f32_16x16x32_bf16 v[128:131], a[108:111], a[12:15], v[128:131]\n" \
"v_cvt_pk_bf16_f32 v220, v152, v153\n" \
"v_cvt_pk_bf16_f32 v221, v154, v155\n" \
"v_mfma_f32_16x16x32_bf16 v[132:135], a[124:127], a[12:15], v[160:163]\n" \
"v_exp_f32_e64 v158, v158\n" \
"v_mfma_f32_16x16x32_bf16 v[132:135], a[120:123], a[8:11], v[132:135]\n" \
"s_waitcnt lgkmcnt(0)\n" \
"v_mfma_f32_16x16x32_bf16 v[132:135], a[116:119], a[4:7], v[132:135]\n" \
"buffer_load_dwordx4 %[vd0], %[srdv], s48 offen offset:0 lds\n" \
"v_mfma_f32_16x16x32_bf16 v[132:135], a[112:115], a[0:3], v[132:135]\n" \
"v_exp_f32_e32 v159, v159\n" \
"v_mfma_f32_16x16x32_bf16 v[32:35], a[160:163], v[196:199], v[32:35]\n" \
"ds_read_b128 a[64:67], %[kb0] offset:16384\n" \
"v_mfma_f32_16x16x32_bf16 v[32:35], a[128:131], v[188:191], v[32:35]\n" \
"v_exp_f32_e64 v128, v128\n" \
"v_mfma_f32_16x16x32_bf16 v[36:39], a[164:167], v[196:199], v[36:39]\n" \
"v_cvt_pk_bf16_f32 v222, v156, v157\n" \
"v_cvt_pk_bf16_f32 v223, v158, v159\n" \
"v_mfma_f32_16x16x32_bf16 v[36:39], a[132:135], v[188:191], v[36:39]\n" \
"v_exp_f32_e64 v129, v129\n" \
"v_mfma_f32_16x16x32_bf16 v[40:43], a[168:171], v[196:199], v[40:43]\n" \
"ds_read_b128 a[68:71], v208 offset:16384\n" \
"v_mfma_f32_16x16x32_bf16 v[40:43], a[136:139], v[188:191], v[40:43]\n" \
"v_exp_f32_e64 v130, v130\n" \
"v_mfma_f32_16x16x32_bf16 v[44:47], a[172:175], v[196:199], v[44:47]\n" \
"ds_read_b128 a[72:75], v209 offset:16384\n" \
"v_mfma_f32_16x16x32_bf16 v[44:47], a[140:143], v[188:191], v[44:47]\n" \
"v_exp_f32_e64 v131, v131\n" \
"v_smfmac_f32_16x16x64_bf16 v[176:179], a[192:195], v[184:191], v228\n" \
"v_mfma_f32_16x16x32_bf16 v[136:139], a[96:99], a[16:19], v[164:167]\n" \
"v_exp_f32_e64 v132, v132\n" \
"v_mfma_f32_16x16x32_bf16 v[136:139], a[100:103], a[20:23], v[136:139]\n" \
"ds_read_b128 a[76:79], v210 offset:16384\n" \
"v_mfma_f32_16x16x32_bf16 v[136:139], a[104:107], a[24:27], v[136:139]\n" \
"v_exp_f32_e64 v133, v133\n" \
"v_mfma_f32_16x16x32_bf16 v[136:139], a[108:111], a[28:31], v[136:139]\n" \
"v_cvt_pk_bf16_f32 v192, v128, v129\n" \
"v_cvt_pk_bf16_f32 v193, v130, v131\n" \
"v_mfma_f32_16x16x32_bf16 v[140:143], a[124:127], a[28:31], v[164:167]\n" \
"v_exp_f32_e64 v134, v134\n" \
"v_mfma_f32_16x16x32_bf16 v[140:143], a[120:123], a[24:27], v[140:143]\n" \
"ds_read_b128 a[92:95], v210 offset:20480\n" \
"v_mfma_f32_16x16x32_bf16 v[140:143], a[116:119], a[20:23], v[140:143]\n" \
"buffer_load_dwordx4 v215, %[srdv], s48 offen offset:1024 lds\n" \
"v_mfma_f32_16x16x32_bf16 v[140:143], a[112:115], a[16:19], v[140:143]\n" \
"v_exp_f32_e64 v135, v135\n" \
"v_mfma_f32_16x16x32_bf16 v[48:51], a[176:179], v[196:199], v[48:51]\n" \
"ds_read_b128 a[88:91], v209 offset:20480\n" \
"v_mfma_f32_16x16x32_bf16 v[48:51], a[144:147], v[188:191], v[48:51]\n" \
"v_exp_f32_e64 v136, v136\n" \
"v_mfma_f32_16x16x32_bf16 v[52:55], a[180:183], v[196:199], v[52:55]\n" \
"v_cvt_pk_bf16_f32 v194, v132, v133\n" \
"v_cvt_pk_bf16_f32 v195, v134, v135\n" \
"v_mfma_f32_16x16x32_bf16 v[52:55], a[148:151], v[188:191], v[52:55]\n" \
"v_exp_f32_e64 v137, v137\n" \
"v_mfma_f32_16x16x32_bf16 v[56:59], a[184:187], v[196:199], v[56:59]\n" \
"v_exp_f32_e64 v138, v138\n" \
"v_mfma_f32_16x16x32_bf16 v[56:59], a[152:155], v[188:191], v[56:59]\n" \
"ds_read_b128 a[84:87], v208 offset:20480\n" \
"v_mfma_f32_16x16x32_bf16 v[60:63], a[188:191], v[196:199], v[60:63]\n" \
"v_exp_f32_e64 v139, v139\n" \
"v_mfma_f32_16x16x32_bf16 v[60:63], a[156:159], v[188:191], v[60:63]\n" \
"s_cmp_lg_u32 %[wid], 3\n" \
"s_cbranch_scc1 LtB01_%=\n" \
"global_load_dword v237, v236, s[58:59]\n" \
"s_add_u32 s61, s48, 49152\n" \
"s_mov_b32 exec_hi, 0\n" \
"s_nop 0\n" \
"buffer_load_dword v237, v224, %[srdp], s61 offen\n" \
"s_mov_b32 exec_hi, -1\n" \
"s_nop 0\n" \
"LtB01_%=:\n" \
"v_mfma_f32_16x16x32_bf16 v[144:147], a[96:99], a[32:35], v[168:171]\n" \
"v_exp_f32_e64 v140, v140\n" \
"v_mfma_f32_16x16x32_bf16 v[144:147], a[100:103], a[36:39], v[144:147]\n" \
"ds_read_b128 a[80:83], %[kb0] offset:20480\n" \
"v_mfma_f32_16x16x32_bf16 v[144:147], a[104:107], a[40:43], v[144:147]\n" \
"v_exp_f32_e64 v141, v141\n" \
"v_mfma_f32_16x16x32_bf16 v[144:147], a[108:111], a[44:47], v[144:147]\n" \
"v_cvt_pk_bf16_f32 v196, v136, v137\n" \
"v_cvt_pk_bf16_f32 v197, v138, v139\n" \
"v_mfma_f32_16x16x32_bf16 v[148:151], a[124:127], a[44:47], v[168:171]\n" \
"v_exp_f32_e64 v142, v142\n" \
"v_mfma_f32_16x16x32_bf16 v[148:151], a[120:123], a[40:43], v[148:151]\n" \
"v_mfma_f32_16x16x32_bf16 v[148:151], a[116:119], a[36:39], v[148:151]\n" \
"buffer_load_dwordx4 %[vd0], %[srdv], s48 offen offset:2048 lds\n" \
"v_mfma_f32_16x16x32_bf16 v[148:151], a[112:115], a[32:35], v[148:151]\n" \
"v_exp_f32_e64 v143, v143\n" \
"v_mfma_f32_16x16x32_bf16 v[96:99], a[160:163], v[204:207], v[96:99]\n" \
"ds_read_b64_tr_b16 a[160:161], %[vvb] offset:8192\n" \
"v_mfma_f32_16x16x32_bf16 v[96:99], a[128:131], v[220:223], v[96:99]\n" \
"v_exp_f32_e64 v144, v144\n" \
"v_mfma_f32_16x16x32_bf16 v[100:103], a[164:167], v[204:207], v[100:103]\n" \
"v_cvt_pk_bf16_f32 v198, v140, v141\n" \
"v_cvt_pk_bf16_f32 v199, v142, v143\n" \
"v_mfma_f32_16x16x32_bf16 v[100:103], a[132:135], v[220:223], v[100:103]\n" \
"v_exp_f32_e64 v145, v145\n" \
"v_mfma_f32_16x16x32_bf16 v[104:107], a[168:171], v[204:207], v[104:107]\n" \
"ds_read_b64_tr_b16 a[162:163], %[vvb] offset:12288\n" \
"ds_read_b64_tr_b16 a[164:165], v211 offset:8192\n" \
"v_mfma_f32_16x16x32_bf16 v[104:107], a[136:139], v[220:223], v[104:107]\n" \
"v_exp_f32_e64 v146, v146\n" \
"v_mfma_f32_16x16x32_bf16 v[108:111], a[172:175], v[204:207], v[108:111]\n" \
"ds_read_b64_tr_b16 a[166:167], v211 offset:12288\n" \
"v_mfma_f32_16x16x32_bf16 v[108:111], a[140:143], v[220:223], v[108:111]\n" \
"v_exp_f32_e64 v147, v147\n" \
"v_smfmac_f32_16x16x64_bf16 v[180:183], a[192:195], v[216:223], v228\n" \
"ds_read_b64_tr_b16 a[168:169], %[vvb] offset:8704\n" \
"v_mfma_f32_16x16x32_bf16 v[152:155], a[96:99], a[48:51], v[172:175]\n" \
"v_exp_f32_e64 v148, v148\n" \
"v_mfma_f32_16x16x32_bf16 v[152:155], a[100:103], a[52:55], v[152:155]\n" \
"ds_read_b64_tr_b16 a[170:171], %[vvb] offset:12800\n" \
"v_mfma_f32_16x16x32_bf16 v[152:155], a[104:107], a[56:59], v[152:155]\n" \
"v_exp_f32_e64 v149, v149\n" \
"v_mfma_f32_16x16x32_bf16 v[152:155], a[108:111], a[60:63], v[152:155]\n" \
"v_cvt_pk_bf16_f32 v200, v144, v145\n" \
"v_cvt_pk_bf16_f32 v201, v146, v147\n" \
"v_mfma_f32_16x16x32_bf16 v[156:159], a[124:127], a[60:63], v[172:175]\n" \
"v_exp_f32_e64 v150, v150\n" \
"v_mfma_f32_16x16x32_bf16 v[156:159], a[120:123], a[56:59], v[156:159]\n" \
"ds_read_b64_tr_b16 a[172:173], v211 offset:8704\n" \
"v_mfma_f32_16x16x32_bf16 v[156:159], a[116:119], a[52:55], v[156:159]\n" \
"buffer_load_dwordx4 v215, %[srdv], s48 offen offset:3072 lds\n" \
"v_mfma_f32_16x16x32_bf16 v[156:159], a[112:115], a[48:51], v[156:159]\n" \
"v_exp_f32_e64 v151, v151\n" \
"v_mfma_f32_16x16x32_bf16 v[112:115], a[176:179], v[204:207], v[112:115]\n" \
"ds_read_b64_tr_b16 a[174:175], v211 offset:12800\n" \
"ds_read_b64_tr_b16 a[176:177], %[vvb] offset:9216\n" \
"v_mfma_f32_16x16x32_bf16 v[112:115], a[144:147], v[220:223], v[112:115]\n" \
"v_exp_f32_e64 v152, v152\n" \
"v_mfma_f32_16x16x32_bf16 v[116:119], a[180:183], v[204:207], v[116:119]\n" \
"v_cvt_pk_bf16_f32 v202, v148, v149\n" \
"v_cvt_pk_bf16_f32 v203, v150, v151\n" \
"v_mfma_f32_16x16x32_bf16 v[116:119], a[148:151], v[220:223], v[116:119]\n" \
"v_exp_f32_e64 v153, v153\n" \
"v_mfma_f32_16x16x32_bf16 v[120:123], a[184:187], v[204:207], v[120:123]\n" \
"v_exp_f32_e64 v154, v154\n" \
"v_mfma_f32_16x16x32_bf16 v[120:123], a[152:155], v[220:223], v[120:123]\n" \
"ds_read_b64_tr_b16 a[178:179], %[vvb] offset:13312\n" \
"v_mfma_f32_16x16x32_bf16 v[124:127], a[188:191], v[204:207], v[124:127]\n" \
"v_exp_f32_e64 v155, v155\n" \
"v_mfma_f32_16x16x32_bf16 v[124:127], a[156:159], v[220:223], v[124:127]\n" \
"ds_read_b64_tr_b16 a[180:181], v211 offset:9216\n" \
"s_waitcnt lgkmcnt(13)\n" \
"s_cmp_lg_u32 s60, 7\n" \
"s_cbranch_scc1 LmB_%=\n" \
"s_nop 0\n" \
"v_readlane_b32 s48, %[tlo], 11\n" \
"v_readlane_b32 s58, %[tlo], 12\n" \
"v_mfma_f32_16x16x32_bf16 v[128:131], a[64:67], a[0:3], v[160:163]\n" \
"v_exp_f32_e64 v156, v156\n" \
"v_mfma_f32_16x16x32_bf16 v[128:131], a[68:71], a[4:7], v[128:131]\n" \
"ds_read_b64_tr_b16 a[182:183], v211 offset:13312\n" \
"s_add_u32 m0, %[m0b], 0\n" \
"s_mov_b32 s49, s48\n" \
"v_mfma_f32_16x16x32_bf16 v[128:131], a[72:75], a[8:11], v[128:131]\n" \
"v_exp_f32_e64 v157, v157\n" \
"v_mfma_f32_16x16x32_bf16 v[128:131], a[76:79], a[12:15], v[128:131]\n" \
"v_cvt_pk_bf16_f32 v204, v152, v153\n" \
"v_cvt_pk_bf16_f32 v205, v154, v155\n" \
"v_mfma_f32_16x16x32_bf16 v[132:135], a[92:95], a[12:15], v[160:163]\n" \
"v_exp_f32_e64 v158, v158\n" \
"v_mfma_f32_16x16x32_bf16 v[132:135], a[88:91], a[8:11], v[132:135]\n" \
"ds_read_b64_tr_b16 a[184:185], %[vvb] offset:9728\n" \
"s_lshl_b32 s49, s49, 18\n" \
"s_or_b32 s49, s49, s58\n" \
"v_accvgpr_write_b32 a228, v225\n" \
"s_waitcnt lgkmcnt(5)\n" \
"v_mfma_f32_16x16x32_bf16 v[132:135], a[84:87], a[4:7], v[132:135]\n" \
"buffer_load_dwordx4 %[wo0], %[srdt], s49 offen offset:0 lds\n" \
"v_mfma_f32_16x16x32_bf16 v[132:135], a[80:83], a[0:3], v[132:135]\n" \
"v_exp_f32_e32 v159, v159\n" \
"v_mfma_f32_16x16x32_bf16 v[64:67], a[128:131], v[216:219], v[64:67]\n" \
"ds_read_b64_tr_b16 a[186:187], %[vvb] offset:13824\n" \
"ds_read_b64_tr_b16 a[188:189], v211 offset:9728\n" \
"v_readlane_b32 s60, %[thi], 11\n" \
"v_mfma_f32_16x16x32_bf16 v[64:67], a[160:163], v[200:203], v[64:67]\n" \
"v_exp_f32_e64 v128, v128\n" \
"v_mfma_f32_16x16x32_bf16 v[68:71], a[132:135], v[216:219], v[68:71]\n" \
"v_cvt_pk_bf16_f32 v206, v156, v157\n" \
"v_cvt_pk_bf16_f32 v207, v158, v159\n" \
"v_mfma_f32_16x16x32_bf16 v[68:71], a[164:167], v[200:203], v[68:71]\n" \
"v_exp_f32_e64 v129, v129\n" \
"v_mfma_f32_16x16x32_bf16 v[72:75], a[136:139], v[216:219], v[72:75]\n" \
"ds_read_b64_tr_b16 a[190:191], v211 offset:13824\n" \
"ds_read_b64_tr_b16 a[128:129], %[vvb] offset:16384\n" \
"v_readlane_b32 s59, %[thi], 12\n" \
"v_mfma_f32_16x16x32_bf16 v[72:75], a[168:171], v[200:203], v[72:75]\n" \
"v_exp_f32_e64 v130, v130\n" \
"v_mfma_f32_16x16x32_bf16 v[76:79], a[140:143], v[216:219], v[76:79]\n" \
"ds_read_b64_tr_b16 a[130:131], %[vvb] offset:20480\n" \
"ds_read_b64_tr_b16 a[132:133], v211 offset:16384\n" \
"v_readlane_b32 s53, %[tlo], 13\n" \
"v_mfma_f32_16x16x32_bf16 v[76:79], a[172:175], v[200:203], v[76:79]\n" \
"v_exp_f32_e64 v131, v131\n" \
"v_smfmac_f32_16x16x64_bf16 v[176:179], a[192:195], v[192:199], v228\n" \
"ds_read_b64_tr_b16 a[134:135], v211 offset:20480\n" \
"ds_read_b64_tr_b16 a[136:137], %[vvb] offset:16896\n" \
"v_readlane_b32 s50, %[thi], 13\n" \
"v_mfma_f32_16x16x32_bf16 v[136:139], a[64:67], a[16:19], v[164:167]\n" \
"v_exp_f32_e64 v132, v132\n" \
"v_mfma_f32_16x16x32_bf16 v[136:139], a[68:71], a[20:23], v[136:139]\n" \
"ds_read_b64_tr_b16 a[138:139], %[vvb] offset:20992\n" \
"ds_read_b64_tr_b16 a[140:141], v211 offset:16896\n" \
"v_readlane_b32 s46, %[tlo], 14\n" \
"v_add_u32_e32 v212, 0x3c00, %[wo0]\n" \
"v_mfma_f32_16x16x32_bf16 v[136:139], a[72:75], a[24:27], v[136:139]\n" \
"v_exp_f32_e64 v133, v133\n" \
"v_mfma_f32_16x16x32_bf16 v[136:139], a[76:79], a[28:31], v[136:139]\n" \
"v_cvt_pk_bf16_f32 v184, v128, v129\n" \
"v_cvt_pk_bf16_f32 v185, v130, v131\n" \
"v_mfma_f32_16x16x32_bf16 v[140:143], a[92:95], a[28:31], v[164:167]\n" \
"v_exp_f32_e64 v134, v134\n" \
"v_mfma_f32_16x16x32_bf16 v[140:143], a[88:91], a[24:27], v[140:143]\n" \
"ds_read_b64_tr_b16 a[142:143], v211 offset:20992\n" \
"v_readlane_b32 s47, %[thi], 14\n" \
"v_add_u32_e32 v213, 0x7800, %[wo0]\n" \
"s_waitcnt lgkmcnt(8)\n" \
"v_mfma_f32_16x16x32_bf16 v[140:143], a[84:87], a[20:23], v[140:143]\n" \
"buffer_load_dwordx4 v212, %[srdt], s49 offen offset:1024 lds\n" \
"v_mfma_f32_16x16x32_bf16 v[140:143], a[80:83], a[16:19], v[140:143]\n" \
"v_exp_f32_e32 v135, v135\n" \
"v_mfma_f32_16x16x32_bf16 v[80:83], a[144:147], v[216:219], v[80:83]\n" \
"ds_read_b64_tr_b16 a[144:145], %[vvb] offset:17408\n" \
"ds_read_b64_tr_b16 a[146:147], %[vvb] offset:21504\n" \
"ds_read_b32 v230, %[rd0]\n" \
"v_mfma_f32_16x16x32_bf16 v[80:83], a[176:179], v[200:203], v[80:83]\n" \
"v_exp_f32_e64 v136, v136\n" \
"v_mfma_f32_16x16x32_bf16 v[84:87], a[148:151], v[216:219], v[84:87]\n" \
"v_cvt_pk_bf16_f32 v186, v132, v133\n" \
"v_cvt_pk_bf16_f32 v187, v134, v135\n" \
"v_mfma_f32_16x16x32_bf16 v[84:87], a[180:183], v[200:203], v[84:87]\n" \
"v_exp_f32_e64 v137, v137\n" \
"v_mfma_f32_16x16x32_bf16 v[88:91], a[152:155], v[216:219], v[88:91]\n" \
"v_exp_f32_e64 v138, v138\n" \
"v_mfma_f32_16x16x32_bf16 v[88:91], a[184:187], v[200:203], v[88:91]\n" \
"ds_read_b64_tr_b16 a[148:149], v211 offset:17408\n" \
"v_readlane_b32 s55, %[thi], 15\n" \
"ds_read_b64_tr_b16 a[150:151], v211 offset:21504\n" \
"v_lshrrev_b32_e64 v234, 5, v224\n" \
"v_mfma_f32_16x16x32_bf16 v[92:95], a[156:159], v[216:219], v[92:95]\n" \
"v_exp_f32_e64 v139, v139\n" \
"v_mfma_f32_16x16x32_bf16 v[92:95], a[188:191], v[200:203], v[92:95]\n" \
"ds_read_b64_tr_b16 a[152:153], %[vvb] offset:17920\n" \
"v_mfma_f32_16x16x32_bf16 v[144:147], a[64:67], a[32:35], v[168:171]\n" \
"v_exp_f32_e64 v140, v140\n" \
"v_mfma_f32_16x16x32_bf16 v[144:147], a[68:71], a[36:39], v[144:147]\n" \
"ds_read_b64_tr_b16 a[154:155], %[vvb] offset:22016\n" \
"ds_read_b64_tr_b16 a[156:157], v211 offset:17920\n" \
"s_waitcnt lgkmcnt(5)\n" \
"v_add_f32_dpp v230, v230, v230 row_ror:4 row_mask:0xf bank_mask:0xf\n" \
"v_mfma_f32_16x16x32_bf16 v[144:147], a[72:75], a[40:43], v[144:147]\n" \
"v_exp_f32_e32 v141, v141\n" \
"v_mfma_f32_16x16x32_bf16 v[144:147], a[76:79], a[44:47], v[144:147]\n" \
"v_cvt_pk_bf16_f32 v188, v136, v137\n" \
"v_cvt_pk_bf16_f32 v189, v138, v139\n" \
"v_mfma_f32_16x16x32_bf16 v[148:151], a[92:95], a[44:47], v[168:171]\n" \
"v_exp_f32_e64 v142, v142\n" \
"v_mfma_f32_16x16x32_bf16 v[148:151], a[88:91], a[40:43], v[148:151]\n" \
"ds_read_b64_tr_b16 a[158:159], v211 offset:22016\n" \
"v_add_f32_dpp v230, v230, v230 row_ror:8 row_mask:0xf bank_mask:0xf\n" \
"s_waitcnt lgkmcnt(9)\n" \
"v_mfma_f32_16x16x32_bf16 v[148:151], a[84:87], a[36:39], v[148:151]\n" \
"buffer_load_dwordx4 v213, %[srdt], s49 offen offset:2048 lds\n" \
"v_mfma_f32_16x16x32_bf16 v[148:151], a[80:83], a[32:35], v[148:151]\n" \
"v_exp_f32_e32 v143, v143\n" \
"v_mfma_f32_16x16x32_bf16 v[0:3], a[160:163], v[192:195], v[0:3]\n" \
"ds_read_b128 a[96:99], %[kb0] offset:24576\n" \
"v_cvt_pk_bf16_f32 v230, v230, v230\n" \
"v_mfma_f32_16x16x32_bf16 v[0:3], a[128:131], v[184:187], v[0:3]\n" \
"v_exp_f32_e64 v144, v144\n" \
"v_mfma_f32_16x16x32_bf16 v[4:7], a[164:167], v[192:195], v[4:7]\n" \
"v_cvt_pk_bf16_f32 v190, v140, v141\n" \
"v_cvt_pk_bf16_f32 v191, v142, v143\n" \
"v_mfma_f32_16x16x32_bf16 v[4:7], a[132:135], v[184:187], v[4:7]\n" \
"v_exp_f32_e64 v145, v145\n" \
"v_mfma_f32_16x16x32_bf16 v[8:11], a[168:171], v[192:195], v[8:11]\n" \
"ds_read_b128 a[100:103], v208 offset:24576\n" \
"v_lshlrev_b32_e64 v230, 16, v230\n" \
"v_mfma_f32_16x16x32_bf16 v[8:11], a[136:139], v[184:187], v[8:11]\n" \
"v_exp_f32_e64 v146, v146\n" \
"v_mfma_f32_16x16x32_bf16 v[12:15], a[172:175], v[192:195], v[12:15]\n" \
"ds_read_b128 a[104:107], v209 offset:24576\n" \
"v_add_f32_e64 v230, s55, v230\n" \
"v_add_u32_e32 v214, 0xb400, %[wo0]\n" \
"v_mfma_f32_16x16x32_bf16 v[12:15], a[140:143], v[184:187], v[12:15]\n" \
"v_exp_f32_e64 v147, v147\n" \
"v_smfmac_f32_16x16x64_bf16 v[180:183], a[192:195], v[200:207], v228\n" \
"ds_read_b128 a[108:111], v210 offset:24576\n" \
"v_cvt_pk_bf16_f32 v230, v230, v230\n" \
"v_readlane_b32 s55, %[tlo], 15\n" \
"v_mfma_f32_16x16x32_bf16 v[152:155], a[64:67], a[48:51], v[172:175]\n" \
"v_exp_f32_e64 v148, v148\n" \
"v_mfma_f32_16x16x32_bf16 v[152:155], a[68:71], a[52:55], v[152:155]\n" \
"ds_read_b128 a[124:127], v210 offset:28672\n" \
"v_lshl_or_b32 v230, v230, 16, 1\n" \
"v_mfma_f32_16x16x32_bf16 v[152:155], a[72:75], a[56:59], v[152:155]\n" \
"v_exp_f32_e64 v149, v149\n" \
"v_mfma_f32_16x16x32_bf16 v[152:155], a[76:79], a[60:63], v[152:155]\n" \
"v_cvt_pk_bf16_f32 v216, v144, v145\n" \
"v_cvt_pk_bf16_f32 v217, v146, v147\n" \
"v_mfma_f32_16x16x32_bf16 v[156:159], a[92:95], a[60:63], v[172:175]\n" \
"v_exp_f32_e64 v150, v150\n" \
"v_mfma_f32_16x16x32_bf16 v[156:159], a[88:91], a[56:59], v[156:159]\n" \
"ds_read_b128 a[120:123], v209 offset:28672\n" \
"s_cmp_lg_u32 s52, 0\n" \
"s_cselect_b32 s55, 0x80000000, s55\n" \
"s_mov_b64 exec, 15\n" \
"buffer_store_dword v230, v234, %[srdz], s55 offen sc1\n" \
"s_mov_b64 exec, -1\n" \
"s_waitcnt lgkmcnt(6)\n" \
"v_mfma_f32_16x16x32_bf16 v[156:159], a[84:87], a[52:55], v[156:159]\n" \
"buffer_load_dwordx4 v214, %[srdt], s49 offen offset:3072 lds\n" \
".if . - adaln_body != 32768\n" \
".error \"size model\"\n" \
".endif\n" \
"Lk7_%=:\n" \
"s_cbranch_execz Lk8_%=\n" \
"s_nop 0\n" \
"v_mfma_f32_16x16x32_bf16 v[156:159], a[80:83], a[48:51], v[156:159]\n" \
"v_exp_f32_e64 v151, v151\n" \
"v_mfma_f32_16x16x32_bf16 v[16:19], a[176:179], v[192:195], v[16:19]\n" \
"ds_read_b128 a[116:119], v208 offset:28672\n" \
"v_mfma_f32_16x16x32_bf16 v[16:19], a[144:147], v[184:187], v[16:19]\n" \
"v_exp_f32_e64 v152, v152\n" \
"v_mfma_f32_16x16x32_bf16 v[20:23], a[180:183], v[192:195], v[20:23]\n" \
"v_cvt_pk_bf16_f32 v218, v148, v149\n" \
"v_cvt_pk_bf16_f32 v219, v150, v151\n" \
"v_mfma_f32_16x16x32_bf16 v[20:23], a[148:151], v[184:187], v[20:23]\n" \
"v_exp_f32_e64 v153, v153\n" \
"v_mfma_f32_16x16x32_bf16 v[24:27], a[184:187], v[192:195], v[24:27]\n" \
"v_exp_f32_e64 v154, v154\n" \
"v_mfma_f32_16x16x32_bf16 v[24:27], a[152:155], v[184:187], v[24:27]\n" \
"ds_read_b128 a[112:115], %[kb0] offset:28672\n" \
"s_mov_b32 m0, s44\n" \
"s_addk_i32 s44, 0x400\n" \
"buffer_load_dwordx4 v229, s[64:67], s45 offen offset:0 lds\n" \
"s_add_u32 s45, s45, 229376\n" \
"v_mfma_f32_16x16x32_bf16 v[28:31], a[188:191], v[192:195], v[28:31]\n" \
"v_exp_f32_e64 v155, v155\n" \
"v_mfma_f32_16x16x32_bf16 v[28:31], a[156:159], v[184:187], v[28:31]\n" \
"s_waitcnt vmcnt(6) lgkmcnt(2)\n" \
"s_barrier\n" \
"v_mfma_f32_16x16x32_bf16 v[128:131], a[96:99], a[0:3], v[160:163]\n" \
"v_exp_f32_e64 v156, v156\n" \
"s_mov_b32 m0, s44\n" \
"s_addk_i32 s44, 0x400\n" \
"buffer_load_dwordx4 v229, s[64:67], s45 offen offset:0 lds\n" \
"s_add_u32 s45, s45, 229376\n" \
"v_readlane_b32 s64, %[tlo], 8\n" \
"v_readlane_b32 s65, %[thi], 8\n" \
"v_mfma_f32_16x16x32_bf16 v[128:131], a[100:103], a[4:7], v[128:131]\n" \
"v_mbcnt_lo_u32_b32 v231, -1, 0\n" \
"v_mfma_f32_16x16x32_bf16 v[128:131], a[104:107], a[8:11], v[128:131]\n" \
"v_exp_f32_e64 v157, v157\n" \
"v_mfma_f32_16x16x32_bf16 v[128:131], a[108:111], a[12:15], v[128:131]\n" \
"v_cvt_pk_bf16_f32 v220, v152, v153\n" \
"v_cvt_pk_bf16_f32 v221, v154, v155\n" \
"v_mfma_f32_16x16x32_bf16 v[132:135], a[124:127], a[12:15], v[160:163]\n" \
"v_exp_f32_e64 v158, v158\n" \
"v_mfma_f32_16x16x32_bf16 v[132:135], a[120:123], a[8:11], v[132:135]\n" \
"v_mbcnt_hi_u32_b32 v231, -1, v231\n" \
"s_waitcnt lgkmcnt(0)\n" \
"v_mfma_f32_16x16x32_bf16 v[132:135], a[116:119], a[4:7], v[132:135]\n" \
"s_add_u32 m0, %[m0b], 65536\n" \
"s_mov_b32 s61, 0\n" \
"buffer_load_dwordx4 %[vg0], s[64:67], s61 offen offset:0 lds\n" \
"v_mfma_f32_16x16x32_bf16 v[132:135], a[112:115], a[0:3], v[132:135]\n" \
"v_exp_f32_e64 v159, v159\n" \
"v_mfma_f32_16x16x32_bf16 v[32:35], a[160:163], v[196:199], v[32:35]\n" \
"ds_read_b128 a[64:67], %[kb0] offset:32768\n" \
"v_and_b32_e64 v226, 31, v231\n" \
"v_mfma_f32_16x16x32_bf16 v[32:35], a[128:131], v[188:191], v[32:35]\n" \
"v_exp_f32_e64 v128, v128\n" \
"v_mfma_f32_16x16x32_bf16 v[36:39], a[164:167], v[196:199], v[36:39]\n" \
"v_cvt_pk_bf16_f32 v222, v156, v157\n" \
"v_cvt_pk_bf16_f32 v223, v158, v159\n" \
"v_mfma_f32_16x16x32_bf16 v[36:39], a[132:135], v[188:191], v[36:39]\n" \
"v_exp_f32_e64 v129, v129\n" \
"v_mfma_f32_16x16x32_bf16 v[40:43], a[168:171], v[196:199], v[40:43]\n" \
"ds_read_b128 a[68:71], v208 offset:32768\n" \
"v_lshlrev_b32_e64 v226, 2, v226\n" \
"v_mfma_f32_16x16x32_bf16 v[40:43], a[136:139], v[188:191], v[40:43]\n" \
"v_exp_f32_e64 v130, v130\n" \
"v_mfma_f32_16x16x32_bf16 v[44:47], a[172:175], v[196:199], v[44:47]\n" \
"ds_read_b128 a[72:75], v209 offset:32768\n" \
"v_cmp_gt_u32_e32 vcc, 1, v231\n" \
"v_mfma_f32_16x16x32_bf16 v[44:47], a[140:143], v[188:191], v[44:47]\n" \
"v_exp_f32_e32 v131, v131\n" \
"v_smfmac_f32_16x16x64_bf16 v[176:179], a[192:195], v[184:191], v228\n" \
"v_mov_b32_e32 v225, 0x80000000\n" \
"v_mfma_f32_16x16x32_bf16 v[136:139], a[96:99], a[16:19], v[164:167]\n" \
"v_exp_f32_e64 v132, v132\n" \
"v_mfma_f32_16x16x32_bf16 v[136:139], a[100:103], a[20:23], v[136:139]\n" \
"ds_read_b128 a[76:79], v210 offset:32768\n" \
"v_mfma_f32_16x16x32_bf16 v[136:139], a[104:107], a[24:27], v[136:139]\n" \
"v_exp_f32_e64 v133, v133\n" \
"v_mfma_f32_16x16x32_bf16 v[136:139], a[108:111], a[28:31], v[136:139]\n" \
"v_cvt_pk_bf16_f32 v192, v128, v129\n" \
"v_cvt_pk_bf16_f32 v193, v130, v131\n" \
"v_mfma_f32_16x16x32_bf16 v[140:143], a[124:127], a[28:31], v[164:167]\n" \
"v_exp_f32_e64 v134, v134\n" \
"v_mfma_f32_16x16x32_bf16 v[140:143], a[120:123], a[24:27], v[140:143]\n" \
"ds_read_b128 a[92:95], v210 offset:36864\n" \
"v_cndmask_b32_e32 v225, v225, v224, vcc\n" \
"v_mfma_f32_16x16x32_bf16 v[140:143], a[116:119], a[20:23], v[140:143]\n" \
"s_add_u32 m0, %[m0b], 66560\n" \
"s_mov_b32 s61, 229376\n" \
"buffer_load_dwordx4 %[vg0], s[64:67], s61 offen offset:0 lds\n" \
"v_mfma_f32_16x16x32_bf16 v[140:143], a[112:115], a[16:19], v[140:143]\n" \
"v_exp_f32_e32 v135, v135\n" \
"v_mfma_f32_16x16x32_bf16 v[48:51], a[176:179], v[196:199], v[48:51]\n" \
"ds_read_b128 a[88:91], v209 offset:36864\n" \
"v_mfma_f32_16x16x32_bf16 v[48:51], a[144:147], v[188:191], v[48:51]\n" \
"v_exp_f32_e64 v136, v136\n" \
"v_mfma_f32_16x16x32_bf16 v[52:55], a[180:183], v[196:199], v[52:55]\n" \
"v_cvt_pk_bf16_f32 v194, v132, v133\n" \
"v_cvt_pk_bf16_f32 v195, v134, v135\n" \
"v_mfma_f32_16x16x32_bf16 v[52:55], a[148:151], v[188:191], v[52:55]\n" \
"v_exp_f32_e64 v137, v137\n" \
"v_mfma_f32_16x16x32_bf16 v[56:59], a[184:187], v[196:199], v[56:59]\n" \
"v_exp_f32_e64 v138, v138\n" \
"v_mfma_f32_16x16x32_bf16 v[56:59], a[152:155], v[188:191], v[56:59]\n" \
"ds_read_b128 a[84:87], v208 offset:36864\n" \
"v_mfma_f32_16x16x32_bf16 v[60:63], a[188:191], v[196:199], v[60:63]\n" \
"v_exp_f32_e64 v139, v139\n" \
"v_mfma_f32_16x16x32_bf16 v[60:63], a[156:159], v[188:191], v[60:63]\n" \
"v_mfma_f32_16x16x32_bf16 v[144:147], a[96:99], a[32:35], v[168:171]\n" \
"v_exp_f32_e64 v140, v140\n" \
"v_mfma_f32_16x16x32_bf16 v[144:147], a[100:103], a[36:39], v[144:147]\n" \
"ds_read_b128 a[80:83], %[kb0] offset:36864\n" \
"s_sub_u32 s61, s48, 0\n" \
"s_cmp_lt_u32 s61, 5\n" \
"s_cselect_b64 vcc, -1, 0\n" \
"v_mfma_f32_16x16x32_bf16 v[144:147], a[104:107], a[40:43], v[144:147]\n" \
"v_exp_f32_e32 v141, v141\n" \
"v_mfma_f32_16x16x32_bf16 v[144:147], a[108:111], a[44:47], v[144:147]\n" \
"v_cvt_pk_bf16_f32 v196, v136, v137\n" \
"v_cvt_pk_bf16_f32 v197, v138, v139\n" \
"v_mfma_f32_16x16x32_bf16 v[148:151], a[124:127], a[44:47], v[168:171]\n" \
"v_exp_f32_e64 v142, v142\n" \
"v_mfma_f32_16x16x32_bf16 v[148:151], a[120:123], a[40:43], v[148:151]\n" \
"v_readlane_b32 s55, %[tlo], 16\n" \
"v_mfma_f32_16x16x32_bf16 v[148:151], a[116:119], a[36:39], v[148:151]\n" \
"s_add_u32 m0, %[m0b], 67584\n" \
"s_mov_b32 s61, 458752\n" \
"buffer_load_dwordx4 %[vg0], s[64:67], s61 offen offset:0 lds\n" \
"v_mfma_f32_16x16x32_bf16 v[148:151], a[112:115], a[32:35], v[148:151]\n" \
"v_exp_f32_e64 v143, v143\n" \
"v_mfma_f32_16x16x32_bf16 v[96:99], a[160:163], v[204:207], v[96:99]\n" \
"ds_read_b64_tr_b16 a[160:161], %[vvb] offset:24576\n" \
"v_and_b32_e64 v232, 31, v231\n" \
"v_mfma_f32_16x16x32_bf16 v[96:99], a[128:131], v[220:223], v[96:99]\n" \
"v_exp_f32_e64 v144, v144\n" \
"v_mfma_f32_16x16x32_bf16 v[100:103], a[164:167], v[204:207], v[100:103]\n" \
"v_cvt_pk_bf16_f32 v198, v140, v141\n" \
"v_cvt_pk_bf16_f32 v199, v142, v143\n" \
"v_mfma_f32_16x16x32_bf16 v[100:103], a[132:135], v[220:223], v[100:103]\n" \
"v_exp_f32_e64 v145, v145\n" \
"v_mfma_f32_16x16x32_bf16 v[104:107], a[168:171], v[204:207], v[104:107]\n" \
"ds_read_b64_tr_b16 a[162:163], %[vvb] offset:28672\n" \
"ds_read_b64_tr_b16 a[164:165], v211 offset:24576\n" \
"v_lshlrev_b32_e64 v232, 9, v232\n" \
"v_mfma_f32_16x16x32_bf16 v[104:107], a[136:139], v[220:223], v[104:107]\n" \
"v_exp_f32_e64 v146, v146\n" \
"v_mfma_f32_16x16x32_bf16 v[108:111], a[172:175], v[204:207], v[108:111]\n" \
"ds_read_b64_tr_b16 a[166:167], v211 offset:28672\n" \
"v_and_b32_e64 v233, 32, v231\n" \
"v_mfma_f32_16x16x32_bf16 v[108:111], a[140:143], v[220:223], v[108:111]\n" \
"v_exp_f32_e64 v147, v147\n" \
"v_smfmac_f32_16x16x64_bf16 v[180:183], a[192:195], v[216:223], v228\n" \
"ds_read_b64_tr_b16 a[168:169], %[vvb] offset:25088\n" \
"v_mfma_f32_16x16x32_bf16 v[152:155], a[96:99], a[48:51], v[172:175]\n" \
"v_exp_f32_e64 v148, v148\n" \
"v_mfma_f32_16x16x32_bf16 v[152:155], a[100:103], a[52:55], v[152:155]\n" \
"ds_read_b64_tr_b16 a[170:171], %[vvb] offset:29184\n" \
"v_lshl_add_u32 v232, v233, 2, v232\n" \
"v_mfma_f32_16x16x32_bf16 v[152:155], a[104:107], a[56:59], v[152:155]\n" \
"v_exp_f32_e64 v149, v149\n" \
"v_mfma_f32_16x16x32_bf16 v[152:155], a[108:111], a[60:63], v[152:155]\n" \
"v_cvt_pk_bf16_f32 v200, v144, v145\n" \
"v_cvt_pk_bf16_f32 v201, v146, v147\n" \
"v_mfma_f32_16x16x32_bf16 v[156:159], a[124:127], a[60:63], v[172:175]\n" \
"v_exp_f32_e64 v150, v150\n" \
"v_mfma_f32_16x16x32_bf16 v[156:159], a[120:123], a[56:59], v[156:159]\n" \
"ds_read_b64_tr_b16 a[172:173], v211 offset:25088\n" \
"v_mov_b32_e64 v230, 1\n" \
"v_mfma_f32_16x16x32_bf16 v[156:159], a[116:119], a[52:55], v[156:159]\n" \
"s_add_u32 m0, %[m0b], 68608\n" \
"s_mov_b32 s61, 688128\n" \
"buffer_load_dwordx4 %[vg0], s[64:67], s61 offen offset:0 lds\n" \
"v_mfma_f32_16x16x32_bf16 v[156:159], a[112:115], a[48:51], v[156:159]\n" \
"v_exp_f32_e64 v151, v151\n" \
"v_mfma_f32_16x16x32_bf16 v[112:115], a[176:179], v[204:207], v[112:115]\n" \
"ds_read_b64_tr_b16 a[174:175], v211 offset:29184\n" \
"ds_read_b64_tr_b16 a[176:177], %[vvb] offset:25600\n" \
"v_mfma_f32_16x16x32_bf16 v[112:115], a[144:147], v[220:223], v[112:115]\n" \
"v_exp_f32_e64 v152, v152\n" \
"v_mfma_f32_16x16x32_bf16 v[116:119], a[180:183], v[204:207], v[116:119]\n" \
"v_cvt_pk_bf16_f32 v202, v148, v149\n" \
"v_cvt_pk_bf16_f32 v203, v150, v151\n" \
"v_mfma_f32_16x16x32_bf16 v[116:119], a[148:151], v[220:223], v[116:119]\n" \
"v_exp_f32_e64 v153, v153\n" \
"v_mfma_f32_16x16x32_bf16 v[120:123], a[184:187], v[204:207], v[120:123]\n" \
"v_exp_f32_e64 v154, v154\n" \
"v_mfma_f32_16x16x32_bf16 v[120:123], a[152:155], v[220:223], v[120:123]\n" \
"ds_read_b64_tr_b16 a[178:179], %[vvb] offset:29696\n" \
"buffer_store_dword v230, v232, %[srdz], s55 offen sc1\n" \
"v_mfma_f32_16x16x32_bf16 v[124:127], a[188:191], v[204:207], v[124:127]\n" \
"v_exp_f32_e64 v155, v155\n" \
"v_mfma_f32_16x16x32_bf16 v[124:127], a[156:159], v[220:223], v[124:127]\n" \
"ds_read_b64_tr_b16 a[180:181], v211 offset:25600\n" \
"s_waitcnt lgkmcnt(13)\n" \
"v_mfma_f32_16x16x32_bf16 v[128:131], a[64:67], a[0:3], v[160:163]\n" \
"v_exp_f32_e32 v156, v156\n" \
"v_mfma_f32_16x16x32_bf16 v[128:131], a[68:71], a[4:7], v[128:131]\n" \
"ds_read_b64_tr_b16 a[182:183], v211 offset:29696\n" \
"s_add_u32 m0, %[m0b], 16384\n" \
"s_mov_b32 s49, s48\n" \
"v_mfma_f32_16x16x32_bf16 v[128:131], a[72:75], a[8:11], v[128:131]\n" \
"v_exp_f32_e32 v157, v157\n" \
"v_mfma_f32_16x16x32_bf16 v[128:131], a[76:79], a[12:15], v[128:131]\n" \
"v_cvt_pk_bf16_f32 v204, v152, v153\n" \
"v_cvt_pk_bf16_f32 v205, v154, v155\n" \
"v_mfma_f32_16x16x32_bf16 v[132:135], a[92:95], a[12:15], v[160:163]\n" \
"v_exp_f32_e64 v158, v158\n" \
"v_mfma_f32_16x16x32_bf16 v[132:135], a[88:91], a[8:11], v[132:135]\n" \
"ds_read_b64_tr_b16 a[184:185], %[vvb] offset:26112\n" \
"s_lshl_b32 s49, s49, 18\n" \
"s_or_b32 s49, s49, s59\n" \
"s_waitcnt lgkmcnt(5)\n" \
"v_mfma_f32_16x16x32_bf16 v[132:135], a[84:87], a[4:7], v[132:135]\n" \
"buffer_load_dwordx4 %[wo0], %[srdt], s49 offen offset:0 lds\n" \
"v_mfma_f32_16x16x32_bf16 v[132:135], a[80:83], a[0:3], v[132:135]\n" \
"v_exp_f32_e32 v159, v159\n" \
"v_mfma_f32_16x16x32_bf16 v[64:67], a[128:131], v[216:219], v[64:67]\n" \
"ds_read_b64_tr_b16 a[186:187], %[vvb] offset:30208\n" \
"ds_read_b64_tr_b16 a[188:189], v211 offset:26112\n" \
"v_xor_b32_e32 v232, 0x80, %[vx0]\n" \
"v_mfma_f32_16x16x32_bf16 v[64:67], a[160:163], v[200:203], v[64:67]\n" \
"v_exp_f32_e64 v128, v128\n" \
"v_mfma_f32_16x16x32_bf16 v[68:71], a[132:135], v[216:219], v[68:71]\n" \
"v_cvt_pk_bf16_f32 v206, v156, v157\n" \
"v_cvt_pk_bf16_f32 v207, v158, v159\n" \
"v_mfma_f32_16x16x32_bf16 v[68:71], a[164:167], v[200:203], v[68:71]\n" \
"v_exp_f32_e64 v129, v129\n" \
"v_mfma_f32_16x16x32_bf16 v[72:75], a[136:139], v[216:219], v[72:75]\n" \
"ds_read_b64_tr_b16 a[190:191], v211 offset:30208\n" \
"ds_read_b64_tr_b16 a[128:129], %[vvb] offset:32768\n" \
"v_mfma_f32_16x16x32_bf16 v[72:75], a[168:171], v[200:203], v[72:75]\n" \
"v_exp_f32_e64 v130, v130\n" \
"v_mfma_f32_16x16x32_bf16 v[76:79], a[140:143], v[216:219], v[76:79]\n" \
"ds_read_b64_tr_b16 a[130:131], %[vvb] offset:36864\n" \
"ds_read_b64_tr_b16 a[132:133], v211 offset:32768\n" \
"v_mfma_f32_16x16x32_bf16 v[76:79], a[172:175], v[200:203], v[76:79]\n" \
"v_exp_f32_e64 v131, v131\n" \
"v_smfmac_f32_16x16x64_bf16 v[176:179], a[192:195], v[192:199], v228\n" \
"ds_read_b64_tr_b16 a[134:135], v211 offset:36864\n" \
"ds_read_b64_tr_b16 a[136:137], %[vvb] offset:33280\n" \
"global_load_dwordx2 a[240:241], %[vx0], %[px] offset:0\n" \
"v_mfma_f32_16x16x32_bf16 v[136:139], a[64:67], a[16:19], v[164:167]\n" \
"v_exp_f32_e64 v132, v132\n" \
"v_mfma_f32_16x16x32_bf16 v[136:139], a[68:71], a[20:23], v[136:139]\n" \
"ds_read_b64_tr_b16 a[138:139], %[vvb] offset:37376\n" \
"ds_read_b64_tr_b16 a[140:141], v211 offset:33280\n" \
"v_mfma_f32_16x16x32_bf16 v[136:139], a[72:75], a[24:27], v[136:139]\n" \
"v_exp_f32_e64 v133, v133\n" \
"v_mfma_f32_16x16x32_bf16 v[136:139], a[76:79], a[28:31], v[136:139]\n" \
"v_cvt_pk_bf16_f32 v184, v128, v129\n" \
"v_cvt_pk_bf16_f32 v185, v130, v131\n" \
"v_mfma_f32_16x16x32_bf16 v[140:143], a[92:95], a[28:31], v[164:167]\n" \
"v_exp_f32_e64 v134, v134\n" \
"v_mfma_f32_16x16x32_bf16 v[140:143], a[88:91], a[24:27], v[140:143]\n" \
"ds_read_b64_tr_b16 a[142:143], v211 offset:37376\n" \
"s_waitcnt lgkmcnt(8)\n" \
"v_mfma_f32_16x16x32_bf16 v[140:143], a[84:87], a[20:23], v[140:143]\n" \
"buffer_load_dwordx4 v212, %[srdt], s49 offen offset:1024 lds\n" \
"v_mfma_f32_16x16x32_bf16 v[140:143], a[80:83], a[16:19], v[140:143]\n" \
"v_exp_f32_e32 v135, v135\n" \
"v_mfma_f32_16x16x32_bf16 v[80:83], a[144:147], v[216:219], v[80:83]\n" \
"ds_read_b64_tr_b16 a[144:145], %[vvb] offset:33792\n" \
"ds_read_b64_tr_b16 a[146:147], %[vvb] offset:37888\n" \
"v_mfma_f32_16x16x32_bf16 v[80:83], a[176:179], v[200:203], v[80:83]\n" \
"v_exp_f32_e64 v136, v136\n" \
"v_mfma_f32_16x16x32_bf16 v[84:87], a[148:151], v[216:219], v[84:87]\n" \
"v_cvt_pk_bf16_f32 v186, v132, v133\n" \
"v_cvt_pk_bf16_f32 v187, v134, v135\n" \
"v_mfma_f32_16x16x32_bf16 v[84:87], a[180:183], v[200:203], v[84:87]\n" \
"v_exp_f32_e64 v137, v137\n" \
"v_mfma_f32_16x16x32_bf16 v[88:91], a[152:155], v[216:219], v[88:91]\n" \
"v_exp_f32_e64 v138, v138\n" \
"v_mfma_f32_16x16x32_bf16 v[88:91], a[184:187], v[200:203], v[88:91]\n" \
"ds_read_b64_tr_b16 a[148:149], v211 offset:33792\n" \
"ds_read_b64_tr_b16 a[150:151], v211 offset:37888\n" \
"v_mfma_f32_16x16x32_bf16 v[92:95], a[156:159], v[216:219], v[92:95]\n" \
"v_exp_f32_e64 v139, v139\n" \
"v_mfma_f32_16x16x32_bf16 v[92:95], a[188:191], v[200:203], v[92:95]\n" \
"ds_read_b64_tr_b16 a[152:153], %[vvb] offset:34304\n" \
"global_load_dwordx2 a[242:243], %[vx0], %[px] offset:1024\n" \
"v_mfma_f32_16x16x32_bf16 v[144:147], a[64:67], a[32:35], v[168:171]\n" \
"v_exp_f32_e64 v140, v140\n" \
"v_mfma_f32_16x16x32_bf16 v[144:147], a[68:71], a[36:39], v[144:147]\n" \
"ds_read_b64_tr_b16 a[154:155], %[vvb] offset:38400\n" \
"ds_read_b64_tr_b16 a[156:157], v211 offset:34304\n" \
"v_mfma_f32_16x16x32_bf16 v[144:147], a[72:75], a[40:43], v[144:147]\n" \
"v_exp_f32_e64 v141, v141\n" \
"v_mfma_f32_16x16x32_bf16 v[144:147], a[76:79], a[44:47], v[144:147]\n" \
"v_cvt_pk_bf16_f32 v188, v136, v137\n" \
"v_cvt_pk_bf16_f32 v189, v138, v139\n" \
"v_mfma_f32_16x16x32_bf16 v[148:151], a[92:95], a[44:47], v[168:171]\n" \
"v_exp_f32_e64 v142, v142\n" \
"v_mfma_f32_16x16x32_bf16 v[148:151], a[88:91], a[40:43], v[148:151]\n" \
"ds_read_b64_tr_b16 a[158:159], v211 offset:38400\n" \
"s_waitcnt lgkmcnt(8)\n" \
"v_mfma_f32_16x16x32_bf16 v[148:151], a[84:87], a[36:39], v[148:151]\n" \
"buffer_load_dwordx4 v213, %[srdt], s49 offen offset:2048 lds\n" \
"v_mfma_f32_16x16x32_bf16 v[148:151], a[80:83], a[32:35], v[148:151]\n" \
"v_exp_f32_e32 v143, v143\n" \
"v_mfma_f32_16x16x32_bf16 v[0:3], a[160:163], v[192:195], v[0:3]\n" \
"ds_read_b128 a[96:99], %[kb0] offset:40960\n" \
"v_mfma_f32_16x16x32_bf16 v[0:3], a[128:131], v[184:187], v[0:3]\n" \
"v_exp_f32_e64 v144, v144\n" \
"v_mfma_f32_16x16x32_bf16 v[4:7], a[164:167], v[192:195], v[4:7]\n" \
"v_cvt_pk_bf16_f32 v190, v140, v141\n" \
"v_cvt_pk_bf16_f32 v191, v142, v143\n" \
"v_mfma_f32_16x16x32_bf16 v[4:7], a[132:135], v[184:187], v[4:7]\n" \
"v_exp_f32_e64 v145, v145\n" \
"v_mfma_f32_16x16x32_bf16 v[8:11], a[168:171], v[192:195], v[8:11]\n" \
"ds_read_b128 a[100:103], v208 offset:40960\n" \
"v_mfma_f32_16x16x32_bf16 v[8:11], a[136:139], v[184:187], v[8:11]\n" \
"v_exp_f32_e64 v146, v146\n" \
"v_mfma_f32_16x16x32_bf16 v[12:15], a[172:175], v[192:195], v[12:15]\n" \
"ds_read_b128 a[104:107], v209 offset:40960\n" \
"v_mfma_f32_16x16x32_bf16 v[12:15], a[140:143], v[184:187], v[12:15]\n" \
"v_exp_f32_e64 v147, v147\n" \
"v_smfmac_f32_16x16x64_bf16 v[180:183], a[192:195], v[200:207], v228\n" \
"ds_read_b128 a[108:111], v210 offset:40960\n" \
"global_load_dwordx2 a[248:249], v232, %[px] offset:0\n" \
"v_mfma_f32_16x16x32_bf16 v[152:155], a[64:67], a[48:51], v[172:175]\n" \
"v_exp_f32_e64 v148, v148\n" \
"v_mfma_f32_16x16x32_bf16 v[152:155], a[68:71], a[52:55], v[152:155]\n" \
"ds_read_b128 a[124:127], v210 offset:45056\n" \
"v_mfma_f32_16x16x32_bf16 v[152:155], a[72:75], a[56:59], v[152:155]\n" \
"v_exp_f32_e64 v149, v149\n" \
"v_mfma_f32_16x16x32_bf16 v[152:155], a[76:79], a[60:63], v[152:155]\n" \
"v_cvt_pk_bf16_f32 v216, v144, v145\n" \
"v_cvt_pk_bf16_f32 v217, v146, v147\n" \
"v_mfma_f32_16x16x32_bf16 v[156:159], a[92:95], a[60:63], v[172:175]\n" \
"v_exp_f32_e64 v150, v150\n" \
"v_mfma_f32_16x16x32_bf16 v[156:159], a[88:91], a[56:59], v[156:159]\n" \
"ds_read_b128 a[120:123], v209 offset:45056\n" \
"s_waitcnt lgkmcnt(6)\n" \
"v_mfma_f32_16x16x32_bf16 v[156:159], a[84:87], a[52:55], v[156:159]\n" \
"buffer_load_dwordx4 v214, %[srdt], s49 offen offset:3072 lds\n" \
"v_mfma_f32_16x16x32_bf16 v[156:159], a[80:83], a[48:51], v[156:159]\n" \
"v_exp_f32_e32 v151, v151\n" \
"v_mfma_f32_16x16x32_bf16 v[16:19], a[176:179], v[192:195], v[16:19]\n" \
"ds_read_b128 a[116:119], v208 offset:45056\n" \
"v_mfma_f32_16x16x32_bf16 v[16:19], a[144:147], v[184:187], v[16:19]\n" \
"v_exp_f32_e64 v152, v152\n" \
"v_mfma_f32_16x16x32_bf16 v[20:23], a[180:183], v[192:195], v[20:23]\n" \
"v_cvt_pk_bf16_f32 v218, v148, v149\n" \
"v_cvt_pk_bf16_f32 v219, v150, v151\n" \
"v_mfma_f32_16x16x32_bf16 v[20:23], a[148:151], v[184:187], v[20:23]\n" \
"v_exp_f32_e64 v153, v153\n" \
"v_mfma_f32_16x16x32_bf16 v[24:27], a[184:187], v[192:195], v[24:27]\n" \
"v_exp_f32_e64 v154, v154\n" \
"v_mfma_f32_16x16x32_bf16 v[24:27], a[152:155], v[184:187], v[24:27]\n" \
"ds_read_b128 a[112:115], %[kb0] offset:45056\n" \
"global_load_dwordx2 a[250:251], v232, %[px] offset:1024\n" \
"v_mfma_f32_16x16x32_bf16 v[28:31], a[188:191], v[192:195], v[28:31]\n" \
"v_exp_f32_e64 v155, v155\n" \
"v_mfma_f32_16x16x32_bf16 v[28:31], a[156:159], v[184:187], v[28:31]\n" \
"s_waitcnt vmcnt(63) lgkmcnt(2)\n" \
"s_barrier\n" \
"v_mfma_f32_16x16x32_bf16 v[128:131], a[96:99], a[0:3], v[160:163]\n" \
"v_exp_f32_e64 v156, v156\n" \
"v_mfma_f32_16x16x32_bf16 v[128:131], a[100:103], a[4:7], v[128:131]\n" \
"v_lshrrev_b32_e64 v227, 5, v231\n" \
"v_mfma_f32_16x16x32_bf16 v[128:131], a[104:107], a[8:11], v[128:131]\n" \
"v_exp_f32_e64 v157, v157\n" \
"v_mfma_f32_16x16x32_bf16 v[128:131], a[108:111], a[12:15], v[128:131]\n" \
"v_cvt_pk_bf16_f32 v220, v152, v153\n" \
"v_cvt_pk_bf16_f32 v221, v154, v155\n" \
"v_mfma_f32_16x16x32_bf16 v[132:135], a[124:127], a[12:15], v[160:163]\n" \
"v_exp_f32_e64 v158, v158\n" \
"v_mfma_f32_16x16x32_bf16 v[132:135], a[120:123], a[8:11], v[132:135]\n" \
"v_lshlrev_b32_e32 v227, 10, v227\n" \
"s_waitcnt lgkmcnt(0)\n" \
"v_mfma_f32_16x16x32_bf16 v[132:135], a[116:119], a[4:7], v[132:135]\n" \
"s_add_u32 m0, %[m0b], 81920\n" \
"s_mov_b32 s61, 917504\n" \
"buffer_load_dwordx4 %[vg0], s[64:67], s61 offen offset:0 lds\n" \
"v_mfma_f32_16x16x32_bf16 v[132:135], a[112:115], a[0:3], v[132:135]\n" \
"v_exp_f32_e64 v159, v159\n" \
"v_mfma_f32_16x16x32_bf16 v[32:35], a[160:163], v[196:199], v[32:35]\n" \
"ds_read_b128 a[64:67], %[kb0] offset:49152\n" \
"v_add_u32_e32 v227, 0x10000, v227\n" \
"v_mfma_f32_16x16x32_bf16 v[32:35], a[128:131], v[188:191], v[32:35]\n" \
"v_exp_f32_e64 v128, v128\n" \
"v_mfma_f32_16x16x32_bf16 v[36:39], a[164:167], v[196:199], v[36:39]\n" \
"v_cvt_pk_bf16_f32 v222, v156, v157\n" \
"v_cvt_pk_bf16_f32 v223, v158, v159\n" \
"v_mfma_f32_16x16x32_bf16 v[36:39], a[132:135], v[188:191], v[36:39]\n" \
"v_exp_f32_e64 v129, v129\n" \
"v_mfma_f32_16x16x32_bf16 v[40:43], a[168:171], v[196:199], v[40:43]\n" \
"ds_read_b128 a[68:71], v208 offset:49152\n" \
"v_sub_u32_e64 v215, %[vvb], v227\n" \
"v_mfma_f32_16x16x32_bf16 v[40:43], a[136:139], v[188:191], v[40:43]\n" \
"v_exp_f32_e64 v130, v130\n" \
"v_mfma_f32_16x16x32_bf16 v[44:47], a[172:175], v[196:199], v[44:47]\n" \
"ds_read_b128 a[72:75], v209 offset:49152\n" \
"v_xor_b32_e64 v227, 32, v215\n" \
"v_mfma_f32_16x16x32_bf16 v[44:47], a[140:143], v[188:191], v[44:47]\n" \
"v_exp_f32_e64 v131, v131\n" \
"v_smfmac_f32_16x16x64_bf16 v[176:179], a[192:195], v[184:191], v228\n" \
"global_load_dwordx2 a[244:245], %[vx0], %[px] offset:64\n" \
"v_mfma_f32_16x16x32_bf16 v[136:139], a[96:99], a[16:19], v[164:167]\n" \
"v_exp_f32_e64 v132, v132\n" \
"v_mfma_f32_16x16x32_bf16 v[136:139], a[100:103], a[20:23], v[136:139]\n" \
"ds_read_b128 a[76:79], v210 offset:49152\n" \
"v_mfma_f32_16x16x32_bf16 v[136:139], a[104:107], a[24:27], v[136:139]\n" \
"v_exp_f32_e64 v133, v133\n" \
"v_mfma_f32_16x16x32_bf16 v[136:139], a[108:111], a[28:31], v[136:139]\n" \
"v_cvt_pk_bf16_f32 v192, v128, v129\n" \
"v_cvt_pk_bf16_f32 v193, v130, v131\n" \
"v_mfma_f32_16x16x32_bf16 v[140:143], a[124:127], a[28:31], v[164:167]\n" \
"v_exp_f32_e64 v134, v134\n" \
"v_mfma_f32_16x16x32_bf16 v[140:143], a[120:123], a[24:27], v[140:143]\n" \
"ds_read_b128 a[92:95], v210 offset:53248\n" \
"v_mfma_f32_16x16x32_bf16 v[140:143], a[116:119], a[20:23], v[140:143]\n" \
"s_add_u32 m0, %[m0b], 82944\n" \
"s_mov_b32 s61, 1146880\n" \
"buffer_load_dwordx4 %[vg0], s[64:67], s61 offen offset:0 lds\n" \
"v_mfma_f32_16x16x32_bf16 v[140:143], a[112:115], a[16:19], v[140:143]\n" \
"v_exp_f32_e64 v135, v135\n" \
"v_mfma_f32_16x16x32_bf16 v[48:51], a[176:179], v[196:199], v[48:51]\n" \
"ds_read_b128 a[88:91], v209 offset:53248\n" \
"v_mfma_f32_16x16x32_bf16 v[48:51], a[144:147], v[188:191], v[48:51]\n" \
"v_exp_f32_e64 v136, v136\n" \
"v_mfma_f32_16x16x32_bf16 v[52:55], a[180:183], v[196:199], v[52:55]\n" \
"v_cvt_pk_bf16_f32 v194, v132, v133\n" \
"v_cvt_pk_bf16_f32 v195, v134, v135\n" \
"v_mfma_f32_16x16x32_bf16 v[52:55], a[148:151], v[188:191], v[52:55]\n" \
"v_exp_f32_e64 v137, v137\n" \
"v_mfma_f32_16x16x32_bf16 v[56:59], a[184:187], v[196:199], v[56:59]\n" \
"v_exp_f32_e64 v138, v138\n" \
"v_mfma_f32_16x16x32_bf16 v[56:59], a[152:155], v[188:191], v[56:59]\n" \
"ds_read_b128 a[84:87], v208 offset:53248\n" \
"v_mfma_f32_16x16x32_bf16 v[60:63], a[188:191], v[196:199], v[60:63]\n" \
"v_exp_f32_e64 v139, v139\n" \
"v_mfma_f32_16x16x32_bf16 v[60:63], a[156:159], v[188:191], v[60:63]\n" \
"global_load_dwordx2 a[246:247], %[vx0], %[px] offset:1088\n" \
"v_mfma_f32_16x16x32_bf16 v[144:147], a[96:99], a[32:35], v[168:171]\n" \
"v_exp_f32_e64 v140, v140\n" \
"v_mfma_f32_16x16x32_bf16 v[144:147], a[100:103], a[36:39], v[144:147]\n" \
"ds_read_b128 a[80:83], %[kb0] offset:53248\n" \
"v_mfma_f32_16x16x32_bf16 v[144:147], a[104:107], a[40:43], v[144:147]\n" \
"v_exp_f32_e64 v141, v141\n" \
"v_mfma_f32_16x16x32_bf16 v[144:147], a[108:111], a[44:47], v[144:147]\n" \
"v_cvt_pk_bf16_f32 v196, v136, v137\n" \
"v_cvt_pk_bf16_f32 v197, v138, v139\n" \
"v_mfma_f32_16x16x32_bf16 v[148:151], a[124:127], a[44:47], v[168:171]\n" \
"v_exp_f32_e64 v142, v142\n" \
"v_mfma_f32_16x16x32_bf16 v[148:151], a[120:123], a[40:43], v[148:151]\n" \
"v_mfma_f32_16x16x32_bf16 v[148:151], a[116:119], a[36:39], v[148:151]\n" \
"s_add_u32 m0, %[m0b], 83968\n" \
"s_mov_b32 s61, 1376256\n" \
"buffer_load_dwordx4 %[vg0], s[64:67], s61 offen offset:0 lds\n" \
"v_mfma_f32_16x16x32_bf16 v[148:151], a[112:115], a[32:35], v[148:151]\n" \
"v_exp_f32_e64 v143, v143\n" \
"v_mfma_f32_16x16x32_bf16 v[96:99], a[160:163], v[204:207], v[96:99]\n" \
"ds_read_b64_tr_b16 a[160:161], %[vvb] offset:40960\n" \
"v_mfma_f32_16x16x32_bf16 v[96:99], a[128:131], v[220:223], v[96:99]\n" \
"v_exp_f32_e64 v144, v144\n" \
"v_mfma_f32_16x16x32_bf16 v[100:103], a[164:167], v[204:207], v[100:103]\n" \
"v_cvt_pk_bf16_f32 v198, v140, v141\n" \
"v_cvt_pk_bf16_f32 v199, v142, v143\n" \
"v_mfma_f32_16x16x32_bf16 v[100:103], a[132:135], v[220:223], v[100:103]\n" \
"v_exp_f32_e64 v145, v145\n" \
"v_mfma_f32_16x16x32_bf16 v[104:107], a[168:171], v[204:207], v[104:107]\n" \
"ds_read_b64_tr_b16 a[162:163], %[vvb] offset:45056\n" \
"ds_read_b64_tr_b16 a[164:165], v211 offset:40960\n" \
"v_mfma_f32_16x16x32_bf16 v[104:107], a[136:139], v[220:223], v[104:107]\n" \
"v_exp_f32_e64 v146, v146\n" \
"v_mfma_f32_16x16x32_bf16 v[108:111], a[172:175], v[204:207], v[108:111]\n" \
"ds_read_b64_tr_b16 a[166:167], v211 offset:45056\n" \
"v_mfma_f32_16x16x32_bf16 v[108:111], a[140:143], v[220:223], v[108:111]\n" \
"v_exp_f32_e64 v147, v147\n" \
"v_smfmac_f32_16x16x64_bf16 v[180:183], a[192:195], v[216:223], v228\n" \
"ds_read_b64_tr_b16 a[168:169], %[vvb] offset:41472\n" \
"global_load_dwordx2 a[252:253], v232, %[px] offset:64\n" \
"v_mfma_f32_16x16x32_bf16 v[152:155], a[96:99], a[48:51], v[172:175]\n" \
"v_exp_f32_e64 v148, v148\n" \
"v_mfma_f32_16x16x32_bf16 v[152:155], a[100:103], a[52:55], v[152:155]\n" \
"ds_read_b64_tr_b16 a[170:171], %[vvb] offset:45568\n" \
"v_mfma_f32_16x16x32_bf16 v[152:155], a[104:107], a[56:59], v[152:155]\n" \
"v_exp_f32_e64 v149, v149\n" \
"v_mfma_f32_16x16x32_bf16 v[152:155], a[108:111], a[60:63], v[152:155]\n" \
"v_cvt_pk_bf16_f32 v200, v144, v145\n" \
"v_cvt_pk_bf16_f32 v201, v146, v147\n" \
"v_mfma_f32_16x16x32_bf16 v[156:159], a[124:127], a[60:63], v[172:175]\n" \
"v_exp_f32_e64 v150, v150\n" \
"v_mfma_f32_16x16x32_bf16 v[156:159], a[120:123], a[56:59], v[156:159]\n" \
"ds_read_b64_tr_b16 a[172:173], v211 offset:41472\n" \
"v_mfma_f32_16x16x32_bf16 v[156:159], a[116:119], a[52:55], v[156:159]\n" \
"s_add_u32 m0, %[m0b], 84992\n" \
"s_mov_b32 s61, 1605632\n" \
"buffer_load_dwordx4 %[vg0], s[64:67], s61 offen offset:0 lds\n" \
"v_mfma_f32_16x16x32_bf16 v[156:159], a[112:115], a[48:51], v[156:159]\n" \
"v_exp_f32_e64 v151, v151\n" \
"v_mfma_f32_16x16x32_bf16 v[112:115], a[176:179], v[204:207], v[112:115]\n" \
"ds_read_b64_tr_b16 a[174:175], v211 offset:45568\n" \
"ds_read_b64_tr_b16 a[176:177], %[vvb] offset:41984\n" \
"v_mfma_f32_16x16x32_bf16 v[112:115], a[144:147], v[220:223], v[112:115]\n" \
"v_exp_f32_e64 v152, v152\n" \
"v_mfma_f32_16x16x32_bf16 v[116:119], a[180:183], v[204:207], v[116:119]\n" \
"v_cvt_pk_bf16_f32 v202, v148, v149\n" \
"v_cvt_pk_bf16_f32 v203, v150, v151\n" \
"v_mfma_f32_16x16x32_bf16 v[116:119], a[148:151], v[220:223], v[116:119]\n" \
"v_exp_f32_e64 v153, v153\n" \
"v_mfma_f32_16x16x32_bf16 v[120:123], a[184:187], v[204:207], v[120:123]\n" \
"v_exp_f32_e64 v154, v154\n" \
"v_mfma_f32_16x16x32_bf16 v[120:123], a[152:155], v[220:223], v[120:123]\n" \
"ds_read_b64_tr_b16 a[178:179], %[vvb] offset:46080\n" \
"global_load_dwordx2 a[254:255], v232, %[px] offset:1088\n" \
"v_mfma_f32_16x16x32_bf16 v[124:127], a[188:191], v[204:207], v[124:127]\n" \
"v_exp_f32_e64 v155, v155\n" \
"v_mfma_f32_16x16x32_bf16 v[124:127], a[156:159], v[220:223], v[124:127]\n" \
"ds_read_b64_tr_b16 a[180:181], v211 offset:41984\n" \
"s_waitcnt lgkmcnt(13)\n" \
"v_mfma_f32_16x16x32_bf16 v[128:131], a[64:67], a[0:3], v[160:163]\n" \
"v_exp_f32_e32 v156, v156\n" \
"v_mfma_f32_16x16x32_bf16 v[128:131], a[68:71], a[4:7], v[128:131]\n" \
"ds_read_b64_tr_b16 a[182:183], v211 offset:46080\n" \
"s_add_u32 m0, %[m0b], 32768\n" \
"s_cmp_gt_u32 s48, 0\n" \
"v_mfma_f32_16x16x32_bf16 v[128:131], a[72:75], a[8:11], v[128:131]\n" \
"v_exp_f32_e32 v157, v157\n" \
"v_mfma_f32_16x16x32_bf16 v[128:131], a[76:79], a[12:15], v[128:131]\n" \
"v_cvt_pk_bf16_f32 v204, v152, v153\n" \
"v_cvt_pk_bf16_f32 v205, v154, v155\n" \
"v_mfma_f32_16x16x32_bf16 v[132:135], a[92:95], a[12:15], v[160:163]\n" \
"v_exp_f32_e64 v158, v158\n" \
"v_mfma_f32_16x16x32_bf16 v[132:135], a[88:91], a[8:11], v[132:135]\n" \
"ds_read_b64_tr_b16 a[184:185], %[vvb] offset:42496\n" \
"s_cselect_b32 s49, 0, 1\n" \
"s_lshl_b32 s49, s49, 18\n" \
"s_or_b32 s49, s49, s58\n" \
"s_waitcnt lgkmcnt(5)\n" \
"v_mfma_f32_16x16x32_bf16 v[132:135], a[84:87], a[4:7], v[132:135]\n" \
".if . - adaln_body != 36864\n" \
".error \"size model\"\n" \
".endif\n" \
"Lk8_%=:\n" \
"s_cbranch_execz Lk9_%=\n" \
"s_nop 0\n" \
"buffer_load_dwordx4 %[wo0], %[srdt], s49 offen offset:0 lds\n" \
"v_mfma_f32_16x16x32_bf16 v[132:135], a[80:83], a[0:3], v[132:135]\n" \
"v_exp_f32_e64 v159, v159\n" \
"v_mfma_f32_16x16x32_bf16 v[64:67], a[128:131], v[216:219], v[64:67]\n" \
"ds_read_b64_tr_b16 a[186:187], %[vvb] offset:46592\n" \
"ds_read_b64_tr_b16 a[188:189], v211 offset:42496\n" \
"v_mfma_f32_16x16x32_bf16 v[64:67], a[160:163], v[200:203], v[64:67]\n" \
"v_exp_f32_e64 v128, v128\n" \
"v_mfma_f32_16x16x32_bf16 v[68:71], a[132:135], v[216:219], v[68:71]\n" \
"v_cvt_pk_bf16_f32 v206, v156, v157\n" \
"v_cvt_pk_bf16_f32 v207, v158, v159\n" \
"v_mfma_f32_16x16x32_bf16 v[68:71], a[164:167], v[200:203], v[68:71]\n" \
"v_exp_f32_e64 v129, v129\n" \
"v_mfma_f32_16x16x32_bf16 v[72:75], a[136:139], v[216:219], v[72:75]\n" \
"ds_read_b64_tr_b16 a[190:191], v211 offset:46592\n" \
"ds_read_b64_tr_b16 a[128:129], %[vvb] offset:49152\n" \
"v_mfma_f32_16x16x32_bf16 v[72:75], a[168:171], v[200:203], v[72:75]\n" \
"v_exp_f32_e64 v130, v130\n" \
"v_mfma_f32_16x16x32_bf16 v[76:79], a[140:143], v[216:219], v[76:79]\n" \
"ds_read_b64_tr_b16 a[130:131], %[vvb] offset:53248\n" \
"ds_read_b64_tr_b16 a[132:133], v211 offset:49152\n" \
"v_mfma_f32_16x16x32_bf16 v[76:79], a[172:175], v[200:203], v[76:79]\n" \
"v_exp_f32_e64 v131, v131\n" \
"v_smfmac_f32_16x16x64_bf16 v[176:179], a[192:195], v[192:199], v228\n" \
"ds_read_b64_tr_b16 a[134:135], v211 offset:53248\n" \
"ds_read_b64_tr_b16 a[136:137], %[vvb] offset:49664\n" \
"v_mfma_f32_16x16x32_bf16 v[136:139], a[64:67], a[16:19], v[164:167]\n" \
"v_exp_f32_e64 v132, v132\n" \
"v_mfma_f32_16x16x32_bf16 v[136:139], a[68:71], a[20:23], v[136:139]\n" \
"ds_read_b64_tr_b16 a[138:139], %[vvb] offset:53760\n" \
"ds_read_b64_tr_b16 a[140:141], v211 offset:49664\n" \
"v_mfma_f32_16x16x32_bf16 v[136:139], a[72:75], a[24:27], v[136:139]\n" \
"v_exp_f32_e64 v133, v133\n" \
"v_mfma_f32_16x16x32_bf16 v[136:139], a[76:79], a[28:31], v[136:139]\n" \
"v_cvt_pk_bf16_f32 v184, v128, v129\n" \
"v_cvt_pk_bf16_f32 v185, v130, v131\n" \
"v_mfma_f32_16x16x32_bf16 v[140:143], a[92:95], a[28:31], v[164:167]\n" \
"v_exp_f32_e64 v134, v134\n" \
"v_mfma_f32_16x16x32_bf16 v[140:143], a[88:91], a[24:27], v[140:143]\n" \
"ds_read_b64_tr_b16 a[142:143], v211 offset:53760\n" \
"s_waitcnt lgkmcnt(8)\n" \
"v_mfma_f32_16x16x32_bf16 v[140:143], a[84:87], a[20:23], v[140:143]\n" \
"buffer_load_dwordx4 v212, %[srdt], s49 offen offset:1024 lds\n" \
"v_mfma_f32_16x16x32_bf16 v[140:143], a[80:83], a[16:19], v[140:143]\n" \
"v_exp_f32_e32 v135, v135\n" \
"v_mfma_f32_16x16x32_bf16 v[80:83], a[144:147], v[216:219], v[80:83]\n" \
"ds_read_b64_tr_b16 a[144:145], %[vvb] offset:50176\n" \
"ds_read_b64_tr_b16 a[146:147], %[vvb] offset:54272\n" \
"v_mfma_f32_16x16x32_bf16 v[80:83], a[176:179], v[200:203], v[80:83]\n" \
"v_exp_f32_e64 v136, v136\n" \
"v_mfma_f32_16x16x32_bf16 v[84:87], a[148:151], v[216:219], v[84:87]\n" \
"v_cvt_pk_bf16_f32 v186, v132, v133\n" \
"v_cvt_pk_bf16_f32 v187, v134, v135\n" \
"v_mfma_f32_16x16x32_bf16 v[84:87], a[180:183], v[200:203], v[84:87]\n" \
"v_exp_f32_e64 v137, v137\n" \
"v_mfma_f32_16x16x32_bf16 v[88:91], a[152:155], v[216:219], v[88:91]\n" \
"v_exp_f32_e64 v138, v138\n" \
"v_mfma_f32_16x16x32_bf16 v[88:91], a[184:187], v[200:203], v[88:91]\n" \
"ds_read_b64_tr_b16 a[148:149], v211 offset:50176\n" \
"ds_read_b64_tr_b16 a[150:151], v211 offset:54272\n" \
"v_readlane_b32 s64, %[tlo], 10\n" \
"v_mfma_f32_16x16x32_bf16 v[92:95], a[156:159], v[216:219], v[92:95]\n" \
"v_exp_f32_e64 v139, v139\n" \
"v_mfma_f32_16x16x32_bf16 v[92:95], a[188:191], v[200:203], v[92:95]\n" \
"ds_read_b64_tr_b16 a[152:153], %[vvb] offset:50688\n" \
"v_readlane_b32 s65, %[thi], 10\n" \
"v_mfma_f32_16x16x32_bf16 v[144:147], a[64:67], a[32:35], v[168:171]\n" \
"v_exp_f32_e64 v140, v140\n" \
"v_mfma_f32_16x16x32_bf16 v[144:147], a[68:71], a[36:39], v[144:147]\n" \
"ds_read_b64_tr_b16 a[154:155], %[vvb] offset:54784\n" \
"ds_read_b64_tr_b16 a[156:157], v211 offset:50688\n" \
"v_mfma_f32_16x16x32_bf16 v[144:147], a[72:75], a[40:43], v[144:147]\n" \
"v_exp_f32_e64 v141, v141\n" \
"v_mfma_f32_16x16x32_bf16 v[144:147], a[76:79], a[44:47], v[144:147]\n" \
"v_cvt_pk_bf16_f32 v188, v136, v137\n" \
"v_cvt_pk_bf16_f32 v189, v138, v139\n" \
"v_mfma_f32_16x16x32_bf16 v[148:151], a[92:95], a[44:47], v[168:171]\n" \
"v_exp_f32_e64 v142, v142\n" \
"v_mfma_f32_16x16x32_bf16 v[148:151], a[88:91], a[40:43], v[148:151]\n" \
"ds_read_b64_tr_b16 a[158:159], v211 offset:54784\n" \
"s_waitcnt lgkmcnt(8)\n" \
"v_mfma_f32_16x16x32_bf16 v[148:151], a[84:87], a[36:39], v[148:151]\n" \
"buffer_load_dwordx4 v213, %[srdt], s49 offen offset:2048 lds\n" \
"v_max3_f32 v232, v33, v38, v43\n" \
"v_mfma_f32_16x16x32_bf16 v[148:151], a[80:83], a[32:35], v[148:151]\n" \
"v_exp_f32_e32 v143, v143\n" \
"v_mfma_f32_16x16x32_bf16 v[0:3], a[160:163], v[192:195], v[0:3]\n" \
"ds_read_b128 a[96:99], %[kb0] offset:57344\n" \
"v_min3_f32 v233, v33, v38, v43\n" \
"v_mfma_f32_16x16x32_bf16 v[0:3], a[128:131], v[184:187], v[0:3]\n" \
"v_exp_f32_e64 v144, v144\n" \
"v_mfma_f32_16x16x32_bf16 v[4:7], a[164:167], v[192:195], v[4:7]\n" \
"v_cvt_pk_bf16_f32 v190, v140, v141\n" \
"v_cvt_pk_bf16_f32 v191, v142, v143\n" \
"v_mfma_f32_16x16x32_bf16 v[4:7], a[132:135], v[184:187], v[4:7]\n" \
"v_exp_f32_e64 v145, v145\n" \
"v_mfma_f32_16x16x32_bf16 v[8:11], a[168:171], v[192:195], v[8:11]\n" \
"ds_read_b128 a[100:103], v208 offset:57344\n" \
"v_max3_f32 v232, v232, v48, v53\n" \
"v_mfma_f32_16x16x32_bf16 v[8:11], a[136:139], v[184:187], v[8:11]\n" \
"v_exp_f32_e64 v146, v146\n" \
"v_mfma_f32_16x16x32_bf16 v[12:15], a[172:175], v[192:195], v[12:15]\n" \
"ds_read_b128 a[104:107], v209 offset:57344\n" \
"v_min3_f32 v233, v233, v48, v53\n" \
"v_mfma_f32_16x16x32_bf16 v[12:15], a[140:143], v[184:187], v[12:15]\n" \
"v_exp_f32_e64 v147, v147\n" \
"v_smfmac_f32_16x16x64_bf16 v[180:183], a[192:195], v[200:207], v228\n" \
"ds_read_b128 a[108:111], v210 offset:57344\n" \
"buffer_load_dword v229, v226, %[srdz], s50 offen sc1\n" \
"v_mfma_f32_16x16x32_bf16 v[152:155], a[64:67], a[48:51], v[172:175]\n" \
"v_exp_f32_e64 v148, v148\n" \
"v_mfma_f32_16x16x32_bf16 v[152:155], a[68:71], a[52:55], v[152:155]\n" \
"ds_read_b128 a[124:127], v210 offset:61440\n" \
"v_sub_f32_e64 v234, v232, v233\n" \
"v_mfma_f32_16x16x32_bf16 v[152:155], a[72:75], a[56:59], v[152:155]\n" \
"v_exp_f32_e64 v149, v149\n" \
"v_mfma_f32_16x16x32_bf16 v[152:155], a[76:79], a[60:63], v[152:155]\n" \
"v_cvt_pk_bf16_f32 v216, v144, v145\n" \
"v_cvt_pk_bf16_f32 v217, v146, v147\n" \
"v_mfma_f32_16x16x32_bf16 v[156:159], a[92:95], a[60:63], v[172:175]\n" \
"v_exp_f32_e64 v150, v150\n" \
"v_mfma_f32_16x16x32_bf16 v[156:159], a[88:91], a[56:59], v[156:159]\n" \
"ds_read_b128 a[120:123], v209 offset:61440\n" \
"s_waitcnt lgkmcnt(6)\n" \
"v_mfma_f32_16x16x32_bf16 v[156:159], a[84:87], a[52:55], v[156:159]\n" \
"buffer_load_dwordx4 v214, %[srdt], s49 offen offset:3072 lds\n" \
"v_mfma_f32_16x16x32_bf16 v[156:159], a[80:83], a[48:51], v[156:159]\n" \
"v_exp_f32_e32 v151, v151\n" \
"v_mfma_f32_16x16x32_bf16 v[16:19], a[176:179], v[192:195], v[16:19]\n" \
"ds_read_b128 a[116:119], v208 offset:61440\n" \
"v_mfma_f32_16x16x32_bf16 v[16:19], a[144:147], v[184:187], v[16:19]\n" \
"v_exp_f32_e64 v152, v152\n" \
"v_mfma_f32_16x16x32_bf16 v[20:23], a[180:183], v[192:195], v[20:23]\n" \
"v_cvt_pk_bf16_f32 v218, v148, v149\n" \
"v_cvt_pk_bf16_f32 v219, v150, v151\n" \
"v_mfma_f32_16x16x32_bf16 v[20:23], a[148:151], v[184:187], v[20:23]\n" \
"v_exp_f32_e64 v153, v153\n" \
"v_mfma_f32_16x16x32_bf16 v[24:27], a[184:187], v[192:195], v[24:27]\n" \
"v_exp_f32_e64 v154, v154\n" \
"v_mfma_f32_16x16x32_bf16 v[24:27], a[152:155], v[184:187], v[24:27]\n" \
"ds_read_b128 a[112:115], %[kb0] offset:61440\n" \
"v_mfma_f32_16x16x32_bf16 v[28:31], a[188:191], v[192:195], v[28:31]\n" \
"v_exp_f32_e64 v155, v155\n" \
"v_mfma_f32_16x16x32_bf16 v[28:31], a[156:159], v[184:187], v[28:31]\n" \
"s_waitcnt vmcnt(28) lgkmcnt(2)\n" \
"s_barrier\n" \
"v_mfma_f32_16x16x32_bf16 v[128:131], a[96:99], a[0:3], v[160:163]\n" \
"v_exp_f32_e64 v156, v156\n" \
"v_mfma_f32_16x16x32_bf16 v[128:131], a[100:103], a[4:7], v[128:131]\n" \
"s_add_u32 m0, %[m0b], 98304\n" \
"s_cmp_gt_u32 s48, 0\n" \
"v_mfma_f32_16x16x32_bf16 v[128:131], a[104:107], a[8:11], v[128:131]\n" \
"v_exp_f32_e32 v157, v157\n" \
"v_mfma_f32_16x16x32_bf16 v[128:131], a[108:111], a[12:15], v[128:131]\n" \
"v_cvt_pk_bf16_f32 v220, v152, v153\n" \
"v_cvt_pk_bf16_f32 v221, v154, v155\n" \
"v_mfma_f32_16x16x32_bf16 v[132:135], a[124:127], a[12:15], v[160:163]\n" \
"v_exp_f32_e64 v158, v158\n" \
"v_mfma_f32_16x16x32_bf16 v[132:135], a[120:123], a[8:11], v[132:135]\n" \
"s_cselect_b32 s49, 0, 1\n" \
"s_lshl_b32 s49, s49, 18\n" \
"s_or_b32 s49, s49, s59\n" \
"v_max3_f32 v232, v97, v102, v107\n" \
"s_waitcnt lgkmcnt(0)\n" \
"v_mfma_f32_16x16x32_bf16 v[132:135], a[116:119], a[4:7], v[132:135]\n" \
"buffer_load_dwordx4 %[wo0], %[srdt], s49 offen offset:0 lds\n" \
"v_mfma_f32_16x16x32_bf16 v[132:135], a[112:115], a[0:3], v[132:135]\n" \
"v_exp_f32_e64 v159, v159\n" \
"v_mfma_f32_16x16x32_bf16 v[32:35], a[160:163], v[196:199], v[32:35]\n" \
"ds_read_b64_tr_b16 a[196:197], v215 offset:0\n" \
"v_min3_f32 v233, v97, v102, v107\n" \
"v_mfma_f32_16x16x32_bf16 v[32:35], a[128:131], v[188:191], v[32:35]\n" \
"v_exp_f32_e64 v128, v128\n" \
"v_mfma_f32_16x16x32_bf16 v[36:39], a[164:167], v[196:199], v[36:39]\n" \
"v_cvt_pk_bf16_f32 v222, v156, v157\n" \
"v_cvt_pk_bf16_f32 v223, v158, v159\n" \
"v_mfma_f32_16x16x32_bf16 v[36:39], a[132:135], v[188:191], v[36:39]\n" \
"v_exp_f32_e64 v129, v129\n" \
"v_mfma_f32_16x16x32_bf16 v[40:43], a[168:171], v[196:199], v[40:43]\n" \
"ds_read_b64_tr_b16 a[198:199], v215 offset:2048\n" \
"v_max3_f32 v232, v232, v112, v117\n" \
"v_mfma_f32_16x16x32_bf16 v[40:43], a[136:139], v[188:191], v[40:43]\n" \
"v_exp_f32_e64 v130, v130\n" \
"v_mfma_f32_16x16x32_bf16 v[44:47], a[172:175], v[196:199], v[44:47]\n" \
"ds_read_b64_tr_b16 a[200:201], v215 offset:4096\n" \
"v_min3_f32 v233, v233, v112, v117\n" \
"v_mfma_f32_16x16x32_bf16 v[44:47], a[140:143], v[188:191], v[44:47]\n" \
"v_exp_f32_e64 v131, v131\n" \
"v_smfmac_f32_16x16x64_bf16 v[176:179], a[192:195], v[184:191], v228\n" \
"v_sub_f32_e32 v232, v232, v233\n" \
"v_min_f32_e32 v234, v234, v232\n" \
"v_mfma_f32_16x16x32_bf16 v[136:139], a[96:99], a[16:19], v[164:167]\n" \
"v_exp_f32_e64 v132, v132\n" \
"v_mfma_f32_16x16x32_bf16 v[136:139], a[100:103], a[20:23], v[136:139]\n" \
"ds_read_b64_tr_b16 a[202:203], v215 offset:6144\n" \
"v_max3_f32 v232, v1, v6, v11\n" \
"v_mfma_f32_16x16x32_bf16 v[136:139], a[104:107], a[24:27], v[136:139]\n" \
"v_exp_f32_e64 v133, v133\n" \
"v_mfma_f32_16x16x32_bf16 v[136:139], a[108:111], a[28:31], v[136:139]\n" \
"v_cvt_pk_bf16_f32 v192, v128, v129\n" \
"v_cvt_pk_bf16_f32 v193, v130, v131\n" \
"v_mfma_f32_16x16x32_bf16 v[140:143], a[124:127], a[28:31], v[164:167]\n" \
"v_exp_f32_e64 v134, v134\n" \
"v_mfma_f32_16x16x32_bf16 v[140:143], a[120:123], a[24:27], v[140:143]\n" \
"ds_read_b64_tr_b16 a[204:205], v215 offset:8192\n" \
"v_min3_f32 v233, v1, v6, v11\n" \
"v_mfma_f32_16x16x32_bf16 v[140:143], a[116:119], a[20:23], v[140:143]\n" \
"buffer_load_dwordx4 v212, %[srdt], s49 offen offset:1024 lds\n" \
"v_max3_f32 v232, v232, v16, v21\n" \
"v_mfma_f32_16x16x32_bf16 v[140:143], a[112:115], a[16:19], v[140:143]\n" \
"v_exp_f32_e64 v135, v135\n" \
"v_mfma_f32_16x16x32_bf16 v[48:51], a[176:179], v[196:199], v[48:51]\n" \
"ds_read_b64_tr_b16 a[206:207], v215 offset:10240\n" \
"v_add_u32_e32 v208, 0x1000, %[oq0]\n" \
"v_mfma_f32_16x16x32_bf16 v[48:51], a[144:147], v[188:191], v[48:51]\n" \
"v_exp_f32_e64 v136, v136\n" \
"v_mfma_f32_16x16x32_bf16 v[52:55], a[180:183], v[196:199], v[52:55]\n" \
"v_cvt_pk_bf16_f32 v194, v132, v133\n" \
"v_cvt_pk_bf16_f32 v195, v134, v135\n" \
"v_mfma_f32_16x16x32_bf16 v[52:55], a[148:151], v[188:191], v[52:55]\n" \
"v_exp_f32_e64 v137, v137\n" \
"v_mfma_f32_16x16x32_bf16 v[56:59], a[184:187], v[196:199], v[56:59]\n" \
"v_exp_f32_e64 v138, v138\n" \
"v_mfma_f32_16x16x32_bf16 v[56:59], a[152:155], v[188:191], v[56:59]\n" \
"ds_read_b64_tr_b16 a[208:209], v215 offset:12288\n" \
"v_add_u32_e32 v209, 0x4000, %[oq0]\n" \
"v_mfma_f32_16x16x32_bf16 v[60:63], a[188:191], v[196:199], v[60:63]\n" \
"v_exp_f32_e64 v139, v139\n" \
"v_mfma_f32_16x16x32_bf16 v[60:63], a[156:159], v[188:191], v[60:63]\n" \
"v_add_u32_e32 v210, 0x5000, %[oq0]\n" \
"v_mfma_f32_16x16x32_bf16 v[144:147], a[96:99], a[32:35], v[168:171]\n" \
"v_exp_f32_e64 v140, v140\n" \
"v_mfma_f32_16x16x32_bf16 v[144:147], a[100:103], a[36:39], v[144:147]\n" \
"ds_read_b64_tr_b16 a[210:211], v215 offset:14336\n" \
"v_min3_f32 v233, v233, v16, v21\n" \
"v_mfma_f32_16x16x32_bf16 v[144:147], a[104:107], a[40:43], v[144:147]\n" \
"v_exp_f32_e64 v141, v141\n" \
"v_mfma_f32_16x16x32_bf16 v[144:147], a[108:111], a[44:47], v[144:147]\n" \
"v_cvt_pk_bf16_f32 v196, v136, v137\n" \
"v_cvt_pk_bf16_f32 v197, v138, v139\n" \
"v_mfma_f32_16x16x32_bf16 v[148:151], a[124:127], a[44:47], v[168:171]\n" \
"v_exp_f32_e64 v142, v142\n" \
"v_mfma_f32_16x16x32_bf16 v[148:151], a[120:123], a[40:43], v[148:151]\n" \
"v_sub_f32_e32 v232, v232, v233\n" \
"v_min_f32_e32 v234, v234, v232\n" \
"v_mfma_f32_16x16x32_bf16 v[148:151], a[116:119], a[36:39], v[148:151]\n" \
"buffer_load_dwordx4 v213, %[srdt], s49 offen offset:2048 lds\n" \
"v_max3_f32 v232, v65, v70, v75\n" \
"v_mfma_f32_16x16x32_bf16 v[148:151], a[112:115], a[32:35], v[148:151]\n" \
"v_exp_f32_e64 v143, v143\n" \
"v_mfma_f32_16x16x32_bf16 v[96:99], a[160:163], v[204:207], v[96:99]\n" \
"ds_read_b64_tr_b16 a[160:161], %[vvb] offset:57344\n" \
"v_min3_f32 v233, v65, v70, v75\n" \
"v_mfma_f32_16x16x32_bf16 v[96:99], a[128:131], v[220:223], v[96:99]\n" \
"v_exp_f32_e64 v144, v144\n" \
"v_mfma_f32_16x16x32_bf16 v[100:103], a[164:167], v[204:207], v[100:103]\n" \
"v_cvt_pk_bf16_f32 v198, v140, v141\n" \
"v_cvt_pk_bf16_f32 v199, v142, v143\n" \
"v_mfma_f32_16x16x32_bf16 v[100:103], a[132:135], v[220:223], v[100:103]\n" \
"v_exp_f32_e64 v145, v145\n" \
"v_mfma_f32_16x16x32_bf16 v[104:107], a[168:171], v[204:207], v[104:107]\n" \
"ds_read_b64_tr_b16 a[162:163], %[vvb] offset:61440\n" \
"ds_read_b64_tr_b16 a[164:165], v211 offset:57344\n" \
"v_mfma_f32_16x16x32_bf16 v[104:107], a[136:139], v[220:223], v[104:107]\n" \
"v_exp_f32_e64 v146, v146\n" \
"v_mfma_f32_16x16x32_bf16 v[108:111], a[172:175], v[204:207], v[108:111]\n" \
"ds_read_b64_tr_b16 a[166:167], v211 offset:61440\n" \
"v_max3_f32 v232, v232, v80, v85\n" \
"v_mfma_f32_16x16x32_bf16 v[108:111], a[140:143], v[220:223], v[108:111]\n" \
"v_exp_f32_e64 v147, v147\n" \
"v_smfmac_f32_16x16x64_bf16 v[180:183], a[192:195], v[216:223], v228\n" \
"ds_read_b64_tr_b16 a[168:169], %[vvb] offset:57856\n" \
"v_min3_f32 v233, v233, v80, v85\n" \
"v_mfma_f32_16x16x32_bf16 v[152:155], a[96:99], a[48:51], v[172:175]\n" \
"v_exp_f32_e64 v148, v148\n" \
"v_mfma_f32_16x16x32_bf16 v[152:155], a[100:103], a[52:55], v[152:155]\n" \
"ds_read_b64_tr_b16 a[170:171], %[vvb] offset:61952\n" \
"v_sub_f32_e64 v232, v232, v233\n" \
"v_mfma_f32_16x16x32_bf16 v[152:155], a[104:107], a[56:59], v[152:155]\n" \
"v_exp_f32_e64 v149, v149\n" \
"v_mfma_f32_16x16x32_bf16 v[152:155], a[108:111], a[60:63], v[152:155]\n" \
"v_cvt_pk_bf16_f32 v200, v144, v145\n" \
"v_cvt_pk_bf16_f32 v201, v146, v147\n" \
"v_mfma_f32_16x16x32_bf16 v[156:159], a[124:127], a[60:63], v[172:175]\n" \
"v_exp_f32_e64 v150, v150\n" \
"v_mfma_f32_16x16x32_bf16 v[156:159], a[120:123], a[56:59], v[156:159]\n" \
"ds_read_b64_tr_b16 a[172:173], v211 offset:57856\n" \
"v_min_f32_e64 v234, v234, v232\n" \
"v_mfma_f32_16x16x32_bf16 v[156:159], a[116:119], a[52:55], v[156:159]\n" \
"buffer_load_dwordx4 v214, %[srdt], s49 offen offset:3072 lds\n" \
"v_accvgpr_read_b32 v233, a228\n" \
"v_mul_f32_e32 v233, 0x3a000000, v233\n" \
"v_mfma_f32_16x16x32_bf16 v[156:159], a[112:115], a[48:51], v[156:159]\n" \
"v_exp_f32_e64 v151, v151\n" \
"v_mfma_f32_16x16x32_bf16 v[112:115], a[176:179], v[204:207], v[112:115]\n" \
"ds_read_b64_tr_b16 a[174:175], v211 offset:61952\n" \
"ds_read_b64_tr_b16 a[176:177], %[vvb] offset:58368\n" \
"v_mfma_f32_16x16x32_bf16 v[112:115], a[144:147], v[220:223], v[112:115]\n" \
"v_exp_f32_e64 v152, v152\n" \
"v_mfma_f32_16x16x32_bf16 v[116:119], a[180:183], v[204:207], v[116:119]\n" \
"v_cvt_pk_bf16_f32 v202, v148, v149\n" \
"v_cvt_pk_bf16_f32 v203, v150, v151\n" \
"v_mfma_f32_16x16x32_bf16 v[116:119], a[148:151], v[220:223], v[116:119]\n" \
"v_exp_f32_e64 v153, v153\n" \
"v_mfma_f32_16x16x32_bf16 v[120:123], a[184:187], v[204:207], v[120:123]\n" \
"v_exp_f32_e64 v154, v154\n" \
"v_mfma_f32_16x16x32_bf16 v[120:123], a[152:155], v[220:223], v[120:123]\n" \
"ds_read_b64_tr_b16 a[178:179], %[vvb] offset:62464\n" \
"s_waitcnt vmcnt(5)\n" \
"v_cmp_eq_u32_e64 s[56:57], 0, v229\n" \
"s_cmp_lg_u64 s[56:57], 0\n" \
"s_cbranch_scc1 Lspk_%=\n" \
"Lbkk_%=:\n" \
"v_mfma_f32_16x16x32_bf16 v[124:127], a[188:191], v[204:207], v[124:127]\n" \
"v_exp_f32_e32 v155, v155\n" \
"v_mfma_f32_16x16x32_bf16 v[124:127], a[156:159], v[220:223], v[124:127]\n" \
"ds_read_b64_tr_b16 a[180:181], v211 offset:58368\n" \
"ds_read_b64_tr_b16 a[182:183], v211 offset:62464\n" \
"ds_read_b64_tr_b16 a[184:185], %[vvb] offset:58880\n" \
"ds_read_b64_tr_b16 a[186:187], %[vvb] offset:62976\n" \
"ds_read_b64_tr_b16 a[188:189], v211 offset:58880\n" \
"ds_read_b64_tr_b16 a[190:191], v211 offset:62976\n" \
"s_waitcnt lgkmcnt(8)\n" \
"s_nop 0\n" \
"v_smfmac_f32_16x16x64_bf16 v[176:179], a[192:195], v[192:199], v228\n" \
"v_exp_f32_e64 v156, v156\n" \
"ds_read_b64_tr_b16 a[212:213], v227 offset:0\n" \
"v_mfma_f32_16x16x32_bf16 v[0:3], a[160:163], v[192:195], v[0:3]\n" \
"v_exp_f32_e64 v157, v157\n" \
"ds_read_b64_tr_b16 a[214:215], v227 offset:2048\n" \
"v_mfma_f32_16x16x32_bf16 v[4:7], a[164:167], v[192:195], v[4:7]\n" \
"v_cvt_pk_bf16_f32 v204, v152, v153\n" \
"ds_read_b64_tr_b16 a[216:217], v227 offset:4096\n" \
"v_mfma_f32_16x16x32_bf16 v[8:11], a[168:171], v[192:195], v[8:11]\n" \
"v_cvt_pk_bf16_f32 v205, v154, v155\n" \
"ds_read_b64_tr_b16 a[218:219], v227 offset:6144\n" \
"v_mfma_f32_16x16x32_bf16 v[12:15], a[172:175], v[192:195], v[12:15]\n" \
"v_exp_f32_e64 v158, v158\n" \
"ds_read_b64_tr_b16 a[220:221], v227 offset:8192\n" \
"s_waitcnt lgkmcnt(5)\n" \
"s_nop 0\n" \
"v_mfma_f32_16x16x32_bf16 v[16:19], a[176:179], v[192:195], v[16:19]\n" \
"v_exp_f32_e64 v159, v159\n" \
"ds_read_b64_tr_b16 a[222:223], v227 offset:10240\n" \
"v_mfma_f32_16x16x32_bf16 v[20:23], a[180:183], v[192:195], v[20:23]\n" \
"v_cvt_pk_bf16_f32 v206, v156, v157\n" \
"ds_read_b64_tr_b16 a[224:225], v227 offset:12288\n" \
"v_accvgpr_read_b32 v229, a228\n" \
"v_mfma_f32_16x16x32_bf16 v[24:27], a[184:187], v[192:195], v[24:27]\n" \
"v_cvt_pk_bf16_f32 v207, v158, v159\n" \
"ds_read_b64_tr_b16 a[226:227], v227 offset:14336\n" \
"v_mfma_f32_16x16x32_bf16 v[28:31], a[188:191], v[192:195], v[28:31]\n" \
"v_add_f32_e64 v176, v176, v177\n" \
"v_smfmac_f32_16x16x64_bf16 v[180:183], a[192:195], v[200:207], v228\n" \
"v_add_f32_e32 v178, v178, v179\n" \
"v_add_f32_e32 v231, v176, v178\n" \
"v_rcp_f32_e32 v176, v176\n" \
"v_rcp_f32_e32 v178, v178\n" \
"v_mul_f32_e64 v177, -v229, v176\n" \
"v_mul_f32_e64 v179, -v229, v178\n" \
"v_mfma_f32_16x16x32_bf16 v[32:35], a[160:163], v[196:199], v[32:35]\n" \
"v_pk_fma_f32 v[0:1], v[0:1], v[176:177], v[176:177] op_sel:[0,0,1] op_sel_hi:[1,0,1]\n" \
"v_pk_fma_f32 v[2:3], v[2:3], v[176:177], v[176:177] op_sel:[0,0,1] op_sel_hi:[1,0,1]\n" \
"v_pk_fma_f32 v[4:5], v[4:5], v[176:177], v[176:177] op_sel:[0,0,1] op_sel_hi:[1,0,1]\n" \
"v_pk_fma_f32 v[6:7], v[6:7], v[176:177], v[176:177] op_sel:[0,0,1] op_sel_hi:[1,0,1]\n" \
"v_cvt_pk_bf16_f32 v0, v0, v1\n" \
"v_cvt_pk_bf16_f32 v1, v2, v3\n" \
"v_cvt_pk_bf16_f32 v2, v4, v5\n" \
"v_cvt_pk_bf16_f32 v3, v6, v7\n" \
"v_mfma_f32_16x16x32_bf16 v[36:39], a[164:167], v[196:199], v[36:39]\n" \
"s_mov_b32 s55, 0x4e800000\n" \
"v_mov_b32_e32 v236, %[flag]\n" \
"v_add_f32_e32 v180, v180, v181\n" \
"v_add_f32_e32 v182, v182, v183\n" \
"v_add_f32_e32 v231, v231, v180\n" \
"v_add_f32_e32 v231, v231, v182\n" \
"v_rcp_f32_e32 v180, v180\n" \
"v_cmp_nle_f32_e64 s[56:57], v231, s55\n" \
"v_mfma_f32_16x16x32_bf16 v[40:43], a[168:171], v[196:199], v[40:43]\n" \
"v_rcp_f32_e64 v182, v182\n" \
"v_cmp_nle_f32_e64 s[44:45], v233, v234\n" \
"v_mul_f32_e64 v181, -v229, v180\n" \
"v_mul_f32_e64 v183, -v229, v182\n" \
"s_nop 1\n" \
"s_or_b64 s[56:57], s[56:57], s[44:45]\n" \
"s_cselect_b32 s55, 1, 0\n" \
"v_mov_b32_e32 v230, s55\n" \
"v_mfma_f32_16x16x32_bf16 v[44:47], a[172:175], v[196:199], v[44:47]\n" \
"s_mov_b32 s69, s55\n" \
"s_andn2_b32 s55, s55, s52\n" \
"s_cselect_b64 vcc, 0, vcc\n" \
"s_nop 0\n" \
"ds_write_b32 v236, v230\n" \
"v_and_b32_e32 v236, -16, v236\n" \
"s_cbranch_vccz Lg21_%=\n" \
"buffer_store_dwordx4 v[0:3], %[oq0], %[srdz], s60 offen offset:0 sc1\n" \
"Lg21_%=:\n" \
"v_pk_fma_f32 v[8:9], v[8:9], v[176:177], v[176:177] op_sel:[0,0,1] op_sel_hi:[1,0,1]\n" \
"v_pk_fma_f32 v[10:11], v[10:11], v[176:177], v[176:177] op_sel:[0,0,1] op_sel_hi:[1,0,1]\n" \
"v_mfma_f32_16x16x32_bf16 v[48:51], a[176:179], v[196:199], v[48:51]\n" \
"v_pk_fma_f32 v[12:13], v[12:13], v[176:177], v[176:177] op_sel:[0,0,1] op_sel_hi:[1,0,1]\n" \
"v_pk_fma_f32 v[14:15], v[14:15], v[176:177], v[176:177] op_sel:[0,0,1] op_sel_hi:[1,0,1]\n" \
"v_cvt_pk_bf16_f32 v8, v8, v9\n" \
"v_cvt_pk_bf16_f32 v9, v10, v11\n" \
"v_cvt_pk_bf16_f32 v10, v12, v13\n" \
"v_cvt_pk_bf16_f32 v11, v14, v15\n" \
"s_cbranch_vccz Lg22_%=\n" \
"s_nop 0\n" \
"buffer_store_dwordx4 v[8:11], %[oq0], %[srdz], s60 offen offset:1024 sc1\n" \
"Lg22_%=:\n" \
"v_pk_fma_f32 v[16:17], v[16:17], v[176:177], v[176:177] op_sel:[0,0,1] op_sel_hi:[1,0,1]\n" \
"v_mfma_f32_16x16x32_bf16 v[52:55], a[180:183], v[196:199], v[52:55]\n" \
"v_pk_fma_f32 v[18:19], v[18:19], v[176:177], v[176:177] op_sel:[0,0,1] op_sel_hi:[1,0,1]\n" \
"v_pk_fma_f32 v[20:21], v[20:21], v[176:177], v[176:177] op_sel:[0,0,1] op_sel_hi:[1,0,1]\n" \
"v_pk_fma_f32 v[22:23], v[22:23], v[176:177], v[176:177] op_sel:[0,0,1] op_sel_hi:[1,0,1]\n" \
"v_cvt_pk_bf16_f32 v16, v16, v17\n" \
"v_cvt_pk_bf16_f32 v17, v18, v19\n" \
"v_cvt_pk_bf16_f32 v18, v20, v21\n" \
"v_cvt_pk_bf16_f32 v19, v22, v23\n" \
"s_cbranch_vccz Lg23_%=\n" \
"s_nop 0\n" \
"buffer_store_dwordx4 v[16:19], %[oq0], %[srdz], s60 offen offset:2048 sc1\n" \
"Lg23_%=:\n" \
"v_mfma_f32_16x16x32_bf16 v[56:59], a[184:187], v[196:199], v[56:59]\n" \
"v_pk_fma_f32 v[24:25], v[24:25], v[176:177], v[176:177] op_sel:[0,0,1] op_sel_hi:[1,0,1]\n" \
"v_pk_fma_f32 v[26:27], v[26:27], v[176:177], v[176:177] op_sel:[0,0,1] op_sel_hi:[1,0,1]\n" \
"v_pk_fma_f32 v[28:29], v[28:29], v[176:177], v[176:177] op_sel:[0,0,1] op_sel_hi:[1,0,1]\n" \
"v_pk_fma_f32 v[30:31], v[30:31], v[176:177], v[176:177] op_sel:[0,0,1] op_sel_hi:[1,0,1]\n" \
"v_cvt_pk_bf16_f32 v24, v24, v25\n" \
"v_cvt_pk_bf16_f32 v25, v26, v27\n" \
"v_cvt_pk_bf16_f32 v26, v28, v29\n" \
"v_cvt_pk_bf16_f32 v27, v30, v31\n" \
"v_mfma_f32_16x16x32_bf16 v[60:63], a[188:191], v[196:199], v[60:63]\n" \
"s_cbranch_vccz Lg24_%=\n" \
"s_nop 0\n" \
"buffer_store_dwordx4 v[24:27], %[oq0], %[srdz], s60 offen offset:3072 sc1\n" \
"Lg24_%=:\n" \
"v_mfma_f32_16x16x32_bf16 v[64:67], a[128:131], v[216:219], v[64:67]\n" \
"v_fma_f32 v32, v32, v178, v179\n" \
"v_fma_f32 v33, v33, v178, v179\n" \
"v_fma_f32 v34, v34, v178, v179\n" \
"v_fma_f32 v35, v35, v178, v179\n" \
"v_fma_f32 v36, v36, v178, v179\n" \
"v_fma_f32 v37, v37, v178, v179\n" \
"v_mfma_f32_16x16x32_bf16 v[64:67], a[160:163], v[200:203], v[64:67]\n" \
"v_fma_f32 v38, v38, v178, v179\n" \
"v_fma_f32 v39, v39, v178, v179\n" \
"v_cvt_pk_bf16_f32 v32, v32, v33\n" \
"v_cvt_pk_bf16_f32 v33, v34, v35\n" \
"v_mfma_f32_16x16x32_bf16 v[68:71], a[132:135], v[216:219], v[68:71]\n" \
"v_cvt_pk_bf16_f32 v34, v36, v37\n" \
"v_cvt_pk_bf16_f32 v35, v38, v39\n" \
"s_cbranch_vccz Lg25_%=\n" \
"s_nop 0\n" \
"buffer_store_dwordx4 v[32:35], v208, %[srdz], s60 offen offset:0 sc1\n" \
"Lg25_%=:\n" \
"v_mfma_f32_16x16x32_bf16 v[68:71], a[164:167], v[200:203], v[68:71]\n" \
"v_fma_f32 v40, v40, v178, v179\n" \
"v_fma_f32 v41, v41, v178, v179\n" \
"v_fma_f32 v42, v42, v178, v179\n" \
"v_fma_f32 v43, v43, v178, v179\n" \
"v_fma_f32 v44, v44, v178, v179\n" \
"v_fma_f32 v45, v45, v178, v179\n" \
"v_mfma_f32_16x16x32_bf16 v[72:75], a[136:139], v[216:219], v[72:75]\n" \
"v_fma_f32 v46, v46, v178, v179\n" \
"v_fma_f32 v47, v47, v178, v179\n" \
"v_cvt_pk_bf16_f32 v40, v40, v41\n" \
"v_cvt_pk_bf16_f32 v41, v42, v43\n" \
"v_mfma_f32_16x16x32_bf16 v[72:75], a[168:171], v[200:203], v[72:75]\n" \
"v_cvt_pk_bf16_f32 v42, v44, v45\n" \
"v_cvt_pk_bf16_f32 v43, v46, v47\n" \
"s_cbranch_vccz Lg26_%=\n" \
"s_nop 0\n" \
"buffer_store_dwordx4 v[40:43], v208, %[srdz], s60 offen offset:1024 sc1\n" \
"Lg26_%=:\n" \
"v_mfma_f32_16x16x32_bf16 v[76:79], a[140:143], v[216:219], v[76:79]\n" \
"v_fma_f32 v48, v48, v178, v179\n" \
"v_fma_f32 v49, v49, v178, v179\n" \
"v_fma_f32 v50, v50, v178, v179\n" \
"v_fma_f32 v51, v51, v178, v179\n" \
"v_fma_f32 v52, v52, v178, v179\n" \
"v_fma_f32 v53, v53, v178, v179\n" \
"v_mfma_f32_16x16x32_bf16 v[76:79], a[172:175], v[200:203], v[76:79]\n" \
"v_fma_f32 v54, v54, v178, v179\n" \
"v_fma_f32 v55, v55, v178, v179\n" \
"v_cvt_pk_bf16_f32 v48, v48, v49\n" \
"v_cvt_pk_bf16_f32 v49, v50, v51\n" \
"v_mfma_f32_16x16x32_bf16 v[80:83], a[144:147], v[216:219], v[80:83]\n" \
"v_cvt_pk_bf16_f32 v50, v52, v53\n" \
"v_cvt_pk_bf16_f32 v51, v54, v55\n" \
"s_cbranch_vccz Lg27_%=\n" \
"s_nop 0\n" \
"buffer_store_dwordx4 v[48:51], v208, %[srdz], s60 offen offset:2048 sc1\n" \
"Lg27_%=:\n" \
"v_mfma_f32_16x16x32_bf16 v[80:83], a[176:179], v[200:203], v[80:83]\n" \
"v_fma_f32 v56, v56, v178, v179\n" \
"v_fma_f32 v57, v57, v178, v179\n" \
"v_fma_f32 v58, v58, v178, v179\n" \
"v_fma_f32 v59, v59, v178, v179\n" \
"v_fma_f32 v60, v60, v178, v179\n" \
"v_fma_f32 v61, v61, v178, v179\n" \
"v_mfma_f32_16x16x32_bf16 v[84:87], a[148:151], v[216:219], v[84:87]\n" \
"v_fma_f32 v62, v62, v178, v179\n" \
"v_fma_f32 v63, v63, v178, v179\n" \
"v_cvt_pk_bf16_f32 v56, v56, v57\n" \
"v_cvt_pk_bf16_f32 v57, v58, v59\n" \
"v_mfma_f32_16x16x32_bf16 v[84:87], a[180:183], v[200:203], v[84:87]\n" \
"v_cvt_pk_bf16_f32 v58, v60, v61\n" \
"v_cvt_pk_bf16_f32 v59, v62, v63\n" \
"s_cbranch_vccz Lg28_%=\n" \
"s_nop 0\n" \
"buffer_store_dwordx4 v[56:59], v208, %[srdz], s60 offen offset:3072 sc1\n" \
"Lg28_%=:\n" \
"v_mfma_f32_16x16x32_bf16 v[88:91], a[152:155], v[216:219], v[88:91]\n" \
"v_mfma_f32_16x16x32_bf16 v[88:91], a[184:187], v[200:203], v[88:91]\n" \
"v_mfma_f32_16x16x32_bf16 v[92:95], a[156:159], v[216:219], v[92:95]\n" \
"v_mfma_f32_16x16x32_bf16 v[92:95], a[188:191], v[200:203], v[92:95]\n" \
"s_waitcnt vmcnt(12) lgkmcnt(0)\n" \
"s_bitcmp1_b32 0x1f, s48\n" \
"s_cbranch_scc1 Lcw1_%=\n" \
"s_waitcnt vmcnt(4)\n" \
"Lcw1_%=:\n" \
"s_barrier\n" \
"s_nop 0\n" \
"ds_read_b128 v[232:235], v236\n" \
"v_mfma_f32_16x16x32_bf16 v[96:99], a[160:163], v[204:207], v[96:99]\n" \
"v_pk_fma_f32 v[64:65], v[64:65], v[180:181], v[180:181] op_sel:[0,0,1] op_sel_hi:[1,0,1]\n" \
"v_pk_fma_f32 v[66:67], v[66:67], v[180:181], v[180:181] op_sel:[0,0,1] op_sel_hi:[1,0,1]\n" \
"v_pk_fma_f32 v[68:69], v[68:69], v[180:181], v[180:181] op_sel:[0,0,1] op_sel_hi:[1,0,1]\n" \
"v_pk_fma_f32 v[70:71], v[70:71], v[180:181], v[180:181] op_sel:[0,0,1] op_sel_hi:[1,0,1]\n" \
"v_cvt_pk_bf16_f32 v64, v64, v65\n" \
"v_mfma_f32_16x16x32_bf16 v[100:103], a[164:167], v[204:207], v[100:103]\n" \
"v_cvt_pk_bf16_f32 v65, v66, v67\n" \
"v_cvt_pk_bf16_f32 v66, v68, v69\n" \
"v_cvt_pk_bf16_f32 v67, v70, v71\n" \
"s_cbranch_vccz Lg29_%=\n" \
"s_nop 0\n" \
"buffer_store_dwordx4 v[64:67], v209, %[srdz], s60 offen offset:0 sc1\n" \
"Lg29_%=:\n" \
"v_pk_fma_f32 v[72:73], v[72:73], v[180:181], v[180:181] op_sel:[0,0,1] op_sel_hi:[1,0,1]\n" \
"v_mfma_f32_16x16x32_bf16 v[104:107], a[168:171], v[204:207], v[104:107]\n" \
"v_pk_fma_f32 v[74:75], v[74:75], v[180:181], v[180:181] op_sel:[0,0,1] op_sel_hi:[1,0,1]\n" \
"v_pk_fma_f32 v[76:77], v[76:77], v[180:181], v[180:181] op_sel:[0,0,1] op_sel_hi:[1,0,1]\n" \
"v_pk_fma_f32 v[78:79], v[78:79], v[180:181], v[180:181] op_sel:[0,0,1] op_sel_hi:[1,0,1]\n" \
"v_cvt_pk_bf16_f32 v72, v72, v73\n" \
"v_cvt_pk_bf16_f32 v73, v74, v75\n" \
"v_mfma_f32_16x16x32_bf16 v[108:111], a[172:175], v[204:207], v[108:111]\n" \
"v_cvt_pk_bf16_f32 v74, v76, v77\n" \
"v_cvt_pk_bf16_f32 v75, v78, v79\n" \
"s_cbranch_vccz Lg30_%=\n" \
"s_nop 0\n" \
"buffer_store_dwordx4 v[72:75], v209, %[srdz], s60 offen offset:1024 sc1\n" \
"Lg30_%=:\n" \
"v_pk_fma_f32 v[80:81], v[80:81], v[180:181], v[180:181] op_sel:[0,0,1] op_sel_hi:[1,0,1]\n" \
"v_pk_fma_f32 v[82:83], v[82:83], v[180:181], v[180:181] op_sel:[0,0,1] op_sel_hi:[1,0,1]\n" \
"v_mfma_f32_16x16x32_bf16 v[112:115], a[176:179], v[204:207], v[112:115]\n" \
"v_pk_fma_f32 v[84:85], v[84:85], v[180:181], v[180:181] op_sel:[0,0,1] op_sel_hi:[1,0,1]\n" \
"v_pk_fma_f32 v[86:87], v[86:87], v[180:181], v[180:181] op_sel:[0,0,1] op_sel_hi:[1,0,1]\n" \
".if . - adaln_body != 40960\n" \
".error \"size model\"\n" \
".endif\n" \
"Lk9_%=:\n" \
"s_cbranch_execz Lk10_%=\n" \
"s_nop 0\n" \
"v_cvt_pk_bf16_f32 v80, v80, v81\n" \
"v_cvt_pk_bf16_f32 v81, v82, v83\n" \
"v_cvt_pk_bf16_f32 v82, v84, v85\n" \
"v_mfma_f32_16x16x32_bf16 v[116:119], a[180:183], v[204:207], v[116:119]\n" \
"v_cvt_pk_bf16_f32 v83, v86, v87\n" \
"s_cbranch_vccz Lg31_%=\n" \
"s_nop 0\n" \
"buffer_store_dwordx4 v[80:83], v209, %[srdz], s60 offen offset:2048 sc1\n" \
"Lg31_%=:\n" \
"v_pk_fma_f32 v[88:89], v[88:89], v[180:181], v[180:181] op_sel:[0,0,1] op_sel_hi:[1,0,1]\n" \
"v_pk_fma_f32 v[90:91], v[90:91], v[180:181], v[180:181] op_sel:[0,0,1] op_sel_hi:[1,0,1]\n" \
"v_pk_fma_f32 v[92:93], v[92:93], v[180:181], v[180:181] op_sel:[0,0,1] op_sel_hi:[1,0,1]\n" \
"v_mfma_f32_16x16x32_bf16 v[120:123], a[184:187], v[204:207], v[120:123]\n" \
"v_pk_fma_f32 v[94:95], v[94:95], v[180:181], v[180:181] op_sel:[0,0,1] op_sel_hi:[1,0,1]\n" \
"v_cvt_pk_bf16_f32 v88, v88, v89\n" \
"v_cvt_pk_bf16_f32 v89, v90, v91\n" \
"v_cvt_pk_bf16_f32 v90, v92, v93\n" \
"v_cvt_pk_bf16_f32 v91, v94, v95\n" \
"v_mfma_f32_16x16x32_bf16 v[124:127], a[188:191], v[204:207], v[124:127]\n" \
"s_cbranch_vccz Lg32_%=\n" \
"s_nop 0\n" \
"buffer_store_dwordx4 v[88:91], v209, %[srdz], s60 offen offset:3072 sc1\n" \
"Lg32_%=:\n" \
"s_waitcnt lgkmcnt(0)\n" \
"s_nop 0\n" \
"v_or3_b32 v232, v232, v233, v234\n" \
"v_or_b32_e32 v232, v232, v235\n" \
"s_nop 0\n" \
"v_readfirstlane_b32 s55, v232\n" \
"s_nop 3\n" \
"s_andn2_b32 s55, s55, s52\n" \
"s_cbranch_scc1 Lslow_%=\n" \
"v_mfma_f32_16x16x32_bf16 v[192:195], a[196:199], v[0:3], 0\n" \
"v_mfma_f32_16x16x32_bf16 v[192:195], a[200:203], v[8:11], v[192:195]\n" \
"v_fma_f32 v96, v96, v182, v183\n" \
"v_fma_f32 v97, v97, v182, v183\n" \
"v_fma_f32 v98, v98, v182, v183\n" \
"v_fma_f32 v99, v99, v182, v183\n" \
"v_mfma_f32_16x16x32_bf16 v[192:195], a[204:207], v[16:19], v[192:195]\n" \
"v_fma_f32 v100, v100, v182, v183\n" \
"v_fma_f32 v101, v101, v182, v183\n" \
"v_fma_f32 v102, v102, v182, v183\n" \
"v_fma_f32 v103, v103, v182, v183\n" \
"s_add_u32 m0, %[m0b], 114688\n" \
"s_cmp_gt_u32 s48, 1\n" \
"s_cselect_b32 s49, 1, 2\n" \
"s_lshl_b32 s49, s49, 18\n" \
"s_or_b32 s49, s49, s58\n" \
"buffer_load_dwordx4 %[wo0], %[srdt], s49 offen offset:0 lds\n" \
"v_mfma_f32_16x16x32_bf16 v[192:195], a[208:211], v[24:27], v[192:195]\n" \
"v_cvt_pk_bf16_f32 v96, v96, v97\n" \
"v_cvt_pk_bf16_f32 v97, v98, v99\n" \
"v_mfma_f32_16x16x32_bf16 v[144:147], a[212:215], v[0:3], 0\n" \
"v_cvt_pk_bf16_f32 v98, v100, v101\n" \
"v_mfma_f32_16x16x32_bf16 v[144:147], a[216:219], v[8:11], v[144:147]\n" \
"v_cvt_pk_bf16_f32 v99, v102, v103\n" \
"s_cbranch_vccz Lg1_%=\n" \
"s_nop 0\n" \
"buffer_store_dwordx4 v[96:99], v210, %[srdz], s60 offen offset:0 sc1\n" \
"Lg1_%=:\n" \
"v_mfma_f32_16x16x32_bf16 v[144:147], a[220:223], v[16:19], v[144:147]\n" \
"ds_read_b64_tr_b16 a[228:229], v215 offset:512\n" \
"v_fma_f32 v104, v104, v182, v183\n" \
"v_fma_f32 v105, v105, v182, v183\n" \
"v_fma_f32 v106, v106, v182, v183\n" \
"v_fma_f32 v107, v107, v182, v183\n" \
"v_mfma_f32_16x16x32_bf16 v[144:147], a[224:227], v[24:27], v[144:147]\n" \
"ds_read_b64_tr_b16 a[230:231], v215 offset:2560\n" \
"v_fma_f32 v108, v108, v182, v183\n" \
"v_fma_f32 v109, v109, v182, v183\n" \
"v_mfma_f32_16x16x32_bf16 v[196:199], a[196:199], v[32:35], 0\n" \
"ds_read_b64_tr_b16 a[232:233], v215 offset:4608\n" \
"v_fma_f32 v110, v110, v182, v183\n" \
"v_fma_f32 v111, v111, v182, v183\n" \
"v_cvt_pk_bf16_f32 v104, v104, v105\n" \
"v_mfma_f32_16x16x32_bf16 v[196:199], a[200:203], v[40:43], v[196:199]\n" \
"ds_read_b64_tr_b16 a[234:235], v215 offset:6656\n" \
"v_cvt_pk_bf16_f32 v105, v106, v107\n" \
"v_cvt_pk_bf16_f32 v106, v108, v109\n" \
"v_mfma_f32_16x16x32_bf16 v[196:199], a[204:207], v[48:51], v[196:199]\n" \
"v_cvt_pk_bf16_f32 v107, v110, v111\n" \
"s_cbranch_vccz Lg2_%=\n" \
"s_nop 0\n" \
"buffer_store_dwordx4 v[104:107], v210, %[srdz], s60 offen offset:1024 sc1\n" \
"Lg2_%=:\n" \
"v_mfma_f32_16x16x32_bf16 v[196:199], a[208:211], v[56:59], v[196:199]\n" \
"v_fma_f32 v112, v112, v182, v183\n" \
"v_fma_f32 v113, v113, v182, v183\n" \
"v_mfma_f32_16x16x32_bf16 v[148:151], a[212:215], v[32:35], 0\n" \
"v_fma_f32 v114, v114, v182, v183\n" \
"v_fma_f32 v115, v115, v182, v183\n" \
"v_fma_f32 v116, v116, v182, v183\n" \
"v_fma_f32 v117, v117, v182, v183\n" \
"buffer_load_dwordx4 v212, %[srdt], s49 offen offset:1024 lds\n" \
"v_mfma_f32_16x16x32_bf16 v[148:151], a[216:219], v[40:43], v[148:151]\n" \
"v_fma_f32 v118, v118, v182, v183\n" \
"v_fma_f32 v119, v119, v182, v183\n" \
"v_cvt_pk_bf16_f32 v112, v112, v113\n" \
"v_mfma_f32_16x16x32_bf16 v[148:151], a[220:223], v[48:51], v[148:151]\n" \
"ds_read_b64_tr_b16 a[236:237], v215 offset:8704\n" \
"v_cvt_pk_bf16_f32 v113, v114, v115\n" \
"v_mfma_f32_16x16x32_bf16 v[148:151], a[224:227], v[56:59], v[148:151]\n" \
"v_cvt_pk_bf16_f32 v114, v116, v117\n" \
"v_cvt_pk_bf16_f32 v115, v118, v119\n" \
"v_mfma_f32_16x16x32_bf16 v[200:203], a[196:199], v[64:67], 0\n" \
"ds_read_b64_tr_b16 a[238:239], v215 offset:10752\n" \
"s_cbranch_vccz Lg3_%=\n" \
"s_nop 0\n" \
"buffer_store_dwordx4 v[112:115], v210, %[srdz], s60 offen offset:2048 sc1\n" \
"Lg3_%=:\n" \
"v_fma_f32 v120, v120, v182, v183\n" \
"v_fma_f32 v121, v121, v182, v183\n" \
"v_mfma_f32_16x16x32_bf16 v[200:203], a[200:203], v[72:75], v[200:203]\n" \
"v_fma_f32 v122, v122, v182, v183\n" \
"v_fma_f32 v123, v123, v182, v183\n" \
"v_fma_f32 v124, v124, v182, v183\n" \
"v_fma_f32 v125, v125, v182, v183\n" \
"v_mfma_f32_16x16x32_bf16 v[200:203], a[204:207], v[80:83], v[200:203]\n" \
"v_fma_f32 v126, v126, v182, v183\n" \
"v_fma_f32 v127, v127, v182, v183\n" \
"v_mfma_f32_16x16x32_bf16 v[200:203], a[208:211], v[88:91], v[200:203]\n" \
"v_cvt_pk_bf16_f32 v120, v120, v121\n" \
"v_cvt_pk_bf16_f32 v121, v122, v123\n" \
"v_mfma_f32_16x16x32_bf16 v[152:155], a[212:215], v[64:67], 0\n" \
"v_cvt_pk_bf16_f32 v122, v124, v125\n" \
"v_cvt_pk_bf16_f32 v123, v126, v127\n" \
"v_mfma_f32_16x16x32_bf16 v[152:155], a[216:219], v[72:75], v[152:155]\n" \
"s_cbranch_vccz Lg4_%=\n" \
"s_nop 0\n" \
"buffer_store_dwordx4 v[120:123], v210, %[srdz], s60 offen offset:3072 sc1\n" \
"Lg4_%=:\n" \
"v_mfma_f32_16x16x32_bf16 v[152:155], a[220:223], v[80:83], v[152:155]\n" \
"v_add_u32_e32 v220, 0x10000, v215\n" \
"v_mfma_f32_16x16x32_bf16 v[152:155], a[224:227], v[88:91], v[152:155]\n" \
"v_mov_b32_e64 v230, 1\n" \
"v_readlane_b32 s62, %[tlo], 19\n" \
"v_readlane_b32 s63, %[thi], 19\n" \
"v_and_b32_e32 v233, 0x380, v224\n" \
"v_lshlrev_b32_e64 v233, 2, v233\n" \
"buffer_load_dwordx4 v213, %[srdt], s49 offen offset:2048 lds\n" \
"v_mfma_f32_16x16x32_bf16 v[204:207], a[196:199], v[96:99], 0\n" \
"v_add_u32_e32 v221, 0x10000, v227\n" \
"v_mfma_f32_16x16x32_bf16 v[204:207], a[200:203], v[104:107], v[204:207]\n" \
"ds_read_b64_tr_b16 a[196:197], v215 offset:12800\n" \
"v_mfma_f32_16x16x32_bf16 v[204:207], a[204:207], v[112:115], v[204:207]\n" \
"ds_read_b64_tr_b16 a[198:199], v215 offset:14848\n" \
"v_mfma_f32_16x16x32_bf16 v[204:207], a[208:211], v[120:123], v[204:207]\n" \
"ds_read_b64_tr_b16 a[200:201], v227 offset:512\n" \
"v_mfma_f32_16x16x32_bf16 v[156:159], a[212:215], v[96:99], 0\n" \
"ds_read_b64_tr_b16 a[202:203], v227 offset:2560\n" \
"v_mfma_f32_16x16x32_bf16 v[156:159], a[216:219], v[104:107], v[156:159]\n" \
"ds_read_b64_tr_b16 a[204:205], v227 offset:4608\n" \
"v_mfma_f32_16x16x32_bf16 v[156:159], a[220:223], v[112:115], v[156:159]\n" \
"ds_read_b64_tr_b16 a[206:207], v227 offset:6656\n" \
"v_mfma_f32_16x16x32_bf16 v[156:159], a[224:227], v[120:123], v[156:159]\n" \
"s_waitcnt vmcnt(63) lgkmcnt(4)\n" \
"s_nop 0\n" \
"v_mfma_f32_16x16x32_bf16 v[160:163], a[228:231], v[0:3], 0\n" \
"ds_read_b64_tr_b16 a[208:209], v227 offset:8704\n" \
"v_mfma_f32_16x16x32_bf16 v[160:163], a[232:235], v[8:11], v[160:163]\n" \
"ds_read_b64_tr_b16 a[210:211], v227 offset:10752\n" \
"buffer_load_dwordx4 v214, %[srdt], s49 offen offset:3072 lds\n" \
"v_mfma_f32_16x16x32_bf16 v[164:167], a[228:231], v[32:35], 0\n" \
"ds_read_b64_tr_b16 a[212:213], v227 offset:12800\n" \
"v_mfma_f32_16x16x32_bf16 v[164:167], a[232:235], v[40:43], v[164:167]\n" \
"ds_read_b64_tr_b16 a[214:215], v227 offset:14848\n" \
"v_mfma_f32_16x16x32_bf16 v[168:171], a[228:231], v[64:67], 0\n" \
"v_mfma_f32_16x16x32_bf16 v[168:171], a[232:235], v[72:75], v[168:171]\n" \
"v_mfma_f32_16x16x32_bf16 v[172:175], a[228:231], v[96:99], 0\n" \
"ds_read_b64_tr_b16 a[216:217], v215 offset:16384\n" \
"v_mfma_f32_16x16x32_bf16 v[172:175], a[232:235], v[104:107], v[172:175]\n" \
"ds_read_b64_tr_b16 a[218:219], v215 offset:18432\n" \
"v_mfma_f32_16x16x32_bf16 v[160:163], a[236:239], v[16:19], v[160:163]\n" \
"ds_read_b64_tr_b16 a[220:221], v215 offset:20480\n" \
"v_mfma_f32_16x16x32_bf16 v[160:163], a[196:199], v[24:27], v[160:163]\n" \
"ds_read_b64_tr_b16 a[222:223], v215 offset:22528\n" \
"v_mfma_f32_16x16x32_bf16 v[164:167], a[236:239], v[48:51], v[164:167]\n" \
"v_mfma_f32_16x16x32_bf16 v[164:167], a[196:199], v[56:59], v[164:167]\n" \
"v_mfma_f32_16x16x32_bf16 v[168:171], a[236:239], v[80:83], v[168:171]\n" \
"s_waitcnt vmcnt(63) lgkmcnt(0)\n" \
"s_barrier\n" \
"v_mfma_f32_16x16x32_bf16 v[168:171], a[196:199], v[88:91], v[168:171]\n" \
"v_mfma_f32_16x16x32_bf16 v[172:175], a[236:239], v[112:115], v[172:175]\n" \
"ds_read_b64_tr_b16 a[224:225], v215 offset:24576\n" \
"v_mfma_f32_16x16x32_bf16 v[172:175], a[196:199], v[120:123], v[172:175]\n" \
"s_waitcnt vmcnt(63) lgkmcnt(5)\n" \
"s_nop 0\n" \
"v_mfma_f32_16x16x32_bf16 v[176:179], a[200:203], v[0:3], 0\n" \
"ds_read_b64_tr_b16 a[226:227], v215 offset:26624\n" \
"v_mfma_f32_16x16x32_bf16 v[176:179], a[204:207], v[8:11], v[176:179]\n" \
"ds_read_b64_tr_b16 a[228:229], v215 offset:28672\n" \
"s_add_u32 m0, %[m0b], 49152\n" \
"s_cmp_gt_u32 s48, 1\n" \
"s_cselect_b32 s49, 1, 2\n" \
"s_lshl_b32 s49, s49, 18\n" \
"s_or_b32 s49, s49, s59\n" \
"buffer_load_dwordx4 %[wo0], %[srdt], s49 offen offset:0 lds\n" \
"v_mfma_f32_16x16x32_bf16 v[180:183], a[200:203], v[32:35], 0\n" \
"ds_read_b64_tr_b16 a[230:231], v215 offset:30720\n" \
"v_mfma_f32_16x16x32_bf16 v[180:183], a[204:207], v[40:43], v[180:183]\n" \
"v_mfma_f32_16x16x32_bf16 v[184:187], a[200:203], v[64:67], 0\n" \
"v_mfma_f32_16x16x32_bf16 v[184:187], a[204:207], v[72:75], v[184:187]\n" \
"buffer_load_dwordx4 v212, %[srdt], s49 offen offset:1024 lds\n" \
"v_mfma_f32_16x16x32_bf16 v[188:191], a[200:203], v[96:99], 0\n" \
"ds_read_b64_tr_b16 a[232:233], v227 offset:16384\n" \
"v_mfma_f32_16x16x32_bf16 v[188:191], a[204:207], v[104:107], v[188:191]\n" \
"ds_read_b64_tr_b16 a[234:235], v227 offset:18432\n" \
"v_mfma_f32_16x16x32_bf16 v[176:179], a[208:211], v[16:19], v[176:179]\n" \
"ds_read_b64_tr_b16 a[236:237], v227 offset:20480\n" \
"v_mfma_f32_16x16x32_bf16 v[176:179], a[212:215], v[24:27], v[176:179]\n" \
"ds_read_b64_tr_b16 a[238:239], v227 offset:22528\n" \
"v_mfma_f32_16x16x32_bf16 v[180:183], a[208:211], v[48:51], v[180:183]\n" \
"v_mfma_f32_16x16x32_bf16 v[180:183], a[212:215], v[56:59], v[180:183]\n" \
"v_mfma_f32_16x16x32_bf16 v[184:187], a[208:211], v[80:83], v[184:187]\n" \
"v_mfma_f32_16x16x32_bf16 v[184:187], a[212:215], v[88:91], v[184:187]\n" \
"buffer_load_dwordx4 v213, %[srdt], s49 offen offset:2048 lds\n" \
"v_mfma_f32_16x16x32_bf16 v[188:191], a[208:211], v[112:115], v[188:191]\n" \
"ds_read_b64_tr_b16 a[196:197], v227 offset:24576\n" \
"v_mfma_f32_16x16x32_bf16 v[188:191], a[212:215], v[120:123], v[188:191]\n" \
"s_waitcnt vmcnt(63) lgkmcnt(5)\n" \
"s_nop 0\n" \
"v_mfma_f32_16x16x32_bf16 v[4:7], a[216:219], v[0:3], 0\n" \
"ds_read_b64_tr_b16 a[198:199], v227 offset:26624\n" \
"v_mfma_f32_16x16x32_bf16 v[4:7], a[220:223], v[8:11], v[4:7]\n" \
"ds_read_b64_tr_b16 a[200:201], v227 offset:28672\n" \
"v_mfma_f32_16x16x32_bf16 v[36:39], a[216:219], v[32:35], 0\n" \
"ds_read_b64_tr_b16 a[202:203], v227 offset:30720\n" \
"v_mfma_f32_16x16x32_bf16 v[36:39], a[220:223], v[40:43], v[36:39]\n" \
"v_mfma_f32_16x16x32_bf16 v[68:71], a[216:219], v[64:67], 0\n" \
"buffer_load_dwordx4 v214, %[srdt], s49 offen offset:3072 lds\n" \
"v_mfma_f32_16x16x32_bf16 v[68:71], a[220:223], v[72:75], v[68:71]\n" \
"v_mfma_f32_16x16x32_bf16 v[100:103], a[216:219], v[96:99], 0\n" \
"ds_read_b64_tr_b16 a[204:205], v215 offset:16896\n" \
"v_mfma_f32_16x16x32_bf16 v[100:103], a[220:223], v[104:107], v[100:103]\n" \
"ds_read_b64_tr_b16 a[206:207], v215 offset:18944\n" \
"v_mfma_f32_16x16x32_bf16 v[4:7], a[224:227], v[16:19], v[4:7]\n" \
"ds_read_b64_tr_b16 a[208:209], v215 offset:20992\n" \
"v_mfma_f32_16x16x32_bf16 v[4:7], a[228:231], v[24:27], v[4:7]\n" \
"ds_read_b64_tr_b16 a[210:211], v215 offset:23040\n" \
"v_mfma_f32_16x16x32_bf16 v[36:39], a[224:227], v[48:51], v[36:39]\n" \
"v_mfma_f32_16x16x32_bf16 v[36:39], a[228:231], v[56:59], v[36:39]\n" \
"v_mfma_f32_16x16x32_bf16 v[68:71], a[224:227], v[80:83], v[68:71]\n" \
"v_mfma_f32_16x16x32_bf16 v[68:71], a[228:231], v[88:91], v[68:71]\n" \
"v_mfma_f32_16x16x32_bf16 v[100:103], a[224:227], v[112:115], v[100:103]\n" \
"ds_read_b64_tr_b16 a[212:213], v215 offset:25088\n" \
"v_mfma_f32_16x16x32_bf16 v[100:103], a[228:231], v[120:123], v[100:103]\n" \
"s_waitcnt vmcnt(63) lgkmcnt(5)\n" \
"s_nop 0\n" \
"v_mfma_f32_16x16x32_bf16 v[12:15], a[232:235], v[0:3], 0\n" \
"ds_read_b64_tr_b16 a[214:215], v215 offset:27136\n" \
"s_cmp_gt_u32 s48, 0\n" \
"s_cselect_b32 s50, 0, 1\n" \
"s_mul_i32 s50, s50, 262144\n" \
"buffer_load_dwordx4 a[64:67], %[oq0], %[srdz], s50 offen offset:0 sc1\n" \
"v_mfma_f32_16x16x32_bf16 v[12:15], a[236:239], v[8:11], v[12:15]\n" \
"ds_read_b64_tr_b16 a[216:217], v215 offset:29184\n" \
"v_mfma_f32_16x16x32_bf16 v[44:47], a[232:235], v[32:35], 0\n" \
"ds_read_b64_tr_b16 a[218:219], v215 offset:31232\n" \
"buffer_load_dwordx4 a[68:71], %[oq0], %[srdz], s50 offen offset:1024 sc1\n" \
"v_mfma_f32_16x16x32_bf16 v[44:47], a[236:239], v[40:43], v[44:47]\n" \
"v_mfma_f32_16x16x32_bf16 v[76:79], a[232:235], v[64:67], 0\n" \
"buffer_load_dwordx4 a[80:83], v208, %[srdz], s50 offen offset:0 sc1\n" \
"v_mfma_f32_16x16x32_bf16 v[76:79], a[236:239], v[72:75], v[76:79]\n" \
"v_mfma_f32_16x16x32_bf16 v[108:111], a[232:235], v[96:99], 0\n" \
"ds_read_b64_tr_b16 a[220:221], v227 offset:16896\n" \
"buffer_load_dwordx4 a[84:87], v208, %[srdz], s50 offen offset:1024 sc1\n" \
"v_mfma_f32_16x16x32_bf16 v[108:111], a[236:239], v[104:107], v[108:111]\n" \
"ds_read_b64_tr_b16 a[222:223], v227 offset:18944\n" \
"v_mfma_f32_16x16x32_bf16 v[12:15], a[196:199], v[16:19], v[12:15]\n" \
"ds_read_b64_tr_b16 a[224:225], v227 offset:25088\n" \
"buffer_load_dwordx4 a[96:99], v209, %[srdz], s50 offen offset:0 sc1\n" \
"v_mfma_f32_16x16x32_bf16 v[12:15], a[200:203], v[24:27], v[12:15]\n" \
"ds_read_b64_tr_b16 a[226:227], v227 offset:27136\n" \
"v_mfma_f32_16x16x32_bf16 v[44:47], a[196:199], v[48:51], v[44:47]\n" \
"ds_read_b64_tr_b16 a[228:229], v227 offset:20992\n" \
"buffer_load_dwordx4 a[100:103], v209, %[srdz], s50 offen offset:1024 sc1\n" \
"v_mfma_f32_16x16x32_bf16 v[44:47], a[200:203], v[56:59], v[44:47]\n" \
"ds_read_b64_tr_b16 a[230:231], v227 offset:23040\n" \
"v_mfma_f32_16x16x32_bf16 v[76:79], a[196:199], v[80:83], v[76:79]\n" \
"ds_read_b64_tr_b16 a[232:233], v227 offset:29184\n" \
"buffer_load_dwordx4 a[112:115], v210, %[srdz], s50 offen offset:0 sc1\n" \
"v_mfma_f32_16x16x32_bf16 v[76:79], a[200:203], v[88:91], v[76:79]\n" \
"ds_read_b64_tr_b16 a[234:235], v227 offset:31232\n" \
"v_mfma_f32_16x16x32_bf16 v[108:111], a[196:199], v[112:115], v[108:111]\n" \
"ds_read_b64_tr_b16 a[236:237], v215 offset:32768\n" \
"buffer_load_dwordx4 a[116:119], v210, %[srdz], s50 offen offset:1024 sc1\n" \
"v_mfma_f32_16x16x32_bf16 v[108:111], a[200:203], v[120:123], v[108:111]\n" \
"s_waitcnt vmcnt(63) lgkmcnt(13)\n" \
"s_nop 0\n" \
"v_mfma_f32_16x16x32_bf16 v[20:23], a[204:207], v[0:3], 0\n" \
"ds_read_b64_tr_b16 a[238:239], v215 offset:34816\n" \
"buffer_load_dwordx4 a[72:75], %[oq0], %[srdz], s50 offen offset:2048 sc1\n" \
"v_mfma_f32_16x16x32_bf16 v[20:23], a[208:211], v[8:11], v[20:23]\n" \
"ds_read_b64_tr_b16 a[196:197], v215 offset:36864\n" \
"v_mfma_f32_16x16x32_bf16 v[52:55], a[204:207], v[32:35], 0\n" \
"ds_read_b64_tr_b16 a[198:199], v215 offset:38912\n" \
"buffer_load_dwordx4 a[76:79], %[oq0], %[srdz], s50 offen offset:3072 sc1\n" \
"v_mfma_f32_16x16x32_bf16 v[52:55], a[208:211], v[40:43], v[52:55]\n" \
"v_mfma_f32_16x16x32_bf16 v[84:87], a[204:207], v[64:67], 0\n" \
"s_waitcnt vmcnt(63) lgkmcnt(4)\n" \
"s_barrier\n" \
"v_mfma_f32_16x16x32_bf16 v[84:87], a[208:211], v[72:75], v[84:87]\n" \
"buffer_load_dwordx4 a[88:91], v208, %[srdz], s50 offen offset:2048 sc1\n" \
"v_mfma_f32_16x16x32_bf16 v[116:119], a[204:207], v[96:99], 0\n" \
"ds_read_b64_tr_b16 a[200:201], v215 offset:40960\n" \
"v_mfma_f32_16x16x32_bf16 v[116:119], a[208:211], v[104:107], v[116:119]\n" \
"ds_read_b64_tr_b16 a[202:203], v215 offset:43008\n" \
"buffer_load_dwordx4 a[92:95], v208, %[srdz], s50 offen offset:3072 sc1\n" \
"v_mfma_f32_16x16x32_bf16 v[20:23], a[212:215], v[16:19], v[20:23]\n" \
"ds_read_b64_tr_b16 a[204:205], v215 offset:45056\n" \
"v_mfma_f32_16x16x32_bf16 v[20:23], a[216:219], v[24:27], v[20:23]\n" \
"ds_read_b64_tr_b16 a[206:207], v215 offset:47104\n" \
"buffer_load_dwordx4 a[104:107], v209, %[srdz], s50 offen offset:2048 sc1\n" \
"v_mfma_f32_16x16x32_bf16 v[52:55], a[212:215], v[48:51], v[52:55]\n" \
"v_mfma_f32_16x16x32_bf16 v[52:55], a[216:219], v[56:59], v[52:55]\n" \
"buffer_load_dwordx4 a[108:111], v209, %[srdz], s50 offen offset:3072 sc1\n" \
"v_mfma_f32_16x16x32_bf16 v[84:87], a[212:215], v[80:83], v[84:87]\n" \
"v_mfma_f32_16x16x32_bf16 v[84:87], a[216:219], v[88:91], v[84:87]\n" \
"buffer_load_dwordx4 a[120:123], v210, %[srdz], s50 offen offset:2048 sc1\n" \
"v_mfma_f32_16x16x32_bf16 v[116:119], a[212:215], v[112:115], v[116:119]\n" \
"v_mfma_f32_16x16x32_bf16 v[116:119], a[216:219], v[120:123], v[116:119]\n" \
"s_waitcnt vmcnt(63) lgkmcnt(10)\n" \
"s_nop 0\n" \
"v_mfma_f32_16x16x32_bf16 v[28:31], a[220:223], v[0:3], 0\n" \
"buffer_load_dwordx4 a[124:127], v210, %[srdz], s50 offen offset:3072 sc1\n" \
"v_mfma_f32_16x16x32_bf16 v[28:31], a[228:231], v[8:11], v[28:31]\n" \
"s_sub_u32 s61, s48, 5\n" \
"s_cmp_lt_u32 s61, 3\n" \
"s_cselect_b64 vcc, -1, 0\n" \
"s_nop 0\n" \
"v_mfma_f32_16x16x32_bf16 v[60:63], a[220:223], v[32:35], 0\n" \
"s_cbranch_vccz Lg5_%=\n" \
"s_nop 0\n" \
"buffer_store_dwordx4 v[0:3], %[oq0], %[srdz], s60 offen offset:0 sc1\n" \
"Lg5_%=:\n" \
"v_mfma_f32_16x16x32_bf16 v[60:63], a[228:231], v[40:43], v[60:63]\n" \
"v_mfma_f32_16x16x32_bf16 v[92:95], a[220:223], v[64:67], 0\n" \
"s_cmp_gt_u32 s48, 1\n" \
"s_cselect_b32 s50, 1, 2\n" \
"s_mul_i32 s50, s50, 262144\n" \
"buffer_load_dwordx4 a[0:3], %[oq0], %[srdz], s50 offen offset:0 sc1\n" \
"v_mfma_f32_16x16x32_bf16 v[92:95], a[228:231], v[72:75], v[92:95]\n" \
"v_mfma_f32_16x16x32_bf16 v[124:127], a[220:223], v[96:99], 0\n" \
"ds_read_b64_tr_b16 a[208:209], v227 offset:32768\n" \
"v_mfma_f32_16x16x32_bf16 v[124:127], a[228:231], v[104:107], v[124:127]\n" \
"ds_read_b64_tr_b16 a[210:211], v227 offset:34816\n" \
"s_waitcnt vmcnt(15) lgkmcnt(6)\n" \
"s_nop 0\n" \
"v_mfma_f32_16x16x32_bf16 v[128:131], a[236:239], a[64:67], v[192:195]\n" \
"ds_read_b64_tr_b16 a[212:213], v227 offset:36864\n" \
"v_mfma_f32_16x16x32_bf16 v[128:131], a[196:199], a[68:71], v[128:131]\n" \
"ds_read_b64_tr_b16 a[214:215], v227 offset:38912\n" \
"buffer_load_dwordx4 a[4:7], %[oq0], %[srdz], s50 offen offset:1024 sc1\n" \
"s_waitcnt vmcnt(14) lgkmcnt(15)\n" \
"s_nop 0\n" \
"v_mfma_f32_16x16x32_bf16 v[132:135], a[236:239], a[80:83], v[196:199]\n" \
"v_mfma_f32_16x16x32_bf16 v[132:135], a[196:199], a[84:87], v[132:135]\n" \
"buffer_load_dwordx4 a[8:11], %[oq0], %[srdz], s50 offen offset:2048 sc1\n" \
"s_waitcnt vmcnt(13) lgkmcnt(15)\n" \
"s_nop 0\n" \
"v_mfma_f32_16x16x32_bf16 v[136:139], a[236:239], a[96:99], v[200:203]\n" \
"v_mfma_f32_16x16x32_bf16 v[136:139], a[196:199], a[100:103], v[136:139]\n" \
"s_cbranch_vccz Lg6_%=\n" \
"s_nop 0\n" \
"buffer_store_dwordx4 v[8:11], %[oq0], %[srdz], s60 offen offset:1024 sc1\n" \
"Lg6_%=:\n" \
"s_waitcnt vmcnt(13) lgkmcnt(15)\n" \
"s_nop 0\n" \
"s_bitcmp1_b32 0xe0, s48\n" \
"s_cbranch_scc1 Lcw2_%=\n" \
"s_waitcnt vmcnt(11)\n" \
"Lcw2_%=:\n" \
"v_mfma_f32_16x16x32_bf16 v[140:143], a[236:239], a[112:115], v[204:207]\n" \
"ds_read_b64_tr_b16 a[216:217], v227 offset:40960\n" \
"v_mfma_f32_16x16x32_bf16 v[140:143], a[196:199], a[116:119], v[140:143]\n" \
"s_waitcnt vmcnt(11) lgkmcnt(5)\n" \
"s_nop 0\n" \
"s_bitcmp1_b32 0xe0, s48\n" \
"s_cbranch_scc1 Lcw3_%=\n" \
"s_waitcnt vmcnt(9)\n" \
"Lcw3_%=:\n" \
"v_mfma_f32_16x16x32_bf16 v[128:131], a[200:203], a[72:75], v[128:131]\n" \
"ds_read_b64_tr_b16 a[218:219], v227 offset:43008\n" \
"v_mfma_f32_16x16x32_bf16 v[128:131], a[204:207], a[76:79], v[128:131]\n" \
"ds_read_b64_tr_b16 a[220:221], v227 offset:45056\n" \
"buffer_load_dwordx4 a[12:15], %[oq0], %[srdz], s50 offen offset:3072 sc1\n" \
"s_waitcnt vmcnt(10) lgkmcnt(15)\n" \
"s_nop 0\n" \
"s_bitcmp1_b32 0xe0, s48\n" \
"s_cbranch_scc1 Lcw4_%=\n" \
"s_waitcnt vmcnt(8)\n" \
"Lcw4_%=:\n" \
"v_mfma_f32_16x16x32_bf16 v[132:135], a[200:203], a[88:91], v[132:135]\n" \
"ds_read_b64_tr_b16 a[222:223], v227 offset:47104\n" \
"v_mfma_f32_16x16x32_bf16 v[132:135], a[204:207], a[92:95], v[132:135]\n" \
"buffer_load_dwordx4 a[16:19], v208, %[srdz], s50 offen offset:0 sc1\n" \
"s_waitcnt vmcnt(7) lgkmcnt(14)\n" \
"s_nop 0\n" \
"s_bitcmp1_b32 0xe0, s48\n" \
"s_cbranch_scc1 Lcw5_%=\n" \
"s_waitcnt vmcnt(5)\n" \
"Lcw5_%=:\n" \
"v_mfma_f32_16x16x32_bf16 v[136:139], a[200:203], a[104:107], v[136:139]\n" \
"v_mfma_f32_16x16x32_bf16 v[136:139], a[204:207], a[108:111], v[136:139]\n" \
"v_mfma_f32_16x16x32_bf16 v[140:143], a[200:203], a[120:123], v[140:143]\n" \
"ds_read_b64_tr_b16 a[228:229], v215 offset:33280\n" \
"v_mfma_f32_16x16x32_bf16 v[140:143], a[204:207], a[124:127], v[140:143]\n" \
"ds_read_b64_tr_b16 a[230:231], v215 offset:35328\n" \
"v_mfma_f32_16x16x32_bf16 v[28:31], a[224:227], v[16:19], v[28:31]\n" \
"v_mfma_f32_16x16x32_bf16 v[28:31], a[232:235], v[24:27], v[28:31]\n" \
"s_cbranch_vccz Lg7_%=\n" \
"s_nop 0\n" \
"buffer_store_dwordx4 v[16:19], %[oq0], %[srdz], s60 offen offset:2048 sc1\n" \
"Lg7_%=:\n" \
"v_mfma_f32_16x16x32_bf16 v[60:63], a[224:227], v[48:51], v[60:63]\n" \
"v_mfma_f32_16x16x32_bf16 v[60:63], a[232:235], v[56:59], v[60:63]\n" \
"buffer_load_dwordx4 a[20:23], v208, %[srdz], s50 offen offset:1024 sc1\n" \
"v_mfma_f32_16x16x32_bf16 v[92:95], a[224:227], v[80:83], v[92:95]\n" \
"v_mfma_f32_16x16x32_bf16 v[92:95], a[232:235], v[88:91], v[92:95]\n" \
"v_cmp_u_f32_e64 s[44:45], v128, v132\n" \
"buffer_load_dwordx4 a[24:27], v208, %[srdz], s50 offen offset:2048 sc1\n" \
"v_mfma_f32_16x16x32_bf16 v[124:127], a[224:227], v[112:115], v[124:127]\n" \
"v_mfma_f32_16x16x32_bf16 v[124:127], a[232:235], v[120:123], v[124:127]\n" \
"v_cmp_u_f32_e64 s[56:57], v136, v140\n" \
"s_or_b64 s[56:57], s[56:57], s[44:45]\n" \
"s_cbranch_scc1 Lsp1_%=\n" \
"Lbk1_%=:\n" \
"s_waitcnt vmcnt(63) lgkmcnt(2)\n" \
"s_nop 0\n" \
"v_mfma_f32_16x16x32_bf16 v[144:147], a[208:211], a[64:67], v[144:147]\n" \
"ds_read_b64_tr_b16 a[224:225], v215 offset:37376\n" \
"v_mfma_f32_16x16x32_bf16 v[144:147], a[212:215], a[68:71], v[144:147]\n" \
"ds_read_b64_tr_b16 a[226:227], v215 offset:39424\n" \
"buffer_load_dwordx4 a[28:31], v208, %[srdz], s50 offen offset:3072 sc1\n" \
"v_mfma_f32_16x16x32_bf16 v[148:151], a[208:211], a[80:83], v[148:151]\n" \
"ds_read_b64_tr_b16 a[196:197], v215 offset:41472\n" \
"v_mfma_f32_16x16x32_bf16 v[148:151], a[212:215], a[84:87], v[148:151]\n" \
"ds_read_b64_tr_b16 a[198:199], v215 offset:43520\n" \
"v_mfma_f32_16x16x32_bf16 v[152:155], a[208:211], a[96:99], v[152:155]\n" \
"ds_read_b64_tr_b16 a[200:201], v215 offset:45568\n" \
"s_cbranch_vccz Lg8_%=\n" \
"s_nop 0\n" \
"buffer_store_dwordx4 v[24:27], %[oq0], %[srdz], s60 offen offset:3072 sc1\n" \
"Lg8_%=:\n" \
"v_mfma_f32_16x16x32_bf16 v[152:155], a[212:215], a[100:103], v[152:155]\n" \
"ds_read_b64_tr_b16 a[202:203], v215 offset:47616\n" \
"v_mfma_f32_16x16x32_bf16 v[156:159], a[208:211], a[112:115], v[156:159]\n" \
"ds_read_b64_tr_b16 a[204:205], v227 offset:33280\n" \
"buffer_load_dwordx4 a[32:35], v209, %[srdz], s50 offen offset:0 sc1\n" \
"v_mfma_f32_16x16x32_bf16 v[156:159], a[212:215], a[116:119], v[156:159]\n" \
"ds_read_b64_tr_b16 a[206:207], v227 offset:35328\n" \
"v_mfma_f32_16x16x32_bf16 v[144:147], a[216:219], a[72:75], v[144:147]\n" \
"ds_read_b64_tr_b16 a[232:233], v227 offset:37376\n" \
"v_mfma_f32_16x16x32_bf16 v[144:147], a[220:223], a[76:79], v[144:147]\n" \
"ds_read_b64_tr_b16 a[234:235], v227 offset:39424\n" \
"v_mfma_f32_16x16x32_bf16 v[148:151], a[216:219], a[88:91], v[148:151]\n" \
"buffer_load_dwordx4 a[36:39], v209, %[srdz], s50 offen offset:1024 sc1\n" \
"v_mfma_f32_16x16x32_bf16 v[148:151], a[220:223], a[92:95], v[148:151]\n" \
"v_mfma_f32_16x16x32_bf16 v[152:155], a[216:219], a[104:107], v[152:155]\n" \
"buffer_load_dwordx4 a[40:43], v209, %[srdz], s50 offen offset:2048 sc1\n" \
"v_mfma_f32_16x16x32_bf16 v[152:155], a[220:223], a[108:111], v[152:155]\n" \
"v_mfma_f32_16x16x32_bf16 v[156:159], a[216:219], a[120:123], v[156:159]\n" \
"ds_read_b64_tr_b16 a[236:237], v227 offset:41472\n" \
"v_mfma_f32_16x16x32_bf16 v[156:159], a[220:223], a[124:127], v[156:159]\n" \
"s_waitcnt vmcnt(63) lgkmcnt(5)\n" \
"s_nop 0\n" \
"v_mfma_f32_16x16x32_bf16 v[160:163], a[228:231], a[64:67], v[160:163]\n" \
"ds_read_b64_tr_b16 a[238:239], v227 offset:43520\n" \
"s_cbranch_vccz Lg9_%=\n" \
"s_nop 0\n" \
"buffer_store_dwordx4 v[32:35], v208, %[srdz], s60 offen offset:0 sc1\n" \
"Lg9_%=:\n" \
"v_mfma_f32_16x16x32_bf16 v[160:163], a[224:227], a[68:71], v[160:163]\n" \
"ds_read_b64_tr_b16 a[208:209], v227 offset:45568\n" \
"v_mfma_f32_16x16x32_bf16 v[164:167], a[228:231], a[80:83], v[164:167]\n" \
"ds_read_b64_tr_b16 a[210:211], v227 offset:47616\n" \
"buffer_load_dwordx4 a[44:47], v209, %[srdz], s50 offen offset:3072 sc1\n" \
"v_mfma_f32_16x16x32_bf16 v[164:167], a[224:227], a[84:87], v[164:167]\n" \
"v_mfma_f32_16x16x32_bf16 v[168:171], a[228:231], a[96:99], v[168:171]\n" \
"buffer_load_dwordx4 a[48:51], v210, %[srdz], s50 offen offset:0 sc1\n" \
"v_mfma_f32_16x16x32_bf16 v[168:171], a[224:227], a[100:103], v[168:171]\n" \
"v_mfma_f32_16x16x32_bf16 v[172:175], a[228:231], a[112:115], v[172:175]\n" \
"v_mfma_f32_16x16x32_bf16 v[172:175], a[224:227], a[116:119], v[172:175]\n" \
"buffer_load_dwordx4 a[52:55], v210, %[srdz], s50 offen offset:1024 sc1\n" \
"v_mfma_f32_16x16x32_bf16 v[160:163], a[196:199], a[72:75], v[160:163]\n" \
"v_mfma_f32_16x16x32_bf16 v[160:163], a[200:203], a[76:79], v[160:163]\n" \
"v_mfma_f32_16x16x32_bf16 v[164:167], a[196:199], a[88:91], v[164:167]\n" \
"v_mfma_f32_16x16x32_bf16 v[164:167], a[200:203], a[92:95], v[164:167]\n" \
"buffer_load_dwordx4 a[56:59], v210, %[srdz], s50 offen offset:2048 sc1\n" \
"v_mfma_f32_16x16x32_bf16 v[168:171], a[196:199], a[104:107], v[168:171]\n" \
"s_waitcnt vmcnt(55) lgkmcnt(0)\n" \
"s_bitcmp1_b32 0x1f, s48\n" \
"s_cbranch_scc1 Lcw6_%=\n" \
"s_waitcnt vmcnt(44)\n" \
"s_bitcmp1_b32 0xe0, s48\n" \
"s_cbranch_scc1 Lcw6_%=\n" \
"s_waitcnt vmcnt(39)\n" \
"Lcw6_%=:\n" \
"s_barrier\n" \
"s_nop 0\n" \
"v_mfma_f32_16x16x32_bf16 v[168:171], a[200:203], a[108:111], v[168:171]\n" \
"ds_read_b64_tr_b16 a[212:213], v220 offset:32768\n" \
"s_cbranch_vccz Lg10_%=\n" \
"s_nop 0\n" \
"buffer_store_dwordx4 v[40:43], v208, %[srdz], s60 offen offset:1024 sc1\n" \
"Lg10_%=:\n" \
"v_mfma_f32_16x16x32_bf16 v[172:175], a[196:199], a[120:123], v[172:175]\n" \
"ds_read_b64_tr_b16 a[214:215], v220 offset:34816\n" \
"v_mfma_f32_16x16x32_bf16 v[172:175], a[200:203], a[124:127], v[172:175]\n" \
"s_waitcnt vmcnt(63) lgkmcnt(2)\n" \
"s_nop 0\n" \
"v_mfma_f32_16x16x32_bf16 v[176:179], a[204:207], a[64:67], v[176:179]\n" \
"ds_read_b64_tr_b16 a[216:217], v220 offset:36864\n" \
"v_mfma_f32_16x16x32_bf16 v[176:179], a[232:235], a[68:71], v[176:179]\n" \
"ds_read_b64_tr_b16 a[218:219], v220 offset:38912\n" \
"v_mfma_f32_16x16x32_bf16 v[180:183], a[204:207], a[80:83], v[180:183]\n" \
"ds_read_b64_tr_b16 a[220:221], v220 offset:40960\n" \
"buffer_load_dwordx4 a[60:63], v210, %[srdz], s50 offen offset:3072 sc1\n" \
"v_mfma_f32_16x16x32_bf16 v[180:183], a[232:235], a[84:87], v[180:183]\n" \
"ds_read_b64_tr_b16 a[222:223], v220 offset:43008\n" \
"v_mfma_f32_16x16x32_bf16 v[184:187], a[204:207], a[96:99], v[184:187]\n" \
"ds_read_b64_tr_b16 a[228:229], v220 offset:45056\n" \
"v_mfma_f32_16x16x32_bf16 v[184:187], a[232:235], a[100:103], v[184:187]\n" \
"ds_read_b64_tr_b16 a[230:231], v220 offset:47104\n" \
"v_mfma_f32_16x16x32_bf16 v[188:191], a[204:207], a[112:115], v[188:191]\n" \
"ds_read_b64_tr_b16 a[224:225], v221 offset:32768\n" \
"v_mfma_f32_16x16x32_bf16 v[188:191], a[232:235], a[116:119], v[188:191]\n" \
"ds_read_b64_tr_b16 a[226:227], v221 offset:34816\n" \
"s_cbranch_vccz Lg11_%=\n" \
"s_nop 0\n" \
"buffer_store_dwordx4 v[48:51], v208, %[srdz], s60 offen offset:2048 sc1\n" \
"Lg11_%=:\n" \
"v_mfma_f32_16x16x32_bf16 v[176:179], a[236:239], a[72:75], v[176:179]\n" \
"ds_read_b64_tr_b16 a[196:197], v221 offset:36864\n" \
"v_mfma_f32_16x16x32_bf16 v[176:179], a[208:211], a[76:79], v[176:179]\n" \
"ds_read_b64_tr_b16 a[198:199], v221 offset:38912\n" \
"v_mfma_f32_16x16x32_bf16 v[180:183], a[236:239], a[88:91], v[180:183]\n" \
"s_cmp_gt_u32 s48, 2\n" \
"s_cselect_b32 s50, 2, 3\n" \
"s_mul_i32 s50, s50, 262144\n" \
"buffer_load_dwordx4 a[128:131], %[oq0], %[srdz], s50 offen offset:0 sc1\n" \
"v_mfma_f32_16x16x32_bf16 v[180:183], a[208:211], a[92:95], v[180:183]\n" \
"v_mfma_f32_16x16x32_bf16 v[184:187], a[236:239], a[104:107], v[184:187]\n" \
"v_mfma_f32_16x16x32_bf16 v[184:187], a[208:211], a[108:111], v[184:187]\n" \
"v_mfma_f32_16x16x32_bf16 v[188:191], a[236:239], a[120:123], v[188:191]\n" \
"ds_read_b64_tr_b16 a[200:201], v221 offset:40960\n" \
"v_mfma_f32_16x16x32_bf16 v[188:191], a[208:211], a[124:127], v[188:191]\n" \
"s_waitcnt vmcnt(63) lgkmcnt(5)\n" \
"s_nop 0\n" \
"v_mfma_f32_16x16x32_bf16 v[4:7], a[212:215], a[64:67], v[4:7]\n" \
"ds_read_b64_tr_b16 a[202:203], v221 offset:43008\n" \
"v_mfma_f32_16x16x32_bf16 v[4:7], a[216:219], a[68:71], v[4:7]\n" \
"ds_read_b64_tr_b16 a[204:205], v221 offset:45056\n" \
"s_add_u32 m0, %[m0b], 0\n" \
"s_cmp_gt_u32 s48, 2\n" \
"s_cselect_b32 s49, 2, 3\n" \
"s_lshl_b32 s49, s49, 18\n" \
"s_or_b32 s49, s49, s58\n" \
"s_nop 0\n" \
"buffer_load_dwordx4 %[wo0], %[srdt], s49 offen offset:0 lds\n" \
"v_mfma_f32_16x16x32_bf16 v[36:39], a[212:215], a[80:83], v[36:39]\n" \
"ds_read_b64_tr_b16 a[206:207], v221 offset:47104\n" \
"v_mfma_f32_16x16x32_bf16 v[36:39], a[216:219], a[84:87], v[36:39]\n" \
"s_cbranch_vccz Lg12_%=\n" \
"s_nop 0\n" \
"buffer_store_dwordx4 v[56:59], v208, %[srdz], s60 offen offset:3072 sc1\n" \
"Lg12_%=:\n" \
"v_mfma_f32_16x16x32_bf16 v[68:71], a[212:215], a[96:99], v[68:71]\n" \
"v_mfma_f32_16x16x32_bf16 v[68:71], a[216:219], a[100:103], v[68:71]\n" \
"buffer_load_dwordx4 a[132:135], %[oq0], %[srdz], s50 offen offset:1024 sc1\n" \
"v_mfma_f32_16x16x32_bf16 v[100:103], a[212:215], a[112:115], v[100:103]\n" \
"ds_read_b64_tr_b16 a[232:233], v220 offset:33280\n" \
"v_mfma_f32_16x16x32_bf16 v[100:103], a[216:219], a[116:119], v[100:103]\n" \
"ds_read_b64_tr_b16 a[234:235], v220 offset:35328\n" \
"v_mfma_f32_16x16x32_bf16 v[4:7], a[220:223], a[72:75], v[4:7]\n" \
"ds_read_b64_tr_b16 a[236:237], v220 offset:37376\n" \
"v_mfma_f32_16x16x32_bf16 v[4:7], a[228:231], a[76:79], v[4:7]\n" \
"ds_read_b64_tr_b16 a[238:239], v220 offset:39424\n" \
"buffer_load_dwordx4 a[136:139], %[oq0], %[srdz], s50 offen offset:2048 sc1\n" \
"v_mfma_f32_16x16x32_bf16 v[36:39], a[220:223], a[88:91], v[36:39]\n" \
"v_mfma_f32_16x16x32_bf16 v[36:39], a[228:231], a[92:95], v[36:39]\n" \
"v_mfma_f32_16x16x32_bf16 v[68:71], a[220:223], a[104:107], v[68:71]\n" \
"v_mfma_f32_16x16x32_bf16 v[68:71], a[228:231], a[108:111], v[68:71]\n" \
"buffer_load_dwordx4 a[140:143], %[oq0], %[srdz], s50 offen offset:3072 sc1\n" \
"v_mfma_f32_16x16x32_bf16 v[100:103], a[220:223], a[120:123], v[100:103]\n" \
"ds_read_b64_tr_b16 a[208:209], v220 offset:41472\n" \
"v_mfma_f32_16x16x32_bf16 v[100:103], a[228:231], a[124:127], v[100:103]\n" \
"s_waitcnt vmcnt(63) lgkmcnt(5)\n" \
"s_nop 0\n" \
"v_mfma_f32_16x16x32_bf16 v[12:15], a[224:227], a[64:67], v[12:15]\n" \
".if . - adaln_body != 45056\n" \
".error \"size model\"\n" \
".endif\n" \
"Lk10_%=:\n" \
"s_cbranch_execz Lk11_%=\n" \
"s_nop 0\n" \
"ds_read_b64_tr_b16 a[210:211], v220 offset:43520\n" \
"s_cbranch_vccz Lg13_%=\n" \
"s_nop 0\n" \
"buffer_store_dwordx4 v[64:67], v209, %[srdz], s60 offen offset:0 sc1\n" \
"Lg13_%=:\n" \
"v_mfma_f32_16x16x32_bf16 v[12:15], a[196:199], a[68:71], v[12:15]\n" \
"ds_read_b64_tr_b16 a[212:213], v220 offset:45568\n" \
"v_mfma_f32_16x16x32_bf16 v[44:47], a[224:227], a[80:83], v[44:47]\n" \
"ds_read_b64_tr_b16 a[214:215], v220 offset:47616\n" \
"buffer_load_dwordx4 v212, %[srdt], s49 offen offset:1024 lds\n" \
"v_mfma_f32_16x16x32_bf16 v[44:47], a[196:199], a[84:87], v[44:47]\n" \
"v_mfma_f32_16x16x32_bf16 v[76:79], a[224:227], a[96:99], v[76:79]\n" \
"v_mfma_f32_16x16x32_bf16 v[76:79], a[196:199], a[100:103], v[76:79]\n" \
"buffer_load_dwordx4 a[144:147], v208, %[srdz], s50 offen offset:0 sc1\n" \
"v_mfma_f32_16x16x32_bf16 v[108:111], a[224:227], a[112:115], v[108:111]\n" \
"ds_read_b64_tr_b16 a[216:217], v221 offset:33280\n" \
"v_mfma_f32_16x16x32_bf16 v[108:111], a[196:199], a[116:119], v[108:111]\n" \
"ds_read_b64_tr_b16 a[218:219], v221 offset:35328\n" \
"v_mfma_f32_16x16x32_bf16 v[12:15], a[200:203], a[72:75], v[12:15]\n" \
"ds_read_b64_tr_b16 a[220:221], v221 offset:41472\n" \
"v_mfma_f32_16x16x32_bf16 v[12:15], a[204:207], a[76:79], v[12:15]\n" \
"ds_read_b64_tr_b16 a[222:223], v221 offset:43520\n" \
"v_mfma_f32_16x16x32_bf16 v[44:47], a[200:203], a[88:91], v[44:47]\n" \
"ds_read_b64_tr_b16 a[228:229], v221 offset:37376\n" \
"s_cbranch_vccz Lg14_%=\n" \
"s_nop 0\n" \
"buffer_store_dwordx4 v[72:75], v209, %[srdz], s60 offen offset:1024 sc1\n" \
"Lg14_%=:\n" \
"v_mfma_f32_16x16x32_bf16 v[44:47], a[204:207], a[92:95], v[44:47]\n" \
"ds_read_b64_tr_b16 a[230:231], v221 offset:39424\n" \
"v_mfma_f32_16x16x32_bf16 v[76:79], a[200:203], a[104:107], v[76:79]\n" \
"ds_read_b64_tr_b16 a[224:225], v221 offset:45568\n" \
"v_mfma_f32_16x16x32_bf16 v[76:79], a[204:207], a[108:111], v[76:79]\n" \
"ds_read_b64_tr_b16 a[226:227], v221 offset:47616\n" \
"v_mfma_f32_16x16x32_bf16 v[108:111], a[200:203], a[120:123], v[108:111]\n" \
"buffer_load_dwordx4 a[148:151], v208, %[srdz], s50 offen offset:1024 sc1\n" \
"v_mfma_f32_16x16x32_bf16 v[108:111], a[204:207], a[124:127], v[108:111]\n" \
"s_waitcnt vmcnt(63) lgkmcnt(12)\n" \
"s_nop 0\n" \
"v_mfma_f32_16x16x32_bf16 v[20:23], a[232:235], a[64:67], v[20:23]\n" \
"v_mfma_f32_16x16x32_bf16 v[20:23], a[236:239], a[68:71], v[20:23]\n" \
"buffer_load_dwordx4 v213, %[srdt], s49 offen offset:2048 lds\n" \
"v_mfma_f32_16x16x32_bf16 v[52:55], a[232:235], a[80:83], v[52:55]\n" \
"v_mfma_f32_16x16x32_bf16 v[52:55], a[236:239], a[84:87], v[52:55]\n" \
"buffer_load_dwordx4 a[152:155], v208, %[srdz], s50 offen offset:2048 sc1\n" \
"v_mfma_f32_16x16x32_bf16 v[84:87], a[232:235], a[96:99], v[84:87]\n" \
"s_waitcnt vmcnt(56) lgkmcnt(0)\n" \
"s_nop 0\n" \
"s_bitcmp1_b32 0xe0, s48\n" \
"s_cbranch_scc1 Lcw7_%=\n" \
"s_waitcnt vmcnt(46)\n" \
"Lcw7_%=:\n" \
"s_barrier\n" \
"s_nop 0\n" \
"v_mfma_f32_16x16x32_bf16 v[84:87], a[236:239], a[100:103], v[84:87]\n" \
"ds_read_b64_tr_b16 a[196:197], v220 offset:49152\n" \
"v_mfma_f32_16x16x32_bf16 v[116:119], a[232:235], a[112:115], v[116:119]\n" \
"ds_read_b64_tr_b16 a[198:199], v220 offset:51200\n" \
"s_cbranch_vccz Lg15_%=\n" \
"s_nop 0\n" \
"buffer_store_dwordx4 v[80:83], v209, %[srdz], s60 offen offset:2048 sc1\n" \
"Lg15_%=:\n" \
"v_mfma_f32_16x16x32_bf16 v[116:119], a[236:239], a[116:119], v[116:119]\n" \
"ds_read_b64_tr_b16 a[200:201], v220 offset:53248\n" \
"v_mfma_f32_16x16x32_bf16 v[20:23], a[208:211], a[72:75], v[20:23]\n" \
"ds_read_b64_tr_b16 a[202:203], v220 offset:55296\n" \
"v_mfma_f32_16x16x32_bf16 v[20:23], a[212:215], a[76:79], v[20:23]\n" \
"ds_read_b64_tr_b16 a[204:205], v220 offset:57344\n" \
"v_mfma_f32_16x16x32_bf16 v[52:55], a[208:211], a[88:91], v[52:55]\n" \
"ds_read_b64_tr_b16 a[206:207], v220 offset:59392\n" \
"v_mfma_f32_16x16x32_bf16 v[52:55], a[212:215], a[92:95], v[52:55]\n" \
"ds_read_b64_tr_b16 a[232:233], v220 offset:61440\n" \
"v_mfma_f32_16x16x32_bf16 v[84:87], a[208:211], a[104:107], v[84:87]\n" \
"ds_read_b64_tr_b16 a[234:235], v220 offset:63488\n" \
"v_mfma_f32_16x16x32_bf16 v[84:87], a[212:215], a[108:111], v[84:87]\n" \
"buffer_load_dwordx4 a[156:159], v208, %[srdz], s50 offen offset:3072 sc1\n" \
"v_mfma_f32_16x16x32_bf16 v[116:119], a[208:211], a[120:123], v[116:119]\n" \
"v_mfma_f32_16x16x32_bf16 v[116:119], a[212:215], a[124:127], v[116:119]\n" \
"s_waitcnt vmcnt(19) lgkmcnt(4)\n" \
"s_nop 0\n" \
"s_bitcmp1_b32 0xe0, s48\n" \
"s_cbranch_scc1 Lcw8_%=\n" \
"s_waitcnt vmcnt(13)\n" \
"Lcw8_%=:\n" \
"v_mfma_f32_16x16x32_bf16 v[28:31], a[216:219], a[64:67], v[28:31]\n" \
"v_mfma_f32_16x16x32_bf16 v[28:31], a[228:231], a[68:71], v[28:31]\n" \
"s_cbranch_vccz Lg16_%=\n" \
"s_nop 0\n" \
"buffer_store_dwordx4 v[88:91], v209, %[srdz], s60 offen offset:3072 sc1\n" \
"Lg16_%=:\n" \
"v_mfma_f32_16x16x32_bf16 v[60:63], a[216:219], a[80:83], v[60:63]\n" \
"v_mfma_f32_16x16x32_bf16 v[60:63], a[228:231], a[84:87], v[60:63]\n" \
"buffer_load_dwordx4 v214, %[srdt], s49 offen offset:3072 lds\n" \
"v_mfma_f32_16x16x32_bf16 v[92:95], a[216:219], a[96:99], v[92:95]\n" \
"v_mfma_f32_16x16x32_bf16 v[92:95], a[228:231], a[100:103], v[92:95]\n" \
"buffer_load_dwordx4 a[160:163], v209, %[srdz], s50 offen offset:0 sc1\n" \
"v_mfma_f32_16x16x32_bf16 v[124:127], a[216:219], a[112:115], v[124:127]\n" \
"ds_read_b64_tr_b16 a[236:237], v221 offset:49152\n" \
"v_mfma_f32_16x16x32_bf16 v[124:127], a[228:231], a[116:119], v[124:127]\n" \
"ds_read_b64_tr_b16 a[238:239], v221 offset:51200\n" \
"v_mfma_f32_16x16x32_bf16 v[192:195], a[196:199], a[0:3], v[128:131]\n" \
"ds_read_b64_tr_b16 a[208:209], v221 offset:53248\n" \
"v_mfma_f32_16x16x32_bf16 v[192:195], a[200:203], a[4:7], v[192:195]\n" \
"ds_read_b64_tr_b16 a[210:211], v221 offset:55296\n" \
"v_mfma_f32_16x16x32_bf16 v[196:199], a[196:199], a[16:19], v[132:135]\n" \
"buffer_load_dwordx4 a[164:167], v209, %[srdz], s50 offen offset:1024 sc1\n" \
"v_mfma_f32_16x16x32_bf16 v[196:199], a[200:203], a[20:23], v[196:199]\n" \
"v_mfma_f32_16x16x32_bf16 v[200:203], a[196:199], a[32:35], v[136:139]\n" \
"v_mfma_f32_16x16x32_bf16 v[200:203], a[200:203], a[36:39], v[200:203]\n" \
"s_cbranch_vccz Lg17_%=\n" \
"s_nop 0\n" \
"buffer_store_dwordx4 v[96:99], v210, %[srdz], s60 offen offset:0 sc1\n" \
"Lg17_%=:\n" \
"v_mfma_f32_16x16x32_bf16 v[204:207], a[196:199], a[48:51], v[140:143]\n" \
"ds_read_b64_tr_b16 a[212:213], v221 offset:57344\n" \
"v_mfma_f32_16x16x32_bf16 v[204:207], a[200:203], a[52:55], v[204:207]\n" \
"s_waitcnt vmcnt(21) lgkmcnt(5)\n" \
"s_nop 0\n" \
"s_bitcmp1_b32 0xe0, s48\n" \
"s_cbranch_scc1 Lcw9_%=\n" \
"s_waitcnt vmcnt(14)\n" \
"Lcw9_%=:\n" \
"v_mfma_f32_16x16x32_bf16 v[192:195], a[204:207], a[8:11], v[192:195]\n" \
"ds_read_b64_tr_b16 a[214:215], v221 offset:59392\n" \
"v_mfma_f32_16x16x32_bf16 v[192:195], a[232:235], a[12:15], v[192:195]\n" \
"ds_read_b64_tr_b16 a[216:217], v221 offset:61440\n" \
"v_mfma_f32_16x16x32_bf16 v[196:199], a[204:207], a[24:27], v[196:199]\n" \
"ds_read_b64_tr_b16 a[218:219], v221 offset:63488\n" \
"v_mfma_f32_16x16x32_bf16 v[196:199], a[232:235], a[28:31], v[196:199]\n" \
"buffer_load_dwordx4 a[168:171], v209, %[srdz], s50 offen offset:2048 sc1\n" \
"v_mfma_f32_16x16x32_bf16 v[200:203], a[204:207], a[40:43], v[200:203]\n" \
"v_mfma_f32_16x16x32_bf16 v[200:203], a[232:235], a[44:47], v[200:203]\n" \
"v_mfma_f32_16x16x32_bf16 v[204:207], a[204:207], a[56:59], v[204:207]\n" \
"ds_read_b64_tr_b16 a[228:229], v220 offset:49664\n" \
"v_mfma_f32_16x16x32_bf16 v[204:207], a[232:235], a[60:63], v[204:207]\n" \
"ds_read_b64_tr_b16 a[230:231], v220 offset:51712\n" \
"v_mfma_f32_16x16x32_bf16 v[28:31], a[220:223], a[72:75], v[28:31]\n" \
"v_mfma_f32_16x16x32_bf16 v[28:31], a[224:227], a[76:79], v[28:31]\n" \
"s_cbranch_vccz Lg18_%=\n" \
"s_nop 0\n" \
"buffer_store_dwordx4 v[104:107], v210, %[srdz], s60 offen offset:1024 sc1\n" \
"Lg18_%=:\n" \
"v_mfma_f32_16x16x32_bf16 v[60:63], a[220:223], a[88:91], v[60:63]\n" \
"v_mfma_f32_16x16x32_bf16 v[60:63], a[224:227], a[92:95], v[60:63]\n" \
"buffer_load_dwordx4 a[172:175], v209, %[srdz], s50 offen offset:3072 sc1\n" \
"v_mfma_f32_16x16x32_bf16 v[92:95], a[220:223], a[104:107], v[92:95]\n" \
"v_mfma_f32_16x16x32_bf16 v[92:95], a[224:227], a[108:111], v[92:95]\n" \
"v_cmp_u_f32_e64 s[44:45], v192, v196\n" \
"v_mfma_f32_16x16x32_bf16 v[124:127], a[220:223], a[120:123], v[124:127]\n" \
"s_add_u32 m0, %[m0b], 16384\n" \
"s_cmp_gt_u32 s48, 2\n" \
"s_cselect_b32 s49, 2, 3\n" \
"s_lshl_b32 s49, s49, 18\n" \
"s_or_b32 s49, s49, s59\n" \
"buffer_load_dwordx4 %[wo0], %[srdt], s49 offen offset:0 lds\n" \
"v_mfma_f32_16x16x32_bf16 v[124:127], a[224:227], a[124:127], v[124:127]\n" \
"v_cmp_u_f32_e64 s[56:57], v200, v204\n" \
"s_or_b64 s[56:57], s[56:57], s[44:45]\n" \
"s_cbranch_scc1 Lsp2_%=\n" \
"Lbk2_%=:\n" \
"s_waitcnt vmcnt(63) lgkmcnt(2)\n" \
"s_nop 0\n" \
"v_mfma_f32_16x16x32_bf16 v[144:147], a[236:239], a[0:3], v[144:147]\n" \
"ds_read_b64_tr_b16 a[220:221], v220 offset:53760\n" \
"v_mfma_f32_16x16x32_bf16 v[144:147], a[208:211], a[4:7], v[144:147]\n" \
"ds_read_b64_tr_b16 a[222:223], v220 offset:55808\n" \
"v_mfma_f32_16x16x32_bf16 v[148:151], a[236:239], a[16:19], v[148:151]\n" \
"ds_read_b64_tr_b16 a[196:197], v220 offset:57856\n" \
"buffer_load_dwordx4 a[176:179], v210, %[srdz], s50 offen offset:0 sc1\n" \
"v_mfma_f32_16x16x32_bf16 v[148:151], a[208:211], a[20:23], v[148:151]\n" \
"ds_read_b64_tr_b16 a[198:199], v220 offset:59904\n" \
"v_mfma_f32_16x16x32_bf16 v[152:155], a[236:239], a[32:35], v[152:155]\n" \
"ds_read_b64_tr_b16 a[200:201], v220 offset:61952\n" \
"s_cbranch_vccz Lg19_%=\n" \
"s_nop 0\n" \
"buffer_store_dwordx4 v[112:115], v210, %[srdz], s60 offen offset:2048 sc1\n" \
"Lg19_%=:\n" \
"v_mfma_f32_16x16x32_bf16 v[152:155], a[208:211], a[36:39], v[152:155]\n" \
"ds_read_b64_tr_b16 a[202:203], v220 offset:64000\n" \
"v_mfma_f32_16x16x32_bf16 v[156:159], a[236:239], a[48:51], v[156:159]\n" \
"ds_read_b64_tr_b16 a[204:205], v221 offset:49664\n" \
"v_mfma_f32_16x16x32_bf16 v[156:159], a[208:211], a[52:55], v[156:159]\n" \
"ds_read_b64_tr_b16 a[206:207], v221 offset:51712\n" \
"v_mfma_f32_16x16x32_bf16 v[144:147], a[212:215], a[8:11], v[144:147]\n" \
"ds_read_b64_tr_b16 a[224:225], v221 offset:53760\n" \
"v_mfma_f32_16x16x32_bf16 v[144:147], a[216:219], a[12:15], v[144:147]\n" \
"ds_read_b64_tr_b16 a[226:227], v221 offset:55808\n" \
"buffer_load_dwordx4 a[180:183], v210, %[srdz], s50 offen offset:1024 sc1\n" \
"v_mfma_f32_16x16x32_bf16 v[148:151], a[212:215], a[24:27], v[148:151]\n" \
"v_mfma_f32_16x16x32_bf16 v[148:151], a[216:219], a[28:31], v[148:151]\n" \
"v_mfma_f32_16x16x32_bf16 v[152:155], a[212:215], a[40:43], v[152:155]\n" \
"v_mfma_f32_16x16x32_bf16 v[152:155], a[216:219], a[44:47], v[152:155]\n" \
"buffer_load_dwordx4 v212, %[srdt], s49 offen offset:1024 lds\n" \
"v_mfma_f32_16x16x32_bf16 v[156:159], a[212:215], a[56:59], v[156:159]\n" \
"ds_read_b64_tr_b16 a[232:233], v221 offset:57856\n" \
"v_mfma_f32_16x16x32_bf16 v[156:159], a[216:219], a[60:63], v[156:159]\n" \
"s_waitcnt vmcnt(63) lgkmcnt(5)\n" \
"s_nop 0\n" \
"v_mfma_f32_16x16x32_bf16 v[160:163], a[228:231], a[0:3], v[160:163]\n" \
"ds_read_b64_tr_b16 a[234:235], v221 offset:59904\n" \
"s_cbranch_vccz Lg20_%=\n" \
"s_nop 0\n" \
"buffer_store_dwordx4 v[120:123], v210, %[srdz], s60 offen offset:3072 sc1\n" \
"Lg20_%=:\n" \
"v_mfma_f32_16x16x32_bf16 v[160:163], a[220:223], a[4:7], v[160:163]\n" \
"ds_read_b64_tr_b16 a[236:237], v221 offset:61952\n" \
"v_mfma_f32_16x16x32_bf16 v[164:167], a[228:231], a[16:19], v[164:167]\n" \
"ds_read_b64_tr_b16 a[238:239], v221 offset:64000\n" \
"buffer_load_dwordx4 a[184:187], v210, %[srdz], s50 offen offset:2048 sc1\n" \
"v_mfma_f32_16x16x32_bf16 v[164:167], a[220:223], a[20:23], v[164:167]\n" \
"v_mfma_f32_16x16x32_bf16 v[168:171], a[228:231], a[32:35], v[168:171]\n" \
"buffer_load_dwordx4 a[188:191], v210, %[srdz], s50 offen offset:3072 sc1\n" \
"v_mfma_f32_16x16x32_bf16 v[168:171], a[220:223], a[36:39], v[168:171]\n" \
"v_mfma_f32_16x16x32_bf16 v[172:175], a[228:231], a[48:51], v[172:175]\n" \
"v_mfma_f32_16x16x32_bf16 v[172:175], a[220:223], a[52:55], v[172:175]\n" \
"v_mfma_f32_16x16x32_bf16 v[160:163], a[196:199], a[8:11], v[160:163]\n" \
"v_mfma_f32_16x16x32_bf16 v[160:163], a[200:203], a[12:15], v[160:163]\n" \
"v_mfma_f32_16x16x32_bf16 v[164:167], a[196:199], a[24:27], v[164:167]\n" \
"v_mfma_f32_16x16x32_bf16 v[164:167], a[200:203], a[28:31], v[164:167]\n" \
"v_mfma_f32_16x16x32_bf16 v[168:171], a[196:199], a[40:43], v[168:171]\n" \
"s_waitcnt vmcnt(63) lgkmcnt(0)\n" \
"s_nop 0\n" \
"s_bitcmp1_b32 0xe0, s48\n" \
"s_cbranch_scc1 Lcw10_%=\n" \
"s_waitcnt vmcnt(54)\n" \
"Lcw10_%=:\n" \
"s_barrier\n" \
"s_nop 0\n" \
"v_mfma_f32_16x16x32_bf16 v[168:171], a[200:203], a[44:47], v[168:171]\n" \
"ds_read_b64_tr_b16 a[208:209], v215 offset:49152\n" \
"v_mfma_f32_16x16x32_bf16 v[172:175], a[196:199], a[56:59], v[172:175]\n" \
"ds_read_b64_tr_b16 a[210:211], v215 offset:51200\n" \
"v_mfma_f32_16x16x32_bf16 v[172:175], a[200:203], a[60:63], v[172:175]\n" \
"s_waitcnt vmcnt(63) lgkmcnt(2)\n" \
"s_nop 0\n" \
"v_mfma_f32_16x16x32_bf16 v[176:179], a[204:207], a[0:3], v[176:179]\n" \
"ds_read_b64_tr_b16 a[212:213], v215 offset:53248\n" \
"global_load_dword v237, %[vct], %[pct]\n" \
"v_mfma_f32_16x16x32_bf16 v[176:179], a[224:227], a[4:7], v[176:179]\n" \
"ds_read_b64_tr_b16 a[214:215], v215 offset:55296\n" \
"v_mfma_f32_16x16x32_bf16 v[180:183], a[204:207], a[16:19], v[180:183]\n" \
"ds_read_b64_tr_b16 a[216:217], v215 offset:57344\n" \
"buffer_load_dwordx4 v213, %[srdt], s49 offen offset:2048 lds\n" \
"v_mfma_f32_16x16x32_bf16 v[180:183], a[224:227], a[20:23], v[180:183]\n" \
"ds_read_b64_tr_b16 a[218:219], v215 offset:59392\n" \
"v_mfma_f32_16x16x32_bf16 v[184:187], a[204:207], a[32:35], v[184:187]\n" \
"ds_read_b64_tr_b16 a[228:229], v215 offset:61440\n" \
"v_mfma_f32_16x16x32_bf16 v[184:187], a[224:227], a[36:39], v[184:187]\n" \
"ds_read_b64_tr_b16 a[230:231], v215 offset:63488\n" \
"v_mfma_f32_16x16x32_bf16 v[188:191], a[204:207], a[48:51], v[188:191]\n" \
"ds_read_b64_tr_b16 a[220:221], v227 offset:49152\n" \
"v_mfma_f32_16x16x32_bf16 v[188:191], a[224:227], a[52:55], v[188:191]\n" \
"ds_read_b64_tr_b16 a[222:223], v227 offset:51200\n" \
"v_mfma_f32_16x16x32_bf16 v[176:179], a[232:235], a[8:11], v[176:179]\n" \
"ds_read_b64_tr_b16 a[196:197], v227 offset:53248\n" \
"v_mfma_f32_16x16x32_bf16 v[176:179], a[236:239], a[12:15], v[176:179]\n" \
"ds_read_b64_tr_b16 a[198:199], v227 offset:55296\n" \
"s_cmp_gt_u32 s48, 3\n" \
"s_cselect_b32 s50, 3, 4\n" \
"s_mul_i32 s50, s50, 262144\n" \
"buffer_load_dwordx4 a[64:67], %[oq0], %[srdz], s50 offen offset:0 sc1\n" \
"v_mfma_f32_16x16x32_bf16 v[180:183], a[232:235], a[24:27], v[180:183]\n" \
"v_mfma_f32_16x16x32_bf16 v[180:183], a[236:239], a[28:31], v[180:183]\n" \
"buffer_load_dwordx4 v214, %[srdt], s49 offen offset:3072 lds\n" \
"v_mfma_f32_16x16x32_bf16 v[184:187], a[232:235], a[40:43], v[184:187]\n" \
"v_mfma_f32_16x16x32_bf16 v[184:187], a[236:239], a[44:47], v[184:187]\n" \
"buffer_load_dwordx4 a[68:71], %[oq0], %[srdz], s50 offen offset:1024 sc1\n" \
"v_mfma_f32_16x16x32_bf16 v[188:191], a[232:235], a[56:59], v[188:191]\n" \
"ds_read_b64_tr_b16 a[200:201], v227 offset:57344\n" \
"v_mfma_f32_16x16x32_bf16 v[188:191], a[236:239], a[60:63], v[188:191]\n" \
"s_waitcnt vmcnt(63) lgkmcnt(5)\n" \
"s_nop 0\n" \
"v_mfma_f32_16x16x32_bf16 v[4:7], a[208:211], a[0:3], v[4:7]\n" \
"ds_read_b64_tr_b16 a[202:203], v227 offset:59392\n" \
"v_mfma_f32_16x16x32_bf16 v[4:7], a[212:215], a[4:7], v[4:7]\n" \
"ds_read_b64_tr_b16 a[204:205], v227 offset:61440\n" \
"v_mfma_f32_16x16x32_bf16 v[36:39], a[208:211], a[16:19], v[36:39]\n" \
"ds_read_b64_tr_b16 a[206:207], v227 offset:63488\n" \
"v_mfma_f32_16x16x32_bf16 v[36:39], a[212:215], a[20:23], v[36:39]\n" \
"v_mfma_f32_16x16x32_bf16 v[68:71], a[208:211], a[32:35], v[68:71]\n" \
"buffer_load_dwordx4 a[72:75], %[oq0], %[srdz], s50 offen offset:2048 sc1\n" \
"v_mfma_f32_16x16x32_bf16 v[68:71], a[212:215], a[36:39], v[68:71]\n" \
"v_mfma_f32_16x16x32_bf16 v[100:103], a[208:211], a[48:51], v[100:103]\n" \
"ds_read_b64_tr_b16 a[224:225], v215 offset:49664\n" \
"v_mfma_f32_16x16x32_bf16 v[100:103], a[212:215], a[52:55], v[100:103]\n" \
"ds_read_b64_tr_b16 a[226:227], v215 offset:51712\n" \
"v_mfma_f32_16x16x32_bf16 v[4:7], a[216:219], a[8:11], v[4:7]\n" \
"ds_read_b64_tr_b16 a[232:233], v215 offset:53760\n" \
"v_mfma_f32_16x16x32_bf16 v[4:7], a[228:231], a[12:15], v[4:7]\n" \
"ds_read_b64_tr_b16 a[234:235], v215 offset:55808\n" \
"v_mfma_f32_16x16x32_bf16 v[36:39], a[216:219], a[24:27], v[36:39]\n" \
"v_mfma_f32_16x16x32_bf16 v[36:39], a[228:231], a[28:31], v[36:39]\n" \
"buffer_load_dwordx4 a[76:79], %[oq0], %[srdz], s50 offen offset:3072 sc1\n" \
"v_mfma_f32_16x16x32_bf16 v[68:71], a[216:219], a[40:43], v[68:71]\n" \
"v_mfma_f32_16x16x32_bf16 v[68:71], a[228:231], a[44:47], v[68:71]\n" \
"v_mfma_f32_16x16x32_bf16 v[100:103], a[216:219], a[56:59], v[100:103]\n" \
"ds_read_b64_tr_b16 a[236:237], v215 offset:57856\n" \
"v_mfma_f32_16x16x32_bf16 v[100:103], a[228:231], a[60:63], v[100:103]\n" \
"s_waitcnt vmcnt(63) lgkmcnt(5)\n" \
"s_nop 0\n" \
"v_mfma_f32_16x16x32_bf16 v[12:15], a[220:223], a[0:3], v[12:15]\n" \
"ds_read_b64_tr_b16 a[238:239], v215 offset:59904\n" \
"v_mfma_f32_16x16x32_bf16 v[12:15], a[196:199], a[4:7], v[12:15]\n" \
"ds_read_b64_tr_b16 a[208:209], v215 offset:61952\n" \
"v_mfma_f32_16x16x32_bf16 v[44:47], a[220:223], a[16:19], v[44:47]\n" \
"ds_read_b64_tr_b16 a[210:211], v215 offset:64000\n" \
"v_mfma_f32_16x16x32_bf16 v[44:47], a[196:199], a[20:23], v[44:47]\n" \
"buffer_load_dwordx4 a[80:83], v208, %[srdz], s50 offen offset:0 sc1\n" \
"v_mfma_f32_16x16x32_bf16 v[76:79], a[220:223], a[32:35], v[76:79]\n" \
"v_mfma_f32_16x16x32_bf16 v[76:79], a[196:199], a[36:39], v[76:79]\n" \
"s_add_u32 m0, %[m0b], 32768\n" \
"s_cmp_gt_u32 s48, 3\n" \
"s_cselect_b32 s49, 3, 4\n" \
"s_lshl_b32 s49, s49, 18\n" \
"s_or_b32 s49, s49, s58\n" \
"buffer_load_dwordx4 %[wo0], %[srdt], s49 offen offset:0 lds\n" \
"v_mfma_f32_16x16x32_bf16 v[108:111], a[220:223], a[48:51], v[108:111]\n" \
"ds_read_b64_tr_b16 a[212:213], v227 offset:49664\n" \
"v_mfma_f32_16x16x32_bf16 v[108:111], a[196:199], a[52:55], v[108:111]\n" \
"ds_read_b64_tr_b16 a[214:215], v227 offset:51712\n" \
"v_mfma_f32_16x16x32_bf16 v[12:15], a[200:203], a[8:11], v[12:15]\n" \
"ds_read_b64_tr_b16 a[216:217], v227 offset:57856\n" \
"v_mfma_f32_16x16x32_bf16 v[12:15], a[204:207], a[12:15], v[12:15]\n" \
"ds_read_b64_tr_b16 a[218:219], v227 offset:59904\n" \
"v_mfma_f32_16x16x32_bf16 v[44:47], a[200:203], a[24:27], v[44:47]\n" \
"ds_read_b64_tr_b16 a[228:229], v227 offset:53760\n" \
"v_mfma_f32_16x16x32_bf16 v[44:47], a[204:207], a[28:31], v[44:47]\n" \
"ds_read_b64_tr_b16 a[230:231], v227 offset:55808\n" \
"v_mfma_f32_16x16x32_bf16 v[76:79], a[200:203], a[40:43], v[76:79]\n" \
"ds_read_b64_tr_b16 a[220:221], v227 offset:61952\n" \
"buffer_load_dwordx4 a[84:87], v208, %[srdz], s50 offen offset:1024 sc1\n" \
"v_mfma_f32_16x16x32_bf16 v[76:79], a[204:207], a[44:47], v[76:79]\n" \
"ds_read_b64_tr_b16 a[222:223], v227 offset:64000\n" \
"v_mfma_f32_16x16x32_bf16 v[108:111], a[200:203], a[56:59], v[108:111]\n" \
"v_mfma_f32_16x16x32_bf16 v[108:111], a[204:207], a[60:63], v[108:111]\n" \
"s_waitcnt vmcnt(63) lgkmcnt(12)\n" \
"s_nop 0\n" \
"v_mfma_f32_16x16x32_bf16 v[20:23], a[224:227], a[0:3], v[20:23]\n" \
"v_mfma_f32_16x16x32_bf16 v[20:23], a[232:235], a[4:7], v[20:23]\n" \
"buffer_load_dwordx4 a[88:91], v208, %[srdz], s50 offen offset:2048 sc1\n" \
"v_mfma_f32_16x16x32_bf16 v[52:55], a[224:227], a[16:19], v[52:55]\n" \
"v_mfma_f32_16x16x32_bf16 v[52:55], a[232:235], a[20:23], v[52:55]\n" \
"buffer_load_dwordx4 v212, %[srdt], s49 offen offset:1024 lds\n" \
"v_mfma_f32_16x16x32_bf16 v[84:87], a[224:227], a[32:35], v[84:87]\n" \
"s_waitcnt vmcnt(26) lgkmcnt(0)\n" \
"s_nop 0\n" \
"s_bitcmp1_b32 0xe0, s48\n" \
"s_cbranch_scc1 Lcw11_%=\n" \
"s_waitcnt vmcnt(22)\n" \
"Lcw11_%=:\n" \
"s_barrier\n" \
"s_nop 0\n" \
"v_mfma_f32_16x16x32_bf16 v[84:87], a[232:235], a[36:39], v[84:87]\n" \
"ds_read_b64_tr_b16 a[196:197], v215 offset:0\n" \
"v_mfma_f32_16x16x32_bf16 v[116:119], a[224:227], a[48:51], v[116:119]\n" \
"ds_read_b64_tr_b16 a[198:199], v215 offset:2048\n" \
"v_mfma_f32_16x16x32_bf16 v[116:119], a[232:235], a[52:55], v[116:119]\n" \
"ds_read_b64_tr_b16 a[200:201], v215 offset:4096\n" \
"v_mfma_f32_16x16x32_bf16 v[20:23], a[236:239], a[8:11], v[20:23]\n" \
"ds_read_b64_tr_b16 a[202:203], v215 offset:6144\n" \
"v_mfma_f32_16x16x32_bf16 v[20:23], a[208:211], a[12:15], v[20:23]\n" \
"ds_read_b64_tr_b16 a[204:205], v215 offset:8192\n" \
"v_mfma_f32_16x16x32_bf16 v[52:55], a[236:239], a[24:27], v[52:55]\n" \
"ds_read_b64_tr_b16 a[206:207], v215 offset:10240\n" \
"v_mfma_f32_16x16x32_bf16 v[52:55], a[208:211], a[28:31], v[52:55]\n" \
"ds_read_b64_tr_b16 a[224:225], v215 offset:12288\n" \
"buffer_load_dwordx4 a[92:95], v208, %[srdz], s50 offen offset:3072 sc1\n" \
"v_mfma_f32_16x16x32_bf16 v[84:87], a[236:239], a[40:43], v[84:87]\n" \
"ds_read_b64_tr_b16 a[226:227], v215 offset:14336\n" \
"v_mfma_f32_16x16x32_bf16 v[84:87], a[208:211], a[44:47], v[84:87]\n" \
"v_mfma_f32_16x16x32_bf16 v[116:119], a[236:239], a[56:59], v[116:119]\n" \
"v_mfma_f32_16x16x32_bf16 v[116:119], a[208:211], a[60:63], v[116:119]\n" \
"s_waitcnt vmcnt(16) lgkmcnt(4)\n" \
"s_nop 0\n" \
"v_mfma_f32_16x16x32_bf16 v[28:31], a[212:215], a[0:3], v[28:31]\n" \
"buffer_load_dwordx4 a[96:99], v209, %[srdz], s50 offen offset:0 sc1\n" \
"v_mfma_f32_16x16x32_bf16 v[28:31], a[228:231], a[4:7], v[28:31]\n" \
"v_mfma_f32_16x16x32_bf16 v[60:63], a[212:215], a[16:19], v[60:63]\n" \
"buffer_load_dwordx4 v213, %[srdt], s49 offen offset:2048 lds\n" \
"v_mfma_f32_16x16x32_bf16 v[60:63], a[228:231], a[20:23], v[60:63]\n" \
"v_mfma_f32_16x16x32_bf16 v[92:95], a[212:215], a[32:35], v[92:95]\n" \
"v_mfma_f32_16x16x32_bf16 v[92:95], a[228:231], a[36:39], v[92:95]\n" \
"v_mfma_f32_16x16x32_bf16 v[124:127], a[212:215], a[48:51], v[124:127]\n" \
"ds_read_b64_tr_b16 a[232:233], v227 offset:0\n" \
"v_mfma_f32_16x16x32_bf16 v[124:127], a[228:231], a[52:55], v[124:127]\n" \
"ds_read_b64_tr_b16 a[234:235], v227 offset:2048\n" \
"v_mfma_f32_16x16x32_bf16 v[128:131], a[196:199], a[128:131], v[192:195]\n" \
"ds_read_b64_tr_b16 a[236:237], v227 offset:4096\n" \
"v_mfma_f32_16x16x32_bf16 v[128:131], a[200:203], a[132:135], v[128:131]\n" \
"ds_read_b64_tr_b16 a[238:239], v227 offset:6144\n" \
"buffer_load_dwordx4 a[100:103], v209, %[srdz], s50 offen offset:1024 sc1\n" \
"v_mfma_f32_16x16x32_bf16 v[132:135], a[196:199], a[144:147], v[196:199]\n" \
"v_mfma_f32_16x16x32_bf16 v[132:135], a[200:203], a[148:151], v[132:135]\n" \
"v_mfma_f32_16x16x32_bf16 v[136:139], a[196:199], a[160:163], v[200:203]\n" \
"v_mfma_f32_16x16x32_bf16 v[136:139], a[200:203], a[164:167], v[136:139]\n" \
"buffer_load_dwordx4 a[104:107], v209, %[srdz], s50 offen offset:2048 sc1\n" \
"v_mfma_f32_16x16x32_bf16 v[140:143], a[196:199], a[176:179], v[204:207]\n" \
"ds_read_b64_tr_b16 a[208:209], v227 offset:8192\n" \
"v_mfma_f32_16x16x32_bf16 v[140:143], a[200:203], a[180:183], v[140:143]\n" \
"s_waitcnt vmcnt(17) lgkmcnt(5)\n" \
"s_nop 0\n" \
"v_mfma_f32_16x16x32_bf16 v[128:131], a[204:207], a[136:139], v[128:131]\n" \
"ds_read_b64_tr_b16 a[210:211], v227 offset:10240\n" \
"v_mfma_f32_16x16x32_bf16 v[128:131], a[224:227], a[140:143], v[128:131]\n" \
"ds_read_b64_tr_b16 a[212:213], v227 offset:12288\n" \
"v_mfma_f32_16x16x32_bf16 v[132:135], a[204:207], a[152:155], v[132:135]\n" \
"ds_read_b64_tr_b16 a[214:215], v227 offset:14336\n" \
"v_mfma_f32_16x16x32_bf16 v[132:135], a[224:227], a[156:159], v[132:135]\n" \
"buffer_load_dwordx4 v214, %[srdt], s49 offen offset:3072 lds\n" \
"v_mfma_f32_16x16x32_bf16 v[136:139], a[204:207], a[168:171], v[136:139]\n" \
"v_mfma_f32_16x16x32_bf16 v[136:139], a[224:227], a[172:175], v[136:139]\n" \
"buffer_load_dwordx4 a[108:111], v209, %[srdz], s50 offen offset:3072 sc1\n" \
"v_mfma_f32_16x16x32_bf16 v[140:143], a[204:207], a[184:187], v[140:143]\n" \
"ds_read_b64_tr_b16 a[228:229], v215 offset:512\n" \
"v_mfma_f32_16x16x32_bf16 v[140:143], a[224:227], a[188:191], v[140:143]\n" \
"ds_read_b64_tr_b16 a[230:231], v215 offset:2560\n" \
"v_mfma_f32_16x16x32_bf16 v[28:31], a[216:219], a[8:11], v[28:31]\n" \
"v_mfma_f32_16x16x32_bf16 v[28:31], a[220:223], a[12:15], v[28:31]\n" \
"v_mfma_f32_16x16x32_bf16 v[60:63], a[216:219], a[24:27], v[60:63]\n" \
"v_mfma_f32_16x16x32_bf16 v[60:63], a[220:223], a[28:31], v[60:63]\n" \
"v_mfma_f32_16x16x32_bf16 v[92:95], a[216:219], a[40:43], v[92:95]\n" \
"buffer_load_dwordx4 a[112:115], v210, %[srdz], s50 offen offset:0 sc1\n" \
"v_mfma_f32_16x16x32_bf16 v[92:95], a[220:223], a[44:47], v[92:95]\n" \
"v_cmp_u_f32_e64 s[44:45], v128, v132\n" \
"v_mfma_f32_16x16x32_bf16 v[124:127], a[216:219], a[56:59], v[124:127]\n" \
"v_mfma_f32_16x16x32_bf16 v[124:127], a[220:223], a[60:63], v[124:127]\n" \
"v_cmp_u_f32_e64 s[56:57], v136, v140\n" \
"s_or_b64 s[56:57], s[56:57], s[44:45]\n" \
"s_cbranch_scc1 Lsp3_%=\n" \
"Lbk3_%=:\n" \
"s_waitcnt vmcnt(63) lgkmcnt(2)\n" \
"s_nop 0\n" \
"v_mfma_f32_16x16x32_bf16 v[144:147], a[232:235], a[128:131], v[144:147]\n" \
"ds_read_b64_tr_b16 a[216:217], v215 offset:4608\n" \
"v_mfma_f32_16x16x32_bf16 v[144:147], a[236:239], a[132:135], v[144:147]\n" \
"ds_read_b64_tr_b16 a[218:219], v215 offset:6656\n" \
"v_mfma_f32_16x16x32_bf16 v[148:151], a[232:235], a[144:147], v[148:151]\n" \
"ds_read_b64_tr_b16 a[196:197], v215 offset:8704\n" \
"v_mfma_f32_16x16x32_bf16 v[148:151], a[236:239], a[148:151], v[148:151]\n" \
"ds_read_b64_tr_b16 a[198:199], v215 offset:10752\n" \
"v_mfma_f32_16x16x32_bf16 v[152:155], a[232:235], a[160:163], v[152:155]\n" \
"ds_read_b64_tr_b16 a[200:201], v215 offset:12800\n" \
"v_mfma_f32_16x16x32_bf16 v[152:155], a[236:239], a[164:167], v[152:155]\n" \
"ds_read_b64_tr_b16 a[202:203], v215 offset:14848\n" \
"v_mfma_f32_16x16x32_bf16 v[156:159], a[232:235], a[176:179], v[156:159]\n" \
"ds_read_b64_tr_b16 a[204:205], v227 offset:512\n" \
"buffer_load_dwordx4 a[116:119], v210, %[srdz], s50 offen offset:1024 sc1\n" \
"v_mfma_f32_16x16x32_bf16 v[156:159], a[236:239], a[180:183], v[156:159]\n" \
"ds_read_b64_tr_b16 a[206:207], v227 offset:2560\n" \
"v_mfma_f32_16x16x32_bf16 v[144:147], a[208:211], a[136:139], v[144:147]\n" \
"ds_read_b64_tr_b16 a[220:221], v227 offset:4608\n" \
"v_mfma_f32_16x16x32_bf16 v[144:147], a[212:215], a[140:143], v[144:147]\n" \
"ds_read_b64_tr_b16 a[222:223], v227 offset:6656\n" \
"v_mfma_f32_16x16x32_bf16 v[148:151], a[208:211], a[152:155], v[148:151]\n" \
"s_add_u32 m0, %[m0b], 98304\n" \
"s_cmp_gt_u32 s48, 4\n" \
"s_cselect_b32 s49, 4, 5\n" \
"s_lshl_b32 s49, s49, 18\n" \
"s_or_b32 s49, s49, s58\n" \
"buffer_load_dwordx4 %[wo0], %[srdt], s49 offen offset:0 lds\n" \
"v_mfma_f32_16x16x32_bf16 v[148:151], a[212:215], a[156:159], v[148:151]\n" \
"v_mfma_f32_16x16x32_bf16 v[152:155], a[208:211], a[168:171], v[152:155]\n" \
"buffer_load_dwordx4 a[120:123], v210, %[srdz], s50 offen offset:2048 sc1\n" \
"v_mfma_f32_16x16x32_bf16 v[152:155], a[212:215], a[172:175], v[152:155]\n" \
"v_mfma_f32_16x16x32_bf16 v[156:159], a[208:211], a[184:187], v[156:159]\n" \
"ds_read_b64_tr_b16 a[224:225], v227 offset:8704\n" \
"v_mfma_f32_16x16x32_bf16 v[156:159], a[212:215], a[188:191], v[156:159]\n" \
"s_waitcnt vmcnt(63) lgkmcnt(5)\n" \
"s_nop 0\n" \
"v_mfma_f32_16x16x32_bf16 v[160:163], a[228:231], a[128:131], v[160:163]\n" \
"ds_read_b64_tr_b16 a[226:227], v227 offset:10752\n" \
"v_mfma_f32_16x16x32_bf16 v[160:163], a[216:219], a[132:135], v[160:163]\n" \
"ds_read_b64_tr_b16 a[232:233], v227 offset:12800\n" \
"v_mfma_f32_16x16x32_bf16 v[164:167], a[228:231], a[144:147], v[164:167]\n" \
"ds_read_b64_tr_b16 a[234:235], v227 offset:14848\n" \
"v_mfma_f32_16x16x32_bf16 v[164:167], a[216:219], a[148:151], v[164:167]\n" \
"buffer_load_dwordx4 a[124:127], v210, %[srdz], s50 offen offset:3072 sc1\n" \
"v_mfma_f32_16x16x32_bf16 v[168:171], a[228:231], a[160:163], v[168:171]\n" \
"v_mfma_f32_16x16x32_bf16 v[168:171], a[216:219], a[164:167], v[168:171]\n" \
"v_mfma_f32_16x16x32_bf16 v[172:175], a[228:231], a[176:179], v[172:175]\n" \
"v_mfma_f32_16x16x32_bf16 v[172:175], a[216:219], a[180:183], v[172:175]\n" \
"v_mfma_f32_16x16x32_bf16 v[160:163], a[196:199], a[136:139], v[160:163]\n" \
"v_mfma_f32_16x16x32_bf16 v[160:163], a[200:203], a[140:143], v[160:163]\n" \
"buffer_load_dwordx4 v212, %[srdt], s49 offen offset:1024 lds\n" \
"v_mfma_f32_16x16x32_bf16 v[164:167], a[196:199], a[152:155], v[164:167]\n" \
"v_mfma_f32_16x16x32_bf16 v[164:167], a[200:203], a[156:159], v[164:167]\n" \
"v_mfma_f32_16x16x32_bf16 v[168:171], a[196:199], a[168:171], v[168:171]\n" \
"s_waitcnt vmcnt(21) lgkmcnt(0)\n" \
"s_barrier\n" \
"v_mfma_f32_16x16x32_bf16 v[168:171], a[200:203], a[172:175], v[168:171]\n" \
"ds_read_b64_tr_b16 a[236:237], v215 offset:16384\n" \
"v_mfma_f32_16x16x32_bf16 v[172:175], a[196:199], a[184:187], v[172:175]\n" \
"ds_read_b64_tr_b16 a[238:239], v215 offset:18432\n" \
"v_mfma_f32_16x16x32_bf16 v[172:175], a[200:203], a[188:191], v[172:175]\n" \
"s_waitcnt vmcnt(63) lgkmcnt(2)\n" \
"s_nop 0\n" \
"v_mfma_f32_16x16x32_bf16 v[176:179], a[204:207], a[128:131], v[176:179]\n" \
"ds_read_b64_tr_b16 a[208:209], v215 offset:20480\n" \
"s_cmp_gt_u32 s48, 4\n" \
"s_cselect_b32 s50, 4, 5\n" \
"s_mul_i32 s50, s50, 262144\n" \
"buffer_load_dwordx4 v[0:3], %[oq0], %[srdz], s50 offen offset:0 sc1\n" \
"v_mfma_f32_16x16x32_bf16 v[176:179], a[220:223], a[132:135], v[176:179]\n" \
"ds_read_b64_tr_b16 a[210:211], v215 offset:22528\n" \
"v_mfma_f32_16x16x32_bf16 v[180:183], a[204:207], a[144:147], v[180:183]\n" \
"ds_read_b64_tr_b16 a[212:213], v215 offset:24576\n" \
"v_mfma_f32_16x16x32_bf16 v[180:183], a[220:223], a[148:151], v[180:183]\n" \
"ds_read_b64_tr_b16 a[214:215], v215 offset:26624\n" \
"v_mfma_f32_16x16x32_bf16 v[184:187], a[204:207], a[160:163], v[184:187]\n" \
"ds_read_b64_tr_b16 a[228:229], v215 offset:28672\n" \
"buffer_load_dwordx4 v[8:11], %[oq0], %[srdz], s50 offen offset:1024 sc1\n" \
"v_mfma_f32_16x16x32_bf16 v[184:187], a[220:223], a[164:167], v[184:187]\n" \
"ds_read_b64_tr_b16 a[230:231], v215 offset:30720\n" \
"v_mfma_f32_16x16x32_bf16 v[188:191], a[204:207], a[176:179], v[188:191]\n" \
"ds_read_b64_tr_b16 a[216:217], v227 offset:16384\n" \
"v_mfma_f32_16x16x32_bf16 v[188:191], a[220:223], a[180:183], v[188:191]\n" \
"ds_read_b64_tr_b16 a[218:219], v227 offset:18432\n" \
"v_mfma_f32_16x16x32_bf16 v[176:179], a[224:227], a[136:139], v[176:179]\n" \
"ds_read_b64_tr_b16 a[196:197], v227 offset:20480\n" \
"buffer_load_dwordx4 v[16:19], %[oq0], %[srdz], s50 offen offset:2048 sc1\n" \
"v_mfma_f32_16x16x32_bf16 v[176:179], a[232:235], a[140:143], v[176:179]\n" \
"ds_read_b64_tr_b16 a[198:199], v227 offset:22528\n" \
"v_mfma_f32_16x16x32_bf16 v[180:183], a[224:227], a[152:155], v[180:183]\n" \
"buffer_load_dwordx4 v213, %[srdt], s49 offen offset:2048 lds\n" \
"v_mfma_f32_16x16x32_bf16 v[180:183], a[232:235], a[156:159], v[180:183]\n" \
"v_mfma_f32_16x16x32_bf16 v[184:187], a[224:227], a[168:171], v[184:187]\n" \
"buffer_load_dwordx4 v[24:27], %[oq0], %[srdz], s50 offen offset:3072 sc1\n" \
"v_mfma_f32_16x16x32_bf16 v[184:187], a[232:235], a[172:175], v[184:187]\n" \
"v_mfma_f32_16x16x32_bf16 v[188:191], a[224:227], a[184:187], v[188:191]\n" \
"ds_read_b64_tr_b16 a[200:201], v227 offset:24576\n" \
"v_mfma_f32_16x16x32_bf16 v[188:191], a[232:235], a[188:191], v[188:191]\n" \
"s_waitcnt vmcnt(63) lgkmcnt(5)\n" \
"s_nop 0\n" \
"v_mfma_f32_16x16x32_bf16 v[4:7], a[236:239], a[128:131], v[4:7]\n" \
"ds_read_b64_tr_b16 a[202:203], v227 offset:26624\n" \
"buffer_load_dwordx4 v[32:35], v208, %[srdz], s50 offen offset:0 sc1\n" \
"v_mfma_f32_16x16x32_bf16 v[4:7], a[208:211], a[132:135], v[4:7]\n" \
"ds_read_b64_tr_b16 a[204:205], v227 offset:28672\n" \
"v_mfma_f32_16x16x32_bf16 v[36:39], a[236:239], a[144:147], v[36:39]\n" \
"ds_read_b64_tr_b16 a[206:207], v227 offset:30720\n" \
"v_mfma_f32_16x16x32_bf16 v[36:39], a[208:211], a[148:151], v[36:39]\n" \
"buffer_load_dwordx4 v[40:43], v208, %[srdz], s50 offen offset:1024 sc1\n" \
"v_mfma_f32_16x16x32_bf16 v[68:71], a[236:239], a[160:163], v[68:71]\n" \
"v_mfma_f32_16x16x32_bf16 v[68:71], a[208:211], a[164:167], v[68:71]\n" \
"buffer_load_dwordx4 v[48:51], v208, %[srdz], s50 offen offset:2048 sc1\n" \
"v_mfma_f32_16x16x32_bf16 v[100:103], a[236:239], a[176:179], v[100:103]\n" \
"ds_read_b64_tr_b16 a[220:221], v215 offset:16896\n" \
"v_mfma_f32_16x16x32_bf16 v[100:103], a[208:211], a[180:183], v[100:103]\n" \
"ds_read_b64_tr_b16 a[222:223], v215 offset:18944\n" \
"v_mfma_f32_16x16x32_bf16 v[4:7], a[212:215], a[136:139], v[4:7]\n" \
"ds_read_b64_tr_b16 a[224:225], v215 offset:20992\n" \
"v_mfma_f32_16x16x32_bf16 v[4:7], a[228:231], a[140:143], v[4:7]\n" \
"ds_read_b64_tr_b16 a[226:227], v215 offset:23040\n" \
"v_mfma_f32_16x16x32_bf16 v[36:39], a[212:215], a[152:155], v[36:39]\n" \
"buffer_load_dwordx4 v214, %[srdt], s49 offen offset:3072 lds\n" \
"v_mfma_f32_16x16x32_bf16 v[36:39], a[228:231], a[156:159], v[36:39]\n" \
"v_mfma_f32_16x16x32_bf16 v[68:71], a[212:215], a[168:171], v[68:71]\n" \
"buffer_load_dwordx4 v[56:59], v208, %[srdz], s50 offen offset:3072 sc1\n" \
"v_mfma_f32_16x16x32_bf16 v[68:71], a[228:231], a[172:175], v[68:71]\n" \
"v_mfma_f32_16x16x32_bf16 v[100:103], a[212:215], a[184:187], v[100:103]\n" \
"ds_read_b64_tr_b16 a[232:233], v215 offset:25088\n" \
"v_mfma_f32_16x16x32_bf16 v[100:103], a[228:231], a[188:191], v[100:103]\n" \
"s_waitcnt vmcnt(63) lgkmcnt(5)\n" \
"s_nop 0\n" \
"v_mfma_f32_16x16x32_bf16 v[12:15], a[216:219], a[128:131], v[12:15]\n" \
"ds_read_b64_tr_b16 a[234:235], v215 offset:27136\n" \
"buffer_load_dwordx4 v[64:67], v209, %[srdz], s50 offen offset:0 sc1\n" \
"v_mfma_f32_16x16x32_bf16 v[12:15], a[196:199], a[132:135], v[12:15]\n" \
"ds_read_b64_tr_b16 a[236:237], v215 offset:29184\n" \
"v_mfma_f32_16x16x32_bf16 v[44:47], a[216:219], a[144:147], v[44:47]\n" \
"ds_read_b64_tr_b16 a[238:239], v215 offset:31232\n" \
"v_mfma_f32_16x16x32_bf16 v[44:47], a[196:199], a[148:151], v[44:47]\n" \
".if . - adaln_body != 49152\n" \
".error \"size model\"\n" \
".endif\n" \
"Lk11_%=:\n" \
"s_cbranch_execz adaln_kr\n" \
"s_nop 0\n" \
"buffer_load_dwordx4 v[72:75], v209, %[srdz], s50 offen offset:1024 sc1\n" \
"v_mfma_f32_16x16x32_bf16 v[76:79], a[216:219], a[160:163], v[76:79]\n" \
"v_mfma_f32_16x16x32_bf16 v[76:79], a[196:199], a[164:167], v[76:79]\n" \
"buffer_load_dwordx4 v[80:83], v209, %[srdz], s50 offen offset:2048 sc1\n" \
"v_mfma_f32_16x16x32_bf16 v[108:111], a[216:219], a[176:179], v[108:111]\n" \
"ds_read_b64_tr_b16 a[208:209], v227 offset:16896\n" \
"v_mfma_f32_16x16x32_bf16 v[108:111], a[196:199], a[180:183], v[108:111]\n" \
"ds_read_b64_tr_b16 a[210:211], v227 offset:18944\n" \
"v_mfma_f32_16x16x32_bf16 v[12:15], a[200:203], a[136:139], v[12:15]\n" \
"ds_read_b64_tr_b16 a[212:213], v227 offset:25088\n" \
"v_mfma_f32_16x16x32_bf16 v[12:15], a[204:207], a[140:143], v[12:15]\n" \
"ds_read_b64_tr_b16 a[214:215], v227 offset:27136\n" \
"v_mfma_f32_16x16x32_bf16 v[44:47], a[200:203], a[152:155], v[44:47]\n" \
"ds_read_b64_tr_b16 a[228:229], v227 offset:20992\n" \
"v_mfma_f32_16x16x32_bf16 v[44:47], a[204:207], a[156:159], v[44:47]\n" \
"ds_read_b64_tr_b16 a[230:231], v227 offset:23040\n" \
"buffer_load_dwordx4 v[88:91], v209, %[srdz], s50 offen offset:3072 sc1\n" \
"v_mfma_f32_16x16x32_bf16 v[76:79], a[200:203], a[168:171], v[76:79]\n" \
"ds_read_b64_tr_b16 a[216:217], v227 offset:29184\n" \
"v_mfma_f32_16x16x32_bf16 v[76:79], a[204:207], a[172:175], v[76:79]\n" \
"ds_read_b64_tr_b16 a[218:219], v227 offset:31232\n" \
"v_mfma_f32_16x16x32_bf16 v[108:111], a[200:203], a[184:187], v[108:111]\n" \
"buffer_load_dwordx4 v[96:99], v210, %[srdz], s50 offen offset:0 sc1\n" \
"v_mfma_f32_16x16x32_bf16 v[108:111], a[204:207], a[188:191], v[108:111]\n" \
"s_waitcnt vmcnt(63) lgkmcnt(12)\n" \
"s_nop 0\n" \
"v_mfma_f32_16x16x32_bf16 v[20:23], a[220:223], a[128:131], v[20:23]\n" \
"s_add_u32 m0, %[m0b], 114688\n" \
"s_cmp_gt_u32 s48, 5\n" \
"s_cselect_b32 s49, 5, 6\n" \
"s_lshl_b32 s49, s49, 18\n" \
"s_or_b32 s49, s49, s58\n" \
"buffer_load_dwordx4 %[wo0], %[srdt], s49 offen offset:0 lds\n" \
"v_mfma_f32_16x16x32_bf16 v[20:23], a[224:227], a[132:135], v[20:23]\n" \
"v_mfma_f32_16x16x32_bf16 v[52:55], a[220:223], a[144:147], v[52:55]\n" \
"buffer_load_dwordx4 v[104:107], v210, %[srdz], s50 offen offset:1024 sc1\n" \
"v_mfma_f32_16x16x32_bf16 v[52:55], a[224:227], a[148:151], v[52:55]\n" \
"v_mfma_f32_16x16x32_bf16 v[84:87], a[220:223], a[160:163], v[84:87]\n" \
"s_waitcnt vmcnt(24) lgkmcnt(0)\n" \
"s_barrier\n" \
"v_mfma_f32_16x16x32_bf16 v[84:87], a[224:227], a[164:167], v[84:87]\n" \
"ds_read_b64_tr_b16 a[196:197], v215 offset:32768\n" \
"v_mfma_f32_16x16x32_bf16 v[116:119], a[220:223], a[176:179], v[116:119]\n" \
"ds_read_b64_tr_b16 a[198:199], v215 offset:34816\n" \
"v_mfma_f32_16x16x32_bf16 v[116:119], a[224:227], a[180:183], v[116:119]\n" \
"ds_read_b64_tr_b16 a[200:201], v215 offset:36864\n" \
"buffer_load_dwordx4 v[112:115], v210, %[srdz], s50 offen offset:2048 sc1\n" \
"v_mfma_f32_16x16x32_bf16 v[20:23], a[232:235], a[136:139], v[20:23]\n" \
"ds_read_b64_tr_b16 a[202:203], v215 offset:38912\n" \
"v_mfma_f32_16x16x32_bf16 v[20:23], a[236:239], a[140:143], v[20:23]\n" \
"ds_read_b64_tr_b16 a[204:205], v215 offset:40960\n" \
"v_mfma_f32_16x16x32_bf16 v[52:55], a[232:235], a[152:155], v[52:55]\n" \
"ds_read_b64_tr_b16 a[206:207], v215 offset:43008\n" \
"v_mfma_f32_16x16x32_bf16 v[52:55], a[236:239], a[156:159], v[52:55]\n" \
"ds_read_b64_tr_b16 a[220:221], v215 offset:45056\n" \
"buffer_load_dwordx4 v[120:123], v210, %[srdz], s50 offen offset:3072 sc1\n" \
"v_mfma_f32_16x16x32_bf16 v[84:87], a[232:235], a[168:171], v[84:87]\n" \
"ds_read_b64_tr_b16 a[222:223], v215 offset:47104\n" \
"v_readlane_b32 s54, %[thi], 16\n" \
"v_mfma_f32_16x16x32_bf16 v[84:87], a[236:239], a[172:175], v[84:87]\n" \
"v_mfma_f32_16x16x32_bf16 v[116:119], a[232:235], a[184:187], v[116:119]\n" \
"v_mfma_f32_16x16x32_bf16 v[116:119], a[236:239], a[188:191], v[116:119]\n" \
"s_waitcnt vmcnt(23) lgkmcnt(4)\n" \
"s_nop 0\n" \
"v_mfma_f32_16x16x32_bf16 v[28:31], a[208:211], a[128:131], v[28:31]\n" \
"buffer_load_dwordx4 v212, %[srdt], s49 offen offset:1024 lds\n" \
"v_mfma_f32_16x16x32_bf16 v[28:31], a[228:231], a[132:135], v[28:31]\n" \
"v_mfma_f32_16x16x32_bf16 v[60:63], a[208:211], a[144:147], v[60:63]\n" \
"s_cmp_gt_u32 s48, 5\n" \
"s_cselect_b32 s50, 5, 6\n" \
"s_mul_i32 s50, s50, 262144\n" \
"buffer_load_dwordx4 a[0:3], %[oq0], %[srdz], s50 offen offset:0 sc1\n" \
"v_mfma_f32_16x16x32_bf16 v[60:63], a[228:231], a[148:151], v[60:63]\n" \
"v_mfma_f32_16x16x32_bf16 v[92:95], a[208:211], a[160:163], v[92:95]\n" \
"buffer_load_dword v232, v226, %[srdz], s54 offen sc1\n" \
"v_mfma_f32_16x16x32_bf16 v[92:95], a[228:231], a[164:167], v[92:95]\n" \
"v_mfma_f32_16x16x32_bf16 v[124:127], a[208:211], a[176:179], v[124:127]\n" \
"ds_read_b64_tr_b16 a[224:225], v227 offset:32768\n" \
"buffer_load_dwordx4 a[4:7], %[oq0], %[srdz], s50 offen offset:1024 sc1\n" \
"v_mfma_f32_16x16x32_bf16 v[124:127], a[228:231], a[180:183], v[124:127]\n" \
"ds_read_b64_tr_b16 a[226:227], v227 offset:34816\n" \
"v_mfma_f32_16x16x32_bf16 v[192:195], a[196:199], a[64:67], v[128:131]\n" \
"ds_read_b64_tr_b16 a[232:233], v227 offset:36864\n" \
"v_mfma_f32_16x16x32_bf16 v[192:195], a[200:203], a[68:71], v[192:195]\n" \
"ds_read_b64_tr_b16 a[234:235], v227 offset:38912\n" \
"v_mfma_f32_16x16x32_bf16 v[196:199], a[196:199], a[80:83], v[132:135]\n" \
"buffer_load_dwordx4 a[8:11], %[oq0], %[srdz], s50 offen offset:2048 sc1\n" \
"v_mfma_f32_16x16x32_bf16 v[196:199], a[200:203], a[84:87], v[196:199]\n" \
"v_mfma_f32_16x16x32_bf16 v[200:203], a[196:199], a[96:99], v[136:139]\n" \
"buffer_load_dwordx4 a[12:15], %[oq0], %[srdz], s50 offen offset:3072 sc1\n" \
"v_mfma_f32_16x16x32_bf16 v[200:203], a[200:203], a[100:103], v[200:203]\n" \
"v_mfma_f32_16x16x32_bf16 v[204:207], a[196:199], a[112:115], v[140:143]\n" \
"ds_read_b64_tr_b16 a[236:237], v227 offset:40960\n" \
"v_mfma_f32_16x16x32_bf16 v[204:207], a[200:203], a[116:119], v[204:207]\n" \
"s_waitcnt vmcnt(26) lgkmcnt(5)\n" \
"s_nop 0\n" \
"v_mfma_f32_16x16x32_bf16 v[192:195], a[204:207], a[72:75], v[192:195]\n" \
"ds_read_b64_tr_b16 a[238:239], v227 offset:43008\n" \
"v_mfma_f32_16x16x32_bf16 v[192:195], a[220:223], a[76:79], v[192:195]\n" \
"ds_read_b64_tr_b16 a[208:209], v227 offset:45056\n" \
"buffer_load_dwordx4 v213, %[srdt], s49 offen offset:2048 lds\n" \
"v_mfma_f32_16x16x32_bf16 v[196:199], a[204:207], a[88:91], v[196:199]\n" \
"ds_read_b64_tr_b16 a[210:211], v227 offset:47104\n" \
"v_mfma_f32_16x16x32_bf16 v[196:199], a[220:223], a[92:95], v[196:199]\n" \
"buffer_load_dwordx4 a[16:19], v208, %[srdz], s50 offen offset:0 sc1\n" \
"v_mfma_f32_16x16x32_bf16 v[200:203], a[204:207], a[104:107], v[200:203]\n" \
"v_mfma_f32_16x16x32_bf16 v[200:203], a[220:223], a[108:111], v[200:203]\n" \
"buffer_load_dwordx4 a[20:23], v208, %[srdz], s50 offen offset:1024 sc1\n" \
"v_mfma_f32_16x16x32_bf16 v[204:207], a[204:207], a[120:123], v[204:207]\n" \
"ds_read_b64_tr_b16 a[228:229], v215 offset:33280\n" \
"v_mfma_f32_16x16x32_bf16 v[204:207], a[220:223], a[124:127], v[204:207]\n" \
"ds_read_b64_tr_b16 a[230:231], v215 offset:35328\n" \
"v_mfma_f32_16x16x32_bf16 v[28:31], a[212:215], a[136:139], v[28:31]\n" \
"buffer_load_dwordx4 a[24:27], v208, %[srdz], s50 offen offset:2048 sc1\n" \
"v_mfma_f32_16x16x32_bf16 v[28:31], a[216:219], a[140:143], v[28:31]\n" \
"v_mfma_f32_16x16x32_bf16 v[60:63], a[212:215], a[152:155], v[60:63]\n" \
"buffer_load_dwordx4 a[28:31], v208, %[srdz], s50 offen offset:3072 sc1\n" \
"v_mfma_f32_16x16x32_bf16 v[60:63], a[216:219], a[156:159], v[60:63]\n" \
"v_mfma_f32_16x16x32_bf16 v[92:95], a[212:215], a[168:171], v[92:95]\n" \
"v_mfma_f32_16x16x32_bf16 v[92:95], a[216:219], a[172:175], v[92:95]\n" \
"v_cmp_u_f32_e64 s[44:45], v192, v196\n" \
"buffer_load_dwordx4 v214, %[srdt], s49 offen offset:3072 lds\n" \
"v_mfma_f32_16x16x32_bf16 v[124:127], a[212:215], a[184:187], v[124:127]\n" \
"v_mfma_f32_16x16x32_bf16 v[124:127], a[216:219], a[188:191], v[124:127]\n" \
"v_cmp_u_f32_e64 s[56:57], v200, v204\n" \
"s_or_b64 s[56:57], s[56:57], s[44:45]\n" \
"s_cbranch_scc1 Lsp4_%=\n" \
"Lbk4_%=:\n" \
"s_waitcnt vmcnt(63) lgkmcnt(2)\n" \
"s_nop 0\n" \
"v_mfma_f32_16x16x32_bf16 v[144:147], a[224:227], a[64:67], v[144:147]\n" \
"ds_read_b64_tr_b16 a[212:213], v215 offset:37376\n" \
"v_mfma_f32_16x16x32_bf16 v[144:147], a[232:235], a[68:71], v[144:147]\n" \
"ds_read_b64_tr_b16 a[214:215], v215 offset:39424\n" \
"buffer_load_dwordx4 a[32:35], v209, %[srdz], s50 offen offset:0 sc1\n" \
"v_mfma_f32_16x16x32_bf16 v[148:151], a[224:227], a[80:83], v[148:151]\n" \
"ds_read_b64_tr_b16 a[196:197], v215 offset:41472\n" \
"v_mfma_f32_16x16x32_bf16 v[148:151], a[232:235], a[84:87], v[148:151]\n" \
"ds_read_b64_tr_b16 a[198:199], v215 offset:43520\n" \
"v_mfma_f32_16x16x32_bf16 v[152:155], a[224:227], a[96:99], v[152:155]\n" \
"ds_read_b64_tr_b16 a[200:201], v215 offset:45568\n" \
"v_mfma_f32_16x16x32_bf16 v[152:155], a[232:235], a[100:103], v[152:155]\n" \
"ds_read_b64_tr_b16 a[202:203], v215 offset:47616\n" \
"buffer_load_dwordx4 a[36:39], v209, %[srdz], s50 offen offset:1024 sc1\n" \
"v_mfma_f32_16x16x32_bf16 v[156:159], a[224:227], a[112:115], v[156:159]\n" \
"ds_read_b64_tr_b16 a[204:205], v227 offset:33280\n" \
"v_mfma_f32_16x16x32_bf16 v[156:159], a[232:235], a[116:119], v[156:159]\n" \
"ds_read_b64_tr_b16 a[206:207], v227 offset:35328\n" \
"v_mfma_f32_16x16x32_bf16 v[144:147], a[236:239], a[72:75], v[144:147]\n" \
"ds_read_b64_tr_b16 a[216:217], v227 offset:41472\n" \
"v_mfma_f32_16x16x32_bf16 v[144:147], a[208:211], a[76:79], v[144:147]\n" \
"ds_read_b64_tr_b16 a[218:219], v227 offset:43520\n" \
"buffer_load_dwordx4 a[40:43], v209, %[srdz], s50 offen offset:2048 sc1\n" \
"v_mfma_f32_16x16x32_bf16 v[148:151], a[236:239], a[88:91], v[148:151]\n" \
"ds_read_b64_tr_b16 a[220:221], v227 offset:37376\n" \
"v_mfma_f32_16x16x32_bf16 v[148:151], a[208:211], a[92:95], v[148:151]\n" \
"ds_read_b64_tr_b16 a[222:223], v227 offset:39424\n" \
"v_mfma_f32_16x16x32_bf16 v[152:155], a[236:239], a[104:107], v[152:155]\n" \
"ds_read_b64_tr_b16 a[224:225], v227 offset:45568\n" \
"v_mfma_f32_16x16x32_bf16 v[152:155], a[208:211], a[108:111], v[152:155]\n" \
"ds_read_b64_tr_b16 a[226:227], v227 offset:47616\n" \
"buffer_load_dwordx4 a[44:47], v209, %[srdz], s50 offen offset:3072 sc1\n" \
"v_mfma_f32_16x16x32_bf16 v[156:159], a[236:239], a[120:123], v[156:159]\n" \
"v_mfma_f32_16x16x32_bf16 v[156:159], a[208:211], a[124:127], v[156:159]\n" \
"s_waitcnt vmcnt(63) lgkmcnt(12)\n" \
"s_nop 0\n" \
"v_mfma_f32_16x16x32_bf16 v[160:163], a[228:231], a[64:67], v[160:163]\n" \
"buffer_load_dwordx4 a[48:51], v210, %[srdz], s50 offen offset:0 sc1\n" \
"v_mfma_f32_16x16x32_bf16 v[160:163], a[212:215], a[68:71], v[160:163]\n" \
"v_mfma_f32_16x16x32_bf16 v[164:167], a[228:231], a[80:83], v[164:167]\n" \
"buffer_load_dwordx4 a[52:55], v210, %[srdz], s50 offen offset:1024 sc1\n" \
"v_mfma_f32_16x16x32_bf16 v[164:167], a[212:215], a[84:87], v[164:167]\n" \
"v_mfma_f32_16x16x32_bf16 v[168:171], a[228:231], a[96:99], v[168:171]\n" \
"s_waitcnt vmcnt(28) lgkmcnt(0)\n" \
"s_barrier\n" \
"v_mfma_f32_16x16x32_bf16 v[168:171], a[212:215], a[100:103], v[168:171]\n" \
"ds_read_b64_tr_b16 a[232:233], v220 offset:32768\n" \
"v_mfma_f32_16x16x32_bf16 v[172:175], a[228:231], a[112:115], v[172:175]\n" \
"ds_read_b64_tr_b16 a[234:235], v220 offset:34816\n" \
"v_mfma_f32_16x16x32_bf16 v[172:175], a[212:215], a[116:119], v[172:175]\n" \
"ds_read_b64_tr_b16 a[236:237], v220 offset:36864\n" \
"v_mfma_f32_16x16x32_bf16 v[160:163], a[196:199], a[72:75], v[160:163]\n" \
"ds_read_b64_tr_b16 a[238:239], v220 offset:38912\n" \
"v_mfma_f32_16x16x32_bf16 v[160:163], a[200:203], a[76:79], v[160:163]\n" \
"ds_read_b64_tr_b16 a[208:209], v220 offset:40960\n" \
"s_add_u32 m0, %[m0b], 49152\n" \
"s_cmp_gt_u32 s48, 6\n" \
"s_cselect_b32 s49, 6, 7\n" \
"s_lshl_b32 s49, s49, 18\n" \
"s_or_b32 s49, s49, s58\n" \
"buffer_load_dwordx4 %[wo0], %[srdt], s49 offen offset:0 lds\n" \
"v_mfma_f32_16x16x32_bf16 v[164:167], a[196:199], a[88:91], v[164:167]\n" \
"ds_read_b64_tr_b16 a[210:211], v220 offset:43008\n" \
"v_mfma_f32_16x16x32_bf16 v[164:167], a[200:203], a[92:95], v[164:167]\n" \
"ds_read_b64_tr_b16 a[228:229], v220 offset:45056\n" \
"buffer_load_dwordx4 a[56:59], v210, %[srdz], s50 offen offset:2048 sc1\n" \
"v_mfma_f32_16x16x32_bf16 v[168:171], a[196:199], a[104:107], v[168:171]\n" \
"ds_read_b64_tr_b16 a[230:231], v220 offset:47104\n" \
"v_mfma_f32_16x16x32_bf16 v[168:171], a[200:203], a[108:111], v[168:171]\n" \
"buffer_load_dwordx4 a[60:63], v210, %[srdz], s50 offen offset:3072 sc1\n" \
"v_mfma_f32_16x16x32_bf16 v[172:175], a[196:199], a[120:123], v[172:175]\n" \
"v_mfma_f32_16x16x32_bf16 v[172:175], a[200:203], a[124:127], v[172:175]\n" \
"s_waitcnt vmcnt(23) lgkmcnt(4)\n" \
"s_nop 0\n" \
"v_mfma_f32_16x16x32_bf16 v[176:179], a[204:207], a[64:67], v[176:179]\n" \
"v_mfma_f32_16x16x32_bf16 v[176:179], a[220:223], a[68:71], v[176:179]\n" \
"s_cmp_gt_u32 s48, 6\n" \
"s_cselect_b32 s50, 6, 7\n" \
"s_mul_i32 s50, s50, 262144\n" \
"buffer_load_dwordx4 a[128:131], %[oq0], %[srdz], s50 offen offset:0 sc1\n" \
"v_mfma_f32_16x16x32_bf16 v[180:183], a[204:207], a[80:83], v[180:183]\n" \
"v_mfma_f32_16x16x32_bf16 v[180:183], a[220:223], a[84:87], v[180:183]\n" \
"v_mfma_f32_16x16x32_bf16 v[184:187], a[204:207], a[96:99], v[184:187]\n" \
"v_mfma_f32_16x16x32_bf16 v[184:187], a[220:223], a[100:103], v[184:187]\n" \
"buffer_load_dwordx4 v212, %[srdt], s49 offen offset:1024 lds\n" \
"v_mfma_f32_16x16x32_bf16 v[188:191], a[204:207], a[112:115], v[188:191]\n" \
"ds_read_b64_tr_b16 a[212:213], v221 offset:32768\n" \
"v_mfma_f32_16x16x32_bf16 v[188:191], a[220:223], a[116:119], v[188:191]\n" \
"ds_read_b64_tr_b16 a[214:215], v221 offset:34816\n" \
"v_mfma_f32_16x16x32_bf16 v[128:131], a[232:235], v[0:3], v[192:195]\n" \
"ds_read_b64_tr_b16 a[196:197], v221 offset:36864\n" \
"buffer_load_dwordx4 a[132:135], %[oq0], %[srdz], s50 offen offset:1024 sc1\n" \
"v_mfma_f32_16x16x32_bf16 v[128:131], a[236:239], v[8:11], v[128:131]\n" \
"ds_read_b64_tr_b16 a[198:199], v221 offset:38912\n" \
"v_mfma_f32_16x16x32_bf16 v[132:135], a[232:235], v[32:35], v[196:199]\n" \
"buffer_load_dwordx4 a[136:139], %[oq0], %[srdz], s50 offen offset:2048 sc1\n" \
"v_mfma_f32_16x16x32_bf16 v[132:135], a[236:239], v[40:43], v[132:135]\n" \
"v_mfma_f32_16x16x32_bf16 v[136:139], a[232:235], v[64:67], v[200:203]\n" \
"v_mfma_f32_16x16x32_bf16 v[136:139], a[236:239], v[72:75], v[136:139]\n" \
"buffer_load_dwordx4 a[140:143], %[oq0], %[srdz], s50 offen offset:3072 sc1\n" \
"v_mfma_f32_16x16x32_bf16 v[140:143], a[232:235], v[96:99], v[204:207]\n" \
"ds_read_b64_tr_b16 a[200:201], v221 offset:40960\n" \
"v_mfma_f32_16x16x32_bf16 v[140:143], a[236:239], v[104:107], v[140:143]\n" \
"s_waitcnt vmcnt(26) lgkmcnt(5)\n" \
"s_nop 0\n" \
"v_mfma_f32_16x16x32_bf16 v[128:131], a[208:211], v[16:19], v[128:131]\n" \
"ds_read_b64_tr_b16 a[202:203], v221 offset:43008\n" \
"v_mfma_f32_16x16x32_bf16 v[128:131], a[228:231], v[24:27], v[128:131]\n" \
"ds_read_b64_tr_b16 a[204:205], v221 offset:45056\n" \
"v_mfma_f32_16x16x32_bf16 v[132:135], a[208:211], v[48:51], v[132:135]\n" \
"ds_read_b64_tr_b16 a[206:207], v221 offset:47104\n" \
"v_mfma_f32_16x16x32_bf16 v[132:135], a[228:231], v[56:59], v[132:135]\n" \
"buffer_load_dwordx4 a[144:147], v208, %[srdz], s50 offen offset:0 sc1\n" \
"v_mfma_f32_16x16x32_bf16 v[136:139], a[208:211], v[80:83], v[136:139]\n" \
"v_mfma_f32_16x16x32_bf16 v[136:139], a[228:231], v[88:91], v[136:139]\n" \
"buffer_load_dwordx4 v213, %[srdt], s49 offen offset:2048 lds\n" \
"v_mfma_f32_16x16x32_bf16 v[140:143], a[208:211], v[112:115], v[140:143]\n" \
"ds_read_b64_tr_b16 a[220:221], v220 offset:33280\n" \
"v_mfma_f32_16x16x32_bf16 v[140:143], a[228:231], v[120:123], v[140:143]\n" \
"ds_read_b64_tr_b16 a[222:223], v220 offset:35328\n" \
"v_mfma_f32_16x16x32_bf16 v[176:179], a[216:219], a[72:75], v[176:179]\n" \
"buffer_load_dwordx4 a[148:151], v208, %[srdz], s50 offen offset:1024 sc1\n" \
"v_mfma_f32_16x16x32_bf16 v[176:179], a[224:227], a[76:79], v[176:179]\n" \
"v_mfma_f32_16x16x32_bf16 v[180:183], a[216:219], a[88:91], v[180:183]\n" \
"buffer_load_dwordx4 a[152:155], v208, %[srdz], s50 offen offset:2048 sc1\n" \
"v_mfma_f32_16x16x32_bf16 v[180:183], a[224:227], a[92:95], v[180:183]\n" \
"v_mfma_f32_16x16x32_bf16 v[184:187], a[216:219], a[104:107], v[184:187]\n" \
"v_mfma_f32_16x16x32_bf16 v[184:187], a[224:227], a[108:111], v[184:187]\n" \
"v_cmp_u_f32_e64 s[44:45], v128, v132\n" \
"buffer_load_dwordx4 a[156:159], v208, %[srdz], s50 offen offset:3072 sc1\n" \
"v_mfma_f32_16x16x32_bf16 v[188:191], a[216:219], a[120:123], v[188:191]\n" \
"v_mfma_f32_16x16x32_bf16 v[188:191], a[224:227], a[124:127], v[188:191]\n" \
"v_cmp_u_f32_e64 s[56:57], v136, v140\n" \
"s_or_b64 s[56:57], s[56:57], s[44:45]\n" \
"s_cbranch_scc1 Lsp5_%=\n" \
"Lbk5_%=:\n" \
"s_waitcnt vmcnt(63) lgkmcnt(2)\n" \
"s_nop 0\n" \
"v_mfma_f32_16x16x32_bf16 v[144:147], a[212:215], v[0:3], v[144:147]\n" \
"ds_read_b64_tr_b16 a[216:217], v220 offset:37376\n" \
"v_mfma_f32_16x16x32_bf16 v[144:147], a[196:199], v[8:11], v[144:147]\n" \
"ds_read_b64_tr_b16 a[218:219], v220 offset:39424\n" \
"v_mfma_f32_16x16x32_bf16 v[148:151], a[212:215], v[32:35], v[148:151]\n" \
"ds_read_b64_tr_b16 a[208:209], v220 offset:41472\n" \
"v_mfma_f32_16x16x32_bf16 v[148:151], a[196:199], v[40:43], v[148:151]\n" \
"ds_read_b64_tr_b16 a[210:211], v220 offset:43520\n" \
"v_mfma_f32_16x16x32_bf16 v[152:155], a[212:215], v[64:67], v[152:155]\n" \
"ds_read_b64_tr_b16 a[224:225], v220 offset:45568\n" \
"buffer_load_dwordx4 a[160:163], v209, %[srdz], s50 offen offset:0 sc1\n" \
"v_mfma_f32_16x16x32_bf16 v[152:155], a[196:199], v[72:75], v[152:155]\n" \
"ds_read_b64_tr_b16 a[226:227], v220 offset:47616\n" \
"v_mfma_f32_16x16x32_bf16 v[156:159], a[212:215], v[96:99], v[156:159]\n" \
"ds_read_b64_tr_b16 a[228:229], v221 offset:33280\n" \
"buffer_load_dwordx4 v214, %[srdt], s49 offen offset:3072 lds\n" \
"v_mfma_f32_16x16x32_bf16 v[156:159], a[196:199], v[104:107], v[156:159]\n" \
"ds_read_b64_tr_b16 a[230:231], v221 offset:35328\n" \
"v_mfma_f32_16x16x32_bf16 v[144:147], a[200:203], v[16:19], v[144:147]\n" \
"ds_read_b64_tr_b16 a[232:233], v221 offset:41472\n" \
"buffer_load_dwordx4 a[164:167], v209, %[srdz], s50 offen offset:1024 sc1\n" \
"v_mfma_f32_16x16x32_bf16 v[144:147], a[204:207], v[24:27], v[144:147]\n" \
"ds_read_b64_tr_b16 a[234:235], v221 offset:43520\n" \
"v_mfma_f32_16x16x32_bf16 v[148:151], a[200:203], v[48:51], v[148:151]\n" \
"ds_read_b64_tr_b16 a[236:237], v221 offset:37376\n" \
"v_mfma_f32_16x16x32_bf16 v[148:151], a[204:207], v[56:59], v[148:151]\n" \
"ds_read_b64_tr_b16 a[238:239], v221 offset:39424\n" \
"v_mfma_f32_16x16x32_bf16 v[152:155], a[200:203], v[80:83], v[152:155]\n" \
"ds_read_b64_tr_b16 a[212:213], v221 offset:45568\n" \
"buffer_load_dwordx4 a[168:171], v209, %[srdz], s50 offen offset:2048 sc1\n" \
"v_mfma_f32_16x16x32_bf16 v[152:155], a[204:207], v[88:91], v[152:155]\n" \
"ds_read_b64_tr_b16 a[214:215], v221 offset:47616\n" \
"v_mfma_f32_16x16x32_bf16 v[156:159], a[200:203], v[112:115], v[156:159]\n" \
"buffer_load_dwordx4 a[172:175], v209, %[srdz], s50 offen offset:3072 sc1\n" \
"v_mfma_f32_16x16x32_bf16 v[156:159], a[204:207], v[120:123], v[156:159]\n" \
"s_waitcnt vmcnt(63) lgkmcnt(12)\n" \
"s_nop 0\n" \
"v_mfma_f32_16x16x32_bf16 v[160:163], a[220:223], v[0:3], v[160:163]\n" \
"v_mfma_f32_16x16x32_bf16 v[160:163], a[216:219], v[8:11], v[160:163]\n" \
"buffer_load_dwordx4 a[176:179], v210, %[srdz], s50 offen offset:0 sc1\n" \
"v_mfma_f32_16x16x32_bf16 v[164:167], a[220:223], v[32:35], v[164:167]\n" \
"v_mfma_f32_16x16x32_bf16 v[164:167], a[216:219], v[40:43], v[164:167]\n" \
"v_mfma_f32_16x16x32_bf16 v[168:171], a[220:223], v[64:67], v[168:171]\n" \
"s_waitcnt vmcnt(25) lgkmcnt(0)\n" \
"s_barrier\n" \
"v_mfma_f32_16x16x32_bf16 v[168:171], a[216:219], v[72:75], v[168:171]\n" \
"ds_read_b64_tr_b16 a[196:197], v220 offset:49152\n" \
"v_mfma_f32_16x16x32_bf16 v[172:175], a[220:223], v[96:99], v[172:175]\n" \
"ds_read_b64_tr_b16 a[198:199], v220 offset:51200\n" \
"v_mfma_f32_16x16x32_bf16 v[172:175], a[216:219], v[104:107], v[172:175]\n" \
"ds_read_b64_tr_b16 a[200:201], v220 offset:53248\n" \
"v_mfma_f32_16x16x32_bf16 v[160:163], a[208:211], v[16:19], v[160:163]\n" \
"ds_read_b64_tr_b16 a[202:203], v220 offset:55296\n" \
"buffer_load_dwordx4 a[180:183], v210, %[srdz], s50 offen offset:1024 sc1\n" \
"v_mfma_f32_16x16x32_bf16 v[160:163], a[224:227], v[24:27], v[160:163]\n" \
"ds_read_b64_tr_b16 a[204:205], v220 offset:57344\n" \
"v_mfma_f32_16x16x32_bf16 v[164:167], a[208:211], v[48:51], v[164:167]\n" \
"ds_read_b64_tr_b16 a[206:207], v220 offset:59392\n" \
"v_mfma_f32_16x16x32_bf16 v[164:167], a[224:227], v[56:59], v[164:167]\n" \
"ds_read_b64_tr_b16 a[220:221], v220 offset:61440\n" \
"v_mfma_f32_16x16x32_bf16 v[168:171], a[208:211], v[80:83], v[168:171]\n" \
"ds_read_b64_tr_b16 a[222:223], v220 offset:63488\n" \
"buffer_load_dwordx4 a[184:187], v210, %[srdz], s50 offen offset:2048 sc1\n" \
"v_mfma_f32_16x16x32_bf16 v[168:171], a[224:227], v[88:91], v[168:171]\n" \
"v_mfma_f32_16x16x32_bf16 v[172:175], a[208:211], v[112:115], v[172:175]\n" \
"buffer_load_dwordx4 a[188:191], v210, %[srdz], s50 offen offset:3072 sc1\n" \
"v_mfma_f32_16x16x32_bf16 v[172:175], a[224:227], v[120:123], v[172:175]\n" \
"s_waitcnt vmcnt(63) lgkmcnt(4)\n" \
"s_nop 0\n" \
"v_mfma_f32_16x16x32_bf16 v[176:179], a[228:231], v[0:3], v[176:179]\n" \
"v_mfma_f32_16x16x32_bf16 v[176:179], a[236:239], v[8:11], v[176:179]\n" \
"v_mfma_f32_16x16x32_bf16 v[180:183], a[228:231], v[32:35], v[180:183]\n" \
"v_mfma_f32_16x16x32_bf16 v[180:183], a[236:239], v[40:43], v[180:183]\n" \
"v_mfma_f32_16x16x32_bf16 v[184:187], a[228:231], v[64:67], v[184:187]\n" \
"v_mfma_f32_16x16x32_bf16 v[184:187], a[236:239], v[72:75], v[184:187]\n" \
"v_mfma_f32_16x16x32_bf16 v[188:191], a[228:231], v[96:99], v[188:191]\n" \
"ds_read_b64_tr_b16 a[216:217], v221 offset:49152\n" \
"s_waitcnt vmcnt(22)\n" \
"v_cmp_eq_u32_e32 vcc, 0, v232\n" \
"s_cbranch_vccnz Lspp_%=\n" \
"Lbkp_%=:\n" \
"s_nop 0\n" \
"v_mfma_f32_16x16x32_bf16 v[188:191], a[236:239], v[104:107], v[188:191]\n" \
"ds_read_b64_tr_b16 a[218:219], v221 offset:51200\n" \
"v_mfma_f32_16x16x32_bf16 v[192:195], a[196:199], a[0:3], v[128:131]\n" \
"ds_read_b64_tr_b16 a[208:209], v221 offset:53248\n" \
"v_mfma_f32_16x16x32_bf16 v[192:195], a[200:203], a[4:7], v[192:195]\n" \
"ds_read_b64_tr_b16 a[210:211], v221 offset:55296\n" \
"s_add_u32 m0, %[m0b], 0\n" \
"s_cmp_gt_u32 s48, 3\n" \
"s_cselect_b32 s49, 3, 4\n" \
"s_lshl_b32 s49, s49, 18\n" \
"s_or_b32 s49, s49, s59\n" \
"s_nop 0\n" \
"buffer_load_dwordx4 %[wo0], %[srdt], s49 offen offset:0 lds\n" \
"v_mfma_f32_16x16x32_bf16 v[196:199], a[196:199], a[16:19], v[132:135]\n" \
"v_mfma_f32_16x16x32_bf16 v[196:199], a[200:203], a[20:23], v[196:199]\n" \
"v_mfma_f32_16x16x32_bf16 v[200:203], a[196:199], a[32:35], v[136:139]\n" \
"v_mfma_f32_16x16x32_bf16 v[200:203], a[200:203], a[36:39], v[200:203]\n" \
"v_mfma_f32_16x16x32_bf16 v[204:207], a[196:199], a[48:51], v[140:143]\n" \
"ds_read_b64_tr_b16 a[224:225], v221 offset:57344\n" \
"v_mfma_f32_16x16x32_bf16 v[204:207], a[200:203], a[52:55], v[204:207]\n" \
"s_waitcnt vmcnt(20) lgkmcnt(5)\n" \
"s_nop 0\n" \
"v_mfma_f32_16x16x32_bf16 v[192:195], a[204:207], a[8:11], v[192:195]\n" \
"ds_read_b64_tr_b16 a[226:227], v221 offset:59392\n" \
"v_mfma_f32_16x16x32_bf16 v[192:195], a[220:223], a[12:15], v[192:195]\n" \
"ds_read_b64_tr_b16 a[228:229], v221 offset:61440\n" \
"v_mfma_f32_16x16x32_bf16 v[196:199], a[204:207], a[24:27], v[196:199]\n" \
"ds_read_b64_tr_b16 a[230:231], v221 offset:63488\n" \
"v_mfma_f32_16x16x32_bf16 v[196:199], a[220:223], a[28:31], v[196:199]\n" \
"buffer_load_dwordx4 v212, %[srdt], s49 offen offset:1024 lds\n" \
"v_mfma_f32_16x16x32_bf16 v[200:203], a[204:207], a[40:43], v[200:203]\n" \
"v_mfma_f32_16x16x32_bf16 v[200:203], a[220:223], a[44:47], v[200:203]\n" \
"v_mfma_f32_16x16x32_bf16 v[204:207], a[204:207], a[56:59], v[204:207]\n" \
"ds_read_b64_tr_b16 a[236:237], v220 offset:49664\n" \
"v_mfma_f32_16x16x32_bf16 v[204:207], a[220:223], a[60:63], v[204:207]\n" \
"ds_read_b64_tr_b16 a[238:239], v220 offset:51712\n" \
"v_mfma_f32_16x16x32_bf16 v[176:179], a[232:235], v[16:19], v[176:179]\n" \
"v_mfma_f32_16x16x32_bf16 v[176:179], a[212:215], v[24:27], v[176:179]\n" \
"v_mfma_f32_16x16x32_bf16 v[180:183], a[232:235], v[48:51], v[180:183]\n" \
"buffer_load_dwordx4 v213, %[srdt], s49 offen offset:2048 lds\n" \
"v_mfma_f32_16x16x32_bf16 v[180:183], a[212:215], v[56:59], v[180:183]\n" \
"v_mfma_f32_16x16x32_bf16 v[184:187], a[232:235], v[80:83], v[184:187]\n" \
"s_add_u32 m0, %[m0b], 98304\n" \
"s_mov_b32 s61, 0\n" \
"s_nop 0\n" \
"buffer_load_dwordx4 %[vdn], s[64:67], s61 offen offset:0 lds\n" \
"v_mfma_f32_16x16x32_bf16 v[184:187], a[212:215], v[88:91], v[184:187]\n" \
"v_cmp_u_f32_e64 s[44:45], v192, v196\n" \
"v_mfma_f32_16x16x32_bf16 v[188:191], a[232:235], v[112:115], v[188:191]\n" \
"v_mfma_f32_16x16x32_bf16 v[188:191], a[212:215], v[120:123], v[188:191]\n" \
"v_cmp_u_f32_e64 s[56:57], v200, v204\n" \
"s_or_b64 s[56:57], s[56:57], s[44:45]\n" \
"s_cbranch_scc1 Lsp6_%=\n" \
"Lbk6_%=:\n" \
"s_waitcnt vmcnt(63) lgkmcnt(2)\n" \
"s_nop 0\n" \
"v_mfma_f32_16x16x32_bf16 v[144:147], a[216:219], a[0:3], v[144:147]\n" \
"ds_read_b64_tr_b16 a[232:233], v220 offset:53760\n" \
"v_mfma_f32_16x16x32_bf16 v[144:147], a[208:211], a[4:7], v[144:147]\n" \
"ds_read_b64_tr_b16 a[234:235], v220 offset:55808\n" \
"v_mfma_f32_16x16x32_bf16 v[148:151], a[216:219], a[16:19], v[148:151]\n" \
"ds_read_b64_tr_b16 a[196:197], v220 offset:57856\n" \
"v_mfma_f32_16x16x32_bf16 v[148:151], a[208:211], a[20:23], v[148:151]\n" \
"ds_read_b64_tr_b16 a[198:199], v220 offset:59904\n" \
"v_mfma_f32_16x16x32_bf16 v[152:155], a[216:219], a[32:35], v[152:155]\n" \
"ds_read_b64_tr_b16 a[200:201], v220 offset:61952\n" \
"v_mfma_f32_16x16x32_bf16 v[152:155], a[208:211], a[36:39], v[152:155]\n" \
"ds_read_b64_tr_b16 a[202:203], v220 offset:64000\n" \
"v_mfma_f32_16x16x32_bf16 v[156:159], a[216:219], a[48:51], v[156:159]\n" \
"ds_read_b64_tr_b16 a[204:205], v221 offset:49664\n" \
"v_mfma_f32_16x16x32_bf16 v[156:159], a[208:211], a[52:55], v[156:159]\n" \
"ds_read_b64_tr_b16 a[206:207], v221 offset:51712\n" \
"s_add_u32 m0, %[m0b], 0\n" \
"s_nop 0\n" \
"buffer_load_dwordx4 v214, %[srdt], s49 offen offset:3072 lds\n" \
"v_mfma_f32_16x16x32_bf16 v[144:147], a[224:227], a[8:11], v[144:147]\n" \
"ds_read_b64_tr_b16 a[212:213], v221 offset:57856\n" \
"v_mfma_f32_16x16x32_bf16 v[144:147], a[228:231], a[12:15], v[144:147]\n" \
"ds_read_b64_tr_b16 a[214:215], v221 offset:59904\n" \
"v_mfma_f32_16x16x32_bf16 v[148:151], a[224:227], a[24:27], v[148:151]\n" \
"ds_read_b64_tr_b16 a[220:221], v221 offset:53760\n" \
"v_mfma_f32_16x16x32_bf16 v[148:151], a[228:231], a[28:31], v[148:151]\n" \
"ds_read_b64_tr_b16 a[222:223], v221 offset:55808\n" \
"v_mfma_f32_16x16x32_bf16 v[152:155], a[224:227], a[40:43], v[152:155]\n" \
"ds_read_b64_tr_b16 a[216:217], v221 offset:61952\n" \
"v_mfma_f32_16x16x32_bf16 v[152:155], a[228:231], a[44:47], v[152:155]\n" \
"ds_read_b64_tr_b16 a[218:219], v221 offset:64000\n" \
"v_mfma_f32_16x16x32_bf16 v[156:159], a[224:227], a[56:59], v[156:159]\n" \
"s_add_u32 m0, %[m0b], 99328\n" \
"s_mov_b32 s61, 4096\n" \
"buffer_load_dwordx4 %[vdn], s[64:67], s61 offen offset:0 lds\n" \
"v_mfma_f32_16x16x32_bf16 v[156:159], a[228:231], a[60:63], v[156:159]\n" \
"s_waitcnt vmcnt(63) lgkmcnt(12)\n" \
"s_nop 0\n" \
"v_mfma_f32_16x16x32_bf16 v[160:163], a[236:239], a[0:3], v[160:163]\n" \
"v_mfma_f32_16x16x32_bf16 v[160:163], a[232:235], a[4:7], v[160:163]\n" \
"v_mfma_f32_16x16x32_bf16 v[164:167], a[236:239], a[16:19], v[164:167]\n" \
"v_mfma_f32_16x16x32_bf16 v[164:167], a[232:235], a[20:23], v[164:167]\n" \
"s_add_u32 m0, %[m0b], 16384\n" \
"s_cmp_gt_u32 s48, 4\n" \
"s_cselect_b32 s49, 4, 5\n" \
"s_lshl_b32 s49, s49, 18\n" \
"s_or_b32 s49, s49, s59\n" \
"buffer_load_dwordx4 %[wo0], %[srdt], s49 offen offset:0 lds\n" \
"v_mfma_f32_16x16x32_bf16 v[168:171], a[236:239], a[32:35], v[168:171]\n" \
"s_waitcnt vmcnt(14) lgkmcnt(0)\n" \
"s_barrier\n" \
"v_mfma_f32_16x16x32_bf16 v[168:171], a[232:235], a[36:39], v[168:171]\n" \
"ds_read_b64_tr_b16 a[208:209], v215 offset:49152\n" \
"v_mfma_f32_16x16x32_bf16 v[172:175], a[236:239], a[48:51], v[172:175]\n" \
"ds_read_b64_tr_b16 a[210:211], v215 offset:51200\n" \
"v_mfma_f32_16x16x32_bf16 v[172:175], a[232:235], a[52:55], v[172:175]\n" \
"ds_read_b64_tr_b16 a[224:225], v215 offset:53248\n" \
"v_mfma_f32_16x16x32_bf16 v[160:163], a[196:199], a[8:11], v[160:163]\n" \
"ds_read_b64_tr_b16 a[226:227], v215 offset:55296\n" \
"v_mfma_f32_16x16x32_bf16 v[160:163], a[200:203], a[12:15], v[160:163]\n" \
"ds_read_b64_tr_b16 a[228:229], v215 offset:57344\n" \
"v_mfma_f32_16x16x32_bf16 v[164:167], a[196:199], a[24:27], v[164:167]\n" \
"ds_read_b64_tr_b16 a[230:231], v215 offset:59392\n" \
"v_mfma_f32_16x16x32_bf16 v[164:167], a[200:203], a[28:31], v[164:167]\n" \
"ds_read_b64_tr_b16 a[236:237], v215 offset:61440\n" \
"v_mfma_f32_16x16x32_bf16 v[168:171], a[196:199], a[40:43], v[168:171]\n" \
"ds_read_b64_tr_b16 a[238:239], v215 offset:63488\n" \
"v_mfma_f32_16x16x32_bf16 v[168:171], a[200:203], a[44:47], v[168:171]\n" \
"buffer_load_dwordx4 v212, %[srdt], s49 offen offset:1024 lds\n" \
"v_mfma_f32_16x16x32_bf16 v[172:175], a[196:199], a[56:59], v[172:175]\n" \
"v_mfma_f32_16x16x32_bf16 v[172:175], a[200:203], a[60:63], v[172:175]\n" \
"s_waitcnt vmcnt(10) lgkmcnt(4)\n" \
"s_nop 0\n" \
"v_mfma_f32_16x16x32_bf16 v[176:179], a[204:207], a[0:3], v[176:179]\n" \
"s_add_u32 m0, %[m0b], 100352\n" \
"s_mov_b32 s61, 8192\n" \
"buffer_load_dwordx4 %[vdn], s[64:67], s61 offen offset:0 lds\n" \
"v_mfma_f32_16x16x32_bf16 v[176:179], a[220:223], a[4:7], v[176:179]\n" \
"v_mfma_f32_16x16x32_bf16 v[180:183], a[204:207], a[16:19], v[180:183]\n" \
"v_mfma_f32_16x16x32_bf16 v[180:183], a[220:223], a[20:23], v[180:183]\n" \
"v_mfma_f32_16x16x32_bf16 v[184:187], a[204:207], a[32:35], v[184:187]\n" \
"v_mfma_f32_16x16x32_bf16 v[184:187], a[220:223], a[36:39], v[184:187]\n" \
"v_mfma_f32_16x16x32_bf16 v[188:191], a[204:207], a[48:51], v[188:191]\n" \
"ds_read_b64_tr_b16 a[232:233], v227 offset:49152\n" \
"v_mfma_f32_16x16x32_bf16 v[188:191], a[220:223], a[52:55], v[188:191]\n" \
"ds_read_b64_tr_b16 a[234:235], v227 offset:51200\n" \
"v_mfma_f32_16x16x32_bf16 v[128:131], a[208:211], a[128:131], v[192:195]\n" \
"ds_read_b64_tr_b16 a[196:197], v227 offset:53248\n" \
"v_mfma_f32_16x16x32_bf16 v[128:131], a[224:227], a[132:135], v[128:131]\n" \
"ds_read_b64_tr_b16 a[198:199], v227 offset:55296\n" \
"v_mfma_f32_16x16x32_bf16 v[132:135], a[208:211], a[144:147], v[196:199]\n" \
"s_add_u32 m0, %[m0b], 16384\n" \
"s_nop 0\n" \
"s_nop 0\n" \
"buffer_load_dwordx4 v213, %[srdt], s49 offen offset:2048 lds\n" \
"v_mfma_f32_16x16x32_bf16 v[132:135], a[224:227], a[148:151], v[132:135]\n" \
"v_mfma_f32_16x16x32_bf16 v[136:139], a[208:211], a[160:163], v[200:203]\n" \
"v_mfma_f32_16x16x32_bf16 v[136:139], a[224:227], a[164:167], v[136:139]\n" \
"v_mfma_f32_16x16x32_bf16 v[140:143], a[208:211], a[176:179], v[204:207]\n" \
"ds_read_b64_tr_b16 a[200:201], v227 offset:57344\n" \
"v_mfma_f32_16x16x32_bf16 v[140:143], a[224:227], a[180:183], v[140:143]\n" \
"s_waitcnt vmcnt(10) lgkmcnt(5)\n" \
"s_nop 0\n" \
"v_mfma_f32_16x16x32_bf16 v[128:131], a[228:231], a[136:139], v[128:131]\n" \
"ds_read_b64_tr_b16 a[202:203], v227 offset:59392\n" \
"v_mfma_f32_16x16x32_bf16 v[128:131], a[236:239], a[140:143], v[128:131]\n" \
"ds_read_b64_tr_b16 a[204:205], v227 offset:61440\n" \
"s_add_u32 m0, %[m0b], 101376\n" \
"s_mov_b32 s61, 12288\n" \
"buffer_load_dwordx4 %[vdn], s[64:67], s61 offen offset:0 lds\n" \
"v_mfma_f32_16x16x32_bf16 v[132:135], a[228:231], a[152:155], v[132:135]\n" \
"ds_read_b64_tr_b16 a[206:207], v227 offset:63488\n" \
"v_mfma_f32_16x16x32_bf16 v[132:135], a[236:239], a[156:159], v[132:135]\n" \
"s_add_u32 m0, %[m0b], 16384\n" \
"s_nop 0\n" \
"s_nop 0\n" \
"buffer_load_dwordx4 v214, %[srdt], s49 offen offset:3072 lds\n" \
"v_mfma_f32_16x16x32_bf16 v[136:139], a[228:231], a[168:171], v[136:139]\n" \
"v_mfma_f32_16x16x32_bf16 v[136:139], a[236:239], a[172:175], v[136:139]\n" \
"v_mfma_f32_16x16x32_bf16 v[140:143], a[228:231], a[184:187], v[140:143]\n" \
"ds_read_b64_tr_b16 a[220:221], v215 offset:49664\n" \
"v_mfma_f32_16x16x32_bf16 v[140:143], a[236:239], a[188:191], v[140:143]\n" \
"ds_read_b64_tr_b16 a[222:223], v215 offset:51712\n" \
"v_mfma_f32_16x16x32_bf16 v[176:179], a[212:215], a[8:11], v[176:179]\n" \
"v_mfma_f32_16x16x32_bf16 v[176:179], a[216:219], a[12:15], v[176:179]\n" \
"v_mfma_f32_16x16x32_bf16 v[180:183], a[212:215], a[24:27], v[180:183]\n" \
"v_mfma_f32_16x16x32_bf16 v[180:183], a[216:219], a[28:31], v[180:183]\n" \
"v_mfma_f32_16x16x32_bf16 v[184:187], a[212:215], a[40:43], v[184:187]\n" \
"v_mfma_f32_16x16x32_bf16 v[184:187], a[216:219], a[44:47], v[184:187]\n" \
"v_cmp_u_f32_e64 s[44:45], v128, v132\n" \
"v_mfma_f32_16x16x32_bf16 v[188:191], a[212:215], a[56:59], v[188:191]\n" \
"v_mfma_f32_16x16x32_bf16 v[188:191], a[216:219], a[60:63], v[188:191]\n" \
"v_cmp_u_f32_e64 s[56:57], v136, v140\n" \
"s_or_b64 s[56:57], s[56:57], s[44:45]\n" \
"s_cbranch_scc1 Lsp7_%=\n" \
"Lbk7_%=:\n" \
"s_waitcnt vmcnt(63) lgkmcnt(2)\n" \
"s_nop 0\n" \
"v_mfma_f32_16x16x32_bf16 v[144:147], a[232:235], a[128:131], v[144:147]\n" \
"ds_read_b64_tr_b16 a[212:213], v215 offset:53760\n" \
"v_mfma_f32_16x16x32_bf16 v[144:147], a[196:199], a[132:135], v[144:147]\n" \
"ds_read_b64_tr_b16 a[214:215], v215 offset:55808\n" \
"v_mfma_f32_16x16x32_bf16 v[148:151], a[232:235], a[144:147], v[148:151]\n" \
"ds_read_b64_tr_b16 a[208:209], v215 offset:57856\n" \
"v_mfma_f32_16x16x32_bf16 v[148:151], a[196:199], a[148:151], v[148:151]\n" \
"ds_read_b64_tr_b16 a[210:211], v215 offset:59904\n" \
"s_lshl_b32 s61, %[wid], 10\n" \
"v_lshrrev_b32_e32 v231, 3, v224\n" \
"v_add_u32_e64 v231, s61, v231\n" \
"s_add_u32 m0, %[m0b], 32768\n" \
"s_cmp_gt_u32 s48, 5\n" \
"s_cselect_b32 s49, 5, 6\n" \
"s_lshl_b32 s49, s49, 18\n" \
"s_or_b32 s49, s49, s59\n" \
".if . - adaln_body != 53248\n" \
".error \"size model\"\n" \
".endif\n" \
"Lk12_%=:\n" \
"s_cbranch_execz Lk13_%=\n" \
"s_nop 0\n" \
"buffer_load_dwordx4 %[wo0], %[srdt], s49 offen offset:0 lds\n" \
"v_mfma_f32_16x16x32_bf16 v[152:155], a[232:235], a[160:163], v[152:155]\n" \
"ds_read_b64_tr_b16 a[216:217], v215 offset:61952\n" \
"v_readlane_b32 s44, %[tlo], 20\n" \
"v_readlane_b32 s45, %[thi], 20\n" \
"v_lshrrev_b32_e64 v236, 1, v231\n" \
"v_mfma_f32_16x16x32_bf16 v[152:155], a[196:199], a[164:167], v[152:155]\n" \
"ds_read_b64_tr_b16 a[218:219], v215 offset:64000\n" \
"v_mfma_f32_16x16x32_bf16 v[156:159], a[232:235], a[176:179], v[156:159]\n" \
"ds_read_b64_tr_b16 a[224:225], v227 offset:49664\n" \
"v_mfma_f32_16x16x32_bf16 v[156:159], a[196:199], a[180:183], v[156:159]\n" \
"ds_read_b64_tr_b16 a[226:227], v227 offset:51712\n" \
"v_mfma_f32_16x16x32_bf16 v[144:147], a[200:203], a[136:139], v[144:147]\n" \
"ds_read_b64_tr_b16 a[228:229], v227 offset:53760\n" \
"v_mfma_f32_16x16x32_bf16 v[144:147], a[204:207], a[140:143], v[144:147]\n" \
"ds_read_b64_tr_b16 a[230:231], v227 offset:55808\n" \
"v_mfma_f32_16x16x32_bf16 v[148:151], a[200:203], a[152:155], v[148:151]\n" \
"s_mov_b32 s61, 262144\n" \
"buffer_load_dwordx4 v[192:195], v231, %[srdz], s61 offen offset:0 sc1\n" \
"v_mfma_f32_16x16x32_bf16 v[148:151], a[204:207], a[156:159], v[148:151]\n" \
"v_mfma_f32_16x16x32_bf16 v[152:155], a[200:203], a[168:171], v[152:155]\n" \
"buffer_load_dwordx4 v212, %[srdt], s49 offen offset:1024 lds\n" \
"v_mfma_f32_16x16x32_bf16 v[152:155], a[204:207], a[172:175], v[152:155]\n" \
"v_mfma_f32_16x16x32_bf16 v[156:159], a[200:203], a[184:187], v[156:159]\n" \
"ds_read_b64_tr_b16 a[236:237], v227 offset:57856\n" \
"v_mfma_f32_16x16x32_bf16 v[156:159], a[204:207], a[188:191], v[156:159]\n" \
"s_waitcnt vmcnt(63) lgkmcnt(5)\n" \
"s_nop 0\n" \
"v_mfma_f32_16x16x32_bf16 v[160:163], a[220:223], a[128:131], v[160:163]\n" \
"ds_read_b64_tr_b16 a[238:239], v227 offset:59904\n" \
"v_mfma_f32_16x16x32_bf16 v[160:163], a[212:215], a[132:135], v[160:163]\n" \
"ds_read_b64_tr_b16 a[232:233], v227 offset:61952\n" \
"v_mfma_f32_16x16x32_bf16 v[164:167], a[220:223], a[144:147], v[164:167]\n" \
"ds_read_b64_tr_b16 a[234:235], v227 offset:64000\n" \
"s_mov_b32 s61, 266240\n" \
"buffer_load_dwordx4 v[196:199], v231, %[srdz], s61 offen offset:0 sc1\n" \
"v_mfma_f32_16x16x32_bf16 v[164:167], a[212:215], a[148:151], v[164:167]\n" \
"v_mfma_f32_16x16x32_bf16 v[168:171], a[220:223], a[160:163], v[168:171]\n" \
"v_cvt_pk_bf16_f32 v128, v128, v129\n" \
"buffer_load_dwordx4 v213, %[srdt], s49 offen offset:2048 lds\n" \
"v_mfma_f32_16x16x32_bf16 v[168:171], a[212:215], a[164:167], v[168:171]\n" \
"v_cvt_pk_bf16_f32 v129, v130, v131\n" \
"v_mfma_f32_16x16x32_bf16 v[172:175], a[220:223], a[176:179], v[172:175]\n" \
"v_cvt_pk_bf16_f32 v130, v144, v145\n" \
"v_mfma_f32_16x16x32_bf16 v[172:175], a[212:215], a[180:183], v[172:175]\n" \
"v_cvt_pk_bf16_f32 v131, v146, v147\n" \
"v_mfma_f32_16x16x32_bf16 v[160:163], a[208:211], a[136:139], v[160:163]\n" \
"s_add_u32 s55, s46, 0\n" \
"s_nop 0\n" \
"buffer_store_dwordx4 v[128:131], %[vxs], %[srdz], s55 offen offset:0 sc1\n" \
"v_mfma_f32_16x16x32_bf16 v[160:163], a[216:219], a[140:143], v[160:163]\n" \
"v_mfma_f32_16x16x32_bf16 v[164:167], a[208:211], a[152:155], v[164:167]\n" \
"v_cvt_pk_bf16_f32 v132, v132, v133\n" \
"s_mov_b32 s61, 270336\n" \
"buffer_load_dwordx4 v[200:203], v231, %[srdz], s61 offen offset:0 sc1\n" \
"v_mfma_f32_16x16x32_bf16 v[164:167], a[216:219], a[156:159], v[164:167]\n" \
"v_cvt_pk_bf16_f32 v133, v134, v135\n" \
"v_mfma_f32_16x16x32_bf16 v[168:171], a[208:211], a[168:171], v[168:171]\n" \
"s_waitcnt vmcnt(14) lgkmcnt(0)\n" \
"s_barrier\n" \
"v_mfma_f32_16x16x32_bf16 v[168:171], a[216:219], a[172:175], v[168:171]\n" \
"ds_read_b64_tr_b16 a[196:197], v215 offset:0\n" \
"v_cvt_pk_bf16_f32 v134, v148, v149\n" \
"v_mfma_f32_16x16x32_bf16 v[172:175], a[208:211], a[184:187], v[172:175]\n" \
"ds_read_b64_tr_b16 a[198:199], v215 offset:2048\n" \
"v_cvt_pk_bf16_f32 v135, v150, v151\n" \
"v_mfma_f32_16x16x32_bf16 v[172:175], a[216:219], a[188:191], v[172:175]\n" \
"s_waitcnt vmcnt(63) lgkmcnt(2)\n" \
"s_nop 0\n" \
"v_mfma_f32_16x16x32_bf16 v[176:179], a[224:227], a[128:131], v[176:179]\n" \
"ds_read_b64_tr_b16 a[200:201], v215 offset:4096\n" \
"v_cvt_pk_bf16_f32 v136, v136, v137\n" \
"v_mfma_f32_16x16x32_bf16 v[176:179], a[228:231], a[132:135], v[176:179]\n" \
"ds_read_b64_tr_b16 a[202:203], v215 offset:6144\n" \
"v_cvt_pk_bf16_f32 v137, v138, v139\n" \
"v_mfma_f32_16x16x32_bf16 v[180:183], a[224:227], a[144:147], v[180:183]\n" \
"ds_read_b64_tr_b16 a[204:205], v215 offset:8192\n" \
"s_add_u32 s55, s46, 524288\n" \
"buffer_store_dwordx4 v[132:135], %[vxs], %[srdz], s55 offen offset:0 sc1\n" \
"v_mfma_f32_16x16x32_bf16 v[180:183], a[228:231], a[148:151], v[180:183]\n" \
"ds_read_b64_tr_b16 a[206:207], v215 offset:10240\n" \
"v_cvt_pk_bf16_f32 v138, v152, v153\n" \
"v_mfma_f32_16x16x32_bf16 v[184:187], a[224:227], a[160:163], v[184:187]\n" \
"ds_read_b64_tr_b16 a[220:221], v215 offset:12288\n" \
"v_cvt_pk_bf16_f32 v139, v154, v155\n" \
"s_mov_b32 s61, 274432\n" \
"buffer_load_dwordx4 v[204:207], v231, %[srdz], s61 offen offset:0 sc1\n" \
"v_mfma_f32_16x16x32_bf16 v[184:187], a[228:231], a[164:167], v[184:187]\n" \
"ds_read_b64_tr_b16 a[222:223], v215 offset:14336\n" \
"v_mfma_f32_16x16x32_bf16 v[188:191], a[224:227], a[176:179], v[188:191]\n" \
"ds_read_b64_tr_b16 a[212:213], v227 offset:0\n" \
"v_cvt_pk_bf16_f32 v140, v140, v141\n" \
"buffer_load_dwordx4 v214, %[srdt], s49 offen offset:3072 lds\n" \
"v_mfma_f32_16x16x32_bf16 v[188:191], a[228:231], a[180:183], v[188:191]\n" \
"ds_read_b64_tr_b16 a[214:215], v227 offset:2048\n" \
"v_cvt_pk_bf16_f32 v141, v142, v143\n" \
"v_mfma_f32_16x16x32_bf16 v[176:179], a[236:239], a[136:139], v[176:179]\n" \
"ds_read_b64_tr_b16 a[208:209], v227 offset:4096\n" \
"s_add_u32 s55, s46, 1048576\n" \
"buffer_store_dwordx4 v[136:139], %[vxs], %[srdz], s55 offen offset:0 sc1\n" \
"v_mfma_f32_16x16x32_bf16 v[176:179], a[232:235], a[140:143], v[176:179]\n" \
"ds_read_b64_tr_b16 a[210:211], v227 offset:6144\n" \
"v_cvt_pk_bf16_f32 v142, v156, v157\n" \
"v_mfma_f32_16x16x32_bf16 v[180:183], a[236:239], a[152:155], v[180:183]\n" \
"v_cvt_pk_bf16_f32 v143, v158, v159\n" \
"v_mfma_f32_16x16x32_bf16 v[180:183], a[232:235], a[156:159], v[180:183]\n" \
"s_add_u32 s55, s46, 1572864\n" \
"buffer_store_dwordx4 v[140:143], %[vxs], %[srdz], s55 offen offset:0 sc1\n" \
"v_mfma_f32_16x16x32_bf16 v[184:187], a[236:239], a[168:171], v[184:187]\n" \
"v_mfma_f32_16x16x32_bf16 v[184:187], a[232:235], a[172:175], v[184:187]\n" \
"global_load_dwordx2 v[232:233], v236, s[44:45]\n" \
"v_mfma_f32_16x16x32_bf16 v[188:191], a[236:239], a[184:187], v[188:191]\n" \
"ds_read_b64_tr_b16 a[216:217], v227 offset:8192\n" \
"v_mfma_f32_16x16x32_bf16 v[188:191], a[232:235], a[188:191], v[188:191]\n" \
"s_waitcnt vmcnt(63) lgkmcnt(5)\n" \
"s_nop 0\n" \
"v_mfma_f32_16x16x32_bf16 v[4:7], a[196:199], a[64:67], v[4:7]\n" \
"ds_read_b64_tr_b16 a[218:219], v227 offset:10240\n" \
"v_mfma_f32_16x16x32_bf16 v[4:7], a[200:203], a[68:71], v[4:7]\n" \
"ds_read_b64_tr_b16 a[224:225], v227 offset:12288\n" \
"v_mfma_f32_16x16x32_bf16 v[36:39], a[196:199], a[80:83], v[36:39]\n" \
"ds_read_b64_tr_b16 a[226:227], v227 offset:14336\n" \
"v_mfma_f32_16x16x32_bf16 v[36:39], a[200:203], a[84:87], v[36:39]\n" \
"v_mfma_f32_16x16x32_bf16 v[68:71], a[196:199], a[96:99], v[68:71]\n" \
"v_cvt_pk_bf16_f32 v160, v160, v161\n" \
"v_mfma_f32_16x16x32_bf16 v[68:71], a[200:203], a[100:103], v[68:71]\n" \
"v_cvt_pk_bf16_f32 v161, v162, v163\n" \
"v_mfma_f32_16x16x32_bf16 v[100:103], a[196:199], a[112:115], v[100:103]\n" \
"ds_read_b64_tr_b16 a[228:229], v215 offset:512\n" \
"v_cvt_pk_bf16_f32 v162, v176, v177\n" \
"v_mfma_f32_16x16x32_bf16 v[100:103], a[200:203], a[116:119], v[100:103]\n" \
"ds_read_b64_tr_b16 a[230:231], v215 offset:2560\n" \
"v_cvt_pk_bf16_f32 v163, v178, v179\n" \
"v_mfma_f32_16x16x32_bf16 v[4:7], a[204:207], a[72:75], v[4:7]\n" \
"ds_read_b64_tr_b16 a[236:237], v215 offset:4608\n" \
"v_mfma_f32_16x16x32_bf16 v[4:7], a[220:223], a[76:79], v[4:7]\n" \
"ds_read_b64_tr_b16 a[238:239], v215 offset:6656\n" \
"v_mfma_f32_16x16x32_bf16 v[36:39], a[204:207], a[88:91], v[36:39]\n" \
"s_add_u32 s55, s46, 4096\n" \
"buffer_store_dwordx4 v[160:163], %[vxs], %[srdz], s55 offen offset:0 sc1\n" \
"v_mfma_f32_16x16x32_bf16 v[36:39], a[220:223], a[92:95], v[36:39]\n" \
"v_cvt_pk_bf16_f32 v164, v164, v165\n" \
"v_cvt_pk_bf16_f32 v165, v166, v167\n" \
"v_mfma_f32_16x16x32_bf16 v[68:71], a[204:207], a[104:107], v[68:71]\n" \
"v_cvt_pk_bf16_f32 v166, v180, v181\n" \
"s_add_u32 m0, %[m0b], 49152\n" \
"s_cmp_gt_u32 s48, 6\n" \
"s_cselect_b32 s49, 6, 7\n" \
"s_lshl_b32 s49, s49, 18\n" \
"s_or_b32 s49, s49, s59\n" \
"buffer_load_dwordx4 %[wo0], %[srdt], s49 offen offset:0 lds\n" \
"v_mfma_f32_16x16x32_bf16 v[68:71], a[220:223], a[108:111], v[68:71]\n" \
"v_cvt_pk_bf16_f32 v167, v182, v183\n" \
"v_mfma_f32_16x16x32_bf16 v[100:103], a[204:207], a[120:123], v[100:103]\n" \
"ds_read_b64_tr_b16 a[232:233], v215 offset:8704\n" \
"v_mfma_f32_16x16x32_bf16 v[100:103], a[220:223], a[124:127], v[100:103]\n" \
"s_waitcnt vmcnt(63) lgkmcnt(5)\n" \
"s_nop 0\n" \
"v_mfma_f32_16x16x32_bf16 v[12:15], a[212:215], a[64:67], v[12:15]\n" \
"ds_read_b64_tr_b16 a[234:235], v215 offset:10752\n" \
"v_cvt_pk_bf16_f32 v168, v168, v169\n" \
"v_mfma_f32_16x16x32_bf16 v[12:15], a[208:211], a[68:71], v[12:15]\n" \
"ds_read_b64_tr_b16 a[196:197], v215 offset:12800\n" \
"s_add_u32 s55, s46, 528384\n" \
"buffer_store_dwordx4 v[164:167], %[vxs], %[srdz], s55 offen offset:0 sc1\n" \
"v_mfma_f32_16x16x32_bf16 v[44:47], a[212:215], a[80:83], v[44:47]\n" \
"ds_read_b64_tr_b16 a[198:199], v215 offset:14848\n" \
"v_cvt_pk_bf16_f32 v169, v170, v171\n" \
"v_mfma_f32_16x16x32_bf16 v[44:47], a[208:211], a[84:87], v[44:47]\n" \
"v_cvt_pk_bf16_f32 v170, v184, v185\n" \
"v_cvt_pk_bf16_f32 v171, v186, v187\n" \
"v_mfma_f32_16x16x32_bf16 v[76:79], a[212:215], a[96:99], v[76:79]\n" \
"s_add_u32 s55, s46, 1052672\n" \
"buffer_store_dwordx4 v[168:171], %[vxs], %[srdz], s55 offen offset:0 sc1\n" \
"v_mfma_f32_16x16x32_bf16 v[76:79], a[208:211], a[100:103], v[76:79]\n" \
"v_mfma_f32_16x16x32_bf16 v[108:111], a[212:215], a[112:115], v[108:111]\n" \
"ds_read_b64_tr_b16 a[200:201], v227 offset:512\n" \
"v_cvt_pk_bf16_f32 v172, v172, v173\n" \
"buffer_load_dwordx4 v212, %[srdt], s49 offen offset:1024 lds\n" \
"v_mfma_f32_16x16x32_bf16 v[108:111], a[208:211], a[116:119], v[108:111]\n" \
"ds_read_b64_tr_b16 a[202:203], v227 offset:2560\n" \
"v_cvt_pk_bf16_f32 v173, v174, v175\n" \
"v_mfma_f32_16x16x32_bf16 v[12:15], a[216:219], a[72:75], v[12:15]\n" \
"ds_read_b64_tr_b16 a[204:205], v227 offset:4608\n" \
"v_cvt_pk_bf16_f32 v174, v188, v189\n" \
"v_mfma_f32_16x16x32_bf16 v[12:15], a[224:227], a[76:79], v[12:15]\n" \
"ds_read_b64_tr_b16 a[206:207], v227 offset:6656\n" \
"v_cvt_pk_bf16_f32 v175, v190, v191\n" \
"v_mfma_f32_16x16x32_bf16 v[44:47], a[216:219], a[88:91], v[44:47]\n" \
"s_add_u32 s55, s46, 1576960\n" \
"buffer_store_dwordx4 v[172:175], %[vxs], %[srdz], s55 offen offset:0 sc1\n" \
"v_mfma_f32_16x16x32_bf16 v[44:47], a[224:227], a[92:95], v[44:47]\n" \
"v_mfma_f32_16x16x32_bf16 v[76:79], a[216:219], a[104:107], v[76:79]\n" \
"buffer_load_dwordx4 v213, %[srdt], s49 offen offset:2048 lds\n" \
"v_mfma_f32_16x16x32_bf16 v[76:79], a[224:227], a[108:111], v[76:79]\n" \
"v_mfma_f32_16x16x32_bf16 v[108:111], a[216:219], a[120:123], v[108:111]\n" \
"ds_read_b64_tr_b16 a[220:221], v227 offset:8704\n" \
"v_accvgpr_read_b32 v129, a240\n" \
"v_mfma_f32_16x16x32_bf16 v[108:111], a[224:227], a[124:127], v[108:111]\n" \
"s_waitcnt vmcnt(63) lgkmcnt(5)\n" \
"s_nop 0\n" \
"v_mfma_f32_16x16x32_bf16 v[20:23], a[228:231], a[64:67], v[20:23]\n" \
"ds_read_b64_tr_b16 a[222:223], v227 offset:10752\n" \
"v_lshlrev_b32_e64 v128, 16, v129\n" \
"v_mfma_f32_16x16x32_bf16 v[20:23], a[236:239], a[68:71], v[20:23]\n" \
"ds_read_b64_tr_b16 a[212:213], v227 offset:12800\n" \
"v_and_b32_e32 v129, 0xffff0000, v129\n" \
"v_mfma_f32_16x16x32_bf16 v[52:55], a[228:231], a[80:83], v[52:55]\n" \
"ds_read_b64_tr_b16 a[214:215], v227 offset:14848\n" \
"v_accvgpr_read_b32 v131, a241\n" \
"v_mfma_f32_16x16x32_bf16 v[52:55], a[236:239], a[84:87], v[52:55]\n" \
"v_lshlrev_b32_e64 v130, 16, v131\n" \
"v_and_b32_e32 v131, 0xffff0000, v131\n" \
"global_load_dword v237, %[vct], %[pct]\n" \
"v_mfma_f32_16x16x32_bf16 v[84:87], a[228:231], a[96:99], v[84:87]\n" \
"v_accvgpr_read_b32 v133, a242\n" \
"v_mfma_f32_16x16x32_bf16 v[84:87], a[236:239], a[100:103], v[84:87]\n" \
"v_lshlrev_b32_e64 v132, 16, v133\n" \
"s_add_u32 m0, %[m0b], 114688\n" \
"s_mov_b32 s61, 16384\n" \
"buffer_load_dwordx4 %[vdn], s[64:67], s61 offen offset:0 lds\n" \
"v_mfma_f32_16x16x32_bf16 v[116:119], a[228:231], a[112:115], v[116:119]\n" \
"v_and_b32_e32 v133, 0xffff0000, v133\n" \
"v_mfma_f32_16x16x32_bf16 v[116:119], a[236:239], a[116:119], v[116:119]\n" \
"v_accvgpr_read_b32 v135, a243\n" \
"s_add_u32 m0, %[m0b], 49152\n" \
"s_nop 0\n" \
"s_nop 0\n" \
"buffer_load_dwordx4 v214, %[srdt], s49 offen offset:3072 lds\n" \
"v_mfma_f32_16x16x32_bf16 v[20:23], a[232:235], a[72:75], v[20:23]\n" \
"v_lshlrev_b32_e64 v134, 16, v135\n" \
"v_mfma_f32_16x16x32_bf16 v[20:23], a[196:199], a[76:79], v[20:23]\n" \
"v_and_b32_e32 v135, 0xffff0000, v135\n" \
"v_mfma_f32_16x16x32_bf16 v[52:55], a[232:235], a[88:91], v[52:55]\n" \
"v_accvgpr_read_b32 v137, a244\n" \
"v_mfma_f32_16x16x32_bf16 v[52:55], a[196:199], a[92:95], v[52:55]\n" \
"v_lshlrev_b32_e64 v136, 16, v137\n" \
"v_mfma_f32_16x16x32_bf16 v[84:87], a[232:235], a[104:107], v[84:87]\n" \
"s_waitcnt vmcnt(23) lgkmcnt(0)\n" \
"s_barrier\n" \
"v_mfma_f32_16x16x32_bf16 v[84:87], a[196:199], a[108:111], v[84:87]\n" \
"ds_read_b64_tr_b16 a[208:209], v215 offset:16384\n" \
"v_and_b32_e32 v137, 0xffff0000, v137\n" \
"v_mfma_f32_16x16x32_bf16 v[116:119], a[232:235], a[120:123], v[116:119]\n" \
"ds_read_b64_tr_b16 a[210:211], v215 offset:18432\n" \
"v_accvgpr_read_b32 v139, a245\n" \
"v_mfma_f32_16x16x32_bf16 v[116:119], a[196:199], a[124:127], v[116:119]\n" \
"s_waitcnt vmcnt(63) lgkmcnt(2)\n" \
"s_nop 0\n" \
"v_mfma_f32_16x16x32_bf16 v[28:31], a[200:203], a[64:67], v[28:31]\n" \
"ds_read_b64_tr_b16 a[216:217], v215 offset:20480\n" \
"v_lshlrev_b32_e64 v138, 16, v139\n" \
"v_mfma_f32_16x16x32_bf16 v[28:31], a[204:207], a[68:71], v[28:31]\n" \
"ds_read_b64_tr_b16 a[218:219], v215 offset:22528\n" \
"v_and_b32_e32 v139, 0xffff0000, v139\n" \
"v_mfma_f32_16x16x32_bf16 v[60:63], a[200:203], a[80:83], v[60:63]\n" \
"ds_read_b64_tr_b16 a[224:225], v215 offset:24576\n" \
"v_accvgpr_read_b32 v141, a246\n" \
"v_mfma_f32_16x16x32_bf16 v[60:63], a[204:207], a[84:87], v[60:63]\n" \
"ds_read_b64_tr_b16 a[226:227], v215 offset:26624\n" \
"v_lshlrev_b32_e64 v140, 16, v141\n" \
"v_mfma_f32_16x16x32_bf16 v[92:95], a[200:203], a[96:99], v[92:95]\n" \
"ds_read_b64_tr_b16 a[228:229], v215 offset:28672\n" \
"v_and_b32_e32 v141, 0xffff0000, v141\n" \
"s_add_u32 m0, %[m0b], 115712\n" \
"s_mov_b32 s61, 20480\n" \
"buffer_load_dwordx4 %[vdn], s[64:67], s61 offen offset:0 lds\n" \
"v_mfma_f32_16x16x32_bf16 v[92:95], a[204:207], a[100:103], v[92:95]\n" \
"ds_read_b64_tr_b16 a[230:231], v215 offset:30720\n" \
"v_accvgpr_read_b32 v143, a247\n" \
"v_mfma_f32_16x16x32_bf16 v[124:127], a[200:203], a[112:115], v[124:127]\n" \
"ds_read_b64_tr_b16 a[236:237], v227 offset:16384\n" \
"v_lshlrev_b32_e64 v142, 16, v143\n" \
"v_mfma_f32_16x16x32_bf16 v[124:127], a[204:207], a[116:119], v[124:127]\n" \
"ds_read_b64_tr_b16 a[238:239], v227 offset:18432\n" \
"v_and_b32_e32 v143, 0xffff0000, v143\n" \
"v_mfma_f32_16x16x32_bf16 v[28:31], a[220:223], a[72:75], v[28:31]\n" \
"ds_read_b64_tr_b16 a[232:233], v227 offset:20480\n" \
"v_accvgpr_read_b32 v145, a248\n" \
"v_mfma_f32_16x16x32_bf16 v[28:31], a[212:215], a[76:79], v[28:31]\n" \
"ds_read_b64_tr_b16 a[234:235], v227 offset:22528\n" \
"v_lshlrev_b32_e64 v144, 16, v145\n" \
"v_mfma_f32_16x16x32_bf16 v[60:63], a[220:223], a[88:91], v[60:63]\n" \
"v_and_b32_e32 v145, 0xffff0000, v145\n" \
"v_accvgpr_read_b32 v147, a249\n" \
"v_mfma_f32_16x16x32_bf16 v[60:63], a[212:215], a[92:95], v[60:63]\n" \
"v_lshlrev_b32_e64 v146, 16, v147\n" \
"v_and_b32_e32 v147, 0xffff0000, v147\n" \
"v_mfma_f32_16x16x32_bf16 v[92:95], a[220:223], a[104:107], v[92:95]\n" \
"v_accvgpr_read_b32 v149, a250\n" \
"v_mfma_f32_16x16x32_bf16 v[92:95], a[212:215], a[108:111], v[92:95]\n" \
"v_lshlrev_b32_e64 v148, 16, v149\n" \
"v_mfma_f32_16x16x32_bf16 v[124:127], a[220:223], a[120:123], v[124:127]\n" \
"ds_read_b64_tr_b16 a[196:197], v227 offset:24576\n" \
"v_and_b32_e32 v149, 0xffff0000, v149\n" \
"v_mfma_f32_16x16x32_bf16 v[124:127], a[212:215], a[124:127], v[124:127]\n" \
"s_waitcnt vmcnt(63) lgkmcnt(5)\n" \
"s_nop 0\n" \
"v_mfma_f32_16x16x32_bf16 v[4:7], a[208:211], v[0:3], v[4:7]\n" \
"ds_read_b64_tr_b16 a[198:199], v227 offset:26624\n" \
"v_accvgpr_read_b32 v151, a251\n" \
"v_mfma_f32_16x16x32_bf16 v[4:7], a[216:219], v[8:11], v[4:7]\n" \
"ds_read_b64_tr_b16 a[200:201], v227 offset:28672\n" \
"v_lshlrev_b32_e64 v150, 16, v151\n" \
"v_mfma_f32_16x16x32_bf16 v[36:39], a[208:211], v[32:35], v[36:39]\n" \
"ds_read_b64_tr_b16 a[202:203], v227 offset:30720\n" \
"v_and_b32_e32 v151, 0xffff0000, v151\n" \
"v_mfma_f32_16x16x32_bf16 v[36:39], a[216:219], v[40:43], v[36:39]\n" \
"v_accvgpr_read_b32 v153, a252\n" \
"v_lshlrev_b32_e64 v152, 16, v153\n" \
"s_add_u32 m0, %[m0b], 116736\n" \
"s_mov_b32 s61, 24576\n" \
"buffer_load_dwordx4 %[vdn], s[64:67], s61 offen offset:0 lds\n" \
"v_mfma_f32_16x16x32_bf16 v[68:71], a[208:211], v[64:67], v[68:71]\n" \
"v_and_b32_e32 v153, 0xffff0000, v153\n" \
"v_mfma_f32_16x16x32_bf16 v[68:71], a[216:219], v[72:75], v[68:71]\n" \
"v_accvgpr_read_b32 v155, a253\n" \
"v_mfma_f32_16x16x32_bf16 v[100:103], a[208:211], v[96:99], v[100:103]\n" \
"v_lshlrev_b32_e64 v154, 16, v155\n" \
"v_mfma_f32_16x16x32_bf16 v[100:103], a[216:219], v[104:107], v[100:103]\n" \
"v_and_b32_e32 v155, 0xffff0000, v155\n" \
"v_mfma_f32_16x16x32_bf16 v[4:7], a[224:227], v[16:19], v[4:7]\n" \
"v_accvgpr_read_b32 v157, a254\n" \
"v_mfma_f32_16x16x32_bf16 v[4:7], a[228:231], v[24:27], v[4:7]\n" \
"v_lshlrev_b32_e64 v156, 16, v157\n" \
"v_mfma_f32_16x16x32_bf16 v[36:39], a[224:227], v[48:51], v[36:39]\n" \
"v_and_b32_e32 v157, 0xffff0000, v157\n" \
"v_mfma_f32_16x16x32_bf16 v[36:39], a[228:231], v[56:59], v[36:39]\n" \
"v_accvgpr_read_b32 v159, a255\n" \
"v_mfma_f32_16x16x32_bf16 v[68:71], a[224:227], v[80:83], v[68:71]\n" \
"s_waitcnt vmcnt(15) lgkmcnt(15)\n" \
"s_barrier\n" \
"v_mfma_f32_16x16x32_bf16 v[68:71], a[228:231], v[88:91], v[68:71]\n" \
"ds_read_b64_tr_b16 a[204:205], v215 offset:32768\n" \
"v_lshlrev_b32_e64 v158, 16, v159\n" \
"v_mfma_f32_16x16x32_bf16 v[100:103], a[224:227], v[112:115], v[100:103]\n" \
"ds_read_b64_tr_b16 a[206:207], v215 offset:34816\n" \
"v_and_b32_e32 v159, 0xffff0000, v159\n" \
"v_mfma_f32_16x16x32_bf16 v[100:103], a[228:231], v[120:123], v[100:103]\n" \
"s_waitcnt vmcnt(63) lgkmcnt(2)\n" \
"s_nop 0\n" \
"v_mfma_f32_16x16x32_bf16 v[12:15], a[236:239], v[0:3], v[12:15]\n" \
"ds_read_b64_tr_b16 a[220:221], v215 offset:36864\n" \
"v_and_b32_e32 v208, 0x7ff, %[vx0]\n" \
"v_mfma_f32_16x16x32_bf16 v[12:15], a[232:235], v[8:11], v[12:15]\n" \
"ds_read_b64_tr_b16 a[222:223], v215 offset:38912\n" \
"v_lshlrev_b32_e64 v208, 1, v208\n" \
"v_mfma_f32_16x16x32_bf16 v[44:47], a[236:239], v[32:35], v[44:47]\n" \
"ds_read_b64_tr_b16 a[212:213], v215 offset:40960\n" \
"v_xor_b32_e32 v209, 0x100, v208\n" \
"v_mfma_f32_16x16x32_bf16 v[44:47], a[232:235], v[40:43], v[44:47]\n" \
"ds_read_b64_tr_b16 a[214:215], v215 offset:43008\n" \
"v_lshrrev_b32_e64 v210, 3, v224\n" \
"v_mfma_f32_16x16x32_bf16 v[76:79], a[236:239], v[64:67], v[76:79]\n" \
"ds_read_b64_tr_b16 a[208:209], v215 offset:45056\n" \
"v_and_b32_e64 v210, 48, v210\n" \
"v_mfma_f32_16x16x32_bf16 v[76:79], a[232:235], v[72:75], v[76:79]\n" \
"ds_read_b64_tr_b16 a[210:211], v215 offset:47104\n" \
"s_lshl_b32 s61, %[wid], 12\n" \
"s_sub_u32 s61, %[m0b], s61\n" \
"v_add_u32_e32 v208, s61, v208\n" \
"v_add_u32_e32 v209, s61, v209\n" \
"v_add_u32_e64 v210, s61, v210\n" \
"v_mfma_f32_16x16x32_bf16 v[108:111], a[236:239], v[96:99], v[108:111]\n" \
"ds_read_b64_tr_b16 a[216:217], v227 offset:32768\n" \
"v_mfma_f32_16x16x32_bf16 v[108:111], a[232:235], v[104:107], v[108:111]\n" \
"ds_read_b64_tr_b16 a[218:219], v227 offset:34816\n" \
"s_waitcnt vmcnt(12)\n" \
"s_nop 0\n" \
"v_and_or_b32 v236, v192, v193, 0\n" \
"s_add_u32 m0, %[m0b], 117760\n" \
"s_mov_b32 s61, 28672\n" \
"buffer_load_dwordx4 %[vdn], s[64:67], s61 offen offset:0 lds\n" \
"v_mfma_f32_16x16x32_bf16 v[12:15], a[196:199], v[16:19], v[12:15]\n" \
"ds_read_b64_tr_b16 a[224:225], v227 offset:36864\n" \
"v_bitop3_b32 v236, v236, v194, v195 bitop3:0x80\n" \
"v_mfma_f32_16x16x32_bf16 v[12:15], a[200:203], v[24:27], v[12:15]\n" \
"ds_read_b64_tr_b16 a[226:227], v227 offset:38912\n" \
"v_bitop3_b32 v236, v236, v196, v197 bitop3:0x80\n" \
"v_mfma_f32_16x16x32_bf16 v[44:47], a[196:199], v[48:51], v[44:47]\n" \
"v_bitop3_b32 v236, v236, v198, v199 bitop3:0x80\n" \
"v_mfma_f32_16x16x32_bf16 v[44:47], a[200:203], v[56:59], v[44:47]\n" \
"v_bitop3_b32 v236, v236, v200, v201 bitop3:0x80\n" \
"v_mfma_f32_16x16x32_bf16 v[76:79], a[196:199], v[80:83], v[76:79]\n" \
"v_bitop3_b32 v236, v236, v202, v203 bitop3:0x80\n" \
"v_mfma_f32_16x16x32_bf16 v[76:79], a[200:203], v[88:91], v[76:79]\n" \
"v_bitop3_b32 v236, v236, v204, v205 bitop3:0x80\n" \
"v_mfma_f32_16x16x32_bf16 v[108:111], a[196:199], v[112:115], v[108:111]\n" \
"ds_read_b64_tr_b16 a[228:229], v227 offset:40960\n" \
"v_bitop3_b32 v236, v236, v206, v207 bitop3:0x80\n" \
"v_mfma_f32_16x16x32_bf16 v[108:111], a[200:203], v[120:123], v[108:111]\n" \
"s_waitcnt vmcnt(63) lgkmcnt(5)\n" \
"s_nop 0\n" \
"v_mfma_f32_16x16x32_bf16 v[4:7], a[204:207], a[0:3], v[4:7]\n" \
"ds_read_b64_tr_b16 a[230:231], v227 offset:43008\n" \
"s_mov_b32 s54, 0x07060302\n" \
"v_and_b32_e64 v236, 1, v236\n" \
"v_mfma_f32_16x16x32_bf16 v[4:7], a[220:223], a[4:7], v[4:7]\n" \
"ds_read_b64_tr_b16 a[236:237], v227 offset:45056\n" \
"v_lshrrev_b32_e64 v218, 1, v231\n" \
"v_mfma_f32_16x16x32_bf16 v[36:39], a[204:207], a[16:19], v[36:39]\n" \
"ds_read_b64_tr_b16 a[238:239], v227 offset:47104\n" \
"v_cmp_eq_u32_e64 s[56:57], 0, v236\n" \
"v_mfma_f32_16x16x32_bf16 v[36:39], a[220:223], a[20:23], v[36:39]\n" \
"s_waitcnt vmcnt(7)\n" \
"v_mov_b32_e32 v230, 1\n" \
"buffer_store_dword v230, v225, %[srdz], s62 offen offset:68 sc1\n" \
"v_mfma_f32_16x16x32_bf16 v[68:71], a[204:207], a[32:35], v[68:71]\n" \
"s_lshl_b32 s61, %[wid], 12\n" \
"s_sub_u32 s61, %[m0b], s61\n" \
"v_add_u32_e32 v231, s61, v231\n" \
"v_add_u32_e32 v218, s61, v218\n" \
"v_mov_b32_e64 v219, s61\n" \
"ds_write_b128 v231, v[192:195] offset:0\n" \
"v_mfma_f32_16x16x32_bf16 v[68:71], a[220:223], a[36:39], v[68:71]\n" \
"v_lshlrev_b32_e64 v234, 16, v233\n" \
"v_and_b32_e32 v235, 0xffff0000, v233\n" \
"v_mfma_f32_16x16x32_bf16 v[100:103], a[204:207], a[48:51], v[100:103]\n" \
"v_and_b32_e32 v233, 0xffff0000, v232\n" \
"v_mfma_f32_16x16x32_bf16 v[100:103], a[220:223], a[52:55], v[100:103]\n" \
"v_lshlrev_b32_e64 v232, 16, v232\n" \
"v_mfma_f32_16x16x32_bf16 v[4:7], a[212:215], a[8:11], v[4:7]\n" \
"v_mul_f32_e64 v234, v202, v234\n" \
"v_mfma_f32_16x16x32_bf16 v[4:7], a[208:211], a[12:15], v[4:7]\n" \
"v_mul_f32_e64 v235, v203, v235\n" \
"v_mfma_f32_16x16x32_bf16 v[36:39], a[212:215], a[24:27], v[36:39]\n" \
"v_mul_f32_e64 v233, v201, v233\n" \
"v_mfma_f32_16x16x32_bf16 v[36:39], a[208:211], a[28:31], v[36:39]\n" \
"v_mul_f32_e64 v232, v200, v232\n" \
"v_mfma_f32_16x16x32_bf16 v[68:71], a[212:215], a[40:43], v[68:71]\n" \
"s_waitcnt vmcnt(4) lgkmcnt(15)\n" \
"s_barrier\n" \
"v_mfma_f32_16x16x32_bf16 v[68:71], a[208:211], a[44:47], v[68:71]\n" \
"ds_read_b64_tr_b16 a[232:233], v215 offset:49152\n" \
"v_add_f32_e64 v223, v234, v235\n" \
"v_mfma_f32_16x16x32_bf16 v[100:103], a[212:215], a[56:59], v[100:103]\n" \
"ds_read_b64_tr_b16 a[234:235], v215 offset:51200\n" \
"v_add_f32_e64 v222, v232, v233\n" \
"v_mfma_f32_16x16x32_bf16 v[100:103], a[208:211], a[60:63], v[100:103]\n" \
"s_waitcnt vmcnt(63) lgkmcnt(3)\n" \
"s_nop 0\n" \
"v_mfma_f32_16x16x32_bf16 v[12:15], a[216:219], a[0:3], v[12:15]\n" \
"ds_read_b64_tr_b16 a[196:197], v215 offset:53248\n" \
"ds_write_b128 v231, v[232:235] offset:4096\n" \
"v_mfma_f32_16x16x32_bf16 v[12:15], a[224:227], a[4:7], v[12:15]\n" \
"ds_read_b64_tr_b16 a[198:199], v215 offset:55296\n" \
"v_add_f32_e64 v222, v222, v223\n" \
"v_mfma_f32_16x16x32_bf16 v[44:47], a[216:219], a[16:19], v[44:47]\n" \
"ds_read_b64_tr_b16 a[200:201], v215 offset:57344\n" \
"v_perm_b32 v196, v197, v196, s54\n" \
"v_mfma_f32_16x16x32_bf16 v[44:47], a[224:227], a[20:23], v[44:47]\n" \
"ds_read_b64_tr_b16 a[202:203], v215 offset:59392\n" \
"v_add_f32_dpp v222, v222, v222 quad_perm:[1,0,3,2] row_mask:0xf bank_mask:0xf\n" \
"v_mfma_f32_16x16x32_bf16 v[76:79], a[216:219], a[32:35], v[76:79]\n" \
"ds_read_b64_tr_b16 a[204:205], v215 offset:61440\n" \
"v_perm_b32 v197, v199, v198, s54\n" \
"v_mfma_f32_16x16x32_bf16 v[76:79], a[224:227], a[36:39], v[76:79]\n" \
"ds_read_b64_tr_b16 a[206:207], v215 offset:63488\n" \
"v_add_f32_dpp v222, v222, v222 quad_perm:[2,3,0,1] row_mask:0xf bank_mask:0xf\n" \
"v_mfma_f32_16x16x32_bf16 v[108:111], a[216:219], a[48:51], v[108:111]\n" \
"ds_read_b64_tr_b16 a[220:221], v227 offset:49152\n" \
"ds_write_b64 v218, v[196:197] offset:8192\n" \
"v_mfma_f32_16x16x32_bf16 v[108:111], a[224:227], a[52:55], v[108:111]\n" \
"ds_read_b64_tr_b16 a[222:223], v227 offset:51200\n" \
"v_add_f32_dpp v222, v222, v222 row_ror:4 row_mask:0xf bank_mask:0xf\n" \
"v_mfma_f32_16x16x32_bf16 v[12:15], a[228:231], a[8:11], v[12:15]\n" \
"ds_read_b64_tr_b16 a[212:213], v227 offset:53248\n" \
"v_perm_b32 v200, v201, v200, s54\n" \
"v_mfma_f32_16x16x32_bf16 v[12:15], a[236:239], a[12:15], v[12:15]\n" \
"ds_read_b64_tr_b16 a[214:215], v227 offset:55296\n" \
"v_add_f32_dpp v222, v222, v222 row_ror:8 row_mask:0xf bank_mask:0xf\n" \
"v_mfma_f32_16x16x32_bf16 v[44:47], a[228:231], a[24:27], v[44:47]\n" \
"v_perm_b32 v201, v203, v202, s54\n" \
"v_mov_b32_e64 v223, v222\n" \
"v_mfma_f32_16x16x32_bf16 v[44:47], a[236:239], a[28:31], v[44:47]\n" \
"ds_write_b64 v218, v[200:201] offset:10240\n" \
"s_nop 0\n" \
"v_permlane16_swap_b32_e32 v222, v223\n" \
"v_mfma_f32_16x16x32_bf16 v[76:79], a[228:231], a[40:43], v[76:79]\n" \
"v_perm_b32 v204, v205, v204, s54\n" \
"v_mfma_f32_16x16x32_bf16 v[76:79], a[236:239], a[44:47], v[76:79]\n" \
"v_add_f32_e64 v222, v222, v223\n" \
"v_mfma_f32_16x16x32_bf16 v[108:111], a[228:231], a[56:59], v[108:111]\n" \
"ds_read_b64_tr_b16 a[208:209], v227 offset:57344\n" \
"v_perm_b32 v205, v207, v206, s54\n" \
"v_mfma_f32_16x16x32_bf16 v[108:111], a[236:239], a[60:63], v[108:111]\n" \
"s_waitcnt vmcnt(63) lgkmcnt(7)\n" \
"s_nop 0\n" \
"v_mfma_f32_16x16x32_bf16 v[4:7], a[232:235], a[128:131], v[4:7]\n" \
"ds_read_b64_tr_b16 a[210:211], v227 offset:59392\n" \
"v_mov_b32_e64 v223, v222\n" \
"v_mfma_f32_16x16x32_bf16 v[4:7], a[196:199], a[132:135], v[4:7]\n" \
"ds_read_b64_tr_b16 a[216:217], v227 offset:61440\n" \
"ds_write_b64 v218, v[204:205] offset:14336\n" \
"v_mfma_f32_16x16x32_bf16 v[36:39], a[232:235], a[144:147], v[36:39]\n" \
"ds_read_b64_tr_b16 a[218:219], v227 offset:63488\n" \
"s_nop 0\n" \
"v_permlane32_swap_b32_e32 v222, v223\n" \
"v_mfma_f32_16x16x32_bf16 v[36:39], a[196:199], a[148:151], v[36:39]\n" \
"v_lshl_add_u32 v219, %[wid], 4, v219\n" \
"v_add_f32_e64 v222, v222, v223\n" \
"v_mfma_f32_16x16x32_bf16 v[68:71], a[232:235], a[160:163], v[68:71]\n" \
"s_cmp_lg_u64 s[56:57], 0\n" \
"s_cselect_b32 s61, 1, 0\n" \
"v_mov_b32_e64 v192, s61\n" \
"v_mfma_f32_16x16x32_bf16 v[68:71], a[196:199], a[164:167], v[68:71]\n" \
"v_mov_b32_e64 v193, v192\n" \
"v_mfma_f32_16x16x32_bf16 v[100:103], a[232:235], a[176:179], v[100:103]\n" \
"ds_read_b64_tr_b16 a[224:225], v215 offset:16896\n" \
"ds_write_b32 v219, v222 offset:12424\n" \
"v_mfma_f32_16x16x32_bf16 v[100:103], a[196:199], a[180:183], v[100:103]\n" \
"ds_read_b64_tr_b16 a[226:227], v215 offset:18944\n" \
"ds_write_b64 v219, v[192:193] offset:12416\n" \
"v_mfma_f32_16x16x32_bf16 v[4:7], a[200:203], a[136:139], v[4:7]\n" \
"ds_read_b64_tr_b16 a[228:229], v215 offset:20992\n" \
"v_mfma_f32_16x16x32_bf16 v[4:7], a[204:207], a[140:143], v[4:7]\n" \
"ds_read_b64_tr_b16 a[230:231], v215 offset:23040\n" \
"v_mfma_f32_16x16x32_bf16 v[36:39], a[200:203], a[152:155], v[36:39]\n" \
"v_mfma_f32_16x16x32_bf16 v[36:39], a[204:207], a[156:159], v[36:39]\n" \
"v_mfma_f32_16x16x32_bf16 v[68:71], a[200:203], a[168:171], v[68:71]\n" \
"v_mfma_f32_16x16x32_bf16 v[68:71], a[204:207], a[172:175], v[68:71]\n" \
"v_mfma_f32_16x16x32_bf16 v[100:103], a[200:203], a[184:187], v[100:103]\n" \
"ds_read_b64_tr_b16 a[236:237], v215 offset:25088\n" \
"v_mfma_f32_16x16x32_bf16 v[100:103], a[204:207], a[188:191], v[100:103]\n" \
"s_waitcnt vmcnt(63) lgkmcnt(3)\n" \
"s_barrier\n" \
"v_mfma_f32_16x16x32_bf16 v[12:15], a[220:223], a[128:131], v[12:15]\n" \
"ds_read_b64_tr_b16 a[238:239], v215 offset:27136\n" \
"ds_read_b128 v[204:207], v210 offset:12416\n" \
"v_mfma_f32_16x16x32_bf16 v[12:15], a[212:215], a[132:135], v[12:15]\n" \
"ds_read_b64_tr_b16 a[232:233], v215 offset:29184\n" \
"v_lshrrev_b32_e64 v200, 3, v224\n" \
"v_mfma_f32_16x16x32_bf16 v[44:47], a[220:223], a[144:147], v[44:47]\n" \
"ds_read_b64_tr_b16 a[234:235], v215 offset:31232\n" \
"ds_read_b128 v[160:163], v208 offset:0\n" \
"v_mfma_f32_16x16x32_bf16 v[44:47], a[212:215], a[148:151], v[44:47]\n" \
"v_bfe_u32 v201, v224, 8, 3\n" \
"v_and_b32_e64 v200, 16, v200\n" \
"v_mfma_f32_16x16x32_bf16 v[76:79], a[220:223], a[160:163], v[76:79]\n" \
"ds_read_b128 v[164:167], v208 offset:2048\n" \
".if . - adaln_body != 57344\n" \
".error \"size model\"\n" \
".endif\n" \
"Lk13_%=:\n" \
"s_cbranch_execz adaln_pg0\n" \
"s_nop 0\n" \
"v_lshlrev_b32_e64 v201, v201, 1\n" \
"v_mfma_f32_16x16x32_bf16 v[76:79], a[212:215], a[164:167], v[76:79]\n" \
"s_movk_i32 s61, 0x3f80\n" \
"s_nop 0\n" \
"v_lshlrev_b32_e64 v200, v200, s61\n" \
"v_readlane_b32 s68, %[thi], 18\n" \
"v_and_b32_e64 v216, 64, v226\n" \
"v_mfma_f32_16x16x32_bf16 v[108:111], a[220:223], a[176:179], v[108:111]\n" \
"ds_read_b64_tr_b16 a[196:197], v227 offset:16896\n" \
"ds_read_b128 v[168:171], v208 offset:128\n" \
"v_mfma_f32_16x16x32_bf16 v[108:111], a[212:215], a[180:183], v[108:111]\n" \
"ds_read_b64_tr_b16 a[198:199], v227 offset:18944\n" \
"v_bfe_i32 v192, v201, 0, 1\n" \
"v_add_u32_e64 v226, v216, v226\n" \
"v_mfma_f32_16x16x32_bf16 v[12:15], a[208:211], a[136:139], v[12:15]\n" \
"ds_read_b64_tr_b16 a[200:201], v227 offset:20992\n" \
"ds_read_b128 v[172:175], v208 offset:2176\n" \
"v_mfma_f32_16x16x32_bf16 v[12:15], a[216:219], a[140:143], v[12:15]\n" \
"ds_read_b64_tr_b16 a[202:203], v227 offset:23040\n" \
"v_and_b32_e32 v192, v200, v192\n" \
"v_lshlrev_b32_e32 v226, 7, v226\n" \
"v_mfma_f32_16x16x32_bf16 v[44:47], a[208:211], a[152:155], v[44:47]\n" \
"ds_read_b128 v[176:179], v209 offset:0\n" \
"v_bfe_i32 v193, v201, 1, 1\n" \
"v_mfma_f32_16x16x32_bf16 v[44:47], a[216:219], a[156:159], v[44:47]\n" \
"v_and_b32_e64 v193, v200, v193\n" \
"v_bfe_i32 v194, v201, 2, 1\n" \
"v_mfma_f32_16x16x32_bf16 v[76:79], a[208:211], a[168:171], v[76:79]\n" \
"ds_read_b128 v[180:183], v209 offset:2048\n" \
"v_and_b32_e64 v194, v200, v194\n" \
"v_mfma_f32_16x16x32_bf16 v[76:79], a[216:219], a[172:175], v[76:79]\n" \
"v_bfe_i32 v195, v201, 3, 1\n" \
"v_and_b32_e64 v195, v200, v195\n" \
"buffer_load_dword v216, v226, %[srdz], s68 offen offset:68 sc1\n" \
"v_mfma_f32_16x16x32_bf16 v[108:111], a[208:211], a[184:187], v[108:111]\n" \
"ds_read_b64_tr_b16 a[204:205], v227 offset:25088\n" \
"ds_read_b128 v[184:187], v209 offset:128\n" \
"v_lshrrev_b32_e64 v217, 3, v224\n" \
"s_add_u32 s51, %[sin], 4096\n" \
"v_mfma_f32_16x16x32_bf16 v[108:111], a[216:219], a[188:191], v[108:111]\n" \
"s_waitcnt vmcnt(63) lgkmcnt(12)\n" \
"s_nop 0\n" \
"v_mfma_f32_16x16x32_bf16 v[20:23], a[224:227], v[0:3], v[20:23]\n" \
"ds_read_b64_tr_b16 a[206:207], v227 offset:27136\n" \
"ds_read_b128 v[188:191], v209 offset:2176\n" \
"v_mfma_f32_16x16x32_bf16 v[20:23], a[228:231], v[8:11], v[20:23]\n" \
"ds_read_b64_tr_b16 a[220:221], v227 offset:29184\n" \
"v_bfe_i32 v196, v201, 4, 1\n" \
"v_mfma_f32_16x16x32_bf16 v[52:55], a[224:227], v[32:35], v[52:55]\n" \
"ds_read_b64_tr_b16 a[222:223], v227 offset:31232\n" \
"v_and_b32_e64 v196, v200, v196\n" \
"v_mfma_f32_16x16x32_bf16 v[52:55], a[228:231], v[40:43], v[52:55]\n" \
"v_bfe_i32 v197, v201, 5, 1\n" \
"v_and_b32_e64 v197, v200, v197\n" \
"v_mfma_f32_16x16x32_bf16 v[84:87], a[224:227], v[64:67], v[84:87]\n" \
"v_cvt_pk_bf16_f32 v4, v4, v5\n" \
"v_bfe_i32 v198, v201, 6, 1\n" \
"v_mfma_f32_16x16x32_bf16 v[84:87], a[228:231], v[72:75], v[84:87]\n" \
"v_cvt_pk_bf16_f32 v5, v6, v7\n" \
"v_and_b32_e64 v198, v200, v198\n" \
"v_mfma_f32_16x16x32_bf16 v[116:119], a[224:227], v[96:99], v[116:119]\n" \
"ds_read_b64_tr_b16 a[212:213], v215 offset:33280\n" \
"v_cvt_pk_bf16_f32 v6, v12, v13\n" \
"v_mfma_f32_16x16x32_bf16 v[116:119], a[228:231], v[104:107], v[116:119]\n" \
"ds_read_b64_tr_b16 a[214:215], v215 offset:35328\n" \
"v_cvt_pk_bf16_f32 v7, v14, v15\n" \
"v_mfma_f32_16x16x32_bf16 v[20:23], a[236:239], v[16:19], v[20:23]\n" \
"ds_read_b64_tr_b16 a[208:209], v215 offset:37376\n" \
"v_bfe_i32 v199, v201, 7, 1\n" \
"v_mfma_f32_16x16x32_bf16 v[20:23], a[232:235], v[24:27], v[20:23]\n" \
"ds_read_b64_tr_b16 a[210:211], v215 offset:39424\n" \
"v_and_b32_e64 v199, v200, v199\n" \
"v_mfma_f32_16x16x32_bf16 v[52:55], a[236:239], v[48:51], v[52:55]\n" \
"s_add_u32 s55, s46, 16384\n" \
"buffer_store_dwordx4 v[4:7], %[vxs], %[srdz], s55 offen offset:0 sc1\n" \
"v_mfma_f32_16x16x32_bf16 v[52:55], a[232:235], v[56:59], v[52:55]\n" \
"v_cvt_pk_bf16_f32 v36, v36, v37\n" \
"v_cvt_pk_bf16_f32 v37, v38, v39\n" \
"v_mfma_f32_16x16x32_bf16 v[84:87], a[236:239], v[80:83], v[84:87]\n" \
"v_cvt_pk_bf16_f32 v38, v44, v45\n" \
"s_waitcnt lgkmcnt(14)\n" \
"s_nop 0\n" \
"v_add_f32_dpp v206, v206, v206 quad_perm:[1,0,3,2] row_mask:0xf bank_mask:0xf\n" \
"v_mfma_f32_16x16x32_bf16 v[84:87], a[232:235], v[88:91], v[84:87]\n" \
"v_cvt_pk_bf16_f32 v39, v46, v47\n" \
"v_mfma_f32_16x16x32_bf16 v[116:119], a[236:239], v[112:115], v[116:119]\n" \
"ds_read_b64_tr_b16 a[216:217], v215 offset:41472\n" \
"v_add_f32_dpp v206, v206, v206 quad_perm:[2,3,0,1] row_mask:0xf bank_mask:0xf\n" \
"v_mfma_f32_16x16x32_bf16 v[116:119], a[232:235], v[120:123], v[116:119]\n" \
"s_waitcnt vmcnt(63) lgkmcnt(5)\n" \
"s_nop 0\n" \
"v_mfma_f32_16x16x32_bf16 v[28:31], a[196:199], v[0:3], v[28:31]\n" \
"ds_read_b64_tr_b16 a[218:219], v215 offset:43520\n" \
"v_cvt_pk_bf16_f32 v68, v68, v69\n" \
"v_mfma_f32_16x16x32_bf16 v[28:31], a[200:203], v[8:11], v[28:31]\n" \
"ds_read_b64_tr_b16 a[224:225], v215 offset:45568\n" \
"s_add_u32 s55, s46, 540672\n" \
"buffer_store_dwordx4 v[36:39], %[vxs], %[srdz], s55 offen offset:0 sc1\n" \
"v_mfma_f32_16x16x32_bf16 v[60:63], a[196:199], v[32:35], v[60:63]\n" \
"ds_read_b64_tr_b16 a[226:227], v215 offset:47616\n" \
"v_cvt_pk_bf16_f32 v69, v70, v71\n" \
"v_mfma_f32_16x16x32_bf16 v[60:63], a[200:203], v[40:43], v[60:63]\n" \
"v_cvt_pk_bf16_f32 v70, v76, v77\n" \
"v_cvt_pk_bf16_f32 v71, v78, v79\n" \
"v_mfma_f32_16x16x32_bf16 v[92:95], a[196:199], v[64:67], v[92:95]\n" \
"s_add_u32 s55, s46, 1064960\n" \
"buffer_store_dwordx4 v[68:71], %[vxs], %[srdz], s55 offen offset:0 sc1\n" \
"v_mfma_f32_16x16x32_bf16 v[92:95], a[200:203], v[72:75], v[92:95]\n" \
"v_mfma_f32_16x16x32_bf16 v[124:127], a[196:199], v[96:99], v[124:127]\n" \
"ds_read_b64_tr_b16 a[228:229], v227 offset:33280\n" \
"v_cvt_pk_bf16_f32 v100, v100, v101\n" \
"v_mfma_f32_16x16x32_bf16 v[124:127], a[200:203], v[104:107], v[124:127]\n" \
"ds_read_b64_tr_b16 a[230:231], v227 offset:35328\n" \
"v_cvt_pk_bf16_f32 v101, v102, v103\n" \
"v_mfma_f32_16x16x32_bf16 v[28:31], a[204:207], v[16:19], v[28:31]\n" \
"ds_read_b64_tr_b16 a[236:237], v227 offset:37376\n" \
"v_cvt_pk_bf16_f32 v102, v108, v109\n" \
"v_mfma_f32_16x16x32_bf16 v[28:31], a[220:223], v[24:27], v[28:31]\n" \
"ds_read_b64_tr_b16 a[238:239], v227 offset:39424\n" \
"v_cvt_pk_bf16_f32 v103, v110, v111\n" \
"v_mfma_f32_16x16x32_bf16 v[60:63], a[204:207], v[48:51], v[60:63]\n" \
"s_add_u32 s55, s46, 1589248\n" \
"buffer_store_dwordx4 v[100:103], %[vxs], %[srdz], s55 offen offset:0 sc1\n" \
"v_mfma_f32_16x16x32_bf16 v[60:63], a[220:223], v[56:59], v[60:63]\n" \
"v_mfma_f32_16x16x32_bf16 v[92:95], a[204:207], v[80:83], v[92:95]\n" \
"v_mov_b32_e64 v230, 1\n" \
"buffer_store_dword v230, v225, %[srdz], s62 offen offset:64 sc1\n" \
"v_mfma_f32_16x16x32_bf16 v[92:95], a[220:223], v[88:91], v[92:95]\n" \
"ds_read_b128 v[4:7], v208 offset:4096\n" \
"v_mfma_f32_16x16x32_bf16 v[124:127], a[204:207], v[112:115], v[124:127]\n" \
"ds_read_b64_tr_b16 a[232:233], v227 offset:41472\n" \
"v_mfma_f32_16x16x32_bf16 v[124:127], a[220:223], v[120:123], v[124:127]\n" \
"s_waitcnt vmcnt(63) lgkmcnt(6)\n" \
"s_nop 0\n" \
"v_mfma_f32_16x16x32_bf16 v[20:23], a[212:215], a[0:3], v[20:23]\n" \
"ds_read_b64_tr_b16 a[234:235], v227 offset:43520\n" \
"ds_read_b128 v[12:15], v208 offset:6144\n" \
"v_mfma_f32_16x16x32_bf16 v[20:23], a[208:211], a[4:7], v[20:23]\n" \
"ds_read_b64_tr_b16 a[196:197], v227 offset:45568\n" \
"v_mfma_f32_16x16x32_bf16 v[52:55], a[212:215], a[16:19], v[52:55]\n" \
"ds_read_b64_tr_b16 a[198:199], v227 offset:47616\n" \
"v_mfma_f32_16x16x32_bf16 v[52:55], a[208:211], a[20:23], v[52:55]\n" \
"ds_read_b128 v[36:39], v208 offset:4224\n" \
"v_mfma_f32_16x16x32_bf16 v[84:87], a[212:215], a[32:35], v[84:87]\n" \
"v_mfma_f32_16x16x32_bf16 v[84:87], a[208:211], a[36:39], v[84:87]\n" \
"v_mfma_f32_16x16x32_bf16 v[116:119], a[212:215], a[48:51], v[116:119]\n" \
"ds_read_b64_tr_b16 a[200:201], v215 offset:49664\n" \
"ds_read_b128 v[44:47], v208 offset:6272\n" \
"v_mfma_f32_16x16x32_bf16 v[116:119], a[208:211], a[52:55], v[116:119]\n" \
"ds_read_b64_tr_b16 a[202:203], v215 offset:51712\n" \
"v_mfma_f32_16x16x32_bf16 v[20:23], a[216:219], a[8:11], v[20:23]\n" \
"ds_read_b64_tr_b16 a[204:205], v215 offset:53760\n" \
"v_mfma_f32_16x16x32_bf16 v[20:23], a[224:227], a[12:15], v[20:23]\n" \
"ds_read_b64_tr_b16 a[206:207], v215 offset:55808\n" \
"ds_read_b128 v[68:71], v209 offset:4096\n" \
"v_mfma_f32_16x16x32_bf16 v[52:55], a[216:219], a[24:27], v[52:55]\n" \
"ds_read_b64_tr_b16 a[220:221], v215 offset:57856\n" \
"v_mfma_f32_16x16x32_bf16 v[52:55], a[224:227], a[28:31], v[52:55]\n" \
"ds_read_b64_tr_b16 a[222:223], v215 offset:59904\n" \
"v_mfma_f32_16x16x32_bf16 v[84:87], a[216:219], a[40:43], v[84:87]\n" \
"ds_read_b64_tr_b16 a[212:213], v215 offset:61952\n" \
"ds_read_b128 v[76:79], v209 offset:6144\n" \
"v_mfma_f32_16x16x32_bf16 v[84:87], a[224:227], a[44:47], v[84:87]\n" \
"ds_read_b64_tr_b16 a[214:215], v215 offset:64000\n" \
"v_mfma_f32_16x16x32_bf16 v[116:119], a[216:219], a[56:59], v[116:119]\n" \
"ds_read_b64_tr_b16 a[208:209], v227 offset:49664\n" \
"v_mfma_f32_16x16x32_bf16 v[116:119], a[224:227], a[60:63], v[116:119]\n" \
"s_waitcnt vmcnt(63) lgkmcnt(13)\n" \
"s_nop 0\n" \
"v_mfma_f32_16x16x32_bf16 v[28:31], a[228:231], a[0:3], v[28:31]\n" \
"ds_read_b64_tr_b16 a[210:211], v227 offset:51712\n" \
"ds_read_b128 v[100:103], v209 offset:4224\n" \
"v_mfma_f32_16x16x32_bf16 v[28:31], a[236:239], a[4:7], v[28:31]\n" \
"ds_read_b64_tr_b16 a[216:217], v227 offset:53760\n" \
"v_mfma_f32_16x16x32_bf16 v[60:63], a[228:231], a[16:19], v[60:63]\n" \
"ds_read_b64_tr_b16 a[218:219], v227 offset:55808\n" \
"ds_read_b128 v[108:111], v209 offset:6272\n" \
"v_mfma_f32_16x16x32_bf16 v[60:63], a[236:239], a[20:23], v[60:63]\n" \
"ds_read_b64_tr_b16 a[224:225], v227 offset:57856\n" \
"v_mfma_f32_16x16x32_bf16 v[92:95], a[228:231], a[32:35], v[92:95]\n" \
"ds_read_b64_tr_b16 a[226:227], v227 offset:59904\n" \
"v_mfma_f32_16x16x32_bf16 v[92:95], a[236:239], a[36:39], v[92:95]\n" \
"v_mfma_f32_16x16x32_bf16 v[124:127], a[228:231], a[48:51], v[124:127]\n" \
"v_mfma_f32_16x16x32_bf16 v[124:127], a[236:239], a[52:55], v[124:127]\n" \
"ds_read_b64_tr_b16 a[228:229], v227 offset:61952\n" \
"v_mfma_f32_16x16x32_bf16 v[28:31], a[232:235], a[8:11], v[28:31]\n" \
"ds_read_b64_tr_b16 a[230:231], v227 offset:64000\n" \
"s_waitcnt vmcnt(5)\n" \
"v_cmp_eq_u32_e32 vcc, 0, v216\n" \
"s_nop 0\n" \
"s_cmp_eq_u64 vcc, 0\n" \
"s_cselect_b32 %[got], 1, 0\n" \
"s_nop 0\n" \
"s_cselect_b32 s54, %[sin], 0x80000000\n" \
"s_cselect_b32 s51, s51, 0x80000000\n" \
"v_mfma_f32_16x16x32_bf16 v[28:31], a[196:199], a[12:15], v[28:31]\n" \
"v_mfma_f32_16x16x32_bf16 v[60:63], a[232:235], a[24:27], v[60:63]\n" \
"buffer_load_dwordx4 a[64:67], v217, %[srdz], s54 offen offset:0 sc1\n" \
"v_mfma_f32_16x16x32_bf16 v[60:63], a[196:199], a[28:31], v[60:63]\n" \
"v_mfma_f32_16x16x32_bf16 v[92:95], a[232:235], a[40:43], v[92:95]\n" \
"buffer_load_dwordx4 a[68:71], v217, %[srdz], s54 offen offset:1024 sc1\n" \
"v_mfma_f32_16x16x32_bf16 v[92:95], a[196:199], a[44:47], v[92:95]\n" \
"v_mfma_f32_16x16x32_bf16 v[124:127], a[232:235], a[56:59], v[124:127]\n" \
"buffer_load_dwordx4 a[72:75], v217, %[srdz], s54 offen offset:2048 sc1\n" \
"v_mfma_f32_16x16x32_bf16 v[124:127], a[196:199], a[60:63], v[124:127]\n" \
"s_waitcnt vmcnt(63) lgkmcnt(0)\n" \
"s_nop 0\n" \
"v_mfma_f32_16x16x32_bf16 v[20:23], a[200:203], a[128:131], v[20:23]\n" \
"buffer_load_dwordx4 a[76:79], v217, %[srdz], s54 offen offset:3072 sc1\n" \
"v_mfma_f32_16x16x32_bf16 v[20:23], a[204:207], a[132:135], v[20:23]\n" \
"v_mfma_f32_16x16x32_bf16 v[20:23], a[220:223], a[136:139], v[20:23]\n" \
"buffer_load_dwordx4 a[80:83], v217, %[srdz], s51 offen offset:0 sc1\n" \
"v_mfma_f32_16x16x32_bf16 v[20:23], a[212:215], a[140:143], v[20:23]\n" \
"v_mfma_f32_16x16x32_bf16 v[28:31], a[208:211], a[128:131], v[28:31]\n" \
"buffer_load_dwordx4 a[84:87], v217, %[srdz], s51 offen offset:1024 sc1\n" \
"v_mfma_f32_16x16x32_bf16 v[28:31], a[216:219], a[132:135], v[28:31]\n" \
"v_mfma_f32_16x16x32_bf16 v[28:31], a[224:227], a[136:139], v[28:31]\n" \
"buffer_load_dwordx4 a[88:91], v217, %[srdz], s51 offen offset:2048 sc1\n" \
"v_mfma_f32_16x16x32_bf16 v[28:31], a[228:231], a[140:143], v[28:31]\n" \
"v_mfma_f32_16x16x32_bf16 v[52:55], a[200:203], a[144:147], v[52:55]\n" \
"buffer_load_dwordx4 a[92:95], v217, %[srdz], s51 offen offset:3072 sc1\n" \
"v_mfma_f32_16x16x32_bf16 v[52:55], a[204:207], a[148:151], v[52:55]\n" \
"v_mfma_f32_16x16x32_bf16 v[52:55], a[220:223], a[152:155], v[52:55]\n" \
"v_mfma_f32_16x16x32_bf16 v[52:55], a[212:215], a[156:159], v[52:55]\n" \
"v_cvt_pk_bf16_f32 v20, v20, v21\n" \
"v_cvt_pk_bf16_f32 v21, v22, v23\n" \
"v_mfma_f32_16x16x32_bf16 v[60:63], a[208:211], a[144:147], v[60:63]\n" \
"buffer_load_dwordx2 v[228:229], v226, %[srdz], s68 offen offset:64 sc1\n" \
"v_mfma_f32_16x16x32_bf16 v[60:63], a[216:219], a[148:151], v[60:63]\n" \
"v_cvt_pk_bf16_f32 v22, v28, v29\n" \
"v_cvt_pk_bf16_f32 v23, v30, v31\n" \
"v_mfma_f32_16x16x32_bf16 v[60:63], a[224:227], a[152:155], v[60:63]\n" \
"s_add_u32 s55, s46, 20480\n" \
"buffer_store_dwordx4 v[20:23], %[vxs], %[srdz], s55 offen offset:0 sc1\n" \
"v_mfma_f32_16x16x32_bf16 v[60:63], a[228:231], a[156:159], v[60:63]\n" \
"v_mfma_f32_16x16x32_bf16 v[84:87], a[200:203], a[160:163], v[84:87]\n" \
"v_mfma_f32_16x16x32_bf16 v[84:87], a[204:207], a[164:167], v[84:87]\n" \
"v_mfma_f32_16x16x32_bf16 v[84:87], a[220:223], a[168:171], v[84:87]\n" \
"v_mfma_f32_16x16x32_bf16 v[84:87], a[212:215], a[172:175], v[84:87]\n" \
"v_cvt_pk_bf16_f32 v52, v52, v53\n" \
"v_cvt_pk_bf16_f32 v53, v54, v55\n" \
"v_mfma_f32_16x16x32_bf16 v[92:95], a[208:211], a[160:163], v[92:95]\n" \
"v_mfma_f32_16x16x32_bf16 v[92:95], a[216:219], a[164:167], v[92:95]\n" \
"v_cvt_pk_bf16_f32 v54, v60, v61\n" \
"v_cvt_pk_bf16_f32 v55, v62, v63\n" \
"v_mfma_f32_16x16x32_bf16 v[92:95], a[224:227], a[168:171], v[92:95]\n" \
"s_add_u32 s55, s46, 544768\n" \
"buffer_store_dwordx4 v[52:55], %[vxs], %[srdz], s55 offen offset:0 sc1\n" \
"v_mfma_f32_16x16x32_bf16 v[92:95], a[228:231], a[172:175], v[92:95]\n" \
"v_mfma_f32_16x16x32_bf16 v[116:119], a[200:203], a[176:179], v[116:119]\n" \
"v_mfma_f32_16x16x32_bf16 v[116:119], a[204:207], a[180:183], v[116:119]\n" \
"v_mfma_f32_16x16x32_bf16 v[116:119], a[220:223], a[184:187], v[116:119]\n" \
"v_mfma_f32_16x16x32_bf16 v[116:119], a[212:215], a[188:191], v[116:119]\n" \
"v_cvt_pk_bf16_f32 v84, v84, v85\n" \
"v_cvt_pk_bf16_f32 v85, v86, v87\n" \
"v_mfma_f32_16x16x32_bf16 v[124:127], a[208:211], a[176:179], v[124:127]\n" \
"v_mfma_f32_16x16x32_bf16 v[124:127], a[216:219], a[180:183], v[124:127]\n" \
"v_cvt_pk_bf16_f32 v86, v92, v93\n" \
"v_cvt_pk_bf16_f32 v87, v94, v95\n" \
"v_mfma_f32_16x16x32_bf16 v[124:127], a[224:227], a[184:187], v[124:127]\n" \
"s_add_u32 s55, s46, 1069056\n" \
"buffer_store_dwordx4 v[84:87], %[vxs], %[srdz], s55 offen offset:0 sc1\n" \
"v_mfma_f32_16x16x32_bf16 v[124:127], a[228:231], a[188:191], v[124:127]\n" \
"v_cvt_pk_bf16_f32 v116, v116, v117\n" \
"v_cvt_pk_bf16_f32 v117, v118, v119\n" \
"s_waitcnt lgkmcnt(0)\n" \
"s_nop 4\n" \
"v_cvt_pk_bf16_f32 v118, v124, v125\n" \
"v_cvt_pk_bf16_f32 v119, v126, v127\n" \
"s_add_u32 s55, s46, 1593344\n" \
"buffer_store_dwordx4 v[116:119], %[vxs], %[srdz], s55 offen offset:0 sc1\n" \
"s_branch Lend_%=\n" \
"s_nop 0\n" \
"Lsp1_%=:\n" \
"s_movk_i32 s51, 0x1000\n" \
"s_cmp_gt_u32 s48, 0\n" \
"s_cselect_b32 s61, 0, 1\n" \
"s_nop 0\n" \
"s_mul_i32 s61, s61, 262144\n" \
"Lsq1_%=:\n" \
"buffer_load_dwordx4 a[64:67], %[oq0], %[srdz], s61 offen offset:0 sc1\n" \
"buffer_load_dwordx4 a[68:71], %[oq0], %[srdz], s61 offen offset:1024 sc1\n" \
"buffer_load_dwordx4 a[72:75], %[oq0], %[srdz], s61 offen offset:2048 sc1\n" \
"buffer_load_dwordx4 a[76:79], %[oq0], %[srdz], s61 offen offset:3072 sc1\n" \
"buffer_load_dwordx4 a[80:83], v208, %[srdz], s61 offen offset:0 sc1\n" \
"buffer_load_dwordx4 a[84:87], v208, %[srdz], s61 offen offset:1024 sc1\n" \
"buffer_load_dwordx4 a[88:91], v208, %[srdz], s61 offen offset:2048 sc1\n" \
"buffer_load_dwordx4 a[92:95], v208, %[srdz], s61 offen offset:3072 sc1\n" \
"buffer_load_dwordx4 a[96:99], v209, %[srdz], s61 offen offset:0 sc1\n" \
"buffer_load_dwordx4 a[100:103], v209, %[srdz], s61 offen offset:1024 sc1\n" \
"buffer_load_dwordx4 a[104:107], v209, %[srdz], s61 offen offset:2048 sc1\n" \
"buffer_load_dwordx4 a[108:111], v209, %[srdz], s61 offen offset:3072 sc1\n" \
"buffer_load_dwordx4 a[112:115], v210, %[srdz], s61 offen offset:0 sc1\n" \
"buffer_load_dwordx4 a[116:119], v210, %[srdz], s61 offen offset:1024 sc1\n" \
"buffer_load_dwordx4 a[120:123], v210, %[srdz], s61 offen offset:2048 sc1\n" \
"buffer_load_dwordx4 a[124:127], v210, %[srdz], s61 offen offset:3072 sc1\n" \
"s_waitcnt vmcnt(0)\n" \
"s_nop 0\n" \
"v_mfma_f32_16x16x32_bf16 v[128:131], a[236:239], a[64:67], v[192:195]\n" \
"v_mfma_f32_16x16x32_bf16 v[128:131], a[196:199], a[68:71], v[128:131]\n" \
"v_mfma_f32_16x16x32_bf16 v[128:131], a[200:203], a[72:75], v[128:131]\n" \
"v_mfma_f32_16x16x32_bf16 v[128:131], a[204:207], a[76:79], v[128:131]\n" \
"v_mfma_f32_16x16x32_bf16 v[132:135], a[236:239], a[80:83], v[196:199]\n" \
"v_mfma_f32_16x16x32_bf16 v[132:135], a[196:199], a[84:87], v[132:135]\n" \
"v_mfma_f32_16x16x32_bf16 v[132:135], a[200:203], a[88:91], v[132:135]\n" \
"v_mfma_f32_16x16x32_bf16 v[132:135], a[204:207], a[92:95], v[132:135]\n" \
"v_mfma_f32_16x16x32_bf16 v[136:139], a[236:239], a[96:99], v[200:203]\n" \
"v_mfma_f32_16x16x32_bf16 v[136:139], a[196:199], a[100:103], v[136:139]\n" \
"v_mfma_f32_16x16x32_bf16 v[136:139], a[200:203], a[104:107], v[136:139]\n" \
"v_mfma_f32_16x16x32_bf16 v[136:139], a[204:207], a[108:111], v[136:139]\n" \
"v_mfma_f32_16x16x32_bf16 v[140:143], a[236:239], a[112:115], v[204:207]\n" \
"v_mfma_f32_16x16x32_bf16 v[140:143], a[196:199], a[116:119], v[140:143]\n" \
"v_mfma_f32_16x16x32_bf16 v[140:143], a[200:203], a[120:123], v[140:143]\n" \
"v_mfma_f32_16x16x32_bf16 v[140:143], a[204:207], a[124:127], v[140:143]\n" \
"s_nop 7\n" \
"s_nop 0\n" \
"v_cmp_u_f32_e64 s[44:45], v128, v132\n" \
"v_cmp_u_f32_e64 s[56:57], v136, v140\n" \
"s_or_b64 s[56:57], s[56:57], s[44:45]\n" \
"s_cbranch_scc0 Lbk1_%=\n" \
"s_sub_u32 s51, s51, 1\n" \
"s_cbranch_scc0 Lsq1_%=\n" \
"s_branch Lbk1_%=\n" \
"Lsp2_%=:\n" \
"s_movk_i32 s51, 0x1000\n" \
"s_cmp_gt_u32 s48, 1\n" \
"s_cselect_b32 s61, 1, 2\n" \
"s_mul_i32 s61, s61, 262144\n" \
"Lsq2_%=:\n" \
"buffer_load_dwordx4 a[0:3], %[oq0], %[srdz], s61 offen offset:0 sc1\n" \
"buffer_load_dwordx4 a[4:7], %[oq0], %[srdz], s61 offen offset:1024 sc1\n" \
"buffer_load_dwordx4 a[8:11], %[oq0], %[srdz], s61 offen offset:2048 sc1\n" \
"buffer_load_dwordx4 a[12:15], %[oq0], %[srdz], s61 offen offset:3072 sc1\n" \
"buffer_load_dwordx4 a[16:19], v208, %[srdz], s61 offen offset:0 sc1\n" \
"buffer_load_dwordx4 a[20:23], v208, %[srdz], s61 offen offset:1024 sc1\n" \
"buffer_load_dwordx4 a[24:27], v208, %[srdz], s61 offen offset:2048 sc1\n" \
"buffer_load_dwordx4 a[28:31], v208, %[srdz], s61 offen offset:3072 sc1\n" \
"buffer_load_dwordx4 a[32:35], v209, %[srdz], s61 offen offset:0 sc1\n" \
"buffer_load_dwordx4 a[36:39], v209, %[srdz], s61 offen offset:1024 sc1\n" \
"buffer_load_dwordx4 a[40:43], v209, %[srdz], s61 offen offset:2048 sc1\n" \
"buffer_load_dwordx4 a[44:47], v209, %[srdz], s61 offen offset:3072 sc1\n" \
"buffer_load_dwordx4 a[48:51], v210, %[srdz], s61 offen offset:0 sc1\n" \
"buffer_load_dwordx4 a[52:55], v210, %[srdz], s61 offen offset:1024 sc1\n" \
"buffer_load_dwordx4 a[56:59], v210, %[srdz], s61 offen offset:2048 sc1\n" \
"buffer_load_dwordx4 a[60:63], v210, %[srdz], s61 offen offset:3072 sc1\n" \
"s_waitcnt vmcnt(0)\n" \
"s_nop 0\n" \
"v_mfma_f32_16x16x32_bf16 v[192:195], a[196:199], a[0:3], v[128:131]\n" \
"v_mfma_f32_16x16x32_bf16 v[192:195], a[200:203], a[4:7], v[192:195]\n" \
"v_mfma_f32_16x16x32_bf16 v[192:195], a[204:207], a[8:11], v[192:195]\n" \
"v_mfma_f32_16x16x32_bf16 v[192:195], a[232:235], a[12:15], v[192:195]\n" \
"v_mfma_f32_16x16x32_bf16 v[196:199], a[196:199], a[16:19], v[132:135]\n" \
"v_mfma_f32_16x16x32_bf16 v[196:199], a[200:203], a[20:23], v[196:199]\n" \
"v_mfma_f32_16x16x32_bf16 v[196:199], a[204:207], a[24:27], v[196:199]\n" \
"v_mfma_f32_16x16x32_bf16 v[196:199], a[232:235], a[28:31], v[196:199]\n" \
"v_mfma_f32_16x16x32_bf16 v[200:203], a[196:199], a[32:35], v[136:139]\n" \
"v_mfma_f32_16x16x32_bf16 v[200:203], a[200:203], a[36:39], v[200:203]\n" \
"v_mfma_f32_16x16x32_bf16 v[200:203], a[204:207], a[40:43], v[200:203]\n" \
"v_mfma_f32_16x16x32_bf16 v[200:203], a[232:235], a[44:47], v[200:203]\n" \
"v_mfma_f32_16x16x32_bf16 v[204:207], a[196:199], a[48:51], v[140:143]\n" \
"v_mfma_f32_16x16x32_bf16 v[204:207], a[200:203], a[52:55], v[204:207]\n" \
"v_mfma_f32_16x16x32_bf16 v[204:207], a[204:207], a[56:59], v[204:207]\n" \
"v_mfma_f32_16x16x32_bf16 v[204:207], a[232:235], a[60:63], v[204:207]\n" \
"s_nop 7\n" \
"s_nop 0\n" \
"v_cmp_u_f32_e64 s[44:45], v192, v196\n" \
"v_cmp_u_f32_e64 s[56:57], v200, v204\n" \
"s_or_b64 s[56:57], s[56:57], s[44:45]\n" \
"s_cbranch_scc0 Lbk2_%=\n" \
"s_sub_u32 s51, s51, 1\n" \
"s_cbranch_scc0 Lsq2_%=\n" \
"s_branch Lbk2_%=\n" \
"Lsp3_%=:\n" \
"s_movk_i32 s51, 0x1000\n" \
"s_cmp_gt_u32 s48, 2\n" \
"s_cselect_b32 s61, 2, 3\n" \
"s_mul_i32 s61, s61, 262144\n" \
"Lsq3_%=:\n" \
"buffer_load_dwordx4 a[128:131], %[oq0], %[srdz], s61 offen offset:0 sc1\n" \
"buffer_load_dwordx4 a[132:135], %[oq0], %[srdz], s61 offen offset:1024 sc1\n" \
"buffer_load_dwordx4 a[136:139], %[oq0], %[srdz], s61 offen offset:2048 sc1\n" \
"buffer_load_dwordx4 a[140:143], %[oq0], %[srdz], s61 offen offset:3072 sc1\n" \
"buffer_load_dwordx4 a[144:147], v208, %[srdz], s61 offen offset:0 sc1\n" \
"buffer_load_dwordx4 a[148:151], v208, %[srdz], s61 offen offset:1024 sc1\n" \
"buffer_load_dwordx4 a[152:155], v208, %[srdz], s61 offen offset:2048 sc1\n" \
"buffer_load_dwordx4 a[156:159], v208, %[srdz], s61 offen offset:3072 sc1\n" \
"buffer_load_dwordx4 a[160:163], v209, %[srdz], s61 offen offset:0 sc1\n" \
"buffer_load_dwordx4 a[164:167], v209, %[srdz], s61 offen offset:1024 sc1\n" \
"buffer_load_dwordx4 a[168:171], v209, %[srdz], s61 offen offset:2048 sc1\n" \
"buffer_load_dwordx4 a[172:175], v209, %[srdz], s61 offen offset:3072 sc1\n" \
"buffer_load_dwordx4 a[176:179], v210, %[srdz], s61 offen offset:0 sc1\n" \
"buffer_load_dwordx4 a[180:183], v210, %[srdz], s61 offen offset:1024 sc1\n" \
"buffer_load_dwordx4 a[184:187], v210, %[srdz], s61 offen offset:2048 sc1\n" \
"buffer_load_dwordx4 a[188:191], v210, %[srdz], s61 offen offset:3072 sc1\n" \
"s_waitcnt vmcnt(0)\n" \
"s_nop 0\n" \
"v_mfma_f32_16x16x32_bf16 v[128:131], a[196:199], a[128:131], v[192:195]\n" \
"v_mfma_f32_16x16x32_bf16 v[128:131], a[200:203], a[132:135], v[128:131]\n" \
"v_mfma_f32_16x16x32_bf16 v[128:131], a[204:207], a[136:139], v[128:131]\n" \
"v_mfma_f32_16x16x32_bf16 v[128:131], a[224:227], a[140:143], v[128:131]\n" \
"v_mfma_f32_16x16x32_bf16 v[132:135], a[196:199], a[144:147], v[196:199]\n" \
"v_mfma_f32_16x16x32_bf16 v[132:135], a[200:203], a[148:151], v[132:135]\n" \
"v_mfma_f32_16x16x32_bf16 v[132:135], a[204:207], a[152:155], v[132:135]\n" \
"v_mfma_f32_16x16x32_bf16 v[132:135], a[224:227], a[156:159], v[132:135]\n" \
"v_mfma_f32_16x16x32_bf16 v[136:139], a[196:199], a[160:163], v[200:203]\n" \
"v_mfma_f32_16x16x32_bf16 v[136:139], a[200:203], a[164:167], v[136:139]\n" \
"v_mfma_f32_16x16x32_bf16 v[136:139], a[204:207], a[168:171], v[136:139]\n" \
"v_mfma_f32_16x16x32_bf16 v[136:139], a[224:227], a[172:175], v[136:139]\n" \
"v_mfma_f32_16x16x32_bf16 v[140:143], a[196:199], a[176:179], v[204:207]\n" \
"v_mfma_f32_16x16x32_bf16 v[140:143], a[200:203], a[180:183], v[140:143]\n" \
"v_mfma_f32_16x16x32_bf16 v[140:143], a[204:207], a[184:187], v[140:143]\n" \
"v_mfma_f32_16x16x32_bf16 v[140:143], a[224:227], a[188:191], v[140:143]\n" \
"s_nop 7\n" \
"s_nop 0\n" \
"v_cmp_u_f32_e64 s[44:45], v128, v132\n" \
"v_cmp_u_f32_e64 s[56:57], v136, v140\n" \
"s_or_b64 s[56:57], s[56:57], s[44:45]\n" \
"s_cbranch_scc0 Lbk3_%=\n" \
"s_sub_u32 s51, s51, 1\n" \
"s_cbranch_scc0 Lsq3_%=\n" \
"s_branch Lbk3_%=\n" \
"Lsp4_%=:\n" \
"s_movk_i32 s51, 0x1000\n" \
"s_cmp_gt_u32 s48, 3\n" \
"s_cselect_b32 s61, 3, 4\n" \
"s_mul_i32 s61, s61, 262144\n" \
"Lsq4_%=:\n" \
"buffer_load_dwordx4 a[64:67], %[oq0], %[srdz], s61 offen offset:0 sc1\n" \
"buffer_load_dwordx4 a[68:71], %[oq0], %[srdz], s61 offen offset:1024 sc1\n" \
"buffer_load_dwordx4 a[72:75], %[oq0], %[srdz], s61 offen offset:2048 sc1\n" \
"buffer_load_dwordx4 a[76:79], %[oq0], %[srdz], s61 offen offset:3072 sc1\n" \
"buffer_load_dwordx4 a[80:83], v208, %[srdz], s61 offen offset:0 sc1\n" \
"buffer_load_dwordx4 a[84:87], v208, %[srdz], s61 offen offset:1024 sc1\n" \
"buffer_load_dwordx4 a[88:91], v208, %[srdz], s61 offen offset:2048 sc1\n" \
"buffer_load_dwordx4 a[92:95], v208, %[srdz], s61 offen offset:3072 sc1\n" \
"buffer_load_dwordx4 a[96:99], v209, %[srdz], s61 offen offset:0 sc1\n" \
"buffer_load_dwordx4 a[100:103], v209, %[srdz], s61 offen offset:1024 sc1\n" \
"buffer_load_dwordx4 a[104:107], v209, %[srdz], s61 offen offset:2048 sc1\n" \
"buffer_load_dwordx4 a[108:111], v209, %[srdz], s61 offen offset:3072 sc1\n" \
"buffer_load_dwordx4 a[112:115], v210, %[srdz], s61 offen offset:0 sc1\n" \
"buffer_load_dwordx4 a[116:119], v210, %[srdz], s61 offen offset:1024 sc1\n" \
"buffer_load_dwordx4 a[120:123], v210, %[srdz], s61 offen offset:2048 sc1\n" \
"buffer_load_dwordx4 a[124:127], v210, %[srdz], s61 offen offset:3072 sc1\n" \
"s_waitcnt vmcnt(0)\n" \
"s_nop 0\n" \
"v_mfma_f32_16x16x32_bf16 v[192:195], a[196:199], a[64:67], v[128:131]\n" \
"v_mfma_f32_16x16x32_bf16 v[192:195], a[200:203], a[68:71], v[192:195]\n" \
"v_mfma_f32_16x16x32_bf16 v[192:195], a[204:207], a[72:75], v[192:195]\n" \
"v_mfma_f32_16x16x32_bf16 v[192:195], a[220:223], a[76:79], v[192:195]\n" \
"v_mfma_f32_16x16x32_bf16 v[196:199], a[196:199], a[80:83], v[132:135]\n" \
"v_mfma_f32_16x16x32_bf16 v[196:199], a[200:203], a[84:87], v[196:199]\n" \
"v_mfma_f32_16x16x32_bf16 v[196:199], a[204:207], a[88:91], v[196:199]\n" \
"v_mfma_f32_16x16x32_bf16 v[196:199], a[220:223], a[92:95], v[196:199]\n" \
"v_mfma_f32_16x16x32_bf16 v[200:203], a[196:199], a[96:99], v[136:139]\n" \
"v_mfma_f32_16x16x32_bf16 v[200:203], a[200:203], a[100:103], v[200:203]\n" \
"v_mfma_f32_16x16x32_bf16 v[200:203], a[204:207], a[104:107], v[200:203]\n" \
"v_mfma_f32_16x16x32_bf16 v[200:203], a[220:223], a[108:111], v[200:203]\n" \
"v_mfma_f32_16x16x32_bf16 v[204:207], a[196:199], a[112:115], v[140:143]\n" \
"v_mfma_f32_16x16x32_bf16 v[204:207], a[200:203], a[116:119], v[204:207]\n" \
"v_mfma_f32_16x16x32_bf16 v[204:207], a[204:207], a[120:123], v[204:207]\n" \
"v_mfma_f32_16x16x32_bf16 v[204:207], a[220:223], a[124:127], v[204:207]\n" \
"s_nop 7\n" \
"s_nop 0\n" \
"v_cmp_u_f32_e64 s[44:45], v192, v196\n" \
"v_cmp_u_f32_e64 s[56:57], v200, v204\n" \
"s_or_b64 s[56:57], s[56:57], s[44:45]\n" \
"s_cbranch_scc0 Lbk4_%=\n" \
"s_sub_u32 s51, s51, 1\n" \
"s_cbranch_scc0 Lsq4_%=\n" \
"s_branch Lbk4_%=\n" \
"Lsp5_%=:\n" \
"s_movk_i32 s51, 0x1000\n" \
"s_cmp_gt_u32 s48, 4\n" \
"s_cselect_b32 s61, 4, 5\n" \
"s_mul_i32 s61, s61, 262144\n" \
"Lsq5_%=:\n" \
"buffer_load_dwordx4 v[0:3], %[oq0], %[srdz], s61 offen offset:0 sc1\n" \
"buffer_load_dwordx4 v[8:11], %[oq0], %[srdz], s61 offen offset:1024 sc1\n" \
"buffer_load_dwordx4 v[16:19], %[oq0], %[srdz], s61 offen offset:2048 sc1\n" \
"buffer_load_dwordx4 v[24:27], %[oq0], %[srdz], s61 offen offset:3072 sc1\n" \
"buffer_load_dwordx4 v[32:35], v208, %[srdz], s61 offen offset:0 sc1\n" \
"buffer_load_dwordx4 v[40:43], v208, %[srdz], s61 offen offset:1024 sc1\n" \
"buffer_load_dwordx4 v[48:51], v208, %[srdz], s61 offen offset:2048 sc1\n" \
"buffer_load_dwordx4 v[56:59], v208, %[srdz], s61 offen offset:3072 sc1\n" \
"buffer_load_dwordx4 v[64:67], v209, %[srdz], s61 offen offset:0 sc1\n" \
"buffer_load_dwordx4 v[72:75], v209, %[srdz], s61 offen offset:1024 sc1\n" \
"buffer_load_dwordx4 v[80:83], v209, %[srdz], s61 offen offset:2048 sc1\n" \
"buffer_load_dwordx4 v[88:91], v209, %[srdz], s61 offen offset:3072 sc1\n" \
"buffer_load_dwordx4 v[96:99], v210, %[srdz], s61 offen offset:0 sc1\n" \
"buffer_load_dwordx4 v[104:107], v210, %[srdz], s61 offen offset:1024 sc1\n" \
"buffer_load_dwordx4 v[112:115], v210, %[srdz], s61 offen offset:2048 sc1\n" \
"buffer_load_dwordx4 v[120:123], v210, %[srdz], s61 offen offset:3072 sc1\n" \
"s_waitcnt vmcnt(0)\n" \
"s_nop 0\n" \
"v_mfma_f32_16x16x32_bf16 v[128:131], a[232:235], v[0:3], v[192:195]\n" \
"v_mfma_f32_16x16x32_bf16 v[128:131], a[236:239], v[8:11], v[128:131]\n" \
"v_mfma_f32_16x16x32_bf16 v[128:131], a[208:211], v[16:19], v[128:131]\n" \
"v_mfma_f32_16x16x32_bf16 v[128:131], a[228:231], v[24:27], v[128:131]\n" \
"v_mfma_f32_16x16x32_bf16 v[132:135], a[232:235], v[32:35], v[196:199]\n" \
"v_mfma_f32_16x16x32_bf16 v[132:135], a[236:239], v[40:43], v[132:135]\n" \
"v_mfma_f32_16x16x32_bf16 v[132:135], a[208:211], v[48:51], v[132:135]\n" \
"v_mfma_f32_16x16x32_bf16 v[132:135], a[228:231], v[56:59], v[132:135]\n" \
"v_mfma_f32_16x16x32_bf16 v[136:139], a[232:235], v[64:67], v[200:203]\n" \
"v_mfma_f32_16x16x32_bf16 v[136:139], a[236:239], v[72:75], v[136:139]\n" \
"v_mfma_f32_16x16x32_bf16 v[136:139], a[208:211], v[80:83], v[136:139]\n" \
"v_mfma_f32_16x16x32_bf16 v[136:139], a[228:231], v[88:91], v[136:139]\n" \
"v_mfma_f32_16x16x32_bf16 v[140:143], a[232:235], v[96:99], v[204:207]\n" \
"v_mfma_f32_16x16x32_bf16 v[140:143], a[236:239], v[104:107], v[140:143]\n" \
"v_mfma_f32_16x16x32_bf16 v[140:143], a[208:211], v[112:115], v[140:143]\n" \
"v_mfma_f32_16x16x32_bf16 v[140:143], a[228:231], v[120:123], v[140:143]\n" \
"s_nop 7\n" \
"s_nop 0\n" \
"v_cmp_u_f32_e64 s[44:45], v128, v132\n" \
"v_cmp_u_f32_e64 s[56:57], v136, v140\n" \
"s_or_b64 s[56:57], s[56:57], s[44:45]\n" \
"s_cbranch_scc0 Lbk5_%=\n" \
"s_sub_u32 s51, s51, 1\n" \
"s_cbranch_scc0 Lsq5_%=\n" \
"s_branch Lbk5_%=\n" \
"Lsp6_%=:\n" \
"s_movk_i32 s51, 0x1000\n" \
"s_cmp_gt_u32 s48, 5\n" \
"s_cselect_b32 s61, 5, 6\n" \
"s_mul_i32 s61, s61, 262144\n" \
"Lsq6_%=:\n" \
"buffer_load_dwordx4 a[0:3], %[oq0], %[srdz], s61 offen offset:0 sc1\n" \
"buffer_load_dwordx4 a[4:7], %[oq0], %[srdz], s61 offen offset:1024 sc1\n" \
"buffer_load_dwordx4 a[8:11], %[oq0], %[srdz], s61 offen offset:2048 sc1\n" \
"buffer_load_dwordx4 a[12:15], %[oq0], %[srdz], s61 offen offset:3072 sc1\n" \
"buffer_load_dwordx4 a[16:19], v208, %[srdz], s61 offen offset:0 sc1\n" \
"buffer_load_dwordx4 a[20:23], v208, %[srdz], s61 offen offset:1024 sc1\n" \
"buffer_load_dwordx4 a[24:27], v208, %[srdz], s61 offen offset:2048 sc1\n" \
"buffer_load_dwordx4 a[28:31], v208, %[srdz], s61 offen offset:3072 sc1\n" \
"buffer_load_dwordx4 a[32:35], v209, %[srdz], s61 offen offset:0 sc1\n" \
"buffer_load_dwordx4 a[36:39], v209, %[srdz], s61 offen offset:1024 sc1\n" \
"buffer_load_dwordx4 a[40:43], v209, %[srdz], s61 offen offset:2048 sc1\n" \
"buffer_load_dwordx4 a[44:47], v209, %[srdz], s61 offen offset:3072 sc1\n" \
"buffer_load_dwordx4 a[48:51], v210, %[srdz], s61 offen offset:0 sc1\n" \
"buffer_load_dwordx4 a[52:55], v210, %[srdz], s61 offen offset:1024 sc1\n" \
"buffer_load_dwordx4 a[56:59], v210, %[srdz], s61 offen offset:2048 sc1\n" \
"buffer_load_dwordx4 a[60:63], v210, %[srdz], s61 offen offset:3072 sc1\n" \
"s_waitcnt vmcnt(0)\n" \
"s_nop 0\n" \
"v_mfma_f32_16x16x32_bf16 v[192:195], a[196:199], a[0:3], v[128:131]\n" \
"v_mfma_f32_16x16x32_bf16 v[192:195], a[200:203], a[4:7], v[192:195]\n" \
"v_mfma_f32_16x16x32_bf16 v[192:195], a[204:207], a[8:11], v[192:195]\n" \
"v_mfma_f32_16x16x32_bf16 v[192:195], a[220:223], a[12:15], v[192:195]\n" \
"v_mfma_f32_16x16x32_bf16 v[196:199], a[196:199], a[16:19], v[132:135]\n" \
"v_mfma_f32_16x16x32_bf16 v[196:199], a[200:203], a[20:23], v[196:199]\n" \
"v_mfma_f32_16x16x32_bf16 v[196:199], a[204:207], a[24:27], v[196:199]\n" \
"v_mfma_f32_16x16x32_bf16 v[196:199], a[220:223], a[28:31], v[196:199]\n" \
"v_mfma_f32_16x16x32_bf16 v[200:203], a[196:199], a[32:35], v[136:139]\n" \
"v_mfma_f32_16x16x32_bf16 v[200:203], a[200:203], a[36:39], v[200:203]\n" \
"v_mfma_f32_16x16x32_bf16 v[200:203], a[204:207], a[40:43], v[200:203]\n" \
"v_mfma_f32_16x16x32_bf16 v[200:203], a[220:223], a[44:47], v[200:203]\n" \
"v_mfma_f32_16x16x32_bf16 v[204:207], a[196:199], a[48:51], v[140:143]\n" \
"v_mfma_f32_16x16x32_bf16 v[204:207], a[200:203], a[52:55], v[204:207]\n" \
"v_mfma_f32_16x16x32_bf16 v[204:207], a[204:207], a[56:59], v[204:207]\n" \
"v_mfma_f32_16x16x32_bf16 v[204:207], a[220:223], a[60:63], v[204:207]\n" \
"s_nop 7\n" \
"s_nop 0\n" \
"v_cmp_u_f32_e64 s[44:45], v192, v196\n" \
"v_cmp_u_f32_e64 s[56:57], v200, v204\n" \
"s_or_b64 s[56:57], s[56:57], s[44:45]\n" \
"s_cbranch_scc0 Lbk6_%=\n" \
"s_sub_u32 s51, s51, 1\n" \
"s_cbranch_scc0 Lsq6_%=\n" \
"s_branch Lbk6_%=\n" \
"Lsp7_%=:\n" \
"s_movk_i32 s51, 0x1000\n" \
"s_cmp_gt_u32 s48, 6\n" \
"s_cselect_b32 s61, 6, 7\n" \
"s_mul_i32 s61, s61, 262144\n" \
"Lsq7_%=:\n" \
"buffer_load_dwordx4 a[128:131], %[oq0], %[srdz], s61 offen offset:0 sc1\n" \
"buffer_load_dwordx4 a[132:135], %[oq0], %[srdz], s61 offen offset:1024 sc1\n" \
"buffer_load_dwordx4 a[136:139], %[oq0], %[srdz], s61 offen offset:2048 sc1\n" \
"buffer_load_dwordx4 a[140:143], %[oq0], %[srdz], s61 offen offset:3072 sc1\n" \
"buffer_load_dwordx4 a[144:147], v208, %[srdz], s61 offen offset:0 sc1\n" \
"buffer_load_dwordx4 a[148:151], v208, %[srdz], s61 offen offset:1024 sc1\n" \
"buffer_load_dwordx4 a[152:155], v208, %[srdz], s61 offen offset:2048 sc1\n" \
"buffer_load_dwordx4 a[156:159], v208, %[srdz], s61 offen offset:3072 sc1\n" \
"buffer_load_dwordx4 a[160:163], v209, %[srdz], s61 offen offset:0 sc1\n" \
"buffer_load_dwordx4 a[164:167], v209, %[srdz], s61 offen offset:1024 sc1\n" \
"buffer_load_dwordx4 a[168:171], v209, %[srdz], s61 offen offset:2048 sc1\n" \
"buffer_load_dwordx4 a[172:175], v209, %[srdz], s61 offen offset:3072 sc1\n" \
"buffer_load_dwordx4 a[176:179], v210, %[srdz], s61 offen offset:0 sc1\n" \
"buffer_load_dwordx4 a[180:183], v210, %[srdz], s61 offen offset:1024 sc1\n" \
"buffer_load_dwordx4 a[184:187], v210, %[srdz], s61 offen offset:2048 sc1\n" \
"buffer_load_dwordx4 a[188:191], v210, %[srdz], s61 offen offset:3072 sc1\n" \
"s_waitcnt vmcnt(0)\n" \
"s_nop 0\n" \
"v_mfma_f32_16x16x32_bf16 v[128:131], a[208:211], a[128:131], v[192:195]\n" \
"v_mfma_f32_16x16x32_bf16 v[128:131], a[224:227], a[132:135], v[128:131]\n" \
"v_mfma_f32_16x16x32_bf16 v[128:131], a[228:231], a[136:139], v[128:131]\n" \
"v_mfma_f32_16x16x32_bf16 v[128:131], a[236:239], a[140:143], v[128:131]\n" \
"v_mfma_f32_16x16x32_bf16 v[132:135], a[208:211], a[144:147], v[196:199]\n" \
"v_mfma_f32_16x16x32_bf16 v[132:135], a[224:227], a[148:151], v[132:135]\n" \
"v_mfma_f32_16x16x32_bf16 v[132:135], a[228:231], a[152:155], v[132:135]\n" \
"v_mfma_f32_16x16x32_bf16 v[132:135], a[236:239], a[156:159], v[132:135]\n" \
"v_mfma_f32_16x16x32_bf16 v[136:139], a[208:211], a[160:163], v[200:203]\n" \
"v_mfma_f32_16x16x32_bf16 v[136:139], a[224:227], a[164:167], v[136:139]\n" \
"v_mfma_f32_16x16x32_bf16 v[136:139], a[228:231], a[168:171], v[136:139]\n" \
"v_mfma_f32_16x16x32_bf16 v[136:139], a[236:239], a[172:175], v[136:139]\n" \
"v_mfma_f32_16x16x32_bf16 v[140:143], a[208:211], a[176:179], v[204:207]\n" \
"v_mfma_f32_16x16x32_bf16 v[140:143], a[224:227], a[180:183], v[140:143]\n" \
"v_mfma_f32_16x16x32_bf16 v[140:143], a[228:231], a[184:187], v[140:143]\n" \
"v_mfma_f32_16x16x32_bf16 v[140:143], a[236:239], a[188:191], v[140:143]\n" \
"s_nop 7\n" \
"s_nop 0\n" \
"v_cmp_u_f32_e64 s[44:45], v128, v132\n" \
"v_cmp_u_f32_e64 s[56:57], v136, v140\n" \
"s_or_b64 s[56:57], s[56:57], s[44:45]\n" \
"s_cbranch_scc0 Lbk7_%=\n" \
"s_sub_u32 s51, s51, 1\n" \
"s_cbranch_scc0 Lsq7_%=\n" \
"s_branch Lbk7_%=\n" \
"Lspk_%=:\n" \
"s_nop 0\n" \
"s_mov_b32 s51, 0x10000\n" \
"Lsqk_%=:\n" \
"buffer_load_dword v229, v226, %[srdz], s50 offen sc1\n" \
"s_waitcnt vmcnt(0)\n" \
"s_nop 0\n" \
"v_cmp_eq_u32_e64 s[56:57], 0, v229\n" \
"s_cmp_lg_u64 s[56:57], 0\n" \
"s_cbranch_scc0 Lbkk_%=\n" \
"s_sub_u32 s51, s51, 1\n" \
"s_cbranch_scc0 Lsqk_%=\n" \
"s_branch Lbkk_%=\n" \
"Lspp_%=:\n" \
"s_nop 0\n" \
"s_mov_b32 s51, 0x10000\n" \
"Lsqp_%=:\n" \
"buffer_load_dword v232, v226, %[srdz], s54 offen sc1\n" \
"s_waitcnt vmcnt(0)\n" \
"v_cmp_eq_u32_e32 vcc, 0, v232\n" \
"s_cbranch_vccz Lbkp_%=\n" \
"s_sub_u32 s51, s51, 1\n" \
"s_cbranch_scc0 Lsqp_%=\n" \
"s_branch Lbkp_%=\n" \
"Lprobe_%=:\n" \
"s_mov_b32 s62, -1\n" \
"s_mov_b32 s63, -1\n" \
"s_memtime s[56:57]\n" \
"s_waitcnt lgkmcnt(0)\n" \
"s_nop 0\n" \
"s_load_dword s61, %[pcnt], 0x29900\n" \
"s_waitcnt lgkmcnt(0)\n" \
"s_nop 0\n" \
"s_memtime s[58:59]\n" \
"s_waitcnt lgkmcnt(0)\n" \
"s_sub_u32 s58, s58, s56\n" \
"s_min_u32 s62, s62, s58\n" \
"s_nop 0\n" \
"s_memtime s[56:57]\n" \
"s_waitcnt lgkmcnt(0)\n" \
"s_nop 0\n" \
"s_load_dword s61, %[pcnt], 0x2b900\n" \
"s_waitcnt lgkmcnt(0)\n" \
"s_nop 0\n" \
"s_memtime s[58:59]\n" \
"s_waitcnt lgkmcnt(0)\n" \
"s_sub_u32 s58, s58, s56\n" \
"s_min_u32 s63, s63, s58\n" \
"s_nop 0\n" \
"s_memtime s[56:57]\n" \
"s_waitcnt lgkmcnt(0)\n" \
"s_nop 0\n" \
"s_load_dword s61, %[pcnt], 0x2dd80\n" \
"s_waitcnt lgkmcnt(0)\n" \
"s_nop 0\n" \
"s_memtime s[58:59]\n" \
"s_waitcnt lgkmcnt(0)\n" \
"s_sub_u32 s58, s58, s56\n" \
"s_min_u32 s62, s62, s58\n" \
"s_nop 0\n" \
"s_memtime s[56:57]\n" \
"s_waitcnt lgkmcnt(0)\n" \
"s_nop 0\n" \
"s_load_dword s61, %[pcnt], 0x2fd80\n" \
"s_waitcnt lgkmcnt(0)\n" \
"s_nop 0\n" \
"s_memtime s[58:59]\n" \
"s_waitcnt lgkmcnt(0)\n" \
"s_sub_u32 s58, s58, s56\n" \
"s_min_u32 s63, s63, s58\n" \
"s_nop 0\n" \
"s_memtime s[56:57]\n" \
"s_waitcnt lgkmcnt(0)\n" \
"s_nop 0\n" \
"s_load_dword s61, %[pcnt], 0x2a200\n" \
"s_waitcnt lgkmcnt(0)\n" \
"s_nop 0\n" \
"s_memtime s[58:59]\n" \
"s_waitcnt lgkmcnt(0)\n" \
"s_sub_u32 s58, s58, s56\n" \
"s_min_u32 s62, s62, s58\n" \
"s_nop 0\n" \
"s_memtime s[56:57]\n" \
"s_waitcnt lgkmcnt(0)\n" \
"s_nop 0\n" \
"s_load_dword s61, %[pcnt], 0x2c200\n" \
"s_waitcnt lgkmcnt(0)\n" \
"s_nop 0\n" \
"s_memtime s[58:59]\n" \
"s_waitcnt lgkmcnt(0)\n" \
"s_sub_u32 s58, s58, s56\n" \
"s_min_u32 s63, s63, s58\n" \
"s_cmp_lt_u32 s62, s63\n" \
"s_cselect_b32 s61, 8, 0\n" \
"s_mov_b64 exec, 1\n" \
"buffer_store_dwordx2 v[234:235], off, %[srdz], s61 offset:128 sc1\n" \
"s_mov_b64 exec, -1\n" \
"s_branch Lgo_%=\n" \
"Lnsp_%=:\n" \
"s_mov_b32 s61, 0x10000\n" \
"Lnsq_%=:\n" \
"buffer_load_dwordx4 v[230:233], off, %[srdz], 0 offset:128 sc1\n" \
"s_waitcnt vmcnt(0)\n" \
"v_cmp_eq_u64_e32 vcc, v[230:231], v[234:235]\n" \
"v_cmp_eq_u64_e64 s[56:57], v[232:233], v[234:235]\n" \
"s_nop 3\n" \
"s_or_b64 s[58:59], vcc, s[56:57]\n" \
"s_cbranch_scc1 Lnbk_%=\n" \
"s_sub_u32 s61, s61, 1\n" \
"s_cbranch_scc0 Lnsq_%=\n" \
"s_branch Lnbk_%=\n" \
"Lwalk_%=:\n" \
"s_mov_b64 exec, 0\n" \
"s_cmp_eq_u32 %[wid], 1\n" \
"s_cbranch_scc1 Lk0_%=\n" \
"s_cmp_eq_u32 %[wid], 2\n" \
"s_cbranch_scc1 Lk6_%=\n" \
"s_branch Lk12_%=\n" \
"adaln_kr:\n" \
"s_mov_b64 exec, -1\n" \
"s_branch Lgo_%=\n" \
"Lslow_%=:\n" \
"s_mov_b32 s52, 1\n" \
"s_mov_b32 s48, 0\n" \
"s_mov_b32 s51, 32\n" \
"s_nop 0\n" \
"s_mov_b32 s62, 0x80000000\n" \
"s_mov_b32 s63, 0x80000000\n" \
"s_waitcnt vmcnt(0)\n" \
"s_nop 0\n" \
"v_readlane_b32 s64, %[tlo], 9\n" \
"v_readlane_b32 s65, %[thi], 9\n" \
"s_mov_b32 s66, %[wbytes]\n" \
"v_mov_b32_e32 v229, %[vg0]\n" \
"s_mov_b32 s44, %[m0t]\n" \
"s_mov_b32 s45, 0\n" \
"v_xor_b32_e64 v208, 64, %[kb0]\n" \
"v_xor_b32_e32 v209, 0x80, %[kb0]\n" \
"v_xor_b32_e32 v210, 0xc0, %[kb0]\n" \
"v_xor_b32_e64 v212, 64, %[kd0]\n" \
"v_xor_b32_e32 v213, 0x80, %[kd0]\n" \
"v_xor_b32_e32 v214, 0xc0, %[kd0]\n" \
"v_xor_b32_e64 v211, 32, %[vvb]\n" \
"v_add_u32_e32 v215, 0xfffffc80, %[vd0]\n" \
"s_mov_b32 s66, 0\n" \
"s_add_u32 s40, %[m0b], 0\n" \
"s_add_u32 s41, %[m0b], 16384\n" \
"s_add_u32 s42, %[m0b], 32768\n" \
"s_add_u32 s43, %[m0b], 49152\n" \
"v_mov_b32_e32 v225, 0xff800000\n" \
"v_mov_b32_e32 v226, 0xff800000\n" \
"v_mov_b32_e32 v227, 0xff800000\n" \
"v_mov_b32_e32 v228, 0xff800000\n" \
"s_waitcnt vmcnt(0) lgkmcnt(0)\n" \
"s_barrier\n" \
"Lsl_%=:\n" \
"s_mov_b32 m0, s40\n" \
"s_nop 0\n" \
"buffer_load_dwordx4 %[kd0], %[srdk], s48 offen offset:0 lds\n" \
"buffer_load_dwordx4 v212, %[srdk], s48 offen offset:1024 lds\n" \
"buffer_load_dwordx4 v213, %[srdk], s48 offen offset:2048 lds\n" \
"buffer_load_dwordx4 v214, %[srdk], s48 offen offset:3072 lds\n" \
"s_waitcnt vmcnt(0)\n" \
"s_barrier\n" \
"ds_read_b128 a[64:67], %[kb0] offset:0\n" \
"ds_read_b128 a[68:71], v208 offset:0\n" \
"ds_read_b128 a[72:75], v209 offset:0\n" \
"ds_read_b128 a[76:79], v210 offset:0\n" \
"ds_read_b128 a[92:95], v210 offset:4096\n" \
"ds_read_b128 a[88:91], v209 offset:4096\n" \
"ds_read_b128 a[84:87], v208 offset:4096\n" \
"ds_read_b128 a[80:83], %[kb0] offset:4096\n" \
"ds_read_b128 a[96:99], %[kb0] offset:8192\n" \
"ds_read_b128 a[100:103], v208 offset:8192\n" \
"ds_read_b128 a[104:107], v209 offset:8192\n" \
"ds_read_b128 a[108:111], v210 offset:8192\n" \
"ds_read_b128 a[124:127], v210 offset:12288\n" \
"ds_read_b128 a[120:123], v209 offset:12288\n" \
"ds_read_b128 a[116:119], v208 offset:12288\n" \
"ds_read_b128 a[112:115], %[kb0] offset:12288\n" \
"s_waitcnt lgkmcnt(0)\n" \
"s_barrier\n" \
"v_mfma_f32_16x16x32_bf16 v[128:131], a[64:67], a[0:3], 0\n" \
"v_mfma_f32_16x16x32_bf16 v[128:131], a[68:71], a[4:7], v[128:131]\n" \
"v_mfma_f32_16x16x32_bf16 v[128:131], a[72:75], a[8:11], v[128:131]\n" \
"v_mfma_f32_16x16x32_bf16 v[128:131], a[76:79], a[12:15], v[128:131]\n" \
"v_mfma_f32_16x16x32_bf16 v[132:135], a[92:95], a[12:15], 0\n" \
"v_mfma_f32_16x16x32_bf16 v[132:135], a[88:91], a[8:11], v[132:135]\n" \
"v_mfma_f32_16x16x32_bf16 v[132:135], a[84:87], a[4:7], v[132:135]\n" \
"v_mfma_f32_16x16x32_bf16 v[132:135], a[80:83], a[0:3], v[132:135]\n" \
"v_mfma_f32_16x16x32_bf16 v[136:139], a[64:67], a[16:19], 0\n" \
"v_mfma_f32_16x16x32_bf16 v[136:139], a[68:71], a[20:23], v[136:139]\n" \
"v_mfma_f32_16x16x32_bf16 v[136:139], a[72:75], a[24:27], v[136:139]\n" \
"v_mfma_f32_16x16x32_bf16 v[136:139], a[76:79], a[28:31], v[136:139]\n" \
"v_mfma_f32_16x16x32_bf16 v[140:143], a[92:95], a[28:31], 0\n" \
"v_mfma_f32_16x16x32_bf16 v[140:143], a[88:91], a[24:27], v[140:143]\n" \
"v_mfma_f32_16x16x32_bf16 v[140:143], a[84:87], a[20:23], v[140:143]\n" \
"v_mfma_f32_16x16x32_bf16 v[140:143], a[80:83], a[16:19], v[140:143]\n" \
"v_mfma_f32_16x16x32_bf16 v[144:147], a[64:67], a[32:35], 0\n" \
"v_mfma_f32_16x16x32_bf16 v[144:147], a[68:71], a[36:39], v[144:147]\n" \
"v_mfma_f32_16x16x32_bf16 v[144:147], a[72:75], a[40:43], v[144:147]\n" \
"v_mfma_f32_16x16x32_bf16 v[144:147], a[76:79], a[44:47], v[144:147]\n" \
"v_mfma_f32_16x16x32_bf16 v[148:151], a[92:95], a[44:47], 0\n" \
"v_mfma_f32_16x16x32_bf16 v[148:151], a[88:91], a[40:43], v[148:151]\n" \
"v_mfma_f32_16x16x32_bf16 v[148:151], a[84:87], a[36:39], v[148:151]\n" \
"v_mfma_f32_16x16x32_bf16 v[148:151], a[80:83], a[32:35], v[148:151]\n" \
"v_mfma_f32_16x16x32_bf16 v[152:155], a[64:67], a[48:51], 0\n" \
"v_mfma_f32_16x16x32_bf16 v[152:155], a[68:71], a[52:55], v[152:155]\n" \
"v_mfma_f32_16x16x32_bf16 v[152:155], a[72:75], a[56:59], v[152:155]\n" \
"v_mfma_f32_16x16x32_bf16 v[152:155], a[76:79], a[60:63], v[152:155]\n" \
"v_mfma_f32_16x16x32_bf16 v[156:159], a[92:95], a[60:63], 0\n" \
"v_mfma_f32_16x16x32_bf16 v[156:159], a[88:91], a[56:59], v[156:159]\n" \
"v_mfma_f32_16x16x32_bf16 v[156:159], a[84:87], a[52:55], v[156:159]\n" \
"v_mfma_f32_16x16x32_bf16 v[156:159], a[80:83], a[48:51], v[156:159]\n" \
"s_nop 7\n" \
"s_nop 0\n" \
"v_max3_f32 v230, v128, v129, v130\n" \
"v_max3_f32 v231, v131, v132, v133\n" \
"v_max3_f32 v230, v230, v134, v135\n" \
"v_max3_f32 v225, v230, v231, v225\n" \
"v_max3_f32 v230, v136, v137, v138\n" \
"v_max3_f32 v231, v139, v140, v141\n" \
"v_max3_f32 v230, v230, v142, v143\n" \
"v_max3_f32 v226, v230, v231, v226\n" \
"v_max3_f32 v230, v144, v145, v146\n" \
"v_max3_f32 v231, v147, v148, v149\n" \
"v_max3_f32 v230, v230, v150, v151\n" \
"v_max3_f32 v227, v230, v231, v227\n" \
"v_max3_f32 v230, v152, v153, v154\n" \
"v_max3_f32 v231, v155, v156, v157\n" \
"v_max3_f32 v230, v230, v158, v159\n" \
"v_max3_f32 v228, v230, v231, v228\n" \
"s_nop 3\n" \
"s_nop 0\n" \
"v_mfma_f32_16x16x32_bf16 v[128:131], a[96:99], a[0:3], 0\n" \
"v_mfma_f32_16x16x32_bf16 v[128:131], a[100:103], a[4:7], v[128:131]\n" \
"v_mfma_f32_16x16x32_bf16 v[128:131], a[104:107], a[8:11], v[128:131]\n" \
"v_mfma_f32_16x16x32_bf16 v[128:131], a[108:111], a[12:15], v[128:131]\n" \
"v_mfma_f32_16x16x32_bf16 v[132:135], a[124:127], a[12:15], 0\n" \
"v_mfma_f32_16x16x32_bf16 v[132:135], a[120:123], a[8:11], v[132:135]\n" \
"v_mfma_f32_16x16x32_bf16 v[132:135], a[116:119], a[4:7], v[132:135]\n" \
"v_mfma_f32_16x16x32_bf16 v[132:135], a[112:115], a[0:3], v[132:135]\n" \
"v_mfma_f32_16x16x32_bf16 v[136:139], a[96:99], a[16:19], 0\n" \
"v_mfma_f32_16x16x32_bf16 v[136:139], a[100:103], a[20:23], v[136:139]\n" \
"v_mfma_f32_16x16x32_bf16 v[136:139], a[104:107], a[24:27], v[136:139]\n" \
"v_mfma_f32_16x16x32_bf16 v[136:139], a[108:111], a[28:31], v[136:139]\n" \
"v_mfma_f32_16x16x32_bf16 v[140:143], a[124:127], a[28:31], 0\n" \
"v_mfma_f32_16x16x32_bf16 v[140:143], a[120:123], a[24:27], v[140:143]\n" \
"v_mfma_f32_16x16x32_bf16 v[140:143], a[116:119], a[20:23], v[140:143]\n" \
"v_mfma_f32_16x16x32_bf16 v[140:143], a[112:115], a[16:19], v[140:143]\n" \
"v_mfma_f32_16x16x32_bf16 v[144:147], a[96:99], a[32:35], 0\n" \
"v_mfma_f32_16x16x32_bf16 v[144:147], a[100:103], a[36:39], v[144:147]\n" \
"v_mfma_f32_16x16x32_bf16 v[144:147], a[104:107], a[40:43], v[144:147]\n" \
"v_mfma_f32_16x16x32_bf16 v[144:147], a[108:111], a[44:47], v[144:147]\n" \
"v_mfma_f32_16x16x32_bf16 v[148:151], a[124:127], a[44:47], 0\n" \
"v_mfma_f32_16x16x32_bf16 v[148:151], a[120:123], a[40:43], v[148:151]\n" \
"v_mfma_f32_16x16x32_bf16 v[148:151], a[116:119], a[36:39], v[148:151]\n" \
"v_mfma_f32_16x16x32_bf16 v[148:151], a[112:115], a[32:35], v[148:151]\n" \
"v_mfma_f32_16x16x32_bf16 v[152:155], a[96:99], a[48:51], 0\n" \
"v_mfma_f32_16x16x32_bf16 v[152:155], a[100:103], a[52:55], v[152:155]\n" \
"v_mfma_f32_16x16x32_bf16 v[152:155], a[104:107], a[56:59], v[152:155]\n" \
"v_mfma_f32_16x16x32_bf16 v[152:155], a[108:111], a[60:63], v[152:155]\n" \
"v_mfma_f32_16x16x32_bf16 v[156:159], a[124:127], a[60:63], 0\n" \
"v_mfma_f32_16x16x32_bf16 v[156:159], a[120:123], a[56:59], v[156:159]\n" \
"v_mfma_f32_16x16x32_bf16 v[156:159], a[116:119], a[52:55], v[156:159]\n" \
"v_mfma_f32_16x16x32_bf16 v[156:159], a[112:115], a[48:51], v[156:159]\n" \
"s_nop 7\n" \
"s_nop 0\n" \
"v_max3_f32 v230, v128, v129, v130\n" \
"v_max3_f32 v231, v131, v132, v133\n" \
"v_max3_f32 v230, v230, v134, v135\n" \
"v_max3_f32 v225, v230, v231, v225\n" \
"v_max3_f32 v230, v136, v137, v138\n" \
"v_max3_f32 v231, v139, v140, v141\n" \
"v_max3_f32 v230, v230, v142, v143\n" \
"v_max3_f32 v226, v230, v231, v226\n" \
"v_max3_f32 v230, v144, v145, v146\n" \
"v_max3_f32 v231, v147, v148, v149\n" \
"v_max3_f32 v230, v230, v150, v151\n" \
"v_max3_f32 v227, v230, v231, v227\n" \
"v_max3_f32 v230, v152, v153, v154\n" \
"v_max3_f32 v231, v155, v156, v157\n" \
"v_max3_f32 v230, v230, v158, v159\n" \
"v_max3_f32 v228, v230, v231, v228\n" \
"s_nop 3\n" \
"s_add_u32 s48, s48, s54\n" \
"s_sub_u32 s51, s51, 1\n" \
"s_cmp_lg_u32 s51, 0\n" \
"s_cbranch_scc1 Lsl_%=\n" \
"s_nop 0\n" \
"v_mov_b32_e32 v232, v225\n" \
"v_mov_b32_e32 v233, v226\n" \
"v_mov_b32_e32 v234, v227\n" \
"v_mov_b32_e32 v235, v228\n" \
"s_nop 0\n" \
"v_permlane16_swap_b32_e32 v225, v232\n" \
"v_permlane16_swap_b32_e32 v226, v233\n" \
"v_permlane16_swap_b32_e32 v227, v234\n" \
"v_permlane16_swap_b32_e32 v228, v235\n" \
"v_max_f32_e32 v225, v225, v232\n" \
"v_max_f32_e32 v226, v226, v233\n" \
"v_max_f32_e32 v227, v227, v234\n" \
"v_max_f32_e32 v228, v228, v235\n" \
"v_mov_b32_e32 v232, v225\n" \
"v_mov_b32_e32 v233, v226\n" \
"v_mov_b32_e32 v234, v227\n" \
"v_mov_b32_e32 v235, v228\n" \
"s_nop 0\n" \
"v_permlane32_swap_b32_e32 v225, v232\n" \
"v_permlane32_swap_b32_e32 v226, v233\n" \
"v_permlane32_swap_b32_e32 v227, v234\n" \
"v_permlane32_swap_b32_e32 v228, v235\n" \
"v_max_f32_e32 v225, v225, v232\n" \
"v_max_f32_e32 v226, v226, v233\n" \
"v_max_f32_e32 v227, v227, v234\n" \
"v_max_f32_e32 v228, v228, v235\n" \
"s_cmp_lg_u32 s69, 0\n" \
"s_cbranch_scc1 Lnewm_%=\n" \
"v_sub_f32_e32 v225, 0, v160\n" \
"v_sub_f32_e32 v226, 0, v164\n" \
"v_sub_f32_e64 v227, 0, v168\n" \
"v_sub_f32_e32 v228, 0, v172\n" \
"Lnewm_%=:\n" \
"s_mov_b32 s48, 0\n" \
"s_mov_b32 m0, s40\n" \
"s_nop 0\n" \
"buffer_load_dwordx4 %[kd0], %[srdk], s48 offen offset:0 lds\n" \
"buffer_load_dwordx4 v212, %[srdk], s48 offen offset:1024 lds\n" \
"buffer_load_dwordx4 v213, %[srdk], s48 offen offset:2048 lds\n" \
"buffer_load_dwordx4 v214, %[srdk], s48 offen offset:3072 lds\n" \
"s_mov_b32 s48, 16384\n" \
"s_mov_b32 m0, s41\n" \
"s_nop 0\n" \
"buffer_load_dwordx4 %[kd0], %[srdk], s48 offen offset:0 lds\n" \
"buffer_load_dwordx4 v212, %[srdk], s48 offen offset:1024 lds\n" \
"buffer_load_dwordx4 v213, %[srdk], s48 offen offset:2048 lds\n" \
"buffer_load_dwordx4 v214, %[srdk], s48 offen offset:3072 lds\n" \
"s_add_u32 m0, %[m0b], 65536\n" \
"s_nop 0\n" \
"s_nop 0\n" \
"buffer_load_dwordx4 %[vd0], %[srdv], s48 offen offset:0 lds\n" \
"buffer_load_dwordx4 v215, %[srdv], s48 offen offset:1024 lds\n" \
"buffer_load_dwordx4 %[vd0], %[srdv], s48 offen offset:2048 lds\n" \
"buffer_load_dwordx4 v215, %[srdv], s48 offen offset:3072 lds\n" \
"s_mov_b32 s48, 32768\n" \
"s_mov_b32 m0, s42\n" \
"s_nop 0\n" \
"buffer_load_dwordx4 %[kd0], %[srdk], s48 offen offset:0 lds\n" \
"buffer_load_dwordx4 v212, %[srdk], s48 offen offset:1024 lds\n" \
"buffer_load_dwordx4 v213, %[srdk], s48 offen offset:2048 lds\n" \
"buffer_load_dwordx4 v214, %[srdk], s48 offen offset:3072 lds\n" \
"s_add_u32 m0, %[m0b], 81920\n" \
"s_nop 0\n" \
"s_nop 0\n" \
"buffer_load_dwordx4 %[vd0], %[srdv], s48 offen offset:0 lds\n" \
"buffer_load_dwordx4 v215, %[srdv], s48 offen offset:1024 lds\n" \
"buffer_load_dwordx4 %[vd0], %[srdv], s48 offen offset:2048 lds\n" \
"buffer_load_dwordx4 v215, %[srdv], s48 offen offset:3072 lds\n" \
"s_mov_b32 s48, 49152\n" \
"s_mov_b32 m0, s43\n" \
"s_nop 0\n" \
"buffer_load_dwordx4 %[kd0], %[srdk], s48 offen offset:0 lds\n" \
"buffer_load_dwordx4 v212, %[srdk], s48 offen offset:1024 lds\n" \
"buffer_load_dwordx4 v213, %[srdk], s48 offen offset:2048 lds\n" \
"buffer_load_dwordx4 v214, %[srdk], s48 offen offset:3072 lds\n" \
"s_add_u32 m0, %[m0b], 98304\n" \
"s_nop 0\n" \
"s_nop 0\n" \
"buffer_load_dwordx4 %[vd0], %[srdv], s48 offen offset:0 lds\n" \
"buffer_load_dwordx4 v215, %[srdv], s48 offen offset:1024 lds\n" \
"buffer_load_dwordx4 %[vd0], %[srdv], s48 offen offset:2048 lds\n" \
"buffer_load_dwordx4 v215, %[srdv], s48 offen offset:3072 lds\n" \
"s_waitcnt vmcnt(0)\n" \
"s_barrier\n" \
"ds_read_b128 a[64:67], %[kb0] offset:0\n" \
"ds_read_b128 a[68:71], v208 offset:0\n" \
"ds_read_b128 a[72:75], v209 offset:0\n" \
"ds_read_b128 a[76:79], v210 offset:0\n" \
"ds_read_b128 a[92:95], v210 offset:4096\n" \
"ds_read_b128 a[88:91], v209 offset:4096\n" \
"ds_read_b128 a[84:87], v208 offset:4096\n" \
"ds_read_b128 a[80:83], %[kb0] offset:4096\n" \
"s_mov_b32 s48, 65536\n" \
"s_mov_b32 s60, 0\n" \
"s_waitcnt lgkmcnt(0)\n" \
"v_mfma_f32_16x16x32_bf16 v[128:131], a[64:67], a[0:3], 0\n" \
"ds_read_b128 a[96:99], %[kb0] offset:8192\n" \
"v_mfma_f32_16x16x32_bf16 v[128:131], a[68:71], a[4:7], v[128:131]\n" \
"ds_read_b128 a[100:103], v208 offset:8192\n" \
"v_mfma_f32_16x16x32_bf16 v[128:131], a[72:75], a[8:11], v[128:131]\n" \
"ds_read_b128 a[104:107], v209 offset:8192\n" \
"v_mfma_f32_16x16x32_bf16 v[128:131], a[76:79], a[12:15], v[128:131]\n" \
"ds_read_b128 a[108:111], v210 offset:8192\n" \
"v_mfma_f32_16x16x32_bf16 v[132:135], a[92:95], a[12:15], 0\n" \
"ds_read_b128 a[124:127], v210 offset:12288\n" \
"v_mfma_f32_16x16x32_bf16 v[132:135], a[88:91], a[8:11], v[132:135]\n" \
"ds_read_b128 a[120:123], v209 offset:12288\n" \
"v_mfma_f32_16x16x32_bf16 v[132:135], a[84:87], a[4:7], v[132:135]\n" \
"ds_read_b128 a[116:119], v208 offset:12288\n" \
"v_mfma_f32_16x16x32_bf16 v[132:135], a[80:83], a[0:3], v[132:135]\n" \
"ds_read_b128 a[112:115], %[kb0] offset:12288\n" \
"v_mfma_f32_16x16x32_bf16 v[136:139], a[64:67], a[16:19], 0\n" \
"ds_read_b64_tr_b16 a[128:129], %[vvb] offset:0\n" \
"v_mfma_f32_16x16x32_bf16 v[136:139], a[68:71], a[20:23], v[136:139]\n" \
"ds_read_b64_tr_b16 a[130:131], %[vvb] offset:4096\n" \
"v_mfma_f32_16x16x32_bf16 v[136:139], a[72:75], a[24:27], v[136:139]\n" \
"ds_read_b64_tr_b16 a[132:133], v211 offset:0\n" \
"v_mfma_f32_16x16x32_bf16 v[136:139], a[76:79], a[28:31], v[136:139]\n" \
"ds_read_b64_tr_b16 a[134:135], v211 offset:4096\n" \
"v_mfma_f32_16x16x32_bf16 v[140:143], a[92:95], a[28:31], 0\n" \
"ds_read_b64_tr_b16 a[136:137], %[vvb] offset:512\n" \
"v_mfma_f32_16x16x32_bf16 v[140:143], a[88:91], a[24:27], v[140:143]\n" \
"ds_read_b64_tr_b16 a[138:139], %[vvb] offset:4608\n" \
"v_mfma_f32_16x16x32_bf16 v[140:143], a[84:87], a[20:23], v[140:143]\n" \
"ds_read_b64_tr_b16 a[140:141], v211 offset:512\n" \
"v_mfma_f32_16x16x32_bf16 v[140:143], a[80:83], a[16:19], v[140:143]\n" \
"ds_read_b64_tr_b16 a[142:143], v211 offset:4608\n" \
"v_mfma_f32_16x16x32_bf16 v[144:147], a[64:67], a[32:35], 0\n" \
"ds_read_b64_tr_b16 a[144:145], %[vvb] offset:1024\n" \
"v_mfma_f32_16x16x32_bf16 v[144:147], a[68:71], a[36:39], v[144:147]\n" \
"ds_read_b64_tr_b16 a[146:147], %[vvb] offset:5120\n" \
"v_mfma_f32_16x16x32_bf16 v[144:147], a[72:75], a[40:43], v[144:147]\n" \
"ds_read_b64_tr_b16 a[148:149], v211 offset:1024\n" \
"v_mfma_f32_16x16x32_bf16 v[144:147], a[76:79], a[44:47], v[144:147]\n" \
"ds_read_b64_tr_b16 a[150:151], v211 offset:5120\n" \
"v_mfma_f32_16x16x32_bf16 v[148:151], a[92:95], a[44:47], 0\n" \
"ds_read_b64_tr_b16 a[152:153], %[vvb] offset:1536\n" \
"v_mfma_f32_16x16x32_bf16 v[148:151], a[88:91], a[40:43], v[148:151]\n" \
"ds_read_b64_tr_b16 a[154:155], %[vvb] offset:5632\n" \
"v_mfma_f32_16x16x32_bf16 v[148:151], a[84:87], a[36:39], v[148:151]\n" \
"ds_read_b64_tr_b16 a[156:157], v211 offset:1536\n" \
"v_mfma_f32_16x16x32_bf16 v[148:151], a[80:83], a[32:35], v[148:151]\n" \
"ds_read_b64_tr_b16 a[158:159], v211 offset:5632\n" \
"v_mfma_f32_16x16x32_bf16 v[152:155], a[64:67], a[48:51], 0\n" \
"v_mfma_f32_16x16x32_bf16 v[152:155], a[68:71], a[52:55], v[152:155]\n" \
"v_mfma_f32_16x16x32_bf16 v[152:155], a[72:75], a[56:59], v[152:155]\n" \
"v_mfma_f32_16x16x32_bf16 v[152:155], a[76:79], a[60:63], v[152:155]\n" \
"v_mfma_f32_16x16x32_bf16 v[156:159], a[92:95], a[60:63], 0\n" \
"v_mfma_f32_16x16x32_bf16 v[156:159], a[88:91], a[56:59], v[156:159]\n" \
"v_mfma_f32_16x16x32_bf16 v[156:159], a[84:87], a[52:55], v[156:159]\n" \
"v_mfma_f32_16x16x32_bf16 v[156:159], a[80:83], a[48:51], v[156:159]\n" \
"s_branch Lhavem_%=\n" \
"s_nop 0\n" \
".p2align 12\n" \
"adaln_pg0:\n" \
"s_cbranch_execz adaln_pg1\n" \
"Lend_%=:\n" \
""

#define SRD_WORD3 0x00020000
DI void top2_merge(unsigned& a1, unsigned& a2, unsigned b1, unsigned b2) {
  const unsigned n1 = max(a1, b1), n2 = max(min(a1, b1), max(a2, b2)); a1 = n1; a2 = n2;
}
template <int CTRL> DI void top2_dpp(unsigned& k1, unsigned& k2) {
  const unsigned p1 = __builtin_amdgcn_update_dpp(0u, k1, CTRL, 0xf, 0xf, true), p2 = __builtin_amdgcn_update_dpp(0u, k2, CTRL, 0xf, 0xf, true); top2_merge(k1, k2, p1, p2);
}
DI float row_sum(float v) {
  v = dpp_add<DPP_XOR1>(v); v = dpp_add<DPP_XOR2>(v); v = dpp_add<DPP_ROR8>(v);
  v = swap16_add(v, v); return swap32_add(v, v);
}
#define PAGE(n, nx) asm volatile("s_branch adaln_pe" #n "\n.p2align 12\nadaln_pg" #n ":\n\ts_cbranch_execz " nx "\nadaln_pe" #n ":")
#define STR_(x) #x
#define STR(x) STR_(x)
constexpr int MOE_WGS = DFF / 16;
constexpr unsigned RETRY_CAP = 4096;
#define ASLEEP 2
#define KLOOK 6
#define FSLEEP 14
#define KSLEEP 3
namespace fa {
constexpr unsigned LDS_BYTES = 163840;
constexpr float QSCALE = 0.12751743082f;
struct Args { const u16 *x, *wmod, *wq, *kc, *vc, *wo, *wg, *wu, *wd, *c, *wr; char* ws; u16* out;
};
__global__ __launch_bounds__(256, 1) void fattn_k(const Args p) {
  extern __shared__ __attribute__((aligned(16))) char lds[];
  const unsigned wid = __builtin_amdgcn_readfirstlane(threadIdx.x >> 6);
  const unsigned b = blockIdx.x, idx = b >> 3, kg = b & 3u, m = idx & 7u, hd = 8u * kg + m, y = (b & 4u) + (idx >> 3);
  const unsigned ldsb = (unsigned)(size_t)lds;
  const unsigned m0b = ldsb + 4096u * wid, m0t = ldsb + 131072u + 8192u * wid;
  const __amdgpu_buffer_rsrc_t srdz = __builtin_amdgcn_make_buffer_rsrc((void*)p.ws, (short)0, (int)WS_END, SRD_WORD3);
  const bool moe = b < MOE_WGS;
  const unsigned sl = 4u * ((b & 7u) * 7u + (idx >> 2)) + (idx & 3u), yp2 = (y & 1u) * 2u;
  unsigned hx = (y >> 2) * 8192u;
  unsigned cx = 0, sin = (unsigned)WS_XIN + (((y & 3u) * 32u + hd) * 4u + wid) * BOX + (hx ^ ((~kg & 1u) * 8192u));
  unsigned sfl = (unsigned)WS_OFL + (y & 3u) * 32768u + hx + kg * 128u, spart = (unsigned)WS_PART + (b >> 1) * 16384u + ((wid >> 1) ^ 1u) * 8192u + (b & 1u) * 4096u + (wid & 1u) * 2048u;
  unsigned fl, sd0, got, got2; unsigned long long tag; u16* pout;
  f32x2 xf[16]; f32x4 gaf[8], aw[8]; u32x4 alo, ahi; unsigned late; float arow;
#define FQ(i)
#define TQ(i)
  {
    const unsigned tid = threadIdx.x, lane = tid & 63u, l16 = lane & 15u, g = lane >> 4, hi = lane >> 5, r8 = (lane >> 2) & 7u;
    unsigned kb0 = ldsb + 256u * l16 + 16u * (g ^ l16);
    unsigned vvb = ldsb + 65536u + 2048u * hi + 288u * (g & 1u) + 64u * ((lane >> 2) & 3u) + 8u * (lane & 3u);
    unsigned vwb = ldsb + 65536u + 2048u * g + 64u * ((lane >> 2) & 3u) + 16u * (lane & 3u);
    unsigned kd0 = 4096u * wid + 256u * g + 16u * (l16 ^ g);
    unsigned vd0 = 4096u * wid + 256u * r8 + 64u * hi + 16u * ((lane & 3u) ^ (2u * (r8 >> 2))) + 1024u;
    unsigned wd0 = 8192u * (16u * wid + r8) + 64u * hi + 16u * (lane & 3u) + 4096u;
    unsigned hq0 = (unsigned)WS_H + (16u * y + 4u * wid) * 32768u + 16u * lane;
    unsigned oq0 = (unsigned)WS_XIN + (((y & 3u) * 32u + kg * 8u) * 4u + wid) * BOX + (hx ^ ((~kg & 1u) * 8192u)) + 32768u + 16u * lane;
    unsigned flag = ldsb + 49152u + 4u * wid, wbytes = moe ? DM * DFF * 2 : 0;
    const __amdgpu_buffer_rsrc_t srdw = __builtin_amdgcn_make_buffer_rsrc((void*)((const char*)p.wq + 256u * hd - 4096), (short)0, (int)(DM * DQ * 2 - 256u * hd + 4096u), SRD_WORD3);
    __amdgpu_buffer_rsrc_t srdk = __builtin_amdgcn_make_buffer_rsrc((void*)((const char*)p.kc + (size_t)hd * (SEQ * 256)), (short)0, SEQ * 256, SRD_WORD3);
    __amdgpu_buffer_rsrc_t srdv = __builtin_amdgcn_make_buffer_rsrc((void*)((const char*)p.vc + (size_t)hd * (SEQ * 256) - 17408), (short)0, SEQ * 256 + 17408, SRD_WORD3);
    unsigned long long pct; unsigned clen;
    asm volatile("s_getpc_b64 %0\n\ts_mov_b32 %1, adaln_text_end@rel32@lo+4" : "=s"(pct), "=s"(clen));
    const unsigned vct = min(((idx * 4u + wid) * (PPAR == 2 ? 8u : 6u) + min(lane, PPAR == 2 ? 7u : 5u)) * 128u, (clen - 4u) & ~3u);
      const unsigned gr = 128u * (b & 7u) + 4u * (tid >> 3), gc = 64u * (b >> 3) + 8u * (tid & 7u);
      u32x4 w[4], xs[4]; u32x2 ep, cv;
#define XV(i) ({ u32x4 v_; asm volatile("v_accvgpr_read_b32 %0, a[%4]\n\tv_accvgpr_read_b32 %1, a[%4 + 1]\n\tv_accvgpr_read_b32 %2, a[%4 + 2]\n\tv_accvgpr_read_b32 %3, a[%4 + 3]" : "=v"(v_[0]), "=v"(v_[1]), "=v"(v_[2]), "=v"(v_[3]) : "n"(200 + 4 * (i))); v_; })
      asm volatile("global_load_dwordx2 %0, %1, %2" : "=v"(cv) : "v"(gr * 2u), "s"(p.c) : "memory");
      #pragma unroll
      for (int j = 0; j < 4; ++j) asm volatile("global_load_dwordx4 %0, %1, %2 nt" : "=v"(w[j]) : "v"(((gr + j) * NMOD + gc) * 2u), "s"(p.wmod) : "memory");
      #pragma unroll
      for (int q = 0; q < 4; ++q) asm volatile("global_load_dwordx4 %0, %1, %2 offset:%3 nt" : "=v"(xs[q]) : "v"((8u * (y * 32u + hd) + 2u * wid) * 2048u + 16u * lane), "s"(p.x), "n"(1024 * q) : "memory");
      asm volatile("global_load_dwordx2 %0, %1, %2 sc1" : "=v"(ep) : "v"(0u), "s"(p.ws + WS_EP) : "memory");
      asm volatile("buffer_store_dwordx4 %0, %1, %2, 0 offen sc1\n\ts_nop 1" :: "v"(u32x4{0u, 0u, 0u, 0u}), "v"(tid < Z_SHARE / 16u ? (unsigned)WS_Z + b * Z_SHARE + tid * 16u : 0x80000000u), "s"(srdz) : "memory");
      FQ(6);
      asm volatile("global_load_dword a255, %0, %1" :: "v"(vct), "s"(pct) : "memory");
      asm volatile("s_mov_b64 exec, 0\n\ts_cmp_eq_u32 %0, 1\n\ts_cbranch_scc1 adaln_pg5\n\ts_cmp_eq_u32 %0, 2\n\ts_cbranch_scc1 adaln_pg6\n\ts_cmp_eq_u32 %0, 3\n\ts_cbranch_scc1 adaln_pg7\nadaln_fr:\n\ts_mov_b64 exec, -1" :: "s"(wid) : "scc");
      FQ(7);
      unsigned tq0 = (idx * 8u + wid * 2u + ((lane >> 3) & 1u)) * 8192u + (b & 4u) * 256u + (lane & 7u) * 128u + 4096u - 256u * m;
      const unsigned rb0 = 16u * y + 4u * (hd >> 3), ch = 16u * (hd & 7u) + (tid >> 4);
      unsigned vrw = ldsb + 135168u + wid * 256u + 32u * (tid & 7u) + 8u * g, vrr = ldsb + 135168u + 4u * lane;
      unsigned vmo = wid ? 0x80000000u : (unsigned)WS_MOD + b * 512u + lane * 8u, vst = (lane & 31u) ? 0x80000000u : (unsigned)WS_RST + (8u * (y * 32u + hd) + 2u * wid + hi) * 16u;
      unsigned vx2 = (rb0 * 16u + l16) * 2048u + ch * 16u;
      pout = p.out;
      asm volatile("s_waitcnt vmcnt(11)" : "+v"(cv), "+v"(tq0), "+v"(vrw), "+v"(vrr), "+v"(vmo), "+v"(vst), "+v"(vx2), "+s"(pout) :: "memory");
      auto silu2 = [](unsigned v) { const f32x2 f = unpk(v); return cvtpk(f[0] * __builtin_amdgcn_rcpf(1.f + __builtin_amdgcn_exp2f(f[0] * -1.44269504089f)), f[1] * __builtin_amdgcn_rcpf(1.f + __builtin_amdgcn_exp2f(f[1] * -1.44269504089f))); };
      auto dot2 = [](unsigned x, unsigned z, float c) { return __builtin_amdgcn_fdot2_f32_bf16(__builtin_bit_cast(bf16x2, x), __builtin_bit_cast(bf16x2, z), c, false); };
      unsigned s01 = silu2(cv[0]), s23 = silu2(cv[1]);
      asm volatile("s_waitcnt vmcnt(7)" : "+v"(s01), "+v"(s23), "+v"(w[0]), "+v"(w[1]), "+v"(w[2]), "+v"(w[3]) :: "memory");
      FQ(4);
      asm volatile("buffer_load_dword a255, %0, %1, 0 offen" :: "v"(tq0), "s"(srdw) : "memory");
      {
        float acc[8];
        #pragma unroll
        for (int e = 0; e < 4; ++e) {
          acc[2 * e]     = dot2(__builtin_amdgcn_perm(w[3][e], w[2][e], 0x05040100u), s23, dot2(__builtin_amdgcn_perm(w[1][e], w[0][e], 0x05040100u), s01, 0.f));
          acc[2 * e + 1] = dot2(__builtin_amdgcn_perm(w[3][e], w[2][e], 0x07060302u), s23, dot2(__builtin_amdgcn_perm(w[1][e], w[0][e], 0x07060302u), s01, 0.f));
        }
        float t[4], u[2];
        #pragma unroll
        for (int e = 0; e < 4; ++e) t[e] = swap32_add(acc[e], acc[e + 4]);
        #pragma unroll
        for (int e = 0; e < 2; ++e) u[e] = dpp_add<DPP_ROR8>(swap16_add(t[e], t[e + 2]));
        asm volatile("ds_write_b64 %5, %6\n\ts_waitcnt vmcnt(3)" : "+v"(xs[0]), "+v"(xs[1]), "+v"(xs[2]), "+v"(xs[3]), "+v"(ep) : "v"(vrw), "v"(f32x2{u[0], u[1]}) : "memory");
      }
      tag = (((unsigned long long)(unsigned)__builtin_amdgcn_readfirstlane(ep[1]) << 32) | (unsigned)__builtin_amdgcn_readfirstlane(ep[0])) + 1ull;
      const unsigned tl = (unsigned)tag & 0xffu, th = (unsigned)(tag >> 8);
      u32x4 sg;
      {
        float t4[4] = {0.f, 0.f, 0.f, 0.f};
        #pragma unroll
        for (int e = 0; e < 16; ++e) { const unsigned xw = xs[e >> 2][e & 3]; t4[2 * (e >> 3)] = dot2(xw, 0x3f803f80u, t4[2 * (e >> 3)]); t4[2 * (e >> 3) + 1] = dot2(xw, xw, t4[2 * (e >> 3) + 1]); }
        float u = swap16_add(swap32_add(t4[0], t4[2]), swap32_add(t4[1], t4[3]));
        u = dpp_add<DPP_XOR1>(u); u = dpp_add<DPP_XOR2>(u); u = dpp_add<DPP_ROR4>(u); u = dpp_add<DPP_ROR8>(u);
        const auto r16 = __builtin_amdgcn_permlane16_swap(__float_as_uint(u), __float_as_uint(u), false, false);
        const float mean = __uint_as_float(r16[0]) * (1.f / DM), m2 = mean * mean;
        float var = fmaf(__uint_as_float(r16[1]), 1.f / DM, -m2);
        if (__builtin_expect(__any(!(m2 <= var)), 0)) {
          const f32x2 mm = {__uint_as_float(__builtin_amdgcn_readlane(__float_as_uint(mean), 0)), __uint_as_float(__builtin_amdgcn_readlane(__float_as_uint(mean), 32))};
          f32x2 sq = {0.f, 0.f};
          #pragma unroll
          for (int e = 0; e < 8; ++e) {
            const f32x2 d0 = f32x2{lo16(xs[e >> 2][e & 3]), lo16(xs[2 + (e >> 2)][e & 3])} - mm, d1 = f32x2{hi16(xs[e >> 2][e & 3]), hi16(xs[2 + (e >> 2)][e & 3])} - mm;
            sq = __builtin_elementwise_fma(d1, d1, __builtin_elementwise_fma(d0, d0, sq));
          }
          const float v0 = wave_sum(sq[0]), v1 = wave_sum(sq[1]);
          var = (hi ? v1 : v0) * (1.f / DM);
        }
        sg = u32x4{cvtpk(0.f, mean) | tl, th, cvtpk(0.f, __builtin_amdgcn_sqrtf(rbh(rbh(var) + 1e-5f))) | tl, th};
      }
      {
        f32x2 ra, rc;
        asm volatile("s_waitcnt vmcnt(2) lgkmcnt(0)\n\ts_barrier\n\tds_read2st64_b32 %0, %2 offset1:1\n\tds_read2st64_b32 %1, %2 offset0:2 offset1:3\n\t"
                     "buffer_store_dwordx4 %3, %4, %5, 0 offen sc1\n\ts_waitcnt lgkmcnt(0)" : "=&v"(ra), "=&v"(rc) : "v"(vrr), "v"(sg), "v"(vst), "s"(srdz) : "memory");
        ra += rc;
        asm volatile("buffer_store_dwordx2 %0, %1, %2, 0 offen sc1" :: "v"(u32x2{((__float_as_uint(ra[0] + ra[1]) + 0x80u) & 0xffffff00u) | tl, th}), "v"(vmo), "s"(srdz) : "memory");
      }
      FQ(5);
      asm volatile("global_load_dwordx2 a[216:217], %0, %1" :: "v"(tid * 8u), "s"(p.c) : "memory");
      #pragma unroll
      for (int i = 0; i < 4; ++i) asm volatile("global_load_dwordx4 a[%2:%2 + 3], %0, %1" :: "v"(vx2 + i * 32768u), "s"(p.x), "n"(200 + 4 * i) : "memory");
      PAGE(5, "adaln_fr");
      unsigned lane2 = lane; asm volatile("" : "+v"(lane2));
#define lane lane2
    unsigned wo0 = (32u * wid + r8) * 2048u + m * 256u + 64u * hi + 16u * ((lane & 3u) ^ (2u * (r8 >> 2)));
    __amdgpu_buffer_rsrc_t srdt = __builtin_amdgcn_make_buffer_rsrc((void*)((const char*)p.wo + (size_t)kg * (DM * DM * 2)), (short)0, DM * DM * 2, SRD_WORD3);
    __amdgpu_buffer_rsrc_t srdp = __builtin_amdgcn_make_buffer_rsrc((void*)((const char*)(y < 4u ? p.kc : p.vc) + (size_t)hd * (SEQ * 256) + (y & 3u) * 4096u), (short)0, (int)(SEQ * 256 - (y & 3u) * 4096u), SRD_WORD3);
    unsigned vg0 = (256u * wid + (lane >> 1)) * (DFF * 2u) + sl * 32u + 16u * (lane & 1u);
    unsigned vdn = (16u * sl + hi) * 2048u + 512u * wid + 16u * (lane & 31u);
    const unsigned mc0 = 2u * DM + 64u * ((idx >> 2) * 8u + (b & 7u)) + 16u * (idx & 3u);
    const unsigned md0 = (256u * wid + (lane >> 1)) * (NMOD * 2u) + mc0 * 2u + 16u * (lane & 1u);
    sd0 = ldsb + 131072u + 8192u + 512u * wid + 16u * g;
    fl = md0;
    unsigned rd0 = ldsb + 131072u + 7168u + 8192u * ((lane >> 2) & 3u) + 256u * wid + 4u * (lane & 3u);
    unsigned ta0 = m0t + 256u * g + 32u * ((lane >> 2) & 3u) + 8u * (lane & 3u);
    const unsigned c8 = (b * 4u + wid) * 8192u, cd = moe ? sl * 32768u + wid * 8192u : 0u, cw = (idx * 4u + wid) * 16384u;
    const unsigned cm = moe ? ((sl & 3u) * 4u + wid) * 64u * (DFF * 2u) + (sl >> 2) * 128u : 0u;
    unsigned vxs = BOX * (l16 >> 1) + 64u * (l16 & 1u) + 16u * g;
    const unsigned sxs = (unsigned)WS_XIN + ((y & 3u) * 128u + wid * 32u) * BOX + (hx ^ ((~wid & 1u) * 8192u)) + (m >> 1) * 1024u + kg * 256u + (m & 1u) * 128u;
    unsigned vx0 = ((256u * y + 8u * hd + 2u * wid + ((lane >> 2) & 1u)) * DM + 256u * hi + 128u * (l16 >> 3) + 16u * (g & 1u) + 4u * (lane & 3u) + 32u * yp2) * 2u;
    unsigned tlo = 0, thi = 0;
#define tab(ln, lo, hi2) asm("v_writelane_b32 %0, %2, " #ln "\n\tv_writelane_b32 %1, %3, " #ln : "+v"(tlo), "+v"(thi) : "s"(lo), "s"(hi2))
#define tabp(ln, q, off) do { const unsigned long long a_ = (size_t)(q) + (off); tab(ln, (unsigned)a_, (unsigned)(a_ >> 32)); } while (0)
    tabp(1, p.x, ((y * 32u + hd) * 2u + (wid & 1u)) * 8192u); tabp(2, p.wu, moe ? c8 : 0u); tabp(3, p.wo, c8); tabp(4, p.wd, cd);
    tabp(5, p.wo, kg * 2097152u + cw); tabp(6, p.wo, kg * 2097152u + cw + 8192u); tabp(7, p.wg, cm);
    tabp(17, p.wmod, 0u); tab(18, sin + 32768u, sfl);
    tab(19, (unsigned)WS_OFL + (y & 3u) * 32768u + (hd >> 4) * 16384u + hx + (hd & 15u) * 512u + wid * 128u, 0u);
    tabp(8, p.wg, 0u); tabp(9, p.wu, 0u); tabp(10, p.wd, 0u);
    tab(11, m, m * 4u * BOX); tab(12, (y & 1u) * 128u, 128u - (y & 1u) * 128u);
    tab(13, b, (unsigned)WS_PFL + (y * 32u + hd) * 512u + kg * 128u); tab(14, sxs, 0u);
    tab(15, (unsigned)WS_MODT + (mc0 - 2u * DM + 4u * wid) * 4u, (mc0 >> 10) == 4u ? 0x3f800000u : 0u);
    tab(16, PPAR && (b & 1u) ? 0x80000000u : (unsigned)WS_PFL + y * 16384u + (hd * 4u + wid) * 4u, (unsigned)WS_PFL + (y * 32u + hd) * 512u + wid * 128u);
    const char* psig = p.ws + WS_CNT + y * 8192u + hd * 8u;
    const char* pcnt = p.ws + WS_CNT + y * 8192u + hd * 256u;
    tabp(20, p.wr, 0u); tab(21, spart, (unsigned)WS_ROWS + (y * 32u + hd) * 8192u + wid * 1024u);
      asm volatile("" : "+v"(tlo), "+v"(thi), "+v"(wo0), "+v"(vg0), "+v"(vdn), "+v"(ta0), "+v"(vxs), "+v"(vx0), "+v"(rd0), "+v"(fl), "+v"(sd0), "+v"(kb0), "+v"(vvb), "+v"(vwb), "+v"(kd0), "+v"(vd0), "+v"(wd0), "+v"(hq0), "+v"(oq0));
      asm volatile("" : "+s"(srdk), "+s"(srdv), "+s"(srdt), "+s"(srdp), "+s"(psig), "+s"(pcnt), "+s"(flag), "+s"(wbytes));
#undef lane
      u32x2 mp[8]; u32x4 rs;
      unsigned vlm = ((wid >> 1) * 16u + 2u * (hd & 7u) + (wid & 1u)) * 4096u + lane * 8u, vlr = (unsigned)WS_RST + ((rb0 + wid) * 16u + l16) * 16u;
#define LOOK9 "buffer_load_dwordx2 %0, %[vlm], %[srdz], %[som] offen sc1\n\tbuffer_load_dwordx2 %1, %[vlm], %[srdz], %[som] offen offset:512 sc1\n\tbuffer_load_dwordx2 %2, %[vlm], %[srdz], %[som] offen offset:1024 sc1\n\t" \
              "buffer_load_dwordx2 %3, %[vlm], %[srdz], %[som] offen offset:1536 sc1\n\tbuffer_load_dwordx2 %4, %[vlm], %[srdz], %[som] offen offset:2048 sc1\n\tbuffer_load_dwordx2 %5, %[vlm], %[srdz], %[som] offen offset:2560 sc1\n\t" \
              "buffer_load_dwordx2 %6, %[vlm], %[srdz], %[som] offen offset:3072 sc1\n\tbuffer_load_dwordx2 %7, %[vlm], %[srdz], %[som] offen offset:3584 sc1\n\tbuffer_load_dwordx4 %8, %[vlr], %[srdz], 0 offen sc1\n\t"
#define LREG(c) c "{v[0:1]}"(mp[0]), c "{v[2:3]}"(mp[1]), c "{v[4:5]}"(mp[2]), c "{v[6:7]}"(mp[3]), c "{v[8:9]}"(mp[4]), c "{v[10:11]}"(mp[5]), c "{v[12:13]}"(mp[6]), c "{v[14:15]}"(mp[7]), c "{v[16:19]}"(rs)
      asm volatile(LOOK9 : LREG("=&") : [vlm] "v"(vlm), [vlr] "v"(vlr), [srdz] "s"(srdz), [som] "s"((unsigned)WS_MOD) : "memory");
      #pragma unroll
      for (int q = 0; q < 14; ++q)
        asm volatile("s_mov_b32 m0, %0\n\ts_nop 0\n\tbuffer_load_dwordx4 %1, %2, %3 offen lds" :: "s"(m0b + 65536u + 16384u * (q >> 2) + 1024u * (q & 3)), "v"(wd0), "s"(srdw),
                     "s"(0x80000u * (q >> 2) + 65536u * ((q >> 1) & 1) + 128u * (q & 1)) : "m0", "memory");
      asm volatile("s_waitcnt vmcnt(23)" ::: "memory");
      { const u32x4 cs = XV(4); *(u32x2*)(lds + 131072 + 8192 + tid * 8u) = u32x2{silu2(cs[0]), silu2(cs[1])}; }
      f32x2 xu[16];
      #pragma unroll
      for (int i = 0; i < 4; ++i) { const u32x4 o = XV(i);
        #pragma unroll
        for (int e = 0; e < 4; ++e) { xu[4 * i + e] = unpk(o[e]); asm volatile("" : "+v"(xu[4 * i + e])); } }
      unsigned vpw = ldsb + 131072u + 4u * tid, vpr = ldsb + 131072u + 1024u + 32u * l16 + 8u * wid, vpc = ldsb + 131072u + 32u * (tid >> 4), vpq = ldsb + 131072u + 1024u + 32u * l16;
      unsigned vh = (unsigned)WS_H + rb0 * 32768u + ch * 256u + 16u * l16; float one = wid >> 1 ? 1.f : 0.f;
      {
        unsigned bl, bh, cnt = SPIN_CAP;
#define XO(d, r, t) "v_bitop3_b32 %[" #d "], v" #r ", %[" #t "], %[" #d "] bitop3:0xbe\n\t"
        asm volatile(
          "s_waitcnt vmcnt(14)\n\t"
          "s_branch Lm1_%=\n"
          "Lm0_%=:\n\t"
          "s_sleep " STR(ASLEEP) "\n\t"
          LOOK9
          "s_waitcnt vmcnt(0)\n"
          "Lm1_%=:\n\t"
          "v_bitop3_b32 %[bl], v0, v2, %[tl] bitop3:0x7e\n\t"
          "v_bitop3_b32 %[bh], v1, v3, %[th] bitop3:0x7e\n\t"
          XO(bl, 4, tl) XO(bh, 5, th) XO(bl, 6, tl) XO(bh, 7, th) XO(bl, 8, tl) XO(bh, 9, th) XO(bl, 10, tl) XO(bh, 11, th) XO(bl, 12, tl) XO(bh, 13, th) XO(bl, 14, tl) XO(bh, 15, th)
          XO(bl, 16, tl) XO(bh, 17, th) XO(bl, 18, tl) XO(bh, 19, th)
          "v_and_or_b32 %[bl], %[bl], %[ff], %[bh]\n\t"
          "v_cmp_ne_u32 vcc, 0, %[bl]\n\t"
          "s_sub_u32 %[cnt], %[cnt], 1\n\t"
          "s_cbranch_scc1 Lm2_%=\n\t"
          "s_cbranch_vccnz Lm0_%=\n"
          "Lm2_%=:"
          : LREG("+"), [bl] "=&v"(bl), [bh] "=&v"(bh), [cnt] "+s"(cnt), "+v"(vpw), "+v"(vpr), "+v"(vpc), "+v"(vpq), "+v"(vh), "+v"(one)
          : [vlm] "v"(vlm), [vlr] "v"(vlr), [srdz] "s"(srdz), [som] "s"((unsigned)WS_MOD), [tl] "s"(tl), [th] "s"(th), [ff] "v"(0xffu) : "vcc", "scc", "memory");
      }
      f32x4 a0, a1, c0, c1, t0, t1;
      {
        auto val = [&](int k) { return __uint_as_float(mp[k][0] & 0xffffff00u); };
        const float v = rbh(one + rbh(((val(0) + val(1)) + (val(2) + val(3))) + ((val(4) + val(5)) + (val(6) + val(7)))));
        const float rd = __builtin_amdgcn_rcpf(hi16(rs[2]));
        asm volatile("ds_write_b32 %0, %1\n\tds_write_b64 %2, %3" :: "v"(vpw), "v"(v * QSCALE), "v"(vpr), "v"(f32x2{-hi16(rs[0]) * rd, rd}) : "memory");
      }
      PAGE(6, "adaln_fr");
      asm volatile("s_waitcnt lgkmcnt(0)\n\ts_barrier\n\tds_read_b128 %4, %7\n\tds_read_b128 %0, %6\n\tds_read_b128 %2, %6 offset:512\n\tds_read_b128 %1, %6 offset:16\n\tds_read_b128 %3, %6 offset:528\n\tds_read_b128 %5, %7 offset:16\n\t"
                   "buffer_load_dword a255, %8, %9, %10 offen\n\ts_waitcnt lgkmcnt(0)"
                   : "=&v"(a0), "=&v"(a1), "=&v"(c0), "=&v"(c1), "=&v"(t0), "=&v"(t1) : "v"(vpc), "v"(vpq), "v"(tq0), "s"(srdw), "s"(0x200000u) : "memory");
      FQ(0);
      #pragma unroll
      for (int i = 0; i < 4; ++i) {
        const float mn = i < 2 ? t0[2 * i] : t1[2 * i - 4], rd = i < 2 ? t0[2 * i + 1] : t1[2 * i - 3];
        u32x4 o;
        #pragma unroll
        for (int e = 0; e < 4; ++e) {
          const f32x2 sc = e < 2 ? f32x2{c0[2 * e], c0[2 * e + 1]} : f32x2{c1[2 * e - 4], c1[2 * e - 3]}, sh = e < 2 ? f32x2{a0[2 * e], a0[2 * e + 1]} : f32x2{a1[2 * e - 4], a1[2 * e - 3]};
          o[e] = cvtpk2(__builtin_elementwise_fma(__builtin_elementwise_fma(xu[4 * i + e], f32x2{rd, rd}, f32x2{mn, mn}), sc, sh));
        }
        asm volatile("buffer_store_dwordx4 %0, %1, %2, %3 offen sc1\n\ts_nop 1" :: "v"(o), "v"(vh), "s"(srdz), "s"(32768u * i) : "memory");
      }
      FQ(1);
    asm volatile(FA_BODY
      : FA_OUTS, [sin] "+s"(sin), [oq0] "+v"(oq0), [tlo] "+v"(tlo), [thi] "+v"(thi), [cx] "+s"(cx)
      : [kb0] "v"(kb0), [vvb] "v"(vvb), [kd0] "v"(kd0), [vd0] "v"(vd0),
        [m0b] "s"(m0b), [flag] "s"(flag), [srdz] "s"(srdz), [srdw] "s"(srdw), [srdk] "s"(srdk), [srdv] "s"(srdv), [m0t] "s"(m0t), [srdt] "s"(srdt), [srdp] "s"(srdp),
        [tag] "s"(tag), [pcnt] "s"(pcnt), [psig] "s"(psig), [wid] "s"(wid), [rd0] "v"(rd0), [wo0] "v"(wo0), [vg0] "v"(vg0), [vx0] "v"(vx0), [px] "s"(p.x), [vct] "v"(vct), [pct] "s"(pct), [vdn] "v"(vdn), [ta0] "v"(ta0), [vxs] "v"(vxs),
        [wbytes] "s"(wbytes)
      : FA_CLOBBERS);
    cx = __builtin_amdgcn_readfirstlane(cx); hx ^= cx; sfl ^= cx; spart ^= cx;
  }
#define RDELAYN 16
#define RDELAY2 4
#define RDELAY3 8
#define RCHKN 5
  unsigned lane; asm volatile("v_mbcnt_lo_u32_b32 %0, -1, 0\n\tv_mbcnt_hi_u32_b32 %0, -1, %0" : "=v"(lane));
  const unsigned tid = wid * 64u + lane, n = lane & 15u, g = lane >> 4;
  const unsigned r0 = 256u * y + 8u * hd + 2u * wid, row = r0 + ((n >> 2) & 1u);
  const unsigned colb = 256u * (g >> 1) + 128u * (n >> 3) + 16u * (g & 1u) + 4u * (n & 3u);
  char* par = lds;
  const unsigned cq0 = colb + 32u * yp2, cq1 = colb + 32u * (2u ^ yp2);
  auto parq = [&](int o, int k) { return *(const u32x2*)(par + o + (k >> 2 ? cq1 : cq0) * 2u + 64 * ((k >> 1) & 1) + 1024 * (k & 1)); };
  auto parf = [&](int o, int k) { return *(const f32x4*)(par + o + (k >> 2 ? cq1 : cq0) * 4u + 128 * ((k >> 1) & 1) + 2048 * (k & 1)); };
  if (__builtin_expect(__any(late != 0u), 0)) {
    u32x4 mt[4]; u32x2 wr;
    asm volatile("s_barrier" ::: "memory");
    for (unsigned spins = 0; spins <= SPIN_CAP; ++spins) {
      asm volatile("s_nop 4\n\tglobal_load_dwordx2 %0, %1, %2" : "=v"(wr) : "v"(tid * 8u), "s"(p.wr) : "memory");
      #pragma unroll
      for (int k = 0; k < 4; ++k) asm volatile("buffer_load_dwordx4 %0, %1, %2, %3 offen sc1" : "=v"(mt[k]) : "v"(tid * 16u), "s"(srdz), "s"((unsigned)WS_MODT + 4096u * k) : "memory");
      asm volatile("s_waitcnt vmcnt(0)" : "+v"(mt[0]), "+v"(mt[1]), "+v"(mt[2]), "+v"(mt[3]), "+v"(wr) :: "memory");
      const u32x4 t = mt[0] & mt[1] & mt[2] & mt[3];
      if (__all((t[0] & t[1] & t[2] & t[3] & 1u) != 0u)) break;
    }
    const f32x4 a = __builtin_bit_cast(f32x4, mt[2]) * f32x4{lo16(wr[0]), hi16(wr[0]), lo16(wr[1]), hi16(wr[1])};
    *(u32x4*)(par + tid * 16u) = mt[0]; *(f32x4*)(par + 4096 + tid * 16u) = a;
    #pragma unroll
    for (int k = 1; k < 4; ++k) *(u32x2*)(par + (k == 3 ? 14336 : 6144 + 2048 * k) + tid * 8u) = u32x2{__builtin_amdgcn_perm(mt[k][1], mt[k][0], 0x07060302u), __builtin_amdgcn_perm(mt[k][3], mt[k][2], 0x07060302u)};
    *(float*)(par + 12424 + 16u * wid) = wave_sum((a[0] + a[1]) + (a[2] + a[3]));
    asm volatile("s_waitcnt lgkmcnt(0)\n\ts_barrier" ::: "memory");
    #pragma unroll
    for (int k = 0; k < 8; ++k) { gaf[k] = parf(0, k); aw[k] = parf(4096, k); }
    arow = dpp_add<DPP_XOR2>(dpp_add<DPP_XOR1>(*(const float*)(par + 12424 + 16u * (lane & 3u))));
  }
  unsigned vin = lane * 16u, vin3 = lane * 16u + 20480u, vin2 = lane * 16u + 16384u, kp = (unsigned)WS_KEYS + (b & 3u) * 2048u + hx + 32u * lane;
  TQ(0);
  f32x2 x1[16], s2, d2, q2, sbf[16], gbf[16], c1f[16];
  float mrow, rdrow, drow;
  unsigned vkey, vkb, vkr, nrow, vpub, ta, tu, tb; char* pk;
  {
#define H2(v, h) f32x2{v[2 * h], v[2 * h + 1]}
#define PART(R, c) do { f32x4 t0, t1; \
      asm volatile("v_mfma_f32_16x16x32_bf16 %0, %2, a[" #R ":" #R "+3], 0\n\tv_mfma_f32_16x16x32_bf16 %1, %2, a[" #R "+8:" #R "+11], 0\n\t" \
                   "v_mfma_f32_16x16x32_bf16 %0, %3, a[" #R "+4:" #R "+7], %0\n\tv_mfma_f32_16x16x32_bf16 %1, %3, a[" #R "+12:" #R "+15], %1\n\ts_nop 6" : "=&v"(t0), "=&v"(t1) : "v"(alo), "v"(ahi)); \
      f32x2 u0 = H2(t0, 0), u1 = H2(t0, 1), u2 = H2(t1, 0), u3 = H2(t1, 1); \
      asm volatile("v_pk_fma_f32 %0, %0, %4, %8\n\tv_pk_fma_f32 %1, %1, %5, %9\n\tv_pk_fma_f32 %2, %2, %6, %10\n\tv_pk_fma_f32 %3, %3, %7, %11" : "+v"(u0), "+v"(u1), "+v"(u2), "+v"(u3) \
                   : "v"(H2(gaf[2 * c], 0)), "v"(H2(gaf[2 * c], 1)), "v"(H2(gaf[2 * c + 1], 0)), "v"(H2(gaf[2 * c + 1], 1)), "v"(xf[4 * c]), "v"(xf[4 * c + 1]), "v"(xf[4 * c + 2]), "v"(xf[4 * c + 3])); \
      x1[4 * c] = rb2(u0); x1[4 * c + 1] = rb2(u1); x1[4 * c + 2] = rb2(u2); x1[4 * c + 3] = rb2(u3); } while (0)
#define AC3(k) "v_pk_add_f32 %0, %0, %[y" #k "]\n\tv_pk_fma_f32 %1, %[y" #k "], %[w" #k "], %1\n\tv_pk_fma_f32 %2, %[y" #k "], %[y" #k "], %2\n\t"
    auto part_ = [&](int c2, f32x2& sa, f32x2& da, f32x2& qa) __attribute__((always_inline)) {
      if (c2 == 0) PART(64, 0); else if (c2 == 1) PART(80, 1); else if (c2 == 2) PART(96, 2); else PART(112, 3);
      asm volatile(AC3(0) AC3(1) AC3(2) AC3(3) : "+v"(sa), "+v"(da), "+v"(qa) : [y0] "v"(x1[4 * c2]), [y1] "v"(x1[4 * c2 + 1]), [y2] "v"(x1[4 * c2 + 2]), [y3] "v"(x1[4 * c2 + 3]),
                   [w0] "v"(H2(aw[2 * c2], 0)), [w1] "v"(H2(aw[2 * c2], 1)), [w2] "v"(H2(aw[2 * c2 + 1], 0)), [w3] "v"(H2(aw[2 * c2 + 1], 1)));
    };
#define RCV4(o, a, b, c, d) \
      "buffer_load_dwordx4 " #a ", %[vin" #o "], %[srdz], %[sin] offen sc1\n\t" \
      "buffer_load_dwordx4 " #b ", %[vin" #o "], %[srdz], %[sin] offen offset:1024 sc1\n\t" \
      "buffer_load_dwordx4 " #c ", %[vin" #o "], %[srdz], %[sin] offen offset:2048 sc1\n\t" \
      "buffer_load_dwordx4 " #d ", %[vin" #o "], %[srdz], %[sin] offen offset:3072 sc1\n\t"
#define RWAIT(lv, w) \
      "Lrq" #lv "_%=:\n\t" \
      "v_mov_b32 " w ", 0\n\t" \
      "s_mov_b32 %[sc], " STR(RCHKN) "\n\t" \
      LOOK2 \
      "Lrs" #lv "_%=:\n\t" \
      "ds_read_b32 %[tmp], %[vsh]\n\t" \
      "s_waitcnt lgkmcnt(0)\n\t" \
      "v_and_or_b32 %[tmp], %[tmp], " #lv ", " w "\n\t" \
      "v_cmp_eq_u32 vcc, 0, %[tmp]\n\t" \
      "s_cbranch_vccz Lrd" #lv "_%=\n\t" \
      "s_sub_u32 %[sc], %[sc], 1\n\t" \
      "s_cbranch_scc0 Lrs" #lv "_%=\n\t" \
      "s_sub_u32 %[cnt], %[cnt], 1\n\t" \
      "s_cselect_b32 %[cnt], 0, %[cnt]\n\t" \
      "s_waitcnt vmcnt(0)\n\t" \
      "v_cmp_eq_u32 vcc, 0, " w "\n\t" \
      "s_cbranch_scc1 Lrl" #lv "_%=\n\t" \
      "s_cbranch_vccnz Lrq" #lv "_%=\n" \
      "Lrd" #lv "_%=:\n\t" \
      "v_mov_b32 %[tmp], " #lv "\n\t" \
      "s_mov_b64 exec, 1\n\t" \
      "ds_or_b32 %[vsh], %[tmp]\n\t" \
      "s_mov_b64 exec, -1\n" \
      "Lrl" #lv "_%=:\n\t"
#define RCV8A RCV4(0, a[64:67], a[68:71], a[72:75], a[76:79]) RCV4(1, a[80:83], a[84:87], a[88:91], a[92:95])
#define RCV4C RCV4(2, a[96:99], a[100:103], a[104:107], a[108:111])
#define RCV4D RCV4(3, a[112:115], a[116:119], a[120:123], a[124:127])
#define RCV8B RCV4C RCV4D
#define LOOK2 "buffer_load_dwordx2 v[228:229], %[vfl], %[srdz], %[sfl] offen offset:64 sc1\n\t"
    unsigned cnt, tmp, sc;
    u32x2 sbq[8], gbq[8], c1q[8];
    #pragma unroll
    for (int k = 0; k < 8; ++k) { sbq[k] = parq(8192, k); c1q[k] = parq(10240, k); gbq[k] = parq(14336, k); }
    #pragma unroll
    for (int i = 0; i < 16; ++i) { sbf[i] = unpk(sbq[i >> 1][i & 1]); c1f[i] = unpk(c1q[i >> 1][i & 1]); asm volatile("" : "+v"(sbf[i]), "+v"(c1f[i])); }
    unsigned vfl = (lane & 15u) * 512u + (lane & 16u) * 1024u, vin1 = lane * 16u + 4096u;
    PAGE(1, "adaln_pg2");
    asm volatile(
      "s_mov_b32 %[go], 0\n\t"
      "s_cmp_lg_u32 %[got], 0\n\t"
      "s_waitcnt vmcnt(4)\n\t"
      "s_cbranch_scc1 Lrf_%=\n\t"
      "v_cmp_eq_u32 vcc, 0, v229\n\t"
      "s_cbranch_vccnz Lrk_%=\n\t"
      "s_mov_b32 %[got], 2\n\t"
      RCV8A
      "Lrf_%=:\n\t"
      "v_cmp_eq_u32 vcc, 0, v228\n\t"
      "s_cbranch_vccnz Lrk_%=\n\t"
      "s_mov_b32 %[go], 1\n\t"
      RCV4C
      "s_branch Lrn_%=\n"
      "Lrk_%=:\n\t"
      LOOK2
      "Lrn_%=:"
      : "+{v228}"(sd0), "+{v229}"(fl), [got] "+s"(got), [go] "=&s"(got2), [vfl] "+v"(vfl), [vin1] "+v"(vin1), "+v"(gbq[0]), "+v"(gbq[1]), "+v"(gbq[2]), "+v"(gbq[3]), "+v"(gbq[4]), "+v"(gbq[5]), "+v"(gbq[6]), "+v"(gbq[7])
      : [srdz] "s"(srdz), [sfl] "s"(sfl), [sin] "s"(sin), [vin0] "v"(vin), [vin2] "v"(vin2) : "vcc", "scc", "memory");
#define NRE
    #pragma unroll
    for (int i = 0; i < 16; ++i) { gbf[i] = unpk(gbq[i >> 1][i & 1]); asm volatile("" : "+v"(gbf[i])); }
    vkey = lane * 2048u + b * 8u; vkb = ldsb + 12288u + 8u * wid + (lane & 4u); vkr = ldsb + 12288u + 4u * (lane & 7u); nrow = 2047u - row;
    vpub = (unsigned)WS_ROWS + (y * 32u + hd) * 8192u + (g >> 1) * 512u + ((n >> 3) * 8u + (g & 1u) * 4u + (n & 3u)) * 16u;
    ta = m0b + 65536u + 8u * lane; tu = m0t + 8u * lane; tb = m0b + 98304u + 16384u * (g >> 1) + 2048u * (g & 1u) + 512u * ((lane >> 2) & 3u) + 8u * (lane & 3u);
    pk = p.ws + WS_KEYS;
    s2 = f32x2{0.f, 0.f}; d2 = s2; q2 = s2;
    f32x2 s3, d3, q3;
    asm volatile(
      "s_mov_b32 %[cnt], 0x10000\n\t"
      "s_cmp_lg_u32 %[go], 0\n\t"
      "s_cbranch_scc1 Lro_%=\n\t"
      "s_cmp_lg_u32 %[got], 0\n\t"
      "s_waitcnt vmcnt(0)\n\t"
      "s_cbranch_scc1 Lrf_%=\n\t"
      "v_cmp_eq_u32 vcc, 0, v229\n\t"
      "s_cbranch_vccz Lrl2_%=\n\t"
      RWAIT(2, "v229")
      RCV8A
      "Lrf_%=:\n\t"
      "v_cmp_eq_u32 vcc, 0, v228\n\t"
      "s_cbranch_vccz Lrm_%=\n\t"
      RWAIT(4, "v228")
      RCV4C
      "s_sleep " STR(RDELAYN) "\n\t"
      "s_branch Lrp_%=\n"
      "Lrm_%=:\n\t"
      RCV4C
      "s_sleep " STR(RDELAY3) "\n\t"
      "s_branch Lrp_%=\n"
      "Lro_%=:\n\t"
      "s_sleep " STR(RDELAY2) "\n"
      "Lrp_%=:\n\t"
      RCV4D
      "s_cmp_eq_u32 %[got], 1\n\t"
      "s_cbranch_scc1 Lrz_%=\n\t"
      "s_waitcnt vmcnt(8)\n"
      "Lrz_%=:"
      : "+{v228}"(sd0), "+{v229}"(fl), [cnt] "=&s"(cnt), [tmp] "=&v"(tmp), [sc] "=&s"(sc), "+v"(s2),
        "+v"(vkey), "+v"(vkb), "+v"(vkr), "+v"(nrow), "+v"(vpub), "+v"(ta), "+v"(tu), "+v"(tb), "+s"(pk)
      : [srdz] "s"(srdz), [got] "s"(got), [go] "s"(got2), [vfl] "v"(vfl), [sfl] "s"(sfl), [sin] "s"(sin), [vin0] "v"(vin), [vin1] "v"(vin1), [vin2] "v"(vin2), [vin3] "v"(vin3), [vsh] "v"(ldsb + 12420u)
      : "vcc", "scc", "memory");
    part_(0, s2, d2, q2); part_(1, s2, d2, q2);
    TQ(2);
    s3 = s2; d3 = d2; q3 = q2;
    asm volatile("s_waitcnt vmcnt(4)" : "+{v228}"(sd0), "+{v229}"(fl), "+v"(s2), "+v"(d2), "+v"(q2), "+v"(s3), "+v"(d3), "+v"(q3) :: "memory");
    part_(2, s3, d3, q3);
    asm volatile("s_waitcnt vmcnt(0)" : "+v"(s3), "+v"(d3), "+v"(q3) :: "memory");
    TQ(3);
    part_(3, s3, d3, q3);
    if (__builtin_expect(__any(s3[0] + s3[1] != s3[0] + s3[1]), 0)) {
      unsigned spins = 0;
      do {
        asm volatile(RCV8B "s_waitcnt vmcnt(0)" :: [srdz] "s"(srdz), [sin] "s"(sin), [vin2] "v"(vin2), [vin3] "v"(vin3) : "memory"); NRE;
        s3 = s2; d3 = d2; q3 = q2;
        part_(2, s3, d3, q3); part_(3, s3, d3, q3);
      } while (__any(s3[0] + s3[1] != s3[0] + s3[1]) && ++spins < RETRY_CAP);
    }
    asm volatile("; all in");
    const float sv = s3[0] + s3[1], dv = d3[0] + d3[1], qv = q3[0] + q3[1];
    float u = swap16_add(swap32_add(sv, dv), swap32_add(qv, qv));
    u = dpp_add<DPP_XOR1>(u); u = dpp_add<DPP_XOR2>(u); u = dpp_add<DPP_ROR8>(u);
    const auto r16 = __builtin_amdgcn_permlane16_swap(__float_as_uint(u), __float_as_uint(u), false, false);
    const auto r32 = __builtin_amdgcn_permlane32_swap(r16[0], r16[0], false, false);
    const float mean = __uint_as_float(r32[0]) * (1.f / DM), m2v = mean * mean;
    drow = __uint_as_float(r32[1]);
    float var = fmaf(__uint_as_float(r16[1]), 1.f / DM, -m2v);
    if (__builtin_expect(__any(!(m2v <= var)), 0)) {
      f32x2 qq = {0.f, 0.f};
      #pragma unroll
      for (int e = 0; e < 16; ++e) { const f32x2 t = x1[e] - mean; qq = __builtin_elementwise_fma(t, t, qq); }
      var = row_sum(qq[0] + qq[1]) * (1.f / DM);
    }
    mrow = rbh(mean); rdrow = __builtin_amdgcn_rcpf(rbh(__builtin_amdgcn_sqrtf(rbh(rbh(var) + 1e-5f))));
  }
  unsigned* kb = (unsigned*)(lds + 12288);
  unsigned mykey, k1w;
  { const unsigned bb = __float_as_uint(fmaf(-mrow, arow, drow) * rdrow); mykey = max((bb ^ ((unsigned)((int)bb >> 31) | 0x80000000u)) & 0xfffff800u, 0x800u) | nrow; }
  asm volatile("ds_write_b32 %1, %2\n\ts_waitcnt lgkmcnt(0)\n\ts_barrier\n\tds_read_b32 %0, %3\n\ts_waitcnt lgkmcnt(0)" : "=&v"(k1w) : "v"(vkb), "v"(mykey), "v"(vkr) : "memory");
  bool cand;
  u32x4 ka, kc;
  {
    const unsigned o1 = __builtin_amdgcn_update_dpp(0u, k1w, DPP_XOR1, 0xf, 0xf, true);
    unsigned k1 = max(k1w, o1), k2 = min(k1w, o1);
    top2_dpp<DPP_XOR2>(k1, k2); top2_dpp<0x141>(k1, k2);
    asm volatile("s_cmp_lg_u32 %[wid], 0\n\ts_cbranch_scc1 Lka_%=\n\ts_mov_b64 exec, %[mk]\n\tglobal_atomic_or_x2 %[va], %[kk], %[pk] sc1\n\ts_mov_b64 exec, -1\nLka_%=:"
                 :: [wid] "s"(wid), [mk] "n"((1u << KEY_REP) - 1u), [va] "v"(vkey), [kk] "v"(u32x2{k2, k1}), [pk] "s"(pk) : "scc", "memory");
    cand = mykey >= k2;
    vpub += mykey == k1 ? 0u : 2048u;
  }
  TQ(4);
  PAGE(2, "adaln_pg3");
#define TRA(r, a, o) asm volatile("ds_read_b64_tr_b16 a[%0:%0 + 1], %1 offset:%2" :: "n"(r), "v"(a), "n"(o) : "memory")
#define FRAGS(k) do { \
      TRA(128 + 8 * (k), ta, 2048 * ((k) >> 2) + 512 * ((k) & 1) + 16384 * (((k) >> 1) & 1)); TRA(130 + 8 * (k), ta, 2048 * ((k) >> 2) + 512 * ((k) & 1) + 16384 * (((k) >> 1) & 1) + 1024); \
      TRA(132 + 8 * (k), tu, 2048 * ((k) >> 2) + 512 * ((k) & 1) + 4096 * (((k) >> 1) & 1)); TRA(134 + 8 * (k), tu, 2048 * ((k) >> 2) + 512 * ((k) & 1) + 4096 * (((k) >> 1) & 1) + 1024); \
      TRA(192 + 4 * (k), tb, 64 * (k)); TRA(194 + 4 * (k), tb, 64 * (k) + 32); } while (0)
  f32x2 h2[16];
  {
    asm volatile("" : "+v"(mrow));
    const float nm = -mrow * rdrow;
    #pragma unroll
    for (int k = 0; k < 8; ++k) {
      #pragma unroll
      for (int e = 0; e < 2; ++e) { const int i = 2 * k + e; h2[i] = __builtin_elementwise_fma(__builtin_elementwise_fma(x1[i], f32x2{rdrow, rdrow}, f32x2{nm, nm}), c1f[i], sbf[i]); }
      if (k & 1) { asm volatile("" : "+v"(h2[2 * k]), "+v"(h2[2 * k + 1])); FRAGS(k >> 1); }
    }
  }
  const unsigned cw = __builtin_amdgcn_readfirstlane((unsigned)__ballot(cand) & 0x11u);
  auto publish = [&](const f32x2* v, unsigned off) __attribute__((always_inline)) {
    #pragma unroll
    for (int k = 0; k < 4; ++k) { const int q = 4 * (k >> 1) + (k & 1);
      asm volatile("buffer_store_dwordx4 %0, %1, %2, %3 offen sc1\n\ts_nop 1" :: "v"(u32x4{cvtpk2(v[2 * q]), cvtpk2(v[2 * q + 1]), cvtpk2(v[2 * q + 4]), cvtpk2(v[2 * q + 5])}), "v"(vpub), "s"(srdz), "s"(off + (k & 1) * 1024u + (((k >> 1) * 2u) ^ yp2) * 128u) : "memory"); }
  };
  if (cand) publish(h2, 0u);
  char* st = lds + 32768 + 4096u * wid;
  auto look1 = [&]() __attribute__((always_inline)) {
    asm volatile("s_cmp_lg_u32 %2, 0\n\ts_cbranch_scc1 Lkl_%=\n\tbuffer_load_dwordx4 %0, %3, %4, 0 offen sc1\n\tbuffer_load_dwordx4 %1, %3, %4, 0 offen offset:16 sc1\nLkl_%=:" : "=&{v[240:243]}"(ka), "=&{v[244:247]}"(kc) : "s"(wid), "v"(kp), "s"(srdz) : "scc", "memory");
  };
  if (KLOOK == 0) look1();
  #pragma unroll
  for (int k = 0; k < 8; ++k) {
    u32x2 yq;
    #pragma unroll
    for (int e = 0; e < 2; ++e) yq[e] = cvtpk2(__builtin_elementwise_fma(gbf[2 * k + e], h2[2 * k + e], x1[2 * k + e]));
    *(u32x2*)(st + (row & 1u) * 2048u + (colb + 32u * ((k >> 2) * 2u ^ yp2)) * 2u + 64 * ((k >> 1) & 1) + 1024 * (k & 1)) = yq;
    if (k & 1) FRAGS(4 + (k >> 1));
    if (k == KLOOK - 1) look1();
  }
  auto out_rows = [&](unsigned which) __attribute__((always_inline)) {
    #pragma unroll
    for (int r = 0; r < 2; ++r) {
      if ((which >> (4 * r)) & 1u) {
        const u32x4 a = *(const u32x4*)(st + 2048 * r + 16u * lane), c = *(const u32x4*)(st + 2048 * r + 1024 + 16u * lane);
        asm volatile("s_nop 4\n\tglobal_store_dwordx4 %0, %1, %3 offset:%4 sc1\n\tglobal_store_dwordx4 %0, %2, %3 offset:%5 sc1\n\ts_nop 1" :: "v"(r0 * 2048u + 16u * lane), "v"(a), "v"(c), "s"(pout), "n"(2048 * r), "n"(2048 * r + 1024) : "memory");
      }
    }
  };
  const unsigned un = tid & 127u, ca = 256u * (un >> 5) + 64u * ((un >> 4) & 1u) + 128u * ((un >> 3) & 1u) + 16u * ((un >> 2) & 1u) + 4u * (un & 3u);
  unsigned vrow = (unsigned)WS_ROWS + (moe ? wid * 512u + 16u * g : un * 16u), vsel = ldsb + 12288u + 40u + 4u * (moe ? lane & 1u : wid >> 1);
  unsigned mk[8];
  #pragma unroll
  for (int t = 0; t < 8; ++t) { mk[t] = (unsigned)__builtin_amdgcn_sbfe((int)(1u << (n >> 1)), t, 1); asm volatile("" : "+v"(mk[t])); }
  unsigned vred = ldsb + 24576u + wid * 256u + (lane & 1u) * 64u + 16u * g, vrf = ldsb + 24576u + ((lane & 1u) * 4u + g) * 16u + ((lane >> 1) & 3u) * 4u, vpt = spart + lane * 16u, vkk = ldsb + 12288u + 32u, vkw = vkk + (lane < 48u ? 64u : 0u);
  unsigned long long m2 = 0x0003000300030003ull;
  asm volatile("" : "+v"(vrow), "+v"(vsel), "+v"(vred), "+v"(vrf), "+v"(vpt), "+v"(vkk), "+v"(vkw), "+s"(m2));
  const unsigned later = __builtin_amdgcn_readfirstlane(wid ? cw : 0x11u);
  out_rows(~later);
  TQ(1);
  PAGE(3, "adaln_pg8");
  if (wid == 0) {
    unsigned cnt = SPIN_CAP, t;
    asm volatile(
      "s_waitcnt vmcnt(0)\n"
      "Lk0_%=:\n\t"
      "v_min3_u32 %[t], v240, v242, v244\n\t"
      "v_min_u32 %[t], %[t], v246\n\t"
      "v_cmp_eq_u32 vcc, 0, %[t]\n\t"
      "s_cbranch_vccz Lk1_%=\n\t"
      "s_sleep " STR(KSLEEP) "\n\t"
      "buffer_load_dwordx4 v[240:243], %[kp], %[srdz], 0 offen sc1\n\t"
      "buffer_load_dwordx4 v[244:247], %[kp], %[srdz], 0 offen offset:16 sc1\n\t"
      "s_sub_u32 %[cnt], %[cnt], 1\n\t"
      "s_waitcnt vmcnt(0)\n\t"
      "s_cbranch_scc0 Lk0_%=\n"
      "Lk1_%=:"
      : "+{v[240:243]}"(ka), "+{v[244:247]}"(kc), [t] "=&v"(t), [cnt] "+s"(cnt) : [kp] "v"(kp), [srdz] "s"(srdz) : "vcc", "scc", "memory");
    unsigned k1 = ka[1], k2 = ka[0];
    unsigned l1 = kc[1], l2 = kc[0];
    top2_merge(k1, k2, ka[3], ka[2]); top2_merge(l1, l2, kc[3], kc[2]); top2_merge(k1, k2, l1, l2);
    top2_dpp<DPP_XOR1>(k1, k2); top2_dpp<DPP_XOR2>(k1, k2); top2_dpp<0x141>(k1, k2); top2_dpp<0x140>(k1, k2);
    top2_merge(k1, k2, __builtin_amdgcn_update_dpp(0u, k1, 0x142, 0xa, 0xf, false), __builtin_amdgcn_update_dpp(0u, k2, 0x142, 0xa, 0xf, false));
    top2_merge(k1, k2, __builtin_amdgcn_update_dpp(0u, k1, 0x143, 0xc, 0xf, false), __builtin_amdgcn_update_dpp(0u, k2, 0x143, 0xc, 0xf, false));
    k1 = ~k1 & 2047u; k2 = ~k2 & 2047u;
    *(LAS u32x4*)vkw = u32x4{k1, k2, (k1 >> 3) * 8192u, (k2 >> 3) * 8192u + ((k1 ^ k2) < 8u ? 2048u : 0u)};
  }
  u32x2 kk; unsigned vo;
  asm volatile("s_waitcnt lgkmcnt(0)\n\ts_barrier\n\tds_read_b32 %0, %3\n\tds_read_b64 %1, %4\n\ts_waitcnt lgkmcnt(0)\n\tv_add_u32 %2, %2, %0" : "=&v"(vo), "=&v"(kk), "+v"(vrow) : "v"(vsel), "v"(vkk) : "memory");
  TQ(5);
  unsigned k1, k2, rest;
  auto routed = [&]() __attribute__((always_inline)) {
    asm volatile("" : "+v"(kk));
    k1 = __builtin_amdgcn_readfirstlane(kk[0]); k2 = __builtin_amdgcn_readfirstlane(kk[1]);
    rest = __builtin_amdgcn_readfirstlane(later & ((r0 != k1 && r0 != k2 ? 1u : 0u) | (r0 + 1u != k1 && r0 + 1u != k2 ? 16u : 0u)));
    if (__builtin_expect(rest != later, 0)) { if (row == k1 || row == k2) publish(x1, 4096u); }
  };
  if (__builtin_expect(!moe, 0)) {
    asm volatile("adaln_pg8:\n\ts_cbranch_execz adaln_pg4");
    PAGE(4, "adaln_kr");
    u32x4 ha; u32x2 w0, w1;
    asm volatile("s_nop 4\n\tglobal_load_dwordx2 %0, %2, %3\n\tglobal_load_dwordx2 %1, %2, %3 offset:64" : "=&v"(w0), "=&v"(w1) : "v"(ca * 2u), "s"(p.wr) : "memory");
    asm volatile("buffer_load_dwordx4 %0, %3, %4, 0 offen sc1\n\ts_waitcnt vmcnt(0)" : "=&v"(ha), "+v"(w0), "+v"(w1) : "v"(vrow), "s"(srdz) : "memory");
    for (unsigned spins = 0; __builtin_expect(__any(max(max(ha[0], ha[1]), max(ha[2], ha[3])) == 0xffffffffu), 0) && spins < RETRY_CAP; ++spins)
      asm volatile("buffer_load_dwordx4 %0, %1, %2, 0 offen sc1\n\ts_waitcnt vmcnt(0)" : "=&v"(ha) : "v"(vrow), "s"(srdz) : "memory");
    routed();
    if (b == 255u && tid == 0u) __hip_atomic_store((gu64*)(p.ws + WS_EP), tag, RLX_AGENT);
    char* rows = lds + 16384 + (tid >> 7) * 2048u + ca * 2u;
    *(u32x2*)rows = u32x2{ha[0], ha[1]}; *(u32x2*)(rows + 64) = u32x2{ha[2], ha[3]};
    {
      float dot = 0.f;
      #pragma unroll
      for (int e = 0; e < 4; ++e) { const unsigned w = e < 2 ? w0[e] : w1[e - 2]; dot = fmaf(hi16(ha[e]), hi16(w), fmaf(lo16(ha[e]), lo16(w), dot)); }
      dot = wave_sum(dot);
      if (lane == 0) kb[16u + wid] = __float_as_uint(dot);
    }
    asm volatile("s_waitcnt lgkmcnt(0)\n\ts_barrier" ::: "memory");
    TQ(6);
    const unsigned zj = (b & 4u) * 4u + (idx & 3u) * 4u + (b & 3u), pp = zj * 64u + lane, sq = pp & 1u, nn = 128u * (pp >> 8) + 16u * (2u * ((pp >> 6) & 3u) + ((pp >> 1) & 1u)) + ((pp >> 2) & 15u);
    float gbv = bf2f(*(const u16*)(par + 14336 + nn * 2u)), hv = bf2f(*(const u16*)(lds + 16384 + sq * 2048u + nn * 2u));
    float rsel = rbf(__builtin_amdgcn_rcpf(1.f + __builtin_amdgcn_exp2f(rbf(__uint_as_float(kb[16u + 2u * sq]) + __uint_as_float(kb[17u + 2u * sq])) * -1.44269504089f)));
    u16* vout = pout + (sq ? k2 : k1) * DM + nn;
    unsigned vp = (unsigned)WS_PART + (tid >> 5) * 16384u + (hx ^ 8192u) + ((tid >> 4) & 1u) * 4096u + (zj & 15u) * 256u + (tid & 15u) * 16u;
    unsigned vw = ldsb + 24576u + wid * 256u + (lane & 15u) * 16u + 4u * g, vr = ldsb + 24576u + 4u * lane;
    unsigned vx = (unsigned)WS_ROWS + 4096u + kb[10u + sq] + (nn >> 9) * 1024u + (32u * ((nn >> 8) & 1u) + 16u * ((nn >> 6) & 1u) + 8u * ((nn >> 7) & 1u) + 4u * ((nn >> 4) & 1u) + ((nn >> 2) & 3u)) * 16u + ((nn >> 5) & 1u) * 8u + (nn & 3u) * 2u;
    asm volatile("" : "+v"(gbv), "+v"(hv), "+v"(rsel), "+v"(vout), "+v"(vp), "+v"(vw), "+v"(vr), "+v"(vx));
    out_rows(rest);
    TQ(7);
    __builtin_amdgcn_s_sleep(FSLEEP);
    float u; unsigned xw;
    for (unsigned spins = 0;; ++spins) {
      f32x4 v[14];
      asm volatile("buffer_load_ushort %0, %1, %2, 0 offen sc1" : "=v"(xw) : "v"(vx), "s"(srdz) : "memory");
      #pragma unroll
      for (int k = 0; k < 14; ++k) asm volatile("buffer_load_dwordx4 %0, %1, %2, %3 offen sc1" : "=v"(v[k]) : "v"(vp), "s"(srdz), "s"(131072u * k) : "memory");
      asm volatile("s_waitcnt vmcnt(12)" : "+v"(v[0]), "+v"(v[1]), "+v"(xw) :: "memory");
      f32x4 sum = v[0] + v[1];
#define NEXT2(k) asm volatile("s_waitcnt vmcnt(%3)" : "+v"(v[k]), "+v"(v[k + 1]), "+v"(sum) : "n"(12 - k) : "memory"); sum += v[k]; sum += v[k + 1]
      NEXT2(2); NEXT2(4); NEXT2(6); NEXT2(8); NEXT2(10); NEXT2(12);
      u = swap16_add(swap32_add(sum[0], sum[2]), swap32_add(sum[1], sum[3]));
      if (!__any(u != u || xw == 0xffffu) || spins > RETRY_CAP) break;
    }
    TQ(1);
    f32x2 ra, rc;
    asm volatile("ds_write_b32 %0, %1\n\ts_waitcnt lgkmcnt(0)\n\ts_barrier" :: "v"(vw), "v"(u) : "memory");
    if (wid == 0) {
      asm volatile("ds_read2st64_b32 %0, %2 offset1:1\n\tds_read2st64_b32 %1, %2 offset0:2 offset1:3\n\ts_waitcnt lgkmcnt(0)" : "=&v"(ra), "=&v"(rc) : "v"(vr) : "memory");
      ra += rc;
      const unsigned ov = cvtpk(fmaf(gbv, fmaf(ra[0] + ra[1], rsel, hv), lo16(xw)), 0.f);
      asm volatile("global_store_short %0, %1, off sc1" :: "v"(vout), "v"(ov) : "memory");
    }
  } else {
    f32x4 dg, du;
    bf16x8 hb[8];
    auto fetch = [&]() __attribute__((always_inline)) {
      asm volatile("s_mov_b64 exec, %8\n\t"
                   "buffer_load_dwordx4 %0, %9, %10, 0 offen sc1\n\tbuffer_load_dwordx4 %1, %9, %10, 0 offen offset:64 sc1\n\tbuffer_load_dwordx4 %2, %9, %10, 0 offen offset:128 sc1\n\tbuffer_load_dwordx4 %3, %9, %10, 0 offen offset:192 sc1\n\t"
                   "buffer_load_dwordx4 %4, %9, %10, 0 offen offset:256 sc1\n\tbuffer_load_dwordx4 %5, %9, %10, 0 offen offset:320 sc1\n\tbuffer_load_dwordx4 %6, %9, %10, 0 offen offset:384 sc1\n\tbuffer_load_dwordx4 %7, %9, %10, 0 offen offset:448 sc1\n\t"
                   "s_mov_b64 exec, -1"
                   : "=&v"(hb[0]), "=&v"(hb[1]), "=&v"(hb[2]), "=&v"(hb[3]), "=&v"(hb[4]), "=&v"(hb[5]), "=&v"(hb[6]), "=&v"(hb[7]) : "s"(m2), "v"(vrow), "s"(srdz) : "memory");
    };
#define MM2(a, h, c0, c1) "v_mfma_f32_16x16x32_bf16 %0, a[" #a ":" #a "+3], %" #h ", " c0 "\n\tv_mfma_f32_16x16x32_bf16 %1, a[" #a "+4:" #a "+7], %" #h ", " c1 "\n\t"
#define MM16 MM2(128, 2, "0", "0") MM2(136, 3, "%0", "%1") MM2(144, 4, "%0", "%1") MM2(152, 5, "%0", "%1") MM2(160, 6, "%0", "%1") MM2(168, 7, "%0", "%1") MM2(176, 8, "%0", "%1") MM2(184, 9, "%0", "%1") "s_nop 7"
#define HB "+v"(hb[0]), "+v"(hb[1]), "+v"(hb[2]), "+v"(hb[3]), "+v"(hb[4]), "+v"(hb[5]), "+v"(hb[6]), "+v"(hb[7])
    fetch();
    routed();
    asm volatile("s_cmp_lg_u32 %10, %11\n\ts_waitcnt vmcnt(4)\n\ts_cbranch_scc1 Lh_%=\n\ts_waitcnt vmcnt(0)\nLh_%=:\n\t" MM16 : "=&v"(dg), "=&v"(du), HB : "s"(rest), "s"(later) : "scc", "memory");
    TQ(6);
    for (unsigned spins = 0; __builtin_expect((__ballot(dg[0] != dg[0]) & m2) != 0ull, 0) && spins < RETRY_CAP; ++spins) {
      fetch();
      asm volatile("s_waitcnt vmcnt(0)\n\t" MM16 : "=&v"(dg), "=&v"(du), HB :: "memory");
    }
    f32x2 ra, rc, rd, re;
    asm volatile("s_mov_b64 exec, %7\n\tds_write_b128 %4, %5\n\tds_write_b128 %4, %6 offset:128\n\ts_mov_b64 exec, -1\n\ts_waitcnt lgkmcnt(0)\n\ts_barrier\n\t"
                 "ds_read2_b32 %0, %8 offset1:32\n\tds_read2_b32 %1, %8 offset0:64 offset1:96\n\tds_read2_b32 %2, %8 offset0:128 offset1:160\n\tds_read2_b32 %3, %8 offset0:192 offset1:224\n\ts_waitcnt lgkmcnt(0)"
                 : "=&v"(ra), "=&v"(rc), "=&v"(rd), "=&v"(re) : "v"(vred), "v"(dg), "v"(du), "s"(m2), "v"(vrf) : "memory");
    u32x2 actp;
    {
      const f32x2 gu = rb2((ra + rc) + (rd + re));
      const float sg = __uint_as_float(cvtpk(0.f, gu[0] * __builtin_amdgcn_rcpf(1.f + __builtin_amdgcn_exp2f(gu[0] * -1.44269504089f))));
      const unsigned a = cvtpk(0.f, sg * gu[1]);
      const unsigned o2 = __builtin_amdgcn_update_dpp(0u, a, DPP_XOR2, 0xf, 0xf, true);
      const unsigned pr = (lane & 2u) ? ((o2 >> 16) | a) : ((a >> 16) | o2);
      const unsigned o4 = __builtin_amdgcn_update_dpp(0u, pr, DPP_ROR4, 0xf, 0xf, true);
      actp = (lane & 4u) ? u32x2{o4, pr} : u32x2{pr, o4};
    }
    f32x4 yv[2];
#define ABD(t) "v"(u32x4{actp[0] & mk[t], actp[1] & mk[t], actp[0] & mk[t + 1], actp[1] & mk[t + 1]})
#define MM3(a, w, c0, c1) "v_mfma_f32_16x16x32_bf16 %0, %" #a ", a[" #w ":" #w "+3], " c0 "\n\tv_mfma_f32_16x16x32_bf16 %1, %" #a ", a[" #w "+16:" #w "+19], " c1 "\n\t"
    asm volatile("s_nop 1\n\t" MM3(2, 192, "0", "0") MM3(3, 196, "%0", "%1") MM3(4, 200, "%0", "%1") MM3(5, 204, "%0", "%1") "s_nop 6\n\t"
                 "buffer_store_dwordx4 %0, %6, %7, 0 offen sc1\n\tbuffer_store_dwordx4 %1, %6, %7, 0 offen offset:1024 sc1\n\ts_nop 1"
                 : "=&v"(yv[0]), "=&v"(yv[1]) : ABD(0), ABD(2), ABD(4), ABD(6), "v"(vpt), "s"(srdz) : "memory");
    TQ(7);
    out_rows(rest);
  }
  asm volatile("adaln_text_end:");
}
}

extern "C" void kernel_launch(void* const* d_in, const int* in_sizes, int n_in, void* d_out, int out_size, void* d_ws, size_t ws_size, hipStream_t stream) {
  (void)in_sizes; (void)n_in; (void)out_size;
  if (ws_size < WS_END) return;
  const u16* x    = (const u16*)d_in[0];
  const u16* kc   = (const u16*)d_in[1];
  const u16* vc   = (const u16*)d_in[2];
  const u16* wq   = (const u16*)d_in[3];
  const u16* wo   = (const u16*)d_in[4];
  const u16* wr   = (const u16*)d_in[6];
  const u16* wg   = (const u16*)d_in[7];
  const u16* wu   = (const u16*)d_in[8];
  const u16* wd   = (const u16*)d_in[9];
  const u16* c    = (const u16*)d_in[10];
  const u16* wmod = (const u16*)d_in[11];
  u16* out = (u16*)d_out;
  char* ws = (char*)d_ws;
  fa::Args fr;
  fr.x = x; fr.wmod = wmod; fr.wq = wq; fr.kc = kc; fr.vc = vc; fr.wo = wo; fr.wg = wg; fr.wu = wu; fr.wd = wd; fr.ws = ws; fr.out = out; fr.c = c; fr.wr = wr;
  hipFuncSetAttribute((const void*)fa::fattn_k, hipFuncAttributeMaxDynamicSharedMemorySize, (int)fa::LDS_BYTES);
  hipLaunchKernelGGL(fa::fattn_k, dim3(256), dim3(256), fa::LDS_BYTES, stream, fr);
}
```
